```python
import torch

def _softmax(x, axis=-1):
    return torch.softmax(x, dim=axis)
def _gelu(x):
    return torch.nn.functional.gelu(x, approximate="tanh")
def _silu(x):
    return torch.nn.functional.silu(x)
def _sigmoid(x):
    return torch.sigmoid(x)
def _layernorm(x, axis=-1, eps=1e-5):
    m = x.mean(dim=axis, keepdim=True)
    v = x.var(dim=axis, keepdim=True, unbiased=False)
    return (x - m) / torch.sqrt(v + eps)
def _scatter_add(tgt, idx, src, axis=0):
    return tgt.clone().index_add_(axis, idx, src)
def _segment_sum(src, seg, n):
    out = torch.zeros((n,) + src.shape[1:], dtype=src.dtype)
    return out.index_add_(0, seg, src)
def _pad(x, pw):
    flat = tuple(v for pair in reversed(pw) for v in pair)
    return torch.nn.functional.pad(x, flat)

kernel_name = 'interleave_layers__sliding_block__moe_arch_mod__prompt'

def setup_inputs():

    _w = (lambda a: (lambda t: t.to(torch.bfloat16) if t.is_floating_point() else t)(torch.as_tensor(a)))
    def _A_setup():
        B, S, D = 1, 2048, 1024
        return {
            "x": _w((lambda *s: torch.randn(*s, dtype=torch.bfloat16) * 0.05)(B, S, D)),
            "wqkv": _w((lambda *s: torch.randn(*s, dtype=torch.bfloat16) * 0.05)(D, 3 * D)),
            "wo": _w((lambda *s: torch.randn(*s, dtype=torch.bfloat16) * 0.05)(D, D)),
        }
    _A_in = _A_setup()

    def _B_setup():
        B, S, D, F, C = 1, 2048, 1024, 3584, 1
        return {
            "x": _w((lambda *s: torch.randn(*s, dtype=torch.bfloat16) * 0.05)(B, S, D)),
            "w_r": _w((lambda *s: torch.randn(*s, dtype=torch.bfloat16) * 1.6)(D)),
            "w_gate": _w((lambda *s: torch.randn(*s, dtype=torch.bfloat16) * 0.1)(D, F)),
            "w_up": _w((lambda *s: torch.randn(*s, dtype=torch.bfloat16) * 0.1)(D, F)),
            "w_down": _w((lambda *s: torch.randn(*s, dtype=torch.bfloat16) * 0.1)(F, D)),
        }
    _B_in = _B_setup()

    return {
        "A_x": _A_in["x"],
        "A_wqkv": _A_in["wqkv"],
        "A_wo": _A_in["wo"],
        "B_x": _B_in["x"],
        "B_w_r": _B_in["w_r"],
        "B_w_gate": _B_in["w_gate"],
        "B_w_up": _B_in["w_up"],
        "B_w_down": _B_in["w_down"],
    }

def reference(A_x, A_wqkv, A_wo, B_x, B_w_r, B_w_gate, B_w_up, B_w_down):
    def _A_ref(x, wqkv, wo):
        B, S, D = x.shape
        H = 32; Dh = D // H; W = 256
        qkv = (lambda x, *d: x.permute(*d))((x @ wqkv).reshape(B, S, 3, H, Dh), 2, 0, 3, 1, 4)
        q, k, v = qkv[0], qkv[1], qkv[2]
        scores = torch.einsum("bhqd,bhkd->bhqk", q, k) * (Dh ** -0.5)
        idx = torch.arange(S)
        mask = (idx[:, None] - idx[None, :] >= 0) & (idx[:, None] - idx[None, :] < W)
        scores = torch.where(mask[None, None], scores, -1e9)
        p = _softmax(scores, axis=-1)
        o = torch.einsum("bhqk,bhkd->bhqd", p, v)
        return (lambda x, *d: x.permute(*d))(o, 0, 2, 1, 3).reshape(B, S, D) @ wo

    def _B_ref(x, w_r, w_gate, w_up, w_down):
        B, S, D = x.shape
        C = 1
        r = x @ w_r
        idx = torch.argsort(-r, axis=-1)[:, :C]
        idx_e = (lambda x, n, axis: torch.repeat_interleave(x, n, dim=axis))(idx.reshape(B, C, 1), D, axis=2)
        x_sel = (lambda x, i, axis: torch.take_along_dim(x, i, dim=axis))(x, idx_e, axis=1)
        g = _silu(x_sel @ w_gate)
        u = x_sel @ w_up
        y = (g * u) @ w_down
        r_sel = _sigmoid((lambda x, i, axis: torch.take_along_dim(x, i, dim=axis))(r, idx, axis=1))
        y = y * r_sel.reshape(B, C, 1)
        b_off = (torch.arange(B) * S).reshape(B, 1)
        flat_idx = (idx + b_off).reshape(B * C)
        zero = (x * 0.0).reshape(B * S, D)
        upd = _scatter_add(zero, flat_idx, y.reshape(B * C, D), axis=0)
        return x + upd.reshape(B, S, D)

    x = A_x
    x = x + _A_ref(_layernorm(x, axis=-1), A_wqkv, A_wo)
    x = x + _B_ref(_layernorm(x, axis=-1), B_w_r, B_w_gate, B_w_up, B_w_down)
    x = x + _A_ref(_layernorm(x, axis=-1), A_wqkv, A_wo)
    x = x + _B_ref(_layernorm(x, axis=-1), B_w_r, B_w_gate, B_w_up, B_w_down)
    return x
```

```cpp
#include <hip/hip_runtime.h>
#include <stdint.h>
#include <utility>

typedef __bf16 bf16;
typedef __bf16 bf16x8 __attribute__((ext_vector_type(8)));
typedef __bf16 bf16x2 __attribute__((ext_vector_type(2)));
typedef float f32x4 __attribute__((ext_vector_type(4)));
typedef float f32x2 __attribute__((ext_vector_type(2)));
typedef short s16x4 __attribute__((ext_vector_type(4)));
typedef unsigned int u32x4 __attribute__((ext_vector_type(4)));
typedef unsigned int u32x2 __attribute__((ext_vector_type(2)));
#define LDS_AS __attribute__((address_space(3)))
#define GAS __attribute__((address_space(1)))
typedef GAS unsigned gu32;
typedef GAS unsigned long long gu64;
#define RLX_AGENT __ATOMIC_RELAXED, __HIP_MEMORY_SCOPE_AGENT
typedef __amdgpu_buffer_rsrc_t rsrc_t;

constexpr int F_ = 3584;
constexpr int SC1 = 16;

constexpr int RX = 4;
constexpr unsigned F_EP = 0, F_TMO = 128;
constexpr unsigned F_XN = 4096;
constexpr unsigned F_X1 = 196608;
constexpr unsigned F_O = 32768;
constexpr unsigned F_KV = 65536;
static_assert(F_XN + 8 * RX * 256 <= F_O && F_O + 2 * 64 * 256 <= F_KV, "flag block");
constexpr unsigned OFF_KEY = 131072;
constexpr unsigned OFF_AB = 147456;
constexpr unsigned OFF_XNEW = 262144;
constexpr unsigned OFF_CS = 294912;
constexpr unsigned OFF_FX = 393216;
constexpr unsigned OFF_CA = 524288;
constexpr unsigned OFF_CB = 655360;
constexpr unsigned OFF_STAT = 1u << 20;
constexpr unsigned OFF_PART = 3u << 20;
constexpr unsigned OFF_XN = 8u << 20;
constexpr unsigned OFF_O = 16u << 20;
constexpr unsigned OFF_KV = 32u << 20;
constexpr unsigned OFF_WI = 52u << 20;
constexpr unsigned WS_BYTES = 58u << 20;
constexpr unsigned POISON = 0xFFFFFFFFu;

constexpr int L_ST0 = 147456, L_CNT = 147968, L_CSL = 31872, L_FXL = 32256, L_K = 0, L_V = 32768, L_RING = 65536, L_XCH = 0, L_DN = 32768, L_TOUCH0 = 149504, L_RDY = 31744, L_QC = 31808, LDS_BYTES = 163840;
constexpr int STAGE = 24576;
#define SLOT(t) ((t) & 3)

template <typename F, int... I> __device__ __forceinline__ void static_for_impl(F&& f, std::integer_sequence<int, I...>) {
  (f(std::integral_constant<int, I>{}), ...);
}
template <int N, typename F> __device__ __forceinline__ void static_for(F&& f) { static_for_impl(f, std::make_integer_sequence<int, N>{}); }
__device__ __forceinline__ unsigned pack2(float a, float b) {
  f32x2 v = {a, b};
  return __builtin_bit_cast(unsigned, __builtin_convertvector(v, bf16x2));
}
__device__ __forceinline__ float asf(unsigned u) { return __builtin_bit_cast(float, u); }
__device__ __forceinline__ float lo16(unsigned u) { return __builtin_bit_cast(float, u << 16); }
__device__ __forceinline__ float hi16(unsigned u) { return __builtin_bit_cast(float, u & 0xffff0000u); }
__device__ __forceinline__ u32x4 pack8(const f32x4 a, const f32x4 b) {
  u32x4 o;
  o[0] = pack2(a[0], a[1]); o[1] = pack2(a[2], a[3]); o[2] = pack2(b[0], b[1]); o[3] = pack2(b[2], b[3]);
  return o;
}
__device__ __forceinline__ f32x4 mfma16(u32x4 a, u32x4 b, f32x4 c) {
  return __builtin_amdgcn_mfma_f32_16x16x32_bf16(__builtin_bit_cast(bf16x8, a), __builtin_bit_cast(bf16x8, b), c, 0, 0, 0);
}
__device__ __forceinline__ f32x4 mfma16k16(s16x4 a, s16x4 b, f32x4 c) { return __builtin_amdgcn_mfma_f32_16x16x16bf16_1k(a, b, c, 0, 0, 0); }
template <int CTRL> __device__ __forceinline__ float dppf(float v) {
  return __builtin_bit_cast(float, __builtin_amdgcn_update_dpp(0, __builtin_bit_cast(int, v), CTRL, 0xf, 0xf, false));
}
template <int CTRL> __device__ __forceinline__ unsigned dppu(unsigned v) {
  return (unsigned)__builtin_amdgcn_update_dpp(0, (int)v, CTRL, 0xf, 0xf, false);
}
__device__ __forceinline__ float add_x16(float v) {
  auto r = __builtin_amdgcn_permlane16_swap(__builtin_bit_cast(unsigned, v), __builtin_bit_cast(unsigned, v), false, false);
  return __builtin_bit_cast(float, (unsigned)r[0]) + __builtin_bit_cast(float, (unsigned)r[1]);
}
__device__ __forceinline__ float add_x32(float v) {
  auto r = __builtin_amdgcn_permlane32_swap(__builtin_bit_cast(unsigned, v), __builtin_bit_cast(unsigned, v), false, false);
  return __builtin_bit_cast(float, (unsigned)r[0]) + __builtin_bit_cast(float, (unsigned)r[1]);
}
__device__ __forceinline__ float max_x16(float v) {
  auto r = __builtin_amdgcn_permlane16_swap(__builtin_bit_cast(unsigned, v), __builtin_bit_cast(unsigned, v), false, false);
  return fmaxf(__builtin_bit_cast(float, (unsigned)r[0]), __builtin_bit_cast(float, (unsigned)r[1]));
}
__device__ __forceinline__ float max_x32(float v) {
  auto r = __builtin_amdgcn_permlane32_swap(__builtin_bit_cast(unsigned, v), __builtin_bit_cast(unsigned, v), false, false);
  return fmaxf(__builtin_bit_cast(float, (unsigned)r[0]), __builtin_bit_cast(float, (unsigned)r[1]));
}
__device__ __forceinline__ unsigned umax_x16(unsigned v) {
  auto r = __builtin_amdgcn_permlane16_swap(v, v, false, false);
  return max((unsigned)r[0], (unsigned)r[1]);
}
__device__ __forceinline__ unsigned umax_x32(unsigned v) {
  auto r = __builtin_amdgcn_permlane32_swap(v, v, false, false);
  return max((unsigned)r[0], (unsigned)r[1]);
}
__device__ __forceinline__ float sum_hi8(float v) {
  v += dppf<0x128>(v);
  v = add_x16(v);
  return add_x32(v);
}
__device__ __forceinline__ float sum64(float v) {
  v += dppf<0xB1>(v);
  v += dppf<0x4E>(v);
  v += dppf<0x124>(v);
  v += dppf<0x128>(v);
  v = add_x16(v);
  return add_x32(v);
}

#define BAR() asm volatile("s_waitcnt lgkmcnt(0)\n\ts_barrier" ::: "memory")
#define DRAIN() asm volatile("s_waitcnt vmcnt(0)" ::: "memory")
#define SB() __builtin_amdgcn_sched_barrier(0)
template <int N> __device__ __forceinline__ void wait_vm() { asm volatile("s_waitcnt vmcnt(%0)" ::"n"(N) : "memory"); }

__device__ __forceinline__ void dma16(rsrc_t rs, unsigned voff, unsigned soff, unsigned lds) {
  asm volatile("s_mov_b32 m0, %0\n\ts_nop 0\n\tbuffer_load_dwordx4 %1, %2, %3 offen lds" ::"s"(lds), "v"(voff), "s"(rs), "s"(soff) : "memory");
}
template <int OFF> __device__ __forceinline__ void dma16s(rsrc_t rs, unsigned voff, unsigned soff, unsigned lds) {
  asm volatile("s_mov_b32 m0, %0\n\ts_nop 0\n\tbuffer_load_dwordx4 %1, %2, %3 offen offset:%4 sc1 lds" ::"s"(lds), "v"(voff), "s"(rs), "s"(soff), "n"(OFF) : "memory");
}
__device__ __forceinline__ void touch(unsigned& d, const GAS bf16* ptr) { asm volatile("global_load_dword %0, %1, off" : "+v"(d) : "v"(ptr) : "memory"); }
__device__ __forceinline__ void ldrow(u32x4& a, u32x4& b, rsrc_t rx, unsigned voff, unsigned soff) {
  asm volatile("buffer_load_dwordx4 %0, %2, %3, %4 offen\n\tbuffer_load_dwordx4 %1, %2, %3, %4 offen offset:1024" : "=&v"(a), "=&v"(b) : "v"(voff), "s"(rx), "s"(soff) : "memory");
}
__device__ __forceinline__ void pin4(f32x2* v) { asm volatile("" : "+v"(v[0]), "+v"(v[1]), "+v"(v[2]), "+v"(v[3])); }
__device__ __forceinline__ float rbf1(float x) { return __builtin_bit_cast(float, pack2(0.f, x)); }
__device__ __forceinline__ void vswap(unsigned& a, unsigned& b) { asm volatile("v_swap_b32 %0, %1" : "+v"(a), "+v"(b)); }
__device__ __forceinline__ void vswap(float& a, float& b) { asm volatile("v_swap_b32 %0, %1" : "+v"(a), "+v"(b)); }
template <typename V, int N> __device__ __forceinline__ void vswapn(V& a, V& b) {
#pragma unroll
  for (int i = 0; i < N; ++i) { auto x = a[i], y = b[i]; vswap(x, y); a[i] = x; b[i] = y; }
}
__device__ __forceinline__ u32x4 ld16(rsrc_t rs, unsigned voff, unsigned soff) { return __builtin_amdgcn_raw_buffer_load_b128(rs, voff, soff, SC1); }
template <int OFF> __device__ __forceinline__ void ld16h(u32x4& d, rsrc_t rs, unsigned voff, unsigned soff) {
  asm volatile("buffer_load_dwordx4 %0, %1, %2, %3 offen offset:%4 sc1" : "=v"(d) : "v"(voff), "s"(rs), "s"(soff), "n"(OFF) : "memory");
}
template <int I, int OFF> __device__ __forceinline__ void ld16w(u32x4& d, rsrc_t rs, unsigned voff, unsigned soff) {
  if constexpr (I == 0) asm volatile("buffer_load_dwordx4 %0, %1, %2, %3 offen offset:%4 sc1" : "={v[128:131]}"(d) : "v"(voff), "s"(rs), "s"(soff), "n"(OFF) : "memory");
  else if constexpr (I == 1) asm volatile("buffer_load_dwordx4 %0, %1, %2, %3 offen offset:%4 sc1" : "={v[132:135]}"(d) : "v"(voff), "s"(rs), "s"(soff), "n"(OFF) : "memory");
  else if constexpr (I == 2) asm volatile("buffer_load_dwordx4 %0, %1, %2, %3 offen offset:%4 sc1" : "={v[136:139]}"(d) : "v"(voff), "s"(rs), "s"(soff), "n"(OFF) : "memory");
  else if constexpr (I == 3) asm volatile("buffer_load_dwordx4 %0, %1, %2, %3 offen offset:%4 sc1" : "={v[140:143]}"(d) : "v"(voff), "s"(rs), "s"(soff), "n"(OFF) : "memory");
  else if constexpr (I == 4) asm volatile("buffer_load_dwordx4 %0, %1, %2, %3 offen offset:%4 sc1" : "={v[144:147]}"(d) : "v"(voff), "s"(rs), "s"(soff), "n"(OFF) : "memory");
  else if constexpr (I == 5) asm volatile("buffer_load_dwordx4 %0, %1, %2, %3 offen offset:%4 sc1" : "={v[148:151]}"(d) : "v"(voff), "s"(rs), "s"(soff), "n"(OFF) : "memory");
  else if constexpr (I == 6) asm volatile("buffer_load_dwordx4 %0, %1, %2, %3 offen offset:%4 sc1" : "={v[152:155]}"(d) : "v"(voff), "s"(rs), "s"(soff), "n"(OFF) : "memory");
  else if constexpr (I == 7) asm volatile("buffer_load_dwordx4 %0, %1, %2, %3 offen offset:%4 sc1" : "={v[156:159]}"(d) : "v"(voff), "s"(rs), "s"(soff), "n"(OFF) : "memory");
  else if constexpr (I == 8) asm volatile("buffer_load_dwordx4 %0, %1, %2, %3 offen offset:%4 sc1" : "={v[160:163]}"(d) : "v"(voff), "s"(rs), "s"(soff), "n"(OFF) : "memory");
  else if constexpr (I == 9) asm volatile("buffer_load_dwordx4 %0, %1, %2, %3 offen offset:%4 sc1" : "={v[164:167]}"(d) : "v"(voff), "s"(rs), "s"(soff), "n"(OFF) : "memory");
  else if constexpr (I == 10) asm volatile("buffer_load_dwordx4 %0, %1, %2, %3 offen offset:%4 sc1" : "={v[168:171]}"(d) : "v"(voff), "s"(rs), "s"(soff), "n"(OFF) : "memory");
  else if constexpr (I == 11) asm volatile("buffer_load_dwordx4 %0, %1, %2, %3 offen offset:%4 sc1" : "={v[172:175]}"(d) : "v"(voff), "s"(rs), "s"(soff), "n"(OFF) : "memory");
}
template <int K> __device__ __forceinline__ void pin_w(u32x4& a, u32x4& b) {
  if constexpr (K == 0) asm volatile("" : "+{v[128:131]}"(a), "+{v[132:135]}"(b)::"memory");
  else if constexpr (K == 1) asm volatile("" : "+{v[136:139]}"(a), "+{v[140:143]}"(b)::"memory");
  else if constexpr (K == 2) asm volatile("" : "+{v[144:147]}"(a), "+{v[148:151]}"(b)::"memory");
  else if constexpr (K == 3) asm volatile("" : "+{v[152:155]}"(a), "+{v[156:159]}"(b)::"memory");
  else if constexpr (K == 4) asm volatile("" : "+{v[160:163]}"(a), "+{v[164:167]}"(b)::"memory");
  else if constexpr (K == 5) asm volatile("" : "+{v[168:171]}"(a), "+{v[172:175]}"(b)::"memory");
}
__device__ __forceinline__ void ld8h(u32x2& d, rsrc_t rs, unsigned voff, unsigned soff) {
  asm volatile("buffer_load_dwordx2 %0, %1, %2, %3 offen sc1" : "=v"(d) : "v"(voff), "s"(rs), "s"(soff) : "memory");
}
template <int OFF> __device__ __forceinline__ void ld4h(unsigned& d, rsrc_t rs, unsigned voff, unsigned soff) {
  asm volatile("buffer_load_dword %0, %1, %2, %3 offen offset:%4 sc1" : "=v"(d) : "v"(voff), "s"(rs), "s"(soff), "n"(OFF) : "memory");
}
__device__ __forceinline__ u32x2 ld8(rsrc_t rs, unsigned voff, unsigned soff) { return __builtin_amdgcn_raw_buffer_load_b64(rs, voff, soff, SC1); }
__device__ __forceinline__ void st16(u32x4 v, rsrc_t rs, unsigned voff, unsigned soff) { __builtin_amdgcn_raw_buffer_store_b128(v, rs, voff + soff, 0, SC1); }
__device__ __forceinline__ void st8(u32x2 v, rsrc_t rs, unsigned voff, unsigned soff) { __builtin_amdgcn_raw_buffer_store_b64(v, rs, voff, soff, SC1); }

__device__ __forceinline__ unsigned ld4(rsrc_t rs, unsigned voff, unsigned soff) { return __builtin_amdgcn_raw_buffer_load_b32(rs, voff, soff, SC1); }
__device__ __forceinline__ void st4(unsigned v, rsrc_t rs, unsigned voff, unsigned soff) { __builtin_amdgcn_raw_buffer_store_b32(v, rs, voff, soff, SC1); }

constexpr unsigned kSpinCap = 1u << 16;
__device__ __forceinline__ bool spin_fail(unsigned& spins, rsrc_t rs, u32x2 T) {
  asm volatile("" ::: "memory");
  ++spins;
  if (spins > kSpinCap) { st8(T, rs, 0, F_TMO); return true; }
  if ((spins & 255u) == 0u) {
    const u32x2 v = ld8(rs, 0, F_TMO);
    if (v[0] == T[0] && v[1] == T[1]) return true;
  }
  __builtin_amdgcn_s_sleep(3);
  return false;
}
__device__ __forceinline__ void poll_flags2(rsrc_t rs, unsigned voff, u32x2 T) {
  for (unsigned spins = 0;;) {
    const u32x4 v = ld16(rs, voff, 0);
    if (__all(v[0] == T[0] && v[1] == T[1] && v[2] == T[0] && v[3] == T[1])) break;
    if (spin_fail(spins, rs, T)) break;
  }
  asm volatile("" ::: "memory");
  __builtin_amdgcn_fence(__ATOMIC_ACQUIRE, "wavefront");
}
__device__ __forceinline__ void poll_flags(rsrc_t rs, unsigned voff, unsigned soff, u32x2 T) {
  for (unsigned spins = 0;;) {
    const u32x2 v = ld8(rs, voff, soff);
    if (__all(v[0] == T[0] && v[1] == T[1])) break;
    if (spin_fail(spins, rs, T)) break;
  }
  asm volatile("" ::: "memory");
  __builtin_amdgcn_fence(__ATOMIC_ACQUIRE, "wavefront");
}

__device__ __forceinline__ unsigned toff(unsigned R, unsigned ks, unsigned g) {
  return (((R >> 5) * 64 + ks * 2 + ((R >> 4) & 1)) << 10) + ((g * 16 + (R & 15)) << 4);
}
__device__ __forceinline__ void unpack8v(const u32x4 u, f32x2* v) {
#pragma unroll
  for (int j = 0; j < 4; ++j) v[j] = f32x2{lo16(u[j]), hi16(u[j])};
}
__device__ __forceinline__ u32x4 pack8v(const f32x2* v) {
  u32x4 o;
#pragma unroll
  for (int j = 0; j < 4; ++j) o[j] = pack2(v[j][0], v[j][1]);
  return o;
}
__device__ __forceinline__ f32x2 rbf1v(f32x2 v) { return f32x2{rbf1(v[0]), rbf1(v[1])}; }
__device__ __forceinline__ float ln_rd(float var) { return __builtin_amdgcn_rcpf(rbf1(__builtin_amdgcn_sqrtf(rbf1(rbf1(var) + 1e-5f)))); }
template <int N> __device__ __forceinline__ void ln_apply(float mb, float rd, const f32x2* v, f32x2* y) {
  const f32x2 mb2 = {mb, mb}, rd2 = {rd, rd};
#pragma unroll
  for (int j = 0; j < N; ++j) y[j] = rbf1v(rbf1v(v[j] - mb2) * rd2);
}
__device__ __forceinline__ float hadd(f32x2 v) {
  float a = v[0];
  asm volatile("" : "+v"(a));
  return a + v[1];
}
__device__ __forceinline__ float add_g(float v) { return add_x32(add_x16(v)); }
__device__ __forceinline__ void add_g3(float& x, float& y, float& z) {
  auto a = __builtin_amdgcn_permlane16_swap(__builtin_bit_cast(unsigned, x), __builtin_bit_cast(unsigned, y), false, false);
  auto b = __builtin_amdgcn_permlane16_swap(__builtin_bit_cast(unsigned, z), __builtin_bit_cast(unsigned, z), false, false);
  const float r = asf(a[0]) + asf(a[1]), t = asf(b[0]) + asf(b[1]);
  auto c = __builtin_amdgcn_permlane32_swap(__builtin_bit_cast(unsigned, r), __builtin_bit_cast(unsigned, r), false, false);
  auto d = __builtin_amdgcn_permlane32_swap(__builtin_bit_cast(unsigned, t), __builtin_bit_cast(unsigned, t), false, false);
  const float u = asf(c[0]) + asf(c[1]);
  z = asf(d[0]) + asf(d[1]);
  auto e = __builtin_amdgcn_permlane16_swap(__builtin_bit_cast(unsigned, u), __builtin_bit_cast(unsigned, u), false, false);
  x = asf(e[0]); y = asf(e[1]);
}
__device__ __forceinline__ u32x4 trade16(u32x4 v) {
  auto a = __builtin_amdgcn_permlane16_swap(v[0], v[2], false, false);
  auto b = __builtin_amdgcn_permlane16_swap(v[1], v[3], false, false);
  return u32x4{(unsigned)a[0], (unsigned)b[0], (unsigned)a[1], (unsigned)b[1]};
}
__device__ __forceinline__ unsigned umax4(u32x4 v) { return max(max(v[0], v[1]), max(v[2], v[3])); }
__device__ __forceinline__ u32x2 unpoison2(u32x2 v) { return u32x2{min(v[0], POISON - 1), min(v[1], POISON - 1)}; }
__device__ __forceinline__ u32x4 unpoison4(u32x4 v) {
  return u32x4{min(v[0], POISON - 1), min(v[1], POISON - 1), min(v[2], POISON - 1), min(v[3], POISON - 1)};
}

typedef union { struct { s16x4 a, b; } hf; u32x4 v; } frag_t;
__device__ __forceinline__ s16x4 trread(const char* smem, unsigned off) {
  return __builtin_amdgcn_ds_read_tr16_b64_v4i16((LDS_AS s16x4*)(smem + off));
}
__device__ __forceinline__ s16x4 trabs(unsigned addr) { return __builtin_amdgcn_ds_read_tr16_b64_v4i16((LDS_AS s16x4*)(size_t)addr); }

constexpr int WA = 6;
constexpr int WB = 8, WR = 4;
#define CH 1, 8, 10, 11, 16, 18, 24, 26, 30, 31
constexpr int CHV[10] = {CH};
constexpr int SPL = CHV[0];
constexpr int FSL = CHV[1], FS0 = CHV[2], FS0D = CHV[3], FS1 = CHV[4], FS1S = CHV[5];
constexpr int FXA = CHV[6], FXB = CHV[7], FXL = CHV[8], FXS = CHV[9];
#define C0 27, 3, 16, 19, 22, 23, 2, 5, 4
constexpr int C0V[9] = {C0};
constexpr int IMG = C0V[0], CSD = C0V[1];
constexpr int ERQ = C0V[2], CSL = C0V[3], CSS = C0V[4];
constexpr int RWL = C0V[6], RWS = C0V[7], POI = C0V[8];
#define WTS 99
#define WTG 2
#define PBS 10
#define KP0 26
#define PS0 8
constexpr int cmax(int a, int b) { return a > b ? a : b; }
constexpr int EIN = cmax(CSD, cmax(RWS + 1, POI)) + 3;
constexpr int ABS = 27;
constexpr int abl(int it) { return it ? 25 : C0V[5]; }
constexpr int XU = 3, XL = 2;
constexpr int x_slot(int u) { return u % XU == 0 ? 0 : u % XU == 1 ? 32768 : 49152; }
#define TCH 20, 6, 5, 27
constexpr int TCV[4] = {TCH};
constexpr int TS0 = TCV[0], TSN = TCV[1], TW = TCV[2], TST = TCV[3];
constexpr int QL = 26;
constexpr int SDIR = 24, GSL = 14, RSL = 56, KSL = 3;
constexpr bool tile_piece(int s) { return s >= 3 && ((s - 3) >> 2) + 3 < 8 && ((s - 3) & 3) < 3; }
constexpr int a_req(int s, int n) {
  if (s < 1 || !(n & 1) || (s == 1 && n == 1)) return -1;
  if (s <= 5) return 8 + 3 * s + (n >> 1) - 4;
  return n < 5 && s + 5 < 32 ? 2 * (s + 5) + (n >> 1) : -1;
}
constexpr int vm_n(int it, int s, int n) {
  int c = 0;
  if (n == 0) c += tile_piece(s);
  if (n == 4) c += (s == abl(it) ? 8 : 0) + (it ? (s == FSL ? 5 : s == FXA ? 4 : 0) + (s == FSL - 2) : (s == ERQ) + (s == RWL ? 2 : 0) + (s == RWS + 1) + (s >= IMG && s < IMG + 3));
  if (!it) c += (s == WTS && n == WTG) + (s < 4 && n == 2) + (s == 0 && (n == 0 || n == 4)) + ((n == 3 || n == 5) && s + XL + XU < 32);
  else c += (a_req(s, n) >= 0) + (s == 0 && n == 0) + (s == 2 && n == 2) + (s == SPL && n == 4 ? 4 : 0);
  return c;
}
constexpr int vm_at(int it, int s, int n) {
  int c = it ? 8 : 14;
  for (int u = 0; u < 6 * s + n; ++u) c += vm_n(it, u / 6, u % 6);
  return c;
}
constexpr int vm_a(int it, int k) {
  if (k < 4) return 2 * k + 2;
  for (int u = 0; u < 192; ++u)
    if (a_req(u / 6, u % 6) == 2 * k + 1) return vm_at(it, (u + 1) / 6, (u + 1) % 6);
  return 0;
}
constexpr int vm_tile(int it, int t) { return t >= 3 ? vm_at(it, 4 * t - 7, 1) : t == 1 ? vm_at(0, 0, 5) : vm_at(0, 3, 3); }
constexpr int vm_x(int u) { return u == 2 ? 9 : u < XU + XL ? 12 + 2 * (u - XU) : vm_at(0, u - XU - XL + 1, 0); }

constexpr int L_XC = LDS_BYTES - 64, L_SKEY = 32704, L_SRED = 4096, L_SFIN = 20480;
__device__ __forceinline__ unsigned key_load(rsrc_t rs, int l, int L, int rep) { return ld4(rs, (l & 15) * 4, OFF_KEY + (L * 16 + rep) * 128); }
__device__ __forceinline__ unsigned umax16(unsigned k) {
  k = max(k, dppu<0xB1>(k));
  k = max(k, dppu<0x4E>(k));
  k = max(k, dppu<0x141>(k));
  return max(k, dppu<0x140>(k));
}
__device__ __forceinline__ unsigned key_row(unsigned key) { return (0xffffu - (key & 0xffffu)) & 2047u; }
__device__ __forceinline__ unsigned key_wait(rsrc_t rs, u32x2 T, int l, int L, int rep, unsigned kv) {
  if (!__all(kv != POISON)) {
    unsigned spins = 0;
    do {
      if (spin_fail(spins, rs, T)) break;
      if (L == 0) __builtin_amdgcn_s_sleep(KSL);
      kv = key_load(rs, l, L, rep);
    } while (!__all(kv != POISON));
  }
  return __builtin_amdgcn_readfirstlane(umax16(kv));
}
__device__ __forceinline__ unsigned ffn_xoff(unsigned key, int L, int w) { return OFF_CA + L * 32768 + (key_row(key) >> 7) * 2048 + w * 256; }
__device__ __forceinline__ void ffn_xwait(rsrc_t rs, u32x2 T, unsigned xo, int l, u32x4 (&xv)[4]) {
#define XV_OK() __all(max(max(umax4(xv[0]), umax4(xv[1])), max(umax4(xv[2]), umax4(xv[3]))) != POISON)
  if (!XV_OK()) {
    unsigned spins = 0;
    do {
      if (spin_fail(spins, rs, T)) break;
#pragma unroll
      for (int i = 0; i < 4; ++i) xv[i] = ld16(rs, (l >> 4) * 16 + i * 64, xo);
    } while (!XV_OK());
  }
#undef XV_OK
}
__device__ __forceinline__ void ffn_gu(const u32x4 (&gv)[4], const u32x4 (&uv)[4], const u32x4 (&xv)[4], f32x4& ga, f32x4& ua) {
  ga = mfma16(gv[0], xv[0], f32x4{0.f, 0.f, 0.f, 0.f}); ua = mfma16(uv[0], xv[0], f32x4{0.f, 0.f, 0.f, 0.f});
#pragma unroll
  for (int i = 1; i < 4; ++i) { ga = mfma16(gv[i], xv[i], ga); ua = mfma16(uv[i], xv[i], ua); }
}
__device__ __forceinline__ void ffn_gu_check(rsrc_t rs, u32x2 T, unsigned xo, int l, const u32x4 (&gv)[4], const u32x4 (&uv)[4], u32x4 (&xv)[4], f32x4& ga, f32x4& ua) {
  if (__any(ga[0] != ga[0])) {
    ffn_xwait(rs, T, xo, l, xv);
    ffn_gu(gv, uv, xv, ga, ua);
  }
}
__device__ __forceinline__ void ffn_gu_store(char* smem, int l, int w, f32x4 ga, f32x4 ua) {
  LDS_AS f32x4* sred = (LDS_AS f32x4*)(smem + L_SRED);
  if ((l & 15) == 0) { sred[w * 8 + (l >> 4)] = ga; sred[w * 8 + 4 + (l >> 4)] = ua; }
}
__device__ __forceinline__ void ffn_d_read(char* smem, int l, int w, f32x4& t, s16x4 (&dfr)[8]) {
  t = ((const LDS_AS f32x4*)(smem + L_SRED))[(l & 7) * 8 + ((l >> 3) & 1) * 4 + (l >> 4)];
#pragma unroll
  for (int i = 0; i < 8; ++i) dfr[i] = trread(smem, L_DN + w * 4096 + (l >> 4) * 1024 + ((l & 15) >> 2) * 256 + i * 32 + (l & 3) * 8);
}
__device__ __forceinline__ float ffn_d_sum(float t) { t += dppf<0xB1>(t); t += dppf<0x4E>(t); return t + dppf<0x141>(t); }
__device__ __forceinline__ float ffn_d_h(float t) {
  const float gg = rbf1(t), uu = rbf1(dppf<0x128>(t));
  return rbf1(gg * __builtin_amdgcn_rcpf(1.0f + __expf(-gg))) * uu;
}
__device__ __forceinline__ f32x4 ffn_d_mma(int l, int t, s16x4 d, u32x2 hh, f32x4 y) {
  const bool me = (l & 15) == t;
  return mfma16k16(d, __builtin_bit_cast(s16x4, u32x2{me ? hh[0] : 0u, me ? hh[1] : 0u}), y);
}
__device__ __forceinline__ void ffn_d_store(rsrc_t rs, int l, int w, int L, int slice, f32x4 y) {
  const unsigned c = l & 15;
  if (c < 8) st16(unpoison4(__builtin_bit_cast(u32x4, y)), rs, (4 * w + (c >> 1)) * 32768 + slice * 128 + (c & 1) * 64 + (l >> 4) * 16, OFF_PART + L * (1u << 20));
}
__device__ __forceinline__ void red_load(rsrc_t rs, int tid, int L, unsigned G, unsigned key, u32x4 (&pv)[4], u32x2& cxn, u32x2& cx1) {
  const unsigned co = L * 32768 + (key_row(key) >> 7) * 2048 + G * 64, jj = (tid & 3) * 16 + (tid & 4) * 2;
#pragma unroll
  for (int k = 0; k < 4; ++k) pv[k] = ld16(rs, min(tid + 512 * k, 1791) * 16, OFF_PART + L * (1u << 20) + G * 32768);
  cxn = ld8(rs, jj, OFF_CA + co);
  cx1 = ld8(rs, jj, OFF_CB + co);
}
__device__ __forceinline__ void red_sum(rsrc_t rs, char* smem, u32x2 T, int tid, int w, int L, unsigned G, unsigned key,
                                        u32x4 (&pv)[4], u32x2& cxn, u32x2& cx1) {
#define PV_OK() __all(max(max(max(umax4(pv[0]), umax4(pv[1])), max(umax4(pv[2]), umax4(pv[3]))), max(max(cxn[0], cxn[1]), max(cx1[0], cx1[1]))) != POISON)
  if (!PV_OK()) {
    unsigned spins = 0;
    do {
      if (spin_fail(spins, rs, T)) break;
      red_load(rs, tid, L, G, key, pv, cxn, cx1);
    } while (!PV_OK());
  }
#undef PV_OK
  f32x4 s = (__builtin_bit_cast(f32x4, pv[0]) + __builtin_bit_cast(f32x4, pv[1])) + __builtin_bit_cast(f32x4, pv[2]);
  if (w < 4) s += __builtin_bit_cast(f32x4, pv[3]);
  ((LDS_AS f32x4*)(smem + L_SFIN))[tid] = s;
}
__device__ __forceinline__ u32x2 red_final(char* smem, int l, unsigned key, u32x2 cxn, u32x2 cx1) {
  const LDS_AS f32x4* sf = (const LDS_AS f32x4*)(smem + L_SFIN) + l;
  f32x4 s = ((sf[0] + sf[64]) + (sf[128] + sf[192])) + ((sf[256] + sf[320]) + (sf[384] + sf[448]));
#pragma unroll
  for (int j = 0; j < 4; ++j) s[j] = sum_hi8(s[j]);
  const unsigned k16 = key >> 16;
  const float rmax = __builtin_bit_cast(float, ((k16 & 0x8000u) ? (k16 & 0x7fffu) : (~k16 & 0xffffu)) << 16);
  const float sig = rbf1(__builtin_amdgcn_rcpf(1.0f + __expf(-rmax)));
  const float xn4[4] = {lo16(cxn[0]), hi16(cxn[0]), lo16(cxn[1]), hi16(cxn[1])};
  const float x14[4] = {lo16(cx1[0]), hi16(cx1[0]), lo16(cx1[1]), hi16(cx1[1])};
  float o[4];
#pragma unroll
  for (int j = 0; j < 4; ++j) o[j] = x14[j] + rbf1(xn4[j] + rbf1(rbf1(s[j]) * sig));
  return u32x2{pack2(o[0], o[1]), pack2(o[2], o[3])};
}

struct RowFin { float mean, var, mb, rd; unsigned key; };
__device__ __forceinline__ void fin_wait(rsrc_t rs, u32x2 T, unsigned voff, unsigned so, u32x4 (&sp)[4]) {
#define ST_OK() __all(max(max(umax4(sp[0]), umax4(sp[1])), max(umax4(sp[2]), umax4(sp[3]))) != POISON)
  if (!ST_OK()) {
    unsigned spins = 0;
    do {
      if (spin_fail(spins, rs, T)) break;
#pragma unroll
      for (int i = 0; i < 4; ++i) sp[i] = ld16(rs, voff + i * 2048, so);
    } while (!ST_OK());
  }
#undef ST_OK
}
__device__ __forceinline__ RowFin fin_key(rsrc_t rs, u32x2 T, unsigned voff, unsigned so, char* skey, int l, int w, unsigned R, float wsum, u32x4 (&sp)[4]) {
  RowFin r;
#define ST_SUM() ((__builtin_bit_cast(f32x4, sp[0]) + __builtin_bit_cast(f32x4, sp[1])) + (__builtin_bit_cast(f32x4, sp[2]) + __builtin_bit_cast(f32x4, sp[3])))
  f32x4 sf = ST_SUM();
  const float z = (sf[0] + sf[1]) + sf[2];
  if (__any(z != z)) {
    fin_wait(rs, T, voff, so, sp);
    sf = ST_SUM();
  }
#undef ST_SUM
  float s0 = sf[0], s1 = sf[1], s2 = sf[2];
  add_g3(s0, s1, s2);
  r.mean = s0 * (1.0f / 1024.0f);
  r.var = fmaxf(__builtin_fmaf(-r.mean, r.mean, s1 * (1.0f / 1024.0f)), 0.f);
  r.mb = rbf1(r.mean); r.rd = ln_rd(r.var);
  const unsigned rb16 = pack2((s2 - r.mb * wsum) * r.rd, 0.f) & 0xffffu;
  r.key = min((((rb16 & 0x8000u) ? (~rb16 & 0xffffu) : (rb16 | 0x8000u)) << 16) | (0xffffu - R), POISON - 1);
  const unsigned wk = umax16(r.key);
  if (l == 0) ((LDS_AS unsigned*)skey)[w] = wk;
  return r;
}
__device__ __forceinline__ void fin_cand(rsrc_t rs, char* skey, int L, int g, int rb, int ct, const RowFin& r, const f32x2 (&v)[8]) {
  const u32x4 k0 = *(const LDS_AS u32x4*)skey, k1 = *(const LDS_AS u32x4*)(skey + 16);
  const unsigned best = max(umax4(k0), umax4(k1));
  if (r.key == best) {
    const unsigned co = L * 32768 + rb * 2048 + ct * 128 + g * 16;
    f32x2 t[8], rd2 = {r.rd, r.rd}, of2 = {-r.mb * r.rd, -r.mb * r.rd};
    asm volatile("" : "+v"(rd2), "+v"(of2));
#pragma unroll
    for (int j = 0; j < 8; ++j) t[j] = __builtin_elementwise_fma(v[j], rd2, of2);
    st16(unpoison4(pack8v(t)), rs, co, OFF_CA); st16(unpoison4(pack8v(t + 4)), rs, co + 64, OFF_CA);
    if (g == 0) st4(best, rs, OFF_KEY + (L * 16 + ct) * 128 + rb * 4, 0);
    st16(unpoison4(pack8v(v)), rs, co, OFF_CB); st16(unpoison4(pack8v(v + 4)), rs, co + 64, OFF_CB);
  }
}
__device__ __forceinline__ void fin_x2(const RowFin& r, f32x2 (&v)[8]) {
  f32x2 sc2 = {1.0f + r.rd, 1.0f + r.rd}, of2 = {-r.mb * r.rd, -r.mb * r.rd};
  asm volatile("" : "+v"(sc2), "+v"(of2));
#pragma unroll
  for (int j = 0; j < 8; ++j) v[j] = __builtin_elementwise_fma(v[j], sc2, of2);
}

extern "C" __device__ unsigned long long dispatch_id() __asm("llvm.amdgcn.dispatch.id");

extern "C" __global__ __launch_bounds__(512, 2) void mega_kernel(const bf16* x, char* ws, const bf16* wqkv, const bf16* wo, const bf16* wr,
                                                                 const bf16* wg, const bf16* wu, const bf16* wd, bf16* out) {
  extern __shared__ __attribute__((aligned(16))) char smem[];
  const int w0_ = __builtin_amdgcn_readfirstlane(threadIdx.x >> 6);
  const unsigned lbase = (unsigned)(size_t)(LDS_AS char*)smem;
  {
    unsigned long long pc;
    unsigned len;
    asm volatile("s_getpc_b64 %0\n\ts_mov_b32 %1, .Ltext_end-mega_kernel\n\t.subsection 1\n.Ltext_end:\n\t.subsection 0" : "=s"(pc), "=s"(len));
    const unsigned o = ((blockIdx.x >> 3) * 64 + (threadIdx.x & 63)) * 128;
    if (w0_ == 7 && o < len)
      asm volatile("s_mov_b32 m0, %0\n\ts_nop 0\n\tglobal_load_lds_dword %1, %2" ::"s"(lbase + L_TOUCH0), "v"(o), "s"(pc & ~127ull) : "memory");
  }
  asm volatile("" : "+s"(x), "+s"(ws), "+s"(wqkv));
#define IDS()                                                                                         \
  int tid = threadIdx.x, w = w0_, bid = blockIdx.x;                                                   \
  asm volatile("" : "+v"(tid));                                                                       \
  asm volatile("" : "+s"(w));                                                                         \
  asm volatile("" : "+s"(bid));                                                                       \
  const int l = tid & 63, xcd = bid & 7, bi = bid >> 3;                                               \
  const int p = (xcd & 1) * 4 + (bi & 3), h = (xcd >> 1) * 8 + (bi >> 2);         \
  const int rb = 2 * xcd + (bi & 1), ct = bi >> 1;                               \
  const int slice = xcd * 28 + bi;                                                   \
  const bool ffn_on = bi < 28;                                                                        \
  const int g = l >> 4;                                                                               \
  const int rt = w < 4 ? w : 11 - w;          \
  KARGS();                                                                                            \
  (void)p; (void)h; (void)rb; (void)ct; (void)slice; (void)ffn_on; (void)g; (void)rt;
#define PROT() (2 * (p & 3) + (p >> 2))
  const rsrc_t rs = __builtin_amdgcn_make_buffer_rsrc((void*)ws, 0, WS_BYTES, 0x00020000);
  typedef const void* const __attribute__((address_space(4)))* karg_t;
#define KARGS() karg_t ka = (karg_t)__builtin_amdgcn_kernarg_segment_ptr(); asm volatile("" : "+s"(ka))
#define ARGP(name, i) const GAS bf16* name = (const GAS bf16*)ka[i]; asm volatile("" : "+s"(name))
#define RSRC(ptr, bytes) __builtin_amdgcn_make_buffer_rsrc((void*)(ptr), 0, bytes, 0x00020000)
#define rwq RSRC(pwq, 1024 * 3072 * 2)
#define rwo RSRC(pwo, 1024 * 1024 * 2)
#define rwg RSRC(pwg, 1024 * F_ * 2)
#define rwu RSRC(pwu, 1024 * F_ * 2)
#define rwd RSRC(pwd, 1024 * F_ * 2)
#define rout RSRC(pout, 2048 * 1024 * 2)

#define WQ_VOFF() ((l >> 2) * 6144 + (((l & 3) ^ ((((l >> 4) ^ (l >> 5)) & 1) << 1)) << 4))
#define WQ_PIECE(t, j, dst) dma16(rwq, wqv, ((((t) + rot) & 7) * 128 + ((j) & 7) * 16) * 6144 + ((j) >> 3) * 2048 + h * 64, lbase + (dst) + (j) * 1024)
#define WQ_BOFF(dt) ((g >> 1) * 1024 + (8 * (g & 1) + ((l & 15) >> 2)) * 64 + (l & 3) * 8 + (((dt) ^ (g & 1)) << 5))
#define X_REQ(u, hf) dma16(rx, xv[hf], (p * 256 + rt * 32 + ((u) & 1) * 16 + (hf) * 8) * 2048 + ((((u) >> 2) + rot) & 7) * 256 + (((u) >> 1) & 1) * 128, lbase + rt * 2048 + x_slot(u) + (hf) * 1024)
#define WO_PIECE(j) dma16(rwo, wov, ((w >> 2) * 64 + (w & 3) + 4 * (j)) * 16384 + ct * 128, lbase + 32768 + (j) * 8192 + w * 1024)
#define WD_DMA() _Pragma("unroll") for (int i = 0; i < 4; ++i) WD_DMA1(i)
#define WD_DMA1(i)                                                                                   \
      dma16(rwd, (l >> 4) * 2048 + (l & 15) * 16, (unsigned)(slice * 16 + 4 * (i)) * 2048 + w * 256, lbase + L_DN + w * 4096 + (i) * 1024)
#define FFN_DMA(m) dma16((m) & 1 ? rwu : rwg, fnv, (unsigned)(((4 * w + ((m) >> 1)) * 32) * F_ + slice * 16) * 2, lbase + 65536 + (m) * 8192 + w * 1024)
#define POISON_BIG(L)                                                                                \
  if (w >= 2) {                                                                                      \
    unsigned pz1 = POISON;                                                                           \
    asm volatile("" : "+v"(pz1));                                                                    \
    st16(u32x4{pz1, pz1, pz1, pz1}, rs, (w >= 4 ? OFF_PART + (L) * (1u << 20) + bid * 4096 - 4096 : OFF_STAT + (L) * 524288 + (p * 32 + h) * 2048 - 2048) + tid * 16, 0); \
  }
  u32x4 gv[4], uv[4];
  u32x4 keep[9];
  u32x4 pre[4][2];
#define PRE_LOAD(it_, rot_, s0, s1)                                                                  \
  static_for<(s1) - (s0)>([&](auto c_) __attribute__((always_inline)) {                              \
    constexpr int s = (s0) + decltype(c_)::value;                                                    \
    const unsigned ab_ = OFF_XN + (it_) * (4u << 20) + (((rot_) + (s >> 2)) & 7) * 8192 + ((s >> 1) & 1) * 4096; \
    ld16w<2 * s, (s & 1) * 2048>(pre[s][0], rs, l * 16, ab_ + (p * 8 + rt) * 65536);                         \
    ld16w<2 * s + 1, (s & 1) * 2048 + 1024>(pre[s][1], rs, l * 16, ab_ + (p * 8 + rt) * 65536);                 \
  })
  u32x2 T;
  const GAS bf16* tsrc;
  unsigned xv[2], xa[2];
  u32x2 ep;
#define X_READ(u, d0, d1) { d0 = *(const LDS_AS u32x4*)(size_t)(xa[0] + x_slot(u)); d1 = *(const LDS_AS u32x4*)(size_t)(xa[1] + x_slot(u)); }
  const rsrc_t rx = RSRC(x, 2048 * 1024 * 2);
  {
    IDS();
    const int rot = PROT();
    const bf16* pwq = wqkv;
    const unsigned wqv = WQ_VOFF();
    xv[0] = (l >> 3) * 2048 + (((l & 7) ^ (l >> 4)) << 4); xv[1] = xv[0] ^ 64;
#define WQ_KMAJ(q) (((q) % 3) * 8 + (q) / 3)
    WQ_PIECE(0, WQ_KMAJ(w), L_RING);
    X_REQ(0, 0); X_REQ(0, 1); X_REQ(1, 0); X_REQ(1, 1);
    WQ_PIECE(0, WQ_KMAJ(8 + w), L_RING);
    asm volatile("s_barrier" ::: "memory");
    WQ_PIECE(0, WQ_KMAJ(16 + w), L_RING);
    X_REQ(2, 0); X_REQ(2, 1);
    asm volatile("buffer_load_dwordx2 %0, off, %1, 0 sc1" : "=v"(ep) : "s"(rs) : "memory");
    if constexpr (PBS < 0) { POISON_BIG(0); }
    {
      ARGP(pwo, 3); ARGP(pwg, 5); ARGP(pwu, 6);
      const int t = tid < 288 ? tid - 64 : tid - 288, go = (bi * 32 + t / 7) * F_ + xcd * 448 + (t % 7) * 64;
      tsrc = tid < 64 ? pwo + bid * 4096 + tid * 64 : tid < 288 ? pwg + go : pwu + go;
      asm volatile("" : "+v"(tsrc));
    }
#pragma unroll
    for (int kk = 0; kk < 2; ++kk) {
      const int r = l & 15;
      xa[kk] = lbase + rt * 2048 + (r >> 3) * 1024 + (r & 7) * 128 + (((4 * kk + g) ^ (r >> 1)) << 4);
      asm volatile("" : "+v"(xa[kk]));
    }
    if (tid == 192) *(LDS_AS u32x4*)(smem + L_QC) = u32x4{0u, 0u, 0u, 0u};
    wait_vm<4>();
    X_READ(0, pre[0][0], pre[1][0]); X_READ(1, pre[0][1], pre[1][1]);
    asm volatile("s_waitcnt lgkmcnt(0)\n\ts_barrier" : "+v"(pre[0][0]), "+v"(pre[0][1]), "+v"(pre[1][0]), "+v"(pre[1][1])::"memory");
    X_REQ(3, 0); X_REQ(3, 1); X_REQ(4, 0); X_REQ(4, 1);
  }
  unsigned sel0 = 0x7FFFFFFFu;
  f32x2 v[8];
  u32x4 x2p[2];
  float wsum;

  auto layer = [&](auto it_c) __attribute__((always_inline)) {
    constexpr int it = decltype(it_c)::value;
    f32x4 acc[2][6];
    f32x2 qs2[2];
    u32x4 k0, k1, v0, v1;
    unsigned tdum = 0;
    {
      IDS();
      bool aff = false;
      unsigned aq[2];
      u32x4 av[2];
#define PIN_ACC() asm volatile("" : "+{v[176:179]}"(acc[0][0]), "+{v[180:183]}"(acc[0][1]), "+{v[184:187]}"(acc[0][2]), "+{v[188:191]}"(acc[0][3]), "+{v[192:195]}"(acc[0][4]), "+{v[196:199]}"(acc[0][5]), \
                               "+{v[200:203]}"(acc[1][0]), "+{v[204:207]}"(acc[1][1]), "+{v[208:211]}"(acc[1][2]), "+{v[212:215]}"(acc[1][3]), "+{v[216:219]}"(acc[1][4]), "+{v[220:223]}"(acc[1][5]))
#define PINV4(x) asm volatile("" : "+v"(x[0]), "+v"(x[1]), "+v"(x[2]), "+v"(x[3])::"memory")
      auto qkv_loop = [&](auto it_c) __attribute__((always_inline)) {
        constexpr int IT = decltype(it_c)::value;
        const unsigned abase = OFF_XN + IT * (4u << 20) + (p * 8 + rt) * 65536;
        const int rot = PROT() + 5 * IT;
        unsigned fkey = 0, kv, bq[2];
        u32x4 fx[4], bv[2], af[WA][2];
        u32x2 cxn, cx1, hh;
        f32x4 sa, sb, cs4[2];
        f32x2 b2[2], sv[8];
        s16x4 dfr[8];
#define AB_OFF(a) (OFF_AB + IT * 16384 + (p * 16 + 2 * rt + (a)) * 64)
#define A_LOAD1(k, a) ld16w<(k) % WA * 2 + (a), ((k) & 1) * 2048 + (a) * 1024>(af[(k) % WA][a], rs, l * 16, abase + ((((k) >> 2) + rot) & 7) * 8192 + (((k) >> 1) & 1) * 4096)
#pragma unroll
        for (int s = 0; s < 2 + 2 * IT; ++s) { af[s][0] = pre[s][0]; af[s][1] = pre[s][1]; }
        const bf16* pwq = wqkv;
        const int tstep = TS0 + bid % TSN;
        const unsigned wqv = WQ_VOFF();
#define QKV_DMA1(t, i)                                                                            \
  {                                                                                               \
    if constexpr (IT == 0) WQ_PIECE(t, 3 * w + (i), L_RING + SLOT(t) * STAGE);                    \
    else dma16s<(i) * 1024>(rs, l * 16, OFF_WI + (h * 8 + (((t) + rot) & 7)) * STAGE + w * 3072, lbase + L_RING + SLOT(t) * STAGE + w * 3072); \
  }
        unsigned bo[2][2];
#pragma unroll
        for (int dt = 0; dt < 2 - IT; ++dt) {
          bo[dt][0] = lbase + L_RING + (IT ? l * 16 : WQ_BOFF(dt)); bo[dt][1] = bo[dt][0] + 2 * STAGE;
          asm volatile("" : "+v"(bo[dt][0]), "+v"(bo[dt][1]));
        }
        auto frag0 = [&](int sl, int f) __attribute__((always_inline)) {
          const unsigned o = lbase + L_RING + (4 * g + ((l & 15) >> 2)) * 64 + (l & 3) * 8 + (((f ^ g ^ (g >> 1)) & 1) << 5) + sl * STAGE + ((f % 6) >> 1) * 8192 + (f / 6) * 2048;
          frag_t t;
          t.hf.a = trabs(o); t.hf.b = trabs(o + 1024);
          return t.v;
        };
        u32x4 df[4];
        auto dot_mma = [&](const u32x4 (&xf)[4]) __attribute__((always_inline)) {
          sa = mfma16(df[0], xf[0], f32x4{0.f, 0.f, 0.f, 0.f}); sb = mfma16(df[1], xf[1], f32x4{0.f, 0.f, 0.f, 0.f});
          sa = mfma16(df[2], xf[2], sa); sb = mfma16(df[3], xf[3], sb);
        };
        auto dot_store = [&](unsigned dst) __attribute__((always_inline)) {
          if ((l & 15) == 0) st16(unpoison4(__builtin_bit_cast(u32x4, sa + sb)), rs, g * 16, dst + ((h * 6 + w) * 8 + p) * 64);
        };
        auto tile_sum = [&](u32x4 pt, int dst) __attribute__((always_inline)) {
          f32x4 c = __builtin_bit_cast(f32x4, pt);
#pragma unroll
          for (int e = 0; e < 4; ++e) { c[e] += dppf<0x124>(c[e]); c[e] += dppf<0x128>(c[e]); c[e] = add_x16(c[e]); }
          if ((l & 28) == 0) *(LDS_AS f32x4*)(smem + dst + (w * 2 + (l >> 5)) * 64 + (l & 3) * 16) = c;
        };
        frag_t bfr[6];
#define QKV_BO(s, n) (bo[(n) & 1][SLOT((s) >> 2) >> 1] + (SLOT((s) >> 2) & 1) * STAGE + ((n) >> 1) * 8192 + ((s) & 3) * 2048)
#define QKV_RDA(s, n)                                                                             \
  {                                                                                               \
    if constexpr (IT == 0) bfr[n].hf.a = trabs(QKV_BO(s, n));                                     \
    else bfr[n].v = *(const LDS_AS u32x4*)(size_t)(bo[0][SLOT((s) >> 2) >> 1] + (SLOT((s) >> 2) & 1) * STAGE + (((s) & 3) * 6 + (n)) * 1024); \
  }
#define QKV_RDB(s, n) { if constexpr (IT == 0) bfr[n].hf.b = trabs(QKV_BO(s, (n) ^ 1) + 256); }
#pragma unroll
        for (int n = 0; n < 6; ++n) { QKV_RDA(0, n); if (n < 5) QKV_RDB(0, n); }
#define XF_OFF() (l & 8 ? ((l & 7) * RX + bi % RX) * 256 + h * 8 : (p * RX + (xcd >> 1) % RX) * 256 + (4 * rt + (l & 3)) * 8)
#define XP_OFF() (OFF_XNEW + ((rot + 5) & 7) * 256)
#define FP_OFF(i) (w == 3 ? OFF_XNEW + (i) * 1024 : OFF_FX + h * 3072 + w * 1024)
#define FX_OK(n) __all(((n) == 4 ? max(max(umax4(fx[0]), umax4(fx[1])), max(umax4(fx[2]), umax4(fx[3]))) : max(umax4(fx[0]), umax4(fx[1]))) != POISON)
#define SINCE(s0) (vm_at(IT, s, n) - vm_at(IT, s0, 5))
#define DEF4(x) asm volatile("" : "=v"(x[0]), "=v"(x[1]), "=v"(x[2]), "=v"(x[3]))
        LDS_AS unsigned* qcnt = (LDS_AS unsigned*)(smem + L_QC);
        unsigned qc;
        unsigned qadr = lbase + L_QC, qone = 1;
        asm volatile("" : "+v"(qadr), "+v"(qone));
#define count_in(i) asm volatile("s_mov_b64 exec, 1\n\tds_add_u32 %0, %1 offset:%2\n\ts_mov_b64 exec, -1" ::"v"(qadr), "v"(qone), "n"(4 * (i)) : "memory")
        auto count_wait = [&](int i, unsigned want) __attribute__((always_inline)) {
          if (__builtin_expect((unsigned)__builtin_amdgcn_readfirstlane(qc) < want, 0)) {
            while ((unsigned)__builtin_amdgcn_readfirstlane(__hip_atomic_load(qcnt + i, __ATOMIC_RELAXED, __HIP_MEMORY_SCOPE_WORKGROUP)) < want) {}
          }
          asm volatile("" ::: "memory");
        };
        unsigned long long xok = 0;
        unsigned xg;
        RowFin fin;
        auto x1_need = [&](unsigned long long m) __attribute__((always_inline)) {
          if (__builtin_expect((xok & m) != m, 0)) {
            for (unsigned spins = 0;;) {
              const u32x2 f = ld8(rs, F_X1 + (p * RX + (xcd >> 1) % RX) * 256 + (l & 31) * 8, 0);
              xok = __builtin_amdgcn_ballot_w64(f[0] == T[0] && f[1] == T[1]);
              if ((xok & m) == m) break;
              if (spin_fail(spins, rs, T)) { xok = ~0ull; break; }
              __builtin_amdgcn_s_sleep(KSL);
            }
            asm volatile("" ::: "memory");
          }
        };
        auto gap = [&](auto sc_, auto nc_) __attribute__((always_inline)) {
          constexpr int s = decltype(sc_)::value, n = decltype(nc_)::value;
          {
            constexpr int ts = (s + 3) / 4, tc = (s + 2) / 4, t0 = 1;
            if constexpr (ts >= 3 && ts < 8 && s == 4 * ts - 3 && n == 2) { wait_vm<vm_at(IT, s, n) - vm_tile(IT, ts)>(); count_in(0); }
            if constexpr (!IT && tc >= t0 && tc < 3 && s == 4 * tc - 2 && n == 1) { wait_vm<vm_at(IT, s, n) - vm_tile(IT, tc)>(); count_in(0); }
            if constexpr (IT && ts >= t0 && ts < 3 && s == 4 * ts - 3 && n == 2) count_in(0);
            if constexpr (tc >= t0 && tc < 8 && s == 4 * tc - 2) {
              if constexpr (n == 3) qc = __hip_atomic_load(qcnt, __ATOMIC_RELAXED, __HIP_MEMORY_SCOPE_WORKGROUP);
              if constexpr (n == 5) count_wait(0, 8 * (tc - t0 + 1));
            }
          }
          if constexpr (n == 0 && tile_piece(s)) QKV_DMA1(((s - 3) >> 2) + 3, (s - 3) & 3);
          if constexpr (IT == 0) {
            if constexpr (s == 0 && !(n & 1)) QKV_DMA1(1, n >> 1);
            if constexpr (s >= 1 && s < 4 && n == 2) QKV_DMA1(2, s - 1);
            constexpr int u = s + XL;
            if constexpr (n == 1 && u < 32) {
              wait_vm<vm_at(0, s, n) - vm_x(u)>();
              X_READ(u, af[(u & ~1) % WA][u & 1], af[(u | 1) % WA][u & 1]);
            }
            if constexpr (n == 3 && u + XU < 32) { asm volatile("s_waitcnt lgkmcnt(%0)" ::"n"(s + 1 < QL ? 4 : 3) : "memory"); X_REQ(u + XU, 0); }
            if constexpr (n == 5 && u + XU < 32) X_REQ(u + XU, 1);
            if constexpr (s == WTS && n == WTG) {
              const int i = bi * 192 + w * 24 + min(l, 23);
              touch(tdum, (const GAS bf16*)pwq + (size_t)(i / 12 + 512 * (~xcd & 1)) * 3072 + ((i % 12) >> 2) * 1024 + (xcd >> 1) * 256 + (i & 3) * 64);
            }
            if constexpr (s == PBS && n == 0) { POISON_BIG(0); }
            if constexpr (s == 0 && n == 1) count_in(2);
            if constexpr (s == 0 && n == 3) qc = __hip_atomic_load(qcnt + 2, __ATOMIC_RELAXED, __HIP_MEMORY_SCOPE_WORKGROUP);
            if constexpr (s == 0 && n == 5) count_wait(2, 8);
            if constexpr (s == 1 && n == 1) {
              asm volatile("" : "+v"(ep)::"memory");
              const unsigned long long e = (((unsigned long long)(unsigned)__builtin_amdgcn_readfirstlane(ep[1]) << 32) | (unsigned)__builtin_amdgcn_readfirstlane(ep[0])) + 1ull + dispatch_id() * 0x9E3779B97F4A7C16ull;
              T = u32x2{(unsigned)e, (unsigned)(e >> 32)};
            }
            const unsigned R = p * 256 + h * 8 + w;
            if constexpr (s == RWL && n == 4) ldrow(bv[0], bv[1], rx, l * 16, R * 2048);
            if constexpr (s == RWS && n == 0) {
              wait_vm<SINCE(RWL)>();
              asm volatile("" : "+v"(bv[0]), "+v"(bv[1])::"memory");
              unpack8v(bv[0], sv);
              pin4(sv);
            }
            if constexpr (s == RWS && n == 2) { unpack8v(bv[1], sv + 4); pin4(sv + 4); }
            if constexpr (s == RWS && n == 4) {
              b2[0][0] = hadd(((sv[0] + sv[1]) + (sv[2] + sv[3])) + ((sv[4] + sv[5]) + (sv[6] + sv[7])));
              asm volatile("" : "+v"(b2[0][0]));
            }
            if constexpr (s == RWS && n == 5) { const float m = sum64(b2[0][0]) * (1.0f / 1024.0f); b2[0] = f32x2{m, m}; asm volatile("" : "+v"(b2[0])); }
            if constexpr (s == RWS + 1 && n == 0) {
              f32x2 q0 = {0.f, 0.f}, q1 = {0.f, 0.f};
#pragma unroll
              for (int j = 0; j < 8; j += 2) {
                const f32x2 d0 = sv[j] - b2[0], d1 = sv[j + 1] - b2[0];
                q0 = __builtin_elementwise_fma(d0, d0, q0); q1 = __builtin_elementwise_fma(d1, d1, q1);
              }
              b2[1][0] = hadd(q0 + q1);
              asm volatile("" : "+v"(b2[1][0]));
            }
            if constexpr (s == RWS + 1 && n == 2) { b2[1][0] = sum64(b2[1][0]) * (1.0f / 1024.0f); asm volatile("" : "+v"(b2[1][0])); }
            if constexpr (s == RWS + 1 && n == 4) {
              const float rd = ln_rd(b2[1][0]);
              if (l < 2) st4(__builtin_bit_cast(unsigned, l == 0 ? rd : -rbf1(b2[0][0])), rs, OFF_AB + R * 4 + l * 8192, 0);
            }
            if constexpr (s == POI && n == 4) {
              unsigned pz1 = POISON;
              asm volatile("" : "+v"(pz1));
              const u32x4 pz = {pz1, pz1, pz1, pz1};
              const unsigned prb = 2 * p + (h >> 4), pct = h & 15;
              static_assert(OFF_CB == OFF_CA + 131072, "poison addressing");
              if (w == 4 && l < 32) st16(pz, rs, OFF_CA + (l & 8) * 4096 + (l & 16) * 8192 + prb * 2048 + pct * 128 + (l & 7) * 16, 0);
              if (w == 5 && l < 4) st16(pz, rs, OFF_AB + 16384 + (p * 32 + h) * 32 + (l & 1) * 16 + (l >> 1) * 8192, 0);
              if (w == 7 && l < 24) st16(pz, rs, OFF_FX + ((h * 6 + (l >> 2)) * 8 + p) * 64 + (l & 3) * 16, 0);
              if (w == 6 && l < 4) st4(pz1, rs, l < 2 ? OFF_XNEW + bid * 8 + l * 4 : OFF_KEY + ((l & 1) * 16 + pct) * 128 + prb * 4, 0);
            }
            static_assert(vm_x(EIN + XL) >= vm_at(0, EIN - 3, 5) && IMG >= 1 && CSD >= 1, "first flag");
            if constexpr (s == EIN && n == 1) count_in(1);
            if constexpr (s == EIN + 2 && n == 0) qc = __hip_atomic_load(qcnt + 1, __ATOMIC_RELAXED, __HIP_MEMORY_SCOPE_WORKGROUP);
            if constexpr (s == EIN + 2 && n == 2) {
              if (w == 0) { count_wait(1, 8); if (l < RX) st8(T, rs, F_XN + (p * RX + l) * 256 + h * 8, 0); }
            }
            if constexpr (s == ERQ && n == 4) asm volatile("buffer_load_dwordx2 %0, %1, %2, 0 offen sc1" : "=v"(cxn) : "v"(XF_OFF() + F_XN), "s"(rs) : "memory");
            if constexpr (s == CSL && n == 2) {
              wait_vm<SINCE(ERQ)>();
              asm volatile("" : "+v"(cxn)::"memory");
              if (!__all(cxn[0] == T[0] && cxn[1] == T[1])) poll_flags(rs, XF_OFF(), F_XN, T);
            }
          } else {
#define X1_OFF() (F_X1 + (p * RX + (xcd >> 1) % RX) * 256 + (l & 31) * 8)
            if constexpr ((s == 0 && n == 0) || (s == 2 && n == 2)) asm volatile("buffer_load_dwordx2 %0, %1, %2, 0 offen sc1" : "=v"(cxn) : "v"(X1_OFF()), "s"(rs) : "memory");
            if constexpr ((s == 1 && n == 3) || (s == 4 && n == 0)) {
              wait_vm<vm_at(1, s, n) - (s == 1 ? vm_at(1, 0, 1) : vm_at(1, 2, 3))>();
              asm volatile("" : "+v"(cxn)::"memory");
              xok |= __builtin_amdgcn_ballot_w64(cxn[0] == T[0] && cxn[1] == T[1]);
            }
            if constexpr (a_req(s, n) >= 0 && (a_req(s, n) & 7) == 0) x1_need(0x30003ull << (((rot + a_req(s, n) / 8) & 7) * 2));
            const unsigned R = (rb * 4 + (w & 3)) * 32 + (w >> 2) * 16 + (l & 15), so = OFF_STAT + rb * 32768;
            if constexpr (s == SPL && n == 4) {
#pragma unroll
              for (int i = 0; i < 4; ++i) ld16h<0>(fx[i], rs, (4 * g + i) * 2048 + (R & 127) * 16, so);
            }
            if constexpr (s == SPL + 2 && n == 1) {
              wait_vm<SINCE(SPL)>();
              PINV4(fx);
              fin = fin_key(rs, T, g * 8192 + (R & 127) * 16, so, smem + L_SKEY, l, w, R, wsum, fx);
              count_in(2);
            }
            if constexpr (s == SPL + 3 && n == 0) qc = __hip_atomic_load(qcnt + 2, __ATOMIC_RELAXED, __HIP_MEMORY_SCOPE_WORKGROUP);
            if constexpr (s == SPL + 3 && n == 1) { count_wait(2, 8); fin_cand(rs, smem + L_SKEY, 0, g, rb, ct, fin, v); }
            if constexpr (s == SPL + 3 && n == 3) {
              fin_x2(fin, v);
              x2p[0] = pack8v(v); x2p[1] = pack8v(v + 4);
              asm volatile("" : "+v"(x2p[0]), "+v"(x2p[1]));
            }
            if constexpr (s == SPL + 3 && n == 5) {
              if ((w & 3) * 2 + (w >> 2) == ct >> 1) {
                const float f = 1.0f + fin.rd, rd2 = ln_rd(fin.var * f * f), mb2 = rbf1(__builtin_fmaf(fin.mean - fin.mb, fin.rd, fin.mean));
                if (g < 2 && ((l >> 3) & 1) == (ct & 1))
                  st4(min(__builtin_bit_cast(unsigned, g == 0 ? f * rd2 : -__builtin_fmaf(fin.mb, fin.rd, mb2) * __builtin_amdgcn_rcpf(f)), POISON - 1), rs, OFF_AB + 16384 + R * 4 + g * 8192, 0);
              }
            }
          }
          if constexpr (IT && a_req(s, n) >= 0) A_LOAD1(a_req(s, n) / 2, a_req(s, n) & 1);
          if constexpr (IT == 0 && s == CSD) {
            if constexpr (n == 0) { DEF4(df); asm volatile("" : "=v"(sa), "=v"(sb)); if (w < 6) { for (int i = 0; i < 4; ++i) df[i] = frag0(SLOT(s >> 2), 6 * i + w); } }
            if constexpr (n == 2) {
              if (w < 6) {
                unsigned one2 = 0x3F803F80u;
                asm volatile("" : "+v"(one2));
                const u32x4 o4 = {one2, one2, one2, one2}, of[4] = {o4, o4, o4, o4};
                dot_mma(of);
              }
            }
            if constexpr (n == 4) { if (w < 6) dot_store(OFF_CS); }
          }
          if constexpr (!IT && s == CSL && n == 4) { asm volatile("" : "=v"(fx[0])); if (w < 3) ld16h<0>(fx[0], rs, l * 16, OFF_CS + h * 3072 + w * 1024); }
          if constexpr (!IT && s == CSS && n == 2) {
            if (w < 3) {
              wait_vm<SINCE(CSL)>();
              asm volatile("" : "+v"(fx[0])::"memory");
              tile_sum(fx[0], L_CSL);
            }
          }
          if constexpr (s == abl(IT) && n == 4) {
#pragma unroll
            for (int a = 0; a < 2; ++a) {
              ld4h<0>(aq[a], rs, (l & 15) * 4, AB_OFF(a)); ld4h<0>(bq[a], rs, (l & 15) * 4, AB_OFF(a) + 8192);
              ld16h<0>(av[a], rs, g * 16, AB_OFF(a)); ld16h<0>(bv[a], rs, g * 16, AB_OFF(a) + 8192);
            }
          }
          if constexpr (s == ABS && n == 2) {
#define AB_OK() __all(max(max(max(aq[0], aq[1]), max(bq[0], bq[1])), max(max(umax4(av[0]), umax4(av[1])), max(umax4(bv[0]), umax4(bv[1])))) != POISON)
            wait_vm<SINCE(abl(IT))>();
            asm volatile("" : "+v"(aq[0]), "+v"(aq[1]), "+v"(bq[0]), "+v"(bq[1]), "+v"(av[0]), "+v"(av[1]), "+v"(bv[0]), "+v"(bv[1])::"memory");
            if (IT && !AB_OK()) {
              unsigned spins = 0;
              do {
                if (spin_fail(spins, rs, T)) break;
#pragma unroll
                for (int a = 0; a < 2; ++a) {
                  aq[a] = ld4(rs, (l & 15) * 4, AB_OFF(a)); bq[a] = ld4(rs, (l & 15) * 4, AB_OFF(a) + 8192);
                  av[a] = ld16(rs, g * 16, AB_OFF(a)); bv[a] = ld16(rs, g * 16, AB_OFF(a) + 8192);
                }
              } while (!AB_OK());
            }
#undef AB_OK
          }
          if constexpr (s == ABS && n == 5) {
#pragma unroll
            for (int a = 0; a < 2; ++a) { b2[a] = f32x2{asf(bq[a]), asf(bq[a])}; asm volatile("" : "+v"(b2[a])); }
          }
          if constexpr (s == ABS + 1) {
            constexpr int k = (n + 4) % 6;
            if constexpr (k < 4) cs4[k & 1] = *(const LDS_AS f32x4*)(smem + L_CSL + k * 64 + g * 16);
            else cs4[k & 1][0] = *(const LDS_AS float*)(smem + L_CSL + k * 64 + (l & 15) * 4);
          }
          if constexpr ((s == ABS + 1 && n > 0) || (s == ABS + 2 && n == 0)) {
            constexpr int k = (n + 3) % 6;
            if constexpr (k < 4) {
#pragma unroll
              for (int a = 0; a < 2; ++a) acc[a][k] = __builtin_elementwise_fma(cs4[k & 1], f32x4{b2[a][0], b2[a][1], b2[a][0], b2[a][1]}, acc[a][k]);
            } else {
              f32x2 c2 = {cs4[k & 1][0], cs4[k & 1][0]};
              asm volatile("" : "+v"(c2));
#pragma unroll
              for (int a = 0; a < 2; ++a) acc[a][k] = __builtin_elementwise_fma(f32x4{c2[0], c2[1], c2[0], c2[1]}, __builtin_bit_cast(f32x4, bv[a]), acc[a][k]);
            }
          }
          if constexpr (IT == 0) {
            if constexpr (s >= KP0 && s < KP0 + 3 && (n & 1)) keep[3 * (s - KP0) + (n >> 1)] = frag0(SLOT(s - KP0 + 5), 3 * w + (n >> 1));
            if constexpr (s >= IMG && s < IMG + 3 && n == 4) st16(keep[s - IMG], rs, l * 16 + (s - IMG) * 1024, OFF_WI + (h * 8 + ((rot + 5) & 7)) * STAGE + w * 3072);
            if constexpr (s >= TS0 && s < TS0 + TSN && n == 4) {
              if (tstep == s && w < TW) touch(tdum, tsrc);
            }
            if constexpr (s == TST && n == 4) { if (w >= TW) touch(tdum, tsrc); }
          } else {
            if constexpr (s == 28 && n == 0) { POISON_BIG(1); }
            if constexpr (s == FSL - 2 && n == 4) ld4h<0>(kv, rs, (l & 15) * 4, OFF_KEY + (bid & 15) * 128);
            if constexpr (s == FSL && n == 4) {
              wait_vm<SINCE(FSL - 2)>();
              asm volatile("" : "+v"(kv)::"memory");
              xg = ffn_xoff(__builtin_amdgcn_readfirstlane(umax16(kv == POISON ? 0u : kv)), 0, w);
              ld4h<0>(kv, rs, (l & 15) * 4, OFF_KEY + (bid & 15) * 128);
              ld16h<0>(fx[0], rs, g * 16, xg); ld16h<64>(fx[1], rs, g * 16, xg); ld16h<128>(fx[2], rs, g * 16, xg); ld16h<192>(fx[3], rs, g * 16, xg);
            }
            if constexpr (s == FS0 && n == 1) {
              wait_vm<SINCE(FSL)>();
              asm volatile("" : "+v"(kv)::"memory");
              PINV4(fx);
              fkey = key_wait(rs, T, l, 0, bid & 15, kv);
              sel0 = key_row(fkey);
              aff = (int)(sel0 >> 8) == p;
            }
            if constexpr (s == FS0 && n == 2) {
              asm volatile("" : "=v"(sa), "=v"(sb));
              if (ffn_on) {
                if (xg != ffn_xoff(fkey, 0, w)) {
#pragma unroll
                  for (int i = 0; i < 4; ++i) fx[i] = ld16(rs, g * 16 + i * 64, ffn_xoff(fkey, 0, w));
                }
                ffn_gu(gv, uv, fx, sa, sb);
              }
            }
            if constexpr (s == FS0 && n == 3) {
              if (ffn_on) {
                ffn_gu_check(rs, T, ffn_xoff(fkey, 0, w), l, gv, uv, fx, sa, sb);
                ffn_gu_store(smem, l, w, sa, sb);
                count_in(1);
              }
            }
            if constexpr (s == FS0D) {
              if constexpr (n == 0) qc = __hip_atomic_load(qcnt + 1, __ATOMIC_RELAXED, __HIP_MEMORY_SCOPE_WORKGROUP);
              if constexpr (n == 1) {
                asm volatile("" : "=v"(dfr[0]), "=v"(dfr[1]), "=v"(dfr[2]), "=v"(dfr[3]), "=v"(dfr[4]), "=v"(dfr[5]), "=v"(dfr[6]), "=v"(dfr[7]), "=v"(hh), "=v"(sa));
                if (ffn_on) { count_wait(1, 8); ffn_d_read(smem, l, w, sa, dfr); }
              }
              if constexpr (n == 2) {
                if (ffn_on) {
                  for (int e = 0; e < 4; ++e) sa[e] = ffn_d_h(ffn_d_sum(sa[e]));
                  hh = u32x2{pack2(sa[0], sa[1]), pack2(sa[2], sa[3])};
                }
              }
              if constexpr (n == 3) {
                asm volatile("" : "=v"(sa), "=v"(sb));
                if (ffn_on) {
                  sa = f32x4{0.f, 0.f, 0.f, 0.f}; sb = sa;
#pragma unroll
                  for (int i = 0; i < 8; i += 2) { sa = ffn_d_mma(l, i, dfr[i], hh, sa); sb = ffn_d_mma(l, i + 1, dfr[i + 1], hh, sb); }
                }
              }
              if constexpr (n == 4) { if (ffn_on) ffn_d_store(rs, l, w, 0, slice, sa + sb); }
            }
            if constexpr (s == FS1 && n == 4) {
              DEF4(fx);
              asm volatile("" : "=v"(cxn), "=v"(cx1));
              if (!ffn_on) {
                const unsigned G = xcd * 4 + bi - 28, co = (sel0 >> 7) * 2048 + G * 64, jj = (tid & 3) * 16 + (tid & 4) * 2;
#pragma unroll
                for (int k = 0; k < 4; ++k) ld16h<0>(fx[k], rs, min(tid + 512 * k, 1791) * 16, OFF_PART + G * 32768);
                ld8h(cxn, rs, jj, OFF_CA + co); ld8h(cx1, rs, jj, OFF_CB + co);
              }
            }
            if constexpr (s == FS1S && n == 2) {
              if (!ffn_on) {
                wait_vm<SINCE(FS1)>();
                PINV4(fx);
                asm volatile("" : "+v"(cxn), "+v"(cx1)::"memory");
                red_sum(rs, smem, T, tid, w, 0, xcd * 4 + bi - 28, fkey, fx, cxn, cx1);
                BAR();
                if (w == 0) {
                  const u32x2 ov = red_final(smem, l, fkey, cxn, cx1);
                  if (l < 8) st8(unpoison2(ov), rs, OFF_XNEW + ((xcd * 4 + bi - 28) * 8 + (l & 3) * 2 + (l >> 2)) * 8, 0);
                }
              }
            }
            if constexpr (s == FXA && n == 4) { ld16h<0>(fx[0], rs, g * 16, XP_OFF()); ld16h<64>(fx[1], rs, g * 16, XP_OFF()); ld16h<128>(fx[2], rs, g * 16, XP_OFF()); ld16h<192>(fx[3], rs, g * 16, XP_OFF()); }
            if constexpr (s == FXB) {
#define FX_FRAG(i) (*(const LDS_AS u32x4*)(size_t)(bo[0][0] + SLOT(5) * STAGE + (6 * (i) + min(w, 5)) * 1024))
              if constexpr (n == 0) { df[0] = FX_FRAG(0); df[1] = FX_FRAG(1); }
              if constexpr (n == 1) {
                wait_vm<SINCE(FXA)>();
                PINV4(fx);
                sa = mfma16(df[0], fx[0], f32x4{0.f, 0.f, 0.f, 0.f}); sb = mfma16(df[1], fx[1], f32x4{0.f, 0.f, 0.f, 0.f});
                SB();
                df[0] = FX_FRAG(2); df[1] = FX_FRAG(3);
              }
              if constexpr (n == 2) { sa = mfma16(df[0], fx[2], sa); sb = mfma16(df[1], fx[3], sb); }
              if constexpr (n == 4) {
                const float z = sa[0] + sb[0];
                if (__any(z != z)) {
                  ffn_xwait(rs, T, XP_OFF(), l, fx);
                  f32x4 t0 = mfma16(FX_FRAG(0), fx[0], f32x4{0.f, 0.f, 0.f, 0.f});
                  t0 = mfma16(df[0], fx[2], t0);
                  f32x4 t1 = mfma16(FX_FRAG(1), fx[1], f32x4{0.f, 0.f, 0.f, 0.f});
                  sa = t0; sb = mfma16(df[1], fx[3], t1);
                }
                if (w < 6) dot_store(OFF_FX);
              }
            }
            if constexpr (s == FXL && n == 4) {
              asm volatile("" : "=v"(fx[0]), "=v"(fx[1]));
              if (aff && w < 4) { ld16h<0>(fx[0], rs, l * 16, FP_OFF(0)); ld16h<0>(fx[1], rs, l * 16, FP_OFF(1)); }
            }
            if constexpr (s == FXS && n == 4) {
              if (aff && w < 4) {
                wait_vm<0>();
                asm volatile("" : "+v"(fx[0]), "+v"(fx[1])::"memory");
                if (!FX_OK(2)) {
                  unsigned spins = 0;
                  do {
                    if (spin_fail(spins, rs, T)) break;
                    fx[0] = ld16(rs, l * 16, FP_OFF(0)); fx[1] = ld16(rs, l * 16, FP_OFF(1));
                  } while (!FX_OK(2));
                }
                if (w == 3) {
                  f32x2 nx[8];
                  unpack8v(fx[0], nx); unpack8v(fx[1], nx + 4);
                  const f32x2 sm = ((nx[0] + nx[1]) + (nx[2] + nx[3])) + ((nx[4] + nx[5]) + (nx[6] + nx[7]));
                  const float mean = sum64(hadd(sm)) * (1.0f / 1024.0f);
                  const f32x2 m2 = {mean, mean};
                  f32x2 q = {0.f, 0.f};
#pragma unroll
                  for (int j = 0; j < 8; ++j) { const f32x2 d = nx[j] - m2; q = __builtin_elementwise_fma(d, d, q); }
                  const float rd = ln_rd(sum64(hadd(q)) * (1.0f / 1024.0f));
                  if (l == 0) *(LDS_AS f32x2*)(smem + L_FXL + 384) = f32x2{rd, -rbf1(mean)};
                } else {
                  tile_sum(fx[0], L_FXL);
                }
                count_in(3);
              }
            }
          }
        };
        static_for<32>([&](auto sc_) __attribute__((always_inline)) {
          constexpr int s = decltype(sc_)::value;
          if constexpr (s > 0) PIN_ACC();
          if constexpr (IT == 1) wait_vm<vm_at(IT, s, 0) - vm_a(IT, s)>();
          pin_w<s % WA>(af[s % WA][0], af[s % WA][1]);
          SB();
          static_for<6>([&](auto nc_) __attribute__((always_inline)) {
            constexpr int n = decltype(nc_)::value;
            if constexpr (s == 0) { acc[0][n] = f32x4{0.f, 0.f, 0.f, 0.f}; acc[1][n] = acc[0][n]; }
            constexpr bool on = s < QL || n >= 2;
            if constexpr (on) acc[0][n] = n < 4 ? mfma16(bfr[n].v, af[s % WA][0], acc[0][n]) : mfma16(af[s % WA][0], bfr[n].v, acc[0][n]);
            SB();
            if constexpr (n == 0) { QKV_RDB(s, 5); }
            else if constexpr (s + 1 < 32 && (s + 1 < QL || n > 2)) QKV_RDB(s + 1, n - 1);
            SB();
            if constexpr (on) acc[1][n] = n < 4 ? mfma16(bfr[n].v, af[s % WA][1], acc[1][n]) : mfma16(af[s % WA][1], bfr[n].v, acc[1][n]);
            SB();
            if constexpr (s + 1 < 32 && (s + 1 < QL || n >= 2)) QKV_RDA(s + 1, n);
            SB();
            gap(sc_, nc_);
            SB();
          });
        });
        PIN_ACC();
        frag_t qw[32 - QL][2];
        static_for<32 - QL>([&](auto c_) __attribute__((always_inline)) {
          constexpr int s = QL + decltype(c_)::value;
          QKV_RDA(s, 0); QKV_RDB(s, 0); QKV_RDA(s, 1); QKV_RDB(s, 1);
          qw[s - QL][0] = bfr[0]; qw[s - QL][1] = bfr[1];
        });
        SB();
        const bool own = aff && (int)((sel0 >> 5) & 7) == rt;
        const bool m0 = !(sel0 & 16), mq = (l & 15) == (int)(sel0 & 15), mv = g == (int)((sel0 & 15) >> 2);
        auto patch_qk = [&](int n) __attribute__((always_inline)) {
          f32x4 t = *(const LDS_AS f32x4*)(smem + L_FXL + n * 64 + g * 16);
          const f32x4 c = *(const LDS_AS f32x4*)(smem + L_CSL + n * 64 + g * 16);
          const float nb = *(const LDS_AS float*)(smem + L_FXL + 388);
#pragma unroll
          for (int e = 0; e < 4; ++e) { t[e] = __builtin_fmaf(c[e], nb, t[e]); asm volatile("" : "+v"(t[e])); }
          acc[0][n] = (mq && m0) ? t : acc[0][n];
          acc[1][n] = (mq && !m0) ? t : acc[1][n];
        };
        if (own) {
          while (__hip_atomic_load(qcnt + 3, __ATOMIC_RELAXED, __HIP_MEMORY_SCOPE_WORKGROUP) < 4u) {}
          asm volatile("" ::: "memory");
          patch_qk(2); patch_qk(3);
          const f32x2 nab = *(const LDS_AS f32x2*)(smem + L_FXL + 384);
          const LDS_AS float* fl_ = (const LDS_AS float*)(smem + L_FXL + 256) + (l & 15);
          const LDS_AS float* cl_ = (const LDS_AS float*)(smem + L_CSL + 256) + (l & 15);
          const float t4 = __builtin_fmaf(cl_[0], nab[1], fl_[0]), t5 = __builtin_fmaf(cl_[16], nab[1], fl_[16]);
          const unsigned na = __builtin_bit_cast(unsigned, (float)nab[0]);
#pragma unroll
          for (int a = 0; a < 2; ++a) {
            const bool ma = a ? !m0 : m0;
            aq[a] = (mq && ma) ? na : aq[a];
#pragma unroll
            for (int e = 0; e < 4; ++e) {
              const bool me = mv && ma && e == (int)(sel0 & 3);
              acc[a][4][e] = me ? t4 : acc[a][4][e]; acc[a][5][e] = me ? t5 : acc[a][5][e];
              av[a][e] = me ? na : av[a][e];
            }
          }
        }
        constexpr float C2 = 1.4426950408889634f * 0.17677669529663687f;
#pragma unroll
        for (int a = 0; a < 2; ++a) {
          f32x2 a2 = {asf(aq[a]), asf(aq[a])};
          qs2[a] = f32x2{asf(aq[a]) * C2, asf(aq[a]) * C2};
          asm volatile("" : "+v"(a2), "+v"(qs2[a]));
          acc[a][2] *= f32x4{a2[0], a2[1], a2[0], a2[1]}; acc[a][3] *= f32x4{a2[0], a2[1], a2[0], a2[1]};
          acc[a][4] *= __builtin_bit_cast(f32x4, av[a]); acc[a][5] *= __builtin_bit_cast(f32x4, av[a]);
        }
        k0 = pack8(acc[0][2], acc[0][3]); k1 = pack8(acc[1][2], acc[1][3]); v0 = pack8(acc[0][4], acc[1][4]); v1 = pack8(acc[0][5], acc[1][5]);
        const unsigned kvo = OFF_KV + IT * (8u << 20) + (p * 32 + h) * 32768 + rt * 4096;
        if (p < 7) { st16(k0, rs, l * 16, kvo); st16(k1, rs, l * 16 + 1024, kvo); st16(v0, rs, l * 16 + 2048, kvo); st16(v1, rs, l * 16 + 3072, kvo); }
        char* ownp = smem + (8 + rt) * 2048 + l * 16;
        *(u32x4*)(ownp + L_K) = k0;
        if (rt < 7) *(u32x4*)(ownp + L_K + 1024) = k1;
        *(u32x4*)(ownp + L_V) = v0; *(u32x4*)(ownp + L_V + 1024) = v1;
        SB();
        static_for<32 - QL>([&](auto c_) __attribute__((always_inline)) {
          constexpr int s = QL + decltype(c_)::value;
#pragma unroll
          for (int n = 0; n < 2; ++n) { acc[0][n] = mfma16(qw[s - QL][n].v, af[s % WA][0], acc[0][n]); acc[1][n] = mfma16(qw[s - QL][n].v, af[s % WA][1], acc[1][n]); }
        });
        if (own) { patch_qk(0); patch_qk(1); }
      };
      qkv_loop(it_c);
    }
#undef A_LOAD1
#undef QKV_RDA
#undef QKV_RDB
#undef QKV_BO
    {
      IDS();
      const GAS bf16 *pwo, *pwg, *pwu;
      asm volatile("s_load_dwordx2 %0, %3, 24\n\ts_load_dwordx2 %1, %3, 40\n\ts_load_dwordx2 %2, %3, 48" : "=&s"(pwo), "=&s"(pwg), "=&s"(pwu) : "s"(ka));
      const int ql = l & 15;
      const f32x4 z4 = {0.f, 0.f, 0.f, 0.f}, ninf = {-INFINITY, -INFINITY, -INFINITY, -INFINITY};
      const unsigned kvo = OFF_KV + it * (8u << 20) + (p * 32 + h) * 32768 + rt * 4096;
      LDS_AS unsigned* rdy = (LDS_AS unsigned*)(smem + L_RDY);
      const unsigned rtag = T[0] + it;
      u32x4 qf[2];
      f32x4 cn[2], oa[2][2], sum[2], S[2][2];
      unsigned one2 = 0x3F803F80u;
      asm volatile("" : "+v"(one2));
      const u32x4 ones = {one2, one2, one2, one2};
      auto ex8 = [&](f32x4 a, f32x4 b) __attribute__((always_inline)) {
        return u32x4{pack2(__builtin_amdgcn_exp2f(a[0]), __builtin_amdgcn_exp2f(a[1])), pack2(__builtin_amdgcn_exp2f(a[2]), __builtin_amdgcn_exp2f(a[3])),
                     pack2(__builtin_amdgcn_exp2f(b[0]), __builtin_amdgcn_exp2f(b[1])), pack2(__builtin_amdgcn_exp2f(b[2]), __builtin_amdgcn_exp2f(b[3]))};
      };
      {
#pragma unroll
        for (int a = 0; a < 2; ++a) {
          const f32x4 c4 = {qs2[a][0], qs2[a][1], qs2[a][0], qs2[a][1]};
          qf[a] = pack8(acc[a][0] * c4, acc[a][1] * c4);
        }
        f32x4 mk;
#pragma unroll
        for (int e = 0; e < 4; ++e) mk[e] = (4 * g + e <= ql) ? 0.f : -INFINITY;
        const f32x4 d0 = mfma16(k0, qf[0], mk), d1 = mfma16(k1, qf[1], mk), d2 = mfma16(k0, qf[1], z4);
        const float m0 = -max_x32(max_x16(fmaxf(fmaxf(d0[0], d0[1]), fmaxf(d0[2], d0[3]))));
        const float m1 = -max_x32(max_x16(fmaxf(fmaxf(d1[0], d1[1]), fmaxf(d1[2], d1[3]))));
        cn[0] = f32x4{m0, m0, m0, m0}; cn[1] = f32x4{m1, m1, m1, m1};
        asm volatile("" : "+v"(cn[0]), "+v"(cn[1]));
        const f32x4 e0 = d0 + cn[0];
        const u32x4 p0 = {pack2(__builtin_amdgcn_exp2f(e0[0]), __builtin_amdgcn_exp2f(e0[1])), pack2(__builtin_amdgcn_exp2f(e0[2]), __builtin_amdgcn_exp2f(e0[3])), 0u, 0u};
        const u32x4 p1 = ex8(d2 + cn[1], d1 + cn[1]);
        oa[0][0] = mfma16(v0, p0, z4); oa[0][1] = mfma16(v1, p0, z4); sum[0] = mfma16(ones, p0, z4);
        oa[1][0] = mfma16(v0, p1, z4); oa[1][1] = mfma16(v1, p1, z4); sum[1] = mfma16(ones, p1, z4);
      }
      const char* kb_ = smem + L_K + l * 16;
      const char* vb_ = smem + L_V + l * 16;
      const unsigned wov = (l >> 3) * 2048 + (((((l & 7) >> 1) ^ ((l >> 4) & 3)) * 2 + (l & 1)) << 4), fnv = (l >> 1) * (F_ * 2) + (l & 1) * 16;
      const unsigned pp = __builtin_amdgcn_readfirstlane(p > 0 ? 32 : 0), pvo = kvo - pp * 32768;
      const unsigned pfo = __builtin_amdgcn_readfirstlane(F_KV + (it * 256 + p * 32 + h - pp) * 128 + rt * 8);
      asm volatile("" : "+v"(oa[0][0]), "+v"(oa[0][1]), "+v"(oa[1][0]), "+v"(oa[1][1]), "+v"(sum[0]), "+v"(sum[1]));
      DRAIN();
      asm volatile("" : "+v"(tdum));
      if (l == 0) st8(T, rs, rt * 8, F_KV + (it * 256 + p * 32 + h) * 128);
      BAR();
      asm volatile("" : "+s"(pwo), "+s"(pwg), "+s"(pwu));
      WO_PIECE(15); WO_PIECE(14);
#define KV_FETCH()                                                                                \
  {                                                                                               \
    dma16s<0>(rs, l * 16, pvo, lbase + L_K + rt * 2048); dma16s<1024>(rs, l * 16, pvo, lbase + L_K + rt * 2048);               \
    dma16s<0>(rs, l * 16, pvo + 2048, lbase + L_V + rt * 2048); dma16s<1024>(rs, l * 16, pvo + 2048, lbase + L_V + rt * 2048); \
  }
#define LDS16(a_) (*(const LDS_AS u32x4*)(size_t)(a_))
      u32x4 kf[2], vf[2];
      int nw = 0;
      auto body = [&](unsigned kad, unsigned vad) __attribute__((always_inline)) {
        u32x4 p0 = ex8(S[0][0], S[0][1]);
        asm volatile("" : "+v"(p0));
        SB();
        S[0][0] = mfma16(kf[0], qf[0], cn[0]); S[0][1] = mfma16(kf[1], qf[0], cn[0]);
        SB();
        oa[0][0] = mfma16(vf[0], p0, oa[0][0]); oa[0][1] = mfma16(vf[1], p0, oa[0][1]); sum[0] = mfma16(ones, p0, sum[0]);
        SB();
        u32x4 p1 = ex8(S[1][0], S[1][1]);
        asm volatile("" : "+v"(p1));
        SB();
        S[1][0] = mfma16(kf[0], qf[1], cn[1]); S[1][1] = mfma16(kf[1], qf[1], cn[1]);
        SB();
        kf[0] = LDS16(kad); kf[1] = LDS16(kad + 1024);
        SB();
        oa[1][0] = mfma16(vf[0], p1, oa[1][0]); oa[1][1] = mfma16(vf[1], p1, oa[1][1]);
        SB();
        vf[0] = LDS16(vad + L_V); vf[1] = LDS16(vad + L_V + 1024);
        SB();
        sum[1] = mfma16(ones, p1, sum[1]);
      };
      auto last = [&]() __attribute__((always_inline)) {
        const u32x4 p0 = ex8(S[0][0], S[0][1]), p1 = ex8(S[1][0], S[1][1]);
        oa[0][0] = mfma16(vf[0], p0, oa[0][0]); oa[0][1] = mfma16(vf[1], p0, oa[0][1]); sum[0] = mfma16(ones, p0, sum[0]);
        oa[1][0] = mfma16(vf[0], p1, oa[1][0]); oa[1][1] = mfma16(vf[1], p1, oa[1][1]); sum[1] = mfma16(ones, p1, sum[1]);
      };
      auto piece = [&]() __attribute__((always_inline)) {
        if constexpr (it == 1) { WO_PIECE(13 - nw); }
        else if (ffn_on) { FFN_DMA(nw); }
        ++nw;
      };
      if (rt > 0) {
        unsigned ad = lbase + l * 16 + (7 + rt) * 2048;
        kf[0] = LDS16(ad); kf[1] = LDS16(ad + 1024); vf[0] = LDS16(ad + L_V); vf[1] = LDS16(ad + L_V + 1024);
        S[0][0] = mfma16(kf[0], qf[0], cn[0]); S[0][1] = mfma16(kf[1], qf[0], cn[0]); S[1][0] = mfma16(kf[0], qf[1], cn[1]); S[1][1] = mfma16(kf[1], qf[1], cn[1]);
        SB();
        ad -= 2048;
        kf[0] = LDS16(ad); kf[1] = LDS16(ad + 1024);
#pragma unroll 1
        for (int i = rt - 1; i > 0; --i) {
          body(ad - 2048, ad);
          piece();
          SB();
          ad -= 2048;
        }
        piece();
        last();
      }
      {
        poll_flags(rs, 0, pfo, T);
        KV_FETCH();
        if (p == 0) { cn[0] = ninf; cn[1] = ninf; }
        f32x4 mk;
#pragma unroll
        for (int e = 0; e < 4; ++e) mk[e] = (4 * g + e > ql) ? 0.f : -INFINITY;
        const f32x4 cm0 = cn[0] + mk, cm1 = cn[1] + mk;
        DRAIN();
        if (l == 0) rdy[rt] = rtag;
        if constexpr (it == 1) { WO_PIECE(6); WO_PIECE(5); WO_PIECE(4); }
        else { WO_PIECE(13); WO_PIECE(12); if (ffn_on) FFN_DMA(7); }

        int have = rt;
        auto need = [&](int gq) __attribute__((always_inline)) {
          if (__builtin_expect(gq > have && gq < 8, 0)) {
            do {
              const unsigned t = __hip_atomic_load(rdy + (l & 7), __ATOMIC_RELAXED, __HIP_MEMORY_SCOPE_WORKGROUP);
              have = rt - 1 + __builtin_ctz(~((unsigned)__builtin_amdgcn_ballot_w64(t == rtag) & 0xffu) >> rt);
            } while (gq > have);
            asm volatile("" ::: "memory");
          }
        };
        unsigned ad = lbase + l * 16 + rt * 2048;
        kf[0] = LDS16(ad); kf[1] = LDS16(ad + 1024); vf[0] = LDS16(ad + L_V); vf[1] = LDS16(ad + L_V + 1024);
        S[0][0] = mfma16(kf[0], qf[0], cm0); S[0][1] = mfma16(kf[1], qf[0], cn[0]); S[1][0] = ninf; S[1][1] = mfma16(kf[1], qf[1], cm1);
        SB();
        ad += 2048;
        need(rt + 1);
        kf[0] = LDS16(ad); kf[1] = LDS16(ad + 1024);
#pragma unroll 1
        for (int gq = rt + 1; gq < 8; ++gq) {
          need(gq + 1);
          body(ad + 2048, ad);
          piece();
          SB();
          ad += 2048;
        }
        last();
      }
      {
        float big = 0.f;
#pragma unroll
        for (int a = 0; a < 2; ++a)
#pragma unroll
          for (int d = 0; d < 2; ++d)
            big = __builtin_elementwise_maximum(__builtin_elementwise_maximum(big, __builtin_elementwise_maximum(fabsf(oa[a][d][0]), fabsf(oa[a][d][1]))),
                                                __builtin_elementwise_maximum(fabsf(oa[a][d][2]), fabsf(oa[a][d][3])));
        bool ok = sum[0][0] >= 0.5f && sum[0][0] < 0x1p64f && sum[1][0] >= 0.5f && sum[1][0] < 0x1p64f && big < INFINITY;
        if (!__all(ok)) {
#pragma unroll
          for (int a = 0; a < 2; ++a) {
            const int qs = 2 * rt + a, qq = 256 + 16 * qs + ql, klo = max(qq - 255, p > 0 ? 0 : 256);
            auto score = [&](int t) __attribute__((always_inline)) {
              f32x4 s = mfma16(t == 31 ? k1 : *(const u32x4*)(kb_ + t * 1024), qf[a], z4);
#pragma unroll
              for (int e = 0; e < 4; ++e) { const int kk = 16 * t + 4 * g + e; s[e] = (kk <= qq && kk >= klo) ? s[e] : -INFINITY; }
              return s;
            };
            float mx = -INFINITY;
#pragma unroll 1
            for (int t = qs; t <= qs + 16; ++t) { const f32x4 s = score(t); mx = fmaxf(fmaxf(mx, fmaxf(s[0], s[1])), fmaxf(s[2], s[3])); }
            mx = max_x32(max_x16(mx));
            oa[a][0] = z4; oa[a][1] = z4; sum[a] = z4;
#pragma unroll 1
            for (int G = qs >> 1; G <= (qs + 16) >> 1; ++G) {
              const f32x4 s0 = score(2 * G), s1 = score(2 * G + 1);
              f32x4 e0, e1;
#pragma unroll
              for (int e = 0; e < 4; ++e) { e0[e] = __builtin_amdgcn_exp2f(s0[e] - mx); e1[e] = __builtin_amdgcn_exp2f(s1[e] - mx); }
              const u32x4 pf = pack8(e0, e1);
              oa[a][0] = mfma16(*(const u32x4*)(vb_ + G * 2048), pf, oa[a][0]);
              oa[a][1] = mfma16(*(const u32x4*)(vb_ + G * 2048 + 1024), pf, oa[a][1]);
              sum[a] = mfma16(ones, pf, sum[a]);
            }
          }
        }
      }
#pragma unroll
      for (int a = 0; a < 2; ++a) {
        const float inv = __builtin_amdgcn_rcpf(sum[a][0]);
        oa[a][0] *= inv; oa[a][1] *= inv;
      }
      const unsigned oo = OFF_O + it * (4u << 20) + ((p * 8 + rt) * 64 + h * 2) * 1024;
      st16(pack8(oa[0][0], oa[0][1]), rs, l * 16, oo);
      st16(pack8(oa[1][0], oa[1][1]), rs, l * 16 + 1024, oo);
      DRAIN();
      BAR();
      if (tid == 0) st8(T, rs, h * 8, F_O + (it * 8 + p) * 256);
      if constexpr (it == 0) {
        DEF4(gv); DEF4(uv);
        if (ffn_on) {
          const unsigned ro = 65536 + w * 1024 + (4 * g + ((l & 15) >> 2)) * 32 + (l & 3) * 8;
#pragma unroll
          for (int i = 0; i < 4; ++i) {
            frag_t f;
            f.hf.a = trread(smem, ro + i * 16384); f.hf.b = trread(smem, ro + i * 16384 + 512);
            gv[i] = f.v;
            f.hf.a = trread(smem, ro + i * 16384 + 8192); f.hf.b = trread(smem, ro + i * 16384 + 8704);
            uv[i] = f.v;
          }
        }
        PINV4(gv); PINV4(uv);
        {
          ARGP(px, 0);
          const unsigned xo = ((rb * 4 + (w & 3)) * 32 + (w >> 2) * 16) * 2048 + ct * 128, xw = (l >> 3) * 2048 + (((l & 7) ^ g) << 4);
          dma16(RSRC(px, 2048 * 1024 * 2), xw, xo, lbase + w * 4096); dma16(RSRC(px, 2048 * 1024 * 2), xw ^ 64, xo + 16384, lbase + w * 4096 + 1024);
        }
        WO_PIECE(11); WO_PIECE(10); WO_PIECE(9); WO_PIECE(8); WO_PIECE(7); WO_PIECE(6); WO_PIECE(5); WO_PIECE(4);
      }
      WO_PIECE(3); WO_PIECE(2); WO_PIECE(1); WO_PIECE(0);
    }
    IDS();
    ARGP(pwr, 4); ARGP(pwd, 7); ARGP(pout, 8);
    const int kh = w >> 2, rt2 = rb * 4 + (w & 3);
    const unsigned R = rt2 * 32 + kh * 16 + (l & 15);
    const unsigned to = (rt2 * 64 + ct * 4 + kh) * 1024 + l * 16;
    const unsigned rmo = ct * 64 + ((g & 1) ? 12 + 4 * g : 4 * g);
    f32x2 wv[8];
    {
      u32x4 wa[2], wrp[2], res[2];
#pragma unroll
      for (int j = 0; j < 2; ++j) {
        wrp[j] = *(const GAS u32x4*)(pwr + rmo + 32 * j);
        wa[j] = *(const GAS u32x4*)(pwr + l * 8 + j * 512);
      }
      {
        f32x2 t[8];
        unpack8v(wa[0], t); unpack8v(wa[1], t + 4);
        const f32x2 s = ((t[0] + t[1]) + (t[2] + t[3])) + ((t[4] + t[5]) + (t[6] + t[7]));
        wsum = sum64(hadd(s));
        unpack8v(trade16(wrp[0]), wv); unpack8v(trade16(wrp[1]), wv + 4);
        asm volatile("" : "+v"(wsum), "+v"(wv[0]), "+v"(wv[1]), "+v"(wv[2]), "+v"(wv[3]), "+v"(wv[4]), "+v"(wv[5]), "+v"(wv[6]), "+v"(wv[7]));
      }
      unsigned bt[4];
#pragma unroll
      for (int n = 0; n < 4; ++n) {
        bt[n] = lbase + 32768 + 65536 + kh * 4096 + (4 * g + ((l & 15) >> 2)) * 128 + (l & 3) * 8 + ((n ^ (2 * (g & 1) + ((l & 15) >> 3))) << 5);
        asm volatile("" : "+v"(bt[n]));
      }
      if (w == 0) poll_flags(rs, (l & 31) * 8, F_O + (it * 8 + (rb >> 1)) * 256, T);
      else if (it == 0 && w < 4) poll_flags2(rs, w < 3 ? F_XN + (((w - 1) * 4 + g) * RX + bi % RX) * 256 + (l & 15) * 16 : F_KV + (((l >> 2) & 7) * 32 + h) * 128 + (l & 3) * 16, T);
      asm volatile("s_barrier" ::: "memory");
      const unsigned abase = OFF_O + it * (4u << 20) + (rt2 * 64 + 32 * kh) * 1024;
      const unsigned av0 = l * 16 + kh * 1024, av1 = l * 16 + (kh ^ 1) * 1024;
      u32x4 af[WB][2];
#define O_LOAD(s_) { af[(s_) % WB][0] = ld16(rs, av0 + (~(s_) & 1) * 2048, abase + ((15 - (s_)) >> 1) * 4096); af[(s_) % WB][1] = ld16(rs, av1 + (~(s_) & 1) * 2048, abase + ((15 - (s_)) >> 1) * 4096); }
#pragma unroll
      for (int s = 0; s < WR; ++s) O_LOAD(s);
#pragma unroll
      for (int j = 0; j < 2 * it; ++j) res[j] = ld16(rs, OFF_XNEW + ((2 * ct + j) * 4 + g) * 16, 0);
      SB();
      f32x4 ac[2][4];
#pragma unroll
      for (int a = 0; a < 2; ++a)
#pragma unroll
        for (int n = 0; n < 4; ++n) ac[a][n] = f32x4{0.f, 0.f, 0.f, 0.f};
      u32x4 bfr[2][4];
#define WO_RD(buf, s, n)                                                                             \
  {                                                                                                  \
    const unsigned o_ = bt[n] + ((15 - (s)) & 7) * 8192;                                             \
    frag_t f_;                                                                                       \
    f_.hf.a = trabs(o_); f_.hf.b = trabs(o_ + 2048);                                                 \
    bfr[buf][n] = f_.v;                                                                              \
  }
#pragma unroll
      for (int n = 0; n < 4; ++n) WO_RD(0, 0, n);
#pragma unroll
      for (int s = 0; s < 16; ++s) {
        const int cur = s & 1;
        asm volatile("" : "+{v[176:179]}"(ac[0][0]), "+{v[180:183]}"(ac[0][1]), "+{v[184:187]}"(ac[0][2]), "+{v[188:191]}"(ac[0][3]),
                          "+{v[192:195]}"(ac[1][0]), "+{v[196:199]}"(ac[1][1]), "+{v[200:203]}"(ac[1][2]), "+{v[204:207]}"(ac[1][3]));
        if (s == (it == 0 ? 3 : 11)) asm volatile("s_barrier" ::: "memory");
        if (s == 7) {
#pragma unroll
          for (int n = 0; n < 4; ++n) { bt[n] -= 65536; asm volatile("" : "+v"(bt[n])); }
        }
        if (s < WB - WR) O_LOAD(WR + 2 * s < 16 ? WR + 2 * s : 0);
        SB();
#pragma unroll
        for (int n = 0; n < 4; ++n) {
          ac[0][n] = mfma16(bfr[cur][n], af[s % WB][0], ac[0][n]);
          ac[1][n] = mfma16(bfr[cur][n], af[s % WB][1], ac[1][n]);
          if (s + 1 < 16) WO_RD(cur ^ 1, s + 1, n);
          if (it == 0 && s == 8) {
            const u32x2 t = *(const LDS_AS u32x2*)(smem + w * 4096 + (l & 15) * 128 + (((2 * n + (g >> 1)) ^ ((l & 15) >> 1)) << 4) + (g & 1) * 8);
            res[n >> 1][2 * (n & 1)] = t[0]; res[n >> 1][2 * (n & 1) + 1] = t[1];
          }
          if (s == 9) {
            if (it == 1 && n == 0) { res[0] = R == sel0 ? res[0] : x2p[0]; res[1] = R == sel0 ? res[1] : x2p[1]; }

            v[2 * n] = f32x2{lo16(res[n >> 1][2 * (n & 1)]), hi16(res[n >> 1][2 * (n & 1)])};
            v[2 * n + 1] = f32x2{lo16(res[n >> 1][2 * (n & 1) + 1]), hi16(res[n >> 1][2 * (n & 1) + 1])};
            asm volatile("" : "+v"(v[2 * n]), "+v"(v[2 * n + 1]));
          }
          SB();
        }
        if (s < WB - WR) { O_LOAD(WR + 2 * s + 1); }
        else if (s + WB < 16) { O_LOAD(s + WB); }
        SB();
      }
#undef WO_RD
#undef O_LOAD
#define XCH_AT(v) ((v) == 7 ? 155648 : (v) * 4096)
#pragma unroll
      for (int n = 0; n < 4; ++n) *(f32x4*)(smem + XCH_AT(w) + n * 1024 + l * 16) = ac[1][n];
#pragma unroll
      for (int n = 0; n < 4; ++n) { v[2 * n] += f32x2{ac[0][n][0], ac[0][n][1]}; v[2 * n + 1] += f32x2{ac[0][n][2], ac[0][n][3]}; }
      BAR();
#pragma unroll
      for (int n = 0; n < 4; ++n) {
        const f32x4 o = *(const f32x4*)(smem + XCH_AT(w ^ 4) + n * 1024 + l * 16);
        v[2 * n] += f32x2{o[0], o[1]}; v[2 * n + 1] += f32x2{o[2], o[3]};
      }
    }
    {
      if (it == 0) { st16(pack8v(v), rs, to, OFF_XN + (4u << 20)); st16(pack8v(v + 4), rs, to + 2048, OFF_XN + (4u << 20)); }
      SB();
      const f32x2 s = ((v[0] + v[1]) + (v[2] + v[3])) + ((v[4] + v[5]) + (v[6] + v[7]));
      f32x2 dp = v[0] * wv[0], q = v[0] * v[0];
#pragma unroll
      for (int j = 1; j < 8; ++j) { dp = __builtin_elementwise_fma(v[j], wv[j], dp); q = __builtin_elementwise_fma(v[j], v[j], q); }
      float mt = hadd(s), qq = hadd(q), dot = hadd(dp);
      add_g3(mt, qq, dot);
      const unsigned so = OFF_STAT + it * 524288 + rb * 32768, lrow = R & 127;
      if (g == 0) st16(unpoison4(__builtin_bit_cast(u32x4, f32x4{mt, qq, dot, 0.f})), rs, ct * 2048 + lrow * 16, so);
      SB();
      if (it == 0) wait_vm<1>();
      if constexpr (it == 0) {
        volatile LDS_AS u32x2* in = (volatile LDS_AS u32x2*)(smem + L_XC);
        if (l == 0) in[w] = T;
        const u32x2 t = in[l & 7];
        if (__all(t[0] == T[0] && t[1] == T[1]) && l < RX) st8(T, rs, l * 256 + ((rb & 1) * 16 + ct) * 8, F_X1 + (it * 8 + (rb >> 1)) * RX * 256);
      }
      if (ffn_on) { WD_DMA(); }
      if (it == 0) {
#pragma unroll
        for (int j = 0; j < 9; ++j) *(u32x4*)(smem + L_RING + (j / 3) * STAGE + (w * 3 + j % 3) * 1024 + l * 16) = keep[j];
      }
      if constexpr (it == 0) {
        if (w == 0) poll_flags(rs, ((l & 2) * 8 + ((PROT() + 5) & 7) * 2 + (l & 1)) * 8, F_X1 + (p * RX + (xcd >> 1) % RX) * 256, T);
        if (tid == 192) *(LDS_AS u32x4*)(smem + L_QC) = u32x4{0u, 0u, 0u, 0u};
        BAR();
        PRE_LOAD(1, PROT() + 5, 0, 4);
      } else {
        __builtin_amdgcn_s_sleep(SDIR);
        u32x4 sp[4];
#pragma unroll
        for (int i = 0; i < 4; ++i) sp[i] = ld16(rs, (4 * g + i) * 2048 + lrow * 16, so);
        const RowFin r = fin_key(rs, T, g * 8192 + lrow * 16, so, smem + L_XC, l, w, R, wsum, sp);
        BAR();
        fin_cand(rs, smem + L_XC, 1, g, rb, ct, r, v);
        fin_x2(r, v);
      }
    }
    if (it == 1) {
      const u32x4 op0 = trade16(pack8v(v)), op1 = trade16(pack8v(v + 4));
#define OUT_STORE()                                                                                 \
  if (R != key_row(key)) {                                                                          \
    __builtin_amdgcn_raw_buffer_store_b128(op0, rout, (R * 1024 + rmo) * 2, 0, SC1);                  \
    __builtin_amdgcn_raw_buffer_store_b128(op1, rout, (R * 1024 + rmo + 32) * 2, 0, SC1);             \
  }
      u32x4 xv[4];
      unsigned guess = 0;
      {
        __builtin_amdgcn_s_sleep(GSL);
        const unsigned k_ = key_load(rs, l, 1, bid & 15);
        guess = ffn_xoff(__builtin_amdgcn_readfirstlane(umax16(k_ == POISON ? 0u : k_)), 1, w);
        ld16h<0>(xv[0], rs, g * 16, guess); ld16h<64>(xv[1], rs, g * 16, guess); ld16h<128>(xv[2], rs, g * 16, guess); ld16h<192>(xv[3], rs, g * 16, guess);
      }
      if (w == 0) {
        const unsigned k_ = key_wait(rs, T, l, 1, bid & 15, key_load(rs, l, 1, bid & 15));
        if (l == 0) *(LDS_AS unsigned*)(smem + L_SKEY + 32) = k_;
      }
      DRAIN();
      PINV4(xv);
      BAR();
      const unsigned key = __builtin_amdgcn_readfirstlane(*(LDS_AS unsigned*)(smem + L_SKEY + 32));
      if (ffn_on) {
        if (guess != ffn_xoff(key, 1, w)) {
#pragma unroll
          for (int i = 0; i < 4; ++i) xv[i] = ld16(rs, g * 16 + i * 64, ffn_xoff(key, 1, w));
        }
        f32x4 ga, ua;
        ffn_gu(gv, uv, xv, ga, ua);
        ffn_gu_check(rs, T, ffn_xoff(key, 1, w), l, gv, uv, xv, ga, ua);
        ffn_gu_store(smem, l, w, ga, ua);
        BAR();
        f32x4 t, ya[2] = {{0.f, 0.f, 0.f, 0.f}, {0.f, 0.f, 0.f, 0.f}};
        s16x4 dfr[8];
        ffn_d_read(smem, l, w, t, dfr);
#pragma unroll
        for (int j = 0; j < 4; ++j) t[j] = ffn_d_h(ffn_d_sum(t[j]));
        const u32x2 hh = {pack2(t[0], t[1]), pack2(t[2], t[3])};
#pragma unroll
        for (int i = 0; i < 8; ++i) ya[i & 1] = ffn_d_mma(l, i, dfr[i], hh, ya[i & 1]);
        ffn_d_store(rs, l, w, 1, slice, ya[0] + ya[1]);
        SB();
        OUT_STORE();
      } else {
        OUT_STORE();
        const unsigned G = xcd * 4 + bi - 28;
        if (G == 0 && tid == 0) st8(T, rs, F_EP, 0);
        u32x4 pv[4];
        u32x2 cxn, cx1;
        __builtin_amdgcn_s_sleep(RSL);
        red_load(rs, tid, 1, G, key, pv, cxn, cx1);
        red_sum(rs, smem, T, tid, w, 1, G, key, pv, cxn, cx1);
        BAR();
        if (w == 0) {
          const u32x2 ov = red_final(smem, l, key, cxn, cx1);
          if (l < 8) __builtin_amdgcn_raw_buffer_store_b64(ov, rout, (key_row(key) * 1024 + G * 32 + l * 4) * 2, 0, 0);
        }
      }
#undef OUT_STORE
    }
  };
  layer(std::integral_constant<int, 0>{});
  layer(std::integral_constant<int, 1>{});
}

extern "C" void kernel_launch(void* const* d_in, const int* in_sizes, int n_in, void* d_out, int out_size,
                              void* d_ws, size_t ws_size, hipStream_t stream) {
  (void)hipFuncSetAttribute((const void*)mega_kernel, hipFuncAttributeMaxDynamicSharedMemorySize, LDS_BYTES);
  mega_kernel<<<256, 512, LDS_BYTES, stream>>>((const bf16*)d_in[0], (char*)d_ws, (const bf16*)d_in[1], (const bf16*)d_in[2], (const bf16*)d_in[4],
                                               (const bf16*)d_in[5], (const bf16*)d_in[6], (const bf16*)d_in[7], (bf16*)d_out);
}
```

```cpp
#include <hip/hip_runtime.h>
#include <stdint.h>
#define ECHO_A 39
#define ECHO_B 33
#define ECHO_K 2
#include <utility>

typedef __bf16 bf16;
typedef __bf16 bf16x8 __attribute__((ext_vector_type(8)));
typedef __bf16 bf16x2 __attribute__((ext_vector_type(2)));
typedef float f32x4 __attribute__((ext_vector_type(4)));
typedef float f32x2 __attribute__((ext_vector_type(2)));
typedef short s16x4 __attribute__((ext_vector_type(4)));
typedef unsigned int u32x4 __attribute__((ext_vector_type(4)));
typedef unsigned int u32x2 __attribute__((ext_vector_type(2)));
#define LDS_AS __attribute__((address_space(3)))
#define GAS __attribute__((address_space(1)))
typedef GAS unsigned gu32;
typedef GAS unsigned long long gu64;
#define RLX_AGENT __ATOMIC_RELAXED, __HIP_MEMORY_SCOPE_AGENT
typedef __amdgpu_buffer_rsrc_t rsrc_t;

constexpr int F_ = 3584;
constexpr int SC1 = 16;

constexpr int RX = 4;
constexpr unsigned F_EP = 0, F_TMO = 128;
constexpr unsigned F_XN = 4096;
constexpr unsigned F_X1 = 196608;
constexpr unsigned F_O = 32768;
constexpr unsigned F_KV = 65536;
static_assert(F_XN + 8 * RX * 256 <= F_O && F_O + 2 * 64 * 256 <= F_KV, "flag block");
constexpr unsigned OFF_KEY = 131072;
constexpr unsigned OFF_AB = 147456;
constexpr unsigned OFF_XNEW = 262144;
constexpr unsigned OFF_CS = 294912;
constexpr unsigned OFF_FX = 393216;
constexpr unsigned OFF_CA = 524288;
constexpr unsigned OFF_CB = 655360;
constexpr unsigned OFF_STAT = 1u << 20;
constexpr unsigned OFF_PART = 3u << 20;
constexpr unsigned OFF_STAMP = 6u << 20;
constexpr unsigned OFF_XN = 8u << 20;
constexpr unsigned OFF_O = 16u << 20;
constexpr unsigned OFF_KV = 32u << 20;
constexpr unsigned OFF_WI = 52u << 20;
constexpr unsigned WS_BYTES = 58u << 20;
constexpr unsigned POISON = 0xFFFFFFFFu;

constexpr int L_ST0 = 147456, L_CNT = 147968, L_CSL = 1024, L_FXL = 1536, L_K = 0, L_V = 32768, L_RING = 65536, L_XCH = 0, L_DN = 32768, L_TOUCH0 = 149504, L_TOUCH = 10240, L_QC = 512, LDS_BYTES = 163840;
constexpr int STAGE = 24576;
#define SLOT(t) ((t) & 3)

template <typename F, int... I> __device__ __forceinline__ void static_for_impl(F&& f, std::integer_sequence<int, I...>) {
  (f(std::integral_constant<int, I>{}), ...);
}
template <int N, typename F> __device__ __forceinline__ void static_for(F&& f) { static_for_impl(f, std::make_integer_sequence<int, N>{}); }
__device__ __forceinline__ unsigned pack2(float a, float b) {
  f32x2 v = {a, b};
  return __builtin_bit_cast(unsigned, __builtin_convertvector(v, bf16x2));
}
__device__ __forceinline__ float asf(unsigned u) { return __builtin_bit_cast(float, u); }
__device__ __forceinline__ float lo16(unsigned u) { return __builtin_bit_cast(float, u << 16); }
__device__ __forceinline__ float hi16(unsigned u) { return __builtin_bit_cast(float, u & 0xffff0000u); }
__device__ __forceinline__ u32x4 pack8(const f32x4 a, const f32x4 b) {
  u32x4 o;
  o[0] = pack2(a[0], a[1]); o[1] = pack2(a[2], a[3]); o[2] = pack2(b[0], b[1]); o[3] = pack2(b[2], b[3]);
  return o;
}
__device__ __forceinline__ f32x4 mfma16(u32x4 a, u32x4 b, f32x4 c) {
  return __builtin_amdgcn_mfma_f32_16x16x32_bf16(__builtin_bit_cast(bf16x8, a), __builtin_bit_cast(bf16x8, b), c, 0, 0, 0);
}
__device__ __forceinline__ f32x4 mfma16k16(s16x4 a, s16x4 b, f32x4 c) { return __builtin_amdgcn_mfma_f32_16x16x16bf16_1k(a, b, c, 0, 0, 0); }
template <int CTRL> __device__ __forceinline__ float dppf(float v) {
  return __builtin_bit_cast(float, __builtin_amdgcn_update_dpp(0, __builtin_bit_cast(int, v), CTRL, 0xf, 0xf, false));
}
template <int CTRL> __device__ __forceinline__ unsigned dppu(unsigned v) {
  return (unsigned)__builtin_amdgcn_update_dpp(0, (int)v, CTRL, 0xf, 0xf, false);
}
__device__ __forceinline__ float add_x16(float v) {
  auto r = __builtin_amdgcn_permlane16_swap(__builtin_bit_cast(unsigned, v), __builtin_bit_cast(unsigned, v), false, false);
  return __builtin_bit_cast(float, (unsigned)r[0]) + __builtin_bit_cast(float, (unsigned)r[1]);
}
__device__ __forceinline__ float add_x32(float v) {
  auto r = __builtin_amdgcn_permlane32_swap(__builtin_bit_cast(unsigned, v), __builtin_bit_cast(unsigned, v), false, false);
  return __builtin_bit_cast(float, (unsigned)r[0]) + __builtin_bit_cast(float, (unsigned)r[1]);
}
__device__ __forceinline__ float max_x16(float v) {
  auto r = __builtin_amdgcn_permlane16_swap(__builtin_bit_cast(unsigned, v), __builtin_bit_cast(unsigned, v), false, false);
  return fmaxf(__builtin_bit_cast(float, (unsigned)r[0]), __builtin_bit_cast(float, (unsigned)r[1]));
}
__device__ __forceinline__ float max_x32(float v) {
  auto r = __builtin_amdgcn_permlane32_swap(__builtin_bit_cast(unsigned, v), __builtin_bit_cast(unsigned, v), false, false);
  return fmaxf(__builtin_bit_cast(float, (unsigned)r[0]), __builtin_bit_cast(float, (unsigned)r[1]));
}
__device__ __forceinline__ unsigned umax_x16(unsigned v) {
  auto r = __builtin_amdgcn_permlane16_swap(v, v, false, false);
  return max((unsigned)r[0], (unsigned)r[1]);
}
__device__ __forceinline__ unsigned umax_x32(unsigned v) {
  auto r = __builtin_amdgcn_permlane32_swap(v, v, false, false);
  return max((unsigned)r[0], (unsigned)r[1]);
}
__device__ __forceinline__ float sum_hi8(float v) {
  v += dppf<0x128>(v);
  v = add_x16(v);
  return add_x32(v);
}
__device__ __forceinline__ float sum64(float v) {
  v += dppf<0xB1>(v);
  v += dppf<0x4E>(v);
  v += dppf<0x124>(v);
  v += dppf<0x128>(v);
  v = add_x16(v);
  return add_x32(v);
}

#define BAR() asm volatile("s_waitcnt lgkmcnt(0)\n\ts_barrier" ::: "memory")
#define DRAIN() asm volatile("s_waitcnt vmcnt(0)" ::: "memory")
#define SB() __builtin_amdgcn_sched_barrier(0)
template <int N> __device__ __forceinline__ void wait_vm() { asm volatile("s_waitcnt vmcnt(%0)" ::"n"(N) : "memory"); }

__device__ __forceinline__ void dma16(rsrc_t rs, unsigned voff, unsigned soff, unsigned lds) {
  asm volatile("s_mov_b32 m0, %0\n\ts_nop 0\n\tbuffer_load_dwordx4 %1, %2, %3 offen lds" ::"s"(lds), "v"(voff), "s"(rs), "s"(soff) : "memory");
}
template <int OFF> __device__ __forceinline__ void dma16s(rsrc_t rs, unsigned voff, unsigned soff, unsigned lds) {
  asm volatile("s_mov_b32 m0, %0\n\ts_nop 0\n\tbuffer_load_dwordx4 %1, %2, %3 offen offset:%4 sc1 lds" ::"s"(lds), "v"(voff), "s"(rs), "s"(soff), "n"(OFF) : "memory");
}
__device__ __forceinline__ float rbf1(float x) { return __builtin_bit_cast(float, pack2(0.f, x)); }
__device__ __forceinline__ void vswap(unsigned& a, unsigned& b) { asm volatile("v_swap_b32 %0, %1" : "+v"(a), "+v"(b)); }
__device__ __forceinline__ void vswap(float& a, float& b) { asm volatile("v_swap_b32 %0, %1" : "+v"(a), "+v"(b)); }
template <typename V, int N> __device__ __forceinline__ void vswapn(V& a, V& b) {
#pragma unroll
  for (int i = 0; i < N; ++i) { auto x = a[i], y = b[i]; vswap(x, y); a[i] = x; b[i] = y; }
}
#define ROLE_IF(body) "s_cmp_lg_u32 %[a], %[b]\n\ts_cbranch_scc1 .Lrl%=\n\t" body "s_nop 1\n.Lrl%=:"
#define M2(a, b) "v_mov_b64 v[" #a ":" #a "+1], v[" #b ":" #b "+1]\n\t"
#define Z2(a) "v_mov_b64 v[" #a ":" #a "+1], 0\n\t"
template <int M> __device__ __forceinline__ void role_q(u32x4& qc, const u32x4& qn, f32x4& cc, const f32x4& cx, int wq) {
  asm volatile(ROLE_IF(M2(176, 180) M2(178, 182) M2(184, 188) M2(186, 190))
               : "+{v[176:179]}"(qc), "+{v[184:187]}"(cc) : "{v[180:183]}"(qn), "{v[188:191]}"(cx), [a] "s"(wq), [b] "n"(M) : "scc");
}
template <int M, bool PH> __device__ __forceinline__ void role_o(f32x4 (&oa)[3][2], f32x4& sc, float& s1, float& s2, int wq) {
  if constexpr (!PH)
    asm volatile(ROLE_IF("s_nop 4\n\t" M2(200, 192) M2(202, 194) M2(204, 196) M2(206, 198) "v_mov_b32 v212, v208\n\t" Z2(192) Z2(194) Z2(196) Z2(198) "v_mov_b32 v208, 0\n\t")
                 : "+{v[192:195]}"(oa[0][0]), "+{v[196:199]}"(oa[0][1]), "+{v[200:203]}"(oa[1][0]), "+{v[204:207]}"(oa[1][1]), "+{v[208:211]}"(sc), "+{v212}"(s1)
                 : [a] "s"(wq), [b] "n"(M) : "scc");
  else
    asm volatile(ROLE_IF("s_nop 4\n\t" M2(216, 192) M2(218, 194) M2(220, 196) M2(222, 198) "v_mov_b32 v213, v208\n\t" M2(192, 200) M2(194, 202) M2(196, 204) M2(198, 206) "v_mov_b32 v208, v212\n\t")
                 : "+{v[192:195]}"(oa[0][0]), "+{v[196:199]}"(oa[0][1]), "+{v[216:219]}"(oa[2][0]), "+{v[220:223]}"(oa[2][1]), "+{v[208:211]}"(sc), "+{v213}"(s2)
                 : "{v[200:203]}"(oa[1][0]), "{v[204:207]}"(oa[1][1]), "{v212}"(s1), [a] "s"(wq), [b] "n"(M) : "scc");
}
#undef M2
#undef Z2
#undef ROLE_IF
__device__ __forceinline__ u32x4 ld16(rsrc_t rs, unsigned voff, unsigned soff) { return __builtin_amdgcn_raw_buffer_load_b128(rs, voff, soff, SC1); }
template <int OFF> __device__ __forceinline__ void ld16h(u32x4& d, rsrc_t rs, unsigned voff, unsigned soff) {
  asm volatile("buffer_load_dwordx4 %0, %1, %2, %3 offen offset:%4 sc1" : "=v"(d) : "v"(voff), "s"(rs), "s"(soff), "n"(OFF) : "memory");
}
template <int I, int OFF> __device__ __forceinline__ void ld16w(u32x4& d, rsrc_t rs, unsigned voff, unsigned soff) {
  if constexpr (I == 0) asm volatile("buffer_load_dwordx4 %0, %1, %2, %3 offen offset:%4 sc1" : "={v[128:131]}"(d) : "v"(voff), "s"(rs), "s"(soff), "n"(OFF) : "memory");
  else if constexpr (I == 1) asm volatile("buffer_load_dwordx4 %0, %1, %2, %3 offen offset:%4 sc1" : "={v[132:135]}"(d) : "v"(voff), "s"(rs), "s"(soff), "n"(OFF) : "memory");
  else if constexpr (I == 2) asm volatile("buffer_load_dwordx4 %0, %1, %2, %3 offen offset:%4 sc1" : "={v[136:139]}"(d) : "v"(voff), "s"(rs), "s"(soff), "n"(OFF) : "memory");
  else if constexpr (I == 3) asm volatile("buffer_load_dwordx4 %0, %1, %2, %3 offen offset:%4 sc1" : "={v[140:143]}"(d) : "v"(voff), "s"(rs), "s"(soff), "n"(OFF) : "memory");
  else if constexpr (I == 4) asm volatile("buffer_load_dwordx4 %0, %1, %2, %3 offen offset:%4 sc1" : "={v[144:147]}"(d) : "v"(voff), "s"(rs), "s"(soff), "n"(OFF) : "memory");
  else if constexpr (I == 5) asm volatile("buffer_load_dwordx4 %0, %1, %2, %3 offen offset:%4 sc1" : "={v[148:151]}"(d) : "v"(voff), "s"(rs), "s"(soff), "n"(OFF) : "memory");
  else if constexpr (I == 6) asm volatile("buffer_load_dwordx4 %0, %1, %2, %3 offen offset:%4 sc1" : "={v[152:155]}"(d) : "v"(voff), "s"(rs), "s"(soff), "n"(OFF) : "memory");
  else if constexpr (I == 7) asm volatile("buffer_load_dwordx4 %0, %1, %2, %3 offen offset:%4 sc1" : "={v[156:159]}"(d) : "v"(voff), "s"(rs), "s"(soff), "n"(OFF) : "memory");
  else if constexpr (I == 8) asm volatile("buffer_load_dwordx4 %0, %1, %2, %3 offen offset:%4 sc1" : "={v[160:163]}"(d) : "v"(voff), "s"(rs), "s"(soff), "n"(OFF) : "memory");
  else if constexpr (I == 9) asm volatile("buffer_load_dwordx4 %0, %1, %2, %3 offen offset:%4 sc1" : "={v[164:167]}"(d) : "v"(voff), "s"(rs), "s"(soff), "n"(OFF) : "memory");
  else if constexpr (I == 10) asm volatile("buffer_load_dwordx4 %0, %1, %2, %3 offen offset:%4 sc1" : "={v[168:171]}"(d) : "v"(voff), "s"(rs), "s"(soff), "n"(OFF) : "memory");
  else if constexpr (I == 11) asm volatile("buffer_load_dwordx4 %0, %1, %2, %3 offen offset:%4 sc1" : "={v[172:175]}"(d) : "v"(voff), "s"(rs), "s"(soff), "n"(OFF) : "memory");
}
template <int K> __device__ __forceinline__ void pin_w(u32x4& a, u32x4& b) {
  if constexpr (K == 0) asm volatile("" : "+{v[128:131]}"(a), "+{v[132:135]}"(b)::"memory");
  else if constexpr (K == 1) asm volatile("" : "+{v[136:139]}"(a), "+{v[140:143]}"(b)::"memory");
  else if constexpr (K == 2) asm volatile("" : "+{v[144:147]}"(a), "+{v[148:151]}"(b)::"memory");
  else if constexpr (K == 3) asm volatile("" : "+{v[152:155]}"(a), "+{v[156:159]}"(b)::"memory");
  else if constexpr (K == 4) asm volatile("" : "+{v[160:163]}"(a), "+{v[164:167]}"(b)::"memory");
  else if constexpr (K == 5) asm volatile("" : "+{v[168:171]}"(a), "+{v[172:175]}"(b)::"memory");
}
__device__ __forceinline__ void ld8h(u32x2& d, rsrc_t rs, unsigned voff, unsigned soff) {
  asm volatile("buffer_load_dwordx2 %0, %1, %2, %3 offen sc1" : "=v"(d) : "v"(voff), "s"(rs), "s"(soff) : "memory");
}
template <int OFF> __device__ __forceinline__ void ld4h(unsigned& d, rsrc_t rs, unsigned voff, unsigned soff) {
  asm volatile("buffer_load_dword %0, %1, %2, %3 offen offset:%4 sc1" : "=v"(d) : "v"(voff), "s"(rs), "s"(soff), "n"(OFF) : "memory");
}
__device__ __forceinline__ u32x2 ld8(rsrc_t rs, unsigned voff, unsigned soff) { return __builtin_amdgcn_raw_buffer_load_b64(rs, voff, soff, SC1); }
__device__ __forceinline__ void st16(u32x4 v, rsrc_t rs, unsigned voff, unsigned soff) { __builtin_amdgcn_raw_buffer_store_b128(v, rs, voff + soff, 0, SC1); }
__device__ __forceinline__ void st8(u32x2 v, rsrc_t rs, unsigned voff, unsigned soff) { __builtin_amdgcn_raw_buffer_store_b64(v, rs, voff, soff, SC1); }

__device__ __forceinline__ unsigned ld4(rsrc_t rs, unsigned voff, unsigned soff) { return __builtin_amdgcn_raw_buffer_load_b32(rs, voff, soff, SC1); }
__device__ __forceinline__ void st4(unsigned v, rsrc_t rs, unsigned voff, unsigned soff) { __builtin_amdgcn_raw_buffer_store_b32(v, rs, voff, soff, SC1); }

constexpr unsigned kSpinCap = 1u << 16;
__device__ __forceinline__ bool spin_fail(unsigned& spins, rsrc_t rs, u32x2 T) {
  asm volatile("" ::: "memory");
  ++spins;
  if (spins > kSpinCap) { st8(T, rs, 0, F_TMO); return true; }
  if ((spins & 255u) == 0u) {
    const u32x2 v = ld8(rs, 0, F_TMO);
    if (v[0] == T[0] && v[1] == T[1]) return true;
  }
  __builtin_amdgcn_s_sleep(3);
  return false;
}
__device__ __forceinline__ void poll_flags2(rsrc_t rs, unsigned voff, u32x2 T) {
  for (unsigned spins = 0;;) {
    const u32x4 v = ld16(rs, voff, 0);
    if (__all(v[0] == T[0] && v[1] == T[1] && v[2] == T[0] && v[3] == T[1])) break;
    if (spin_fail(spins, rs, T)) break;
  }
  asm volatile("" ::: "memory");
  __builtin_amdgcn_fence(__ATOMIC_ACQUIRE, "wavefront");
}
__device__ __forceinline__ void poll_flags(rsrc_t rs, unsigned voff, unsigned soff, u32x2 T) {
  for (unsigned spins = 0;;) {
    const u32x2 v = ld8(rs, voff, soff);
    if (__all(v[0] == T[0] && v[1] == T[1])) break;
    if (spin_fail(spins, rs, T)) break;
  }
  asm volatile("" ::: "memory");
  __builtin_amdgcn_fence(__ATOMIC_ACQUIRE, "wavefront");
}

#define STAMP_ON(i) true
#define STAMP_VAL(i) (((i) == 23 || (i) == 22 || (i) == 21) ? __builtin_amdgcn_s_memtime() : __builtin_amdgcn_s_memrealtime())
#define STAMPAT(i) do { if (it * 64 + (i) == ECHO_A) { echo_a = __builtin_amdgcn_s_memrealtime(); asm volatile("" : "+s"(echo_a)); } if (it * 64 + (i) == ECHO_B) { echo_b = __builtin_amdgcn_s_memrealtime(); asm volatile("" : "+s"(echo_b)); } } while (0)

__device__ __forceinline__ unsigned toff(unsigned R, unsigned ks, unsigned g) {
  return (((R >> 5) * 64 + ks * 2 + ((R >> 4) & 1)) << 10) + ((g * 16 + (R & 15)) << 4);
}
__device__ __forceinline__ void unpack8v(const u32x4 u, f32x2* v) {
#pragma unroll
  for (int j = 0; j < 4; ++j) v[j] = f32x2{lo16(u[j]), hi16(u[j])};
}
__device__ __forceinline__ u32x4 pack8v(const f32x2* v) {
  u32x4 o;
#pragma unroll
  for (int j = 0; j < 4; ++j) o[j] = pack2(v[j][0], v[j][1]);
  return o;
}
__device__ __forceinline__ f32x2 rbf1v(f32x2 v) { return f32x2{rbf1(v[0]), rbf1(v[1])}; }
__device__ __forceinline__ float ln_rd(float var) { return __builtin_amdgcn_rcpf(rbf1(__builtin_amdgcn_sqrtf(rbf1(rbf1(var) + 1e-5f)))); }
template <int N> __device__ __forceinline__ void ln_apply(float mb, float rd, const f32x2* v, f32x2* y) {
  const f32x2 mb2 = {mb, mb}, rd2 = {rd, rd};
#pragma unroll
  for (int j = 0; j < N; ++j) y[j] = rbf1v(rbf1v(v[j] - mb2) * rd2);
}
__device__ __forceinline__ float hadd(f32x2 v) {
  float a = v[0];
  asm volatile("" : "+v"(a));
  return a + v[1];
}
__device__ __forceinline__ float add_g(float v) { return add_x32(add_x16(v)); }
__device__ __forceinline__ void add_g3(float& x, float& y, float& z) {
  auto a = __builtin_amdgcn_permlane16_swap(__builtin_bit_cast(unsigned, x), __builtin_bit_cast(unsigned, y), false, false);
  auto b = __builtin_amdgcn_permlane16_swap(__builtin_bit_cast(unsigned, z), __builtin_bit_cast(unsigned, z), false, false);
  const float r = asf(a[0]) + asf(a[1]), t = asf(b[0]) + asf(b[1]);
  auto c = __builtin_amdgcn_permlane32_swap(__builtin_bit_cast(unsigned, r), __builtin_bit_cast(unsigned, r), false, false);
  auto d = __builtin_amdgcn_permlane32_swap(__builtin_bit_cast(unsigned, t), __builtin_bit_cast(unsigned, t), false, false);
  const float u = asf(c[0]) + asf(c[1]);
  z = asf(d[0]) + asf(d[1]);
  auto e = __builtin_amdgcn_permlane16_swap(__builtin_bit_cast(unsigned, u), __builtin_bit_cast(unsigned, u), false, false);
  x = asf(e[0]); y = asf(e[1]);
}
__device__ __forceinline__ u32x4 trade16(u32x4 v) {
  auto a = __builtin_amdgcn_permlane16_swap(v[0], v[2], false, false);
  auto b = __builtin_amdgcn_permlane16_swap(v[1], v[3], false, false);
  return u32x4{(unsigned)a[0], (unsigned)b[0], (unsigned)a[1], (unsigned)b[1]};
}
__device__ __forceinline__ unsigned umax4(u32x4 v) { return max(max(v[0], v[1]), max(v[2], v[3])); }
__device__ __forceinline__ u32x2 unpoison2(u32x2 v) { return u32x2{min(v[0], POISON - 1), min(v[1], POISON - 1)}; }
__device__ __forceinline__ u32x4 unpoison4(u32x4 v) {
  return u32x4{min(v[0], POISON - 1), min(v[1], POISON - 1), min(v[2], POISON - 1), min(v[3], POISON - 1)};
}

typedef union { struct { s16x4 a, b; } hf; u32x4 v; } frag_t;
__device__ __forceinline__ s16x4 trread(const char* smem, unsigned off) {
  return __builtin_amdgcn_ds_read_tr16_b64_v4i16((LDS_AS s16x4*)(smem + off));
}
__device__ __forceinline__ s16x4 trabs(unsigned addr) { return __builtin_amdgcn_ds_read_tr16_b64_v4i16((LDS_AS s16x4*)(size_t)addr); }

constexpr int WA = 6;
#define WBV 8
#define WRV 4
constexpr int WB = WBV, WR = WRV;
#define KVA 3, 1
#define KVL 2
constexpr int KPV[2] = {KVA};
#define KVFV 7, 7, 7, 7
constexpr int KFV[4] = {KVFV};
#define CH 1, 8, 10, 11, 16, 18, 23, 25, 28, 30
constexpr int CHV[10] = {CH};
constexpr int SPL = CHV[0];
constexpr int FSL = CHV[1], FS0 = CHV[2], FS0D = CHV[3], FS1 = CHV[4], FS1S = CHV[5];
constexpr int FXA = CHV[6], FXB = CHV[7], FXL = CHV[8], FXS = CHV[9];
constexpr int CSD = 5, CSL0 = 16, CSL1 = 5, ABL = 25, ABS = 27;
#define SIGN 2
#define KSL 3
#define WDL 0
#define RSL 56
#define SDIR 24
#define RO 1
#define T1L 0
#define GSL 14
#define TSV 20, 6, 5, 0
constexpr int TSA[4] = {TSV}, TS0 = TSA[0], TSN = TSA[1], TW = TSA[2], TWD = TSA[3];
constexpr int POI = 9, IMG = 13, TST = 27;
constexpr bool tile_piece(int s) { return s >= 3 && ((s - 3) >> 2) + 3 < 8 && ((s - 3) & 3) < 3; }
constexpr int a_req(int s, int n) {
  if (s < 1 || !(n & 1) || (s == 1 && n == 1)) return -1;
  if (s <= 5) return 8 + 3 * s + (n >> 1) - 4;
  return n < 5 && s + 5 < 32 ? 2 * (s + 5) + (n >> 1) : -1;
}
constexpr int vm_n(int it, int s, int n) {
  int c = (a_req(s, n) >= 0) + (s == 0 && n == 0);
  if (n == 0) c += tile_piece(s);
  if (n == 4) c += (s == ABL ? 8 : 0) + (it ? (s == FSL ? 5 : s == FXA ? 4 : 0) + (s == FSL - 2) : (s == POI) + (s == IMG ? 3 : 0));
  if (!it) c += (T1L && s == 0 && n >= 1 && n < 4) + (s == 0 && n == 4) + (s >= 2 && s < 5 && n == 2);
  else c += (s == 2 && n == 2) + (s == SPL && n == 4 ? 4 : 0);
  return c;
}
constexpr int vm_at(int it, int s, int n) {
  int c = it ? 8 : 0;
  for (int u = 0; u < 6 * s + n; ++u) c += vm_n(it, u / 6, u % 6);
  return c;
}
constexpr int vm_a(int it, int k) {
  if (k < 4) return 2 * k + 2;
  for (int u = 0; u < 192; ++u)
    if (a_req(u / 6, u % 6) == 2 * k + 1) return vm_at(it, (u + 1) / 6, (u + 1) % 6);
  return 0;
}
constexpr int vm_tile(int it, int t) { return t >= 3 ? vm_at(it, 4 * t - 7, 1) : t == 1 ? (T1L ? vm_at(0, 0, 4) : 0) : vm_at(0, 4, 3); }

constexpr bool wo_fl(int s) { return s >= 0 && s + WB < 16 && (s & 1) == 0; }
constexpr int wo_at(int s, int ph) {
  int c = 2 * WR + 2;
  for (int u = 0; u <= s; ++u) {
    if (u < s || ph > 0) c += wo_fl(u);
    if (u < s || ph > 1) c += u < WB - WR ? 2 : 0;
    if (u < s) c += u + WB < 16 || u < WB - WR ? 2 : 0;
  }
  return c;
}
constexpr int wo_a(int k) { return k < WR ? 2 * k + 2 : k < 2 * WB - WR ? ((k - WR) & 1 ? wo_at((k - WR) / 2 + 1, 0) : wo_at((k - WR) / 2, 2)) : wo_at(k - WB + 1, 0); }

constexpr int L_XC = LDS_BYTES - 64, L_SKEY = 768, L_SRED = 4096, L_SFIN = 20480;
__device__ __forceinline__ unsigned key_load(rsrc_t rs, int l, int L, int rep) { return ld4(rs, (l & 15) * 4, OFF_KEY + (L * 16 + rep) * 128); }
__device__ __forceinline__ unsigned umax16(unsigned k) {
  k = max(k, dppu<0xB1>(k));
  k = max(k, dppu<0x4E>(k));
  k = max(k, dppu<0x141>(k));
  return max(k, dppu<0x140>(k));
}
__device__ __forceinline__ unsigned key_row(unsigned key) { return (0xffffu - (key & 0xffffu)) & 2047u; }
__device__ __forceinline__ unsigned key_wait(rsrc_t rs, u32x2 T, int l, int L, int rep, unsigned kv) {
  if (!__all(kv != POISON)) {
    unsigned spins = 0;
    do {
      if (spin_fail(spins, rs, T)) break;
      if (L == 0) __builtin_amdgcn_s_sleep(KSL);
      kv = key_load(rs, l, L, rep);
    } while (!__all(kv != POISON));
  }
  return __builtin_amdgcn_readfirstlane(umax16(kv));
}
__device__ __forceinline__ unsigned ffn_xoff(unsigned key, int L, int w) { return OFF_CA + L * 32768 + (key_row(key) >> 7) * 2048 + w * 256; }
__device__ __forceinline__ void ffn_xwait(rsrc_t rs, u32x2 T, unsigned xo, int l, u32x4 (&xv)[4]) {
#define XV_OK() __all(max(max(umax4(xv[0]), umax4(xv[1])), max(umax4(xv[2]), umax4(xv[3]))) != POISON)
  if (!XV_OK()) {
    unsigned spins = 0;
    do {
      if (spin_fail(spins, rs, T)) break;
#pragma unroll
      for (int i = 0; i < 4; ++i) xv[i] = ld16(rs, (l >> 4) * 16 + i * 64, xo);
    } while (!XV_OK());
  }
#undef XV_OK
}
__device__ __forceinline__ void ffn_gu(const u32x4 (&gv)[4], const u32x4 (&uv)[4], const u32x4 (&xv)[4], f32x4& ga, f32x4& ua) {
  ga = mfma16(gv[0], xv[0], f32x4{0.f, 0.f, 0.f, 0.f}); ua = mfma16(uv[0], xv[0], f32x4{0.f, 0.f, 0.f, 0.f});
#pragma unroll
  for (int i = 1; i < 4; ++i) { ga = mfma16(gv[i], xv[i], ga); ua = mfma16(uv[i], xv[i], ua); }
}
__device__ __forceinline__ void ffn_gu_check(rsrc_t rs, u32x2 T, unsigned xo, int l, const u32x4 (&gv)[4], const u32x4 (&uv)[4], u32x4 (&xv)[4], f32x4& ga, f32x4& ua) {
  if (__any(ga[0] != ga[0])) {
    ffn_xwait(rs, T, xo, l, xv);
    ffn_gu(gv, uv, xv, ga, ua);
  }
}
__device__ __forceinline__ void ffn_gu_store(char* smem, int l, int w, f32x4 ga, f32x4 ua) {
  LDS_AS f32x4* sred = (LDS_AS f32x4*)(smem + L_SRED);
  if ((l & 15) == 0) { sred[w * 8 + (l >> 4)] = ga; sred[w * 8 + 4 + (l >> 4)] = ua; }
}
__device__ __forceinline__ void ffn_d_read(char* smem, int l, int w, f32x4& t, s16x4 (&dfr)[8]) {
  t = ((const LDS_AS f32x4*)(smem + L_SRED))[(l & 7) * 8 + ((l >> 3) & 1) * 4 + (l >> 4)];
#pragma unroll
  for (int i = 0; i < 8; ++i) dfr[i] = trread(smem, L_DN + w * 4096 + (l >> 4) * 1024 + ((l & 15) >> 2) * 256 + i * 32 + (l & 3) * 8);
}
__device__ __forceinline__ float ffn_d_sum(float t) { t += dppf<0xB1>(t); t += dppf<0x4E>(t); return t + dppf<0x141>(t); }
__device__ __forceinline__ float ffn_d_h(float t) {
  const float gg = rbf1(t), uu = rbf1(dppf<0x128>(t));
  return rbf1(gg * __builtin_amdgcn_rcpf(1.0f + __expf(-gg))) * uu;
}
__device__ __forceinline__ f32x4 ffn_d_mma(int l, int t, s16x4 d, u32x2 hh, f32x4 y) {
  const bool me = (l & 15) == t;
  return mfma16k16(d, __builtin_bit_cast(s16x4, u32x2{me ? hh[0] : 0u, me ? hh[1] : 0u}), y);
}
__device__ __forceinline__ void ffn_d_store(rsrc_t rs, int l, int w, int L, int slice, f32x4 y) {
  const unsigned c = l & 15;
  if (c < 8) st16(unpoison4(__builtin_bit_cast(u32x4, y)), rs, (4 * w + (c >> 1)) * 32768 + slice * 128 + (c & 1) * 64 + (l >> 4) * 16, OFF_PART + L * (1u << 20));
}
__device__ __forceinline__ void red_load(rsrc_t rs, int tid, int L, unsigned G, unsigned key, u32x4 (&pv)[4], u32x2& cxn, u32x2& cx1) {
  const unsigned co = L * 32768 + (key_row(key) >> 7) * 2048 + G * 64, jj = (tid & 3) * 16 + (tid & 4) * 2;
#pragma unroll
  for (int k = 0; k < 4; ++k) pv[k] = ld16(rs, min(tid + 512 * k, 1791) * 16, OFF_PART + L * (1u << 20) + G * 32768);
  cxn = ld8(rs, jj, OFF_CA + co);
  cx1 = ld8(rs, jj, OFF_CB + co);
}
__device__ __forceinline__ void red_sum(rsrc_t rs, char* smem, u32x2 T, int tid, int w, int L, unsigned G, unsigned key,
                                        u32x4 (&pv)[4], u32x2& cxn, u32x2& cx1) {
#define PV_OK() __all(max(max(max(umax4(pv[0]), umax4(pv[1])), max(umax4(pv[2]), umax4(pv[3]))), max(max(cxn[0], cxn[1]), max(cx1[0], cx1[1]))) != POISON)
  if (!PV_OK()) {
    unsigned spins = 0;
    do {
      if (spin_fail(spins, rs, T)) break;
      red_load(rs, tid, L, G, key, pv, cxn, cx1);
    } while (!PV_OK());
  }
#undef PV_OK
  f32x4 s = (__builtin_bit_cast(f32x4, pv[0]) + __builtin_bit_cast(f32x4, pv[1])) + __builtin_bit_cast(f32x4, pv[2]);
  if (w < 4) s += __builtin_bit_cast(f32x4, pv[3]);
  ((LDS_AS f32x4*)(smem + L_SFIN))[tid] = s;
}
__device__ __forceinline__ u32x2 red_final(char* smem, int l, unsigned key, u32x2 cxn, u32x2 cx1) {
  const LDS_AS f32x4* sf = (const LDS_AS f32x4*)(smem + L_SFIN) + l;
  f32x4 s = ((sf[0] + sf[64]) + (sf[128] + sf[192])) + ((sf[256] + sf[320]) + (sf[384] + sf[448]));
#pragma unroll
  for (int j = 0; j < 4; ++j) s[j] = sum_hi8(s[j]);
  const unsigned k16 = key >> 16;
  const float rmax = __builtin_bit_cast(float, ((k16 & 0x8000u) ? (k16 & 0x7fffu) : (~k16 & 0xffffu)) << 16);
  const float sig = rbf1(__builtin_amdgcn_rcpf(1.0f + __expf(-rmax)));
  const float xn4[4] = {lo16(cxn[0]), hi16(cxn[0]), lo16(cxn[1]), hi16(cxn[1])};
  const float x14[4] = {lo16(cx1[0]), hi16(cx1[0]), lo16(cx1[1]), hi16(cx1[1])};
  float o[4];
#pragma unroll
  for (int j = 0; j < 4; ++j) o[j] = x14[j] + rbf1(xn4[j] + rbf1(rbf1(s[j]) * sig));
  return u32x2{pack2(o[0], o[1]), pack2(o[2], o[3])};
}

struct RowFin { float mean, var, mb, rd; unsigned key; };
__device__ __forceinline__ void fin_wait(rsrc_t rs, u32x2 T, unsigned voff, unsigned so, u32x4 (&sp)[4]) {
#define ST_OK() __all(max(max(umax4(sp[0]), umax4(sp[1])), max(umax4(sp[2]), umax4(sp[3]))) != POISON)
  if (!ST_OK()) {
    unsigned spins = 0;
    do {
      if (spin_fail(spins, rs, T)) break;
#pragma unroll
      for (int i = 0; i < 4; ++i) sp[i] = ld16(rs, voff + i * 2048, so);
    } while (!ST_OK());
  }
#undef ST_OK
}
__device__ __forceinline__ RowFin fin_key(rsrc_t rs, u32x2 T, unsigned voff, unsigned so, char* skey, int l, int w, unsigned R, float wsum, u32x4 (&sp)[4]) {
  RowFin r;
#define ST_SUM() ((__builtin_bit_cast(f32x4, sp[0]) + __builtin_bit_cast(f32x4, sp[1])) + (__builtin_bit_cast(f32x4, sp[2]) + __builtin_bit_cast(f32x4, sp[3])))
  f32x4 sf = ST_SUM();
  const float z = (sf[0] + sf[1]) + sf[2];
  if (__any(z != z)) {
    fin_wait(rs, T, voff, so, sp);
    sf = ST_SUM();
  }
#undef ST_SUM
  float s0 = sf[0], s1 = sf[1], s2 = sf[2];
  add_g3(s0, s1, s2);
  r.mean = s0 * (1.0f / 1024.0f);
  r.var = fmaxf(__builtin_fmaf(-r.mean, r.mean, s1 * (1.0f / 1024.0f)), 0.f);
  r.mb = rbf1(r.mean); r.rd = ln_rd(r.var);
  const unsigned rb16 = pack2((s2 - r.mb * wsum) * r.rd, 0.f) & 0xffffu;
  r.key = min((((rb16 & 0x8000u) ? (~rb16 & 0xffffu) : (rb16 | 0x8000u)) << 16) | (0xffffu - R), POISON - 1);
  const unsigned wk = umax16(r.key);
  if (l == 0) ((LDS_AS unsigned*)skey)[w] = wk;
  return r;
}
__device__ __forceinline__ void fin_cand(rsrc_t rs, char* skey, int L, int g, int rb, int ct, const RowFin& r, const f32x2 (&v)[8]) {
  const u32x4 k0 = *(const LDS_AS u32x4*)skey, k1 = *(const LDS_AS u32x4*)(skey + 16);
  const unsigned best = max(umax4(k0), umax4(k1));
  if (r.key == best) {
    const unsigned co = L * 32768 + rb * 2048 + ct * 128 + g * 16;
    f32x2 t[8], rd2 = {r.rd, r.rd}, of2 = {-r.mb * r.rd, -r.mb * r.rd};
    asm volatile("" : "+v"(rd2), "+v"(of2));
#pragma unroll
    for (int j = 0; j < 8; ++j) t[j] = __builtin_elementwise_fma(v[j], rd2, of2);
    st16(unpoison4(pack8v(t)), rs, co, OFF_CA); st16(unpoison4(pack8v(t + 4)), rs, co + 64, OFF_CA);
    if (g == 0) st4(best, rs, OFF_KEY + (L * 16 + ct) * 128 + rb * 4, 0);
    st16(unpoison4(pack8v(v)), rs, co, OFF_CB); st16(unpoison4(pack8v(v + 4)), rs, co + 64, OFF_CB);
  }
}
__device__ __forceinline__ void fin_x2(const RowFin& r, f32x2 (&v)[8]) {
  f32x2 sc2 = {1.0f + r.rd, 1.0f + r.rd}, of2 = {-r.mb * r.rd, -r.mb * r.rd};
  asm volatile("" : "+v"(sc2), "+v"(of2));
#pragma unroll
  for (int j = 0; j < 8; ++j) v[j] = __builtin_elementwise_fma(v[j], sc2, of2);
}

extern "C" __device__ unsigned long long dispatch_id() __asm("llvm.amdgcn.dispatch.id");

extern "C" __global__ __launch_bounds__(512, 2) void mega_kernel(const bf16* x, char* ws, const bf16* wqkv, const bf16* wo, const bf16* wr,
                                                                 const bf16* wg, const bf16* wu, const bf16* wd, bf16* out) {
  extern __shared__ __attribute__((aligned(16))) char smem[];
  unsigned long long echo_a = 0, echo_b = 0;
  if (ECHO_A == 63) asm volatile("s_memrealtime %0\n\ts_waitcnt lgkmcnt(0)" : "=s"(echo_a) :: "memory");
  const int w0_ = __builtin_amdgcn_readfirstlane(threadIdx.x >> 6);
  const unsigned lbase = (unsigned)(size_t)(LDS_AS char*)smem;
  {
    unsigned long long pc;
    unsigned len;
    asm volatile("s_getpc_b64 %0\n\ts_mov_b32 %1, .Ltext_end-mega_kernel\n\t.subsection 1\n.Ltext_end:\n\t.subsection 0" : "=s"(pc), "=s"(len));
    const unsigned o = ((blockIdx.x >> 3) * 64 + (threadIdx.x & 63)) * 128;
    if (w0_ == 7 && o < len)
      asm volatile("s_mov_b32 m0, %0\n\ts_nop 0\n\tglobal_load_lds_dword %1, %2" ::"s"(lbase + L_TOUCH0), "v"(o), "s"(pc & ~127ull) : "memory");
  }
  asm volatile("" : "+s"(x), "+s"(ws), "+s"(wqkv));
#define IDS()                                                                                         \
  int tid = threadIdx.x, w = w0_, bid = blockIdx.x;                                                   \
  asm volatile("" : "+v"(tid));                                                                       \
  asm volatile("" : "+s"(w));                                                                         \
  asm volatile("" : "+s"(bid));                                                                       \
  const int l = tid & 63, xcd = bid & 7, bi = bid >> 3;                                               \
  const int p = (xcd & 1) * 4 + (bi & 3), h = (xcd >> 1) * 8 + (bi >> 2);         \
  const int rb = 2 * xcd + (bi & 1), ct = bi >> 1;                               \
  const int slice = xcd * 28 + bi;                                                   \
  const bool ffn_on = bi < 28;                                                                        \
  const int g = l >> 4;                                                                               \
  KARGS();                                                                                            \
  (void)p; (void)h; (void)rb; (void)ct; (void)slice; (void)ffn_on; (void)g;
  const rsrc_t rs = __builtin_amdgcn_make_buffer_rsrc((void*)ws, 0, WS_BYTES, 0x00020000);
  typedef const void* const __attribute__((address_space(4)))* karg_t;
#define KARGS() karg_t ka = (karg_t)__builtin_amdgcn_kernarg_segment_ptr(); asm volatile("" : "+s"(ka))
#define ARGP(name, i) const GAS bf16* name = (const GAS bf16*)ka[i]; asm volatile("" : "+s"(name))
#define RSRC(ptr, bytes) __builtin_amdgcn_make_buffer_rsrc((void*)(ptr), 0, bytes, 0x00020000)
#define rwq RSRC(pwq, 1024 * 3072 * 2)
#define rwo RSRC(pwo, 1024 * 1024 * 2)
#define rwg RSRC(pwg, 1024 * F_ * 2)
#define rwu RSRC(pwu, 1024 * F_ * 2)
#define rwd RSRC(pwd, 1024 * F_ * 2)
#define rout RSRC(pout, 2048 * 1024 * 2)

#define WQ_VOFF() ((l >> 2) * 6144 + (((l & 3) ^ (((l >> 4) & 1) << 1)) << 4))
#define WQ_PIECE(t, j, dst) dma16(rwq, wqv, ((((t) + rot) & 7) * 128 + ((j) & 7) * 16) * 6144 + ((j) >> 3) * 2048 + h * 64, lbase + (dst) + (j) * 1024)
#define WQ_BOFF(dt) ((4 * g + ((l & 15) >> 2)) * 64 + (l & 3) * 8 + (((dt) ^ (g & 1)) << 5))
#define TOUCH_TO(ptr, dst) asm volatile("s_mov_b32 m0, %0\n\ts_nop 0\n\tglobal_load_lds_dword %1, off" ::"s"(lbase + (dst) + w * 256), "v"(ptr) : "memory")
#define TOUCH(ptr) TOUCH_TO(ptr, L_TOUCH)
#define WO_ROW(j) ((j) < 8 ? ((j) >> 2) * 64 + ((j) & 3) * 8 : (((j) >> 1) & 1) * 64 + ((j) >> 2) * 16 + ((j) & 1) * 8)
#define WO_LDS(j) ((j) < 8 ? 98304 + (j) * 8192 : (j) < 12 ? 65536 + ((j) - 8) * 8192 : 32768 + ((j) - 12) * 8192)
#define WO_PIECE(j) dma16(rwo, (l >> 3) * 2048 + (((((l & 7) >> 1) ^ ((l >> 4) & 3)) * 2 + (l & 1)) << 4), (WO_ROW(j) + w) * 16384 + ct * 128, lbase + WO_LDS(j) + w * 1024)
#define WD_DMA() _Pragma("unroll") for (int i = 0; i < 4; ++i) WD_DMA1(i)
#define WD_DMA1(i)                                                                                   \
      dma16(rwd, (l >> 4) * 2048 + (l & 15) * 16, (unsigned)(slice * 16 + 4 * (i)) * 2048 + w * 256, lbase + L_DN + w * 4096 + (i) * 1024)
#define FFN_DMA(m, dst) dma16((m) & 1 ? rwu : rwg, (l >> 1) * (F_ * 2) + (l & 1) * 16, (unsigned)(((4 * w + ((m) >> 1)) * 32) * F_ + slice * 16) * 2, lbase + (dst) + ((m) & 3) * 1024)
#define FFN_FRAGS(i0, src)                                                                        \
  asm volatile("" : "=v"(gv[i0]), "=v"(uv[i0]), "=v"(gv[(i0) + 1]), "=v"(uv[(i0) + 1]));          \
  if (ffn_on) {                                                                                   \
    const unsigned ro = (src) + (4 * g + ((l & 15) >> 2)) * 32 + (l & 3) * 8;                     \
    _Pragma("unroll") for (int i = 0; i < 2; ++i) {                                               \
      frag_t f;                                                                                   \
      f.hf.a = trread(smem, ro + i * 2048); f.hf.b = trread(smem, ro + i * 2048 + 512);           \
      gv[(i0) + i] = f.v;                                                                         \
      f.hf.a = trread(smem, ro + i * 2048 + 1024); f.hf.b = trread(smem, ro + i * 2048 + 1536);   \
      uv[(i0) + i] = f.v;                                                                         \
      asm volatile("" : "+v"(gv[(i0) + i]), "+v"(uv[(i0) + i]));                                  \
    }                                                                                             \
  }
  u32x4 gv[4], uv[4];
  u32x4 keep[9];
  u32x4 pre[4][2];
#define PRE_LOAD(it_, rot_, s0, s1)                                                                  \
  static_for<(s1) - (s0)>([&](auto c_) __attribute__((always_inline)) {                              \
    constexpr int s = (s0) + decltype(c_)::value;                                                    \
    const unsigned ab_ = OFF_XN + (it_) * (4u << 20) + (((rot_) + (s >> 2)) & 7) * 8192 + ((s >> 1) & 1) * 4096; \
    ld16w<2 * s, (s & 1) * 2048>(pre[s][0], rs, l * 16, ab_ + (p * 8 + w) * 65536);                         \
    ld16w<2 * s + 1, (s & 1) * 2048 + 1024>(pre[s][1], rs, l * 16, ab_ + (p * 8 + 7 - w) * 65536);              \
  })
  u32x2 T;
  const GAS bf16 *tsrc, *tsrc2;
  {
    IDS();
    const int it = 0, rot = p;
    (void)it;
    STAMPAT(0);
    const bf16* pwq = wqkv;
    const int r8 = l & 7, cq = l >> 3;
    const unsigned R = p * 256 + h * 8 + r8;
    LDS_AS f32x2* st = (LDS_AS f32x2*)(smem + L_ST0);
    LDS_AS unsigned* cnt = (LDS_AS unsigned*)(smem + L_CNT);
    const unsigned wqv = WQ_VOFF();
    auto transit = [&]() __attribute__((always_inline)) {
      const rsrc_t rx = RSRC(x, 2048 * 1024 * 2);
      const unsigned r0 = (p * 256 + w * 32) * 2048, r1 = (p * 256 + 240 - w * 32) * 2048;
#pragma unroll
      for (int j = 0; j < 8; ++j)
        dma16(rx, (l >> 4) * 2048 + (((l & 15) ^ (4 * (j & 3) + (l >> 4))) << 4), (j < 4 ? r0 : r1) + (j & 3) * 8192 + p * 256, lbase + w * 8192 + j * 1024);
    };
    if (w >= 4) {
      if (w == 4) TOUCH_TO((const GAS bf16*)x + (size_t)(p * 256 + (h & 7) * 32 + (l >> 1)) * 1024 + p * 128 + (l & 1) * 64, L_TOUCH0);
      {
        const int t = tid - 320;
        if (w > 4) TOUCH_TO((const GAS bf16*)wqkv + (size_t)(p * 128 + (h & 1) * 64 + t / 3) * 3072 + (t % 3) * 1024 + (h & 30) * 32, L_TOUCH0);
      }
      BAR();
      while (__hip_atomic_load(cnt + 4, __ATOMIC_RELAXED, __HIP_MEMORY_SCOPE_WORKGROUP) != 4u) __builtin_amdgcn_s_sleep(1);
#pragma unroll
      for (int i = 0; i < 6; ++i) WQ_PIECE(0, 6 * (w - 4) + i, L_RING);
      transit();
      if constexpr (!T1L) {
#pragma unroll
        for (int i = 0; i < 6; ++i) WQ_PIECE(1, 6 * (w - 4) + i, L_RING + STAGE);
        wait_vm<6>();
      } else {
        DRAIN();
      }
    } else {
      const u32x2 ep = ld8(rs, 0, F_EP);
      SB();
      u32x4 xl[4];
#pragma unroll
      for (int i = 0; i < 4; ++i) {
        const int c = w * 32 + i * 8 + cq;
        xl[i] = *(const GAS u32x4*)((const GAS bf16*)x + (size_t)R * 1024 + 32 * (c >> 2) + ((c & 1) ? 16 + 4 * ((c & 3) - 1) : 4 * (c & 3)));
      }
      SB();
      if (w == 1 && l < 37) {
        const unsigned prb = 2 * p + (h >> 4), pct = h & 15;
        if (l < 36) st16(u32x4{POISON, POISON, POISON, POISON}, rs, l < 8 ? OFF_CA + prb * 2048 + pct * 128 + l * 16 : l < 32 ? OFF_CS + ((h * 6 + ((l - 8) >> 2)) * 8 + p) * 64 + (l & 3) * 16
                                                                    : OFF_AB + (p * 32 + h) * 32 + (l & 1) * 16 + (l & 2) * 4096, 0);
        else st4(POISON, rs, OFF_KEY + pct * 128 + prb * 4, 0);
      }
      SB();
      if (tid < 5) cnt[tid] = 0;
      BAR();
      STAMPAT(38);
      f32x2 v[16];
#pragma unroll
      for (int i = 0; i < 4; ++i) {
        const bool odd = cq & 1;
        const unsigned r0 = dppu<0x128>(odd ? xl[i][0] : xl[i][2]), r1 = dppu<0x128>(odd ? xl[i][1] : xl[i][3]);
        xl[i] = odd ? u32x4{r0, r1, xl[i][2], xl[i][3]} : u32x4{xl[i][0], xl[i][1], r0, r1};
        const int c = w * 32 + i * 8 + cq;
        st16(xl[i], rs, toff(R, c >> 2, c & 3), OFF_XN);
      }
      SB();
      if (l == 0) __hip_atomic_fetch_add(cnt + 4, 1u, __ATOMIC_RELAXED, __HIP_MEMORY_SCOPE_WORKGROUP);
      SB();
      transit();
#pragma unroll
      for (int i = 0; i < 4; ++i) unpack8v(xl[i], v + 4 * i);
      f32x2 s = {0.f, 0.f};
#pragma unroll
      for (int j = 0; j < 16; j += 2) s += v[j] + v[j + 1];
      const float mw = sum_hi8(hadd(s)) * (1.0f / 256.0f);
      const f32x2 m2 = {mw, mw};
      f32x2 q0 = {0.f, 0.f}, q1 = {0.f, 0.f};
#pragma unroll
      for (int j = 0; j < 16; j += 2) {
        const f32x2 d0 = v[j] - m2, d1 = v[j + 1] - m2;
        q0 = __builtin_elementwise_fma(d0, d0, q0); q1 = __builtin_elementwise_fma(d1, d1, q1);
      }
      q0 += q1;
      const float qq = sum_hi8(hadd(q0));
      if (l < 8) st[w * 8 + l] = f32x2{mw, qq};
      STAMPAT(27);
      const unsigned long long e = (((unsigned long long)ep[1] << 32) | ep[0]) + 1ull + dispatch_id() * 0x9E3779B97F4A7C16ull;
      const u32x2 tg = {(unsigned)e, (unsigned)(e >> 32)};
      if (tid == 0) *(LDS_AS u32x2*)(cnt + 2) = tg;
      wait_vm<8>();
      unsigned arr = 0;
      if (l == 0) arr = __hip_atomic_fetch_add(cnt, 1u, __ATOMIC_ACQ_REL, __HIP_MEMORY_SCOPE_WORKGROUP);
      if (__builtin_amdgcn_readfirstlane(arr) == 3u) {
        if (l < RX) st8(tg, rs, F_XN + (p * RX + l) * 256 + h * 8, 0);
        const f32x2 pw = st[l & 31];
        float mean = pw[0] + dppf<0x128>(pw[0]);
        mean = add_x16(mean) * 0.25f;
        const float d = pw[0] - mean;
        float var = __builtin_fmaf(256.0f * d, d, pw[1]);
        var += dppf<0x128>(var);
        const float rd = ln_rd(add_x16(var) * (1.0f / 1024.0f));
        if (l < 16) st4(min(__builtin_bit_cast(unsigned, l < 8 ? rd : -rbf1(mean)), POISON - 1), rs, OFF_AB + R * 4 + (l >> 3) * 8192, 0);
      }
      STAMPAT(28);
      SB();
      if (__builtin_amdgcn_readfirstlane(arr) == 3u) wait_vm<2>();
      else wait_vm<0>();
    }
    {
      ARGP(pwo, 3); ARGP(pwg, 5); ARGP(pwu, 6); ARGP(pwd, 7);
      const int t = tid < 288 ? tid - 64 : tid - 288, go = (bi * 32 + t / 7) * F_ + xcd * 448 + (t % 7) * 64;
      tsrc = tid < 64 ? pwo + bid * 4096 + tid * 64 : tid < 288 ? pwg + go : pwu + go;
      tsrc2 = pwd + (size_t)slice * 16384 + tid * 64;
      asm volatile("" : "+v"(tsrc), "+v"(tsrc2));
    }
    STAMPAT(29);
#pragma unroll
    for (int s = 0; s < 4; ++s)
#pragma unroll
      for (int a = 0; a < 2; ++a) {
        const int r = l & 15;
        const unsigned o = w * 8192 + (a * 16 + r) * 256 + (((s * 4 + (g >> 1)) ^ r) << 4);
        const u32x2 lo = *(const u32x2*)(smem + o + (g & 1) * 8), hi = *(const u32x2*)(smem + (o ^ 32) + (g & 1) * 8);
        pre[s][a] = u32x4{lo[0], lo[1], hi[0], hi[1]};
      }
    static_for<4>([&](auto c_) __attribute__((always_inline)) { pin_w<decltype(c_)::value>(pre[decltype(c_)::value][0], pre[decltype(c_)::value][1]); });
    if (tid == 0) *(LDS_AS u32x4*)(smem + L_QC) = u32x4{0u, 0u, 0u, 0u};
    BAR();
    {
      const u32x2 tg = *(LDS_AS u32x2*)(cnt + 2);
      T = u32x2{(unsigned)__builtin_amdgcn_readfirstlane(tg[0]), (unsigned)__builtin_amdgcn_readfirstlane(tg[1])};
    }
    STAMPAT(31);
  }
  unsigned sel0 = 0x7FFFFFFFu;
  f32x2 v[8];
  u32x4 x2p[2];
  float wsum;
  const GAS bf16* pwd0 = nullptr;

  auto layer = [&](auto it_c) __attribute__((always_inline)) {
    constexpr int it = decltype(it_c)::value;
    STAMPAT(1); STAMPAT(22);
    f32x4 acc[2][6];
    f32x2 qs2[2];
    {
      IDS();
      bool aff = false;
      unsigned aq[2];
      u32x4 av[2];
#define PIN_ACC() asm volatile("" : "+{v[176:179]}"(acc[0][0]), "+{v[180:183]}"(acc[0][1]), "+{v[184:187]}"(acc[0][2]), "+{v[188:191]}"(acc[0][3]), "+{v[192:195]}"(acc[0][4]), "+{v[196:199]}"(acc[0][5]), \
                               "+{v[200:203]}"(acc[1][0]), "+{v[204:207]}"(acc[1][1]), "+{v[208:211]}"(acc[1][2]), "+{v[212:215]}"(acc[1][3]), "+{v[216:219]}"(acc[1][4]), "+{v[220:223]}"(acc[1][5]))
#define PINV4(x) asm volatile("" : "+v"(x[0]), "+v"(x[1]), "+v"(x[2]), "+v"(x[3])::"memory")
      auto qkv_loop = [&](auto it_c) __attribute__((always_inline)) {
        constexpr int IT = decltype(it_c)::value;
        const unsigned abase[2] = {OFF_XN + IT * (4u << 20) + (p * 8 + w) * 65536, OFF_XN + IT * (4u << 20) + (p * 8 + 7 - w) * 65536};
        const int rot = p + 5 * IT;
        unsigned fkey = 0, kv, bq[2];
        u32x4 fx[4], bv[2], af[WA][2];
        u32x2 cxn, cx1, hh;
        f32x4 sa, sb, cs4[2];
        f32x2 b2[2];
        s16x4 dfr[8];
#define AB_OFF(a) (OFF_AB + IT * 16384 + (p * 16 + ((a) ? 15 - 2 * w : 2 * w)) * 64)
#define A_LOAD1(k, a) ld16w<(k) % WA * 2 + (a), ((k) & 1) * 2048 + (a) * 1024>(af[(k) % WA][a], rs, l * 16, abase[a] + ((((k) >> 2) + rot) & 7) * 8192 + (((k) >> 1) & 1) * 4096)
#pragma unroll
        for (int s = 0; s < 4; ++s) { af[s][0] = pre[s][0]; af[s][1] = pre[s][1]; }
        const bf16* pwq = wqkv;
        const int tstep = TS0 + bid % TSN;
        const GAS bf16* pwd = pwd0;
        const unsigned wqv = WQ_VOFF();
#define QKV_DMA1(t, i)                                                                            \
  {                                                                                               \
    if constexpr (IT == 0) WQ_PIECE(t, 3 * w + (i), L_RING + SLOT(t) * STAGE);                    \
    else dma16s<(i) * 1024>(rs, l * 16, OFF_WI + (h * 8 + (((t) + rot) & 7)) * STAGE + w * 3072, lbase + L_RING + SLOT(t) * STAGE + w * 3072); \
  }
        unsigned bo[2][2];
#pragma unroll
        for (int dt = 0; dt < 2 - IT; ++dt) {
          bo[dt][0] = lbase + L_RING + (IT ? l * 16 : WQ_BOFF(dt)); bo[dt][1] = bo[dt][0] + 2 * STAGE;
          asm volatile("" : "+v"(bo[dt][0]), "+v"(bo[dt][1]));
        }
        auto frag0 = [&](int sl, int f) __attribute__((always_inline)) {
          const unsigned o = (bo[0][0] ^ ((f & 1) << 5)) + sl * STAGE + ((f % 6) >> 1) * 8192 + (f / 6) * 2048;
          frag_t t;
          t.hf.a = trabs(o); t.hf.b = trabs(o + 1024);
          return t.v;
        };
        u32x4 df[4];
        auto dot_mma = [&](const u32x4 (&xf)[4]) __attribute__((always_inline)) {
          sa = mfma16(df[0], xf[0], f32x4{0.f, 0.f, 0.f, 0.f}); sb = mfma16(df[1], xf[1], f32x4{0.f, 0.f, 0.f, 0.f});
          sa = mfma16(df[2], xf[2], sa); sb = mfma16(df[3], xf[3], sb);
        };
        auto dot_store = [&](unsigned dst) __attribute__((always_inline)) {
          if ((l & 15) == 0) st16(unpoison4(__builtin_bit_cast(u32x4, sa + sb)), rs, g * 16, dst + ((h * 6 + w) * 8 + p) * 64);
        };
        auto tile_sum = [&](u32x4 pt, int dst) __attribute__((always_inline)) {
          f32x4 c = __builtin_bit_cast(f32x4, pt);
#pragma unroll
          for (int e = 0; e < 4; ++e) { c[e] += dppf<0x124>(c[e]); c[e] += dppf<0x128>(c[e]); c[e] = add_x16(c[e]); }
          if ((l & 28) == 0) *(LDS_AS f32x4*)(smem + dst + ((w - 5) * 2 + (l >> 5)) * 64 + (l & 3) * 16) = c;
        };
        frag_t bfr[6];
#define QKV_BO(s, n) (bo[(n) & 1][SLOT((s) >> 2) >> 1] + (SLOT((s) >> 2) & 1) * STAGE + ((n) >> 1) * 8192 + ((s) & 3) * 2048)
#define QKV_RDA(s, n)                                                                             \
  {                                                                                               \
    if constexpr (IT == 0) bfr[n].hf.a = trabs(QKV_BO(s, n));                                     \
    else bfr[n].v = *(const LDS_AS u32x4*)(size_t)(bo[0][SLOT((s) >> 2) >> 1] + (SLOT((s) >> 2) & 1) * STAGE + (((s) & 3) * 6 + (n)) * 1024); \
  }
#define QKV_RDB(s, n) { if constexpr (IT == 0) bfr[n].hf.b = trabs(QKV_BO(s, n) + 1024); }
        STAMPAT(39);
#pragma unroll
        for (int n = 0; n < 6; ++n) { QKV_RDA(0, n); if (n < 5) QKV_RDB(0, n); }
#define XF_OFF() (l & 8 ? ((l & 7) * RX + bi % RX) * 256 + h * 8 : (p * RX + (xcd >> 1) % RX) * 256 + ((l & 2 ? 30 - 4 * w : 4 * w) + (l & 1)) * 8)
#define XP_OFF() (OFF_XNEW + ((rot + 5) & 7) * 256)
#define FP_OFF(i) (w == 4 ? OFF_XNEW + (i) * 1024 : OFF_FX + h * 3072 + (w - 5) * 1024)
#define FX_OK(n) __all(((n) == 4 ? max(max(umax4(fx[0]), umax4(fx[1])), max(umax4(fx[2]), umax4(fx[3]))) : max(umax4(fx[0]), umax4(fx[1]))) != POISON)
#define SINCE(s0) (vm_at(IT, s, n) - vm_at(IT, s0, 5))
#define DEF4(x) asm volatile("" : "=v"(x[0]), "=v"(x[1]), "=v"(x[2]), "=v"(x[3]))
        LDS_AS unsigned* qcnt = (LDS_AS unsigned*)(smem + L_QC);
        unsigned qc;
        unsigned qadr = lbase + L_QC, qone = 1;
        asm volatile("" : "+v"(qadr), "+v"(qone));
#define count_in(i) asm volatile("s_mov_b64 exec, 1\n\tds_add_u32 %0, %1 offset:%2\n\ts_mov_b64 exec, -1" ::"v"(qadr), "v"(qone), "n"(4 * (i)) : "memory")
        auto count_wait = [&](int i, unsigned want) __attribute__((always_inline)) {
          if (__builtin_expect((unsigned)__builtin_amdgcn_readfirstlane(qc) < want, 0)) {
            while ((unsigned)__builtin_amdgcn_readfirstlane(__hip_atomic_load(qcnt + i, __ATOMIC_RELAXED, __HIP_MEMORY_SCOPE_WORKGROUP)) < want) {}
          }
          asm volatile("" ::: "memory");
        };
        unsigned long long xok = 0;
        unsigned xg;
        RowFin fin;
        auto x1_need = [&](unsigned long long m) __attribute__((always_inline)) {
          if (__builtin_expect((xok & m) != m, 0)) {
            for (unsigned spins = 0;;) {
              const u32x2 f = ld8(rs, F_X1 + (p * RX + (xcd >> 1) % RX) * 256 + (l & 31) * 8, 0);
              xok = __builtin_amdgcn_ballot_w64(f[0] == T[0] && f[1] == T[1]);
              if ((xok & m) == m) break;
              if (spin_fail(spins, rs, T)) { xok = ~0ull; break; }
              __builtin_amdgcn_s_sleep(KSL);
            }
            asm volatile("" ::: "memory");
          }
        };
        auto gap = [&](auto sc_, auto nc_) __attribute__((always_inline)) {
          constexpr int s = decltype(sc_)::value, n = decltype(nc_)::value;
          {
            constexpr int ts = (s + 3) / 4, tc = (s + 2) / 4, t0 = 1;
            if constexpr (ts >= 3 && ts < 8 && s == 4 * ts - 3 && n == SIGN) { wait_vm<vm_at(IT, s, n) - vm_tile(IT, ts)>(); count_in(0); }
            if constexpr (!IT && tc >= t0 && tc < 3 && s == 4 * tc - 2 && n == 1) { wait_vm<vm_at(IT, s, n) - vm_tile(IT, tc)>(); count_in(0); }
            if constexpr (IT && ts >= t0 && ts < 3 && s == 4 * ts - 3 && n == SIGN) count_in(0);
            if constexpr (tc >= t0 && tc < 8 && s == 4 * tc - 2) {
              if constexpr (n == 3) qc = __hip_atomic_load(qcnt, __ATOMIC_RELAXED, __HIP_MEMORY_SCOPE_WORKGROUP);
              if constexpr (n == 5) count_wait(0, 8 * (tc - t0 + 1));
            }
          }
          if constexpr (n == 0 && tile_piece(s)) QKV_DMA1(((s - 3) >> 2) + 3, (s - 3) & 3);
          if constexpr (IT == 0) {
            if constexpr (s == 0 && n == 0) asm volatile("buffer_load_dwordx2 %0, %1, %2, 0 offen sc1" : "=v"(cxn) : "v"(XF_OFF() + F_XN), "s"(rs) : "memory");
            if constexpr (T1L && s == 0 && n >= 1 && n < 4) QKV_DMA1(1, n - 1);
            if constexpr (s == 0 && n == 4) {
              const int i = bi * 192 + w * 24 + min(l, 23);
              TOUCH((const GAS bf16*)pwq + (size_t)(i / 12 + 512 * (~xcd & 1)) * 3072 + ((i % 12) >> 2) * 1024 + (xcd >> 1) * 256 + (i & 3) * 64);
            }
            if constexpr (s == 1 && n == 3) {
              wait_vm<T1L ? 4 : 1>();
              asm volatile("" : "+v"(cxn)::"memory");
              if (!__all(cxn[0] == T[0] && cxn[1] == T[1])) poll_flags(rs, XF_OFF(), F_XN, T);
            }
            if constexpr (s >= 2 && s < 5 && n == 2) QKV_DMA1(2, s - 2);
          } else {
#define X1_OFF() (F_X1 + (p * RX + (xcd >> 1) % RX) * 256 + (l & 31) * 8)
            if constexpr ((s == 0 && n == 0) || (s == 2 && n == 2)) asm volatile("buffer_load_dwordx2 %0, %1, %2, 0 offen sc1" : "=v"(cxn) : "v"(X1_OFF()), "s"(rs) : "memory");
            if constexpr ((s == 1 && n == 3) || (s == 4 && n == 0)) {
              wait_vm<vm_at(1, s, n) - (s == 1 ? vm_at(1, 0, 1) : vm_at(1, 2, 3))>();
              asm volatile("" : "+v"(cxn)::"memory");
              xok |= __builtin_amdgcn_ballot_w64(cxn[0] == T[0] && cxn[1] == T[1]);
            }
            if constexpr (a_req(s, n) >= 0 && (a_req(s, n) & 7) == 0) x1_need(0x30003ull << (((rot + a_req(s, n) / 8) & 7) * 2));
            const unsigned R = (rb * 4 + (w & 3)) * 32 + (w >> 2) * 16 + (l & 15), so = OFF_STAT + rb * 32768;
            if constexpr (s == SPL && n == 4) {
#pragma unroll
              for (int i = 0; i < 4; ++i) ld16h<0>(fx[i], rs, (4 * g + i) * 2048 + (R & 127) * 16, so);
            }
            if constexpr (s == SPL + 2 && n == 1) {
              wait_vm<SINCE(SPL)>();
              PINV4(fx);
              fin = fin_key(rs, T, g * 8192 + (R & 127) * 16, so, smem + L_SKEY, l, w, R, wsum, fx);
              count_in(2);
            }
            if constexpr (s == SPL + 3 && n == 0) qc = __hip_atomic_load(qcnt + 2, __ATOMIC_RELAXED, __HIP_MEMORY_SCOPE_WORKGROUP);
            if constexpr (s == SPL + 3 && n == 1) { count_wait(2, 8); fin_cand(rs, smem + L_SKEY, 0, g, rb, ct, fin, v); STAMPAT(51); }
            if constexpr (s == SPL + 3 && n == 3) {
              fin_x2(fin, v);
              x2p[0] = pack8v(v); x2p[1] = pack8v(v + 4);
              asm volatile("" : "+v"(x2p[0]), "+v"(x2p[1]));
            }
            if constexpr (s == SPL + 3 && n == 5) {
              if ((w & 3) * 2 + (w >> 2) == ct >> 1) {
                const float f = 1.0f + fin.rd, rd2 = ln_rd(fin.var * f * f), mb2 = rbf1(__builtin_fmaf(fin.mean - fin.mb, fin.rd, fin.mean));
                if (g < 2 && ((l >> 3) & 1) == (ct & 1))
                  st4(min(__builtin_bit_cast(unsigned, g == 0 ? f * rd2 : -__builtin_fmaf(fin.mb, fin.rd, mb2) * __builtin_amdgcn_rcpf(f)), POISON - 1), rs, OFF_AB + 16384 + R * 4 + g * 8192, 0);
              }
            }
          }
          if constexpr (a_req(s, n) >= 0) A_LOAD1(a_req(s, n) / 2, a_req(s, n) & 1);
          if constexpr (IT == 0 && s == CSD) {
            if constexpr (n == 0) { DEF4(df); asm volatile("" : "=v"(sa), "=v"(sb)); if (w < 6) { for (int i = 0; i < 4; ++i) df[i] = frag0(SLOT(s >> 2), 6 * i + w); } }
            if constexpr (n == 2) {
              if (w < 6) {
                unsigned one2 = 0x3F803F80u;
                asm volatile("" : "+v"(one2));
                const u32x4 o4 = {one2, one2, one2, one2}, of[4] = {o4, o4, o4, o4};
                dot_mma(of);
              }
            }
            if constexpr (n == 4) { if (w < 6) dot_store(OFF_CS); }
          }
          constexpr int CSL = IT ? CSL1 : CSL0, CSS = CSL + 2;
          if constexpr (s == CSL && n == 4) { asm volatile("" : "=v"(fx[0])); if (w >= 5) ld16h<0>(fx[0], rs, l * 16, OFF_CS + h * 3072 + (w - 5) * 1024); }
          if constexpr (s == CSS && n == 2) {
            if (w >= 5) {
              wait_vm<SINCE(CSL)>();
              asm volatile("" : "+v"(fx[0])::"memory");
              for (unsigned spins = 0; !__all(umax4(fx[0]) != POISON) && !spin_fail(spins, rs, T);) fx[0] = ld16(rs, l * 16, OFF_CS + h * 3072 + (w - 5) * 1024);
              tile_sum(fx[0], L_CSL);
            }
          }
          if constexpr (s == ABL && n == 4) {
#pragma unroll
            for (int a = 0; a < 2; ++a) {
              ld4h<0>(aq[a], rs, (l & 15) * 4, AB_OFF(a)); ld4h<0>(bq[a], rs, (l & 15) * 4, AB_OFF(a) + 8192);
              ld16h<0>(av[a], rs, g * 16, AB_OFF(a)); ld16h<0>(bv[a], rs, g * 16, AB_OFF(a) + 8192);
            }
          }
          if constexpr (s == ABS && n == 2) {
#define AB_OK() __all(max(max(max(aq[0], aq[1]), max(bq[0], bq[1])), max(max(umax4(av[0]), umax4(av[1])), max(umax4(bv[0]), umax4(bv[1])))) != POISON)
            wait_vm<SINCE(ABL)>();
            asm volatile("" : "+v"(aq[0]), "+v"(aq[1]), "+v"(bq[0]), "+v"(bq[1]), "+v"(av[0]), "+v"(av[1]), "+v"(bv[0]), "+v"(bv[1])::"memory");
            if (!AB_OK()) {
              unsigned spins = 0;
              do {
                if (spin_fail(spins, rs, T)) break;
#pragma unroll
                for (int a = 0; a < 2; ++a) {
                  aq[a] = ld4(rs, (l & 15) * 4, AB_OFF(a)); bq[a] = ld4(rs, (l & 15) * 4, AB_OFF(a) + 8192);
                  av[a] = ld16(rs, g * 16, AB_OFF(a)); bv[a] = ld16(rs, g * 16, AB_OFF(a) + 8192);
                }
              } while (!AB_OK());
            }
#undef AB_OK
          }
          if constexpr (s == ABS && n == 5) {
#pragma unroll
            for (int a = 0; a < 2; ++a) { b2[a] = f32x2{asf(bq[a]), asf(bq[a])}; asm volatile("" : "+v"(b2[a])); }
          }
          if constexpr (s == ABS + 1) {
            constexpr int k = (n + 4) % 6;
            if constexpr (k < 4) cs4[k & 1] = *(const LDS_AS f32x4*)(smem + L_CSL + k * 64 + g * 16);
            else cs4[k & 1][0] = *(const LDS_AS float*)(smem + L_CSL + k * 64 + (l & 15) * 4);
          }
          if constexpr ((s == ABS + 1 && n > 0) || (s == ABS + 2 && n == 0)) {
            constexpr int k = (n + 3) % 6;
            if constexpr (k < 4) {
#pragma unroll
              for (int a = 0; a < 2; ++a) acc[a][k] = __builtin_elementwise_fma(cs4[k & 1], f32x4{b2[a][0], b2[a][1], b2[a][0], b2[a][1]}, acc[a][k]);
            } else {
              f32x2 c2 = {cs4[k & 1][0], cs4[k & 1][0]};
              asm volatile("" : "+v"(c2));
#pragma unroll
              for (int a = 0; a < 2; ++a) acc[a][k] = __builtin_elementwise_fma(f32x4{c2[0], c2[1], c2[0], c2[1]}, __builtin_bit_cast(f32x4, bv[a]), acc[a][k]);
            }
          }
          if constexpr (IT == 0) {
            if constexpr (s == POI && n == 4) {
              unsigned pz1 = POISON;
              asm volatile("" : "+v"(pz1));
              const u32x4 pz = {pz1, pz1, pz1, pz1};
              const unsigned prb = 2 * p + (h >> 4), pct = h & 15;
              st16(pz, rs, OFF_PART + (tid >> 8) * (1u << 20) + bid * 4096 + (tid & 255) * 16, 0);
              if (w < 4) st16(pz, rs, OFF_STAT + (tid >> 7) * 524288 + (prb * 16 + pct) * 2048 + (tid & 127) * 16, 0);
              if (w == 4 && l < 24) st16(pz, rs, (l < 8 ? OFF_CA + 32768 : l < 16 ? OFF_CB : OFF_CB + 32768) + prb * 2048 + pct * 128 + (l & 7) * 16, 0);
              if (w == 5 && l < 4) st16(pz, rs, OFF_AB + 16384 + (p * 32 + h) * 32 + (l & 1) * 16 + (l >> 1) * 8192, 0);
              if (w == 7 && l < 24) st16(pz, rs, OFF_FX + ((h * 6 + (l >> 2)) * 8 + p) * 64 + (l & 3) * 16, 0);
              if (w == 6 && l < 3) st4(pz1, rs, l < 2 ? OFF_XNEW + bid * 8 + l * 4 : OFF_KEY + (16 + pct) * 128 + prb * 4, 0);
            }
            if constexpr (s == IMG && n < 3) fx[n] = frag0(SLOT(s >> 2), 3 * w + n);
            if constexpr (s == IMG && n == 4) {
#pragma unroll
              for (int i = 0; i < 3; ++i) st16(fx[i], rs, l * 16 + i * 1024, OFF_WI + (h * 8 + ((rot + (s >> 2)) & 7)) * STAGE + w * 3072);
            }
            if constexpr (s >= 29 && (n & 1)) keep[3 * (s - 29) + (n >> 1)] = frag0(SLOT(s - 24), 3 * w + (n >> 1));
            if constexpr (s >= TS0 && s < TS0 + TSN && n == 4) {
              if (tstep == s && w < TW) {
                TOUCH(tsrc);
                if (TWD && w < 4 && ffn_on) TOUCH(tsrc2);
              }
            }
            if constexpr (s == TST && n == 4) { if (w >= TW) TOUCH(tsrc); }
          } else {
            if constexpr (WDL && s >= WDL && s < WDL + 4 && n == 4) { if (ffn_on) WD_DMA1(s - WDL); }
            if constexpr (s == FSL - 2 && n == 4) ld4h<0>(kv, rs, (l & 15) * 4, OFF_KEY + (bid & 15) * 128);
            if constexpr (s == FSL && n == 4) {
              wait_vm<SINCE(FSL - 2)>();
              asm volatile("" : "+v"(kv)::"memory");
              xg = ffn_xoff(__builtin_amdgcn_readfirstlane(umax16(kv == POISON ? 0u : kv)), 0, w);
              ld4h<0>(kv, rs, (l & 15) * 4, OFF_KEY + (bid & 15) * 128);
              ld16h<0>(fx[0], rs, g * 16, xg); ld16h<64>(fx[1], rs, g * 16, xg); ld16h<128>(fx[2], rs, g * 16, xg); ld16h<192>(fx[3], rs, g * 16, xg);
            }
            if constexpr (s == FS0 && n == 1) {
              wait_vm<SINCE(FSL)>();
              asm volatile("" : "+v"(kv)::"memory");
              PINV4(fx);
              STAMPAT(52);
              fkey = key_wait(rs, T, l, 0, bid & 15, kv);
              STAMPAT(53);
              sel0 = key_row(fkey);
              aff = (int)(sel0 >> 8) == p;
            }
            if constexpr (s == FS0 && n == 2) {
              asm volatile("" : "=v"(sa), "=v"(sb));
              if (ffn_on) {
                if (xg != ffn_xoff(fkey, 0, w)) {
#pragma unroll
                  for (int i = 0; i < 4; ++i) fx[i] = ld16(rs, g * 16 + i * 64, ffn_xoff(fkey, 0, w));
                }
                ffn_gu(gv, uv, fx, sa, sb);
              }
            }
            if constexpr (s == FS0 && n == 3) {
              if (ffn_on) {
                ffn_gu_check(rs, T, ffn_xoff(fkey, 0, w), l, gv, uv, fx, sa, sb);
                ffn_gu_store(smem, l, w, sa, sb);
                count_in(1);
              }
            }
            if constexpr (s == FS0D) {
              if constexpr (n == 0) qc = __hip_atomic_load(qcnt + 1, __ATOMIC_RELAXED, __HIP_MEMORY_SCOPE_WORKGROUP);
              if constexpr (n == 1) {
                asm volatile("" : "=v"(dfr[0]), "=v"(dfr[1]), "=v"(dfr[2]), "=v"(dfr[3]), "=v"(dfr[4]), "=v"(dfr[5]), "=v"(dfr[6]), "=v"(dfr[7]), "=v"(hh), "=v"(sa));
                if (ffn_on) { count_wait(1, 8); ffn_d_read(smem, l, w, sa, dfr); }
              }
              if constexpr (n == 2) {
                if (ffn_on) {
                  for (int e = 0; e < 4; ++e) sa[e] = ffn_d_h(ffn_d_sum(sa[e]));
                  hh = u32x2{pack2(sa[0], sa[1]), pack2(sa[2], sa[3])};
                }
              }
              if constexpr (n == 3) {
                asm volatile("" : "=v"(sa), "=v"(sb));
                if (ffn_on) {
                  sa = f32x4{0.f, 0.f, 0.f, 0.f}; sb = sa;
#pragma unroll
                  for (int i = 0; i < 8; i += 2) { sa = ffn_d_mma(l, i, dfr[i], hh, sa); sb = ffn_d_mma(l, i + 1, dfr[i + 1], hh, sb); }
                }
              }
              if constexpr (n == 4) { if (ffn_on) ffn_d_store(rs, l, w, 0, slice, sa + sb); STAMPAT(54); }
            }
            if constexpr (s == FS1 && n == 4) {
              DEF4(fx);
              asm volatile("" : "=v"(cxn), "=v"(cx1));
              if (!ffn_on) {
                const unsigned G = xcd * 4 + bi - 28, co = (sel0 >> 7) * 2048 + G * 64, jj = (tid & 3) * 16 + (tid & 4) * 2;
#pragma unroll
                for (int k = 0; k < 4; ++k) ld16h<0>(fx[k], rs, min(tid + 512 * k, 1791) * 16, OFF_PART + G * 32768);
                ld8h(cxn, rs, jj, OFF_CA + co); ld8h(cx1, rs, jj, OFF_CB + co);
              }
            }
            if constexpr (s == FS1S && n == 2) {
              if (!ffn_on) {
                wait_vm<SINCE(FS1)>();
                PINV4(fx);
                asm volatile("" : "+v"(cxn), "+v"(cx1)::"memory");
                STAMPAT(55);
                red_sum(rs, smem, T, tid, w, 0, xcd * 4 + bi - 28, fkey, fx, cxn, cx1);
                STAMPAT(56);
                BAR();
                if (w == 0) {
                  const u32x2 ov = red_final(smem, l, fkey, cxn, cx1);
                  if (l < 8) st8(unpoison2(ov), rs, OFF_XNEW + ((xcd * 4 + bi - 28) * 8 + (l & 3) * 2 + (l >> 2)) * 8, 0);
                }
              }
            }
            if constexpr (s == FXA && n == 4) { ld16h<0>(fx[0], rs, g * 16, XP_OFF()); ld16h<64>(fx[1], rs, g * 16, XP_OFF()); ld16h<128>(fx[2], rs, g * 16, XP_OFF()); ld16h<192>(fx[3], rs, g * 16, XP_OFF()); }
            if constexpr (s == FXB) {
#define FX_FRAG(i) (*(const LDS_AS u32x4*)(size_t)(bo[0][0] + SLOT(5) * STAGE + (6 * (i) + min(w, 5)) * 1024))
              if constexpr (n == 0) { STAMPAT(58); df[0] = FX_FRAG(0); df[1] = FX_FRAG(1); }
              if constexpr (n == 1) {
                wait_vm<SINCE(FXA)>();
                PINV4(fx);
                sa = mfma16(df[0], fx[0], f32x4{0.f, 0.f, 0.f, 0.f}); sb = mfma16(df[1], fx[1], f32x4{0.f, 0.f, 0.f, 0.f});
                SB();
                df[0] = FX_FRAG(2); df[1] = FX_FRAG(3);
              }
              if constexpr (n == 2) { sa = mfma16(df[0], fx[2], sa); sb = mfma16(df[1], fx[3], sb); }
              if constexpr (n == 4) {
                const float z = sa[0] + sb[0];
                if (__any(z != z)) {
                  ffn_xwait(rs, T, XP_OFF(), l, fx);
                  f32x4 t0 = mfma16(FX_FRAG(0), fx[0], f32x4{0.f, 0.f, 0.f, 0.f});
                  t0 = mfma16(df[0], fx[2], t0);
                  f32x4 t1 = mfma16(FX_FRAG(1), fx[1], f32x4{0.f, 0.f, 0.f, 0.f});
                  sa = t0; sb = mfma16(df[1], fx[3], t1);
                }
                if (w < 6) dot_store(OFF_FX);
                STAMPAT(57);
              }
            }
            if constexpr (s == FXL && n == 4) {
              asm volatile("" : "=v"(fx[0]), "=v"(fx[1]));
              if (aff && w >= 4) { ld16h<0>(fx[0], rs, l * 16, FP_OFF(0)); ld16h<0>(fx[1], rs, l * 16, FP_OFF(1)); }
            }
            if constexpr (s == FXS && n == 4) {
              if (aff && w >= 4) {
                STAMPAT(59);
                wait_vm<0>();
                asm volatile("" : "+v"(fx[0]), "+v"(fx[1])::"memory");
                if (!FX_OK(2)) {
                  unsigned spins = 0;
                  do {
                    if (spin_fail(spins, rs, T)) break;
                    fx[0] = ld16(rs, l * 16, FP_OFF(0)); fx[1] = ld16(rs, l * 16, FP_OFF(1));
                  } while (!FX_OK(2));
                }
                STAMPAT(60);
                if (w == 4) {
                  f32x2 nx[8];
                  unpack8v(fx[0], nx); unpack8v(fx[1], nx + 4);
                  const f32x2 sm = ((nx[0] + nx[1]) + (nx[2] + nx[3])) + ((nx[4] + nx[5]) + (nx[6] + nx[7]));
                  const float mean = sum64(hadd(sm)) * (1.0f / 1024.0f);
                  const f32x2 m2 = {mean, mean};
                  f32x2 q = {0.f, 0.f};
#pragma unroll
                  for (int j = 0; j < 8; ++j) { const f32x2 d = nx[j] - m2; q = __builtin_elementwise_fma(d, d, q); }
                  const float rd = ln_rd(sum64(hadd(q)) * (1.0f / 1024.0f));
                  if (l == 0) *(LDS_AS f32x2*)(smem + L_FXL + 384) = f32x2{rd, -rbf1(mean)};
                } else {
                  tile_sum(fx[0], L_FXL);
                }
                count_in(3);
              }
            }
          }
        };
        static_for<32>([&](auto sc_) __attribute__((always_inline)) {
          constexpr int s = decltype(sc_)::value;
          if constexpr (s > 0) PIN_ACC();
          if constexpr (IT == 1 || s >= 4) wait_vm<vm_at(IT, s, 0) - vm_a(IT, s)>();
          pin_w<s % WA>(af[s % WA][0], af[s % WA][1]);
          SB();
          static_for<6>([&](auto nc_) __attribute__((always_inline)) {
            constexpr int n = decltype(nc_)::value;
            if constexpr (s == 0) { acc[0][n] = f32x4{0.f, 0.f, 0.f, 0.f}; acc[1][n] = acc[0][n]; }
            acc[0][n] = n < 4 ? mfma16(bfr[n].v, af[s % WA][0], acc[0][n]) : mfma16(af[s % WA][0], bfr[n].v, acc[0][n]);
            SB();
            if constexpr (n == 0) { QKV_RDB(s, 5); }
            else if constexpr (s + 1 < 32) QKV_RDB(s + 1, n - 1);
            SB();
            acc[1][n] = n < 4 ? mfma16(bfr[n].v, af[s % WA][1], acc[1][n]) : mfma16(af[s % WA][1], bfr[n].v, acc[1][n]);
            SB();
            if constexpr (s + 1 < 32) QKV_RDA(s + 1, n);
            SB();
            gap(sc_, nc_);
            SB();
          });
        });
        PIN_ACC();
      };
      qkv_loop(it_c);
      STAMPAT(18);
      if (aff) {
        const int ssub = (int)(sel0 >> 4) & 15, sr = sel0 & 15;
        if (ssub == 2 * w || ssub == 15 - 2 * w) {
          while (__hip_atomic_load((LDS_AS unsigned*)(smem + L_QC) + 3, __ATOMIC_RELAXED, __HIP_MEMORY_SCOPE_WORKGROUP) < 4u) {}
          asm volatile("" ::: "memory");
          const bool m0 = !(ssub & 1), mq = (l & 15) == sr, mv = g == (sr >> 2);
          const f32x2 nab = *(const LDS_AS f32x2*)(smem + L_FXL + 384);
#pragma unroll
          for (int n = 0; n < 4; ++n) {
            f32x4 t = *(const LDS_AS f32x4*)(smem + L_FXL + n * 64 + g * 16);
            const f32x4 c = *(const LDS_AS f32x4*)(smem + L_CSL + n * 64 + g * 16);
#pragma unroll
            for (int e = 0; e < 4; ++e) { t[e] = __builtin_fmaf(c[e], nab[1], t[e]); asm volatile("" : "+v"(t[e])); }
            acc[0][n] = (mq && m0) ? t : acc[0][n];
            acc[1][n] = (mq && !m0) ? t : acc[1][n];
          }
          const LDS_AS float* fl_ = (const LDS_AS float*)(smem + L_FXL + 256) + (l & 15);
          const LDS_AS float* cl_ = (const LDS_AS float*)(smem + L_CSL + 256) + (l & 15);
          const float t4 = __builtin_fmaf(cl_[0], nab[1], fl_[0]), t5 = __builtin_fmaf(cl_[16], nab[1], fl_[16]);
          const unsigned na = __builtin_bit_cast(unsigned, (float)nab[0]);
#pragma unroll
          for (int a = 0; a < 2; ++a) {
            const bool ma = a ? !m0 : m0;
            aq[a] = (mq && ma) ? na : aq[a];
#pragma unroll
            for (int e = 0; e < 4; ++e) {
              const bool me = mv && ma && e == (sr & 3);
              acc[a][4][e] = me ? t4 : acc[a][4][e]; acc[a][5][e] = me ? t5 : acc[a][5][e];
              av[a][e] = me ? na : av[a][e];
            }
          }
        }
      }
      constexpr float C2 = 1.4426950408889634f * 0.17677669529663687f;
#pragma unroll
      for (int a = 0; a < 2; ++a) {
        f32x2 a2 = {asf(aq[a]), asf(aq[a])};
        qs2[a] = f32x2{asf(aq[a]) * C2, asf(aq[a]) * C2};
        asm volatile("" : "+v"(a2), "+v"(qs2[a]));
        acc[a][2] *= f32x4{a2[0], a2[1], a2[0], a2[1]}; acc[a][3] *= f32x4{a2[0], a2[1], a2[0], a2[1]};
        acc[a][4] *= __builtin_bit_cast(f32x4, av[a]); acc[a][5] *= __builtin_bit_cast(f32x4, av[a]);
      }
    }
#undef A_LOAD1
#undef QKV_RDA
#undef QKV_RDB
#undef QKV_BO
    STAMPAT(2); STAMPAT(21);
    {
      IDS();
      const GAS bf16 *pwo, *pwg, *pwu;
      asm volatile("s_load_dwordx2 %0, %3, 24\n\ts_load_dwordx2 %1, %3, 40\n\ts_load_dwordx2 %2, %3, 48" : "=&s"(pwo), "=&s"(pwg), "=&s"(pwu) : "s"(ka));
      constexpr int KVF = KFV[2 * it], KD = KFV[2 * it + 1];
      const int ql = l & 15;
      const f32x4 z4 = {0.f, 0.f, 0.f, 0.f};
      u32x4 qf[2];
      f32x4 cn[2];
      f32x4 mk;
      const unsigned kvo = OFF_KV + it * (8u << 20) + (p * 32 + h) * 32768 + w * 4096;
      {
        const u32x4 k0 = pack8(acc[0][2], acc[0][3]), k1 = pack8(acc[1][2], acc[1][3]);
        u32x4 vh[2];
#pragma unroll
        for (int a = 0; a < 2; ++a) vh[a] = pack8(acc[a][4], acc[a][5]);
        if (p < 7) { st16(k0, rs, l * 16, kvo); st16(k1, rs, l * 16 + 1024, kvo); st16(vh[0], rs, l * 16 + 2048, kvo); st16(vh[1], rs, l * 16 + 3072, kvo); }
#define KV_TO_LDS(off, k0, k1, v0, v1)                                                            \
  {                                                                                               \
    *(u32x4*)(smem + L_K + (2 * (off) + 2 * w) * 1024 + l * 16) = k0;                             \
    *(u32x4*)(smem + L_K + (2 * (off) + 15 - 2 * w) * 1024 + l * 16) = k1;                        \
    *(u32x2*)(smem + L_V + ((off) + w) * 2048 + l * 16) = u32x2{v0[0], v0[1]};                    \
    *(u32x2*)(smem + L_V + ((off) + w) * 2048 + 1024 + l * 16) = u32x2{v0[2], v0[3]};             \
    *(u32x2*)(smem + L_V + ((off) + 7 - w) * 2048 + l * 16 + 8) = u32x2{v1[0], v1[1]};            \
    *(u32x2*)(smem + L_V + ((off) + 7 - w) * 2048 + 1024 + l * 16 + 8) = u32x2{v1[2], v1[3]};     \
  }
        KV_TO_LDS(8, k0, k1, vh[0], vh[1]);
        SB();
#pragma unroll
        for (int a = 0; a < 2; ++a) {
          const f32x4 c4 = {qs2[a][0], qs2[a][1], qs2[a][0], qs2[a][1]};
          qf[a] = pack8(acc[a][0] * c4, acc[a][1] * c4);
        }
#pragma unroll
        for (int e = 0; e < 4; ++e) mk[e] = (4 * g + e <= ql) ? 0.f : -INFINITY;
        const f32x4 d0 = mfma16(k0, qf[0], mk), d1 = mfma16(k1, qf[1], mk);
        const float m0 = -max_x32(max_x16(fmaxf(fmaxf(d0[0], d0[1]), fmaxf(d0[2], d0[3]))));
        const float m1 = -max_x32(max_x16(fmaxf(fmaxf(d1[0], d1[1]), fmaxf(d1[2], d1[3]))));
        cn[0] = f32x4{m0, m0, m0, m0}; cn[1] = f32x4{m1, m1, m1, m1};
        asm volatile("" : "+v"(cn[0]), "+v"(cn[1]), "+v"(qf[0]), "+v"(qf[1]));
      }
      const char* kb_ = smem + L_K + l * 16;
      const char* vb_ = smem + L_V + l * 16;
      int wq = 7 - w;
      unsigned ad[9];
#define SLOT_EV(m, PH) (((m) == 8) ? !(PH) : ((m) == 0) ? (PH) : (((m) > wq) != (PH)))
#define SLOT_GRP(m, PH) (SLOT_EV(m, PH) ? w + (m) : 15 - w - (m))
#define SLOT_ADDR(PH)                                                                             \
  _Pragma("unroll") for (int m = 0; m < 9; ++m) {                                                 \
    ad[m] = lbase + L_K + l * 16 + SLOT_GRP(m, PH) * 2048;                                        \
    asm volatile("" : "+v"(ad[m]));                                                               \
  }
#define LDS16(a_) (*(const LDS_AS u32x4*)(size_t)(a_))
#define K_READ(m, PH)                                                                             \
  {                                                                                               \
    if (!((m) == 8 && (PH))) kf[0] = LDS16(ad[m]);                                                \
    if (!((m) == 8 && !(PH))) kf[1] = LDS16(ad[m] + 1024);                                        \
  }
#define V_READ(m) { vf[0] = LDS16(ad[m] + (L_V - L_K)); vf[1] = LDS16(ad[m] + (L_V - L_K) + 1024); }
#define QK_SLOT(m, PH)                                                                            \
  {                                                                                               \
    const int tj = ((m) == 8 || (m) == 0) ? ((((m) == 8) != (PH)) ? 0 : 1) : -1;                  \
    if (!((m) == 8 && (PH))) S[0] = mfma16(kf[0], qc, tj == 0 ? cc + mk : cc);                    \
    if (!((m) == 8 && !(PH))) S[1] = mfma16(kf[1], qc, tj == 1 ? cc + mk : cc);                   \
  }
      f32x4 sc = z4;
      float s1, s2;
      unsigned one2 = 0x3F803F80u;
      asm volatile("" : "+v"(one2));
      const u32x4 ones = {one2, one2, one2, one2};
      f32x4 oa[3][2];
      oa[0][0] = z4; oa[0][1] = z4;
      asm volatile("" : "=v"(oa[1][0]), "=v"(oa[1][1]), "=v"(oa[2][0]), "=v"(oa[2][1]), "=v"(s1), "=v"(s2));
      u32x4 kf[2], vf[2], qc = qf[0];
      f32x4 S[2], cc = cn[0];
#define SWEEP_HEAD(PH)                                                                            \
  {                                                                                               \
    K_READ(8, PH); V_READ(8);                                                                     \
    SB();                                                                                         \
    QK_SLOT(8, PH);                                                                               \
    SB();                                                                                         \
    K_READ(7, PH);                                                                                \
    SB();                                                                                         \
  }
      auto att_slot = [&](auto m_, auto ph_) __attribute__((always_inline)) {
        constexpr int m = decltype(m_)::value;
        constexpr bool PH = decltype(ph_)::value;
        u32x4 pf = {0u, 0u, 0u, 0u};
#pragma unroll
        for (int j = 0; j < 2; ++j) {
          if (m == 8 && j == (PH ? 0 : 1)) continue;
#pragma unroll
          for (int e = 0; e < 4; e += 2) pf[2 * j + (e >> 1)] = pack2(__builtin_amdgcn_exp2f(S[j][e]), __builtin_amdgcn_exp2f(S[j][e + 1]));
        }
        asm volatile("" : "+v"(pf));
        SB();
        if constexpr (m > 0) role_q<m - 1>(qc, qf[PH ? 0 : 1], cc, cn[PH ? 0 : 1], wq);
        SB();
        if constexpr (m > 0) QK_SLOT(m - 1, PH);
        SB();
        if constexpr (m >= 2) K_READ(m - 2, PH);
        if constexpr (m < 8) role_o<m, PH>(oa, sc, s1, s2, wq);
        SB();
        oa[0][0] = mfma16(vf[0], pf, oa[0][0]);
        oa[0][1] = mfma16(vf[1], pf, oa[0][1]);
        SB();
        if constexpr (m > 0) V_READ(m - 1);
        SB();
        sc = mfma16(ones, pf, sc);
        if constexpr (it == 0 && !PH && m <= KD && KD - m < 4) { if (ffn_on) FFN_DMA(KD - m, 65536 + w * 4096); }
        if constexpr (!PH && m <= KD && KD - m < 4) { if (it == 1) WO_PIECE(8 + KD - m); }
        if constexpr (!PH && m == 0) { WO_PIECE(0); WO_PIECE(1); }
        if constexpr (PH) {
          constexpr int j0 = 8 - m;
          if constexpr (j0 < 6) WO_PIECE(2 + j0);
          if constexpr (it == 0 && j0 < 4) { if (ffn_on) FFN_DMA(4 + j0, 65536 + w * 4096); }
        }
        SB();
      };
#define ATT_SLOTS(m0, n, PH) static_for<n>([&](auto i_) __attribute__((always_inline)) { att_slot(std::integral_constant<int, (m0) - decltype(i_)::value>{}, std::bool_constant<PH>{}); })
      SLOT_ADDR(false);
      asm volatile("" : "+{v[176:179]}"(qc), "+{v[180:183]}"(qf[1]), "+{v[184:187]}"(cc), "+{v[188:191]}"(cn[1]));
      STAMPAT(32);
      BAR();
      asm volatile("" : "+s"(pwo), "+s"(pwg), "+s"(pwu));
      STAMPAT(33);
      SWEEP_HEAD(false);
      ATT_SLOTS(8, 8 - KVF, false);
      STAMPAT(34);
      DRAIN();
      STAMPAT(35);
      if (l == 0) st8(T, rs, w * 8, F_KV + (it * 256 + p * 32 + h) * 128);
      SB();
      constexpr int KP1 = KPV[0], KP2 = KPV[1];
      constexpr bool ASY = (KVL >> it) & 1;
      const unsigned pp = __builtin_amdgcn_readfirstlane(p > 0 ? 32 : 0), pvo = kvo - pp * 32768;
      const unsigned pfo = __builtin_amdgcn_readfirstlane(F_KV + (it * 256 + p * 32 + h - pp) * 128 + w * 8);
      u32x4 pv[4];
      unsigned late = 1;
      if constexpr (ASY) {
        u32x2 kfl;
        ATT_SLOTS(KVF, KVF - KP1, false);
        asm volatile("s_nop 4\n\tbuffer_load_dwordx2 %0, off, %1, %2 sc1" : "=v"(kfl) : "s"(rs), "s"(pfo) : "memory");
        ATT_SLOTS(KP1, KP1 - KP2, false);
        constexpr int ND = (KP1 < KD ? KP1 : KD) - (KP2 + 1 > KD - 3 ? KP2 + 1 : KD - 3) + 1;
        if (it == 1 || ffn_on) wait_vm<(ND > 0 ? ND : 0)>();
        else wait_vm<0>();
        asm volatile("" : "+v"(kfl), "=v"(pv[0]), "=v"(pv[1]), "=v"(pv[2]), "=v"(pv[3])::"memory");
        late = ((unsigned)__builtin_amdgcn_readfirstlane(kfl[0]) ^ T[0]) | ((unsigned)__builtin_amdgcn_readfirstlane(kfl[1]) ^ T[1]);
        asm volatile("" : "+s"(late));
        if (late == 0) { ld16h<0>(pv[0], rs, l * 16, pvo); ld16h<1024>(pv[1], rs, l * 16, pvo); ld16h<2048>(pv[2], rs, l * 16, pvo); ld16h<3072>(pv[3], rs, l * 16, pvo); }
        SB();
        ATT_SLOTS(KP2, KP2 + 1, false);
      } else {
        ATT_SLOTS(KVF, KVF + 1, false);
      }
      asm volatile("" : "+v"(sc), "+v"(oa[0][0]), "+v"(oa[0][1]));
      SB();
      STAMPAT(26);
      asm volatile("" : "+s"(late));
      if (late != 0) {
        poll_flags(rs, 0, pfo, T);
        ld16h<0>(pv[0], rs, l * 16, pvo); ld16h<1024>(pv[1], rs, l * 16, pvo); ld16h<2048>(pv[2], rs, l * 16, pvo); ld16h<3072>(pv[3], rs, l * 16, pvo);
        wait_vm<0>();
      }
      STAMPAT(36);
      if (p == 0) { cn[0] = f32x4{-INFINITY, -INFINITY, -INFINITY, -INFINITY}; cc = cn[0]; }
      asm volatile("" : "+{v[180:183]}"(qf[0]), "+{v[188:191]}"(cn[0]), "+{v[184:187]}"(cc));
#pragma unroll
      for (int e = 0; e < 4; ++e) mk[e] = (4 * g + e > ql) ? 0.f : -INFINITY;
      SLOT_ADDR(true);
      SB();
      wait_vm<2>();
      PINV4(pv);
      KV_TO_LDS(0, pv[0], pv[1], pv[2], pv[3]);
      if constexpr (it == 0) DRAIN();
      if constexpr (it == 0) { FFN_FRAGS(0, 65536 + w * 4096); }
      BAR();
      STAMPAT(3);
      SB();
      SWEEP_HEAD(true);
      ATT_SLOTS(8, 9, true);
      f32x4 sum[2] = {sc, f32x4{s2, 0.f, 0.f, 0.f}};
      oa[1][0] = oa[2][0]; oa[1][1] = oa[2][1];
#undef QK_SLOT
#undef ATT_SLOTS
#undef SWEEP_HEAD
#undef K_READ
#undef V_READ
      STAMPAT(25);
      {
        float big = 0.f;
#pragma unroll
        for (int a = 0; a < 2; ++a)
#pragma unroll
          for (int d = 0; d < 2; ++d)
            big = __builtin_elementwise_maximum(__builtin_elementwise_maximum(big, __builtin_elementwise_maximum(fabsf(oa[a][d][0]), fabsf(oa[a][d][1]))),
                                                __builtin_elementwise_maximum(fabsf(oa[a][d][2]), fabsf(oa[a][d][3])));
        bool ok = sum[0][0] >= 0.5f && sum[0][0] < 0x1p64f && sum[1][0] >= 0.5f && sum[1][0] < 0x1p64f && big < INFINITY;
        if (!__all(ok)) {
#pragma unroll
          for (int a = 0; a < 2; ++a) {
            const int qs = a ? 15 - 2 * w : 2 * w, qq = 256 + 16 * qs + ql, klo = max(qq - 255, p > 0 ? 0 : 256);
            auto score = [&](int t) __attribute__((always_inline)) {
              f32x4 s = mfma16(*(const u32x4*)(kb_ + t * 1024), qf[a], z4);
#pragma unroll
              for (int e = 0; e < 4; ++e) { const int kk = 16 * t + 4 * g + e; s[e] = (kk <= qq && kk >= klo) ? s[e] : -INFINITY; }
              return s;
            };
            float mx = -INFINITY;
#pragma unroll 1
            for (int t = qs; t <= qs + 16; ++t) { const f32x4 s = score(t); mx = fmaxf(fmaxf(mx, fmaxf(s[0], s[1])), fmaxf(s[2], s[3])); }
            mx = max_x32(max_x16(mx));
            oa[a][0] = z4; oa[a][1] = z4; sum[a] = z4;
#pragma unroll 1
            for (int G = qs >> 1; G <= (qs + 16) >> 1; ++G) {
              const f32x4 s0 = score(2 * G), s1 = score(2 * G + 1);
              f32x4 e0, e1;
#pragma unroll
              for (int e = 0; e < 4; ++e) { e0[e] = __builtin_amdgcn_exp2f(s0[e] - mx); e1[e] = __builtin_amdgcn_exp2f(s1[e] - mx); }
              const u32x4 pf = pack8(e0, e1);
              oa[a][0] = mfma16(*(const u32x4*)(vb_ + G * 2048), pf, oa[a][0]);
              oa[a][1] = mfma16(*(const u32x4*)(vb_ + G * 2048 + 1024), pf, oa[a][1]);
              sum[a] = mfma16(ones, pf, sum[a]);
            }
          }
        }
      }
#pragma unroll
      for (int a = 0; a < 2; ++a) {
        const float inv = __builtin_amdgcn_rcpf(sum[a][0]);
        oa[a][0] *= inv; oa[a][1] *= inv;
      }
      const unsigned oo = OFF_O + it * (4u << 20) + (p * 8 * 64 + h * 2) * 1024;
      st16(pack8(oa[0][0], oa[0][1]), rs, l * 16, oo + w * 65536);
      st16(pack8(oa[1][0], oa[1][1]), rs, l * 16 + 1024, oo + (7 - w) * 65536);
      STAMPAT(4);
      DRAIN();
      if constexpr (it == 0) { FFN_FRAGS(2, 65536 + w * 4096); }
      BAR();
      if (tid < RO) st8(T, rs, tid * 256 + h * 8, F_O + (it * 8 + p) * RX * 256);
      if (it == 0) { WO_PIECE(8); WO_PIECE(9); WO_PIECE(10); WO_PIECE(11); }
      WO_PIECE(12); WO_PIECE(13); WO_PIECE(14); WO_PIECE(15);
    }
    STAMPAT(5);
    IDS();
    ARGP(pwr, 4); ARGP(pwd, 7); ARGP(pout, 8);
    pwd0 = pwd;
    const int kh = w >> 2, rt2 = rb * 4 + (w & 3);
    const unsigned R = rt2 * 32 + kh * 16 + (l & 15);
    const unsigned to = (rt2 * 64 + ct * 4 + kh) * 1024 + l * 16;
    const unsigned rmo = ct * 64 + ((g & 1) ? 12 + 4 * g : 4 * g);
    f32x2 wv[8];
    {
      u32x4 wa[2], wrp[2], res[2];
#pragma unroll
      for (int j = 0; j < 2; ++j) {
        wrp[j] = *(const GAS u32x4*)(pwr + rmo + 32 * j);
        wa[j] = *(const GAS u32x4*)(pwr + l * 8 + j * 512);
      }
      {
        f32x2 t[8];
        unpack8v(wa[0], t); unpack8v(wa[1], t + 4);
        const f32x2 s = ((t[0] + t[1]) + (t[2] + t[3])) + ((t[4] + t[5]) + (t[6] + t[7]));
        wsum = sum64(hadd(s));
        unpack8v(trade16(wrp[0]), wv); unpack8v(trade16(wrp[1]), wv + 4);
        asm volatile("" : "+v"(wsum), "+v"(wv[0]), "+v"(wv[1]), "+v"(wv[2]), "+v"(wv[3]), "+v"(wv[4]), "+v"(wv[5]), "+v"(wv[6]), "+v"(wv[7]));
      }
      unsigned bt[4];
#pragma unroll
      for (int n = 0; n < 4; ++n) {
        bt[n] = lbase + 98304 + kh * 32768 + (4 * g + ((l & 15) >> 2)) * 128 + (l & 3) * 8 + ((n ^ (2 * (g & 1) + ((l & 15) >> 3))) << 5);
        asm volatile("" : "+v"(bt[n]));
      }
      if (w == 0) poll_flags(rs, (l & 31) * 8, F_O + ((it * 8 + (rb >> 1)) * RX + bi % RO) * 256, T);
      else if (it == 0 && w < 4) poll_flags2(rs, w < 3 ? F_XN + (((w - 1) * 4 + g) * RX + bi % RX) * 256 + (l & 15) * 16 : F_KV + (((l >> 2) & 7) * 32 + h) * 128 + (l & 3) * 16, T);
      asm volatile("s_barrier" ::: "memory");
      STAMPAT(6);
      const unsigned abase = OFF_O + it * (4u << 20) + (rt2 * 64 + 32 * kh) * 1024;
      const unsigned av0 = l * 16 + kh * 1024, av1 = l * 16 + (kh ^ 1) * 1024;
      u32x4 af[WB][2];
#define O_LOAD(s_) { af[(s_) % WB][0] = ld16(rs, av0 + ((s_) & 1) * 2048, abase + ((s_) >> 1) * 4096); af[(s_) % WB][1] = ld16(rs, av1 + ((s_) & 1) * 2048, abase + ((s_) >> 1) * 4096); }
#pragma unroll
      for (int s = 0; s < WR; ++s) O_LOAD(s);
#pragma unroll
      for (int j = 0; j < 2; ++j) res[j] = ld16(rs, it == 0 ? OFF_XN + to + j * 2048 : OFF_XNEW + ((2 * ct + j) * 4 + g) * 16, 0);
      SB();
      f32x4 ac[2][4];
#pragma unroll
      for (int a = 0; a < 2; ++a)
#pragma unroll
        for (int n = 0; n < 4; ++n) ac[a][n] = f32x4{0.f, 0.f, 0.f, 0.f};
      u32x4 bfr[2][4];
#define WO_RD(buf, s, n)                                                                             \
  {                                                                                                  \
    const unsigned o_ = bt[n] + ((s) < 8 ? (s) * 4096 : (s) < 12 ? 32768 + ((s) - 8) * 4096 : ((s) - 12) * 4096); \
    frag_t f_;                                                                                       \
    f_.hf.a = trabs(o_); f_.hf.b = trabs(o_ + 2048);                                                 \
    bfr[buf][n] = f_.v;                                                                              \
  }
#pragma unroll
      for (int n = 0; n < 4; ++n) WO_RD(0, 0, n);
#pragma unroll
      for (int s = 0; s < 16; ++s) {
        const int cur = s & 1;
        asm volatile("" : "+{v[176:179]}"(ac[0][0]), "+{v[180:183]}"(ac[0][1]), "+{v[184:187]}"(ac[0][2]), "+{v[188:191]}"(ac[0][3]),
                          "+{v[192:195]}"(ac[1][0]), "+{v[196:199]}"(ac[1][1]), "+{v[200:203]}"(ac[1][2]), "+{v[204:207]}"(ac[1][3]));
        if (s == 7) {
          asm volatile("s_barrier" ::: "memory");
#pragma unroll
          for (int n = 0; n < 4; ++n) { bt[n] -= 65536 + kh * 16384; asm volatile("" : "+v"(bt[n])); }
        }
        if (s < WB - WR) O_LOAD(WR + 2 * s < 16 ? WR + 2 * s : 0);
        SB();
#pragma unroll
        for (int n = 0; n < 4; ++n) {
          ac[0][n] = mfma16(bfr[cur][n], af[s % WB][0], ac[0][n]);
          ac[1][n] = mfma16(bfr[cur][n], af[s % WB][1], ac[1][n]);
          if (s + 1 < 16) WO_RD(cur ^ 1, s + 1, n);
          if (s == 9) {
            if (it == 1 && n == 0) { res[0] = R == sel0 ? res[0] : x2p[0]; res[1] = R == sel0 ? res[1] : x2p[1]; }
            v[2 * n] = f32x2{lo16(res[n >> 1][2 * (n & 1)]), hi16(res[n >> 1][2 * (n & 1)])};
            v[2 * n + 1] = f32x2{lo16(res[n >> 1][2 * (n & 1) + 1]), hi16(res[n >> 1][2 * (n & 1) + 1])};
            asm volatile("" : "+v"(v[2 * n]), "+v"(v[2 * n + 1]));
          }
          SB();
        }
        if (s < WB - WR) { O_LOAD(WR + 2 * s + 1); }
        else if (s + WB < 16) { O_LOAD(s + WB); }
        SB();
      }
#undef WO_RD
#undef O_LOAD
      STAMPAT(7);
#pragma unroll
      for (int n = 0; n < 4; ++n) *(f32x4*)(smem + L_XCH + w * 4096 + n * 1024 + l * 16) = ac[1][n];
#pragma unroll
      for (int n = 0; n < 4; ++n) { v[2 * n] += f32x2{ac[0][n][0], ac[0][n][1]}; v[2 * n + 1] += f32x2{ac[0][n][2], ac[0][n][3]}; }
      BAR();
#pragma unroll
      for (int n = 0; n < 4; ++n) {
        const f32x4 o = *(const f32x4*)(smem + L_XCH + (w ^ 4) * 4096 + n * 1024 + l * 16);
        v[2 * n] += f32x2{o[0], o[1]}; v[2 * n + 1] += f32x2{o[2], o[3]};
      }
    }
    {
      if (it == 0) { st16(pack8v(v), rs, to, OFF_XN + (4u << 20)); st16(pack8v(v + 4), rs, to + 2048, OFF_XN + (4u << 20)); }
      SB();
      const f32x2 s = ((v[0] + v[1]) + (v[2] + v[3])) + ((v[4] + v[5]) + (v[6] + v[7]));
      f32x2 dp = v[0] * wv[0], q = v[0] * v[0];
#pragma unroll
      for (int j = 1; j < 8; ++j) { dp = __builtin_elementwise_fma(v[j], wv[j], dp); q = __builtin_elementwise_fma(v[j], v[j], q); }
      float mt = hadd(s), qq = hadd(q), dot = hadd(dp);
      add_g3(mt, qq, dot);
      const unsigned so = OFF_STAT + it * 524288 + rb * 32768, lrow = R & 127;
      if (g == 0) st16(unpoison4(__builtin_bit_cast(u32x4, f32x4{mt, qq, dot, 0.f})), rs, ct * 2048 + lrow * 16, so);
      SB();
      if (it == 0) wait_vm<1>();
      if constexpr (it == 0) {
        volatile LDS_AS u32x2* in = (volatile LDS_AS u32x2*)(smem + L_XC);
        if (l == 0) in[w] = T;
        const u32x2 t = in[l & 7];
        if (__all(t[0] == T[0] && t[1] == T[1]) && l < RX) st8(T, rs, l * 256 + ((rb & 1) * 16 + ct) * 8, F_X1 + (it * 8 + (rb >> 1)) * RX * 256);
      }
      STAMPAT(8);
      if (ffn_on && (it == 1 || !WDL)) { WD_DMA(); }
      if (it == 0) {
#pragma unroll
        for (int j = 0; j < 9; ++j) *(u32x4*)(smem + L_RING + (j / 3) * STAGE + (w * 3 + j % 3) * 1024 + l * 16) = keep[j];
      }
      STAMPAT(16);
      if constexpr (it == 0) {
        if (w == 0) poll_flags(rs, ((l & 2) * 8 + ((p + 5) & 7) * 2 + (l & 1)) * 8, F_X1 + (p * RX + (xcd >> 1) % RX) * 256, T);
        if (tid == 256) *(LDS_AS u32x4*)(smem + L_QC) = u32x4{0u, 0u, 0u, 0u};
        BAR();
        PRE_LOAD(1, p + 5, 0, 4);
        STAMPAT(9);
      } else {
        __builtin_amdgcn_s_sleep(SDIR);
        u32x4 sp[4];
#pragma unroll
        for (int i = 0; i < 4; ++i) sp[i] = ld16(rs, (4 * g + i) * 2048 + lrow * 16, so);
        const RowFin r = fin_key(rs, T, g * 8192 + lrow * 16, so, smem + L_XC, l, w, R, wsum, sp);
        STAMPAT(9);
        BAR();
        fin_cand(rs, smem + L_XC, 1, g, rb, ct, r, v);
        STAMPAT(10);
        fin_x2(r, v);
      }
    }
    STAMPAT(11);
    if (it == 1) {
      const u32x4 op0 = trade16(pack8v(v)), op1 = trade16(pack8v(v + 4));
#define OUT_STORE()                                                                                 \
  if (R != key_row(key)) {                                                                          \
    __builtin_amdgcn_raw_buffer_store_b128(op0, rout, (R * 1024 + rmo) * 2, 0, SC1);                  \
    __builtin_amdgcn_raw_buffer_store_b128(op1, rout, (R * 1024 + rmo + 32) * 2, 0, SC1);             \
  }
      u32x4 xv[4];
      unsigned guess = 0;
      if constexpr (GSL > 0) {
        __builtin_amdgcn_s_sleep(GSL);
        const unsigned k_ = key_load(rs, l, 1, bid & 15);
        guess = ffn_xoff(__builtin_amdgcn_readfirstlane(umax16(k_ == POISON ? 0u : k_)), 1, w);
        ld16h<0>(xv[0], rs, g * 16, guess); ld16h<64>(xv[1], rs, g * 16, guess); ld16h<128>(xv[2], rs, g * 16, guess); ld16h<192>(xv[3], rs, g * 16, guess);
      }
      if (w == 0) {
        const unsigned k_ = key_wait(rs, T, l, 1, bid & 15, key_load(rs, l, 1, bid & 15));
        if (l == 0) *(LDS_AS unsigned*)(smem + L_SKEY + 32) = k_;
      }
      DRAIN();
      PINV4(xv);
      BAR();
      const unsigned key = __builtin_amdgcn_readfirstlane(*(LDS_AS unsigned*)(smem + L_SKEY + 32));
      STAMPAT(12);
      if (ffn_on) {
        if (guess != ffn_xoff(key, 1, w)) {
#pragma unroll
          for (int i = 0; i < 4; ++i) xv[i] = ld16(rs, g * 16 + i * 64, ffn_xoff(key, 1, w));
        }
        f32x4 ga, ua;
        ffn_gu(gv, uv, xv, ga, ua);
        ffn_gu_check(rs, T, ffn_xoff(key, 1, w), l, gv, uv, xv, ga, ua);
        ffn_gu_store(smem, l, w, ga, ua);
        BAR();
        f32x4 t, ya[2] = {{0.f, 0.f, 0.f, 0.f}, {0.f, 0.f, 0.f, 0.f}};
        s16x4 dfr[8];
        ffn_d_read(smem, l, w, t, dfr);
#pragma unroll
        for (int j = 0; j < 4; ++j) t[j] = ffn_d_h(ffn_d_sum(t[j]));
        const u32x2 hh = {pack2(t[0], t[1]), pack2(t[2], t[3])};
#pragma unroll
        for (int i = 0; i < 8; ++i) ya[i & 1] = ffn_d_mma(l, i, dfr[i], hh, ya[i & 1]);
        ffn_d_store(rs, l, w, 1, slice, ya[0] + ya[1]);
        SB();
        OUT_STORE();
        STAMPAT(13);
      } else {
        OUT_STORE();
        const unsigned G = xcd * 4 + bi - 28;
        if (G == 0 && tid == 0) st8(T, rs, F_EP, 0);
        u32x4 pv[4];
        u32x2 cxn, cx1;
        __builtin_amdgcn_s_sleep(RSL);
        red_load(rs, tid, 1, G, key, pv, cxn, cx1);
        red_sum(rs, smem, T, tid, w, 1, G, key, pv, cxn, cx1);
        BAR();
        if (w == 0) {
          const u32x2 ov = red_final(smem, l, key, cxn, cx1);
          if (l < 8) __builtin_amdgcn_raw_buffer_store_b64(ov, rout, (key_row(key) * 1024 + G * 32 + l * 4) * 2, 0, 0);
        }
      }
#undef OUT_STORE
    }
    STAMPAT(15); STAMPAT(23);
  };
  layer(std::integral_constant<int, 0>{});
  layer(std::integral_constant<int, 1>{});
  if (blockIdx.x == 224 && w0_ == 0) { const unsigned long long t1 = __builtin_amdgcn_s_memrealtime(); while (__builtin_amdgcn_s_memrealtime() - t1 < (echo_b - echo_a) * ECHO_K) __builtin_amdgcn_s_sleep(1); }
}

extern "C" void kernel_launch(void* const* d_in, const int* in_sizes, int n_in, void* d_out, int out_size,
                              void* d_ws, size_t ws_size, hipStream_t stream) {
  (void)hipFuncSetAttribute((const void*)mega_kernel, hipFuncAttributeMaxDynamicSharedMemorySize, LDS_BYTES);
  mega_kernel<<<256, 512, LDS_BYTES, stream>>>((const bf16*)d_in[0], (char*)d_ws, (const bf16*)d_in[1], (const bf16*)d_in[2], (const bf16*)d_in[4],
                                               (const bf16*)d_in[5], (const bf16*)d_in[6], (const bf16*)d_in[7], (bf16*)d_out);
}
```

```cpp
#include <hip/hip_runtime.h>
#include <stdint.h>
#include <utility>

typedef __bf16 bf16;
typedef __bf16 bf16x8 __attribute__((ext_vector_type(8)));
typedef __bf16 bf16x2 __attribute__((ext_vector_type(2)));
typedef float f32x4 __attribute__((ext_vector_type(4)));
typedef float f32x2 __attribute__((ext_vector_type(2)));
typedef short s16x4 __attribute__((ext_vector_type(4)));
typedef unsigned int u32x4 __attribute__((ext_vector_type(4)));
typedef unsigned int u32x2 __attribute__((ext_vector_type(2)));
#define LDS_AS __attribute__((address_space(3)))
#define GAS __attribute__((address_space(1)))
typedef GAS unsigned gu32;
typedef GAS unsigned long long gu64;
#define RLX_AGENT __ATOMIC_RELAXED, __HIP_MEMORY_SCOPE_AGENT
typedef __amdgpu_buffer_rsrc_t rsrc_t;

constexpr int F_ = 3584;
constexpr int SC1 = 16;

constexpr int RX = 4;
constexpr unsigned F_EP = 0, F_TMO = 128;
constexpr unsigned F_XN = 4096;
constexpr unsigned F_X1 = 196608;
constexpr unsigned F_O = 32768;
constexpr unsigned F_KV = 65536;
static_assert(F_XN + 8 * RX * 256 <= F_O && F_O + 2 * 64 * 256 <= F_KV, "flag block");
constexpr unsigned OFF_KEY = 131072;
constexpr unsigned OFF_AB = 147456;
constexpr unsigned OFF_XNEW = 262144;
constexpr unsigned OFF_CS = 294912;
constexpr unsigned OFF_FX = 393216;
constexpr unsigned OFF_CA = 524288;
constexpr unsigned OFF_CB = 655360;
constexpr unsigned OFF_STAT = 1u << 20;
constexpr unsigned OFF_PART = 3u << 20;
constexpr unsigned OFF_XN = 8u << 20;
constexpr unsigned OFF_O = 16u << 20;
constexpr unsigned OFF_KV = 32u << 20;
constexpr unsigned OFF_WI = 52u << 20;
constexpr unsigned WS_BYTES = 58u << 20;
constexpr unsigned POISON = 0xFFFFFFFFu;

constexpr int L_ST0 = 147456, L_CNT = 147968, L_CSL = 31872, L_FXL = 32256, L_K = 0, L_V = 32768, L_RING = 65536, L_XCH = 0, L_DN = 32768, L_TOUCH0 = 149504, L_RDY = 31744, L_QC = 31808, LDS_BYTES = 163840;
constexpr int STAGE = 24576;
#define SLOT(t) ((t) & 3)

template <typename F, int... I> __device__ __forceinline__ void static_for_impl(F&& f, std::integer_sequence<int, I...>) {
  (f(std::integral_constant<int, I>{}), ...);
}
template <int N, typename F> __device__ __forceinline__ void static_for(F&& f) { static_for_impl(f, std::make_integer_sequence<int, N>{}); }
__device__ __forceinline__ unsigned pack2(float a, float b) {
  f32x2 v = {a, b};
  return __builtin_bit_cast(unsigned, __builtin_convertvector(v, bf16x2));
}
__device__ __forceinline__ float asf(unsigned u) { return __builtin_bit_cast(float, u); }
__device__ __forceinline__ float lo16(unsigned u) { return __builtin_bit_cast(float, u << 16); }
__device__ __forceinline__ float hi16(unsigned u) { return __builtin_bit_cast(float, u & 0xffff0000u); }
__device__ __forceinline__ u32x4 pack8(const f32x4 a, const f32x4 b) {
  u32x4 o;
  o[0] = pack2(a[0], a[1]); o[1] = pack2(a[2], a[3]); o[2] = pack2(b[0], b[1]); o[3] = pack2(b[2], b[3]);
  return o;
}
__device__ __forceinline__ f32x4 mfma16(u32x4 a, u32x4 b, f32x4 c) {
  return __builtin_amdgcn_mfma_f32_16x16x32_bf16(__builtin_bit_cast(bf16x8, a), __builtin_bit_cast(bf16x8, b), c, 0, 0, 0);
}
__device__ __forceinline__ f32x4 mfma16k16(s16x4 a, s16x4 b, f32x4 c) { return __builtin_amdgcn_mfma_f32_16x16x16bf16_1k(a, b, c, 0, 0, 0); }
template <int CTRL> __device__ __forceinline__ float dppf(float v) {
  return __builtin_bit_cast(float, __builtin_amdgcn_update_dpp(0, __builtin_bit_cast(int, v), CTRL, 0xf, 0xf, true));
}
template <int CTRL> __device__ __forceinline__ unsigned dppu(unsigned v) {
  return (unsigned)__builtin_amdgcn_update_dpp(0, (int)v, CTRL, 0xf, 0xf, true);
}
__device__ __forceinline__ float add_x16(float v) {
  auto r = __builtin_amdgcn_permlane16_swap(__builtin_bit_cast(unsigned, v), __builtin_bit_cast(unsigned, v), false, false);
  return __builtin_bit_cast(float, (unsigned)r[0]) + __builtin_bit_cast(float, (unsigned)r[1]);
}
__device__ __forceinline__ float add_x32(float v) {
  auto r = __builtin_amdgcn_permlane32_swap(__builtin_bit_cast(unsigned, v), __builtin_bit_cast(unsigned, v), false, false);
  return __builtin_bit_cast(float, (unsigned)r[0]) + __builtin_bit_cast(float, (unsigned)r[1]);
}
__device__ __forceinline__ float max_x16(float v) {
  auto r = __builtin_amdgcn_permlane16_swap(__builtin_bit_cast(unsigned, v), __builtin_bit_cast(unsigned, v), false, false);
  return fmaxf(__builtin_bit_cast(float, (unsigned)r[0]), __builtin_bit_cast(float, (unsigned)r[1]));
}
__device__ __forceinline__ float max_x32(float v) {
  auto r = __builtin_amdgcn_permlane32_swap(__builtin_bit_cast(unsigned, v), __builtin_bit_cast(unsigned, v), false, false);
  return fmaxf(__builtin_bit_cast(float, (unsigned)r[0]), __builtin_bit_cast(float, (unsigned)r[1]));
}
__device__ __forceinline__ unsigned umax_x16(unsigned v) {
  auto r = __builtin_amdgcn_permlane16_swap(v, v, false, false);
  return max((unsigned)r[0], (unsigned)r[1]);
}
__device__ __forceinline__ unsigned umax_x32(unsigned v) {
  auto r = __builtin_amdgcn_permlane32_swap(v, v, false, false);
  return max((unsigned)r[0], (unsigned)r[1]);
}
__device__ __forceinline__ float sum_hi8(float v) {
  v += dppf<0x128>(v);
  v = add_x16(v);
  return add_x32(v);
}
__device__ __forceinline__ float sum64(float v) {
  v += dppf<0xB1>(v);
  v += dppf<0x4E>(v);
  v += dppf<0x124>(v);
  v += dppf<0x128>(v);
  v = add_x16(v);
  return add_x32(v);
}

#define BAR() asm volatile("s_waitcnt lgkmcnt(0)\n\ts_barrier" ::: "memory")
#define DRAIN() asm volatile("s_waitcnt vmcnt(0)" ::: "memory")
#define SB() __builtin_amdgcn_sched_barrier(0)
template <int N> __device__ __forceinline__ void wait_vm() { asm volatile("s_waitcnt vmcnt(%0)" ::"n"(N) : "memory"); }

__device__ __forceinline__ void dma16(rsrc_t rs, unsigned voff, unsigned soff, unsigned lds) {
  asm volatile("s_mov_b32 m0, %0\n\ts_nop 0\n\tbuffer_load_dwordx4 %1, %2, %3 offen lds" ::"s"(lds), "v"(voff), "s"(rs), "s"(soff) : "memory");
}
template <int OFF> __device__ __forceinline__ void dma16s(rsrc_t rs, unsigned voff, unsigned soff, unsigned lds) {
  asm volatile("s_mov_b32 m0, %0\n\ts_nop 0\n\tbuffer_load_dwordx4 %1, %2, %3 offen offset:%4 sc1 lds" ::"s"(lds), "v"(voff), "s"(rs), "s"(soff), "n"(OFF) : "memory");
}
__device__ __forceinline__ void touch(unsigned& d, const GAS bf16* ptr) { asm volatile("global_load_dword %0, %1, off" : "+v"(d) : "v"(ptr) : "memory"); }
__device__ __forceinline__ void ldrow(u32x4& a, u32x4& b, rsrc_t rx, unsigned voff, unsigned soff) {
  asm volatile("buffer_load_dwordx4 %0, %2, %3, %4 offen\n\tbuffer_load_dwordx4 %1, %2, %3, %4 offen offset:1024" : "=&v"(a), "=&v"(b) : "v"(voff), "s"(rx), "s"(soff) : "memory");
}
__device__ __forceinline__ void pin4(f32x2* v) { asm volatile("" : "+v"(v[0]), "+v"(v[1]), "+v"(v[2]), "+v"(v[3])); }
__device__ __forceinline__ float rbf1(float x) { return __builtin_bit_cast(float, pack2(0.f, x)); }
__device__ __forceinline__ void vswap(unsigned& a, unsigned& b) { asm volatile("v_swap_b32 %0, %1" : "+v"(a), "+v"(b)); }
__device__ __forceinline__ void vswap(float& a, float& b) { asm volatile("v_swap_b32 %0, %1" : "+v"(a), "+v"(b)); }
template <typename V, int N> __device__ __forceinline__ void vswapn(V& a, V& b) {
#pragma unroll
  for (int i = 0; i < N; ++i) { auto x = a[i], y = b[i]; vswap(x, y); a[i] = x; b[i] = y; }
}
__device__ __forceinline__ u32x4 ld16(rsrc_t rs, unsigned voff, unsigned soff) { return __builtin_amdgcn_raw_buffer_load_b128(rs, voff, soff, SC1); }
template <int OFF> __device__ __forceinline__ void ld16h(u32x4& d, rsrc_t rs, unsigned voff, unsigned soff) {
  asm volatile("buffer_load_dwordx4 %0, %1, %2, %3 offen offset:%4 sc1" : "=v"(d) : "v"(voff), "s"(rs), "s"(soff), "n"(OFF) : "memory");
}
template <int I, int OFF> __device__ __forceinline__ void ld16w(u32x4& d, rsrc_t rs, unsigned voff, unsigned soff) {
  if constexpr (I == 0) asm volatile("buffer_load_dwordx4 %0, %1, %2, %3 offen offset:%4 sc1" : "={v[128:131]}"(d) : "v"(voff), "s"(rs), "s"(soff), "n"(OFF) : "memory");
  else if constexpr (I == 1) asm volatile("buffer_load_dwordx4 %0, %1, %2, %3 offen offset:%4 sc1" : "={v[132:135]}"(d) : "v"(voff), "s"(rs), "s"(soff), "n"(OFF) : "memory");
  else if constexpr (I == 2) asm volatile("buffer_load_dwordx4 %0, %1, %2, %3 offen offset:%4 sc1" : "={v[136:139]}"(d) : "v"(voff), "s"(rs), "s"(soff), "n"(OFF) : "memory");
  else if constexpr (I == 3) asm volatile("buffer_load_dwordx4 %0, %1, %2, %3 offen offset:%4 sc1" : "={v[140:143]}"(d) : "v"(voff), "s"(rs), "s"(soff), "n"(OFF) : "memory");
  else if constexpr (I == 4) asm volatile("buffer_load_dwordx4 %0, %1, %2, %3 offen offset:%4 sc1" : "={v[144:147]}"(d) : "v"(voff), "s"(rs), "s"(soff), "n"(OFF) : "memory");
  else if constexpr (I == 5) asm volatile("buffer_load_dwordx4 %0, %1, %2, %3 offen offset:%4 sc1" : "={v[148:151]}"(d) : "v"(voff), "s"(rs), "s"(soff), "n"(OFF) : "memory");
  else if constexpr (I == 6) asm volatile("buffer_load_dwordx4 %0, %1, %2, %3 offen offset:%4 sc1" : "={v[152:155]}"(d) : "v"(voff), "s"(rs), "s"(soff), "n"(OFF) : "memory");
  else if constexpr (I == 7) asm volatile("buffer_load_dwordx4 %0, %1, %2, %3 offen offset:%4 sc1" : "={v[156:159]}"(d) : "v"(voff), "s"(rs), "s"(soff), "n"(OFF) : "memory");
  else if constexpr (I == 8) asm volatile("buffer_load_dwordx4 %0, %1, %2, %3 offen offset:%4 sc1" : "={v[160:163]}"(d) : "v"(voff), "s"(rs), "s"(soff), "n"(OFF) : "memory");
  else if constexpr (I == 9) asm volatile("buffer_load_dwordx4 %0, %1, %2, %3 offen offset:%4 sc1" : "={v[164:167]}"(d) : "v"(voff), "s"(rs), "s"(soff), "n"(OFF) : "memory");
  else if constexpr (I == 10) asm volatile("buffer_load_dwordx4 %0, %1, %2, %3 offen offset:%4 sc1" : "={v[168:171]}"(d) : "v"(voff), "s"(rs), "s"(soff), "n"(OFF) : "memory");
  else if constexpr (I == 11) asm volatile("buffer_load_dwordx4 %0, %1, %2, %3 offen offset:%4 sc1" : "={v[172:175]}"(d) : "v"(voff), "s"(rs), "s"(soff), "n"(OFF) : "memory");
}
template <int K> __device__ __forceinline__ void pin_w(u32x4& a, u32x4& b) {
  if constexpr (K == 0) asm volatile("" : "+{v[128:131]}"(a), "+{v[132:135]}"(b)::"memory");
  else if constexpr (K == 1) asm volatile("" : "+{v[136:139]}"(a), "+{v[140:143]}"(b)::"memory");
  else if constexpr (K == 2) asm volatile("" : "+{v[144:147]}"(a), "+{v[148:151]}"(b)::"memory");
  else if constexpr (K == 3) asm volatile("" : "+{v[152:155]}"(a), "+{v[156:159]}"(b)::"memory");
  else if constexpr (K == 4) asm volatile("" : "+{v[160:163]}"(a), "+{v[164:167]}"(b)::"memory");
  else if constexpr (K == 5) asm volatile("" : "+{v[168:171]}"(a), "+{v[172:175]}"(b)::"memory");
}
__device__ __forceinline__ void ld8h(u32x2& d, rsrc_t rs, unsigned voff, unsigned soff) {
  asm volatile("buffer_load_dwordx2 %0, %1, %2, %3 offen sc1" : "=v"(d) : "v"(voff), "s"(rs), "s"(soff) : "memory");
}
__device__ __forceinline__ void ld16u(u32x4& d, rsrc_t rs, unsigned voff, unsigned soff) {
  asm volatile("buffer_load_dwordx4 %0, %1, %2, %3 offen sc1" : "+v"(d) : "v"(voff), "s"(rs), "s"(soff) : "memory");
}
template <int OFF> __device__ __forceinline__ void ld4h(unsigned& d, rsrc_t rs, unsigned voff, unsigned soff) {
  asm volatile("buffer_load_dword %0, %1, %2, %3 offen offset:%4 sc1" : "=v"(d) : "v"(voff), "s"(rs), "s"(soff), "n"(OFF) : "memory");
}
__device__ __forceinline__ void ld4x16(u32x4 (&d)[4], rsrc_t rs, unsigned voff, unsigned soff) {
  asm volatile("buffer_load_dwordx4 %0, %4, %5, %6 offen sc1\n\tbuffer_load_dwordx4 %1, %4, %5, %6 offen offset:64 sc1\n\t"
               "buffer_load_dwordx4 %2, %4, %5, %6 offen offset:128 sc1\n\tbuffer_load_dwordx4 %3, %4, %5, %6 offen offset:192 sc1\n\ts_waitcnt vmcnt(0)"
               : "=&v"(d[0]), "=&v"(d[1]), "=&v"(d[2]), "=&v"(d[3]) : "v"(voff), "s"(rs), "s"(soff) : "memory");
}
__device__ __forceinline__ u32x2 ld8(rsrc_t rs, unsigned voff, unsigned soff) { return __builtin_amdgcn_raw_buffer_load_b64(rs, voff, soff, SC1); }
__device__ __forceinline__ void st16(u32x4 v, rsrc_t rs, unsigned voff, unsigned soff) { __builtin_amdgcn_raw_buffer_store_b128(v, rs, voff + soff, 0, SC1); }
__device__ __forceinline__ void st8(u32x2 v, rsrc_t rs, unsigned voff, unsigned soff) { __builtin_amdgcn_raw_buffer_store_b64(v, rs, voff, soff, SC1); }

__device__ __forceinline__ unsigned ld4(rsrc_t rs, unsigned voff, unsigned soff) { return __builtin_amdgcn_raw_buffer_load_b32(rs, voff, soff, SC1); }
__device__ __forceinline__ void st4(unsigned v, rsrc_t rs, unsigned voff, unsigned soff) { __builtin_amdgcn_raw_buffer_store_b32(v, rs, voff, soff, SC1); }

constexpr unsigned kSpinCap = 1u << 16;
__device__ __forceinline__ bool spin_fail(unsigned& spins, rsrc_t rs, u32x2 T) {
  asm volatile("" ::: "memory");
  ++spins;
  if (spins > kSpinCap) { st8(T, rs, 0, F_TMO); return true; }
  if ((spins & 255u) == 0u) {
    const u32x2 v = ld8(rs, 0, F_TMO);
    if (v[0] == T[0] && v[1] == T[1]) return true;
  }
  __builtin_amdgcn_s_sleep(3);
  return false;
}
__device__ __forceinline__ void poll_flags2(rsrc_t rs, unsigned voff, u32x2 T) {
  for (unsigned spins = 0;;) {
    const u32x4 v = ld16(rs, voff, 0);
    if (__all(v[0] == T[0] && v[1] == T[1] && v[2] == T[0] && v[3] == T[1])) break;
    if (spin_fail(spins, rs, T)) break;
  }
  asm volatile("" ::: "memory");
  __builtin_amdgcn_fence(__ATOMIC_ACQUIRE, "wavefront");
}
__device__ __forceinline__ void poll_flags(rsrc_t rs, unsigned voff, unsigned soff, u32x2 T) {
  for (unsigned spins = 0;;) {
    const u32x2 v = ld8(rs, voff, soff);
    if (__all(v[0] == T[0] && v[1] == T[1])) break;
    if (spin_fail(spins, rs, T)) break;
  }
  asm volatile("" ::: "memory");
  __builtin_amdgcn_fence(__ATOMIC_ACQUIRE, "wavefront");
}

__device__ __forceinline__ unsigned toff(unsigned R, unsigned ks, unsigned g) {
  return (((R >> 5) * 64 + ks * 2 + ((R >> 4) & 1)) << 10) + ((g * 16 + (R & 15)) << 4);
}
__device__ __forceinline__ void unpack8v(const u32x4 u, f32x2* v) {
#pragma unroll
  for (int j = 0; j < 4; ++j) v[j] = f32x2{lo16(u[j]), hi16(u[j])};
}
__device__ __forceinline__ u32x4 pack8v(const f32x2* v) {
  u32x4 o;
#pragma unroll
  for (int j = 0; j < 4; ++j) o[j] = pack2(v[j][0], v[j][1]);
  return o;
}
__device__ __forceinline__ f32x2 rbf1v(f32x2 v) { return f32x2{rbf1(v[0]), rbf1(v[1])}; }
__device__ __forceinline__ float ln_rd(float var) { return __builtin_amdgcn_rcpf(rbf1(__builtin_amdgcn_sqrtf(rbf1(rbf1(var) + 1e-5f)))); }
template <int N> __device__ __forceinline__ void ln_apply(float mb, float rd, const f32x2* v, f32x2* y) {
  const f32x2 mb2 = {mb, mb}, rd2 = {rd, rd};
#pragma unroll
  for (int j = 0; j < N; ++j) y[j] = rbf1v(rbf1v(v[j] - mb2) * rd2);
}
__device__ __forceinline__ float hadd(f32x2 v) {
  float a = v[0];
  asm volatile("" : "+v"(a));
  return a + v[1];
}
__device__ __forceinline__ float add_g(float v) { return add_x32(add_x16(v)); }
__device__ __forceinline__ void add_g3(float& x, float& y, float& z) {
  auto a = __builtin_amdgcn_permlane16_swap(__builtin_bit_cast(unsigned, x), __builtin_bit_cast(unsigned, y), false, false);
  auto b = __builtin_amdgcn_permlane16_swap(__builtin_bit_cast(unsigned, z), __builtin_bit_cast(unsigned, z), false, false);
  const float r = asf(a[0]) + asf(a[1]), t = asf(b[0]) + asf(b[1]);
  auto c = __builtin_amdgcn_permlane32_swap(__builtin_bit_cast(unsigned, r), __builtin_bit_cast(unsigned, r), false, false);
  auto d = __builtin_amdgcn_permlane32_swap(__builtin_bit_cast(unsigned, t), __builtin_bit_cast(unsigned, t), false, false);
  const float u = asf(c[0]) + asf(c[1]);
  z = asf(d[0]) + asf(d[1]);
  auto e = __builtin_amdgcn_permlane16_swap(__builtin_bit_cast(unsigned, u), __builtin_bit_cast(unsigned, u), false, false);
  x = asf(e[0]); y = asf(e[1]);
}
__device__ __forceinline__ u32x4 trade16(u32x4 v) {
  auto a = __builtin_amdgcn_permlane16_swap(v[0], v[2], false, false);
  auto b = __builtin_amdgcn_permlane16_swap(v[1], v[3], false, false);
  return u32x4{(unsigned)a[0], (unsigned)b[0], (unsigned)a[1], (unsigned)b[1]};
}
__device__ __forceinline__ unsigned umax4(u32x4 v) { return max(max(v[0], v[1]), max(v[2], v[3])); }
__device__ __forceinline__ u32x2 unpoison2(u32x2 v) { return u32x2{min(v[0], POISON - 1), min(v[1], POISON - 1)}; }
__device__ __forceinline__ u32x4 unpoison4(u32x4 v) {
  return u32x4{min(v[0], POISON - 1), min(v[1], POISON - 1), min(v[2], POISON - 1), min(v[3], POISON - 1)};
}

typedef union { struct { s16x4 a, b; } hf; u32x4 v; } frag_t;
__device__ __forceinline__ s16x4 trread(const char* smem, unsigned off) {
  return __builtin_amdgcn_ds_read_tr16_b64_v4i16((LDS_AS s16x4*)(smem + off));
}
__device__ __forceinline__ s16x4 trabs(unsigned addr) { return __builtin_amdgcn_ds_read_tr16_b64_v4i16((LDS_AS s16x4*)(size_t)addr); }

constexpr int WA = 6;
constexpr int WB = 8, WR = 4;
#define CH 0, 4, 7, 8, 13, 15, 20, 22, 27, 29
constexpr int CHV[10] = {CH};
constexpr int FSL = CHV[1], FS0 = CHV[2], FS0D = CHV[3], FS1 = CHV[4], FS1S = CHV[5];
constexpr int FXA = CHV[6], FXB = CHV[7], FXL = CHV[8], FXS = CHV[9];
constexpr int FXT = FXB >> 2;
#define C0 27, 3, 16, 19, 22, 23, 2, 5, 4
constexpr int C0V[9] = {C0};
constexpr int IMG = C0V[0], CSD = C0V[1];
constexpr int ERQ = C0V[2], CSL = C0V[3], CSS = C0V[4];
constexpr int RWL = C0V[6], RWS = C0V[7], POI = C0V[8];
#define WTS 99
#define WTG 2
#define PBS 10
#define KP0 26
#define PS0 8
constexpr int cmax(int a, int b) { return a > b ? a : b; }
constexpr int EIN = cmax(CSD, cmax(RWS + 1, POI)) + 3;
constexpr int ABS = 27;
constexpr int abl(int it) { return it ? 23 : C0V[5]; }
constexpr int XU = 3, XL = 2;
constexpr int x_slot(int u) { return u % XU == 0 ? 0 : u % XU == 1 ? 32768 : 49152; }
#define TCH 20, 6, 5, 27
constexpr int TCV[4] = {TCH};
constexpr int TS0 = TCV[0], TSN = TCV[1], TW = TCV[2], TST = TCV[3];
constexpr int QL = 26;
#define FDG 0
#define XSL 4
#define FW0 4
#define FW1 4
#define SDI0 8
#define SLP 24, 14, 56, 3
constexpr int SLV[4] = {SLP};
constexpr int SDIR = SLV[0], GSL = SLV[1], RSL = SLV[2], KSL = SLV[3];
constexpr bool tile_piece(int s) { return s >= 3 && ((s - 3) >> 2) + 3 < 8 && ((s - 3) & 3) < 3; }
constexpr int a_req(int s, int n) {
  if (!(n & 1)) return -1;
  if (s <= 1) return 8 + 3 * s + (n >> 1);
  return n < 5 && s + 5 < 32 ? 2 * (s + 5) + (n >> 1) : -1;
}
constexpr int vm_n(int it, int s, int n) {
  int c = 0;
  if (n == 0) c += tile_piece(s);
  if (n == 4) c += (s == abl(it)) + (it ? (s == FSL ? 5 : 0) : (s == ERQ) + (s == RWL ? 2 : 0) + (s == RWS + 1) + (s >= IMG && s < IMG + 3));
  if (!it) c += (s == WTS && n == WTG) + (s < 4 && n == 2) + (s == 0 && (n == 0 || n == 4)) + ((n == 3 || n == 5) && s + XL + XU < 32);
  else c += (a_req(s, n) >= 0) + ((s == 0 || s == 4) && n == 2);
  return c;
}
constexpr int vm_at(int it, int s, int n) {
  int c = it ? 8 : 14;
  for (int u = 0; u < 6 * s + n; ++u) c += vm_n(it, u / 6, u % 6);
  return c;
}
constexpr int vm_a(int it, int k) {
  if (k < 4) return 2 * k + 2;
  for (int u = 0; u < 192; ++u)
    if (a_req(u / 6, u % 6) == 2 * k + 1) return vm_at(it, (u + 1) / 6, (u + 1) % 6);
  return 0;
}
constexpr int vm_tile(int it, int t) { return t >= 3 ? vm_at(it, 4 * t - 7, 1) : t == 1 ? vm_at(0, 0, 5) : vm_at(0, 3, 3); }
constexpr int vm_x(int u) { return u == 2 ? 9 : u < XU + XL ? 12 + 2 * (u - XU) : vm_at(0, u - XU - XL + 1, 0); }

constexpr int wo_k(int it, int s) { return it || s < 4 ? 15 - s : s < 8 ? 7 - s : 19 - s; }
constexpr int wo_pos(int it, int s) { return 2 * s + 2 + (s >= 4 ? 2 * it : 0); }
constexpr int wo_at(int it, int s) { return 2 * it + (s < 4 ? 10 + 4 * s : s < 8 ? 16 + 2 * s : it ? 32 : 36); }
__device__ __forceinline__ void pin1(unsigned& a) { asm volatile("" : "+v"(a)); }
__device__ __forceinline__ void pin2(u32x4& a, u32x4& b) { asm volatile("" : "+v"(a), "+v"(b)::"memory"); }
__device__ __forceinline__ void pin2s(float& a, float& b) { asm volatile("" : "+v"(a), "+v"(b)); }
template <int OFF> __device__ __forceinline__ void lds2(u32x4& a, u32x4& b, unsigned ad) {
  asm volatile("ds_read_b128 %0, %2 offset:%3\n\tds_read_b128 %1, %2 offset:%4" : "=&v"(a), "=&v"(b) : "v"(ad), "n"(OFF), "n"(OFF + 1024) : "memory");
}
template <int N> __device__ __forceinline__ void lds_wait(u32x4& a, u32x4& b) { asm volatile("s_waitcnt lgkmcnt(%2)" : "+v"(a), "+v"(b) : "n"(N) : "memory"); }
__device__ __forceinline__ void pin2f(f32x2& a, f32x2& b) { asm volatile("" : "+v"(a), "+v"(b)); }
__device__ __forceinline__ void pin_ac(f32x4 (&ac)[2][4]) {
  asm volatile("" : "+{v[176:179]}"(ac[0][0]), "+{v[180:183]}"(ac[0][1]), "+{v[184:187]}"(ac[0][2]), "+{v[188:191]}"(ac[0][3]),
                    "+{v[192:195]}"(ac[1][0]), "+{v[196:199]}"(ac[1][1]), "+{v[200:203]}"(ac[1][2]), "+{v[204:207]}"(ac[1][3]));
}

constexpr int L_XC = LDS_BYTES - 64, L_SKEY = 32704, L_SRED = 4096, L_SFIN = 20480;
__device__ __forceinline__ unsigned key_load(rsrc_t rs, int l, int L, int rep) { return ld4(rs, (l & 15) * 4, OFF_KEY + (L * 16 + rep) * 128); }
__device__ __forceinline__ unsigned umax16(unsigned k) {
  k = max(k, dppu<0xB1>(k));
  k = max(k, dppu<0x4E>(k));
  k = max(k, dppu<0x141>(k));
  return max(k, dppu<0x140>(k));
}
__device__ __forceinline__ unsigned key_row(unsigned key) { return (0xffffu - (key & 0xffffu)) & 2047u; }
__device__ __forceinline__ unsigned key_wait(rsrc_t rs, u32x2 T, int l, int L, int rep, unsigned kv) {
  if (!__all(kv != POISON)) {
    unsigned spins = 0;
    do {
      if (spin_fail(spins, rs, T)) break;
      if (L == 0) __builtin_amdgcn_s_sleep(KSL);
      kv = key_load(rs, l, L, rep);
    } while (!__all(kv != POISON));
  }
  return __builtin_amdgcn_readfirstlane(umax16(kv));
}
__device__ __forceinline__ unsigned ffn_xoff(unsigned key, int L, int w) { return OFF_CA + L * 32768 + (key_row(key) >> 7) * 2048 + w * 256; }
__device__ __forceinline__ void row_load(rsrc_t rs, int g, unsigned xo, u32x4 (&xv)[4]) {
  ld16h<0>(xv[0], rs, g * 16, xo); ld16h<64>(xv[1], rs, g * 16, xo); ld16h<128>(xv[2], rs, g * 16, xo); ld16h<192>(xv[3], rs, g * 16, xo);
}
template <int SL = 0> __device__ __forceinline__ void ffn_xwait(rsrc_t rs, u32x2 T, unsigned xo, int l, u32x4 (&xv)[4]) {
#define XV_OK() __all(max(max(umax4(xv[0]), umax4(xv[1])), max(umax4(xv[2]), umax4(xv[3]))) != POISON)
  if (!XV_OK()) {
    unsigned spins = 0;
    do {
      if (spin_fail(spins, rs, T)) break;
      if (SL) __builtin_amdgcn_s_sleep(SL);
#pragma unroll
      for (int i = 0; i < 4; ++i) xv[i] = ld16(rs, (l >> 4) * 16 + i * 64, xo);
    } while (!XV_OK());
  }
#undef XV_OK
}
__device__ __forceinline__ void ffn_gu(const u32x4 (&gv)[4], const u32x4 (&uv)[4], const u32x4 (&xv)[4], f32x4& ga, f32x4& ua) {
  ga = mfma16(gv[0], xv[0], f32x4{0.f, 0.f, 0.f, 0.f}); ua = mfma16(uv[0], xv[0], f32x4{0.f, 0.f, 0.f, 0.f});
#pragma unroll
  for (int i = 1; i < 4; ++i) { ga = mfma16(gv[i], xv[i], ga); ua = mfma16(uv[i], xv[i], ua); }
}
__device__ __forceinline__ void ffn_gu_check(rsrc_t rs, u32x2 T, unsigned xo, int l, const u32x4 (&gv)[4], const u32x4 (&uv)[4], u32x4 (&xv)[4], f32x4& ga, f32x4& ua) {
  if (__builtin_expect(__any(ga[0] != ga[0]), 0)) {
    ffn_xwait(rs, T, xo, l, xv);
    ffn_gu(gv, uv, xv, ga, ua);
  }
}
__device__ __forceinline__ void ffn_gu_store(char* smem, int l, int w, f32x4 ga, f32x4 ua) {
  LDS_AS f32x4* sred = (LDS_AS f32x4*)(smem + L_SRED);
  if ((l & 15) == 0) { sred[w * 8 + (l >> 4)] = ga; sred[w * 8 + 4 + (l >> 4)] = ua; }
}
__device__ __forceinline__ f32x4 ffn_t_read(char* smem, int l) { return ((const LDS_AS f32x4*)(smem + L_SRED))[(l & 7) * 8 + ((l >> 3) & 1) * 4 + (l >> 4)]; }
__device__ __forceinline__ void ffn_d_read(char* smem, int l, int blk, s16x4 (&dfr)[8]) {
#pragma unroll
  for (int i = 0; i < 8; ++i) dfr[i] = trread(smem, L_DN + blk * 4096 + (l >> 4) * 1024 + ((l & 15) >> 2) * 256 + i * 32 + (l & 3) * 8);
}
__device__ __forceinline__ float ffn_d_sum(float t) { t += dppf<0xB1>(t); t += dppf<0x4E>(t); return t + dppf<0x141>(t); }
__device__ __forceinline__ u32x2 ffn_h(f32x4 t, int l) {
#pragma unroll
  for (int e = 0; e < 4; ++e) t[e] = ffn_d_sum(t[e]);
  const float a = (l & 1) ? t[1] : t[0], b = (l & 1) ? t[3] : t[2], s = (l & 2) ? b : a;
  const float gg = rbf1(s), uu = rbf1(dppf<0x128>(s));
  const float h = rbf1(gg * __builtin_amdgcn_rcpf(1.0f + __expf(-gg))) * uu;
  const unsigned h0 = pack2(h, dppf<0x55>(h)), h1 = pack2(dppf<0xAA>(h), dppf<0xFF>(h));
  return (l & 15) == 0 ? u32x2{h0, h1} : u32x2{0u, 0u};
}
__device__ __forceinline__ void ffn_d_mma(const s16x4 (&dfr)[8], u32x2& hh, f32x4& ya, f32x4& yb) {
#pragma unroll
  for (int i = 0; i < 8; ++i) {
    if (i & 1) yb = mfma16k16(dfr[i], __builtin_bit_cast(s16x4, hh), yb);
    else ya = mfma16k16(dfr[i], __builtin_bit_cast(s16x4, hh), ya);
    hh = u32x2{dppu<0x111>(hh[0]), dppu<0x111>(hh[1])};
  }
}
__device__ __forceinline__ void ffn_d_store(rsrc_t rs, int l, int G0, int L, int slice, f32x4 y) {
  const unsigned c = l & 15;
  st16(unpoison4(__builtin_bit_cast(u32x4, y)), rs, (G0 + (c >> 1)) * 32768 + slice * 128 + (c & 1) * 64 + (l >> 4) * 16, OFF_PART + L * (1u << 20));
}
__device__ __forceinline__ void red_load(rsrc_t rs, int tid, int L, unsigned G, unsigned key, u32x4 (&pv)[4], u32x2& cxn, u32x2& cx1) {
#pragma unroll
  for (int k = 0; k < 4; ++k) pv[k] = ld16(rs, min(tid + 512 * k, 1791) * 16, OFF_PART + L * (1u << 20) + G * 32768);
  cxn = ld8(rs, (tid & 3) * 16 + (tid & 4) * 2, OFF_CA + L * 32768 + (key_row(key) >> 7) * 2048 + G * 64);
  cx1 = ld8(rs, (tid & 3) * 16 + (tid & 4) * 2, OFF_CB + L * 32768 + (key_row(key) >> 7) * 2048 + G * 64);
}
__device__ __forceinline__ void red_sum(rsrc_t rs, char* smem, u32x2 T, int tid, int w, int L, unsigned G, unsigned key,
                                        u32x4 (&pv)[4], u32x2& cxn, u32x2& cx1) {
#define PV_OK() __all(max(max(max(umax4(pv[0]), umax4(pv[1])), max(umax4(pv[2]), umax4(pv[3]))), max(max(cxn[0], cxn[1]), max(cx1[0], cx1[1]))) != POISON)
  if (!PV_OK()) {
    unsigned spins = 0;
    do {
      if (spin_fail(spins, rs, T)) break;
      red_load(rs, tid, L, G, key, pv, cxn, cx1);
    } while (!PV_OK());
  }
#undef PV_OK
  f32x4 s = (__builtin_bit_cast(f32x4, pv[0]) + __builtin_bit_cast(f32x4, pv[1])) + __builtin_bit_cast(f32x4, pv[2]);
  if (w < 4) s += __builtin_bit_cast(f32x4, pv[3]);
  ((LDS_AS f32x4*)(smem + L_SFIN))[tid] = s;
}
template <int NW> __device__ __forceinline__ u32x2 red_final(char* smem, int l, unsigned key, u32x2 cxn, u32x2 cx1) {
  const LDS_AS f32x4* sf = (const LDS_AS f32x4*)(smem + L_SFIN) + l;
  f32x4 s = (sf[0] + sf[64]) + (sf[128] + sf[192]);
  if constexpr (NW == 8) s += (sf[256] + sf[320]) + (sf[384] + sf[448]);
#pragma unroll
  for (int j = 0; j < 4; ++j) s[j] = sum_hi8(s[j]);
  const unsigned k16 = key >> 16;
  const float rmax = __builtin_bit_cast(float, ((k16 & 0x8000u) ? (k16 & 0x7fffu) : (~k16 & 0xffffu)) << 16);
  const float sig = rbf1(__builtin_amdgcn_rcpf(1.0f + __expf(-rmax)));
  const float xn4[4] = {lo16(cxn[0]), hi16(cxn[0]), lo16(cxn[1]), hi16(cxn[1])};
  const float x14[4] = {lo16(cx1[0]), hi16(cx1[0]), lo16(cx1[1]), hi16(cx1[1])};
  float o[4];
#pragma unroll
  for (int j = 0; j < 4; ++j) o[j] = x14[j] + rbf1(xn4[j] + rbf1(rbf1(s[j]) * sig));
  return u32x2{pack2(o[0], o[1]), pack2(o[2], o[3])};
}

struct RowFin { float mean, var, mb, rd; unsigned key; };
__device__ __forceinline__ void fin_wait(rsrc_t rs, u32x2 T, unsigned voff, unsigned so, u32x4 (&sp)[4]) {
#define ST_OK() __all(max(max(umax4(sp[0]), umax4(sp[1])), max(umax4(sp[2]), umax4(sp[3]))) != POISON)
  if (!ST_OK()) {
    unsigned spins = 0;
    do {
      if (spin_fail(spins, rs, T)) break;
#pragma unroll
      for (int i = 0; i < 4; ++i) sp[i] = ld16(rs, voff + i * 2048, so);
    } while (!ST_OK());
  }
#undef ST_OK
}
__device__ __forceinline__ RowFin fin_key(rsrc_t rs, u32x2 T, unsigned voff, unsigned so, char* skey, int l, int w, unsigned R, float wsum, u32x4 (&sp)[4]) {
  RowFin r;
#define ST_SUM() ((__builtin_bit_cast(f32x4, sp[0]) + __builtin_bit_cast(f32x4, sp[1])) + (__builtin_bit_cast(f32x4, sp[2]) + __builtin_bit_cast(f32x4, sp[3])))
  f32x4 sf = ST_SUM();
  const float z = (sf[0] + sf[1]) + sf[2];
  if (__any(z != z)) {
    fin_wait(rs, T, voff, so, sp);
    sf = ST_SUM();
  }
#undef ST_SUM
  float s0 = sf[0], s1 = sf[1], s2 = sf[2];
  add_g3(s0, s1, s2);
  r.mean = s0 * (1.0f / 1024.0f);
  r.var = fmaxf(__builtin_fmaf(-r.mean, r.mean, s1 * (1.0f / 1024.0f)), 0.f);
  r.mb = rbf1(r.mean); r.rd = ln_rd(r.var);
  const unsigned rb16 = pack2((s2 - r.mb * wsum) * r.rd, 0.f) & 0xffffu;
  r.key = min((((rb16 & 0x8000u) ? (~rb16 & 0xffffu) : (rb16 | 0x8000u)) << 16) | (0xffffu - R), POISON - 1);
  const unsigned wk = umax16(r.key);
  if (l == 0) ((LDS_AS unsigned*)skey)[w] = wk;
  return r;
}
__device__ __forceinline__ unsigned fin_cand(rsrc_t rs, char* skey, int L, int g, int rb, int ct, const RowFin& r, const f32x2 (&v)[8]) {
  const u32x4 k0 = *(const LDS_AS u32x4*)skey, k1 = *(const LDS_AS u32x4*)(skey + 16);
  const unsigned best = max(umax4(k0), umax4(k1));
  if (r.key == best) {
    const unsigned co = L * 32768 + rb * 2048 + ct * 128 + g * 16;
    f32x2 t[8], rd2 = {r.rd, r.rd}, of2 = {-r.mb * r.rd, -r.mb * r.rd};
    asm volatile("" : "+v"(rd2), "+v"(of2));
#pragma unroll
    for (int j = 0; j < 8; ++j) t[j] = __builtin_elementwise_fma(v[j], rd2, of2);
    st16(unpoison4(pack8v(t)), rs, co, OFF_CA); st16(unpoison4(pack8v(t + 4)), rs, co + 64, OFF_CA);
    if (g == 0) st4(best, rs, OFF_KEY + (L * 16 + ct) * 128 + rb * 4, 0);
    st16(unpoison4(pack8v(v)), rs, co, OFF_CB); st16(unpoison4(pack8v(v + 4)), rs, co + 64, OFF_CB);
  }
  return best;
}
__device__ __forceinline__ void fin_x2(const RowFin& r, f32x2 (&v)[8]) {
  f32x2 sc2 = {1.0f + r.rd, 1.0f + r.rd}, of2 = {-r.mb * r.rd, -r.mb * r.rd};
  asm volatile("" : "+v"(sc2), "+v"(of2));
#pragma unroll
  for (int j = 0; j < 8; ++j) v[j] = __builtin_elementwise_fma(v[j], sc2, of2);
}

extern "C" __device__ unsigned long long dispatch_id() __asm("llvm.amdgcn.dispatch.id");

extern "C" __global__ __launch_bounds__(512, 2) void mega_kernel(const bf16* x, char* ws, const bf16* wqkv, const bf16* wo, const bf16* wr,
                                                                 const bf16* wg, const bf16* wu, const bf16* wd, bf16* out) {
  extern __shared__ __attribute__((aligned(16))) char smem[];
  const int w0_ = __builtin_amdgcn_readfirstlane(threadIdx.x >> 6);
  const unsigned lbase = (unsigned)(size_t)(LDS_AS char*)smem;
  {
    unsigned long long pc;
    unsigned len;
    asm volatile("s_getpc_b64 %0\n\ts_mov_b32 %1, .Ltext_end-mega_kernel\n\t.subsection 1\n.Ltext_end:\n\t.subsection 0" : "=s"(pc), "=s"(len));
    const unsigned o = ((blockIdx.x >> 3) * 64 + (threadIdx.x & 63)) * 128;
    if (w0_ == 7 && o < len)
      asm volatile("s_mov_b32 m0, %0\n\ts_nop 0\n\tglobal_load_lds_dword %1, %2" ::"s"(lbase + L_TOUCH0), "v"(o), "s"(pc & ~127ull) : "memory");
  }
  asm volatile("" : "+s"(x), "+s"(ws), "+s"(wqkv));
#define IDS()                                                                                         \
  int tid = threadIdx.x, w = w0_, bid = blockIdx.x;                                                   \
  asm volatile("" : "+v"(tid));                                                                       \
  asm volatile("" : "+s"(w));                                                                         \
  asm volatile("" : "+s"(bid));                                                                       \
  const int l = tid & 63, xcd = bid & 7, bi = bid >> 3;                                               \
  const int p = (xcd & 1) * 4 + (bi & 3), h = (xcd >> 1) * 8 + (bi >> 2);         \
  const int rb = 2 * xcd + (bi & 1), ct = bi >> 1;                               \
  const int slice = xcd * 28 + bi;                                                   \
  const bool ffn_on = bi < 28;                                                                        \
  const int g = l >> 4;                                                                               \
  const int rt = w < 4 ? w : 11 - w;          \
  KARGS();                                                                                            \
  (void)p; (void)h; (void)rb; (void)ct; (void)slice; (void)ffn_on; (void)g; (void)rt;
#define PROT() (2 * (p & 3) + (p >> 2))
  const rsrc_t rs = __builtin_amdgcn_make_buffer_rsrc((void*)ws, 0, WS_BYTES, 0x00020000);
  typedef const void* const __attribute__((address_space(4)))* karg_t;
#define KARGS() karg_t ka = (karg_t)__builtin_amdgcn_kernarg_segment_ptr(); asm volatile("" : "+s"(ka))
#define ARGP(name, i) const GAS bf16* name = (const GAS bf16*)ka[i]; asm volatile("" : "+s"(name))
#define RSRC(ptr, bytes) __builtin_amdgcn_make_buffer_rsrc((void*)(ptr), 0, bytes, 0x00020000)
#define rwq RSRC(pwq, 1024 * 3072 * 2)
#define rwo RSRC(pwo, 1024 * 1024 * 2)
#define rwg RSRC(pwg, 1024 * F_ * 2)
#define rwu RSRC(pwu, 1024 * F_ * 2)
#define rwd RSRC(pwd, 1024 * F_ * 2)
#define rout RSRC(pout, 2048 * 1024 * 2)

#define WQ_VOFF() ((l >> 2) * 6144 + (((l & 3) ^ ((((l >> 4) ^ (l >> 5)) & 1) << 1)) << 4))
#define WQ_PIECE(t, j, dst) dma16(rwq, wqv, ((((t) + rot) & 7) * 128 + ((j) & 7) * 16) * 6144 + ((j) >> 3) * 2048 + h * 64, lbase + (dst) + (j) * 1024)
#define WQ_BOFF(dt) ((g >> 1) * 1024 + (8 * (g & 1) + ((l & 15) >> 2)) * 64 + (l & 3) * 8 + (((dt) ^ (g & 1)) << 5))
#define X_REQ(u, hf) dma16(rx, xv[hf], (p * 256 + rt * 32 + ((u) & 1) * 16 + (hf) * 8) * 2048 + ((((u) >> 2) + rot) & 7) * 256 + (((u) >> 1) & 1) * 128, lbase + rt * 2048 + x_slot(u) + (hf) * 1024)
#define WO_PIECE(j) dma16(rwo, wov, ((w >> 2) * 64 + (w & 3) + 4 * (j)) * 16384 + ct * 128, lbase + 32768 + (j) * 8192 + w * 1024)
#define WD_DMA() _Pragma("unroll") for (int i = 0; i < 4; ++i) WD_DMA1(i)
#define WD_DMA1(i)                                                                                   \
      dma16(rwd, (l >> 4) * 2048 + (l & 15) * 16, (unsigned)(slice * 16 + 4 * (i)) * 2048 + w * 256, lbase + L_DN + w * 4096 + (i) * 1024)
#define FFN_DMA(m) dma16((m) & 1 ? rwu : rwg, fnv, (unsigned)(((4 * w + ((m) >> 1)) * 32) * F_ + slice * 16) * 2, lbase + 65536 + (m) * 8192 + w * 1024)
#define POISON_BIG(L)                                                                                \
  if (w >= 2) {                                                                                      \
    unsigned pz1 = POISON;                                                                           \
    asm volatile("" : "+v"(pz1));                                                                    \
    st16(u32x4{pz1, pz1, pz1, pz1}, rs, (w >= 4 ? OFF_PART + (L) * (1u << 20) + bid * 4096 - 4096 : OFF_STAT + (L) * 524288 + (p * 32 + h) * 2048 - 2048) + tid * 16, 0); \
  }
  u32x4 gv[4], uv[4];
  u32x4 keep[9];
  u32x4 pre[4][2];
#define PRE_LOAD(it_, rot_, s0, s1)                                                                  \
  static_for<(s1) - (s0)>([&](auto c_) __attribute__((always_inline)) {                              \
    constexpr int s = (s0) + decltype(c_)::value;                                                    \
    const unsigned ab_ = OFF_XN + (it_) * (4u << 20) + (((rot_) + (s >> 2)) & 7) * 8192 + ((s >> 1) & 1) * 4096; \
    ld16w<2 * s, (s & 1) * 2048>(pre[s][0], rs, l * 16, ab_ + (p * 8 + rt) * 65536);                         \
    ld16w<2 * s + 1, (s & 1) * 2048 + 1024>(pre[s][1], rs, l * 16, ab_ + (p * 8 + rt) * 65536);                 \
  })
  u32x2 T;
  const GAS bf16* tsrc;
  unsigned xv[2], xa[2];
  u32x2 ep;
#define X_READ(u, d0, d1) { d0 = *(const LDS_AS u32x4*)(size_t)(xa[0] + x_slot(u)); d1 = *(const LDS_AS u32x4*)(size_t)(xa[1] + x_slot(u)); }
  const rsrc_t rx = RSRC(x, 2048 * 1024 * 2);
  {
    IDS();
    const int rot = PROT();
    const bf16* pwq = wqkv;
    const unsigned wqv = WQ_VOFF();
    xv[0] = (l >> 3) * 2048 + (((l & 7) ^ (l >> 4)) << 4); xv[1] = xv[0] ^ 64;
#define WQ_KMAJ(q) (((q) % 3) * 8 + (q) / 3)
    WQ_PIECE(0, WQ_KMAJ(w), L_RING);
    X_REQ(0, 0); X_REQ(0, 1); X_REQ(1, 0); X_REQ(1, 1);
    WQ_PIECE(0, WQ_KMAJ(8 + w), L_RING);
    asm volatile("s_barrier" ::: "memory");
    WQ_PIECE(0, WQ_KMAJ(16 + w), L_RING);
    X_REQ(2, 0); X_REQ(2, 1);
    asm volatile("buffer_load_dwordx2 %0, off, %1, 0 sc1" : "=v"(ep) : "s"(rs) : "memory");
    if constexpr (PBS < 0) { POISON_BIG(0); }
    {
      ARGP(pwo, 3); ARGP(pwg, 5); ARGP(pwu, 6);
      const int t = tid < 288 ? tid - 64 : tid - 288, go = (bi * 32 + t / 7) * F_ + xcd * 448 + (t % 7) * 64;
      tsrc = tid < 64 ? pwo + bid * 4096 + tid * 64 : tid < 288 ? pwg + go : pwu + go;
      asm volatile("" : "+v"(tsrc));
    }
#pragma unroll
    for (int kk = 0; kk < 2; ++kk) {
      const int r = l & 15;
      xa[kk] = lbase + rt * 2048 + (r >> 3) * 1024 + (r & 7) * 128 + (((4 * kk + g) ^ (r >> 1)) << 4);
      asm volatile("" : "+v"(xa[kk]));
    }
    if (tid == 192) *(LDS_AS u32x4*)(smem + L_QC) = u32x4{0u, 0u, 0u, 0u};
    wait_vm<4>();
    X_READ(0, pre[0][0], pre[1][0]); X_READ(1, pre[0][1], pre[1][1]);
    asm volatile("s_waitcnt lgkmcnt(0)\n\ts_barrier" : "+v"(pre[0][0]), "+v"(pre[0][1]), "+v"(pre[1][0]), "+v"(pre[1][1])::"memory");
    X_REQ(3, 0); X_REQ(3, 1); X_REQ(4, 0); X_REQ(4, 1);
  }
  unsigned sel0 = 0x7FFFFFFFu;
  u32x4 x2p[2];
  unsigned xm0 = 0, kg0 = 0;

  auto layer = [&](auto it_c) __attribute__((always_inline)) {
    constexpr int it = decltype(it_c)::value;
    f32x4 acc[2][6];
    f32x2 qs2[2];
    u32x4 k0, k1, v0, v1;
    unsigned tdum = 0;
    {
      IDS();
      bool aff = false;
      unsigned aq[2];
      u32x4 av[2];
#define PIN_ACC() asm volatile("" : "+{v[176:179]}"(acc[0][0]), "+{v[180:183]}"(acc[0][1]), "+{v[184:187]}"(acc[0][2]), "+{v[188:191]}"(acc[0][3]), "+{v[192:195]}"(acc[0][4]), "+{v[196:199]}"(acc[0][5]), \
                               "+{v[200:203]}"(acc[1][0]), "+{v[204:207]}"(acc[1][1]), "+{v[208:211]}"(acc[1][2]), "+{v[212:215]}"(acc[1][3]), "+{v[216:219]}"(acc[1][4]), "+{v[220:223]}"(acc[1][5]))
#define PINV4(x) asm volatile("" : "+v"(x[0]), "+v"(x[1]), "+v"(x[2]), "+v"(x[3])::"memory")
      auto qkv_loop = [&](auto it_c) __attribute__((always_inline)) {
        constexpr int IT = decltype(it_c)::value;
        const unsigned abase = OFF_XN + IT * (4u << 20) + (p * 8 + rt) * 65536;
        const int rot = PROT() + 5 * IT;
        unsigned fkey = 0, kv, bq[2];
        u32x4 fx[4], bv[2], af[WA][2];
        u32x2 cxn, cx1;
        f32x4 sa, sb, cs4[2];
        f32x2 b2[2], sv[8];
#define AB_OFF() (OFF_AB + IT * 16384 + (p * 8 + rt) * 128)
#define A_LOAD1(k, a) ld16w<(k) % WA * 2 + (a), ((k) & 1) * 2048 + (a) * 1024>(af[(k) % WA][a], rs, l * 16, abase + ((((k) >> 2) + rot) & 7) * 8192 + (((k) >> 1) & 1) * 4096)
#pragma unroll
        for (int s = 0; s < 2 + 2 * IT; ++s) { af[s][0] = pre[s][0]; af[s][1] = pre[s][1]; }
        const bf16* pwq = wqkv;
        const int tstep = TS0 + bid % TSN;
        const unsigned wqv = WQ_VOFF();
#define QKV_DMA1(t, i)                                                                            \
  {                                                                                               \
    if constexpr (IT == 0) WQ_PIECE(t, 3 * w + (i), L_RING + SLOT(t) * STAGE);                    \
    else dma16s<(i) * 1024>(rs, l * 16, OFF_WI + (h * 8 + (((t) + rot) & 7)) * STAGE + w * 3072, lbase + L_RING + SLOT(t) * STAGE + w * 3072); \
  }
        unsigned bo[2][2];
#pragma unroll
        for (int dt = 0; dt < 2 - IT; ++dt) {
          bo[dt][0] = lbase + L_RING + (IT ? l * 16 : WQ_BOFF(dt)); bo[dt][1] = bo[dt][0] + 2 * STAGE;
          asm volatile("" : "+v"(bo[dt][0]), "+v"(bo[dt][1]));
        }
        auto frag0 = [&](int sl, int f) __attribute__((always_inline)) {
          const unsigned o = lbase + L_RING + (4 * g + ((l & 15) >> 2)) * 64 + (l & 3) * 8 + (((f ^ g ^ (g >> 1)) & 1) << 5) + sl * STAGE + ((f % 6) >> 1) * 8192 + (f / 6) * 2048;
          frag_t t;
          t.hf.a = trabs(o); t.hf.b = trabs(o + 1024);
          return t.v;
        };
        u32x4 df[4];
        auto dot_mma = [&](const u32x4 (&xf)[4]) __attribute__((always_inline)) {
          sa = mfma16(df[0], xf[0], f32x4{0.f, 0.f, 0.f, 0.f}); sb = mfma16(df[1], xf[1], f32x4{0.f, 0.f, 0.f, 0.f});
          sa = mfma16(df[2], xf[2], sa); sb = mfma16(df[3], xf[3], sb);
        };
        auto dot_store = [&](unsigned dst) __attribute__((always_inline)) {
          if ((l & 15) == 0) st16(unpoison4(__builtin_bit_cast(u32x4, sa + sb)), rs, g * 16, dst + ((h * 6 + w) * 8 + p) * 64);
        };
        auto tile_sum = [&](u32x4 pt, int dst) __attribute__((always_inline)) {
          f32x4 c = __builtin_bit_cast(f32x4, pt);
#pragma unroll
          for (int e = 0; e < 4; ++e) { c[e] += dppf<0x124>(c[e]); c[e] += dppf<0x128>(c[e]); c[e] = add_x16(c[e]); }
          if ((l & 28) == 0) *(LDS_AS f32x4*)(smem + dst + (w * 2 + (l >> 5)) * 64 + (l & 3) * 16) = c;
        };
        frag_t bfr[6];
#define QKV_BO(s, n) (bo[(n) & 1][SLOT((s) >> 2) >> 1] + (SLOT((s) >> 2) & 1) * STAGE + ((n) >> 1) * 8192 + ((s) & 3) * 2048)
#define QKV_RDA(s, n)                                                                             \
  {                                                                                               \
    if constexpr (IT == 0) bfr[n].hf.a = trabs(QKV_BO(s, n));                                     \
    else bfr[n].v = *(const LDS_AS u32x4*)(size_t)(bo[0][SLOT((s) >> 2) >> 1] + (SLOT((s) >> 2) & 1) * STAGE + (((s) & 3) * 6 + (n)) * 1024); \
  }
#define QKV_RDB(s, n) { if constexpr (IT == 0) bfr[n].hf.b = trabs(QKV_BO(s, (n) ^ 1) + 256); }
#pragma unroll
        for (int n = 0; n < 6; ++n) { QKV_RDA(0, n); if (n < 5) QKV_RDB(0, n); }
#define XF_OFF() (l & 8 ? ((l & 7) * RX + bi % RX) * 256 + h * 8 : (p * RX + (xcd >> 1) % RX) * 256 + (4 * rt + (l & 3)) * 8)
#define XP_OFF() (OFF_XNEW + ((rot + FXT) & 7) * 256)
#define FP_OFF(i) (w == 3 ? OFF_XNEW + (i) * 1024 : OFF_FX + h * 3072 + w * 1024)
#define FX_OK(n) __all(((n) == 4 ? max(max(umax4(fx[0]), umax4(fx[1])), max(umax4(fx[2]), umax4(fx[3]))) : max(umax4(fx[0]), umax4(fx[1]))) != POISON)
#define SINCE(s0) (vm_at(IT, s, n) - vm_at(IT, s0, 5))
#define DEF4(x) asm volatile("" : "=v"(x[0]), "=v"(x[1]), "=v"(x[2]), "=v"(x[3]))
        LDS_AS unsigned* qcnt = (LDS_AS unsigned*)(smem + L_QC);
        unsigned qc;
        unsigned qadr = lbase + L_QC, qone = 1;
        asm volatile("" : "+v"(qadr), "+v"(qone));
#define count_in(i) asm volatile("s_mov_b64 exec, 1\n\tds_add_u32 %0, %1 offset:%2\n\ts_mov_b64 exec, -1" ::"v"(qadr), "v"(qone), "n"(4 * (i)) : "memory")
        auto count_wait = [&](int i, unsigned want) __attribute__((always_inline)) {
          if (__builtin_expect((unsigned)__builtin_amdgcn_readfirstlane(qc) < want, 0)) {
            while ((unsigned)__builtin_amdgcn_readfirstlane(__hip_atomic_load(qcnt + i, __ATOMIC_RELAXED, __HIP_MEMORY_SCOPE_WORKGROUP)) < want) {}
          }
          asm volatile("" ::: "memory");
        };
        unsigned long long xok = xm0;
        auto x1_need = [&](unsigned long long m) __attribute__((always_inline)) {
          if (__builtin_expect((xok & m) != m, 0)) {
            for (unsigned spins = 0;;) {
              const u32x2 f = ld8(rs, F_X1 + (p * RX + (xcd >> 1) % RX) * 256 + (l & 31) * 8, 0);
              xok = __builtin_amdgcn_ballot_w64(f[0] == T[0] && f[1] == T[1]);
              if ((xok & m) == m) break;
              if (spin_fail(spins, rs, T)) { xok = ~0ull; break; }
              __builtin_amdgcn_s_sleep(KSL);
            }
            asm volatile("" ::: "memory");
          }
        };
        auto gap = [&](auto sc_, auto nc_) __attribute__((always_inline)) {
          constexpr int s = decltype(sc_)::value, n = decltype(nc_)::value;
          {
            constexpr int ts = (s + 3) / 4, tc = (s + 2) / 4, t0 = 1;
            if constexpr (ts >= 3 && ts < 8 && s == 4 * ts - 3 && n == 2) { wait_vm<vm_at(IT, s, n) - vm_tile(IT, ts)>(); count_in(0); }
            if constexpr (!IT && tc >= t0 && tc < 3 && s == 4 * tc - 2 && n == 1) { wait_vm<vm_at(IT, s, n) - vm_tile(IT, tc)>(); count_in(0); }
            if constexpr (IT && ts >= t0 && ts < 3 && s == 4 * ts - 3 && n == 2) count_in(0);
            if constexpr (tc >= t0 && tc < 8 && s == 4 * tc - 2) {
              if constexpr (n == 3) qc = __hip_atomic_load(qcnt, __ATOMIC_RELAXED, __HIP_MEMORY_SCOPE_WORKGROUP);
              if constexpr (n == 5) count_wait(0, 8 * (tc - t0 + 1));
            }
          }
          if constexpr (n == 0 && tile_piece(s)) QKV_DMA1(((s - 3) >> 2) + 3, (s - 3) & 3);
          if constexpr (IT == 0) {
            if constexpr (s == 0 && !(n & 1)) QKV_DMA1(1, n >> 1);
            if constexpr (s >= 1 && s < 4 && n == 2) QKV_DMA1(2, s - 1);
            constexpr int u = s + XL;
            if constexpr (n == 1 && u < 32) {
              wait_vm<vm_at(0, s, n) - vm_x(u)>();
              X_READ(u, af[(u & ~1) % WA][u & 1], af[(u | 1) % WA][u & 1]);
            }
            if constexpr (n == 3 && u + XU < 32) { asm volatile("s_waitcnt lgkmcnt(%0)" ::"n"(s + 1 < QL ? 4 : 3) : "memory"); X_REQ(u + XU, 0); }
            if constexpr (n == 5 && u + XU < 32) X_REQ(u + XU, 1);
            if constexpr (s == WTS && n == WTG) {
              const int i = bi * 192 + w * 24 + min(l, 23);
              touch(tdum, (const GAS bf16*)pwq + (size_t)(i / 12 + 512 * (~xcd & 1)) * 3072 + ((i % 12) >> 2) * 1024 + (xcd >> 1) * 256 + (i & 3) * 64);
            }
            if constexpr (s == PBS && n == 0) { POISON_BIG(0); }
            if constexpr (s == 0 && n == 1) count_in(2);
            if constexpr (s == 0 && n == 3) qc = __hip_atomic_load(qcnt + 2, __ATOMIC_RELAXED, __HIP_MEMORY_SCOPE_WORKGROUP);
            if constexpr (s == 0 && n == 5) count_wait(2, 8);
            if constexpr (s == 1 && n == 1) {
              asm volatile("" : "+v"(ep)::"memory");
              const unsigned long long e = (((unsigned long long)(unsigned)__builtin_amdgcn_readfirstlane(ep[1]) << 32) | (unsigned)__builtin_amdgcn_readfirstlane(ep[0])) + 1ull + dispatch_id() * 0x9E3779B97F4A7C16ull;
              T = u32x2{(unsigned)e, (unsigned)(e >> 32)};
            }
            const unsigned R = p * 256 + h * 8 + w;
            if constexpr (s == RWL && n == 4) ldrow(bv[0], bv[1], rx, l * 16, R * 2048);
            if constexpr (s == RWS && n == 0) {
              wait_vm<SINCE(RWL)>();
              asm volatile("" : "+v"(bv[0]), "+v"(bv[1])::"memory");
              unpack8v(bv[0], sv);
              pin4(sv);
            }
            if constexpr (s == RWS && n == 2) { unpack8v(bv[1], sv + 4); pin4(sv + 4); }
            if constexpr (s == RWS && n == 4) {
              b2[0][0] = hadd(((sv[0] + sv[1]) + (sv[2] + sv[3])) + ((sv[4] + sv[5]) + (sv[6] + sv[7])));
              asm volatile("" : "+v"(b2[0][0]));
            }
            if constexpr (s == RWS && n == 5) { const float m = sum64(b2[0][0]) * (1.0f / 1024.0f); b2[0] = f32x2{m, m}; asm volatile("" : "+v"(b2[0])); }
            if constexpr (s == RWS + 1 && n == 0) {
              f32x2 q0 = {0.f, 0.f}, q1 = {0.f, 0.f};
#pragma unroll
              for (int j = 0; j < 8; j += 2) {
                const f32x2 d0 = sv[j] - b2[0], d1 = sv[j + 1] - b2[0];
                q0 = __builtin_elementwise_fma(d0, d0, q0); q1 = __builtin_elementwise_fma(d1, d1, q1);
              }
              b2[1][0] = hadd(q0 + q1);
              asm volatile("" : "+v"(b2[1][0]));
            }
            if constexpr (s == RWS + 1 && n == 2) { b2[1][0] = sum64(b2[1][0]) * (1.0f / 1024.0f); asm volatile("" : "+v"(b2[1][0])); }
            if constexpr (s == RWS + 1 && n == 4) {
              const float rd = ln_rd(b2[1][0]);
              if (l < 2) st4(__builtin_bit_cast(unsigned, l == 0 ? rd : -rbf1(b2[0][0])), rs, OFF_AB + R * 4 + l * 8192, 0);
            }
            if constexpr (s == POI && n == 4) {
              unsigned pz1 = POISON;
              asm volatile("" : "+v"(pz1));
              const u32x4 pz = {pz1, pz1, pz1, pz1};
              const unsigned prb = 2 * p + (h >> 4), pct = h & 15;
              static_assert(OFF_CB == OFF_CA + 131072, "poison addressing");
              if (w == 4 && l < 32) st16(pz, rs, OFF_CA + (l & 8) * 4096 + (l & 16) * 8192 + prb * 2048 + pct * 128 + (l & 7) * 16, 0);
              if (w == 5 && l < 4) st16(pz, rs, OFF_AB + 16384 + (p * 32 + h) * 32 + (l & 1) * 16 + (l >> 1) * 8192, 0);
              if (w == 7 && l < 24) st16(pz, rs, OFF_FX + ((h * 6 + (l >> 2)) * 8 + p) * 64 + (l & 3) * 16, 0);
              if (w == 6 && l < 4) st4(pz1, rs, l < 2 ? OFF_XNEW + bid * 8 + l * 4 : OFF_KEY + ((l & 1) * 16 + pct) * 128 + prb * 4, 0);
            }
            static_assert(vm_x(EIN + XL) >= vm_at(0, EIN - 3, 5) && IMG >= 1 && CSD >= 1, "first flag");
            if constexpr (s == EIN && n == 1) count_in(1);
            if constexpr (s == EIN + 2 && n == 0) qc = __hip_atomic_load(qcnt + 1, __ATOMIC_RELAXED, __HIP_MEMORY_SCOPE_WORKGROUP);
            if constexpr (s == EIN + 2 && n == 2) {
              if (w == 0) { count_wait(1, 8); if (l < RX) st8(T, rs, F_XN + (p * RX + l) * 256 + h * 8, 0); }
            }
            if constexpr (s == ERQ && n == 4) asm volatile("buffer_load_dwordx2 %0, %1, %2, 0 offen sc1" : "=v"(cxn) : "v"(XF_OFF() + F_XN), "s"(rs) : "memory");
            if constexpr (s == CSL && n == 2) {
              wait_vm<SINCE(ERQ)>();
              asm volatile("" : "+v"(cxn)::"memory");
              if (!__all(cxn[0] == T[0] && cxn[1] == T[1])) poll_flags(rs, XF_OFF(), F_XN, T);
            }
          } else {
#define X1_OFF() (F_X1 + (p * RX + (xcd >> 1) % RX) * 256 + (l & 31) * 8)
            if constexpr ((s == 0 || s == 4) && n == 2) asm volatile("buffer_load_dwordx2 %0, %1, %2, 0 offen sc1" : "=v"(cxn) : "v"(X1_OFF()), "s"(rs) : "memory");
            if constexpr ((s == 3 || s == 7) && n == 0) {
              wait_vm<vm_at(1, s, 1) - vm_at(1, s - 3, 3)>();
              asm volatile("" : "+v"(cxn)::"memory");
              xok |= __builtin_amdgcn_ballot_w64(cxn[0] == T[0] && cxn[1] == T[1]);
            }
            if constexpr (a_req(s, n) >= 0 && (a_req(s, n) & 7) == 0) x1_need(0x30003ull << (((rot + a_req(s, n) / 8) & 7) * 2));
          }
          if constexpr (IT && a_req(s, n) >= 0) A_LOAD1(a_req(s, n) / 2, a_req(s, n) & 1);
          if constexpr (IT == 0 && s == CSD) {
            if constexpr (n == 0) { DEF4(df); asm volatile("" : "=v"(sa), "=v"(sb)); if (w < 6) { for (int i = 0; i < 4; ++i) df[i] = frag0(SLOT(s >> 2), 6 * i + w); } }
            if constexpr (n == 2) {
              if (w < 6) {
                unsigned one2 = 0x3F803F80u;
                asm volatile("" : "+v"(one2));
                const u32x4 o4 = {one2, one2, one2, one2}, of[4] = {o4, o4, o4, o4};
                dot_mma(of);
              }
            }
            if constexpr (n == 4) { if (w < 6) dot_store(OFF_CS); }
          }
          if constexpr (!IT && s == CSL && n == 4) { asm volatile("" : "=v"(fx[0])); if (w < 3) ld16h<0>(fx[0], rs, l * 16, OFF_CS + h * 3072 + w * 1024); }
          if constexpr (!IT && s == CSS && n == 2) {
            if (w < 3) {
              wait_vm<SINCE(CSL)>();
              asm volatile("" : "+v"(fx[0])::"memory");
              tile_sum(fx[0], L_CSL);
            }
          }
          if constexpr (s == abl(IT) && n == 4) ld4h<0>(aq[0], rs, (l & 31) * 4 + (l >> 5) * 8192, AB_OFF());
          if constexpr (s == ABS && n == 0) {
            wait_vm<SINCE(abl(IT))>();
            asm volatile("" : "+v"(aq[0])::"memory");
            if constexpr (IT) {
              for (unsigned spins = 0; !__all(aq[0] != POISON) && !spin_fail(spins, rs, T);) {
                ld4h<0>(aq[0], rs, (l & 31) * 4 + (l >> 5) * 8192, AB_OFF());
                asm volatile("s_waitcnt vmcnt(0)" : "+v"(aq[0])::"memory");
              }
            }
            *(LDS_AS unsigned*)(smem + L_K + (8 + rt) * 2048 + l * 4) = aq[0];
          }
          if constexpr (s == ABS && n == 2) {
#pragma unroll
            for (int a = 0; a < 2; ++a) {
              const LDS_AS char* ab = (const LDS_AS char*)(smem + L_K + (8 + rt) * 2048 + a * 64);
              aq[a] = *(const LDS_AS unsigned*)(ab + (l & 15) * 4); bq[a] = *(const LDS_AS unsigned*)(ab + 128 + (l & 15) * 4);
              av[a] = *(const LDS_AS u32x4*)(ab + g * 16); bv[a] = *(const LDS_AS u32x4*)(ab + 128 + g * 16);
            }
          }
          if constexpr (s == ABS && n == 5) {
#pragma unroll
            for (int a = 0; a < 2; ++a) { b2[a] = f32x2{asf(bq[a]), asf(bq[a])}; asm volatile("" : "+v"(b2[a])); }
          }
          if constexpr (s == ABS + 1) {
            constexpr int k = (n + 4) % 6;
            if constexpr (k < 4) cs4[k & 1] = *(const LDS_AS f32x4*)(smem + L_CSL + k * 64 + g * 16);
            else cs4[k & 1][0] = *(const LDS_AS float*)(smem + L_CSL + k * 64 + (l & 15) * 4);
          }
          if constexpr ((s == ABS + 1 && n > 0) || (s == ABS + 2 && n == 0)) {
            constexpr int k = (n + 3) % 6;
            if constexpr (k < 4) {
#pragma unroll
              for (int a = 0; a < 2; ++a) acc[a][k] = __builtin_elementwise_fma(cs4[k & 1], f32x4{b2[a][0], b2[a][1], b2[a][0], b2[a][1]}, acc[a][k]);
            } else {
              f32x2 c2 = {cs4[k & 1][0], cs4[k & 1][0]};
              asm volatile("" : "+v"(c2));
#pragma unroll
              for (int a = 0; a < 2; ++a) acc[a][k] = __builtin_elementwise_fma(f32x4{c2[0], c2[1], c2[0], c2[1]}, __builtin_bit_cast(f32x4, bv[a]), acc[a][k]);
            }
          }
          if constexpr (IT == 0) {
            if constexpr (s >= KP0 && s < KP0 + 3 && (n & 1)) keep[3 * (s - KP0) + (n >> 1)] = frag0(SLOT(s - KP0 + 5), 3 * w + (n >> 1));
            if constexpr (s >= IMG && s < IMG + 3 && n == 4) st16(keep[s - IMG], rs, l * 16 + (s - IMG) * 1024, OFF_WI + (h * 8 + ((rot + 5) & 7)) * STAGE + w * 3072);
            if constexpr (s >= TS0 && s < TS0 + TSN && n == 4) {
              if (tstep == s && w < TW) touch(tdum, tsrc);
            }
            if constexpr (s == TST && n == 4) { if (w >= TW) touch(tdum, tsrc); }
          } else {
            if constexpr (s == 28 && n == 0) { POISON_BIG(1); }
            if constexpr (s == FSL && n == 4) { ld4h<0>(kv, rs, (l & 15) * 4, OFF_KEY + (bid & 15) * 128); row_load(rs, g, ffn_xoff(kg0, 0, w), fx); }
            if constexpr (s == FS0 && n == 0) {
              wait_vm<SINCE(FSL)>();
              asm volatile("" : "+v"(kv)::"memory");
              PINV4(fx);
              if (__builtin_expect(!__all(kv != POISON), 0)) {
                LDS_AS unsigned* kw = (LDS_AS unsigned*)(smem + L_SKEY + 40);
                if (w == 0) {
                  kv = key_wait(rs, T, l, 0, bid & 15, kv);
                } else {
                  while ((kv = __hip_atomic_load(kw, __ATOMIC_RELAXED, __HIP_MEMORY_SCOPE_WORKGROUP)) == POISON) __builtin_amdgcn_s_sleep(2);
                }
              }
              fkey = __builtin_amdgcn_readfirstlane(umax16(kv));
              if (w == 0) *(LDS_AS unsigned*)(smem + L_SKEY + 40) = min(fkey, POISON - 1);
              sel0 = key_row(fkey);
              aff = (int)(sel0 >> 8) == p;
              asm volatile("" : "=v"(sa), "=v"(sb));
              if (ffn_on) {
                if (__builtin_expect(((kg0 ^ fkey) & 0x780u) != 0, 0)) ld4x16(fx, rs, g * 16, ffn_xoff(fkey, 0, w));
                ffn_gu(gv, uv, fx, sa, sb);
              }
            }
            if constexpr (s == FS0 && n == 2) {
              if (ffn_on) {
                ffn_gu_check(rs, T, ffn_xoff(fkey, 0, w), l, gv, uv, fx, sa, sb);
                ffn_gu_store(smem, l, w, sa, sb);
                count_in(1);
              }
            }
            if constexpr (s == FS0D && n == FDG) {
              if (ffn_on && w < 4) {
                s16x4 da[8], db[8];
                ffn_d_read(smem, l, 2 * w, da);
                while (__hip_atomic_load(qcnt + 1, __ATOMIC_RELAXED, __HIP_MEMORY_SCOPE_WORKGROUP) < 8u) {}
                asm volatile("" ::: "memory");
                const f32x4 t = ffn_t_read(smem, l);
                ffn_d_read(smem, l, 2 * w + 1, db);
                u32x2 ha = ffn_h(t, l), hb = {dppu<0x118>(ha[0]), dppu<0x118>(ha[1])};
                f32x4 ya = {0.f, 0.f, 0.f, 0.f}, yb = ya;
#pragma unroll
                for (int i = 0; i < 8; ++i) {
                  ya = mfma16k16(da[i], __builtin_bit_cast(s16x4, ha), ya); yb = mfma16k16(db[i], __builtin_bit_cast(s16x4, hb), yb);
                  ha = u32x2{dppu<0x111>(ha[0]), dppu<0x111>(ha[1])}; hb = u32x2{dppu<0x111>(hb[0]), dppu<0x111>(hb[1])};
                }
                ffn_d_store(rs, l, 8 * w, 0, slice, ya + yb);
              }
            }
#define RED_LOAD(ld, l8)                                                                                                    \
  {                                                                                                                         \
    const unsigned G = xcd * 4 + bi - 28, po = OFF_PART + G * 32768;                                                        \
    ld(gv[0], rs, tid * 16, po); ld(gv[1], rs, tid * 16, po + 4096); ld(gv[2], rs, tid * 16, po + 8192); ld(gv[3], rs, tid * 16, po + 12288); \
    ld(uv[0], rs, tid * 16, po + 16384); ld(uv[1], rs, tid * 16, po + 20480); ld(uv[2], rs, tid * 16, po + 24576);        \
    l8(cxn, rs, (l & 3) * 16 + (l & 4) * 2, OFF_CA + (sel0 >> 7) * 2048 + G * 64);                                         \
    l8(cx1, rs, (l & 3) * 16 + (l & 4) * 2, OFF_CB + (sel0 >> 7) * 2048 + G * 64);                                          \
  }
            if constexpr (s == FS1 && n == 4) {
              asm volatile("" : "=v"(cxn), "=v"(cx1));
              if (!ffn_on && w < 4) RED_LOAD(ld16u, ld8h);
            }
            if constexpr (s == FS1S && n == 0) {
              if (!ffn_on && w < 4) {
                wait_vm<SINCE(FS1)>();
                PINV4(gv); PINV4(uv);
                asm volatile("" : "+v"(cxn), "+v"(cx1)::"memory");
                for (unsigned spins = 0; !__all(max(max(max(umax4(gv[0]), umax4(gv[1])), max(umax4(gv[2]), umax4(gv[3]))),
                                                    max(max(umax4(uv[0]), umax4(uv[1])), max(max(umax4(uv[2]), max(cxn[0], cxn[1])), max(cx1[0], cx1[1])))) != POISON) && !spin_fail(spins, rs, T);) {
                  RED_LOAD(ld16u, ld8h);
                  DRAIN();
                  PINV4(gv); PINV4(uv);
                  asm volatile("" : "+v"(cxn), "+v"(cx1)::"memory");
                }
#define F4(x) __builtin_bit_cast(f32x4, x)
                ((LDS_AS f32x4*)(smem + L_SFIN))[tid] = ((F4(gv[0]) + F4(gv[1])) + (F4(gv[2]) + F4(gv[3]))) + ((F4(uv[0]) + F4(uv[1])) + F4(uv[2]));
#undef F4
                count_in(2);
                if (w == 0) {
                  while (__hip_atomic_load(qcnt + 2, __ATOMIC_RELAXED, __HIP_MEMORY_SCOPE_WORKGROUP) < 4u) {}
                  asm volatile("" ::: "memory");
                  const u32x2 ov = red_final<4>(smem, l, fkey, cxn, cx1);
                  if (l < 8) st8(unpoison2(ov), rs, OFF_XNEW + ((xcd * 4 + bi - 28) * 8 + (l & 3) * 2 + (l >> 2)) * 8, 0);
                }
              }
            }
            if constexpr (s == FXA && n == 4) { DEF4(fx); if (w < 3) row_load(rs, g, XP_OFF(), fx); }
            if constexpr (s == FXB && n == 0) {
              if (w < 3) {
                const unsigned fa = bo[0][0] + SLOT(FXT) * STAGE + w * 2048;
                u32x4 f[8];
#pragma unroll
                for (int i = 0; i < 8; ++i) f[i] = *(const LDS_AS u32x4*)(size_t)(fa + (i >> 1) * 6144 + (i & 1) * 1024);
                wait_vm<SINCE(FXA)>();
                PINV4(fx);
                ffn_xwait<XSL>(rs, T, XP_OFF(), l, fx);
                f32x4 y0 = mfma16(f[0], fx[0], f32x4{0.f, 0.f, 0.f, 0.f}), y1 = mfma16(f[1], fx[0], f32x4{0.f, 0.f, 0.f, 0.f});
#pragma unroll
                for (int i = 1; i < 4; ++i) { y0 = mfma16(f[2 * i], fx[i], y0); y1 = mfma16(f[2 * i + 1], fx[i], y1); }
                if ((l & 14) == 0) st16(unpoison4(__builtin_bit_cast(u32x4, (l & 1) ? y1 : y0)), rs, g * 16 + (l & 1) * 512, OFF_FX + ((h * 6 + 2 * w) * 8 + p) * 64);
              }
            }
            if constexpr (s == FXL && n == 4) {
              asm volatile("" : "=v"(fx[0]), "=v"(fx[1]));
              if (aff && w < 4) { ld16h<0>(fx[0], rs, l * 16, FP_OFF(0)); ld16h<0>(fx[1], rs, l * 16, FP_OFF(1)); }
            }
            if constexpr (s == FXS && n == 2) {
              if (aff && w < 4) {
                wait_vm<0>();
                asm volatile("" : "+v"(fx[0]), "+v"(fx[1])::"memory");
                if (!FX_OK(2)) {
                  unsigned spins = 0;
                  do {
                    if (spin_fail(spins, rs, T)) break;
                    fx[0] = ld16(rs, l * 16, FP_OFF(0)); fx[1] = ld16(rs, l * 16, FP_OFF(1));
                  } while (!FX_OK(2));
                }
                if (w == 3) {
                  f32x2 nx[8];
                  unpack8v(fx[0], nx); unpack8v(fx[1], nx + 4);
                  const f32x2 sm = ((nx[0] + nx[1]) + (nx[2] + nx[3])) + ((nx[4] + nx[5]) + (nx[6] + nx[7]));
                  const float mean = sum64(hadd(sm)) * (1.0f / 1024.0f);
                  const f32x2 m2 = {mean, mean};
                  f32x2 q = {0.f, 0.f};
#pragma unroll
                  for (int j = 0; j < 8; ++j) { const f32x2 d = nx[j] - m2; q = __builtin_elementwise_fma(d, d, q); }
                  const float rd = ln_rd(sum64(hadd(q)) * (1.0f / 1024.0f));
                  if (l == 0) *(LDS_AS f32x2*)(smem + L_FXL + 384) = f32x2{rd, -rbf1(mean)};
                } else {
                  tile_sum(fx[0], L_FXL);
                }
                count_in(3);
              }
            }
          }
        };
        static_for<32>([&](auto sc_) __attribute__((always_inline)) {
          constexpr int s = decltype(sc_)::value;
          if constexpr (s > 0) PIN_ACC();
          if constexpr (IT == 1) wait_vm<vm_at(IT, s, 0) - vm_a(IT, s)>();
          pin_w<s % WA>(af[s % WA][0], af[s % WA][1]);
          SB();
          static_for<6>([&](auto nc_) __attribute__((always_inline)) {
            constexpr int n = decltype(nc_)::value;
            if constexpr (s == 0) { acc[0][n] = f32x4{0.f, 0.f, 0.f, 0.f}; acc[1][n] = acc[0][n]; }
            constexpr bool on = s < QL || n >= 2;
            if constexpr (on) acc[0][n] = n < 4 ? mfma16(bfr[n].v, af[s % WA][0], acc[0][n]) : mfma16(af[s % WA][0], bfr[n].v, acc[0][n]);
            SB();
            if constexpr (n == 0) { QKV_RDB(s, 5); }
            else if constexpr (s + 1 < 32 && (s + 1 < QL || n > 2)) QKV_RDB(s + 1, n - 1);
            SB();
            if constexpr (on) acc[1][n] = n < 4 ? mfma16(bfr[n].v, af[s % WA][1], acc[1][n]) : mfma16(af[s % WA][1], bfr[n].v, acc[1][n]);
            SB();
            if constexpr (s + 1 < 32 && (s + 1 < QL || n >= 2)) QKV_RDA(s + 1, n);
            SB();
            gap(sc_, nc_);
            SB();
          });
        });
        PIN_ACC();
        frag_t qw[32 - QL][2];
        static_for<32 - QL>([&](auto c_) __attribute__((always_inline)) {
          constexpr int s = QL + decltype(c_)::value;
          QKV_RDA(s, 0); QKV_RDB(s, 0); QKV_RDA(s, 1); QKV_RDB(s, 1);
          qw[s - QL][0] = bfr[0]; qw[s - QL][1] = bfr[1];
        });
        SB();
        const bool own = aff && (int)((sel0 >> 5) & 7) == rt;
        const bool m0 = !(sel0 & 16), mq = (l & 15) == (int)(sel0 & 15), mv = g == (int)((sel0 & 15) >> 2);
        auto patch_qk = [&](int n) __attribute__((always_inline)) {
          f32x4 t = *(const LDS_AS f32x4*)(smem + L_FXL + n * 64 + g * 16);
          const f32x4 c = *(const LDS_AS f32x4*)(smem + L_CSL + n * 64 + g * 16);
          const float nb = *(const LDS_AS float*)(smem + L_FXL + 388);
#pragma unroll
          for (int e = 0; e < 4; ++e) { t[e] = __builtin_fmaf(c[e], nb, t[e]); asm volatile("" : "+v"(t[e])); }
          acc[0][n] = (mq && m0) ? t : acc[0][n];
          acc[1][n] = (mq && !m0) ? t : acc[1][n];
        };
        if (own) {
          while (__hip_atomic_load(qcnt + 3, __ATOMIC_RELAXED, __HIP_MEMORY_SCOPE_WORKGROUP) < 4u) {}
          asm volatile("" ::: "memory");
          patch_qk(2); patch_qk(3);
          const f32x2 nab = *(const LDS_AS f32x2*)(smem + L_FXL + 384);
          const LDS_AS float* fl_ = (const LDS_AS float*)(smem + L_FXL + 256) + (l & 15);
          const LDS_AS float* cl_ = (const LDS_AS float*)(smem + L_CSL + 256) + (l & 15);
          const float t4 = __builtin_fmaf(cl_[0], nab[1], fl_[0]), t5 = __builtin_fmaf(cl_[16], nab[1], fl_[16]);
          const unsigned na = __builtin_bit_cast(unsigned, (float)nab[0]);
#pragma unroll
          for (int a = 0; a < 2; ++a) {
            const bool ma = a ? !m0 : m0;
            aq[a] = (mq && ma) ? na : aq[a];
#pragma unroll
            for (int e = 0; e < 4; ++e) {
              const bool me = mv && ma && e == (int)(sel0 & 3);
              acc[a][4][e] = me ? t4 : acc[a][4][e]; acc[a][5][e] = me ? t5 : acc[a][5][e];
              av[a][e] = me ? na : av[a][e];
            }
          }
        }
        constexpr float C2 = 1.4426950408889634f * 0.17677669529663687f;
#pragma unroll
        for (int a = 0; a < 2; ++a) {
          f32x2 a2 = {asf(aq[a]), asf(aq[a])};
          qs2[a] = f32x2{asf(aq[a]) * C2, asf(aq[a]) * C2};
          asm volatile("" : "+v"(a2), "+v"(qs2[a]));
          acc[a][2] *= f32x4{a2[0], a2[1], a2[0], a2[1]}; acc[a][3] *= f32x4{a2[0], a2[1], a2[0], a2[1]};
          acc[a][4] *= __builtin_bit_cast(f32x4, av[a]); acc[a][5] *= __builtin_bit_cast(f32x4, av[a]);
        }
        k0 = pack8(acc[0][2], acc[0][3]); k1 = pack8(acc[1][2], acc[1][3]); v0 = pack8(acc[0][4], acc[1][4]); v1 = pack8(acc[0][5], acc[1][5]);
        const unsigned kvo = OFF_KV + IT * (8u << 20) + (p * 32 + h) * 32768 + rt * 4096;
        if (p < 7) { st16(k0, rs, l * 16, kvo); st16(k1, rs, l * 16 + 1024, kvo); st16(v0, rs, l * 16 + 2048, kvo); st16(v1, rs, l * 16 + 3072, kvo); }
        char* ownp = smem + (8 + rt) * 2048 + l * 16;
        *(u32x4*)(ownp + L_K) = k0;
        if (rt < 7) *(u32x4*)(ownp + L_K + 1024) = k1;
        *(u32x4*)(ownp + L_V) = v0; *(u32x4*)(ownp + L_V + 1024) = v1;
        SB();
        static_for<32 - QL>([&](auto c_) __attribute__((always_inline)) {
          constexpr int s = QL + decltype(c_)::value;
#pragma unroll
          for (int n = 0; n < 2; ++n) { acc[0][n] = mfma16(qw[s - QL][n].v, af[s % WA][0], acc[0][n]); acc[1][n] = mfma16(qw[s - QL][n].v, af[s % WA][1], acc[1][n]); }
        });
        if (own) { patch_qk(0); patch_qk(1); }
      };
      qkv_loop(it_c);
    }
#undef A_LOAD1
#undef QKV_RDA
#undef QKV_RDB
#undef QKV_BO
    {
      IDS();
      const GAS bf16 *pwo, *pwg, *pwu;
      asm volatile("s_load_dwordx2 %0, %3, 24\n\ts_load_dwordx2 %1, %3, 40\n\ts_load_dwordx2 %2, %3, 48" : "=&s"(pwo), "=&s"(pwg), "=&s"(pwu) : "s"(ka));
      const int ql = l & 15;
      const f32x4 z4 = {0.f, 0.f, 0.f, 0.f}, ninf = {-INFINITY, -INFINITY, -INFINITY, -INFINITY};
      LDS_AS unsigned* rdy = (LDS_AS unsigned*)(smem + L_RDY);
      const unsigned rtag = T[0] + it;
      u32x4 qf[2];
      f32x4 cn[2], oa[2][2], sum[2], S[2][2];
      unsigned one2 = 0x3F803F80u;
      asm volatile("" : "+v"(one2));
      const u32x4 ones = {one2, one2, one2, one2};
      auto ex8 = [&](f32x4 a, f32x4 b) __attribute__((always_inline)) {
        return u32x4{pack2(__builtin_amdgcn_exp2f(a[0]), __builtin_amdgcn_exp2f(a[1])), pack2(__builtin_amdgcn_exp2f(a[2]), __builtin_amdgcn_exp2f(a[3])),
                     pack2(__builtin_amdgcn_exp2f(b[0]), __builtin_amdgcn_exp2f(b[1])), pack2(__builtin_amdgcn_exp2f(b[2]), __builtin_amdgcn_exp2f(b[3]))};
      };
      {
#pragma unroll
        for (int a = 0; a < 2; ++a) {
          const f32x4 c4 = {qs2[a][0], qs2[a][1], qs2[a][0], qs2[a][1]};
          qf[a] = pack8(acc[a][0] * c4, acc[a][1] * c4);
        }
        f32x4 mk;
#pragma unroll
        for (int e = 0; e < 4; ++e) mk[e] = (4 * g + e <= ql) ? 0.f : -INFINITY;
        const f32x4 d0 = mfma16(k0, qf[0], mk), d1 = mfma16(k1, qf[1], mk), d2 = mfma16(k0, qf[1], z4);
        const float m0 = -max_x32(max_x16(fmaxf(fmaxf(d0[0], d0[1]), fmaxf(d0[2], d0[3]))));
        const float m1 = -max_x32(max_x16(fmaxf(fmaxf(d1[0], d1[1]), fmaxf(d1[2], d1[3]))));
        cn[0] = f32x4{m0, m0, m0, m0}; cn[1] = f32x4{m1, m1, m1, m1};
        asm volatile("" : "+v"(cn[0]), "+v"(cn[1]));
        const f32x4 e0 = d0 + cn[0];
        const u32x4 p0 = {pack2(__builtin_amdgcn_exp2f(e0[0]), __builtin_amdgcn_exp2f(e0[1])), pack2(__builtin_amdgcn_exp2f(e0[2]), __builtin_amdgcn_exp2f(e0[3])), 0u, 0u};
        const u32x4 p1 = ex8(d2 + cn[1], d1 + cn[1]);
        oa[0][0] = mfma16(v0, p0, z4); oa[0][1] = mfma16(v1, p0, z4); sum[0] = mfma16(ones, p0, z4);
        oa[1][0] = mfma16(v0, p1, z4); oa[1][1] = mfma16(v1, p1, z4); sum[1] = mfma16(ones, p1, z4);
      }
      const char* kb_ = smem + L_K + l * 16;
      const char* vb_ = smem + L_V + l * 16;
      const unsigned wov = (l >> 3) * 2048 + (((((l & 7) >> 1) ^ ((l >> 4) & 3)) * 2 + (l & 1)) << 4), fnv = (l >> 1) * (F_ * 2) + (l & 1) * 16;
      const unsigned pp = __builtin_amdgcn_readfirstlane(p > 0 ? 32 : 0), pvo = OFF_KV + it * (8u << 20) + (p * 32 + h - pp) * 32768;
      const unsigned pfo = __builtin_amdgcn_readfirstlane(F_KV + (it * 256 + p * 32 + h - pp) * 128);
      asm volatile("" : "+v"(oa[0][0]), "+v"(oa[0][1]), "+v"(oa[1][0]), "+v"(oa[1][1]), "+v"(sum[0]), "+v"(sum[1]));
      DRAIN();
      asm volatile("" : "+v"(tdum));
      if (l == 0) st8(T, rs, rt * 8, F_KV + (it * 256 + p * 32 + h) * 128);
      BAR();
      asm volatile("" : "+s"(pwo), "+s"(pwg), "+s"(pwu));
      WO_PIECE(15); WO_PIECE(14);
#define LDS16(a_) (*(const LDS_AS u32x4*)(size_t)(a_))
      u32x4 kf[2], vf[2];
      int nw = 0;
      u32x4 p0, p1;
      float e0, e1;
#define EE(v, i) { e0 = __builtin_amdgcn_exp2f(v[i]); e1 = __builtin_amdgcn_exp2f(v[i + 1]); pin2s(e0, e1); SB(); }
#define CV(P, i) { unsigned t_ = pack2(e0, e1); pin1(t_); P[i] = t_; SB(); }
      auto body = [&](unsigned ad, auto dk_) __attribute__((always_inline)) {
        constexpr int dk = decltype(dk_)::value;
        const f32x4 a0 = S[1][0], a1 = S[1][1];
        sum[0] = mfma16(ones, p0, sum[0]); SB();
        EE(a0, 0);
        lds_wait<2>(kf[0], kf[1]);
        S[0][0] = mfma16(kf[0], qf[0], cn[0]); SB();
        CV(p1, 0); EE(a0, 2);
        S[0][1] = mfma16(kf[1], qf[0], cn[0]); SB();
        CV(p1, 1); EE(a1, 0);
        lds_wait<0>(vf[0], vf[1]);
        oa[0][0] = mfma16(vf[0], p0, oa[0][0]); SB();
        CV(p1, 2); EE(a1, 2);
        oa[0][1] = mfma16(vf[1], p0, oa[0][1]); SB();
        CV(p1, 3);
        S[1][0] = mfma16(kf[0], qf[1], cn[1]); SB();
        EE(S[0][0], 0);
        S[1][1] = mfma16(kf[1], qf[1], cn[1]); SB();
        lds2<2048 + dk>(kf[0], kf[1], ad); SB();
        CV(p0, 0); EE(S[0][0], 2);
        oa[1][0] = mfma16(vf[0], p1, oa[1][0]); SB();
        CV(p0, 1); EE(S[0][1], 0);
        oa[1][1] = mfma16(vf[1], p1, oa[1][1]); SB();
        lds2<2048 + L_V>(vf[0], vf[1], ad); SB();
        CV(p0, 2); EE(S[0][1], 2);
        sum[1] = mfma16(ones, p1, sum[1]); SB();
        CV(p0, 3);
      };
      auto last = [&]() __attribute__((always_inline)) {
        sum[0] = mfma16(ones, p0, sum[0]); SB();
        EE(S[1][0], 0);
        lds_wait<0>(vf[0], vf[1]);
        pin2(kf[0], kf[1]);
        oa[0][0] = mfma16(vf[0], p0, oa[0][0]); SB();
        CV(p1, 0); EE(S[1][0], 2);
        oa[0][1] = mfma16(vf[1], p0, oa[0][1]); SB();
        CV(p1, 1); EE(S[1][1], 0);
        const float f0 = __builtin_amdgcn_exp2f(S[1][1][2]), f1 = __builtin_amdgcn_exp2f(S[1][1][3]);
        p1[2] = pack2(e0, e1); p1[3] = pack2(f0, f1);
        oa[1][0] = mfma16(vf[0], p1, oa[1][0]); oa[1][1] = mfma16(vf[1], p1, oa[1][1]); sum[1] = mfma16(ones, p1, sum[1]);
      };
      auto piece = [&]() __attribute__((always_inline)) {
        if constexpr (it == 1) { WO_PIECE(13 - nw); }
        else if (ffn_on) { FFN_DMA(nw); }
        ++nw;
      };
      if (rt > 0) {
        unsigned ad = lbase + l * 16 + (6 + rt) * 2048;
        lds2<2048>(kf[0], kf[1], ad); lds2<2048 + L_V>(vf[0], vf[1], ad);
        lds_wait<2>(kf[0], kf[1]);
        S[0][0] = mfma16(kf[0], qf[0], cn[0]); S[0][1] = mfma16(kf[1], qf[0], cn[0]); S[1][0] = mfma16(kf[0], qf[1], cn[1]); S[1][1] = mfma16(kf[1], qf[1], cn[1]);
        SB();
        lds_wait<0>(vf[0], vf[1]);
        lds2<0>(kf[0], kf[1], ad);
        p0 = ex8(S[0][0], S[0][1]);
        lds_wait<0>(kf[0], kf[1]);
#pragma unroll 1
        for (int i = rt - 1; i > 0; --i) {
          ad -= 2048;
          body(ad, std::integral_constant<int, -2048>{});
          piece();
          SB();
        }
        piece();
        last();
      }
      {
        constexpr int FW = it ? FW1 : FW0, FN = 8 / FW;
        if (w < FW) {
          const int g0 = FN * rt;
          poll_flags(rs, (l & (FN - 1)) * 8, pfo + g0 * 8, T);
#pragma unroll
          for (int j = 0; j < FN; ++j) {
            const unsigned d = lbase + (g0 + j) * 2048, o = pvo + (g0 + j) * 4096;
            dma16s<0>(rs, l * 16, o, d + L_K); dma16s<1024>(rs, l * 16, o, d + L_K);
            dma16s<0>(rs, l * 16, o + 2048, d + L_V); dma16s<1024>(rs, l * 16, o + 2048, d + L_V);
          }
        }
        if (p == 0) { cn[0] = ninf; cn[1] = ninf; }
        f32x4 mk;
#pragma unroll
        for (int e = 0; e < 4; ++e) mk[e] = (4 * g + e > ql) ? 0.f : -INFINITY;
        const f32x4 cm0 = cn[0] + mk, cm1 = cn[1] + mk;
        if (w < FW) {
          if constexpr (FN > 1) { wait_vm<2 * FN>(); if (l < FN / 2) rdy[FN * rt + l] = rtag; }
          DRAIN();
          if (l < FN) rdy[FN * rt + l] = rtag;
        }
        if constexpr (it == 1) { WO_PIECE(6); WO_PIECE(5); WO_PIECE(4); }
        else { WO_PIECE(13); WO_PIECE(12); if (ffn_on) FFN_DMA(7); }

        int have = rt - 1;
        auto need = [&](int gq) __attribute__((always_inline)) {
          if (__builtin_expect(gq > have && gq < 8, 0)) {
            do {
              const unsigned t = __hip_atomic_load(rdy + (l & 7), __ATOMIC_RELAXED, __HIP_MEMORY_SCOPE_WORKGROUP);
              have = rt - 1 + __builtin_ctz(~((unsigned)__builtin_amdgcn_ballot_w64(t == rtag) & 0xffu) >> rt);
            } while (gq > have);
            asm volatile("" ::: "memory");
          }
        };
        unsigned ad = lbase + l * 16 + rt * 2048;
        need(rt);
        lds2<0>(kf[0], kf[1], ad); lds2<L_V>(vf[0], vf[1], ad);
        lds_wait<2>(kf[0], kf[1]);
        S[0][0] = mfma16(kf[0], qf[0], cm0); S[0][1] = mfma16(kf[1], qf[0], cn[0]); S[1][0] = ninf; S[1][1] = mfma16(kf[1], qf[1], cm1);
        SB();
        lds_wait<0>(vf[0], vf[1]);
        need(rt + 1);
        lds2<2048>(kf[0], kf[1], ad);
        p0 = ex8(S[0][0], S[0][1]);
        lds_wait<0>(kf[0], kf[1]);
#pragma unroll 1
        for (int gq = rt + 1; gq < 8; ++gq) {
          need(gq + 1);
          body(ad, std::integral_constant<int, 2048>{});
          piece();
          SB();
          ad += 2048;
        }
        last();
      }
      {
        float big = 0.f;
#pragma unroll
        for (int a = 0; a < 2; ++a)
#pragma unroll
          for (int d = 0; d < 2; ++d)
            big = __builtin_elementwise_maximum(__builtin_elementwise_maximum(big, __builtin_elementwise_maximum(fabsf(oa[a][d][0]), fabsf(oa[a][d][1]))),
                                                __builtin_elementwise_maximum(fabsf(oa[a][d][2]), fabsf(oa[a][d][3])));
        bool ok = sum[0][0] >= 0.5f && sum[0][0] < 0x1p64f && sum[1][0] >= 0.5f && sum[1][0] < 0x1p64f && big < INFINITY;
        if (!__all(ok)) {
#pragma unroll
          for (int a = 0; a < 2; ++a) {
            const int qs = 2 * rt + a, qq = 256 + 16 * qs + ql, klo = max(qq - 255, p > 0 ? 0 : 256);
            auto score = [&](int t) __attribute__((always_inline)) {
              f32x4 s = mfma16(t == 31 ? k1 : *(const u32x4*)(kb_ + t * 1024), qf[a], z4);
#pragma unroll
              for (int e = 0; e < 4; ++e) { const int kk = 16 * t + 4 * g + e; s[e] = (kk <= qq && kk >= klo) ? s[e] : -INFINITY; }
              return s;
            };
            float mx = -INFINITY;
#pragma unroll 1
            for (int t = qs; t <= qs + 16; ++t) { const f32x4 s = score(t); mx = fmaxf(fmaxf(mx, fmaxf(s[0], s[1])), fmaxf(s[2], s[3])); }
            mx = max_x32(max_x16(mx));
            oa[a][0] = z4; oa[a][1] = z4; sum[a] = z4;
#pragma unroll 1
            for (int G = qs >> 1; G <= (qs + 16) >> 1; ++G) {
              const f32x4 s0 = score(2 * G), s1 = score(2 * G + 1);
              f32x4 e0, e1;
#pragma unroll
              for (int e = 0; e < 4; ++e) { e0[e] = __builtin_amdgcn_exp2f(s0[e] - mx); e1[e] = __builtin_amdgcn_exp2f(s1[e] - mx); }
              const u32x4 pf = pack8(e0, e1);
              oa[a][0] = mfma16(*(const u32x4*)(vb_ + G * 2048), pf, oa[a][0]);
              oa[a][1] = mfma16(*(const u32x4*)(vb_ + G * 2048 + 1024), pf, oa[a][1]);
              sum[a] = mfma16(ones, pf, sum[a]);
            }
          }
        }
      }
#pragma unroll
      for (int a = 0; a < 2; ++a) {
        const float inv = __builtin_amdgcn_rcpf(sum[a][0]);
        oa[a][0] *= inv; oa[a][1] *= inv;
      }
      const unsigned oo = OFF_O + it * (4u << 20) + ((p * 8 + rt) * 64 + h * 2) * 1024;
      st16(pack8(oa[0][0], oa[0][1]), rs, l * 16, oo);
      st16(pack8(oa[1][0], oa[1][1]), rs, l * 16 + 1024, oo);
      DRAIN();
      BAR();
      if (tid == 0) st8(T, rs, h * 8, F_O + (it * 8 + p) * 256);
      if constexpr (it == 0) {
        DEF4(gv); DEF4(uv);
        if (ffn_on) {
          const unsigned ro = 65536 + w * 1024 + (4 * g + ((l & 15) >> 2)) * 32 + (l & 3) * 8;
#pragma unroll
          for (int i = 0; i < 4; ++i) {
            frag_t f;
            f.hf.a = trread(smem, ro + i * 16384); f.hf.b = trread(smem, ro + i * 16384 + 512);
            gv[i] = f.v;
            f.hf.a = trread(smem, ro + i * 16384 + 8192); f.hf.b = trread(smem, ro + i * 16384 + 8704);
            uv[i] = f.v;
          }
        }
        PINV4(gv); PINV4(uv);
        {
          ARGP(px, 0);
          const unsigned xo = ((rb * 4 + (w & 3)) * 32 + (w >> 2) * 16) * 2048 + ct * 128, xw = (l >> 3) * 2048 + (((l & 7) ^ g) << 4);
          dma16(RSRC(px, 2048 * 1024 * 2), xw, xo, lbase + w * 4096); dma16(RSRC(px, 2048 * 1024 * 2), xw ^ 64, xo + 16384, lbase + w * 4096 + 1024);
        }
      }
      WO_PIECE(3); WO_PIECE(2); WO_PIECE(1); WO_PIECE(0);
      if constexpr (it == 0) { WO_PIECE(11); WO_PIECE(10); WO_PIECE(9); WO_PIECE(8); WO_PIECE(7); WO_PIECE(6); WO_PIECE(5); WO_PIECE(4); }
    }
    IDS();
    ARGP(pwr, 4); ARGP(pwd, 7); ARGP(pout, 8);
    const int kh = w >> 2, rt2 = rb * 4 + (w & 3);
    const unsigned R = rt2 * 32 + kh * 16 + (l & 15);
    const unsigned to = (rt2 * 64 + ct * 4 + kh) * 1024 + l * 16;
    const unsigned rmo = ct * 64 + ((g & 1) ? 12 + 4 * g : 4 * g);
    f32x2 v[8], wv[8];
    float wsum;
    {
      u32x4 wa[2], wrp[2], res[2];
#pragma unroll
      for (int j = 0; j < 2; ++j) {
        wrp[j] = *(const GAS u32x4*)(pwr + rmo + 32 * j);
        wa[j] = *(const GAS u32x4*)(pwr + l * 8 + j * 512);
      }
      {
        f32x2 t[8];
        unpack8v(wa[0], t); unpack8v(wa[1], t + 4);
        const f32x2 s = ((t[0] + t[1]) + (t[2] + t[3])) + ((t[4] + t[5]) + (t[6] + t[7]));
        wsum = sum64(hadd(s));
        unpack8v(trade16(wrp[0]), wv); unpack8v(trade16(wrp[1]), wv + 4);
        asm volatile("" : "+v"(wsum), "+v"(wv[0]), "+v"(wv[1]), "+v"(wv[2]), "+v"(wv[3]), "+v"(wv[4]), "+v"(wv[5]), "+v"(wv[6]), "+v"(wv[7]));
      }
      unsigned bt[4];
#pragma unroll
      for (int n = 0; n < 4; ++n) {
        bt[n] = lbase + 32768 + 65536 + kh * 4096 + (4 * g + ((l & 15) >> 2)) * 128 + (l & 3) * 8 + ((n ^ (2 * (g & 1) + ((l & 15) >> 3))) << 5);
        asm volatile("" : "+v"(bt[n]));
      }
      if (w == 0) poll_flags(rs, (l & 31) * 8, F_O + (it * 8 + (rb >> 1)) * 256, T);
      else if (it == 0 && w < 4) poll_flags2(rs, w < 3 ? F_XN + (((w - 1) * 4 + g) * RX + bi % RX) * 256 + (l & 15) * 16 : F_KV + (((l >> 2) & 7) * 32 + h) * 128 + (l & 3) * 16, T);
      asm volatile("s_barrier" ::: "memory");
      const unsigned abase = OFF_O + it * (4u << 20) + (rt2 * 64 + 32 * kh) * 1024;
      const unsigned av0 = l * 16 + kh * 1024, av1 = l * 16 + (kh ^ 1) * 1024;
      u32x4 af[WB][2];
#define O_LOAD(s_) { ld16h<(wo_k(it, s_) & 1) * 2048>(af[(s_) % WB][0], rs, av0, abase + (wo_k(it, s_) >> 1) * 4096); ld16h<(wo_k(it, s_) & 1) * 2048>(af[(s_) % WB][1], rs, av1, abase + (wo_k(it, s_) >> 1) * 4096); }
      static_assert(WR == 4 && WB == 8, "wo_at");
      O_LOAD(0); O_LOAD(1); O_LOAD(2); O_LOAD(3);
      if constexpr (it == 1) { ld16h<0>(res[0], rs, g * 16, OFF_XNEW + ct * 128); ld16h<64>(res[1], rs, g * 16, OFF_XNEW + ct * 128); }
      SB();
      f32x4 ac[2][4];
#pragma unroll
      for (int a = 0; a < 2; ++a)
#pragma unroll
        for (int n = 0; n < 4; ++n) ac[a][n] = f32x4{0.f, 0.f, 0.f, 0.f};
      u32x4 bfr[2][4];
#define WO_RD(buf, s, n)                                                                             \
  {                                                                                                  \
    const unsigned o_ = bt[n] + (wo_k(it, s) & 7) * 8192;                                             \
    frag_t f_;                                                                                       \
    f_.hf.a = trabs(o_); f_.hf.b = trabs(o_ + 2048);                                                 \
    bfr[buf][n] = f_.v;                                                                              \
  }
#pragma unroll
      for (int n = 0; n < 4; ++n) WO_RD(0, 0, n);
      static_for<16>([&](auto sc_) __attribute__((always_inline)) {
        constexpr int s = decltype(sc_)::value, cur = s & 1;
        pin_ac(ac);
        if (s == (it == 0 ? 3 : 11)) asm volatile("s_barrier" ::: "memory");
        if constexpr (s < 15 && ((wo_k(it, s) ^ wo_k(it, s + 1)) & 8)) {
#pragma unroll
          for (int n = 0; n < 4; ++n) { bt[n] += (wo_k(it, s) & 8) ? -65536 : 65536; pin1(bt[n]); }
        }
        if constexpr (s < WB - WR) O_LOAD(WR + 2 * s);
        if constexpr (it == 0 && s == 8) {
          asm volatile("s_barrier" ::: "memory");
          WD_DMA();
        }
        wait_vm<wo_at(it, s) - wo_pos(it, s)>();
        pin2(af[s % WB][0], af[s % WB][1]);
        if constexpr (it == 1 && s == 9) pin2(res[0], res[1]);
        SB();
#pragma unroll
        for (int n = 0; n < 4; ++n) {
          ac[0][n] = mfma16(bfr[cur][n], af[s % WB][0], ac[0][n]);
          ac[1][n] = mfma16(bfr[cur][n], af[s % WB][1], ac[1][n]);
          if (s + 1 < 16) WO_RD(cur ^ 1, s + 1, n);
          if (it == 0 && s == 8) {
            const u32x2 t = *(const LDS_AS u32x2*)(smem + w * 4096 + (l & 15) * 128 + (((2 * n + (g >> 1)) ^ ((l & 15) >> 1)) << 4) + (g & 1) * 8);
            res[n >> 1][2 * (n & 1)] = t[0]; res[n >> 1][2 * (n & 1) + 1] = t[1];
          }
          if (s == 9) {
            if (it == 1 && n == 0) { res[0] = R == sel0 ? res[0] : x2p[0]; res[1] = R == sel0 ? res[1] : x2p[1]; }

            v[2 * n] = f32x2{lo16(res[n >> 1][2 * (n & 1)]), hi16(res[n >> 1][2 * (n & 1)])};
            v[2 * n + 1] = f32x2{lo16(res[n >> 1][2 * (n & 1) + 1]), hi16(res[n >> 1][2 * (n & 1) + 1])};
            pin2f(v[2 * n], v[2 * n + 1]);
          }
          SB();
        }
        if constexpr (s < WB - WR) { O_LOAD(WR + 2 * s + 1); }
        else if constexpr (s + WB < 16) { O_LOAD(s + WB); }
        SB();
      });
#undef WO_RD
#undef O_LOAD
#define XCH_AT(v) ((v) == 7 ? 155648 : (v) * 4096)
#pragma unroll
      for (int n = 0; n < 4; ++n) *(f32x4*)(smem + XCH_AT(w) + n * 1024 + l * 16) = ac[1][n];
#pragma unroll
      for (int n = 0; n < 4; ++n) { v[2 * n] += f32x2{ac[0][n][0], ac[0][n][1]}; v[2 * n + 1] += f32x2{ac[0][n][2], ac[0][n][3]}; }
      BAR();
#pragma unroll
      for (int n = 0; n < 4; ++n) {
        const f32x4 o = *(const f32x4*)(smem + XCH_AT(w ^ 4) + n * 1024 + l * 16);
        v[2 * n] += f32x2{o[0], o[1]}; v[2 * n + 1] += f32x2{o[2], o[3]};
      }
    }
    {
      const f32x2 s = ((v[0] + v[1]) + (v[2] + v[3])) + ((v[4] + v[5]) + (v[6] + v[7]));
      f32x2 dp = v[0] * wv[0], q = v[0] * v[0];
#pragma unroll
      for (int j = 1; j < 8; ++j) { dp = __builtin_elementwise_fma(v[j], wv[j], dp); q = __builtin_elementwise_fma(v[j], v[j], q); }
      float mt = hadd(s), qq = hadd(q), dot = hadd(dp);
      add_g3(mt, qq, dot);
      const unsigned so = OFF_STAT + it * 524288 + rb * 32768, lrow = R & 127;
      if (g == 0) st16(unpoison4(__builtin_bit_cast(u32x4, f32x4{mt, qq, dot, 0.f})), rs, ct * 2048 + lrow * 16, so);
      SB();
      if (it == 0) { st16(pack8v(v), rs, to, OFF_XN + (4u << 20)); st16(pack8v(v + 4), rs, to + 2048, OFF_XN + (4u << 20)); }
      SB();
      if (it == 0) wait_vm<0>();
      if constexpr (it == 0) {
        volatile LDS_AS u32x2* in = (volatile LDS_AS u32x2*)(smem + L_XC);
        if (l == 0) in[w] = T;
        const u32x2 t = in[l & 7];
        if (__all(t[0] == T[0] && t[1] == T[1]) && l < RX) st8(T, rs, l * 256 + ((rb & 1) * 16 + ct) * 8, F_X1 + (it * 8 + (rb >> 1)) * RX * 256);
      }
      if (it == 1) { if (ffn_on) { WD_DMA(); } }
      if (it == 0) {
#pragma unroll
        for (int j = 0; j < 9; ++j) *(u32x4*)(smem + L_RING + (j / 3) * STAGE + (w * 3 + j % 3) * 1024 + l * 16) = keep[j];
      }
      __builtin_amdgcn_s_sleep(it == 0 ? SDI0 : SDIR);
      char* const skey = smem + (it == 0 ? L_SKEY : L_XC);
      u32x4 sp[4];
      ld16h<0>(sp[0], rs, g * 8192 + lrow * 16, so); ld16h<2048>(sp[1], rs, g * 8192 + lrow * 16, so);
      ld16h<0>(sp[2], rs, g * 8192 + lrow * 16, so + 4096); ld16h<2048>(sp[3], rs, g * 8192 + lrow * 16, so + 4096);
      u32x2 xf;
      unsigned k_;
      asm volatile("" : "=v"(xf), "=v"(k_));
      if (it == 0 && w == 0) { ld8h(xf, rs, (l & 31) * 8, F_X1 + (p * RX + (xcd >> 1) % RX) * 256); ld4h<0>(k_, rs, (l & 15) * 4, OFF_KEY + (bid & 15) * 128); }
      DRAIN();
      asm volatile("" : "+v"(xf), "+v"(k_)::"memory");
      PINV4(sp);
      const RowFin r = fin_key(rs, T, g * 8192 + lrow * 16, so, skey, l, w, R, wsum, sp);
      if constexpr (it == 0) {
        if (w == 0) {
          const unsigned need = 0x30003u << (((PROT() + 5) & 7) * 2);
          unsigned m = (unsigned)__builtin_amdgcn_ballot_w64(xf[0] == T[0] && xf[1] == T[1]);
          for (unsigned spins = 0; (m & need) != need;) {
            if (spin_fail(spins, rs, T)) { m = ~0u; break; }
            const u32x2 f = ld8(rs, F_X1 + (p * RX + (xcd >> 1) % RX) * 256 + (l & 31) * 8, 0);
            k_ = key_load(rs, l, 0, bid & 15);
            m = (unsigned)__builtin_amdgcn_ballot_w64(f[0] == T[0] && f[1] == T[1]);
          }
          k_ = umax16(k_ == POISON ? 0u : k_);
          if (l == 0) *(LDS_AS u32x4*)(skey + 32) = u32x4{m, k_, POISON, 0u};
        }
        if (tid == 192) *(LDS_AS u32x4*)(smem + L_QC) = u32x4{0u, 0u, 0u, 0u};
      }
      BAR();
      if constexpr (it == 0) {
        PRE_LOAD(1, PROT() + 5, 0, 4);
        const u32x2 mk = *(const LDS_AS u32x2*)(skey + 32);
        xm0 = __builtin_amdgcn_readfirstlane(mk[0]); kg0 = __builtin_amdgcn_readfirstlane(mk[1]);
      }
      const unsigned own = fin_cand(rs, skey, it, g, rb, ct, r, v);
      if constexpr (it == 0) kg0 = max(kg0, (unsigned)__builtin_amdgcn_readfirstlane(own));
      fin_x2(r, v);
      if constexpr (it == 0) {
        x2p[0] = pack8v(v); x2p[1] = pack8v(v + 4);
        asm volatile("" : "+v"(x2p[0]), "+v"(x2p[1]));
        if ((w & 3) * 2 + (w >> 2) == ct >> 1) {
          const float f = 1.0f + r.rd, rd2 = ln_rd(r.var * f * f), mb2 = rbf1(__builtin_fmaf(r.mean - r.mb, r.rd, r.mean));
          if (g < 2 && ((l >> 3) & 1) == (ct & 1))
            st4(min(__builtin_bit_cast(unsigned, g == 0 ? f * rd2 : -__builtin_fmaf(r.mb, r.rd, mb2) * __builtin_amdgcn_rcpf(f)), POISON - 1), rs, OFF_AB + 16384 + R * 4 + g * 8192, 0);
        }
      }
    }
    if (it == 1) {
      const u32x4 op0 = trade16(pack8v(v)), op1 = trade16(pack8v(v + 4));
#define OUT_STORE()                                                                                 \
  if (R != key_row(key)) {                                                                          \
    __builtin_amdgcn_raw_buffer_store_b128(op0, rout, (R * 1024 + rmo) * 2, 0, SC1);                  \
    __builtin_amdgcn_raw_buffer_store_b128(op1, rout, (R * 1024 + rmo + 32) * 2, 0, SC1);             \
  }
      u32x4 xv[4];
      unsigned guess = 0;
      {
        __builtin_amdgcn_s_sleep(GSL);
        const unsigned k_ = key_load(rs, l, 1, bid & 15);
        guess = ffn_xoff(__builtin_amdgcn_readfirstlane(umax16(k_ == POISON ? 0u : k_)), 1, w);
        row_load(rs, g, guess, xv);
      }
      if (w == 0) {
        const unsigned k_ = key_wait(rs, T, l, 1, bid & 15, key_load(rs, l, 1, bid & 15));
        if (l == 0) *(LDS_AS unsigned*)(smem + L_SKEY + 32) = k_;
      }
      DRAIN();
      PINV4(xv);
      BAR();
      const unsigned key = __builtin_amdgcn_readfirstlane(*(LDS_AS unsigned*)(smem + L_SKEY + 32));
      if (ffn_on) {
        f32x4 ga, ua;
        s16x4 dfr[8];
        ffn_d_read(smem, l, w, dfr);
        if (guess != ffn_xoff(key, 1, w)) ld4x16(xv, rs, g * 16, ffn_xoff(key, 1, w));
        ffn_gu(gv, uv, xv, ga, ua);
        ffn_gu_check(rs, T, ffn_xoff(key, 1, w), l, gv, uv, xv, ga, ua);
        ffn_gu_store(smem, l, w, ga, ua);
        BAR();
        f32x4 ya = {0.f, 0.f, 0.f, 0.f}, yb = ya;
        u32x2 hh = ffn_h(ffn_t_read(smem, l), l);
        ffn_d_mma(dfr, hh, ya, yb);
        if ((l & 15) < 8) ffn_d_store(rs, l, 4 * w, 1, slice, ya + yb);
        SB();
        OUT_STORE();
      } else {
        OUT_STORE();
        const unsigned G = xcd * 4 + bi - 28;
        if (G == 0 && tid == 0) st8(T, rs, F_EP, 0);
        u32x4 pv[4];
        u32x2 cxn, cx1;
        __builtin_amdgcn_s_sleep(RSL);
        red_load(rs, tid, 1, G, key, pv, cxn, cx1);
        red_sum(rs, smem, T, tid, w, 1, G, key, pv, cxn, cx1);
        BAR();
        if (w == 0) {
          const u32x2 ov = red_final<8>(smem, l, key, cxn, cx1);
          if (l < 8) __builtin_amdgcn_raw_buffer_store_b64(ov, rout, (key_row(key) * 1024 + G * 32 + l * 4) * 2, 0, 0);
        }
      }
#undef OUT_STORE
    }
  };
  layer(std::integral_constant<int, 0>{});
  layer(std::integral_constant<int, 1>{});
}

extern "C" void kernel_launch(void* const* d_in, const int* in_sizes, int n_in, void* d_out, int out_size,
                              void* d_ws, size_t ws_size, hipStream_t stream) {
  (void)hipFuncSetAttribute((const void*)mega_kernel, hipFuncAttributeMaxDynamicSharedMemorySize, LDS_BYTES);
  mega_kernel<<<256, 512, LDS_BYTES, stream>>>((const bf16*)d_in[0], (char*)d_ws, (const bf16*)d_in[1], (const bf16*)d_in[2], (const bf16*)d_in[4],
                                               (const bf16*)d_in[5], (const bf16*)d_in[6], (const bf16*)d_in[7], (bf16*)d_out);
}
```

```cpp
#include <hip/hip_runtime.h>
#include <stdint.h>
#include <utility>

typedef __bf16 bf16;
typedef __bf16 bf16x8 __attribute__((ext_vector_type(8)));
typedef __bf16 bf16x2 __attribute__((ext_vector_type(2)));
typedef float f32x4 __attribute__((ext_vector_type(4)));
typedef float f32x2 __attribute__((ext_vector_type(2)));
typedef short s16x4 __attribute__((ext_vector_type(4)));
typedef unsigned int u32x4 __attribute__((ext_vector_type(4)));
typedef unsigned int u32x2 __attribute__((ext_vector_type(2)));
#define LDS_AS __attribute__((address_space(3)))
#define GAS __attribute__((address_space(1)))
typedef GAS unsigned gu32;
typedef GAS unsigned long long gu64;
#define RLX_AGENT __ATOMIC_RELAXED, __HIP_MEMORY_SCOPE_AGENT
typedef __amdgpu_buffer_rsrc_t rsrc_t;

constexpr int F_ = 3584;
constexpr int SC1 = 16;

constexpr int RX = 4;
constexpr unsigned F_EP = 0, F_TMO = 128;
constexpr unsigned F_XN = 4096;
constexpr unsigned F_X1 = 196608;
constexpr unsigned F_O = 32768;
constexpr unsigned F_KV = 65536;
static_assert(F_XN + 8 * RX * 256 <= F_O && F_O + 2 * 64 * 256 <= F_KV, "flag block");
constexpr unsigned OFF_KEY = 131072;
constexpr unsigned OFF_AB = 147456;
constexpr unsigned OFF_XNEW = 262144;
constexpr unsigned OFF_CS = 294912;
constexpr unsigned OFF_FX = 393216;
constexpr unsigned OFF_CA = 524288;
constexpr unsigned OFF_CB = 655360;
constexpr unsigned OFF_STAT = 1u << 20;
constexpr unsigned OFF_PART = 3u << 20;
constexpr unsigned OFF_XN = 8u << 20;
constexpr unsigned OFF_O = 16u << 20;
constexpr unsigned OFF_KV = 32u << 20;
constexpr unsigned OFF_WI = 52u << 20;
constexpr unsigned WS_BYTES = 58u << 20;
constexpr unsigned POISON = 0xFFFFFFFFu;

constexpr int L_ST0 = 147456, L_CNT = 147968, L_CSL = 31872, L_FXL = 32256, L_K = 0, L_V = 32768, L_RING = 65536, L_XCH = 0, L_DN = 32768, L_TOUCH0 = 149504, L_RDY = 31744, L_QC = 31808, LDS_BYTES = 163840;
constexpr int STAGE = 24576;
#define SLOT(t) ((t) & 3)

template <typename F, int... I> __device__ __forceinline__ void static_for_impl(F&& f, std::integer_sequence<int, I...>) {
  (f(std::integral_constant<int, I>{}), ...);
}
template <int N, typename F> __device__ __forceinline__ void static_for(F&& f) { static_for_impl(f, std::make_integer_sequence<int, N>{}); }
__device__ __forceinline__ unsigned pack2(float a, float b) {
  f32x2 v = {a, b};
  return __builtin_bit_cast(unsigned, __builtin_convertvector(v, bf16x2));
}
__device__ __forceinline__ float asf(unsigned u) { return __builtin_bit_cast(float, u); }
__device__ __forceinline__ float lo16(unsigned u) { return __builtin_bit_cast(float, u << 16); }
__device__ __forceinline__ float hi16(unsigned u) { return __builtin_bit_cast(float, u & 0xffff0000u); }
__device__ __forceinline__ u32x4 pack8(const f32x4 a, const f32x4 b) {
  u32x4 o;
  o[0] = pack2(a[0], a[1]); o[1] = pack2(a[2], a[3]); o[2] = pack2(b[0], b[1]); o[3] = pack2(b[2], b[3]);
  return o;
}
__device__ __forceinline__ f32x4 mfma16(u32x4 a, u32x4 b, f32x4 c) {
  return __builtin_amdgcn_mfma_f32_16x16x32_bf16(__builtin_bit_cast(bf16x8, a), __builtin_bit_cast(bf16x8, b), c, 0, 0, 0);
}
__device__ __forceinline__ f32x4 mfma16k16(s16x4 a, s16x4 b, f32x4 c) { return __builtin_amdgcn_mfma_f32_16x16x16bf16_1k(a, b, c, 0, 0, 0); }
template <int CTRL> __device__ __forceinline__ float dppf(float v) {
  return __builtin_bit_cast(float, __builtin_amdgcn_update_dpp(0, __builtin_bit_cast(int, v), CTRL, 0xf, 0xf, true));
}
template <int CTRL> __device__ __forceinline__ unsigned dppu(unsigned v) {
  return (unsigned)__builtin_amdgcn_update_dpp(0, (int)v, CTRL, 0xf, 0xf, true);
}
__device__ __forceinline__ float add_x16(float v) {
  auto r = __builtin_amdgcn_permlane16_swap(__builtin_bit_cast(unsigned, v), __builtin_bit_cast(unsigned, v), false, false);
  return __builtin_bit_cast(float, (unsigned)r[0]) + __builtin_bit_cast(float, (unsigned)r[1]);
}
__device__ __forceinline__ float add_x32(float v) {
  auto r = __builtin_amdgcn_permlane32_swap(__builtin_bit_cast(unsigned, v), __builtin_bit_cast(unsigned, v), false, false);
  return __builtin_bit_cast(float, (unsigned)r[0]) + __builtin_bit_cast(float, (unsigned)r[1]);
}
__device__ __forceinline__ float max_x16(float v) {
  auto r = __builtin_amdgcn_permlane16_swap(__builtin_bit_cast(unsigned, v), __builtin_bit_cast(unsigned, v), false, false);
  return fmaxf(__builtin_bit_cast(float, (unsigned)r[0]), __builtin_bit_cast(float, (unsigned)r[1]));
}
__device__ __forceinline__ float max_x32(float v) {
  auto r = __builtin_amdgcn_permlane32_swap(__builtin_bit_cast(unsigned, v), __builtin_bit_cast(unsigned, v), false, false);
  return fmaxf(__builtin_bit_cast(float, (unsigned)r[0]), __builtin_bit_cast(float, (unsigned)r[1]));
}
__device__ __forceinline__ unsigned umax_x16(unsigned v) {
  auto r = __builtin_amdgcn_permlane16_swap(v, v, false, false);
  return max((unsigned)r[0], (unsigned)r[1]);
}
__device__ __forceinline__ unsigned umax_x32(unsigned v) {
  auto r = __builtin_amdgcn_permlane32_swap(v, v, false, false);
  return max((unsigned)r[0], (unsigned)r[1]);
}
__device__ __forceinline__ float sum_hi8(float v) {
  v += dppf<0x128>(v);
  v = add_x16(v);
  return add_x32(v);
}
__device__ __forceinline__ float sum64(float v) {
  v += dppf<0xB1>(v);
  v += dppf<0x4E>(v);
  v += dppf<0x124>(v);
  v += dppf<0x128>(v);
  v = add_x16(v);
  return add_x32(v);
}

#define BAR() asm volatile("s_waitcnt lgkmcnt(0)\n\ts_barrier" ::: "memory")
#define DRAIN() asm volatile("s_waitcnt vmcnt(0)" ::: "memory")
#define SB() __builtin_amdgcn_sched_barrier(0)
template <int N> __device__ __forceinline__ void wait_vm() { asm volatile("s_waitcnt vmcnt(%0)" ::"n"(N) : "memory"); }

__device__ __forceinline__ void dma16(rsrc_t rs, unsigned voff, unsigned soff, unsigned lds) {
  asm volatile("s_mov_b32 m0, %0\n\ts_nop 0\n\tbuffer_load_dwordx4 %1, %2, %3 offen lds" ::"s"(lds), "v"(voff), "s"(rs), "s"(soff) : "memory");
}
template <int OFF> __device__ __forceinline__ void dma16s(rsrc_t rs, unsigned voff, unsigned soff, unsigned lds) {
  asm volatile("s_mov_b32 m0, %0\n\ts_nop 0\n\tbuffer_load_dwordx4 %1, %2, %3 offen offset:%4 sc1 lds" ::"s"(lds), "v"(voff), "s"(rs), "s"(soff), "n"(OFF) : "memory");
}
__device__ __forceinline__ void touch(unsigned& d, const GAS bf16* ptr) { asm volatile("global_load_dword %0, %1, off" : "+v"(d) : "v"(ptr) : "memory"); }
__device__ __forceinline__ void ldrow(u32x4& a, u32x4& b, rsrc_t rx, unsigned voff, unsigned soff) {
  asm volatile("buffer_load_dwordx4 %0, %2, %3, %4 offen\n\tbuffer_load_dwordx4 %1, %2, %3, %4 offen offset:1024" : "=&v"(a), "=&v"(b) : "v"(voff), "s"(rx), "s"(soff) : "memory");
}
__device__ __forceinline__ void pin4(f32x2* v) { asm volatile("" : "+v"(v[0]), "+v"(v[1]), "+v"(v[2]), "+v"(v[3])); }
__device__ __forceinline__ float rbf1(float x) { return __builtin_bit_cast(float, pack2(0.f, x)); }
__device__ __forceinline__ void vswap(unsigned& a, unsigned& b) { asm volatile("v_swap_b32 %0, %1" : "+v"(a), "+v"(b)); }
__device__ __forceinline__ void vswap(float& a, float& b) { asm volatile("v_swap_b32 %0, %1" : "+v"(a), "+v"(b)); }
template <typename V, int N> __device__ __forceinline__ void vswapn(V& a, V& b) {
#pragma unroll
  for (int i = 0; i < N; ++i) { auto x = a[i], y = b[i]; vswap(x, y); a[i] = x; b[i] = y; }
}
__device__ __forceinline__ u32x4 ld16(rsrc_t rs, unsigned voff, unsigned soff) { return __builtin_amdgcn_raw_buffer_load_b128(rs, voff, soff, SC1); }
template <int OFF> __device__ __forceinline__ void ld16h(u32x4& d, rsrc_t rs, unsigned voff, unsigned soff) {
  asm volatile("buffer_load_dwordx4 %0, %1, %2, %3 offen offset:%4 sc1" : "=v"(d) : "v"(voff), "s"(rs), "s"(soff), "n"(OFF) : "memory");
}
template <int I, int OFF> __device__ __forceinline__ void ld16w(u32x4& d, rsrc_t rs, unsigned voff, unsigned soff) {
  if constexpr (I == 0) asm volatile("buffer_load_dwordx4 %0, %1, %2, %3 offen offset:%4 sc1" : "={v[128:131]}"(d) : "v"(voff), "s"(rs), "s"(soff), "n"(OFF) : "memory");
  else if constexpr (I == 1) asm volatile("buffer_load_dwordx4 %0, %1, %2, %3 offen offset:%4 sc1" : "={v[132:135]}"(d) : "v"(voff), "s"(rs), "s"(soff), "n"(OFF) : "memory");
  else if constexpr (I == 2) asm volatile("buffer_load_dwordx4 %0, %1, %2, %3 offen offset:%4 sc1" : "={v[136:139]}"(d) : "v"(voff), "s"(rs), "s"(soff), "n"(OFF) : "memory");
  else if constexpr (I == 3) asm volatile("buffer_load_dwordx4 %0, %1, %2, %3 offen offset:%4 sc1" : "={v[140:143]}"(d) : "v"(voff), "s"(rs), "s"(soff), "n"(OFF) : "memory");
  else if constexpr (I == 4) asm volatile("buffer_load_dwordx4 %0, %1, %2, %3 offen offset:%4 sc1" : "={v[144:147]}"(d) : "v"(voff), "s"(rs), "s"(soff), "n"(OFF) : "memory");
  else if constexpr (I == 5) asm volatile("buffer_load_dwordx4 %0, %1, %2, %3 offen offset:%4 sc1" : "={v[148:151]}"(d) : "v"(voff), "s"(rs), "s"(soff), "n"(OFF) : "memory");
  else if constexpr (I == 6) asm volatile("buffer_load_dwordx4 %0, %1, %2, %3 offen offset:%4 sc1" : "={v[152:155]}"(d) : "v"(voff), "s"(rs), "s"(soff), "n"(OFF) : "memory");
  else if constexpr (I == 7) asm volatile("buffer_load_dwordx4 %0, %1, %2, %3 offen offset:%4 sc1" : "={v[156:159]}"(d) : "v"(voff), "s"(rs), "s"(soff), "n"(OFF) : "memory");
  else if constexpr (I == 8) asm volatile("buffer_load_dwordx4 %0, %1, %2, %3 offen offset:%4 sc1" : "={v[160:163]}"(d) : "v"(voff), "s"(rs), "s"(soff), "n"(OFF) : "memory");
  else if constexpr (I == 9) asm volatile("buffer_load_dwordx4 %0, %1, %2, %3 offen offset:%4 sc1" : "={v[164:167]}"(d) : "v"(voff), "s"(rs), "s"(soff), "n"(OFF) : "memory");
  else if constexpr (I == 10) asm volatile("buffer_load_dwordx4 %0, %1, %2, %3 offen offset:%4 sc1" : "={v[168:171]}"(d) : "v"(voff), "s"(rs), "s"(soff), "n"(OFF) : "memory");
  else if constexpr (I == 11) asm volatile("buffer_load_dwordx4 %0, %1, %2, %3 offen offset:%4 sc1" : "={v[172:175]}"(d) : "v"(voff), "s"(rs), "s"(soff), "n"(OFF) : "memory");
}
template <int K> __device__ __forceinline__ void pin_w(u32x4& a, u32x4& b) {
  if constexpr (K == 0) asm volatile("" : "+{v[128:131]}"(a), "+{v[132:135]}"(b)::"memory");
  else if constexpr (K == 1) asm volatile("" : "+{v[136:139]}"(a), "+{v[140:143]}"(b)::"memory");
  else if constexpr (K == 2) asm volatile("" : "+{v[144:147]}"(a), "+{v[148:151]}"(b)::"memory");
  else if constexpr (K == 3) asm volatile("" : "+{v[152:155]}"(a), "+{v[156:159]}"(b)::"memory");
  else if constexpr (K == 4) asm volatile("" : "+{v[160:163]}"(a), "+{v[164:167]}"(b)::"memory");
  else if constexpr (K == 5) asm volatile("" : "+{v[168:171]}"(a), "+{v[172:175]}"(b)::"memory");
}
__device__ __forceinline__ void ld8h(u32x2& d, rsrc_t rs, unsigned voff, unsigned soff) {
  asm volatile("buffer_load_dwordx2 %0, %1, %2, %3 offen sc1" : "=v"(d) : "v"(voff), "s"(rs), "s"(soff) : "memory");
}
__device__ __forceinline__ void ld16u(u32x4& d, rsrc_t rs, unsigned voff, unsigned soff) {
  asm volatile("buffer_load_dwordx4 %0, %1, %2, %3 offen sc1" : "+v"(d) : "v"(voff), "s"(rs), "s"(soff) : "memory");
}
template <int OFF> __device__ __forceinline__ void ld4h(unsigned& d, rsrc_t rs, unsigned voff, unsigned soff) {
  asm volatile("buffer_load_dword %0, %1, %2, %3 offen offset:%4 sc1" : "=v"(d) : "v"(voff), "s"(rs), "s"(soff), "n"(OFF) : "memory");
}
__device__ __forceinline__ void ld4x16(u32x4 (&d)[4], rsrc_t rs, unsigned voff, unsigned soff) {
  asm volatile("buffer_load_dwordx4 %0, %4, %5, %6 offen sc1\n\tbuffer_load_dwordx4 %1, %4, %5, %6 offen offset:64 sc1\n\t"
               "buffer_load_dwordx4 %2, %4, %5, %6 offen offset:128 sc1\n\tbuffer_load_dwordx4 %3, %4, %5, %6 offen offset:192 sc1\n\ts_waitcnt vmcnt(0)"
               : "=&v"(d[0]), "=&v"(d[1]), "=&v"(d[2]), "=&v"(d[3]) : "v"(voff), "s"(rs), "s"(soff) : "memory");
}
__device__ __forceinline__ u32x2 ld8(rsrc_t rs, unsigned voff, unsigned soff) { return __builtin_amdgcn_raw_buffer_load_b64(rs, voff, soff, SC1); }
__device__ __forceinline__ void st16(u32x4 v, rsrc_t rs, unsigned voff, unsigned soff) { __builtin_amdgcn_raw_buffer_store_b128(v, rs, voff + soff, 0, SC1); }
__device__ __forceinline__ void st8(u32x2 v, rsrc_t rs, unsigned voff, unsigned soff) { __builtin_amdgcn_raw_buffer_store_b64(v, rs, voff, soff, SC1); }

__device__ __forceinline__ unsigned ld4(rsrc_t rs, unsigned voff, unsigned soff) { return __builtin_amdgcn_raw_buffer_load_b32(rs, voff, soff, SC1); }
__device__ __forceinline__ void st4(unsigned v, rsrc_t rs, unsigned voff, unsigned soff) { __builtin_amdgcn_raw_buffer_store_b32(v, rs, voff, soff, SC1); }

constexpr unsigned kSpinCap = 1u << 16;
__device__ __forceinline__ bool spin_fail(unsigned& spins, rsrc_t rs, u32x2 T) {
  asm volatile("" ::: "memory");
  ++spins;
  if (spins > kSpinCap) { st8(T, rs, 0, F_TMO); return true; }
  if ((spins & 255u) == 0u) {
    const u32x2 v = ld8(rs, 0, F_TMO);
    if (v[0] == T[0] && v[1] == T[1]) return true;
  }
  __builtin_amdgcn_s_sleep(3);
  return false;
}
__device__ __forceinline__ void poll_flags2(rsrc_t rs, unsigned voff, u32x2 T) {
  for (unsigned spins = 0;;) {
    const u32x4 v = ld16(rs, voff, 0);
    if (__all(v[0] == T[0] && v[1] == T[1] && v[2] == T[0] && v[3] == T[1])) break;
    if (spin_fail(spins, rs, T)) break;
  }
  asm volatile("" ::: "memory");
  __builtin_amdgcn_fence(__ATOMIC_ACQUIRE, "wavefront");
}
__device__ __forceinline__ void poll_flags(rsrc_t rs, unsigned voff, unsigned soff, u32x2 T) {
  for (unsigned spins = 0;;) {
    const u32x2 v = ld8(rs, voff, soff);
    if (__all(v[0] == T[0] && v[1] == T[1])) break;
    if (spin_fail(spins, rs, T)) break;
  }
  asm volatile("" ::: "memory");
  __builtin_amdgcn_fence(__ATOMIC_ACQUIRE, "wavefront");
}

__device__ __forceinline__ unsigned toff(unsigned R, unsigned ks, unsigned g) {
  return (((R >> 5) * 64 + ks * 2 + ((R >> 4) & 1)) << 10) + ((g * 16 + (R & 15)) << 4);
}
__device__ __forceinline__ void unpack8v(const u32x4 u, f32x2* v) {
#pragma unroll
  for (int j = 0; j < 4; ++j) v[j] = f32x2{lo16(u[j]), hi16(u[j])};
}
__device__ __forceinline__ u32x4 pack8v(const f32x2* v) {
  u32x4 o;
#pragma unroll
  for (int j = 0; j < 4; ++j) o[j] = pack2(v[j][0], v[j][1]);
  return o;
}
__device__ __forceinline__ f32x2 rbf1v(f32x2 v) { return f32x2{rbf1(v[0]), rbf1(v[1])}; }
__device__ __forceinline__ float ln_rd(float var) { return __builtin_amdgcn_rcpf(rbf1(__builtin_amdgcn_sqrtf(rbf1(rbf1(var) + 1e-5f)))); }
template <int N> __device__ __forceinline__ void ln_apply(float mb, float rd, const f32x2* v, f32x2* y) {
  const f32x2 mb2 = {mb, mb}, rd2 = {rd, rd};
#pragma unroll
  for (int j = 0; j < N; ++j) y[j] = rbf1v(rbf1v(v[j] - mb2) * rd2);
}
__device__ __forceinline__ float hadd(f32x2 v) {
  float a = v[0];
  asm volatile("" : "+v"(a));
  return a + v[1];
}
__device__ __forceinline__ float add_g(float v) { return add_x32(add_x16(v)); }
__device__ __forceinline__ void add_g3(float& x, float& y, float& z) {
  auto a = __builtin_amdgcn_permlane16_swap(__builtin_bit_cast(unsigned, x), __builtin_bit_cast(unsigned, y), false, false);
  auto b = __builtin_amdgcn_permlane16_swap(__builtin_bit_cast(unsigned, z), __builtin_bit_cast(unsigned, z), false, false);
  const float r = asf(a[0]) + asf(a[1]), t = asf(b[0]) + asf(b[1]);
  auto c = __builtin_amdgcn_permlane32_swap(__builtin_bit_cast(unsigned, r), __builtin_bit_cast(unsigned, r), false, false);
  auto d = __builtin_amdgcn_permlane32_swap(__builtin_bit_cast(unsigned, t), __builtin_bit_cast(unsigned, t), false, false);
  const float u = asf(c[0]) + asf(c[1]);
  z = asf(d[0]) + asf(d[1]);
  auto e = __builtin_amdgcn_permlane16_swap(__builtin_bit_cast(unsigned, u), __builtin_bit_cast(unsigned, u), false, false);
  x = asf(e[0]); y = asf(e[1]);
}
__device__ __forceinline__ u32x4 trade16(u32x4 v) {
  auto a = __builtin_amdgcn_permlane16_swap(v[0], v[2], false, false);
  auto b = __builtin_amdgcn_permlane16_swap(v[1], v[3], false, false);
  return u32x4{(unsigned)a[0], (unsigned)b[0], (unsigned)a[1], (unsigned)b[1]};
}
__device__ __forceinline__ unsigned umax4(u32x4 v) { return max(max(v[0], v[1]), max(v[2], v[3])); }
__device__ __forceinline__ u32x2 unpoison2(u32x2 v) { return u32x2{min(v[0], POISON - 1), min(v[1], POISON - 1)}; }
__device__ __forceinline__ u32x4 unpoison4(u32x4 v) {
  return u32x4{min(v[0], POISON - 1), min(v[1], POISON - 1), min(v[2], POISON - 1), min(v[3], POISON - 1)};
}

typedef union { struct { s16x4 a, b; } hf; u32x4 v; } frag_t;
__device__ __forceinline__ s16x4 trread(const char* smem, unsigned off) {
  return __builtin_amdgcn_ds_read_tr16_b64_v4i16((LDS_AS s16x4*)(smem + off));
}
__device__ __forceinline__ s16x4 trabs(unsigned addr) { return __builtin_amdgcn_ds_read_tr16_b64_v4i16((LDS_AS s16x4*)(size_t)addr); }

constexpr int WA = 6;
constexpr int WB = 8, WR = 4;
#define CH 0, 4, 7, 8, 13, 15, 20, 22, 27, 29
constexpr int CHV[10] = {CH};
constexpr int FSL = CHV[1], FS0 = CHV[2], FS0D = CHV[3], FS1 = CHV[4], FS1S = CHV[5];
constexpr int FXA = CHV[6], FXB = CHV[7], FXL = CHV[8], FXS = CHV[9];
constexpr int FXT = FXB >> 2;
#define C0 27, 3, 16, 19, 22, 23, 2, 5, 4
constexpr int C0V[9] = {C0};
constexpr int IMG = C0V[0], CSD = C0V[1];
constexpr int ERQ = C0V[2], CSL = C0V[3], CSS = C0V[4];
constexpr int RWL = C0V[6], RWS = C0V[7], POI = C0V[8];
#define WTS 99
#define WTG 2
#define PBS 10
#define KP0 26
#define PS0 8
constexpr int cmax(int a, int b) { return a > b ? a : b; }
constexpr int EIN = cmax(CSD, cmax(RWS + 1, POI)) + 3;
constexpr int ABS = 27;
constexpr int abl(int it) { return it ? 23 : C0V[5]; }
constexpr int XU = 3, XL = 2;
constexpr int x_slot(int u) { return u % XU == 0 ? 0 : u % XU == 1 ? 32768 : 49152; }
#define TCH 20, 6, 5, 27
constexpr int TCV[4] = {TCH};
constexpr int TS0 = TCV[0], TSN = TCV[1], TW = TCV[2], TST = TCV[3];
constexpr int QL = 26;
#define FDG 0
#define XSL 4
#define SDI0 8
#define SLP 24, 14, 56, 3
constexpr int SLV[4] = {SLP};
constexpr int SDIR = SLV[0], GSL = SLV[1], RSL = SLV[2], KSL = SLV[3];
constexpr bool tile_piece(int s) { return s >= 3 && ((s - 3) >> 2) + 3 < 8 && ((s - 3) & 3) < 3; }
constexpr int a_req(int s, int n) {
  if (!(n & 1)) return -1;
  if (s <= 1) return 8 + 3 * s + (n >> 1);
  return n < 5 && s + 5 < 32 ? 2 * (s + 5) + (n >> 1) : -1;
}
constexpr int vm_n(int it, int s, int n) {
  int c = 0;
  if (n == 0) c += tile_piece(s);
  if (n == 4) c += (s == abl(it)) + (it ? (s == FSL ? 5 : 0) : (s == ERQ) + (s == RWL ? 2 : 0) + (s == RWS + 1) + (s >= IMG && s < IMG + 3));
  if (!it) c += (s == WTS && n == WTG) + (s < 4 && n == 2) + (s == 0 && (n == 0 || n == 4)) + ((n == 3 || n == 5) && s + XL + XU < 32);
  else c += (a_req(s, n) >= 0) + ((s == 0 || s == 4) && n == 2);
  return c;
}
constexpr int vm_at(int it, int s, int n) {
  int c = it ? 8 : 14;
  for (int u = 0; u < 6 * s + n; ++u) c += vm_n(it, u / 6, u % 6);
  return c;
}
constexpr int vm_a(int it, int k) {
  if (k < 4) return 2 * k + 2;
  for (int u = 0; u < 192; ++u)
    if (a_req(u / 6, u % 6) == 2 * k + 1) return vm_at(it, (u + 1) / 6, (u + 1) % 6);
  return 0;
}
constexpr int vm_tile(int it, int t) { return t >= 3 ? vm_at(it, 4 * t - 7, 1) : t == 1 ? vm_at(0, 0, 5) : vm_at(0, 3, 3); }
constexpr int vm_x(int u) { return u == 2 ? 9 : u < XU + XL ? 12 + 2 * (u - XU) : vm_at(0, u - XU - XL + 1, 0); }

constexpr int wo_k(int it, int s) { return it || s < 4 ? 15 - s : s < 8 ? 7 - s : 19 - s; }
constexpr int wo_pos(int it, int s) { return 2 * s + 2 + (s >= 4 ? 2 * it : 0); }
constexpr int wo_at(int it, int s) { return 2 * it + (s < 4 ? 10 + 4 * s : s < 8 ? 16 + 2 * s : it ? 32 : 36); }
__device__ __forceinline__ void pin1(unsigned& a) { asm volatile("" : "+v"(a)); }
__device__ __forceinline__ void pin2(u32x4& a, u32x4& b) { asm volatile("" : "+v"(a), "+v"(b)::"memory"); }
__device__ __forceinline__ void pin2f(f32x2& a, f32x2& b) { asm volatile("" : "+v"(a), "+v"(b)); }
__device__ __forceinline__ void pin_ac(f32x4 (&ac)[2][4]) {
  asm volatile("" : "+{v[176:179]}"(ac[0][0]), "+{v[180:183]}"(ac[0][1]), "+{v[184:187]}"(ac[0][2]), "+{v[188:191]}"(ac[0][3]),
                    "+{v[192:195]}"(ac[1][0]), "+{v[196:199]}"(ac[1][1]), "+{v[200:203]}"(ac[1][2]), "+{v[204:207]}"(ac[1][3]));
}

constexpr int L_XC = LDS_BYTES - 64, L_SKEY = 32704, L_SRED = 4096, L_SFIN = 20480;
__device__ __forceinline__ unsigned key_load(rsrc_t rs, int l, int L, int rep) { return ld4(rs, (l & 15) * 4, OFF_KEY + (L * 16 + rep) * 128); }
__device__ __forceinline__ unsigned umax16(unsigned k) {
  k = max(k, dppu<0xB1>(k));
  k = max(k, dppu<0x4E>(k));
  k = max(k, dppu<0x141>(k));
  return max(k, dppu<0x140>(k));
}
__device__ __forceinline__ unsigned key_row(unsigned key) { return (0xffffu - (key & 0xffffu)) & 2047u; }
__device__ __forceinline__ unsigned key_wait(rsrc_t rs, u32x2 T, int l, int L, int rep, unsigned kv) {
  if (!__all(kv != POISON)) {
    unsigned spins = 0;
    do {
      if (spin_fail(spins, rs, T)) break;
      if (L == 0) __builtin_amdgcn_s_sleep(KSL);
      kv = key_load(rs, l, L, rep);
    } while (!__all(kv != POISON));
  }
  return __builtin_amdgcn_readfirstlane(umax16(kv));
}
__device__ __forceinline__ unsigned ffn_xoff(unsigned key, int L, int w) { return OFF_CA + L * 32768 + (key_row(key) >> 7) * 2048 + w * 256; }
__device__ __forceinline__ void row_load(rsrc_t rs, int g, unsigned xo, u32x4 (&xv)[4]) {
  ld16h<0>(xv[0], rs, g * 16, xo); ld16h<64>(xv[1], rs, g * 16, xo); ld16h<128>(xv[2], rs, g * 16, xo); ld16h<192>(xv[3], rs, g * 16, xo);
}
template <int SL = 0> __device__ __forceinline__ void ffn_xwait(rsrc_t rs, u32x2 T, unsigned xo, int l, u32x4 (&xv)[4]) {
#define XV_OK() __all(max(max(umax4(xv[0]), umax4(xv[1])), max(umax4(xv[2]), umax4(xv[3]))) != POISON)
  if (!XV_OK()) {
    unsigned spins = 0;
    do {
      if (spin_fail(spins, rs, T)) break;
      if (SL) __builtin_amdgcn_s_sleep(SL);
#pragma unroll
      for (int i = 0; i < 4; ++i) xv[i] = ld16(rs, (l >> 4) * 16 + i * 64, xo);
    } while (!XV_OK());
  }
#undef XV_OK
}
__device__ __forceinline__ void ffn_gu(const u32x4 (&gv)[4], const u32x4 (&uv)[4], const u32x4 (&xv)[4], f32x4& ga, f32x4& ua) {
  ga = mfma16(gv[0], xv[0], f32x4{0.f, 0.f, 0.f, 0.f}); ua = mfma16(uv[0], xv[0], f32x4{0.f, 0.f, 0.f, 0.f});
#pragma unroll
  for (int i = 1; i < 4; ++i) { ga = mfma16(gv[i], xv[i], ga); ua = mfma16(uv[i], xv[i], ua); }
}
__device__ __forceinline__ void ffn_gu_check(rsrc_t rs, u32x2 T, unsigned xo, int l, const u32x4 (&gv)[4], const u32x4 (&uv)[4], u32x4 (&xv)[4], f32x4& ga, f32x4& ua) {
  if (__builtin_expect(__any(ga[0] != ga[0]), 0)) {
    ffn_xwait(rs, T, xo, l, xv);
    ffn_gu(gv, uv, xv, ga, ua);
  }
}
__device__ __forceinline__ void ffn_gu_store(char* smem, int l, int w, f32x4 ga, f32x4 ua) {
  LDS_AS f32x4* sred = (LDS_AS f32x4*)(smem + L_SRED);
  if ((l & 15) == 0) { sred[w * 8 + (l >> 4)] = ga; sred[w * 8 + 4 + (l >> 4)] = ua; }
}
__device__ __forceinline__ f32x4 ffn_t_read(char* smem, int l) { return ((const LDS_AS f32x4*)(smem + L_SRED))[(l & 7) * 8 + ((l >> 3) & 1) * 4 + (l >> 4)]; }
__device__ __forceinline__ void ffn_d_read(char* smem, int l, int blk, s16x4 (&dfr)[8]) {
#pragma unroll
  for (int i = 0; i < 8; ++i) dfr[i] = trread(smem, L_DN + blk * 4096 + (l >> 4) * 1024 + ((l & 15) >> 2) * 256 + i * 32 + (l & 3) * 8);
}
__device__ __forceinline__ float ffn_d_sum(float t) { t += dppf<0xB1>(t); t += dppf<0x4E>(t); return t + dppf<0x141>(t); }
__device__ __forceinline__ u32x2 ffn_h(f32x4 t, int l) {
#pragma unroll
  for (int e = 0; e < 4; ++e) t[e] = ffn_d_sum(t[e]);
  const float a = (l & 1) ? t[1] : t[0], b = (l & 1) ? t[3] : t[2], s = (l & 2) ? b : a;
  const float gg = rbf1(s), uu = rbf1(dppf<0x128>(s));
  const float h = rbf1(gg * __builtin_amdgcn_rcpf(1.0f + __expf(-gg))) * uu;
  const unsigned h0 = pack2(h, dppf<0x55>(h)), h1 = pack2(dppf<0xAA>(h), dppf<0xFF>(h));
  return (l & 15) == 0 ? u32x2{h0, h1} : u32x2{0u, 0u};
}
__device__ __forceinline__ void ffn_d_mma(const s16x4 (&dfr)[8], u32x2& hh, f32x4& ya, f32x4& yb) {
#pragma unroll
  for (int i = 0; i < 8; ++i) {
    if (i & 1) yb = mfma16k16(dfr[i], __builtin_bit_cast(s16x4, hh), yb);
    else ya = mfma16k16(dfr[i], __builtin_bit_cast(s16x4, hh), ya);
    hh = u32x2{dppu<0x111>(hh[0]), dppu<0x111>(hh[1])};
  }
}
__device__ __forceinline__ void ffn_d_store(rsrc_t rs, int l, int G0, int L, int slice, f32x4 y) {
  const unsigned c = l & 15;
  st16(unpoison4(__builtin_bit_cast(u32x4, y)), rs, (G0 + (c >> 1)) * 32768 + slice * 128 + (c & 1) * 64 + (l >> 4) * 16, OFF_PART + L * (1u << 20));
}
__device__ __forceinline__ void red_load(rsrc_t rs, int tid, int L, unsigned G, unsigned key, u32x4 (&pv)[4], u32x2& cxn, u32x2& cx1) {
#pragma unroll
  for (int k = 0; k < 4; ++k) pv[k] = ld16(rs, min(tid + 512 * k, 1791) * 16, OFF_PART + L * (1u << 20) + G * 32768);
  cxn = ld8(rs, (tid & 3) * 16 + (tid & 4) * 2, OFF_CA + L * 32768 + (key_row(key) >> 7) * 2048 + G * 64);
  cx1 = ld8(rs, (tid & 3) * 16 + (tid & 4) * 2, OFF_CB + L * 32768 + (key_row(key) >> 7) * 2048 + G * 64);
}
__device__ __forceinline__ void red_sum(rsrc_t rs, char* smem, u32x2 T, int tid, int w, int L, unsigned G, unsigned key,
                                        u32x4 (&pv)[4], u32x2& cxn, u32x2& cx1) {
#define PV_OK() __all(max(max(max(umax4(pv[0]), umax4(pv[1])), max(umax4(pv[2]), umax4(pv[3]))), max(max(cxn[0], cxn[1]), max(cx1[0], cx1[1]))) != POISON)
  if (!PV_OK()) {
    unsigned spins = 0;
    do {
      if (spin_fail(spins, rs, T)) break;
      red_load(rs, tid, L, G, key, pv, cxn, cx1);
    } while (!PV_OK());
  }
#undef PV_OK
  f32x4 s = (__builtin_bit_cast(f32x4, pv[0]) + __builtin_bit_cast(f32x4, pv[1])) + __builtin_bit_cast(f32x4, pv[2]);
  if (w < 4) s += __builtin_bit_cast(f32x4, pv[3]);
  ((LDS_AS f32x4*)(smem + L_SFIN))[tid] = s;
}
template <int NW> __device__ __forceinline__ u32x2 red_final(char* smem, int l, unsigned key, u32x2 cxn, u32x2 cx1) {
  const LDS_AS f32x4* sf = (const LDS_AS f32x4*)(smem + L_SFIN) + l;
  f32x4 s = (sf[0] + sf[64]) + (sf[128] + sf[192]);
  if constexpr (NW == 8) s += (sf[256] + sf[320]) + (sf[384] + sf[448]);
#pragma unroll
  for (int j = 0; j < 4; ++j) s[j] = sum_hi8(s[j]);
  const unsigned k16 = key >> 16;
  const float rmax = __builtin_bit_cast(float, ((k16 & 0x8000u) ? (k16 & 0x7fffu) : (~k16 & 0xffffu)) << 16);
  const float sig = rbf1(__builtin_amdgcn_rcpf(1.0f + __expf(-rmax)));
  const float xn4[4] = {lo16(cxn[0]), hi16(cxn[0]), lo16(cxn[1]), hi16(cxn[1])};
  const float x14[4] = {lo16(cx1[0]), hi16(cx1[0]), lo16(cx1[1]), hi16(cx1[1])};
  float o[4];
#pragma unroll
  for (int j = 0; j < 4; ++j) o[j] = x14[j] + rbf1(xn4[j] + rbf1(rbf1(s[j]) * sig));
  return u32x2{pack2(o[0], o[1]), pack2(o[2], o[3])};
}

struct RowFin { float mean, var, mb, rd; unsigned key; };
__device__ __forceinline__ void fin_wait(rsrc_t rs, u32x2 T, unsigned voff, unsigned so, u32x4 (&sp)[4]) {
#define ST_OK() __all(max(max(umax4(sp[0]), umax4(sp[1])), max(umax4(sp[2]), umax4(sp[3]))) != POISON)
  if (!ST_OK()) {
    unsigned spins = 0;
    do {
      if (spin_fail(spins, rs, T)) break;
#pragma unroll
      for (int i = 0; i < 4; ++i) sp[i] = ld16(rs, voff + i * 2048, so);
    } while (!ST_OK());
  }
#undef ST_OK
}
__device__ __forceinline__ RowFin fin_key(rsrc_t rs, u32x2 T, unsigned voff, unsigned so, char* skey, int l, int w, unsigned R, float wsum, u32x4 (&sp)[4]) {
  RowFin r;
#define ST_SUM() ((__builtin_bit_cast(f32x4, sp[0]) + __builtin_bit_cast(f32x4, sp[1])) + (__builtin_bit_cast(f32x4, sp[2]) + __builtin_bit_cast(f32x4, sp[3])))
  f32x4 sf = ST_SUM();
  const float z = (sf[0] + sf[1]) + sf[2];
  if (__any(z != z)) {
    fin_wait(rs, T, voff, so, sp);
    sf = ST_SUM();
  }
#undef ST_SUM
  float s0 = sf[0], s1 = sf[1], s2 = sf[2];
  add_g3(s0, s1, s2);
  r.mean = s0 * (1.0f / 1024.0f);
  r.var = fmaxf(__builtin_fmaf(-r.mean, r.mean, s1 * (1.0f / 1024.0f)), 0.f);
  r.mb = rbf1(r.mean); r.rd = ln_rd(r.var);
  const unsigned rb16 = pack2((s2 - r.mb * wsum) * r.rd, 0.f) & 0xffffu;
  r.key = min((((rb16 & 0x8000u) ? (~rb16 & 0xffffu) : (rb16 | 0x8000u)) << 16) | (0xffffu - R), POISON - 1);
  const unsigned wk = umax16(r.key);
  if (l == 0) ((LDS_AS unsigned*)skey)[w] = wk;
  return r;
}
__device__ __forceinline__ unsigned fin_cand(rsrc_t rs, char* skey, int L, int g, int rb, int ct, const RowFin& r, const f32x2 (&v)[8]) {
  const u32x4 k0 = *(const LDS_AS u32x4*)skey, k1 = *(const LDS_AS u32x4*)(skey + 16);
  const unsigned best = max(umax4(k0), umax4(k1));
  if (r.key == best) {
    const unsigned co = L * 32768 + rb * 2048 + ct * 128 + g * 16;
    f32x2 t[8], rd2 = {r.rd, r.rd}, of2 = {-r.mb * r.rd, -r.mb * r.rd};
    asm volatile("" : "+v"(rd2), "+v"(of2));
#pragma unroll
    for (int j = 0; j < 8; ++j) t[j] = __builtin_elementwise_fma(v[j], rd2, of2);
    st16(unpoison4(pack8v(t)), rs, co, OFF_CA); st16(unpoison4(pack8v(t + 4)), rs, co + 64, OFF_CA);
    if (g == 0) st4(best, rs, OFF_KEY + (L * 16 + ct) * 128 + rb * 4, 0);
    st16(unpoison4(pack8v(v)), rs, co, OFF_CB); st16(unpoison4(pack8v(v + 4)), rs, co + 64, OFF_CB);
  }
  return best;
}
__device__ __forceinline__ void fin_x2(const RowFin& r, f32x2 (&v)[8]) {
  f32x2 sc2 = {1.0f + r.rd, 1.0f + r.rd}, of2 = {-r.mb * r.rd, -r.mb * r.rd};
  asm volatile("" : "+v"(sc2), "+v"(of2));
#pragma unroll
  for (int j = 0; j < 8; ++j) v[j] = __builtin_elementwise_fma(v[j], sc2, of2);
}

extern "C" __device__ unsigned long long dispatch_id() __asm("llvm.amdgcn.dispatch.id");

extern "C" __global__ __launch_bounds__(512, 2) void mega_kernel(const bf16* x, char* ws, const bf16* wqkv, const bf16* wo, const bf16* wr,
                                                                 const bf16* wg, const bf16* wu, const bf16* wd, bf16* out) {
  extern __shared__ __attribute__((aligned(16))) char smem[];
  const int w0_ = __builtin_amdgcn_readfirstlane(threadIdx.x >> 6);
  const unsigned lbase = (unsigned)(size_t)(LDS_AS char*)smem;
  {
    unsigned long long pc;
    unsigned len;
    asm volatile("s_getpc_b64 %0\n\ts_mov_b32 %1, .Ltext_end-mega_kernel\n\t.subsection 1\n.Ltext_end:\n\t.subsection 0" : "=s"(pc), "=s"(len));
    const unsigned o = ((blockIdx.x >> 3) * 64 + (threadIdx.x & 63)) * 128;
    if (w0_ == 7 && o < len)
      asm volatile("s_mov_b32 m0, %0\n\ts_nop 0\n\tglobal_load_lds_dword %1, %2" ::"s"(lbase + L_TOUCH0), "v"(o), "s"(pc & ~127ull) : "memory");
  }
  asm volatile("" : "+s"(x), "+s"(ws), "+s"(wqkv));
#define IDS()                                                                                         \
  int tid = threadIdx.x, w = w0_, bid = blockIdx.x;                                                   \
  asm volatile("" : "+v"(tid));                                                                       \
  asm volatile("" : "+s"(w));                                                                         \
  asm volatile("" : "+s"(bid));                                                                       \
  const int l = tid & 63, xcd = bid & 7, bi = bid >> 3;                                               \
  const int p = (xcd & 1) * 4 + (bi & 3), h = (xcd >> 1) * 8 + (bi >> 2);         \
  const int rb = 2 * xcd + (bi & 1), ct = bi >> 1;                               \
  const int slice = xcd * 28 + bi;                                                   \
  const bool ffn_on = bi < 28;                                                                        \
  const int g = l >> 4;                                                                               \
  const int rt = w < 4 ? w : 11 - w;          \
  KARGS();                                                                                            \
  (void)p; (void)h; (void)rb; (void)ct; (void)slice; (void)ffn_on; (void)g; (void)rt;
#define PROT() (2 * (p & 3) + (p >> 2))
  const rsrc_t rs = __builtin_amdgcn_make_buffer_rsrc((void*)ws, 0, WS_BYTES, 0x00020000);
  typedef const void* const __attribute__((address_space(4)))* karg_t;
#define KARGS() karg_t ka = (karg_t)__builtin_amdgcn_kernarg_segment_ptr(); asm volatile("" : "+s"(ka))
#define ARGP(name, i) const GAS bf16* name = (const GAS bf16*)ka[i]; asm volatile("" : "+s"(name))
#define RSRC(ptr, bytes) __builtin_amdgcn_make_buffer_rsrc((void*)(ptr), 0, bytes, 0x00020000)
#define rwq RSRC(pwq, 1024 * 3072 * 2)
#define rwo RSRC(pwo, 1024 * 1024 * 2)
#define rwg RSRC(pwg, 1024 * F_ * 2)
#define rwu RSRC(pwu, 1024 * F_ * 2)
#define rwd RSRC(pwd, 1024 * F_ * 2)
#define rout RSRC(pout, 2048 * 1024 * 2)

#define WQ_VOFF() ((l >> 2) * 6144 + (((l & 3) ^ ((((l >> 4) ^ (l >> 5)) & 1) << 1)) << 4))
#define WQ_PIECE(t, j, dst) dma16(rwq, wqv, ((((t) + rot) & 7) * 128 + ((j) & 7) * 16) * 6144 + ((j) >> 3) * 2048 + h * 64, lbase + (dst) + (j) * 1024)
#define WQ_BOFF(dt) ((g >> 1) * 1024 + (8 * (g & 1) + ((l & 15) >> 2)) * 64 + (l & 3) * 8 + (((dt) ^ (g & 1)) << 5))
#define X_REQ(u, hf) dma16(rx, xv[hf], (p * 256 + rt * 32 + ((u) & 1) * 16 + (hf) * 8) * 2048 + ((((u) >> 2) + rot) & 7) * 256 + (((u) >> 1) & 1) * 128, lbase + rt * 2048 + x_slot(u) + (hf) * 1024)
#define WO_PIECE(j) dma16(rwo, wov, ((w >> 2) * 64 + (w & 3) + 4 * (j)) * 16384 + ct * 128, lbase + 32768 + (j) * 8192 + w * 1024)
#define WD_DMA() _Pragma("unroll") for (int i = 0; i < 4; ++i) WD_DMA1(i)
#define WD_DMA1(i)                                                                                   \
      dma16(rwd, (l >> 4) * 2048 + (l & 15) * 16, (unsigned)(slice * 16 + 4 * (i)) * 2048 + w * 256, lbase + L_DN + w * 4096 + (i) * 1024)
#define FFN_DMA(m) dma16((m) & 1 ? rwu : rwg, fnv, (unsigned)(((4 * w + ((m) >> 1)) * 32) * F_ + slice * 16) * 2, lbase + 65536 + (m) * 8192 + w * 1024)
#define POISON_BIG(L)                                                                                \
  if (w >= 2) {                                                                                      \
    unsigned pz1 = POISON;                                                                           \
    asm volatile("" : "+v"(pz1));                                                                    \
    st16(u32x4{pz1, pz1, pz1, pz1}, rs, (w >= 4 ? OFF_PART + (L) * (1u << 20) + bid * 4096 - 4096 : OFF_STAT + (L) * 524288 + (p * 32 + h) * 2048 - 2048) + tid * 16, 0); \
  }
  u32x4 gv[4], uv[4];
  u32x4 keep[9];
  u32x4 pre[4][2];
#define PRE_LOAD(it_, rot_, s0, s1)                                                                  \
  static_for<(s1) - (s0)>([&](auto c_) __attribute__((always_inline)) {                              \
    constexpr int s = (s0) + decltype(c_)::value;                                                    \
    const unsigned ab_ = OFF_XN + (it_) * (4u << 20) + (((rot_) + (s >> 2)) & 7) * 8192 + ((s >> 1) & 1) * 4096; \
    ld16w<2 * s, (s & 1) * 2048>(pre[s][0], rs, l * 16, ab_ + (p * 8 + rt) * 65536);                         \
    ld16w<2 * s + 1, (s & 1) * 2048 + 1024>(pre[s][1], rs, l * 16, ab_ + (p * 8 + rt) * 65536);                 \
  })
  u32x2 T;
  const GAS bf16* tsrc;
  unsigned xv[2], xa[2];
  u32x2 ep;
#define X_READ(u, d0, d1) { d0 = *(const LDS_AS u32x4*)(size_t)(xa[0] + x_slot(u)); d1 = *(const LDS_AS u32x4*)(size_t)(xa[1] + x_slot(u)); }
  const rsrc_t rx = RSRC(x, 2048 * 1024 * 2);
  {
    IDS();
    const int rot = PROT();
    const bf16* pwq = wqkv;
    const unsigned wqv = WQ_VOFF();
    xv[0] = (l >> 3) * 2048 + (((l & 7) ^ (l >> 4)) << 4); xv[1] = xv[0] ^ 64;
#define WQ_KMAJ(q) (((q) % 3) * 8 + (q) / 3)
    WQ_PIECE(0, WQ_KMAJ(w), L_RING);
    X_REQ(0, 0); X_REQ(0, 1); X_REQ(1, 0); X_REQ(1, 1);
    WQ_PIECE(0, WQ_KMAJ(8 + w), L_RING);
    asm volatile("s_barrier" ::: "memory");
    WQ_PIECE(0, WQ_KMAJ(16 + w), L_RING);
    X_REQ(2, 0); X_REQ(2, 1);
    asm volatile("buffer_load_dwordx2 %0, off, %1, 0 sc1" : "=v"(ep) : "s"(rs) : "memory");
    if constexpr (PBS < 0) { POISON_BIG(0); }
    {
      ARGP(pwo, 3); ARGP(pwg, 5); ARGP(pwu, 6);
      const int t = tid < 288 ? tid - 64 : tid - 288, go = (bi * 32 + t / 7) * F_ + xcd * 448 + (t % 7) * 64;
      tsrc = tid < 64 ? pwo + bid * 4096 + tid * 64 : tid < 288 ? pwg + go : pwu + go;
      asm volatile("" : "+v"(tsrc));
    }
#pragma unroll
    for (int kk = 0; kk < 2; ++kk) {
      const int r = l & 15;
      xa[kk] = lbase + rt * 2048 + (r >> 3) * 1024 + (r & 7) * 128 + (((4 * kk + g) ^ (r >> 1)) << 4);
      asm volatile("" : "+v"(xa[kk]));
    }
    if (tid == 192) *(LDS_AS u32x4*)(smem + L_QC) = u32x4{0u, 0u, 0u, 0u};
    wait_vm<4>();
    X_READ(0, pre[0][0], pre[1][0]); X_READ(1, pre[0][1], pre[1][1]);
    asm volatile("s_waitcnt lgkmcnt(0)\n\ts_barrier" : "+v"(pre[0][0]), "+v"(pre[0][1]), "+v"(pre[1][0]), "+v"(pre[1][1])::"memory");
    X_REQ(3, 0); X_REQ(3, 1); X_REQ(4, 0); X_REQ(4, 1);
  }
  unsigned sel0 = 0x7FFFFFFFu;
  u32x4 x2p[2];
  unsigned xm0 = 0, kg0 = 0;

  auto layer = [&](auto it_c) __attribute__((always_inline)) {
    constexpr int it = decltype(it_c)::value;
    f32x4 acc[2][6];
    f32x2 qs2[2];
    u32x4 k0, k1, v0, v1;
    unsigned tdum = 0;
    {
      IDS();
      bool aff = false;
      unsigned aq[2];
      u32x4 av[2];
#define PIN_ACC() asm volatile("" : "+{v[176:179]}"(acc[0][0]), "+{v[180:183]}"(acc[0][1]), "+{v[184:187]}"(acc[0][2]), "+{v[188:191]}"(acc[0][3]), "+{v[192:195]}"(acc[0][4]), "+{v[196:199]}"(acc[0][5]), \
                               "+{v[200:203]}"(acc[1][0]), "+{v[204:207]}"(acc[1][1]), "+{v[208:211]}"(acc[1][2]), "+{v[212:215]}"(acc[1][3]), "+{v[216:219]}"(acc[1][4]), "+{v[220:223]}"(acc[1][5]))
#define PINV4(x) asm volatile("" : "+v"(x[0]), "+v"(x[1]), "+v"(x[2]), "+v"(x[3])::"memory")
      auto qkv_loop = [&](auto it_c) __attribute__((always_inline)) {
        constexpr int IT = decltype(it_c)::value;
        const unsigned abase = OFF_XN + IT * (4u << 20) + (p * 8 + rt) * 65536;
        const int rot = PROT() + 5 * IT;
        unsigned fkey = 0, kv, bq[2];
        u32x4 fx[4], bv[2], af[WA][2];
        u32x2 cxn, cx1;
        f32x4 sa, sb, cs4[2];
        f32x2 b2[2], sv[8];
#define AB_OFF() (OFF_AB + IT * 16384 + (p * 8 + rt) * 128)
#define A_LOAD1(k, a) ld16w<(k) % WA * 2 + (a), ((k) & 1) * 2048 + (a) * 1024>(af[(k) % WA][a], rs, l * 16, abase + ((((k) >> 2) + rot) & 7) * 8192 + (((k) >> 1) & 1) * 4096)
#pragma unroll
        for (int s = 0; s < 2 + 2 * IT; ++s) { af[s][0] = pre[s][0]; af[s][1] = pre[s][1]; }
        const bf16* pwq = wqkv;
        const int tstep = TS0 + bid % TSN;
        const unsigned wqv = WQ_VOFF();
#define QKV_DMA1(t, i)                                                                            \
  {                                                                                               \
    if constexpr (IT == 0) WQ_PIECE(t, 3 * w + (i), L_RING + SLOT(t) * STAGE);                    \
    else dma16s<(i) * 1024>(rs, l * 16, OFF_WI + (h * 8 + (((t) + rot) & 7)) * STAGE + w * 3072, lbase + L_RING + SLOT(t) * STAGE + w * 3072); \
  }
        unsigned bo[2][2];
#pragma unroll
        for (int dt = 0; dt < 2 - IT; ++dt) {
          bo[dt][0] = lbase + L_RING + (IT ? l * 16 : WQ_BOFF(dt)); bo[dt][1] = bo[dt][0] + 2 * STAGE;
          asm volatile("" : "+v"(bo[dt][0]), "+v"(bo[dt][1]));
        }
        auto frag0 = [&](int sl, int f) __attribute__((always_inline)) {
          const unsigned o = lbase + L_RING + (4 * g + ((l & 15) >> 2)) * 64 + (l & 3) * 8 + (((f ^ g ^ (g >> 1)) & 1) << 5) + sl * STAGE + ((f % 6) >> 1) * 8192 + (f / 6) * 2048;
          frag_t t;
          t.hf.a = trabs(o); t.hf.b = trabs(o + 1024);
          return t.v;
        };
        u32x4 df[4];
        auto dot_mma = [&](const u32x4 (&xf)[4]) __attribute__((always_inline)) {
          sa = mfma16(df[0], xf[0], f32x4{0.f, 0.f, 0.f, 0.f}); sb = mfma16(df[1], xf[1], f32x4{0.f, 0.f, 0.f, 0.f});
          sa = mfma16(df[2], xf[2], sa); sb = mfma16(df[3], xf[3], sb);
        };
        auto dot_store = [&](unsigned dst) __attribute__((always_inline)) {
          if ((l & 15) == 0) st16(unpoison4(__builtin_bit_cast(u32x4, sa + sb)), rs, g * 16, dst + ((h * 6 + w) * 8 + p) * 64);
        };
        auto tile_sum = [&](u32x4 pt, int dst) __attribute__((always_inline)) {
          f32x4 c = __builtin_bit_cast(f32x4, pt);
#pragma unroll
          for (int e = 0; e < 4; ++e) { c[e] += dppf<0x124>(c[e]); c[e] += dppf<0x128>(c[e]); c[e] = add_x16(c[e]); }
          if ((l & 28) == 0) *(LDS_AS f32x4*)(smem + dst + (w * 2 + (l >> 5)) * 64 + (l & 3) * 16) = c;
        };
        frag_t bfr[6];
#define QKV_BO(s, n) (bo[(n) & 1][SLOT((s) >> 2) >> 1] + (SLOT((s) >> 2) & 1) * STAGE + ((n) >> 1) * 8192 + ((s) & 3) * 2048)
#define QKV_RDA(s, n)                                                                             \
  {                                                                                               \
    if constexpr (IT == 0) bfr[n].hf.a = trabs(QKV_BO(s, n));                                     \
    else bfr[n].v = *(const LDS_AS u32x4*)(size_t)(bo[0][SLOT((s) >> 2) >> 1] + (SLOT((s) >> 2) & 1) * STAGE + (((s) & 3) * 6 + (n)) * 1024); \
  }
#define QKV_RDB(s, n) { if constexpr (IT == 0) bfr[n].hf.b = trabs(QKV_BO(s, (n) ^ 1) + 256); }
#pragma unroll
        for (int n = 0; n < 6; ++n) { QKV_RDA(0, n); if (n < 5) QKV_RDB(0, n); }
#define XF_OFF() (l & 8 ? ((l & 7) * RX + bi % RX) * 256 + h * 8 : (p * RX + (xcd >> 1) % RX) * 256 + (4 * rt + (l & 3)) * 8)
#define XP_OFF() (OFF_XNEW + ((rot + FXT) & 7) * 256)
#define FP_OFF(i) (w == 3 ? OFF_XNEW + (i) * 1024 : OFF_FX + h * 3072 + w * 1024)
#define FX_OK(n) __all(((n) == 4 ? max(max(umax4(fx[0]), umax4(fx[1])), max(umax4(fx[2]), umax4(fx[3]))) : max(umax4(fx[0]), umax4(fx[1]))) != POISON)
#define SINCE(s0) (vm_at(IT, s, n) - vm_at(IT, s0, 5))
#define DEF4(x) asm volatile("" : "=v"(x[0]), "=v"(x[1]), "=v"(x[2]), "=v"(x[3]))
        LDS_AS unsigned* qcnt = (LDS_AS unsigned*)(smem + L_QC);
        unsigned qc;
        unsigned qadr = lbase + L_QC, qone = 1;
        asm volatile("" : "+v"(qadr), "+v"(qone));
#define count_in(i) asm volatile("s_mov_b64 exec, 1\n\tds_add_u32 %0, %1 offset:%2\n\ts_mov_b64 exec, -1" ::"v"(qadr), "v"(qone), "n"(4 * (i)) : "memory")
        auto count_wait = [&](int i, unsigned want) __attribute__((always_inline)) {
          if (__builtin_expect((unsigned)__builtin_amdgcn_readfirstlane(qc) < want, 0)) {
            while ((unsigned)__builtin_amdgcn_readfirstlane(__hip_atomic_load(qcnt + i, __ATOMIC_RELAXED, __HIP_MEMORY_SCOPE_WORKGROUP)) < want) {}
          }
          asm volatile("" ::: "memory");
        };
        unsigned long long xok = xm0;
        auto x1_need = [&](unsigned long long m) __attribute__((always_inline)) {
          if (__builtin_expect((xok & m) != m, 0)) {
            for (unsigned spins = 0;;) {
              const u32x2 f = ld8(rs, F_X1 + (p * RX + (xcd >> 1) % RX) * 256 + (l & 31) * 8, 0);
              xok = __builtin_amdgcn_ballot_w64(f[0] == T[0] && f[1] == T[1]);
              if ((xok & m) == m) break;
              if (spin_fail(spins, rs, T)) { xok = ~0ull; break; }
              __builtin_amdgcn_s_sleep(KSL);
            }
            asm volatile("" ::: "memory");
          }
        };
        auto gap = [&](auto sc_, auto nc_) __attribute__((always_inline)) {
          constexpr int s = decltype(sc_)::value, n = decltype(nc_)::value;
          {
            constexpr int ts = (s + 3) / 4, tc = (s + 2) / 4, t0 = 1;
            if constexpr (ts >= 3 && ts < 8 && s == 4 * ts - 3 && n == 2) { wait_vm<vm_at(IT, s, n) - vm_tile(IT, ts)>(); count_in(0); }
            if constexpr (!IT && tc >= t0 && tc < 3 && s == 4 * tc - 2 && n == 1) { wait_vm<vm_at(IT, s, n) - vm_tile(IT, tc)>(); count_in(0); }
            if constexpr (IT && ts >= t0 && ts < 3 && s == 4 * ts - 3 && n == 2) count_in(0);
            if constexpr (tc >= t0 && tc < 8 && s == 4 * tc - 2) {
              if constexpr (n == 3) qc = __hip_atomic_load(qcnt, __ATOMIC_RELAXED, __HIP_MEMORY_SCOPE_WORKGROUP);
              if constexpr (n == 5) count_wait(0, 8 * (tc - t0 + 1));
            }
          }
          if constexpr (n == 0 && tile_piece(s)) QKV_DMA1(((s - 3) >> 2) + 3, (s - 3) & 3);
          if constexpr (IT == 0) {
            if constexpr (s == 0 && !(n & 1)) QKV_DMA1(1, n >> 1);
            if constexpr (s >= 1 && s < 4 && n == 2) QKV_DMA1(2, s - 1);
            constexpr int u = s + XL;
            if constexpr (n == 1 && u < 32) {
              wait_vm<vm_at(0, s, n) - vm_x(u)>();
              X_READ(u, af[(u & ~1) % WA][u & 1], af[(u | 1) % WA][u & 1]);
            }
            if constexpr (n == 3 && u + XU < 32) { asm volatile("s_waitcnt lgkmcnt(%0)" ::"n"(s + 1 < QL ? 4 : 3) : "memory"); X_REQ(u + XU, 0); }
            if constexpr (n == 5 && u + XU < 32) X_REQ(u + XU, 1);
            if constexpr (s == WTS && n == WTG) {
              const int i = bi * 192 + w * 24 + min(l, 23);
              touch(tdum, (const GAS bf16*)pwq + (size_t)(i / 12 + 512 * (~xcd & 1)) * 3072 + ((i % 12) >> 2) * 1024 + (xcd >> 1) * 256 + (i & 3) * 64);
            }
            if constexpr (s == PBS && n == 0) { POISON_BIG(0); }
            if constexpr (s == 0 && n == 1) count_in(2);
            if constexpr (s == 0 && n == 3) qc = __hip_atomic_load(qcnt + 2, __ATOMIC_RELAXED, __HIP_MEMORY_SCOPE_WORKGROUP);
            if constexpr (s == 0 && n == 5) count_wait(2, 8);
            if constexpr (s == 1 && n == 1) {
              asm volatile("" : "+v"(ep)::"memory");
              const unsigned long long e = (((unsigned long long)(unsigned)__builtin_amdgcn_readfirstlane(ep[1]) << 32) | (unsigned)__builtin_amdgcn_readfirstlane(ep[0])) + 1ull + dispatch_id() * 0x9E3779B97F4A7C16ull;
              T = u32x2{(unsigned)e, (unsigned)(e >> 32)};
            }
            const unsigned R = p * 256 + h * 8 + w;
            if constexpr (s == RWL && n == 4) ldrow(bv[0], bv[1], rx, l * 16, R * 2048);
            if constexpr (s == RWS && n == 0) {
              wait_vm<SINCE(RWL)>();
              asm volatile("" : "+v"(bv[0]), "+v"(bv[1])::"memory");
              unpack8v(bv[0], sv);
              pin4(sv);
            }
            if constexpr (s == RWS && n == 2) { unpack8v(bv[1], sv + 4); pin4(sv + 4); }
            if constexpr (s == RWS && n == 4) {
              b2[0][0] = hadd(((sv[0] + sv[1]) + (sv[2] + sv[3])) + ((sv[4] + sv[5]) + (sv[6] + sv[7])));
              asm volatile("" : "+v"(b2[0][0]));
            }
            if constexpr (s == RWS && n == 5) { const float m = sum64(b2[0][0]) * (1.0f / 1024.0f); b2[0] = f32x2{m, m}; asm volatile("" : "+v"(b2[0])); }
            if constexpr (s == RWS + 1 && n == 0) {
              f32x2 q0 = {0.f, 0.f}, q1 = {0.f, 0.f};
#pragma unroll
              for (int j = 0; j < 8; j += 2) {
                const f32x2 d0 = sv[j] - b2[0], d1 = sv[j + 1] - b2[0];
                q0 = __builtin_elementwise_fma(d0, d0, q0); q1 = __builtin_elementwise_fma(d1, d1, q1);
              }
              b2[1][0] = hadd(q0 + q1);
              asm volatile("" : "+v"(b2[1][0]));
            }
            if constexpr (s == RWS + 1 && n == 2) { b2[1][0] = sum64(b2[1][0]) * (1.0f / 1024.0f); asm volatile("" : "+v"(b2[1][0])); }
            if constexpr (s == RWS + 1 && n == 4) {
              const float rd = ln_rd(b2[1][0]);
              if (l < 2) st4(__builtin_bit_cast(unsigned, l == 0 ? rd : -rbf1(b2[0][0])), rs, OFF_AB + R * 4 + l * 8192, 0);
            }
            if constexpr (s == POI && n == 4) {
              unsigned pz1 = POISON;
              asm volatile("" : "+v"(pz1));
              const u32x4 pz = {pz1, pz1, pz1, pz1};
              const unsigned prb = 2 * p + (h >> 4), pct = h & 15;
              static_assert(OFF_CB == OFF_CA + 131072, "poison addressing");
              if (w == 4 && l < 32) st16(pz, rs, OFF_CA + (l & 8) * 4096 + (l & 16) * 8192 + prb * 2048 + pct * 128 + (l & 7) * 16, 0);
              if (w == 5 && l < 4) st16(pz, rs, OFF_AB + 16384 + (p * 32 + h) * 32 + (l & 1) * 16 + (l >> 1) * 8192, 0);
              if (w == 7 && l < 24) st16(pz, rs, OFF_FX + ((h * 6 + (l >> 2)) * 8 + p) * 64 + (l & 3) * 16, 0);
              if (w == 6 && l < 4) st4(pz1, rs, l < 2 ? OFF_XNEW + bid * 8 + l * 4 : OFF_KEY + ((l & 1) * 16 + pct) * 128 + prb * 4, 0);
            }
            static_assert(vm_x(EIN + XL) >= vm_at(0, EIN - 3, 5) && IMG >= 1 && CSD >= 1, "first flag");
            if constexpr (s == EIN && n == 1) count_in(1);
            if constexpr (s == EIN + 2 && n == 0) qc = __hip_atomic_load(qcnt + 1, __ATOMIC_RELAXED, __HIP_MEMORY_SCOPE_WORKGROUP);
            if constexpr (s == EIN + 2 && n == 2) {
              if (w == 0) { count_wait(1, 8); if (l < RX) st8(T, rs, F_XN + (p * RX + l) * 256 + h * 8, 0); }
            }
            if constexpr (s == ERQ && n == 4) asm volatile("buffer_load_dwordx2 %0, %1, %2, 0 offen sc1" : "=v"(cxn) : "v"(XF_OFF() + F_XN), "s"(rs) : "memory");
            if constexpr (s == CSL && n == 2) {
              wait_vm<SINCE(ERQ)>();
              asm volatile("" : "+v"(cxn)::"memory");
              if (!__all(cxn[0] == T[0] && cxn[1] == T[1])) poll_flags(rs, XF_OFF(), F_XN, T);
            }
          } else {
#define X1_OFF() (F_X1 + (p * RX + (xcd >> 1) % RX) * 256 + (l & 31) * 8)
            if constexpr ((s == 0 || s == 4) && n == 2) asm volatile("buffer_load_dwordx2 %0, %1, %2, 0 offen sc1" : "=v"(cxn) : "v"(X1_OFF()), "s"(rs) : "memory");
            if constexpr ((s == 3 || s == 7) && n == 0) {
              wait_vm<vm_at(1, s, 1) - vm_at(1, s - 3, 3)>();
              asm volatile("" : "+v"(cxn)::"memory");
              xok |= __builtin_amdgcn_ballot_w64(cxn[0] == T[0] && cxn[1] == T[1]);
            }
            if constexpr (a_req(s, n) >= 0 && (a_req(s, n) & 7) == 0) x1_need(0x30003ull << (((rot + a_req(s, n) / 8) & 7) * 2));
          }
          if constexpr (IT && a_req(s, n) >= 0) A_LOAD1(a_req(s, n) / 2, a_req(s, n) & 1);
          if constexpr (IT == 0 && s == CSD) {
            if constexpr (n == 0) { DEF4(df); asm volatile("" : "=v"(sa), "=v"(sb)); if (w < 6) { for (int i = 0; i < 4; ++i) df[i] = frag0(SLOT(s >> 2), 6 * i + w); } }
            if constexpr (n == 2) {
              if (w < 6) {
                unsigned one2 = 0x3F803F80u;
                asm volatile("" : "+v"(one2));
                const u32x4 o4 = {one2, one2, one2, one2}, of[4] = {o4, o4, o4, o4};
                dot_mma(of);
              }
            }
            if constexpr (n == 4) { if (w < 6) dot_store(OFF_CS); }
          }
          if constexpr (!IT && s == CSL && n == 4) { asm volatile("" : "=v"(fx[0])); if (w < 3) ld16h<0>(fx[0], rs, l * 16, OFF_CS + h * 3072 + w * 1024); }
          if constexpr (!IT && s == CSS && n == 2) {
            if (w < 3) {
              wait_vm<SINCE(CSL)>();
              asm volatile("" : "+v"(fx[0])::"memory");
              tile_sum(fx[0], L_CSL);
            }
          }
          if constexpr (s == abl(IT) && n == 4) ld4h<0>(aq[0], rs, (l & 31) * 4 + (l >> 5) * 8192, AB_OFF());
          if constexpr (s == ABS && n == 0) {
            wait_vm<SINCE(abl(IT))>();
            asm volatile("" : "+v"(aq[0])::"memory");
            if constexpr (IT) { for (unsigned spins = 0; !__all(aq[0] != POISON) && !spin_fail(spins, rs, T);) aq[0] = ld4(rs, (l & 31) * 4 + (l >> 5) * 8192, AB_OFF()); }
            *(LDS_AS unsigned*)(smem + L_K + (8 + rt) * 2048 + l * 4) = aq[0];
          }
          if constexpr (s == ABS && n == 2) {
#pragma unroll
            for (int a = 0; a < 2; ++a) {
              const LDS_AS char* ab = (const LDS_AS char*)(smem + L_K + (8 + rt) * 2048 + a * 64);
              aq[a] = *(const LDS_AS unsigned*)(ab + (l & 15) * 4); bq[a] = *(const LDS_AS unsigned*)(ab + 128 + (l & 15) * 4);
              av[a] = *(const LDS_AS u32x4*)(ab + g * 16); bv[a] = *(const LDS_AS u32x4*)(ab + 128 + g * 16);
            }
          }
          if constexpr (s == ABS && n == 5) {
#pragma unroll
            for (int a = 0; a < 2; ++a) { b2[a] = f32x2{asf(bq[a]), asf(bq[a])}; asm volatile("" : "+v"(b2[a])); }
          }
          if constexpr (s == ABS + 1) {
            constexpr int k = (n + 4) % 6;
            if constexpr (k < 4) cs4[k & 1] = *(const LDS_AS f32x4*)(smem + L_CSL + k * 64 + g * 16);
            else cs4[k & 1][0] = *(const LDS_AS float*)(smem + L_CSL + k * 64 + (l & 15) * 4);
          }
          if constexpr ((s == ABS + 1 && n > 0) || (s == ABS + 2 && n == 0)) {
            constexpr int k = (n + 3) % 6;
            if constexpr (k < 4) {
#pragma unroll
              for (int a = 0; a < 2; ++a) acc[a][k] = __builtin_elementwise_fma(cs4[k & 1], f32x4{b2[a][0], b2[a][1], b2[a][0], b2[a][1]}, acc[a][k]);
            } else {
              f32x2 c2 = {cs4[k & 1][0], cs4[k & 1][0]};
              asm volatile("" : "+v"(c2));
#pragma unroll
              for (int a = 0; a < 2; ++a) acc[a][k] = __builtin_elementwise_fma(f32x4{c2[0], c2[1], c2[0], c2[1]}, __builtin_bit_cast(f32x4, bv[a]), acc[a][k]);
            }
          }
          if constexpr (IT == 0) {
            if constexpr (s >= KP0 && s < KP0 + 3 && (n & 1)) keep[3 * (s - KP0) + (n >> 1)] = frag0(SLOT(s - KP0 + 5), 3 * w + (n >> 1));
            if constexpr (s >= IMG && s < IMG + 3 && n == 4) st16(keep[s - IMG], rs, l * 16 + (s - IMG) * 1024, OFF_WI + (h * 8 + ((rot + 5) & 7)) * STAGE + w * 3072);
            if constexpr (s >= TS0 && s < TS0 + TSN && n == 4) {
              if (tstep == s && w < TW) touch(tdum, tsrc);
            }
            if constexpr (s == TST && n == 4) { if (w >= TW) touch(tdum, tsrc); }
          } else {
            if constexpr (s == 28 && n == 0) { POISON_BIG(1); }
            if constexpr (s == FSL && n == 4) { ld4h<0>(kv, rs, (l & 15) * 4, OFF_KEY + (bid & 15) * 128); row_load(rs, g, ffn_xoff(kg0, 0, w), fx); }
            if constexpr (s == FS0 && n == 0) {
              wait_vm<SINCE(FSL)>();
              asm volatile("" : "+v"(kv)::"memory");
              PINV4(fx);
              if (__builtin_expect(!__all(kv != POISON), 0)) {
                LDS_AS unsigned* kw = (LDS_AS unsigned*)(smem + L_SKEY + 40);
                if (w == 0) {
                  kv = key_wait(rs, T, l, 0, bid & 15, kv);
                } else {
                  while ((kv = __hip_atomic_load(kw, __ATOMIC_RELAXED, __HIP_MEMORY_SCOPE_WORKGROUP)) == POISON) __builtin_amdgcn_s_sleep(2);
                }
              }
              fkey = __builtin_amdgcn_readfirstlane(umax16(kv));
              if (w == 0) *(LDS_AS unsigned*)(smem + L_SKEY + 40) = min(fkey, POISON - 1);
              sel0 = key_row(fkey);
              aff = (int)(sel0 >> 8) == p;
              asm volatile("" : "=v"(sa), "=v"(sb));
              if (ffn_on) {
                if (__builtin_expect(((kg0 ^ fkey) & 0x780u) != 0, 0)) ld4x16(fx, rs, g * 16, ffn_xoff(fkey, 0, w));
                ffn_gu(gv, uv, fx, sa, sb);
              }
            }
            if constexpr (s == FS0 && n == 2) {
              if (ffn_on) {
                ffn_gu_check(rs, T, ffn_xoff(fkey, 0, w), l, gv, uv, fx, sa, sb);
                ffn_gu_store(smem, l, w, sa, sb);
                count_in(1);
              }
            }
            if constexpr (s == FS0D && n == FDG) {
              if (ffn_on && w < 4) {
                s16x4 da[8], db[8];
                ffn_d_read(smem, l, 2 * w, da);
                while (__hip_atomic_load(qcnt + 1, __ATOMIC_RELAXED, __HIP_MEMORY_SCOPE_WORKGROUP) < 8u) {}
                asm volatile("" ::: "memory");
                const f32x4 t = ffn_t_read(smem, l);
                ffn_d_read(smem, l, 2 * w + 1, db);
                u32x2 ha = ffn_h(t, l), hb = {dppu<0x118>(ha[0]), dppu<0x118>(ha[1])};
                f32x4 ya = {0.f, 0.f, 0.f, 0.f}, yb = ya;
#pragma unroll
                for (int i = 0; i < 8; ++i) {
                  ya = mfma16k16(da[i], __builtin_bit_cast(s16x4, ha), ya); yb = mfma16k16(db[i], __builtin_bit_cast(s16x4, hb), yb);
                  ha = u32x2{dppu<0x111>(ha[0]), dppu<0x111>(ha[1])}; hb = u32x2{dppu<0x111>(hb[0]), dppu<0x111>(hb[1])};
                }
                ffn_d_store(rs, l, 8 * w, 0, slice, ya + yb);
              }
            }
#define RED_LOAD(ld, l8)                                                                                                    \
  {                                                                                                                         \
    const unsigned G = xcd * 4 + bi - 28, po = OFF_PART + G * 32768;                                                        \
    ld(gv[0], rs, tid * 16, po); ld(gv[1], rs, tid * 16, po + 4096); ld(gv[2], rs, tid * 16, po + 8192); ld(gv[3], rs, tid * 16, po + 12288); \
    ld(uv[0], rs, tid * 16, po + 16384); ld(uv[1], rs, tid * 16, po + 20480); ld(uv[2], rs, tid * 16, po + 24576);        \
    l8(cxn, rs, (l & 3) * 16 + (l & 4) * 2, OFF_CA + (sel0 >> 7) * 2048 + G * 64);                                         \
    l8(cx1, rs, (l & 3) * 16 + (l & 4) * 2, OFF_CB + (sel0 >> 7) * 2048 + G * 64);                                          \
  }
            if constexpr (s == FS1 && n == 4) {
              asm volatile("" : "=v"(cxn), "=v"(cx1));
              if (!ffn_on && w < 4) RED_LOAD(ld16u, ld8h);
            }
            if constexpr (s == FS1S && n == 0) {
              if (!ffn_on && w < 4) {
                wait_vm<SINCE(FS1)>();
                PINV4(gv); PINV4(uv);
                asm volatile("" : "+v"(cxn), "+v"(cx1)::"memory");
                for (unsigned spins = 0; !__all(max(max(max(umax4(gv[0]), umax4(gv[1])), max(umax4(gv[2]), umax4(gv[3]))),
                                                    max(max(umax4(uv[0]), umax4(uv[1])), max(max(umax4(uv[2]), max(cxn[0], cxn[1])), max(cx1[0], cx1[1])))) != POISON) && !spin_fail(spins, rs, T);) {
                  RED_LOAD(ld16u, ld8h);
                  DRAIN();
                  PINV4(gv); PINV4(uv);
                  asm volatile("" : "+v"(cxn), "+v"(cx1)::"memory");
                }
#define F4(x) __builtin_bit_cast(f32x4, x)
                ((LDS_AS f32x4*)(smem + L_SFIN))[tid] = ((F4(gv[0]) + F4(gv[1])) + (F4(gv[2]) + F4(gv[3]))) + ((F4(uv[0]) + F4(uv[1])) + F4(uv[2]));
#undef F4
                count_in(2);
                if (w == 0) {
                  while (__hip_atomic_load(qcnt + 2, __ATOMIC_RELAXED, __HIP_MEMORY_SCOPE_WORKGROUP) < 4u) {}
                  asm volatile("" ::: "memory");
                  const u32x2 ov = red_final<4>(smem, l, fkey, cxn, cx1);
                  if (l < 8) st8(unpoison2(ov), rs, OFF_XNEW + ((xcd * 4 + bi - 28) * 8 + (l & 3) * 2 + (l >> 2)) * 8, 0);
                }
              }
            }
            if constexpr (s == FXA && n == 4) { DEF4(fx); if (w < 3) row_load(rs, g, XP_OFF(), fx); }
            if constexpr (s == FXB && n == 0) {
              if (w < 3) {
                const unsigned fa = bo[0][0] + SLOT(FXT) * STAGE + w * 2048;
                u32x4 f[8];
#pragma unroll
                for (int i = 0; i < 8; ++i) f[i] = *(const LDS_AS u32x4*)(size_t)(fa + (i >> 1) * 6144 + (i & 1) * 1024);
                wait_vm<SINCE(FXA)>();
                PINV4(fx);
                ffn_xwait<XSL>(rs, T, XP_OFF(), l, fx);
                f32x4 y0 = mfma16(f[0], fx[0], f32x4{0.f, 0.f, 0.f, 0.f}), y1 = mfma16(f[1], fx[0], f32x4{0.f, 0.f, 0.f, 0.f});
#pragma unroll
                for (int i = 1; i < 4; ++i) { y0 = mfma16(f[2 * i], fx[i], y0); y1 = mfma16(f[2 * i + 1], fx[i], y1); }
                if ((l & 14) == 0) st16(unpoison4(__builtin_bit_cast(u32x4, (l & 1) ? y1 : y0)), rs, g * 16 + (l & 1) * 512, OFF_FX + ((h * 6 + 2 * w) * 8 + p) * 64);
              }
            }
            if constexpr (s == FXL && n == 4) {
              asm volatile("" : "=v"(fx[0]), "=v"(fx[1]));
              if (aff && w < 4) { ld16h<0>(fx[0], rs, l * 16, FP_OFF(0)); ld16h<0>(fx[1], rs, l * 16, FP_OFF(1)); }
            }
            if constexpr (s == FXS && n == 2) {
              if (aff && w < 4) {
                wait_vm<0>();
                asm volatile("" : "+v"(fx[0]), "+v"(fx[1])::"memory");
                if (!FX_OK(2)) {
                  unsigned spins = 0;
                  do {
                    if (spin_fail(spins, rs, T)) break;
                    fx[0] = ld16(rs, l * 16, FP_OFF(0)); fx[1] = ld16(rs, l * 16, FP_OFF(1));
                  } while (!FX_OK(2));
                }
                if (w == 3) {
                  f32x2 nx[8];
                  unpack8v(fx[0], nx); unpack8v(fx[1], nx + 4);
                  const f32x2 sm = ((nx[0] + nx[1]) + (nx[2] + nx[3])) + ((nx[4] + nx[5]) + (nx[6] + nx[7]));
                  const float mean = sum64(hadd(sm)) * (1.0f / 1024.0f);
                  const f32x2 m2 = {mean, mean};
                  f32x2 q = {0.f, 0.f};
#pragma unroll
                  for (int j = 0; j < 8; ++j) { const f32x2 d = nx[j] - m2; q = __builtin_elementwise_fma(d, d, q); }
                  const float rd = ln_rd(sum64(hadd(q)) * (1.0f / 1024.0f));
                  if (l == 0) *(LDS_AS f32x2*)(smem + L_FXL + 384) = f32x2{rd, -rbf1(mean)};
                } else {
                  tile_sum(fx[0], L_FXL);
                }
                count_in(3);
              }
            }
          }
        };
        static_for<32>([&](auto sc_) __attribute__((always_inline)) {
          constexpr int s = decltype(sc_)::value;
          if constexpr (s > 0) PIN_ACC();
          if constexpr (IT == 1) wait_vm<vm_at(IT, s, 0) - vm_a(IT, s)>();
          pin_w<s % WA>(af[s % WA][0], af[s % WA][1]);
          SB();
          static_for<6>([&](auto nc_) __attribute__((always_inline)) {
            constexpr int n = decltype(nc_)::value;
            if constexpr (s == 0) { acc[0][n] = f32x4{0.f, 0.f, 0.f, 0.f}; acc[1][n] = acc[0][n]; }
            constexpr bool on = s < QL || n >= 2;
            if constexpr (on) acc[0][n] = n < 4 ? mfma16(bfr[n].v, af[s % WA][0], acc[0][n]) : mfma16(af[s % WA][0], bfr[n].v, acc[0][n]);
            SB();
            if constexpr (n == 0) { QKV_RDB(s, 5); }
            else if constexpr (s + 1 < 32 && (s + 1 < QL || n > 2)) QKV_RDB(s + 1, n - 1);
            SB();
            if constexpr (on) acc[1][n] = n < 4 ? mfma16(bfr[n].v, af[s % WA][1], acc[1][n]) : mfma16(af[s % WA][1], bfr[n].v, acc[1][n]);
            SB();
            if constexpr (s + 1 < 32 && (s + 1 < QL || n >= 2)) QKV_RDA(s + 1, n);
            SB();
            gap(sc_, nc_);
            SB();
          });
        });
        PIN_ACC();
        frag_t qw[32 - QL][2];
        static_for<32 - QL>([&](auto c_) __attribute__((always_inline)) {
          constexpr int s = QL + decltype(c_)::value;
          QKV_RDA(s, 0); QKV_RDB(s, 0); QKV_RDA(s, 1); QKV_RDB(s, 1);
          qw[s - QL][0] = bfr[0]; qw[s - QL][1] = bfr[1];
        });
        SB();
        const bool own = aff && (int)((sel0 >> 5) & 7) == rt;
        const bool m0 = !(sel0 & 16), mq = (l & 15) == (int)(sel0 & 15), mv = g == (int)((sel0 & 15) >> 2);
        auto patch_qk = [&](int n) __attribute__((always_inline)) {
          f32x4 t = *(const LDS_AS f32x4*)(smem + L_FXL + n * 64 + g * 16);
          const f32x4 c = *(const LDS_AS f32x4*)(smem + L_CSL + n * 64 + g * 16);
          const float nb = *(const LDS_AS float*)(smem + L_FXL + 388);
#pragma unroll
          for (int e = 0; e < 4; ++e) { t[e] = __builtin_fmaf(c[e], nb, t[e]); asm volatile("" : "+v"(t[e])); }
          acc[0][n] = (mq && m0) ? t : acc[0][n];
          acc[1][n] = (mq && !m0) ? t : acc[1][n];
        };
        if (own) {
          while (__hip_atomic_load(qcnt + 3, __ATOMIC_RELAXED, __HIP_MEMORY_SCOPE_WORKGROUP) < 4u) {}
          asm volatile("" ::: "memory");
          patch_qk(2); patch_qk(3);
          const f32x2 nab = *(const LDS_AS f32x2*)(smem + L_FXL + 384);
          const LDS_AS float* fl_ = (const LDS_AS float*)(smem + L_FXL + 256) + (l & 15);
          const LDS_AS float* cl_ = (const LDS_AS float*)(smem + L_CSL + 256) + (l & 15);
          const float t4 = __builtin_fmaf(cl_[0], nab[1], fl_[0]), t5 = __builtin_fmaf(cl_[16], nab[1], fl_[16]);
          const unsigned na = __builtin_bit_cast(unsigned, (float)nab[0]);
#pragma unroll
          for (int a = 0; a < 2; ++a) {
            const bool ma = a ? !m0 : m0;
            aq[a] = (mq && ma) ? na : aq[a];
#pragma unroll
            for (int e = 0; e < 4; ++e) {
              const bool me = mv && ma && e == (int)(sel0 & 3);
              acc[a][4][e] = me ? t4 : acc[a][4][e]; acc[a][5][e] = me ? t5 : acc[a][5][e];
              av[a][e] = me ? na : av[a][e];
            }
          }
        }
        constexpr float C2 = 1.4426950408889634f * 0.17677669529663687f;
#pragma unroll
        for (int a = 0; a < 2; ++a) {
          f32x2 a2 = {asf(aq[a]), asf(aq[a])};
          qs2[a] = f32x2{asf(aq[a]) * C2, asf(aq[a]) * C2};
          asm volatile("" : "+v"(a2), "+v"(qs2[a]));
          acc[a][2] *= f32x4{a2[0], a2[1], a2[0], a2[1]}; acc[a][3] *= f32x4{a2[0], a2[1], a2[0], a2[1]};
          acc[a][4] *= __builtin_bit_cast(f32x4, av[a]); acc[a][5] *= __builtin_bit_cast(f32x4, av[a]);
        }
        k0 = pack8(acc[0][2], acc[0][3]); k1 = pack8(acc[1][2], acc[1][3]); v0 = pack8(acc[0][4], acc[1][4]); v1 = pack8(acc[0][5], acc[1][5]);
        const unsigned kvo = OFF_KV + IT * (8u << 20) + (p * 32 + h) * 32768 + rt * 4096;
        if (p < 7) { st16(k0, rs, l * 16, kvo); st16(k1, rs, l * 16 + 1024, kvo); st16(v0, rs, l * 16 + 2048, kvo); st16(v1, rs, l * 16 + 3072, kvo); }
        char* ownp = smem + (8 + rt) * 2048 + l * 16;
        *(u32x4*)(ownp + L_K) = k0;
        if (rt < 7) *(u32x4*)(ownp + L_K + 1024) = k1;
        *(u32x4*)(ownp + L_V) = v0; *(u32x4*)(ownp + L_V + 1024) = v1;
        SB();
        static_for<32 - QL>([&](auto c_) __attribute__((always_inline)) {
          constexpr int s = QL + decltype(c_)::value;
#pragma unroll
          for (int n = 0; n < 2; ++n) { acc[0][n] = mfma16(qw[s - QL][n].v, af[s % WA][0], acc[0][n]); acc[1][n] = mfma16(qw[s - QL][n].v, af[s % WA][1], acc[1][n]); }
        });
        if (own) { patch_qk(0); patch_qk(1); }
      };
      qkv_loop(it_c);
    }
#undef A_LOAD1
#undef QKV_RDA
#undef QKV_RDB
#undef QKV_BO
    {
      IDS();
      const GAS bf16 *pwo, *pwg, *pwu;
      asm volatile("s_load_dwordx2 %0, %3, 24\n\ts_load_dwordx2 %1, %3, 40\n\ts_load_dwordx2 %2, %3, 48" : "=&s"(pwo), "=&s"(pwg), "=&s"(pwu) : "s"(ka));
      const int ql = l & 15;
      const f32x4 z4 = {0.f, 0.f, 0.f, 0.f}, ninf = {-INFINITY, -INFINITY, -INFINITY, -INFINITY};
      const unsigned kvo = OFF_KV + it * (8u << 20) + (p * 32 + h) * 32768 + rt * 4096;
      LDS_AS unsigned* rdy = (LDS_AS unsigned*)(smem + L_RDY);
      const unsigned rtag = T[0] + it;
      u32x4 qf[2];
      f32x4 cn[2], oa[2][2], sum[2], S[2][2];
      unsigned one2 = 0x3F803F80u;
      asm volatile("" : "+v"(one2));
      const u32x4 ones = {one2, one2, one2, one2};
      auto ex8 = [&](f32x4 a, f32x4 b) __attribute__((always_inline)) {
        return u32x4{pack2(__builtin_amdgcn_exp2f(a[0]), __builtin_amdgcn_exp2f(a[1])), pack2(__builtin_amdgcn_exp2f(a[2]), __builtin_amdgcn_exp2f(a[3])),
                     pack2(__builtin_amdgcn_exp2f(b[0]), __builtin_amdgcn_exp2f(b[1])), pack2(__builtin_amdgcn_exp2f(b[2]), __builtin_amdgcn_exp2f(b[3]))};
      };
      {
#pragma unroll
        for (int a = 0; a < 2; ++a) {
          const f32x4 c4 = {qs2[a][0], qs2[a][1], qs2[a][0], qs2[a][1]};
          qf[a] = pack8(acc[a][0] * c4, acc[a][1] * c4);
        }
        f32x4 mk;
#pragma unroll
        for (int e = 0; e < 4; ++e) mk[e] = (4 * g + e <= ql) ? 0.f : -INFINITY;
        const f32x4 d0 = mfma16(k0, qf[0], mk), d1 = mfma16(k1, qf[1], mk), d2 = mfma16(k0, qf[1], z4);
        const float m0 = -max_x32(max_x16(fmaxf(fmaxf(d0[0], d0[1]), fmaxf(d0[2], d0[3]))));
        const float m1 = -max_x32(max_x16(fmaxf(fmaxf(d1[0], d1[1]), fmaxf(d1[2], d1[3]))));
        cn[0] = f32x4{m0, m0, m0, m0}; cn[1] = f32x4{m1, m1, m1, m1};
        asm volatile("" : "+v"(cn[0]), "+v"(cn[1]));
        const f32x4 e0 = d0 + cn[0];
        const u32x4 p0 = {pack2(__builtin_amdgcn_exp2f(e0[0]), __builtin_amdgcn_exp2f(e0[1])), pack2(__builtin_amdgcn_exp2f(e0[2]), __builtin_amdgcn_exp2f(e0[3])), 0u, 0u};
        const u32x4 p1 = ex8(d2 + cn[1], d1 + cn[1]);
        oa[0][0] = mfma16(v0, p0, z4); oa[0][1] = mfma16(v1, p0, z4); sum[0] = mfma16(ones, p0, z4);
        oa[1][0] = mfma16(v0, p1, z4); oa[1][1] = mfma16(v1, p1, z4); sum[1] = mfma16(ones, p1, z4);
      }
      const char* kb_ = smem + L_K + l * 16;
      const char* vb_ = smem + L_V + l * 16;
      const unsigned wov = (l >> 3) * 2048 + (((((l & 7) >> 1) ^ ((l >> 4) & 3)) * 2 + (l & 1)) << 4), fnv = (l >> 1) * (F_ * 2) + (l & 1) * 16;
      const unsigned pp = __builtin_amdgcn_readfirstlane(p > 0 ? 32 : 0), pvo = kvo - pp * 32768;
      const unsigned pfo = __builtin_amdgcn_readfirstlane(F_KV + (it * 256 + p * 32 + h - pp) * 128 + rt * 8);
      asm volatile("" : "+v"(oa[0][0]), "+v"(oa[0][1]), "+v"(oa[1][0]), "+v"(oa[1][1]), "+v"(sum[0]), "+v"(sum[1]));
      DRAIN();
      asm volatile("" : "+v"(tdum));
      if (l == 0) st8(T, rs, rt * 8, F_KV + (it * 256 + p * 32 + h) * 128);
      BAR();
      asm volatile("" : "+s"(pwo), "+s"(pwg), "+s"(pwu));
      WO_PIECE(15); WO_PIECE(14);
#define KV_FETCH()                                                                                \
  {                                                                                               \
    dma16s<0>(rs, l * 16, pvo, lbase + L_K + rt * 2048); dma16s<1024>(rs, l * 16, pvo, lbase + L_K + rt * 2048);               \
    dma16s<0>(rs, l * 16, pvo + 2048, lbase + L_V + rt * 2048); dma16s<1024>(rs, l * 16, pvo + 2048, lbase + L_V + rt * 2048); \
  }
#define LDS16(a_) (*(const LDS_AS u32x4*)(size_t)(a_))
      u32x4 kf[2], vf[2];
      int nw = 0;
      auto body = [&](unsigned kad, unsigned vad) __attribute__((always_inline)) {
        u32x4 p0 = ex8(S[0][0], S[0][1]);
        asm volatile("" : "+v"(p0));
        SB();
        S[0][0] = mfma16(kf[0], qf[0], cn[0]); S[0][1] = mfma16(kf[1], qf[0], cn[0]);
        SB();
        oa[0][0] = mfma16(vf[0], p0, oa[0][0]); oa[0][1] = mfma16(vf[1], p0, oa[0][1]); sum[0] = mfma16(ones, p0, sum[0]);
        SB();
        u32x4 p1 = ex8(S[1][0], S[1][1]);
        asm volatile("" : "+v"(p1));
        SB();
        S[1][0] = mfma16(kf[0], qf[1], cn[1]); S[1][1] = mfma16(kf[1], qf[1], cn[1]);
        SB();
        kf[0] = LDS16(kad); kf[1] = LDS16(kad + 1024);
        SB();
        oa[1][0] = mfma16(vf[0], p1, oa[1][0]); oa[1][1] = mfma16(vf[1], p1, oa[1][1]);
        SB();
        vf[0] = LDS16(vad + L_V); vf[1] = LDS16(vad + L_V + 1024);
        SB();
        sum[1] = mfma16(ones, p1, sum[1]);
      };
      auto last = [&]() __attribute__((always_inline)) {
        const u32x4 p0 = ex8(S[0][0], S[0][1]), p1 = ex8(S[1][0], S[1][1]);
        oa[0][0] = mfma16(vf[0], p0, oa[0][0]); oa[0][1] = mfma16(vf[1], p0, oa[0][1]); sum[0] = mfma16(ones, p0, sum[0]);
        oa[1][0] = mfma16(vf[0], p1, oa[1][0]); oa[1][1] = mfma16(vf[1], p1, oa[1][1]); sum[1] = mfma16(ones, p1, sum[1]);
      };
      auto piece = [&]() __attribute__((always_inline)) {
        if constexpr (it == 1) { WO_PIECE(13 - nw); }
        else if (ffn_on) { FFN_DMA(nw); }
        ++nw;
      };
      if (rt > 0) {
        unsigned ad = lbase + l * 16 + (7 + rt) * 2048;
        kf[0] = LDS16(ad); kf[1] = LDS16(ad + 1024); vf[0] = LDS16(ad + L_V); vf[1] = LDS16(ad + L_V + 1024);
        S[0][0] = mfma16(kf[0], qf[0], cn[0]); S[0][1] = mfma16(kf[1], qf[0], cn[0]); S[1][0] = mfma16(kf[0], qf[1], cn[1]); S[1][1] = mfma16(kf[1], qf[1], cn[1]);
        SB();
        ad -= 2048;
        kf[0] = LDS16(ad); kf[1] = LDS16(ad + 1024);
#pragma unroll 1
        for (int i = rt - 1; i > 0; --i) {
          body(ad - 2048, ad);
          piece();
          SB();
          ad -= 2048;
        }
        piece();
        last();
      }
      {
        poll_flags(rs, 0, pfo, T);
        KV_FETCH();
        if (p == 0) { cn[0] = ninf; cn[1] = ninf; }
        f32x4 mk;
#pragma unroll
        for (int e = 0; e < 4; ++e) mk[e] = (4 * g + e > ql) ? 0.f : -INFINITY;
        const f32x4 cm0 = cn[0] + mk, cm1 = cn[1] + mk;
        DRAIN();
        if (l == 0) rdy[rt] = rtag;
        if constexpr (it == 1) { WO_PIECE(6); WO_PIECE(5); WO_PIECE(4); }
        else { WO_PIECE(13); WO_PIECE(12); if (ffn_on) FFN_DMA(7); }

        int have = rt;
        auto need = [&](int gq) __attribute__((always_inline)) {
          if (__builtin_expect(gq > have && gq < 8, 0)) {
            do {
              const unsigned t = __hip_atomic_load(rdy + (l & 7), __ATOMIC_RELAXED, __HIP_MEMORY_SCOPE_WORKGROUP);
              have = rt - 1 + __builtin_ctz(~((unsigned)__builtin_amdgcn_ballot_w64(t == rtag) & 0xffu) >> rt);
            } while (gq > have);
            asm volatile("" ::: "memory");
          }
        };
        unsigned ad = lbase + l * 16 + rt * 2048;
        kf[0] = LDS16(ad); kf[1] = LDS16(ad + 1024); vf[0] = LDS16(ad + L_V); vf[1] = LDS16(ad + L_V + 1024);
        S[0][0] = mfma16(kf[0], qf[0], cm0); S[0][1] = mfma16(kf[1], qf[0], cn[0]); S[1][0] = ninf; S[1][1] = mfma16(kf[1], qf[1], cm1);
        SB();
        ad += 2048;
        need(rt + 1);
        kf[0] = LDS16(ad); kf[1] = LDS16(ad + 1024);
#pragma unroll 1
        for (int gq = rt + 1; gq < 8; ++gq) {
          need(gq + 1);
          body(ad + 2048, ad);
          piece();
          SB();
          ad += 2048;
        }
        last();
      }
      {
        float big = 0.f;
#pragma unroll
        for (int a = 0; a < 2; ++a)
#pragma unroll
          for (int d = 0; d < 2; ++d)
            big = __builtin_elementwise_maximum(__builtin_elementwise_maximum(big, __builtin_elementwise_maximum(fabsf(oa[a][d][0]), fabsf(oa[a][d][1]))),
                                                __builtin_elementwise_maximum(fabsf(oa[a][d][2]), fabsf(oa[a][d][3])));
        bool ok = sum[0][0] >= 0.5f && sum[0][0] < 0x1p64f && sum[1][0] >= 0.5f && sum[1][0] < 0x1p64f && big < INFINITY;
        if (!__all(ok)) {
#pragma unroll
          for (int a = 0; a < 2; ++a) {
            const int qs = 2 * rt + a, qq = 256 + 16 * qs + ql, klo = max(qq - 255, p > 0 ? 0 : 256);
            auto score = [&](int t) __attribute__((always_inline)) {
              f32x4 s = mfma16(t == 31 ? k1 : *(const u32x4*)(kb_ + t * 1024), qf[a], z4);
#pragma unroll
              for (int e = 0; e < 4; ++e) { const int kk = 16 * t + 4 * g + e; s[e] = (kk <= qq && kk >= klo) ? s[e] : -INFINITY; }
              return s;
            };
            float mx = -INFINITY;
#pragma unroll 1
            for (int t = qs; t <= qs + 16; ++t) { const f32x4 s = score(t); mx = fmaxf(fmaxf(mx, fmaxf(s[0], s[1])), fmaxf(s[2], s[3])); }
            mx = max_x32(max_x16(mx));
            oa[a][0] = z4; oa[a][1] = z4; sum[a] = z4;
#pragma unroll 1
            for (int G = qs >> 1; G <= (qs + 16) >> 1; ++G) {
              const f32x4 s0 = score(2 * G), s1 = score(2 * G + 1);
              f32x4 e0, e1;
#pragma unroll
              for (int e = 0; e < 4; ++e) { e0[e] = __builtin_amdgcn_exp2f(s0[e] - mx); e1[e] = __builtin_amdgcn_exp2f(s1[e] - mx); }
              const u32x4 pf = pack8(e0, e1);
              oa[a][0] = mfma16(*(const u32x4*)(vb_ + G * 2048), pf, oa[a][0]);
              oa[a][1] = mfma16(*(const u32x4*)(vb_ + G * 2048 + 1024), pf, oa[a][1]);
              sum[a] = mfma16(ones, pf, sum[a]);
            }
          }
        }
      }
#pragma unroll
      for (int a = 0; a < 2; ++a) {
        const float inv = __builtin_amdgcn_rcpf(sum[a][0]);
        oa[a][0] *= inv; oa[a][1] *= inv;
      }
      const unsigned oo = OFF_O + it * (4u << 20) + ((p * 8 + rt) * 64 + h * 2) * 1024;
      st16(pack8(oa[0][0], oa[0][1]), rs, l * 16, oo);
      st16(pack8(oa[1][0], oa[1][1]), rs, l * 16 + 1024, oo);
      DRAIN();
      BAR();
      if (tid == 0) st8(T, rs, h * 8, F_O + (it * 8 + p) * 256);
      if constexpr (it == 0) {
        DEF4(gv); DEF4(uv);
        if (ffn_on) {
          const unsigned ro = 65536 + w * 1024 + (4 * g + ((l & 15) >> 2)) * 32 + (l & 3) * 8;
#pragma unroll
          for (int i = 0; i < 4; ++i) {
            frag_t f;
            f.hf.a = trread(smem, ro + i * 16384); f.hf.b = trread(smem, ro + i * 16384 + 512);
            gv[i] = f.v;
            f.hf.a = trread(smem, ro + i * 16384 + 8192); f.hf.b = trread(smem, ro + i * 16384 + 8704);
            uv[i] = f.v;
          }
        }
        PINV4(gv); PINV4(uv);
        {
          ARGP(px, 0);
          const unsigned xo = ((rb * 4 + (w & 3)) * 32 + (w >> 2) * 16) * 2048 + ct * 128, xw = (l >> 3) * 2048 + (((l & 7) ^ g) << 4);
          dma16(RSRC(px, 2048 * 1024 * 2), xw, xo, lbase + w * 4096); dma16(RSRC(px, 2048 * 1024 * 2), xw ^ 64, xo + 16384, lbase + w * 4096 + 1024);
        }
      }
      WO_PIECE(3); WO_PIECE(2); WO_PIECE(1); WO_PIECE(0);
      if constexpr (it == 0) { WO_PIECE(11); WO_PIECE(10); WO_PIECE(9); WO_PIECE(8); WO_PIECE(7); WO_PIECE(6); WO_PIECE(5); WO_PIECE(4); }
    }
    IDS();
    ARGP(pwr, 4); ARGP(pwd, 7); ARGP(pout, 8);
    const int kh = w >> 2, rt2 = rb * 4 + (w & 3);
    const unsigned R = rt2 * 32 + kh * 16 + (l & 15);
    const unsigned to = (rt2 * 64 + ct * 4 + kh) * 1024 + l * 16;
    const unsigned rmo = ct * 64 + ((g & 1) ? 12 + 4 * g : 4 * g);
    f32x2 v[8], wv[8];
    float wsum;
    {
      u32x4 wa[2], wrp[2], res[2];
#pragma unroll
      for (int j = 0; j < 2; ++j) {
        wrp[j] = *(const GAS u32x4*)(pwr + rmo + 32 * j);
        wa[j] = *(const GAS u32x4*)(pwr + l * 8 + j * 512);
      }
      {
        f32x2 t[8];
        unpack8v(wa[0], t); unpack8v(wa[1], t + 4);
        const f32x2 s = ((t[0] + t[1]) + (t[2] + t[3])) + ((t[4] + t[5]) + (t[6] + t[7]));
        wsum = sum64(hadd(s));
        unpack8v(trade16(wrp[0]), wv); unpack8v(trade16(wrp[1]), wv + 4);
        asm volatile("" : "+v"(wsum), "+v"(wv[0]), "+v"(wv[1]), "+v"(wv[2]), "+v"(wv[3]), "+v"(wv[4]), "+v"(wv[5]), "+v"(wv[6]), "+v"(wv[7]));
      }
      unsigned bt[4];
#pragma unroll
      for (int n = 0; n < 4; ++n) {
        bt[n] = lbase + 32768 + 65536 + kh * 4096 + (4 * g + ((l & 15) >> 2)) * 128 + (l & 3) * 8 + ((n ^ (2 * (g & 1) + ((l & 15) >> 3))) << 5);
        asm volatile("" : "+v"(bt[n]));
      }
      if (w == 0) poll_flags(rs, (l & 31) * 8, F_O + (it * 8 + (rb >> 1)) * 256, T);
      else if (it == 0 && w < 4) poll_flags2(rs, w < 3 ? F_XN + (((w - 1) * 4 + g) * RX + bi % RX) * 256 + (l & 15) * 16 : F_KV + (((l >> 2) & 7) * 32 + h) * 128 + (l & 3) * 16, T);
      asm volatile("s_barrier" ::: "memory");
      const unsigned abase = OFF_O + it * (4u << 20) + (rt2 * 64 + 32 * kh) * 1024;
      const unsigned av0 = l * 16 + kh * 1024, av1 = l * 16 + (kh ^ 1) * 1024;
      u32x4 af[WB][2];
#define O_LOAD(s_) { ld16h<(wo_k(it, s_) & 1) * 2048>(af[(s_) % WB][0], rs, av0, abase + (wo_k(it, s_) >> 1) * 4096); ld16h<(wo_k(it, s_) & 1) * 2048>(af[(s_) % WB][1], rs, av1, abase + (wo_k(it, s_) >> 1) * 4096); }
      static_assert(WR == 4 && WB == 8, "wo_at");
      O_LOAD(0); O_LOAD(1); O_LOAD(2); O_LOAD(3);
      if constexpr (it == 1) { ld16h<0>(res[0], rs, g * 16, OFF_XNEW + ct * 128); ld16h<64>(res[1], rs, g * 16, OFF_XNEW + ct * 128); }
      SB();
      f32x4 ac[2][4];
#pragma unroll
      for (int a = 0; a < 2; ++a)
#pragma unroll
        for (int n = 0; n < 4; ++n) ac[a][n] = f32x4{0.f, 0.f, 0.f, 0.f};
      u32x4 bfr[2][4];
#define WO_RD(buf, s, n)                                                                             \
  {                                                                                                  \
    const unsigned o_ = bt[n] + (wo_k(it, s) & 7) * 8192;                                             \
    frag_t f_;                                                                                       \
    f_.hf.a = trabs(o_); f_.hf.b = trabs(o_ + 2048);                                                 \
    bfr[buf][n] = f_.v;                                                                              \
  }
#pragma unroll
      for (int n = 0; n < 4; ++n) WO_RD(0, 0, n);
      static_for<16>([&](auto sc_) __attribute__((always_inline)) {
        constexpr int s = decltype(sc_)::value, cur = s & 1;
        pin_ac(ac);
        if (s == (it == 0 ? 3 : 11)) asm volatile("s_barrier" ::: "memory");
        if constexpr (s < 15 && ((wo_k(it, s) ^ wo_k(it, s + 1)) & 8)) {
#pragma unroll
          for (int n = 0; n < 4; ++n) { bt[n] += (wo_k(it, s) & 8) ? -65536 : 65536; pin1(bt[n]); }
        }
        if constexpr (s < WB - WR) O_LOAD(WR + 2 * s);
        if constexpr (it == 0 && s == 8) {
          asm volatile("s_barrier" ::: "memory");
          WD_DMA();
        }
        wait_vm<wo_at(it, s) - wo_pos(it, s)>();
        pin2(af[s % WB][0], af[s % WB][1]);
        if constexpr (it == 1 && s == 9) pin2(res[0], res[1]);
        SB();
#pragma unroll
        for (int n = 0; n < 4; ++n) {
          ac[0][n] = mfma16(bfr[cur][n], af[s % WB][0], ac[0][n]);
          ac[1][n] = mfma16(bfr[cur][n], af[s % WB][1], ac[1][n]);
          if (s + 1 < 16) WO_RD(cur ^ 1, s + 1, n);
          if (it == 0 && s == 8) {
            const u32x2 t = *(const LDS_AS u32x2*)(smem + w * 4096 + (l & 15) * 128 + (((2 * n + (g >> 1)) ^ ((l & 15) >> 1)) << 4) + (g & 1) * 8);
            res[n >> 1][2 * (n & 1)] = t[0]; res[n >> 1][2 * (n & 1) + 1] = t[1];
          }
          if (s == 9) {
            if (it == 1 && n == 0) { res[0] = R == sel0 ? res[0] : x2p[0]; res[1] = R == sel0 ? res[1] : x2p[1]; }

            v[2 * n] = f32x2{lo16(res[n >> 1][2 * (n & 1)]), hi16(res[n >> 1][2 * (n & 1)])};
            v[2 * n + 1] = f32x2{lo16(res[n >> 1][2 * (n & 1) + 1]), hi16(res[n >> 1][2 * (n & 1) + 1])};
            pin2f(v[2 * n], v[2 * n + 1]);
          }
          SB();
        }
        if constexpr (s < WB - WR) { O_LOAD(WR + 2 * s + 1); }
        else if constexpr (s + WB < 16) { O_LOAD(s + WB); }
        SB();
      });
#undef WO_RD
#undef O_LOAD
#define XCH_AT(v) ((v) == 7 ? 155648 : (v) * 4096)
#pragma unroll
      for (int n = 0; n < 4; ++n) *(f32x4*)(smem + XCH_AT(w) + n * 1024 + l * 16) = ac[1][n];
#pragma unroll
      for (int n = 0; n < 4; ++n) { v[2 * n] += f32x2{ac[0][n][0], ac[0][n][1]}; v[2 * n + 1] += f32x2{ac[0][n][2], ac[0][n][3]}; }
      BAR();
#pragma unroll
      for (int n = 0; n < 4; ++n) {
        const f32x4 o = *(const f32x4*)(smem + XCH_AT(w ^ 4) + n * 1024 + l * 16);
        v[2 * n] += f32x2{o[0], o[1]}; v[2 * n + 1] += f32x2{o[2], o[3]};
      }
    }
    {
      const f32x2 s = ((v[0] + v[1]) + (v[2] + v[3])) + ((v[4] + v[5]) + (v[6] + v[7]));
      f32x2 dp = v[0] * wv[0], q = v[0] * v[0];
#pragma unroll
      for (int j = 1; j < 8; ++j) { dp = __builtin_elementwise_fma(v[j], wv[j], dp); q = __builtin_elementwise_fma(v[j], v[j], q); }
      float mt = hadd(s), qq = hadd(q), dot = hadd(dp);
      add_g3(mt, qq, dot);
      const unsigned so = OFF_STAT + it * 524288 + rb * 32768, lrow = R & 127;
      if (g == 0) st16(unpoison4(__builtin_bit_cast(u32x4, f32x4{mt, qq, dot, 0.f})), rs, ct * 2048 + lrow * 16, so);
      SB();
      if (it == 0) { st16(pack8v(v), rs, to, OFF_XN + (4u << 20)); st16(pack8v(v + 4), rs, to + 2048, OFF_XN + (4u << 20)); }
      SB();
      if (it == 0) wait_vm<0>();
      if constexpr (it == 0) {
        volatile LDS_AS u32x2* in = (volatile LDS_AS u32x2*)(smem + L_XC);
        if (l == 0) in[w] = T;
        const u32x2 t = in[l & 7];
        if (__all(t[0] == T[0] && t[1] == T[1]) && l < RX) st8(T, rs, l * 256 + ((rb & 1) * 16 + ct) * 8, F_X1 + (it * 8 + (rb >> 1)) * RX * 256);
      }
      if (it == 1) { if (ffn_on) { WD_DMA(); } }
      if (it == 0) {
#pragma unroll
        for (int j = 0; j < 9; ++j) *(u32x4*)(smem + L_RING + (j / 3) * STAGE + (w * 3 + j % 3) * 1024 + l * 16) = keep[j];
      }
      __builtin_amdgcn_s_sleep(it == 0 ? SDI0 : SDIR);
      char* const skey = smem + (it == 0 ? L_SKEY : L_XC);
      u32x4 sp[4];
      ld16h<0>(sp[0], rs, g * 8192 + lrow * 16, so); ld16h<2048>(sp[1], rs, g * 8192 + lrow * 16, so);
      ld16h<0>(sp[2], rs, g * 8192 + lrow * 16, so + 4096); ld16h<2048>(sp[3], rs, g * 8192 + lrow * 16, so + 4096);
      u32x2 xf;
      unsigned k_;
      asm volatile("" : "=v"(xf), "=v"(k_));
      if (it == 0 && w == 0) { ld8h(xf, rs, (l & 31) * 8, F_X1 + (p * RX + (xcd >> 1) % RX) * 256); ld4h<0>(k_, rs, (l & 15) * 4, OFF_KEY + (bid & 15) * 128); }
      DRAIN();
      asm volatile("" : "+v"(xf), "+v"(k_)::"memory");
      PINV4(sp);
      const RowFin r = fin_key(rs, T, g * 8192 + lrow * 16, so, skey, l, w, R, wsum, sp);
      if constexpr (it == 0) {
        if (w == 0) {
          const unsigned need = 0x30003u << (((PROT() + 5) & 7) * 2);
          unsigned m = (unsigned)__builtin_amdgcn_ballot_w64(xf[0] == T[0] && xf[1] == T[1]);
          for (unsigned spins = 0; (m & need) != need;) {
            if (spin_fail(spins, rs, T)) { m = ~0u; break; }
            const u32x2 f = ld8(rs, F_X1 + (p * RX + (xcd >> 1) % RX) * 256 + (l & 31) * 8, 0);
            k_ = key_load(rs, l, 0, bid & 15);
            m = (unsigned)__builtin_amdgcn_ballot_w64(f[0] == T[0] && f[1] == T[1]);
          }
          k_ = umax16(k_ == POISON ? 0u : k_);
          if (l == 0) *(LDS_AS u32x4*)(skey + 32) = u32x4{m, k_, POISON, 0u};
        }
        if (tid == 192) *(LDS_AS u32x4*)(smem + L_QC) = u32x4{0u, 0u, 0u, 0u};
      }
      BAR();
      if constexpr (it == 0) {
        PRE_LOAD(1, PROT() + 5, 0, 4);
        const u32x2 mk = *(const LDS_AS u32x2*)(skey + 32);
        xm0 = __builtin_amdgcn_readfirstlane(mk[0]); kg0 = __builtin_amdgcn_readfirstlane(mk[1]);
      }
      const unsigned own = fin_cand(rs, skey, it, g, rb, ct, r, v);
      if constexpr (it == 0) kg0 = max(kg0, (unsigned)__builtin_amdgcn_readfirstlane(own));
      fin_x2(r, v);
      if constexpr (it == 0) {
        x2p[0] = pack8v(v); x2p[1] = pack8v(v + 4);
        asm volatile("" : "+v"(x2p[0]), "+v"(x2p[1]));
        if ((w & 3) * 2 + (w >> 2) == ct >> 1) {
          const float f = 1.0f + r.rd, rd2 = ln_rd(r.var * f * f), mb2 = rbf1(__builtin_fmaf(r.mean - r.mb, r.rd, r.mean));
          if (g < 2 && ((l >> 3) & 1) == (ct & 1))
            st4(min(__builtin_bit_cast(unsigned, g == 0 ? f * rd2 : -__builtin_fmaf(r.mb, r.rd, mb2) * __builtin_amdgcn_rcpf(f)), POISON - 1), rs, OFF_AB + 16384 + R * 4 + g * 8192, 0);
        }
      }
    }
    if (it == 1) {
      const u32x4 op0 = trade16(pack8v(v)), op1 = trade16(pack8v(v + 4));
#define OUT_STORE()                                                                                 \
  if (R != key_row(key)) {                                                                          \
    __builtin_amdgcn_raw_buffer_store_b128(op0, rout, (R * 1024 + rmo) * 2, 0, SC1);                  \
    __builtin_amdgcn_raw_buffer_store_b128(op1, rout, (R * 1024 + rmo + 32) * 2, 0, SC1);             \
  }
      u32x4 xv[4];
      unsigned guess = 0;
      {
        __builtin_amdgcn_s_sleep(GSL);
        const unsigned k_ = key_load(rs, l, 1, bid & 15);
        guess = ffn_xoff(__builtin_amdgcn_readfirstlane(umax16(k_ == POISON ? 0u : k_)), 1, w);
        row_load(rs, g, guess, xv);
      }
      if (w == 0) {
        const unsigned k_ = key_wait(rs, T, l, 1, bid & 15, key_load(rs, l, 1, bid & 15));
        if (l == 0) *(LDS_AS unsigned*)(smem + L_SKEY + 32) = k_;
      }
      DRAIN();
      PINV4(xv);
      BAR();
      const unsigned key = __builtin_amdgcn_readfirstlane(*(LDS_AS unsigned*)(smem + L_SKEY + 32));
      if (ffn_on) {
        f32x4 ga, ua;
        s16x4 dfr[8];
        ffn_d_read(smem, l, w, dfr);
        if (guess != ffn_xoff(key, 1, w)) ld4x16(xv, rs, g * 16, ffn_xoff(key, 1, w));
        ffn_gu(gv, uv, xv, ga, ua);
        ffn_gu_check(rs, T, ffn_xoff(key, 1, w), l, gv, uv, xv, ga, ua);
        ffn_gu_store(smem, l, w, ga, ua);
        BAR();
        f32x4 ya = {0.f, 0.f, 0.f, 0.f}, yb = ya;
        u32x2 hh = ffn_h(ffn_t_read(smem, l), l);
        ffn_d_mma(dfr, hh, ya, yb);
        if ((l & 15) < 8) ffn_d_store(rs, l, 4 * w, 1, slice, ya + yb);
        SB();
        OUT_STORE();
      } else {
        OUT_STORE();
        const unsigned G = xcd * 4 + bi - 28;
        if (G == 0 && tid == 0) st8(T, rs, F_EP, 0);
        u32x4 pv[4];
        u32x2 cxn, cx1;
        __builtin_amdgcn_s_sleep(RSL);
        red_load(rs, tid, 1, G, key, pv, cxn, cx1);
        red_sum(rs, smem, T, tid, w, 1, G, key, pv, cxn, cx1);
        BAR();
        if (w == 0) {
          const u32x2 ov = red_final<8>(smem, l, key, cxn, cx1);
          if (l < 8) __builtin_amdgcn_raw_buffer_store_b64(ov, rout, (key_row(key) * 1024 + G * 32 + l * 4) * 2, 0, 0);
        }
      }
#undef OUT_STORE
    }
  };
  layer(std::integral_constant<int, 0>{});
  layer(std::integral_constant<int, 1>{});
}

extern "C" void kernel_launch(void* const* d_in, const int* in_sizes, int n_in, void* d_out, int out_size,
                              void* d_ws, size_t ws_size, hipStream_t stream) {
  (void)hipFuncSetAttribute((const void*)mega_kernel, hipFuncAttributeMaxDynamicSharedMemorySize, LDS_BYTES);
  mega_kernel<<<256, 512, LDS_BYTES, stream>>>((const bf16*)d_in[0], (char*)d_ws, (const bf16*)d_in[1], (const bf16*)d_in[2], (const bf16*)d_in[4],
                                               (const bf16*)d_in[5], (const bf16*)d_in[6], (const bf16*)d_in[7], (bf16*)d_out);
}
```

```cpp
#include <hip/hip_runtime.h>
#include <stdint.h>
#include <utility>

typedef __bf16 bf16;
typedef __bf16 bf16x8 __attribute__((ext_vector_type(8)));
typedef __bf16 bf16x2 __attribute__((ext_vector_type(2)));
typedef float f32x4 __attribute__((ext_vector_type(4)));
typedef float f32x2 __attribute__((ext_vector_type(2)));
typedef short s16x4 __attribute__((ext_vector_type(4)));
typedef unsigned int u32x4 __attribute__((ext_vector_type(4)));
typedef unsigned int u32x2 __attribute__((ext_vector_type(2)));
#define LDS_AS __attribute__((address_space(3)))
#define GAS __attribute__((address_space(1)))
typedef GAS unsigned gu32;
typedef GAS unsigned long long gu64;
#define RLX_AGENT __ATOMIC_RELAXED, __HIP_MEMORY_SCOPE_AGENT
typedef __amdgpu_buffer_rsrc_t rsrc_t;

constexpr int cmax(int a, int b) { return a > b ? a : b; }
constexpr int F_ = 3584;
constexpr int SC1 = 16;

constexpr int RX = 4;
constexpr unsigned F_EP = 0, F_TMO = 128;
constexpr unsigned F_XN = 4096;
#define STR 8, 8, 4, 8
constexpr unsigned STV[4] = {STR}, SO = STV[0], SX = STV[1], SK = STV[2], SV = STV[3];
constexpr unsigned F_O = 8u << 20;
constexpr unsigned F_X1 = F_O + 131072;
constexpr unsigned OFF_KEY = F_O + 393216;
constexpr unsigned F_KV = F_O + 524288;
constexpr unsigned OFF_AB = 147456;
constexpr unsigned OFF_XNEW = 262144;
constexpr unsigned OFF_CS = 294912;
constexpr unsigned OFF_FX = 393216;
constexpr unsigned OFF_CA = 524288;
constexpr unsigned OFF_CB = 655360;
constexpr unsigned OFF_STAT = 1u << 20;
constexpr unsigned OFF_PART = 3u << 20;
constexpr unsigned OFF_XN = 8u << 20;
constexpr unsigned OFF_O = 16u << 20;
constexpr unsigned OFF_KV = 32u << 20;
constexpr unsigned OFF_WI = 52u << 20;
constexpr unsigned WS_BYTES = 58u << 20;
constexpr unsigned POISON = 0xFFFFFFFFu;

constexpr int L_ST0 = 147456, L_CNT = 147968, L_CSL = 31872, L_FXL = 32256, L_K = 0, L_V = 32768, L_RING = 65536, L_XCH = 0, L_DN = 32768, L_TOUCH0 = 149504, L_RDY = 31744, L_QC = 31808, LDS_BYTES = 163840;
constexpr int STAGE = 24576;
#define SLOT(t) ((t) & 3)

template <typename F, int... I> __device__ __forceinline__ void static_for_impl(F&& f, std::integer_sequence<int, I...>) {
  (f(std::integral_constant<int, I>{}), ...);
}
template <int N, typename F> __device__ __forceinline__ void static_for(F&& f) { static_for_impl(f, std::make_integer_sequence<int, N>{}); }
__device__ __forceinline__ unsigned pack2(float a, float b) {
  f32x2 v = {a, b};
  return __builtin_bit_cast(unsigned, __builtin_convertvector(v, bf16x2));
}
__device__ __forceinline__ float asf(unsigned u) { return __builtin_bit_cast(float, u); }
__device__ __forceinline__ float lo16(unsigned u) { return __builtin_bit_cast(float, u << 16); }
__device__ __forceinline__ float hi16(unsigned u) { return __builtin_bit_cast(float, u & 0xffff0000u); }
__device__ __forceinline__ u32x4 pack8(const f32x4 a, const f32x4 b) {
  u32x4 o;
  o[0] = pack2(a[0], a[1]); o[1] = pack2(a[2], a[3]); o[2] = pack2(b[0], b[1]); o[3] = pack2(b[2], b[3]);
  return o;
}
__device__ __forceinline__ f32x4 mfma16(u32x4 a, u32x4 b, f32x4 c) {
  return __builtin_amdgcn_mfma_f32_16x16x32_bf16(__builtin_bit_cast(bf16x8, a), __builtin_bit_cast(bf16x8, b), c, 0, 0, 0);
}
__device__ __forceinline__ f32x4 mfma16k16(s16x4 a, s16x4 b, f32x4 c) { return __builtin_amdgcn_mfma_f32_16x16x16bf16_1k(a, b, c, 0, 0, 0); }
template <int CTRL> __device__ __forceinline__ float dppf(float v) {
  return __builtin_bit_cast(float, __builtin_amdgcn_update_dpp(0, __builtin_bit_cast(int, v), CTRL, 0xf, 0xf, true));
}
template <int CTRL> __device__ __forceinline__ unsigned dppu(unsigned v) {
  return (unsigned)__builtin_amdgcn_update_dpp(0, (int)v, CTRL, 0xf, 0xf, true);
}
__device__ __forceinline__ float add_x16(float v) {
  auto r = __builtin_amdgcn_permlane16_swap(__builtin_bit_cast(unsigned, v), __builtin_bit_cast(unsigned, v), false, false);
  return __builtin_bit_cast(float, (unsigned)r[0]) + __builtin_bit_cast(float, (unsigned)r[1]);
}
__device__ __forceinline__ float add_x32(float v) {
  auto r = __builtin_amdgcn_permlane32_swap(__builtin_bit_cast(unsigned, v), __builtin_bit_cast(unsigned, v), false, false);
  return __builtin_bit_cast(float, (unsigned)r[0]) + __builtin_bit_cast(float, (unsigned)r[1]);
}
__device__ __forceinline__ float max_x16(float v) {
  auto r = __builtin_amdgcn_permlane16_swap(__builtin_bit_cast(unsigned, v), __builtin_bit_cast(unsigned, v), false, false);
  return fmaxf(__builtin_bit_cast(float, (unsigned)r[0]), __builtin_bit_cast(float, (unsigned)r[1]));
}
__device__ __forceinline__ float max_x32(float v) {
  auto r = __builtin_amdgcn_permlane32_swap(__builtin_bit_cast(unsigned, v), __builtin_bit_cast(unsigned, v), false, false);
  return fmaxf(__builtin_bit_cast(float, (unsigned)r[0]), __builtin_bit_cast(float, (unsigned)r[1]));
}
__device__ __forceinline__ unsigned umax_x16(unsigned v) {
  auto r = __builtin_amdgcn_permlane16_swap(v, v, false, false);
  return max((unsigned)r[0], (unsigned)r[1]);
}
__device__ __forceinline__ unsigned umax_x32(unsigned v) {
  auto r = __builtin_amdgcn_permlane32_swap(v, v, false, false);
  return max((unsigned)r[0], (unsigned)r[1]);
}
__device__ __forceinline__ float sum_hi8(float v) {
  v += dppf<0x128>(v);
  v = add_x16(v);
  return add_x32(v);
}
__device__ __forceinline__ float sum32(float v) {
  v += dppf<0xB1>(v);
  v += dppf<0x4E>(v);
  v += dppf<0x124>(v);
  v += dppf<0x128>(v);
  return add_x16(v);
}
__device__ __forceinline__ float sum64(float v) {
  v += dppf<0xB1>(v);
  v += dppf<0x4E>(v);
  v += dppf<0x124>(v);
  v += dppf<0x128>(v);
  v = add_x16(v);
  return add_x32(v);
}

#define BAR() asm volatile("s_waitcnt lgkmcnt(0)\n\ts_barrier" ::: "memory")
#define DRAIN() asm volatile("s_waitcnt vmcnt(0)" ::: "memory")
#define SB() __builtin_amdgcn_sched_barrier(0)
template <int N> __device__ __forceinline__ void wait_vm() { asm volatile("s_waitcnt vmcnt(%0)" ::"n"(N) : "memory"); }

__device__ __forceinline__ void dma16(rsrc_t rs, unsigned voff, unsigned soff, unsigned lds) {
  asm volatile("s_mov_b32 m0, %0\n\ts_nop 0\n\tbuffer_load_dwordx4 %1, %2, %3 offen lds" ::"s"(lds), "v"(voff), "s"(rs), "s"(soff) : "memory");
}
template <int OFF> __device__ __forceinline__ void dma16s(rsrc_t rs, unsigned voff, unsigned soff, unsigned lds) {
  asm volatile("s_mov_b32 m0, %0\n\ts_nop 0\n\tbuffer_load_dwordx4 %1, %2, %3 offen offset:%4 sc1 lds" ::"s"(lds), "v"(voff), "s"(rs), "s"(soff), "n"(OFF) : "memory");
}
__device__ __forceinline__ void touch(unsigned& d, const GAS bf16* ptr) { asm volatile("global_load_dword %0, %1, off" : "+v"(d) : "v"(ptr) : "memory"); }
__device__ __forceinline__ void ldrows(u32x4 (&d)[4], rsrc_t rx, unsigned voff, unsigned soff) {
  asm volatile("buffer_load_dwordx4 %0, %4, %5, %6 offen\n\tbuffer_load_dwordx4 %1, %4, %5, %6 offen offset:512\n\t"
               "buffer_load_dwordx4 %2, %4, %5, %6 offen offset:1024\n\tbuffer_load_dwordx4 %3, %4, %5, %6 offen offset:1536"
               : "=&v"(d[0]), "=&v"(d[1]), "=&v"(d[2]), "=&v"(d[3]) : "v"(voff), "s"(rx), "s"(soff) : "memory");
}
__device__ __forceinline__ void pin4(f32x2* v) { asm volatile("" : "+v"(v[0]), "+v"(v[1]), "+v"(v[2]), "+v"(v[3])); }
__device__ __forceinline__ float rbf1(float x) { return __builtin_bit_cast(float, pack2(0.f, x)); }
__device__ __forceinline__ void vswap(unsigned& a, unsigned& b) { asm volatile("v_swap_b32 %0, %1" : "+v"(a), "+v"(b)); }
__device__ __forceinline__ void vswap(float& a, float& b) { asm volatile("v_swap_b32 %0, %1" : "+v"(a), "+v"(b)); }
template <typename V, int N> __device__ __forceinline__ void vswapn(V& a, V& b) {
#pragma unroll
  for (int i = 0; i < N; ++i) { auto x = a[i], y = b[i]; vswap(x, y); a[i] = x; b[i] = y; }
}
__device__ __forceinline__ u32x4 ld16(rsrc_t rs, unsigned voff, unsigned soff) { return __builtin_amdgcn_raw_buffer_load_b128(rs, voff, soff, SC1); }
template <int OFF> __device__ __forceinline__ void ld16h(u32x4& d, rsrc_t rs, unsigned voff, unsigned soff) {
  asm volatile("buffer_load_dwordx4 %0, %1, %2, %3 offen offset:%4 sc1" : "=v"(d) : "v"(voff), "s"(rs), "s"(soff), "n"(OFF) : "memory");
}
template <int I, int OFF> __device__ __forceinline__ void ld16w(u32x4& d, rsrc_t rs, unsigned voff, unsigned soff) {
  if constexpr (I == 0) asm volatile("buffer_load_dwordx4 %0, %1, %2, %3 offen offset:%4 sc1" : "={v[128:131]}"(d) : "v"(voff), "s"(rs), "s"(soff), "n"(OFF) : "memory");
  else if constexpr (I == 1) asm volatile("buffer_load_dwordx4 %0, %1, %2, %3 offen offset:%4 sc1" : "={v[132:135]}"(d) : "v"(voff), "s"(rs), "s"(soff), "n"(OFF) : "memory");
  else if constexpr (I == 2) asm volatile("buffer_load_dwordx4 %0, %1, %2, %3 offen offset:%4 sc1" : "={v[136:139]}"(d) : "v"(voff), "s"(rs), "s"(soff), "n"(OFF) : "memory");
  else if constexpr (I == 3) asm volatile("buffer_load_dwordx4 %0, %1, %2, %3 offen offset:%4 sc1" : "={v[140:143]}"(d) : "v"(voff), "s"(rs), "s"(soff), "n"(OFF) : "memory");
  else if constexpr (I == 4) asm volatile("buffer_load_dwordx4 %0, %1, %2, %3 offen offset:%4 sc1" : "={v[144:147]}"(d) : "v"(voff), "s"(rs), "s"(soff), "n"(OFF) : "memory");
  else if constexpr (I == 5) asm volatile("buffer_load_dwordx4 %0, %1, %2, %3 offen offset:%4 sc1" : "={v[148:151]}"(d) : "v"(voff), "s"(rs), "s"(soff), "n"(OFF) : "memory");
  else if constexpr (I == 6) asm volatile("buffer_load_dwordx4 %0, %1, %2, %3 offen offset:%4 sc1" : "={v[152:155]}"(d) : "v"(voff), "s"(rs), "s"(soff), "n"(OFF) : "memory");
  else if constexpr (I == 7) asm volatile("buffer_load_dwordx4 %0, %1, %2, %3 offen offset:%4 sc1" : "={v[156:159]}"(d) : "v"(voff), "s"(rs), "s"(soff), "n"(OFF) : "memory");
  else if constexpr (I == 8) asm volatile("buffer_load_dwordx4 %0, %1, %2, %3 offen offset:%4 sc1" : "={v[160:163]}"(d) : "v"(voff), "s"(rs), "s"(soff), "n"(OFF) : "memory");
  else if constexpr (I == 9) asm volatile("buffer_load_dwordx4 %0, %1, %2, %3 offen offset:%4 sc1" : "={v[164:167]}"(d) : "v"(voff), "s"(rs), "s"(soff), "n"(OFF) : "memory");
  else if constexpr (I == 10) asm volatile("buffer_load_dwordx4 %0, %1, %2, %3 offen offset:%4 sc1" : "={v[168:171]}"(d) : "v"(voff), "s"(rs), "s"(soff), "n"(OFF) : "memory");
  else if constexpr (I == 11) asm volatile("buffer_load_dwordx4 %0, %1, %2, %3 offen offset:%4 sc1" : "={v[172:175]}"(d) : "v"(voff), "s"(rs), "s"(soff), "n"(OFF) : "memory");
}
template <int K> __device__ __forceinline__ void pin_w(u32x4& a, u32x4& b) {
  if constexpr (K == 0) asm volatile("" : "+{v[128:131]}"(a), "+{v[132:135]}"(b)::"memory");
  else if constexpr (K == 1) asm volatile("" : "+{v[136:139]}"(a), "+{v[140:143]}"(b)::"memory");
  else if constexpr (K == 2) asm volatile("" : "+{v[144:147]}"(a), "+{v[148:151]}"(b)::"memory");
  else if constexpr (K == 3) asm volatile("" : "+{v[152:155]}"(a), "+{v[156:159]}"(b)::"memory");
  else if constexpr (K == 4) asm volatile("" : "+{v[160:163]}"(a), "+{v[164:167]}"(b)::"memory");
  else if constexpr (K == 5) asm volatile("" : "+{v[168:171]}"(a), "+{v[172:175]}"(b)::"memory");
}
__device__ __forceinline__ void ld8h(u32x2& d, rsrc_t rs, unsigned voff, unsigned soff) {
  asm volatile("buffer_load_dwordx2 %0, %1, %2, %3 offen sc1" : "=v"(d) : "v"(voff), "s"(rs), "s"(soff) : "memory");
}
__device__ __forceinline__ void ld16u(u32x4& d, rsrc_t rs, unsigned voff, unsigned soff) {
  asm volatile("buffer_load_dwordx4 %0, %1, %2, %3 offen sc1" : "+v"(d) : "v"(voff), "s"(rs), "s"(soff) : "memory");
}
template <int OFF> __device__ __forceinline__ void ld4h(unsigned& d, rsrc_t rs, unsigned voff, unsigned soff) {
  asm volatile("buffer_load_dword %0, %1, %2, %3 offen offset:%4 sc1" : "=v"(d) : "v"(voff), "s"(rs), "s"(soff), "n"(OFF) : "memory");
}
__device__ __forceinline__ void ld4x16(u32x4 (&d)[4], rsrc_t rs, unsigned voff, unsigned soff) {
  asm volatile("buffer_load_dwordx4 %0, %4, %5, %6 offen sc1\n\tbuffer_load_dwordx4 %1, %4, %5, %6 offen offset:64 sc1\n\t"
               "buffer_load_dwordx4 %2, %4, %5, %6 offen offset:128 sc1\n\tbuffer_load_dwordx4 %3, %4, %5, %6 offen offset:192 sc1\n\ts_waitcnt vmcnt(0)"
               : "=&v"(d[0]), "=&v"(d[1]), "=&v"(d[2]), "=&v"(d[3]) : "v"(voff), "s"(rs), "s"(soff) : "memory");
}
__device__ __forceinline__ u32x2 ld8(rsrc_t rs, unsigned voff, unsigned soff) { return __builtin_amdgcn_raw_buffer_load_b64(rs, voff, soff, SC1); }
__device__ __forceinline__ void st16(u32x4 v, rsrc_t rs, unsigned voff, unsigned soff) { __builtin_amdgcn_raw_buffer_store_b128(v, rs, voff + soff, 0, SC1); }
__device__ __forceinline__ void st8(u32x2 v, rsrc_t rs, unsigned voff, unsigned soff) { __builtin_amdgcn_raw_buffer_store_b64(v, rs, voff, soff, SC1); }

__device__ __forceinline__ unsigned ld4(rsrc_t rs, unsigned voff, unsigned soff) { return __builtin_amdgcn_raw_buffer_load_b32(rs, voff, soff, SC1); }
__device__ __forceinline__ void st4(unsigned v, rsrc_t rs, unsigned voff, unsigned soff) { __builtin_amdgcn_raw_buffer_store_b32(v, rs, voff, soff, SC1); }

constexpr unsigned kSpinCap = 1u << 16;
__device__ __forceinline__ bool spin_fail(unsigned& spins, rsrc_t rs, u32x2 T) {
  asm volatile("" ::: "memory");
  ++spins;
  if (spins > kSpinCap) { st8(T, rs, 0, F_TMO); return true; }
  if ((spins & 255u) == 0u) {
    const u32x2 v = ld8(rs, 0, F_TMO);
    if (v[0] == T[0] && v[1] == T[1]) return true;
  }
  __builtin_amdgcn_s_sleep(3);
  return false;
}
__device__ __forceinline__ void poll_flags2(rsrc_t rs, unsigned voff, u32x2 T) {
  for (unsigned spins = 0;;) {
    const u32x4 v = ld16(rs, voff, 0);
    if (__all(v[0] == T[0] && v[1] == T[1] && v[2] == T[0] && v[3] == T[1])) break;
    if (spin_fail(spins, rs, T)) break;
  }
  asm volatile("" ::: "memory");
  __builtin_amdgcn_fence(__ATOMIC_ACQUIRE, "wavefront");
}
__device__ __forceinline__ void poll_flags(rsrc_t rs, unsigned voff, unsigned soff, u32x2 T) {
  for (unsigned spins = 0;;) {
    const u32x2 v = ld8(rs, voff, soff);
    if (__all(v[0] == T[0] && v[1] == T[1])) break;
    if (spin_fail(spins, rs, T)) break;
  }
  asm volatile("" ::: "memory");
  __builtin_amdgcn_fence(__ATOMIC_ACQUIRE, "wavefront");
}

__device__ __forceinline__ unsigned toff(unsigned R, unsigned ks, unsigned g) {
  return (((R >> 5) * 64 + ks * 2 + ((R >> 4) & 1)) << 10) + ((g * 16 + (R & 15)) << 4);
}
__device__ __forceinline__ void unpack8v(const u32x4 u, f32x2* v) {
#pragma unroll
  for (int j = 0; j < 4; ++j) v[j] = f32x2{lo16(u[j]), hi16(u[j])};
}
__device__ __forceinline__ u32x4 pack8v(const f32x2* v) {
  u32x4 o;
#pragma unroll
  for (int j = 0; j < 4; ++j) o[j] = pack2(v[j][0], v[j][1]);
  return o;
}
__device__ __forceinline__ f32x2 rbf1v(f32x2 v) { return f32x2{rbf1(v[0]), rbf1(v[1])}; }
__device__ __forceinline__ float ln_rd(float var) { return __builtin_amdgcn_rcpf(rbf1(__builtin_amdgcn_sqrtf(rbf1(rbf1(var) + 1e-5f)))); }
template <int N> __device__ __forceinline__ void ln_apply(float mb, float rd, const f32x2* v, f32x2* y) {
  const f32x2 mb2 = {mb, mb}, rd2 = {rd, rd};
#pragma unroll
  for (int j = 0; j < N; ++j) y[j] = rbf1v(rbf1v(v[j] - mb2) * rd2);
}
__device__ __forceinline__ float hadd(f32x2 v) {
  float a = v[0];
  asm volatile("" : "+v"(a));
  return a + v[1];
}
__device__ __forceinline__ float add_g(float v) { return add_x32(add_x16(v)); }
__device__ __forceinline__ void add_g3(float& x, float& y, float& z) {
  auto a = __builtin_amdgcn_permlane16_swap(__builtin_bit_cast(unsigned, x), __builtin_bit_cast(unsigned, y), false, false);
  auto b = __builtin_amdgcn_permlane16_swap(__builtin_bit_cast(unsigned, z), __builtin_bit_cast(unsigned, z), false, false);
  const float r = asf(a[0]) + asf(a[1]), t = asf(b[0]) + asf(b[1]);
  auto c = __builtin_amdgcn_permlane32_swap(__builtin_bit_cast(unsigned, r), __builtin_bit_cast(unsigned, r), false, false);
  auto d = __builtin_amdgcn_permlane32_swap(__builtin_bit_cast(unsigned, t), __builtin_bit_cast(unsigned, t), false, false);
  const float u = asf(c[0]) + asf(c[1]);
  z = asf(d[0]) + asf(d[1]);
  auto e = __builtin_amdgcn_permlane16_swap(__builtin_bit_cast(unsigned, u), __builtin_bit_cast(unsigned, u), false, false);
  x = asf(e[0]); y = asf(e[1]);
}
__device__ __forceinline__ u32x4 trade16(u32x4 v) {
  auto a = __builtin_amdgcn_permlane16_swap(v[0], v[2], false, false);
  auto b = __builtin_amdgcn_permlane16_swap(v[1], v[3], false, false);
  return u32x4{(unsigned)a[0], (unsigned)b[0], (unsigned)a[1], (unsigned)b[1]};
}
__device__ __forceinline__ unsigned umax4(u32x4 v) { return max(max(v[0], v[1]), max(v[2], v[3])); }
__device__ __forceinline__ u32x2 unpoison2(u32x2 v) { return u32x2{min(v[0], POISON - 1), min(v[1], POISON - 1)}; }
__device__ __forceinline__ u32x4 unpoison4(u32x4 v) {
  return u32x4{min(v[0], POISON - 1), min(v[1], POISON - 1), min(v[2], POISON - 1), min(v[3], POISON - 1)};
}

typedef union { struct { s16x4 a, b; } hf; u32x4 v; } frag_t;
__device__ __forceinline__ s16x4 trread(const char* smem, unsigned off) {
  return __builtin_amdgcn_ds_read_tr16_b64_v4i16((LDS_AS s16x4*)(smem + off));
}
__device__ __forceinline__ s16x4 trabs(unsigned addr) { return __builtin_amdgcn_ds_read_tr16_b64_v4i16((LDS_AS s16x4*)(size_t)addr); }

constexpr int WA = 6;
constexpr int WB = 8, WR = 4;
#define CH 0, 4, 7, 8, 13, 15, 20, 22, 27, 29
constexpr int CHV[10] = {CH};
constexpr int FSL = CHV[1], FS0 = CHV[2], FS0D = CHV[3], FS1 = CHV[4], FS1S = CHV[5];
constexpr int FXA = CHV[6], FXB = CHV[7], FXL = CHV[8], FXS = CHV[9];
constexpr int FXT = FXB >> 2;
#define C0 27, 3, 16, 19, 22, 23, 2, 5, 4
constexpr int C0V[9] = {C0};
constexpr int IMG = C0V[0], CSD = C0V[1];
constexpr int ERQ = C0V[2], CSL = C0V[3], CSS = C0V[4];
constexpr int RWL = C0V[6], RWS = C0V[7], POI = C0V[8];
#define WTS 99
#define WTG 2
#define PBS 10
#define KP0 26
#define PS0 8
constexpr int EIN = cmax(CSD, cmax(RWS, POI)) + 3;
constexpr int ABS = 27;
constexpr int abl(int it) { return it ? 23 : C0V[5]; }
constexpr int XU = 3, XL = 2;
constexpr int x_slot(int u) { return u % XU == 0 ? 0 : u % XU == 1 ? 32768 : 49152; }
#define TCH 20, 6, 5, 27
constexpr int TCV[4] = {TCH};
constexpr int TS0 = TCV[0], TSN = TCV[1], TW = TCV[2], TST = TCV[3];
constexpr int QL = 26;
#define FDG 0
#define XSL 4
#define FW0 4
#define FW1 4
#define X1F 1
#define RWG 0
#define CSG 4
#define SDI0 12
#define SLP 24, 14, 56, 3
constexpr int SLV[4] = {SLP};
constexpr int SDIR = SLV[0], GSL = SLV[1], RSL = SLV[2], KSL = SLV[3];
constexpr bool tile_piece(int s) { return s >= 3 && ((s - 3) >> 2) + 3 < 8 && ((s - 3) & 3) < 3; }
constexpr int a_req(int s, int n) {
  if (!(n & 1)) return -1;
  if (s <= 1) return 8 + 3 * s + (n >> 1);
  return n < 5 && s + 5 < 32 ? 2 * (s + 5) + (n >> 1) : -1;
}
constexpr int vm_n(int it, int s, int n) {
  int c = 0;
  if (n == 0) c += tile_piece(s);
  if (n == 4) c += (s == abl(it)) + (it ? (s == FSL ? 5 : 0) : (s == ERQ) + (s >= IMG && s < IMG + 3));
  if (!it) c += (s == WTS && n == WTG) + (s < 4 && n == 2) + (s == 0 && (n == 0 || n == 4)) + ((n == 3 || n == 5) && s + XL + XU < 32);
  else c += (a_req(s, n) >= 0) + ((s == 0 || s == 4) && n == 2);
  return c;
}
constexpr int vm_at(int it, int s, int n) {
  int c = it ? 8 : 14;
  for (int u = 0; u < 6 * s + n; ++u) c += vm_n(it, u / 6, u % 6);
  return c;
}
constexpr int vm_a(int it, int k) {
  if (k < 4) return 2 * k + 2;
  for (int u = 0; u < 192; ++u)
    if (a_req(u / 6, u % 6) == 2 * k + 1) return vm_at(it, (u + 1) / 6, (u + 1) % 6);
  return 0;
}
constexpr int vm_tile(int it, int t) { return t >= 3 ? vm_at(it, 4 * t - 7, 1) : t == 1 ? vm_at(0, 0, 5) : vm_at(0, 3, 3); }
constexpr int vm_x(int u) { return u == 2 ? 9 : u < XU + XL ? 12 + 2 * (u - XU) : vm_at(0, u - XU - XL + 1, 0); }

#define WO1 0
constexpr int wo_k(int it, int s) { return (it && !WO1) || s < 4 ? 15 - s : s < 8 ? 7 - s : 19 - s; }
constexpr int wo_pos(int it, int s) { return 2 * s + 2 + (s >= 4 ? 2 * it : 0); }
constexpr int wo_at(int it, int s) { return 2 * it + (s < 4 ? 10 + 4 * s : s < 8 ? 16 + 2 * s : it && !WO1 ? 32 : 36); }
__device__ __forceinline__ void pin1(unsigned& a) { asm volatile("" : "+v"(a)); }
__device__ __forceinline__ void pin2(u32x4& a, u32x4& b) { asm volatile("" : "+v"(a), "+v"(b)::"memory"); }
__device__ __forceinline__ void pin2s(float& a, float& b) { asm volatile("" : "+v"(a), "+v"(b)); }
template <int OFF> __device__ __forceinline__ void lds2(u32x4& a, u32x4& b, unsigned ad) {
  asm volatile("ds_read_b128 %0, %2 offset:%3\n\tds_read_b128 %1, %2 offset:%4" : "=&v"(a), "=&v"(b) : "v"(ad), "n"(OFF), "n"(OFF + 1024) : "memory");
}
template <int N> __device__ __forceinline__ void lds_wait(u32x4& a, u32x4& b) { asm volatile("s_waitcnt lgkmcnt(%2)" : "+v"(a), "+v"(b) : "n"(N) : "memory"); }
__device__ __forceinline__ void pin2f(f32x2& a, f32x2& b) { asm volatile("" : "+v"(a), "+v"(b)); }
__device__ __forceinline__ void pin_ac(f32x4 (&ac)[2][4]) {
  asm volatile("" : "+{v[176:179]}"(ac[0][0]), "+{v[180:183]}"(ac[0][1]), "+{v[184:187]}"(ac[0][2]), "+{v[188:191]}"(ac[0][3]),
                    "+{v[192:195]}"(ac[1][0]), "+{v[196:199]}"(ac[1][1]), "+{v[200:203]}"(ac[1][2]), "+{v[204:207]}"(ac[1][3]));
}

constexpr int L_XC = LDS_BYTES - 64, L_SKEY = 32704, L_SRED = 4096, L_SFIN = 20480;
__device__ __forceinline__ unsigned key_load(rsrc_t rs, int l, int L, int rep) { return ld4(rs, (l & 15) * SK, OFF_KEY + (L * 16 + rep) * cmax(16 * SK, 128)); }
__device__ __forceinline__ unsigned umax16(unsigned k) {
  k = max(k, dppu<0xB1>(k));
  k = max(k, dppu<0x4E>(k));
  k = max(k, dppu<0x141>(k));
  return max(k, dppu<0x140>(k));
}
__device__ __forceinline__ unsigned key_row(unsigned key) { return (0xffffu - (key & 0xffffu)) & 2047u; }
__device__ __forceinline__ unsigned key_wait(rsrc_t rs, u32x2 T, int l, int L, int rep, unsigned kv) {
  if (!__all(kv != POISON)) {
    unsigned spins = 0;
    do {
      if (spin_fail(spins, rs, T)) break;
      if (L == 0) __builtin_amdgcn_s_sleep(KSL);
      kv = key_load(rs, l, L, rep);
    } while (!__all(kv != POISON));
  }
  return __builtin_amdgcn_readfirstlane(umax16(kv));
}
__device__ __forceinline__ unsigned ffn_xoff(unsigned key, int L, int w) { return OFF_CA + L * 32768 + (key_row(key) >> 7) * 2048 + w * 256; }
__device__ __forceinline__ void row_load(rsrc_t rs, int g, unsigned xo, u32x4 (&xv)[4]) {
  ld16h<0>(xv[0], rs, g * 16, xo); ld16h<64>(xv[1], rs, g * 16, xo); ld16h<128>(xv[2], rs, g * 16, xo); ld16h<192>(xv[3], rs, g * 16, xo);
}
template <int SL = 0> __device__ __forceinline__ void ffn_xwait(rsrc_t rs, u32x2 T, unsigned xo, int l, u32x4 (&xv)[4]) {
#define XV_OK() __all(max(max(umax4(xv[0]), umax4(xv[1])), max(umax4(xv[2]), umax4(xv[3]))) != POISON)
  if (!XV_OK()) {
    unsigned spins = 0;
    do {
      if (spin_fail(spins, rs, T)) break;
      if (SL) __builtin_amdgcn_s_sleep(SL);
#pragma unroll
      for (int i = 0; i < 4; ++i) xv[i] = ld16(rs, (l >> 4) * 16 + i * 64, xo);
    } while (!XV_OK());
  }
#undef XV_OK
}
__device__ __forceinline__ void ffn_gu(const u32x4 (&gv)[4], const u32x4 (&uv)[4], const u32x4 (&xv)[4], f32x4& ga, f32x4& ua) {
  ga = mfma16(gv[0], xv[0], f32x4{0.f, 0.f, 0.f, 0.f}); ua = mfma16(uv[0], xv[0], f32x4{0.f, 0.f, 0.f, 0.f});
#pragma unroll
  for (int i = 1; i < 4; ++i) { ga = mfma16(gv[i], xv[i], ga); ua = mfma16(uv[i], xv[i], ua); }
}
__device__ __forceinline__ void ffn_gu_check(rsrc_t rs, u32x2 T, unsigned xo, int l, const u32x4 (&gv)[4], const u32x4 (&uv)[4], u32x4 (&xv)[4], f32x4& ga, f32x4& ua) {
  if (__builtin_expect(__any(ga[0] != ga[0]), 0)) {
    ffn_xwait(rs, T, xo, l, xv);
    ffn_gu(gv, uv, xv, ga, ua);
  }
}
__device__ __forceinline__ void ffn_gu_store(char* smem, int l, int w, f32x4 ga, f32x4 ua) {
  LDS_AS f32x4* sred = (LDS_AS f32x4*)(smem + L_SRED);
  if ((l & 15) == 0) { sred[w * 8 + (l >> 4)] = ga; sred[w * 8 + 4 + (l >> 4)] = ua; }
}
__device__ __forceinline__ f32x4 ffn_t_read(char* smem, int l) { return ((const LDS_AS f32x4*)(smem + L_SRED))[(l & 7) * 8 + ((l >> 3) & 1) * 4 + (l >> 4)]; }
__device__ __forceinline__ void ffn_d_read(char* smem, int l, int blk, s16x4 (&dfr)[8]) {
#pragma unroll
  for (int i = 0; i < 8; ++i) dfr[i] = trread(smem, L_DN + blk * 4096 + (l >> 4) * 1024 + ((l & 15) >> 2) * 256 + i * 32 + (l & 3) * 8);
}
__device__ __forceinline__ float ffn_d_sum(float t) { t += dppf<0xB1>(t); t += dppf<0x4E>(t); return t + dppf<0x141>(t); }
__device__ __forceinline__ u32x2 ffn_h(f32x4 t, int l) {
#pragma unroll
  for (int e = 0; e < 4; ++e) t[e] = ffn_d_sum(t[e]);
  const float a = (l & 1) ? t[1] : t[0], b = (l & 1) ? t[3] : t[2], s = (l & 2) ? b : a;
  const float gg = rbf1(s), uu = rbf1(dppf<0x128>(s));
  const float h = rbf1(gg * __builtin_amdgcn_rcpf(1.0f + __expf(-gg))) * uu;
  const unsigned h0 = pack2(h, dppf<0x55>(h)), h1 = pack2(dppf<0xAA>(h), dppf<0xFF>(h));
  return (l & 15) == 0 ? u32x2{h0, h1} : u32x2{0u, 0u};
}
__device__ __forceinline__ void ffn_d_mma(const s16x4 (&dfr)[8], u32x2& hh, f32x4& ya, f32x4& yb) {
#pragma unroll
  for (int i = 0; i < 8; ++i) {
    if (i & 1) yb = mfma16k16(dfr[i], __builtin_bit_cast(s16x4, hh), yb);
    else ya = mfma16k16(dfr[i], __builtin_bit_cast(s16x4, hh), ya);
    hh = u32x2{dppu<0x111>(hh[0]), dppu<0x111>(hh[1])};
  }
}
__device__ __forceinline__ void ffn_d_store(rsrc_t rs, int l, int G0, int L, int slice, f32x4 y) {
  const unsigned c = l & 15;
  st16(unpoison4(__builtin_bit_cast(u32x4, y)), rs, (G0 + (c >> 1)) * 32768 + slice * 128 + (c & 1) * 64 + (l >> 4) * 16, OFF_PART + L * (1u << 20));
}
__device__ __forceinline__ void red_load(rsrc_t rs, int tid, int L, unsigned G, unsigned key, u32x4 (&pv)[4], u32x2& cxn, u32x2& cx1) {
#pragma unroll
  for (int k = 0; k < 4; ++k) pv[k] = ld16(rs, min(tid + 512 * k, 1791) * 16, OFF_PART + L * (1u << 20) + G * 32768);
  cxn = ld8(rs, (tid & 3) * 16 + (tid & 4) * 2, OFF_CA + L * 32768 + (key_row(key) >> 7) * 2048 + G * 64);
  cx1 = ld8(rs, (tid & 3) * 16 + (tid & 4) * 2, OFF_CB + L * 32768 + (key_row(key) >> 7) * 2048 + G * 64);
}
__device__ __forceinline__ void red_sum(rsrc_t rs, char* smem, u32x2 T, int tid, int w, int L, unsigned G, unsigned key,
                                        u32x4 (&pv)[4], u32x2& cxn, u32x2& cx1) {
#define PV_OK() __all(max(max(max(umax4(pv[0]), umax4(pv[1])), max(umax4(pv[2]), umax4(pv[3]))), max(max(cxn[0], cxn[1]), max(cx1[0], cx1[1]))) != POISON)
  if (!PV_OK()) {
    unsigned spins = 0;
    do {
      if (spin_fail(spins, rs, T)) break;
      red_load(rs, tid, L, G, key, pv, cxn, cx1);
    } while (!PV_OK());
  }
#undef PV_OK
  f32x4 s = (__builtin_bit_cast(f32x4, pv[0]) + __builtin_bit_cast(f32x4, pv[1])) + __builtin_bit_cast(f32x4, pv[2]);
  if (w < 4) s += __builtin_bit_cast(f32x4, pv[3]);
  ((LDS_AS f32x4*)(smem + L_SFIN))[tid] = s;
}
template <int NW> __device__ __forceinline__ u32x2 red_final(char* smem, int l, unsigned key, u32x2 cxn, u32x2 cx1) {
  const LDS_AS f32x4* sf = (const LDS_AS f32x4*)(smem + L_SFIN) + l;
  f32x4 s = (sf[0] + sf[64]) + (sf[128] + sf[192]);
  if constexpr (NW == 8) s += (sf[256] + sf[320]) + (sf[384] + sf[448]);
#pragma unroll
  for (int j = 0; j < 4; ++j) s[j] = sum_hi8(s[j]);
  const unsigned k16 = key >> 16;
  const float rmax = __builtin_bit_cast(float, ((k16 & 0x8000u) ? (k16 & 0x7fffu) : (~k16 & 0xffffu)) << 16);
  const float sig = rbf1(__builtin_amdgcn_rcpf(1.0f + __expf(-rmax)));
  const float xn4[4] = {lo16(cxn[0]), hi16(cxn[0]), lo16(cxn[1]), hi16(cxn[1])};
  const float x14[4] = {lo16(cx1[0]), hi16(cx1[0]), lo16(cx1[1]), hi16(cx1[1])};
  float o[4];
#pragma unroll
  for (int j = 0; j < 4; ++j) o[j] = x14[j] + rbf1(xn4[j] + rbf1(rbf1(s[j]) * sig));
  return u32x2{pack2(o[0], o[1]), pack2(o[2], o[3])};
}

struct RowFin { float mean, var, mb, rd; unsigned key; };
__device__ __forceinline__ void fin_wait(rsrc_t rs, u32x2 T, unsigned voff, unsigned so, u32x4 (&sp)[4]) {
#define ST_OK() __all(max(max(umax4(sp[0]), umax4(sp[1])), max(umax4(sp[2]), umax4(sp[3]))) != POISON)
  if (!ST_OK()) {
    unsigned spins = 0;
    do {
      if (spin_fail(spins, rs, T)) break;
#pragma unroll
      for (int i = 0; i < 4; ++i) sp[i] = ld16(rs, voff + i * 2048, so);
    } while (!ST_OK());
  }
#undef ST_OK
}
__device__ __forceinline__ RowFin fin_key(rsrc_t rs, u32x2 T, unsigned voff, unsigned so, char* skey, int l, int w, unsigned R, float wsum, u32x4 (&sp)[4]) {
  RowFin r;
#define ST_SUM() ((__builtin_bit_cast(f32x4, sp[0]) + __builtin_bit_cast(f32x4, sp[1])) + (__builtin_bit_cast(f32x4, sp[2]) + __builtin_bit_cast(f32x4, sp[3])))
  f32x4 sf = ST_SUM();
  const float z = (sf[0] + sf[1]) + sf[2];
  if (__any(z != z)) {
    fin_wait(rs, T, voff, so, sp);
    sf = ST_SUM();
  }
#undef ST_SUM
  float s0 = sf[0], s1 = sf[1], s2 = sf[2];
  add_g3(s0, s1, s2);
  r.mean = s0 * (1.0f / 1024.0f);
  r.var = fmaxf(__builtin_fmaf(-r.mean, r.mean, s1 * (1.0f / 1024.0f)), 0.f);
  r.mb = rbf1(r.mean); r.rd = ln_rd(r.var);
  const unsigned rb16 = pack2((s2 - r.mb * wsum) * r.rd, 0.f) & 0xffffu;
  r.key = min((((rb16 & 0x8000u) ? (~rb16 & 0xffffu) : (rb16 | 0x8000u)) << 16) | (0xffffu - R), POISON - 1);
  const unsigned wk = umax16(r.key);
  if (l == 0) ((LDS_AS unsigned*)skey)[w] = wk;
  return r;
}
__device__ __forceinline__ unsigned fin_cand(rsrc_t rs, char* skey, int L, int g, int rb, int ct, const RowFin& r, const f32x2 (&v)[8]) {
  const u32x4 k0 = *(const LDS_AS u32x4*)skey, k1 = *(const LDS_AS u32x4*)(skey + 16);
  const unsigned best = max(umax4(k0), umax4(k1));
  if (r.key == best) {
    const unsigned co = L * 32768 + rb * 2048 + ct * 128 + g * 16;
    f32x2 t[8], rd2 = {r.rd, r.rd}, of2 = {-r.mb * r.rd, -r.mb * r.rd};
    asm volatile("" : "+v"(rd2), "+v"(of2));
#pragma unroll
    for (int j = 0; j < 8; ++j) t[j] = __builtin_elementwise_fma(v[j], rd2, of2);
    st16(unpoison4(pack8v(t)), rs, co, OFF_CA); st16(unpoison4(pack8v(t + 4)), rs, co + 64, OFF_CA);
    if (g == 0) st4(best, rs, OFF_KEY + (L * 16 + ct) * cmax(16 * SK, 128) + rb * SK, 0);
    st16(unpoison4(pack8v(v)), rs, co, OFF_CB); st16(unpoison4(pack8v(v + 4)), rs, co + 64, OFF_CB);
  }
  return best;
}
__device__ __forceinline__ void fin_x2(const RowFin& r, f32x2 (&v)[8]) {
  f32x2 sc2 = {1.0f + r.rd, 1.0f + r.rd}, of2 = {-r.mb * r.rd, -r.mb * r.rd};
  asm volatile("" : "+v"(sc2), "+v"(of2));
#pragma unroll
  for (int j = 0; j < 8; ++j) v[j] = __builtin_elementwise_fma(v[j], sc2, of2);
}

extern "C" __device__ unsigned long long dispatch_id() __asm("llvm.amdgcn.dispatch.id");

extern "C" __global__ __launch_bounds__(512, 2) void mega_kernel(const bf16* x, char* ws, const bf16* wqkv, const bf16* wo, const bf16* wr,
                                                                 const bf16* wg, const bf16* wu, const bf16* wd, bf16* out) {
  extern __shared__ __attribute__((aligned(16))) char smem[];
  const int w0_ = __builtin_amdgcn_readfirstlane(threadIdx.x >> 6);
  const unsigned lbase = (unsigned)(size_t)(LDS_AS char*)smem;
  {
    unsigned long long pc;
    unsigned len;
    asm volatile("s_getpc_b64 %0\n\ts_mov_b32 %1, .Ltext_end-mega_kernel\n\t.subsection 1\n.Ltext_end:\n\t.subsection 0" : "=s"(pc), "=s"(len));
    const unsigned o = ((blockIdx.x >> 3) * 64 + (threadIdx.x & 63)) * 128;
    if (w0_ == 7 && o < len)
      asm volatile("s_mov_b32 m0, %0\n\ts_nop 0\n\tglobal_load_lds_dword %1, %2" ::"s"(lbase + L_TOUCH0), "v"(o), "s"(pc & ~127ull) : "memory");
  }
  asm volatile("" : "+s"(x), "+s"(ws), "+s"(wqkv));
#define HGRP() ((xcd & 2) + (xcd >> 2))
#define IDS()                                                                                         \
  int tid = threadIdx.x, w = w0_, bid = blockIdx.x;                                                   \
  asm volatile("" : "+v"(tid));                                                                       \
  asm volatile("" : "+s"(w));                                                                         \
  asm volatile("" : "+s"(bid));                                                                       \
  const int l = tid & 63, xcd = bid & 7, bi = bid >> 3;                                               \
  const int p = (xcd & 1) * 4 + (bi & 3), h = HGRP() * 8 + (bi >> 2);         \
  const int rb = 2 * xcd + (bi & 1), ct = bi >> 1;                               \
  const int slice = xcd * 28 + bi;                                                   \
  const bool ffn_on = bi < 28;                                                                        \
  const int g = l >> 4;                                                                               \
  const int rt = w < 4 ? w : 11 - w;          \
  KARGS();                                                                                            \
  (void)p; (void)h; (void)rb; (void)ct; (void)slice; (void)ffn_on; (void)g; (void)rt;
#define PROT() (2 * (p & 3) + (p >> 2))
  const rsrc_t rs = __builtin_amdgcn_make_buffer_rsrc((void*)ws, 0, WS_BYTES, 0x00020000);
  typedef const void* const __attribute__((address_space(4)))* karg_t;
#define KARGS() karg_t ka = (karg_t)__builtin_amdgcn_kernarg_segment_ptr(); asm volatile("" : "+s"(ka))
#define ARGP(name, i) const GAS bf16* name = (const GAS bf16*)ka[i]; asm volatile("" : "+s"(name))
#define RSRC(ptr, bytes) __builtin_amdgcn_make_buffer_rsrc((void*)(ptr), 0, bytes, 0x00020000)
#define rwq RSRC(pwq, 1024 * 3072 * 2)
#define rwo RSRC(pwo, 1024 * 1024 * 2)
#define rwg RSRC(pwg, 1024 * F_ * 2)
#define rwu RSRC(pwu, 1024 * F_ * 2)
#define rwd RSRC(pwd, 1024 * F_ * 2)
#define rout RSRC(pout, 2048 * 1024 * 2)

#define WQ_VOFF() ((l >> 2) * 6144 + (((l & 3) ^ ((((l >> 4) ^ (l >> 5)) & 1) << 1)) << 4))
#define WQ_PIECE(t, j, dst) dma16(rwq, wqv, ((((t) + rot) & 7) * 128 + ((j) & 7) * 16) * 6144 + ((j) >> 3) * 2048 + h * 64, lbase + (dst) + (j) * 1024)
#define WQ_BOFF(dt) ((g >> 1) * 1024 + (8 * (g & 1) + ((l & 15) >> 2)) * 64 + (l & 3) * 8 + (((dt) ^ (g & 1)) << 5))
#define X_REQ(u, hf) dma16(rx, xv[hf], (p * 256 + rt * 32 + ((u) & 1) * 16 + (hf) * 8) * 2048 + ((((u) >> 2) + rot) & 7) * 256 + (((u) >> 1) & 1) * 128, lbase + rt * 2048 + x_slot(u) + (hf) * 1024)
#define WO_PIECE(j) dma16(rwo, wov, ((w >> 2) * 64 + (w & 3) + 4 * (j)) * 16384 + ct * 128, lbase + 32768 + (j) * 8192 + w * 1024)
#define WD_DMA() _Pragma("unroll") for (int i = 0; i < 4; ++i) WD_DMA1(i)
#define WD_DMA1(i)                                                                                   \
      dma16(rwd, (l >> 4) * 2048 + (l & 15) * 16, (unsigned)(slice * 16 + 4 * (i)) * 2048 + w * 256, lbase + L_DN + w * 4096 + (i) * 1024)
#define FFN_DMA(m) dma16((m) & 1 ? rwu : rwg, fnv, (unsigned)(((4 * w + ((m) >> 1)) * 32) * F_ + slice * 16) * 2, lbase + 65536 + (m) * 8192 + w * 1024)
#define POISON_BIG(L)                                                                                \
  if (w >= 2) {                                                                                      \
    unsigned pz1 = POISON;                                                                           \
    asm volatile("" : "+v"(pz1));                                                                    \
    st16(u32x4{pz1, pz1, pz1, pz1}, rs, (w >= 4 ? OFF_PART + (L) * (1u << 20) + bid * 4096 - 4096 : OFF_STAT + (L) * 524288 + (p * 32 + h) * 2048 - 2048) + tid * 16, 0); \
  }
  u32x4 gv[4], uv[4];
  u32x4 keep[9];
  u32x4 pre[4][2];
#define PRE_LOAD(it_, rot_, s0, s1)                                                                  \
  static_for<(s1) - (s0)>([&](auto c_) __attribute__((always_inline)) {                              \
    constexpr int s = (s0) + decltype(c_)::value;                                                    \
    const unsigned ab_ = OFF_XN + (it_) * (4u << 20) + (((rot_) + (s >> 2)) & 7) * 8192 + ((s >> 1) & 1) * 4096; \
    ld16w<2 * s, (s & 1) * 2048>(pre[s][0], rs, l * 16, ab_ + (p * 8 + rt) * 65536);                         \
    ld16w<2 * s + 1, (s & 1) * 2048 + 1024>(pre[s][1], rs, l * 16, ab_ + (p * 8 + rt) * 65536);                 \
  })
  u32x2 T;
  const GAS bf16* tsrc;
  unsigned xv[2], xa[2];
  u32x2 ep;
#define X_READ(u, d0, d1) { d0 = *(const LDS_AS u32x4*)(size_t)(xa[0] + x_slot(u)); d1 = *(const LDS_AS u32x4*)(size_t)(xa[1] + x_slot(u)); }
  const rsrc_t rx = RSRC(x, 2048 * 1024 * 2);
  {
    IDS();
    const int rot = PROT();
    const bf16* pwq = wqkv;
    const unsigned wqv = WQ_VOFF();
    xv[0] = (l >> 3) * 2048 + (((l & 7) ^ (l >> 4)) << 4); xv[1] = xv[0] ^ 64;
#define WQ_KMAJ(q) (((q) % 3) * 8 + (q) / 3)
    WQ_PIECE(0, WQ_KMAJ(w), L_RING);
    X_REQ(0, 0); X_REQ(0, 1); X_REQ(1, 0); X_REQ(1, 1);
    WQ_PIECE(0, WQ_KMAJ(8 + w), L_RING);
    asm volatile("s_barrier" ::: "memory");
    WQ_PIECE(0, WQ_KMAJ(16 + w), L_RING);
    X_REQ(2, 0); X_REQ(2, 1);
    asm volatile("buffer_load_dwordx2 %0, off, %1, 0 sc1" : "=v"(ep) : "s"(rs) : "memory");
    if constexpr (PBS < 0) { POISON_BIG(0); }
    {
      ARGP(pwo, 3); ARGP(pwg, 5); ARGP(pwu, 6);
      const int t = tid < 288 ? tid - 64 : tid - 288, go = (bi * 32 + t / 7) * F_ + xcd * 448 + (t % 7) * 64;
      tsrc = tid < 64 ? pwo + bid * 4096 + tid * 64 : tid < 288 ? pwg + go : pwu + go;
      asm volatile("" : "+v"(tsrc));
    }
#pragma unroll
    for (int kk = 0; kk < 2; ++kk) {
      const int r = l & 15;
      xa[kk] = lbase + rt * 2048 + (r >> 3) * 1024 + (r & 7) * 128 + (((4 * kk + g) ^ (r >> 1)) << 4);
      asm volatile("" : "+v"(xa[kk]));
    }
    if (tid == 192) *(LDS_AS u32x4*)(smem + L_QC) = u32x4{0u, 0u, 0u, 0u};
    wait_vm<4>();
    X_READ(0, pre[0][0], pre[1][0]); X_READ(1, pre[0][1], pre[1][1]);
    asm volatile("s_waitcnt lgkmcnt(0)\n\ts_barrier" : "+v"(pre[0][0]), "+v"(pre[0][1]), "+v"(pre[1][0]), "+v"(pre[1][1])::"memory");
    X_REQ(3, 0); X_REQ(3, 1); X_REQ(4, 0); X_REQ(4, 1);
  }
  unsigned sel0 = 0x7FFFFFFFu;
  u32x4 x2p[2];
  unsigned xm0 = 0, kg0 = 0;

  auto layer = [&](auto it_c) __attribute__((always_inline)) {
    constexpr int it = decltype(it_c)::value;
    f32x4 acc[2][6];
    f32x2 qs2[2];
    u32x4 k0, k1, v0, v1;
    unsigned tdum = 0;
    {
      IDS();
      bool aff = false;
      unsigned aq[2];
      u32x4 av[2];
#define PIN_ACC() asm volatile("" : "+{v[176:179]}"(acc[0][0]), "+{v[180:183]}"(acc[0][1]), "+{v[184:187]}"(acc[0][2]), "+{v[188:191]}"(acc[0][3]), "+{v[192:195]}"(acc[0][4]), "+{v[196:199]}"(acc[0][5]), \
                               "+{v[200:203]}"(acc[1][0]), "+{v[204:207]}"(acc[1][1]), "+{v[208:211]}"(acc[1][2]), "+{v[212:215]}"(acc[1][3]), "+{v[216:219]}"(acc[1][4]), "+{v[220:223]}"(acc[1][5]))
#define PINV4(x) asm volatile("" : "+v"(x[0]), "+v"(x[1]), "+v"(x[2]), "+v"(x[3])::"memory")
      auto qkv_loop = [&](auto it_c) __attribute__((always_inline)) {
        constexpr int IT = decltype(it_c)::value;
        const unsigned abase = OFF_XN + IT * (4u << 20) + (p * 8 + rt) * 65536;
        const int rot = PROT() + 5 * IT;
        unsigned fkey = 0, kv, bq[2];
        u32x4 fx[4], bv[2], af[WA][2];
        u32x2 cxn, cx1;
        f32x4 sa, sb, cs4[2];
        f32x2 b2[2];
#define AB_OFF() (OFF_AB + IT * 16384 + (p * 8 + rt) * 128)
#define A_LOAD1(k, a) ld16w<(k) % WA * 2 + (a), ((k) & 1) * 2048 + (a) * 1024>(af[(k) % WA][a], rs, l * 16, abase + ((((k) >> 2) + rot) & 7) * 8192 + (((k) >> 1) & 1) * 4096)
#pragma unroll
        for (int s = 0; s < 2 + 2 * IT; ++s) { af[s][0] = pre[s][0]; af[s][1] = pre[s][1]; }
        const bf16* pwq = wqkv;
        const int tstep = TS0 + bid % TSN;
        const unsigned wqv = WQ_VOFF();
#define QKV_DMA1(t, i)                                                                            \
  {                                                                                               \
    if constexpr (IT == 0) WQ_PIECE(t, 3 * w + (i), L_RING + SLOT(t) * STAGE);                    \
    else dma16s<(i) * 1024>(rs, l * 16, OFF_WI + (h * 8 + (((t) + rot) & 7)) * STAGE + w * 3072, lbase + L_RING + SLOT(t) * STAGE + w * 3072); \
  }
        unsigned bo[2][2];
#pragma unroll
        for (int dt = 0; dt < 2 - IT; ++dt) {
          bo[dt][0] = lbase + L_RING + (IT ? l * 16 : WQ_BOFF(dt)); bo[dt][1] = bo[dt][0] + 2 * STAGE;
          asm volatile("" : "+v"(bo[dt][0]), "+v"(bo[dt][1]));
        }
#define FRAG_T() (lbase + L_RING + (4 * g + ((l & 15) >> 2)) * 64 + (l & 3) * 8 + (((g ^ (g >> 1)) & 1) << 5))
        auto frag_t2 = [&](unsigned o) __attribute__((always_inline)) {
          frag_t t;
          t.hf.a = trabs(o); t.hf.b = trabs(o + 1024);
          return t.v;
        };
        unsigned ka[3];
        auto tile_sum = [&](u32x4 pt, int dst) __attribute__((always_inline)) {
          f32x4 c = __builtin_bit_cast(f32x4, pt);
#pragma unroll
          for (int e = 0; e < 4; ++e) { c[e] += dppf<0x124>(c[e]); c[e] += dppf<0x128>(c[e]); c[e] = add_x16(c[e]); }
          if ((l & 28) == 0) *(LDS_AS f32x4*)(smem + dst + (w * 2 + (l >> 5)) * 64 + (l & 3) * 16) = c;
        };
        frag_t bfr[6];
#define QKV_BO(s, n) (bo[(n) & 1][SLOT((s) >> 2) >> 1] + (SLOT((s) >> 2) & 1) * STAGE + ((n) >> 1) * 8192 + ((s) & 3) * 2048)
#define QKV_RDA(s, n)                                                                             \
  {                                                                                               \
    if constexpr (IT == 0) bfr[n].hf.a = trabs(QKV_BO(s, n));                                     \
    else bfr[n].v = *(const LDS_AS u32x4*)(size_t)(bo[0][SLOT((s) >> 2) >> 1] + (SLOT((s) >> 2) & 1) * STAGE + (((s) & 3) * 6 + (n)) * 1024); \
  }
#define QKV_RDB(s, n) { if constexpr (IT == 0) bfr[n].hf.b = trabs(QKV_BO(s, (n) ^ 1) + 256); }
#pragma unroll
        for (int n = 0; n < 6; ++n) { QKV_RDA(0, n); if (n < 5) QKV_RDB(0, n); }
#define XF_OFF() (l & 8 ? ((l & 7) * RX + bi % RX) * 256 + h * 8 : (p * RX + (xcd >> 1) % RX) * 256 + (4 * rt + (l & 3)) * 8)
#define XP_OFF() (OFF_XNEW + ((rot + FXT) & 7) * 256)
#define FP_OFF(i) (w == 3 ? OFF_XNEW + (i) * 1024 : OFF_FX + h * 3072 + w * 1024)
#define FX_OK(n) __all(((n) == 4 ? max(max(umax4(fx[0]), umax4(fx[1])), max(umax4(fx[2]), umax4(fx[3]))) : max(umax4(fx[0]), umax4(fx[1]))) != POISON)
#define SINCE(s0) (vm_at(IT, s, n) - vm_at(IT, s0, 5))
#define DEF4(x) asm volatile("" : "=v"(x[0]), "=v"(x[1]), "=v"(x[2]), "=v"(x[3]))
        LDS_AS unsigned* qcnt = (LDS_AS unsigned*)(smem + L_QC);
        unsigned qc;
        unsigned qadr = lbase + L_QC, qone = 1;
        asm volatile("" : "+v"(qadr), "+v"(qone));
#define count_in(i) asm volatile("s_mov_b64 exec, 1\n\tds_add_u32 %0, %1 offset:%2\n\ts_mov_b64 exec, -1" ::"v"(qadr), "v"(qone), "n"(4 * (i)) : "memory")
        auto count_wait = [&](int i, unsigned want) __attribute__((always_inline)) {
          if (__builtin_expect((unsigned)__builtin_amdgcn_readfirstlane(qc) < want, 0)) {
            while ((unsigned)__builtin_amdgcn_readfirstlane(__hip_atomic_load(qcnt + i, __ATOMIC_RELAXED, __HIP_MEMORY_SCOPE_WORKGROUP)) < want) {}
          }
          asm volatile("" ::: "memory");
        };
        unsigned long long xok = xm0;
        auto x1_need = [&](unsigned long long m) __attribute__((always_inline)) {
          if (__builtin_expect((xok & m) != m, 0)) {
            for (unsigned spins = 0;;) {
              const u32x2 f = ld8(rs, F_X1 + ((p * RX + (xcd >> 1) % RX) * 32 + (l & 31)) * SX, 0);
              xok = __builtin_amdgcn_ballot_w64(f[0] == T[0] && f[1] == T[1]);
              if ((xok & m) == m) break;
              if (spin_fail(spins, rs, T)) { xok = ~0ull; break; }
              __builtin_amdgcn_s_sleep(KSL);
            }
            asm volatile("" ::: "memory");
          }
        };
        auto gap = [&](auto sc_, auto nc_) __attribute__((always_inline)) {
          constexpr int s = decltype(sc_)::value, n = decltype(nc_)::value;
          {
            constexpr int ts = (s + 3) / 4, tc = (s + 2) / 4, t0 = 1;
            if constexpr (ts >= 3 && ts < 8 && s == 4 * ts - 3 && n == 2) { wait_vm<vm_at(IT, s, n) - vm_tile(IT, ts)>(); count_in(0); }
            if constexpr (!IT && tc >= t0 && tc < 3 && s == 4 * tc - 2 && n == 1) { wait_vm<vm_at(IT, s, n) - vm_tile(IT, tc)>(); count_in(0); }
            if constexpr (IT && ts >= t0 && ts < 3 && s == 4 * ts - 3 && n == 2) count_in(0);
            if constexpr (tc >= t0 && tc < 8 && s == 4 * tc - 2) {
              if constexpr (n == 3) qc = __hip_atomic_load(qcnt, __ATOMIC_RELAXED, __HIP_MEMORY_SCOPE_WORKGROUP);
              if constexpr (n == 5) count_wait(0, 8 * (tc - t0 + 1));
            }
          }
          if constexpr (n == 0 && tile_piece(s)) QKV_DMA1(((s - 3) >> 2) + 3, (s - 3) & 3);
          if constexpr (IT == 0) {
            if constexpr (s == 0 && !(n & 1)) QKV_DMA1(1, n >> 1);
            if constexpr (s >= 1 && s < 4 && n == 2) QKV_DMA1(2, s - 1);
            constexpr int u = s + XL;
            if constexpr (n == 1 && u < 32) {
              wait_vm<vm_at(0, s, n) - vm_x(u)>();
              X_READ(u, af[(u & ~1) % WA][u & 1], af[(u | 1) % WA][u & 1]);
            }
            if constexpr (n == 3 && u + XU < 32) { asm volatile("s_waitcnt lgkmcnt(%0)" ::"n"(s + 1 < QL ? 4 : 3) : "memory"); X_REQ(u + XU, 0); }
            if constexpr (n == 5 && u + XU < 32) X_REQ(u + XU, 1);
            if constexpr (s == WTS && n == WTG) {
              const int i = bi * 192 + w * 24 + min(l, 23);
              touch(tdum, (const GAS bf16*)pwq + (size_t)(i / 12 + 512 * (~xcd & 1)) * 3072 + ((i % 12) >> 2) * 1024 + (xcd >> 1) * 256 + (i & 3) * 64);
            }
            if constexpr (s == PBS && n == 0) { POISON_BIG(0); }
            if constexpr (s == 0 && n == 1) count_in(2);
            if constexpr (s == 0 && n == 3) qc = __hip_atomic_load(qcnt + 2, __ATOMIC_RELAXED, __HIP_MEMORY_SCOPE_WORKGROUP);
            if constexpr (s == 0 && n == 5) count_wait(2, 8);
            if constexpr (s == 1 && n == 1) {
              asm volatile("" : "+v"(ep)::"memory");
              const unsigned long long e = (((unsigned long long)(unsigned)__builtin_amdgcn_readfirstlane(ep[1]) << 32) | (unsigned)__builtin_amdgcn_readfirstlane(ep[0])) + 1ull + dispatch_id() * 0x9E3779B97F4A7C16ull;
              T = u32x2{(unsigned)e, (unsigned)(e >> 32)};
            }
            if constexpr (s == RWL && n == 4) {
              DEF4(fx);
              if (w < 4) ldrows(fx, rx, (l >> 5) * 2048 + (l & 31) * 16, (p * 256 + h * 8 + 2 * w) * 2048);
            }
            if constexpr (s == RWS && n == RWG) {
              if (w < 4) {
                wait_vm<SINCE(RWL)>();
                PINV4(fx);
                f32x2 sv[16];
#pragma unroll
                for (int i = 0; i < 4; ++i) unpack8v(fx[i], sv + 4 * i);
                f32x2 t0 = sv[0] + sv[1], t1 = sv[2] + sv[3];
#pragma unroll
                for (int j = 4; j < 16; j += 2) { t0 += sv[j]; t1 += sv[j + 1]; }
                const float m = sum32(hadd(t0 + t1)) * (1.0f / 1024.0f);
                f32x2 m2 = {m, m}, q0 = {0.f, 0.f}, q1 = {0.f, 0.f};
                asm volatile("" : "+v"(m2));
#pragma unroll
                for (int j = 0; j < 16; j += 2) {
                  const f32x2 d0 = sv[j] - m2, d1 = sv[j + 1] - m2;
                  q0 = __builtin_elementwise_fma(d0, d0, q0); q1 = __builtin_elementwise_fma(d1, d1, q1);
                }
                const float rd = ln_rd(sum32(hadd(q0 + q1)) * (1.0f / 1024.0f));
                if ((l & 30) == 0) st4(__builtin_bit_cast(unsigned, (l & 1) ? -rbf1(m) : rd), rs, (l >> 5) * 4 + (l & 1) * 8192, OFF_AB + (p * 256 + h * 8 + 2 * w) * 4);
              }
            }
            if constexpr (s == POI && n == 4) {
              if (w == 3) {
                unsigned pz1 = POISON;
                asm volatile("" : "+v"(pz1));
                const unsigned prb = 2 * p + (h >> 4), pct = h & 15, m = l - 32;
                static_assert(OFF_CB == OFF_CA + 131072, "poison addressing");
                if (l < 60) st16(u32x4{pz1, pz1, pz1, pz1}, rs, l < 32 ? OFF_CA + (l & 8) * 4096 + (l & 16) * 8192 + prb * 2048 + pct * 128 + (l & 7) * 16
                                                             : l < 56 ? OFF_FX + ((h * 6 + (m >> 2)) * 8 + p) * 64 + (l & 3) * 16
                                                                      : OFF_AB + 16384 + (p * 32 + h) * 32 + (l & 1) * 16 + (l & 2) * 4096, 0);
                if (l < 4) st4(pz1, rs, l < 2 ? OFF_XNEW + bid * 8 + l * 4 : OFF_KEY + ((l & 1) * 16 + pct) * cmax(16 * SK, 128) + prb * SK, 0);
              }
            }
            static_assert(vm_x(EIN + XL) >= vm_at(0, EIN - 3, 5) && IMG >= 1 && CSD >= 1, "first flag");
            if constexpr (s == EIN && n == 1) { if (w < 4) count_in(1); }
            if constexpr (s == EIN + 2 && n == 0) qc = __hip_atomic_load(qcnt + 1, __ATOMIC_RELAXED, __HIP_MEMORY_SCOPE_WORKGROUP);
            if constexpr (s == EIN + 2 && n == 2) {
              if (w == 0) { count_wait(1, 4); if (l < RX) st8(T, rs, F_XN + (p * RX + l) * 256 + h * 8, 0); }
            }
            if constexpr (s == ERQ && n == 4) asm volatile("buffer_load_dwordx2 %0, %1, %2, 0 offen sc1" : "=v"(cxn) : "v"(XF_OFF() + F_XN), "s"(rs) : "memory");
            if constexpr (s == CSL && n == 2) {
              wait_vm<SINCE(ERQ)>();
              asm volatile("" : "+v"(cxn)::"memory");
              if (!__all(cxn[0] == T[0] && cxn[1] == T[1])) poll_flags(rs, XF_OFF(), F_XN, T);
            }
          } else {
#define X1_OFF() (F_X1 + ((p * RX + (xcd >> 1) % RX) * 32 + (l & 31)) * SX)
            if constexpr ((s == 0 || s == 4) && n == 2) asm volatile("buffer_load_dwordx2 %0, %1, %2, 0 offen sc1" : "=v"(cxn) : "v"(X1_OFF()), "s"(rs) : "memory");
            if constexpr ((s == 3 || s == 7) && n == 0) {
              wait_vm<vm_at(1, s, 1) - vm_at(1, s - 3, 3)>();
              asm volatile("" : "+v"(cxn)::"memory");
              xok |= __builtin_amdgcn_ballot_w64(cxn[0] == T[0] && cxn[1] == T[1]);
            }
            if constexpr (a_req(s, n) >= 0 && (a_req(s, n) & 7) == 0) x1_need(0x30003ull << (((rot + a_req(s, n) / 8) & 7) * 2));
          }
          if constexpr (IT && a_req(s, n) >= 0) A_LOAD1(a_req(s, n) / 2, a_req(s, n) & 1);
          if constexpr (IT == 0 && s == CSD && n == CSG) {
            if (w < 3) {
              unsigned a0 = FRAG_T() + SLOT(s >> 2) * STAGE + w * 8192, a1 = a0 ^ 32, one2 = 0x3F803F80u;
              asm volatile("" : "+v"(a0), "+v"(a1), "+v"(one2));
              const u32x4 o4 = {one2, one2, one2, one2};
              f32x4 y0 = {0.f, 0.f, 0.f, 0.f}, y1 = y0;
#pragma unroll
              for (int i = 0; i < 4; ++i) { y0 = mfma16(frag_t2(a0 + i * 2048), o4, y0); y1 = mfma16(frag_t2(a1 + i * 2048), o4, y1); }
              if ((l & 14) == 0) st16(__builtin_bit_cast(u32x4, (l & 1) ? y1 : y0), rs, g * 16 + (l & 1) * 512, OFF_CS + ((h * 6 + 2 * w) * 8 + p) * 64);
            }
          }
          if constexpr (!IT && s == CSL && n == 4) { asm volatile("" : "=v"(fx[0])); if (w < 3) ld16h<0>(fx[0], rs, l * 16, OFF_CS + h * 3072 + w * 1024); }
          if constexpr (!IT && s == CSS && n == 2) {
            if (w < 3) {
              wait_vm<SINCE(CSL)>();
              asm volatile("" : "+v"(fx[0])::"memory");
              tile_sum(fx[0], L_CSL);
            }
          }
          if constexpr (s == abl(IT) && n == 4) ld4h<0>(aq[0], rs, (l & 31) * 4 + (l >> 5) * 8192, AB_OFF());
          if constexpr (s == ABS && n == 0) {
            wait_vm<SINCE(abl(IT))>();
            asm volatile("" : "+v"(aq[0])::"memory");
            if constexpr (IT) {
              for (unsigned spins = 0; !__all(aq[0] != POISON) && !spin_fail(spins, rs, T);) {
                ld4h<0>(aq[0], rs, (l & 31) * 4 + (l >> 5) * 8192, AB_OFF());
                asm volatile("s_waitcnt vmcnt(0)" : "+v"(aq[0])::"memory");
              }
            }
            *(LDS_AS unsigned*)(smem + L_K + (8 + rt) * 2048 + l * 4) = aq[0];
          }
          if constexpr (s == ABS && n == 2) {
#pragma unroll
            for (int a = 0; a < 2; ++a) {
              const LDS_AS char* ab = (const LDS_AS char*)(smem + L_K + (8 + rt) * 2048 + a * 64);
              aq[a] = *(const LDS_AS unsigned*)(ab + (l & 15) * 4); bq[a] = *(const LDS_AS unsigned*)(ab + 128 + (l & 15) * 4);
              av[a] = *(const LDS_AS u32x4*)(ab + g * 16); bv[a] = *(const LDS_AS u32x4*)(ab + 128 + g * 16);
            }
          }
          if constexpr (s == ABS && n == 5) {
#pragma unroll
            for (int a = 0; a < 2; ++a) { b2[a] = f32x2{asf(bq[a]), asf(bq[a])}; asm volatile("" : "+v"(b2[a])); }
          }
          if constexpr (s == ABS + 1) {
            constexpr int k = (n + 4) % 6;
            if constexpr (k < 4) cs4[k & 1] = *(const LDS_AS f32x4*)(smem + L_CSL + k * 64 + g * 16);
            else cs4[k & 1][0] = *(const LDS_AS float*)(smem + L_CSL + k * 64 + (l & 15) * 4);
          }
          if constexpr ((s == ABS + 1 && n > 0) || (s == ABS + 2 && n == 0)) {
            constexpr int k = (n + 3) % 6;
            if constexpr (k < 4) {
#pragma unroll
              for (int a = 0; a < 2; ++a) acc[a][k] = __builtin_elementwise_fma(cs4[k & 1], f32x4{b2[a][0], b2[a][1], b2[a][0], b2[a][1]}, acc[a][k]);
            } else {
              f32x2 c2 = {cs4[k & 1][0], cs4[k & 1][0]};
              asm volatile("" : "+v"(c2));
#pragma unroll
              for (int a = 0; a < 2; ++a) acc[a][k] = __builtin_elementwise_fma(f32x4{c2[0], c2[1], c2[0], c2[1]}, __builtin_bit_cast(f32x4, bv[a]), acc[a][k]);
            }
          }
          if constexpr (IT == 0) {
            if constexpr (s == KP0 - 1 && n < 3) {
              const int c = 3 * (w & 1) + n;
              ka[n] = (FRAG_T() ^ ((c & 1) << 5)) + (SLOT(5) * STAGE + (c >> 1) * 8192 + (w >> 1) * 2048);
              asm volatile("" : "+v"(ka[n]));
            }
            if constexpr (s >= KP0 && s < KP0 + 3 && (n & 1)) keep[3 * (s - KP0) + (n >> 1)] = frag_t2(ka[n >> 1] + (s - KP0) * STAGE);
            if constexpr (s >= IMG && s < IMG + 3 && n == 4) st16(keep[s - IMG], rs, l * 16 + (s - IMG) * 1024, OFF_WI + (h * 8 + ((rot + 5) & 7)) * STAGE + w * 3072);
            if constexpr (s >= TS0 && s < TS0 + TSN && n == 4) {
              if (tstep == s && w < TW) touch(tdum, tsrc);
            }
            if constexpr (s == TST && n == 4) { if (w >= TW) touch(tdum, tsrc); }
          } else {
            if constexpr (s == 28 && n == 0) { POISON_BIG(1); }
            if constexpr (s == FSL && n == 4) { ld4h<0>(kv, rs, (l & 15) * SK, OFF_KEY + (bid & 15) * cmax(16 * SK, 128)); row_load(rs, g, ffn_xoff(kg0, 0, w), fx); }
            if constexpr (s == FS0 && n == 0) {
              wait_vm<SINCE(FSL)>();
              asm volatile("" : "+v"(kv)::"memory");
              PINV4(fx);
              if (__builtin_expect(!__all(kv != POISON), 0)) {
                LDS_AS unsigned* kw = (LDS_AS unsigned*)(smem + L_SKEY + 40);
                if (w == 0) {
                  kv = key_wait(rs, T, l, 0, bid & 15, kv);
                } else {
                  while ((kv = __hip_atomic_load(kw, __ATOMIC_RELAXED, __HIP_MEMORY_SCOPE_WORKGROUP)) == POISON) __builtin_amdgcn_s_sleep(2);
                }
              }
              fkey = __builtin_amdgcn_readfirstlane(umax16(kv));
              if (w == 0) *(LDS_AS unsigned*)(smem + L_SKEY + 40) = min(fkey, POISON - 1);
              sel0 = key_row(fkey);
              aff = (int)(sel0 >> 8) == p;
              asm volatile("" : "=v"(sa), "=v"(sb));
              if (ffn_on) {
                if (__builtin_expect(((kg0 ^ fkey) & 0x780u) != 0, 0)) ld4x16(fx, rs, g * 16, ffn_xoff(fkey, 0, w));
                ffn_gu(gv, uv, fx, sa, sb);
              }
            }
            if constexpr (s == FS0 && n == 2) {
              if (ffn_on) {
                ffn_gu_check(rs, T, ffn_xoff(fkey, 0, w), l, gv, uv, fx, sa, sb);
                ffn_gu_store(smem, l, w, sa, sb);
                count_in(1);
              }
            }
            if constexpr (s == FS0D && n == FDG) {
              if (ffn_on && w < 4) {
                s16x4 da[8], db[8];
                ffn_d_read(smem, l, 2 * w, da);
                while (__hip_atomic_load(qcnt + 1, __ATOMIC_RELAXED, __HIP_MEMORY_SCOPE_WORKGROUP) < 8u) {}
                asm volatile("" ::: "memory");
                const f32x4 t = ffn_t_read(smem, l);
                ffn_d_read(smem, l, 2 * w + 1, db);
                u32x2 ha = ffn_h(t, l), hb = {dppu<0x118>(ha[0]), dppu<0x118>(ha[1])};
                f32x4 ya = {0.f, 0.f, 0.f, 0.f}, yb = ya;
#pragma unroll
                for (int i = 0; i < 8; ++i) {
                  ya = mfma16k16(da[i], __builtin_bit_cast(s16x4, ha), ya); yb = mfma16k16(db[i], __builtin_bit_cast(s16x4, hb), yb);
                  ha = u32x2{dppu<0x111>(ha[0]), dppu<0x111>(ha[1])}; hb = u32x2{dppu<0x111>(hb[0]), dppu<0x111>(hb[1])};
                }
                ffn_d_store(rs, l, 8 * w, 0, slice, ya + yb);
              }
            }
#define RED_LOAD(ld, l8)                                                                                                    \
  {                                                                                                                         \
    const unsigned G = xcd * 4 + bi - 28, po = OFF_PART + G * 32768;                                                        \
    ld(gv[0], rs, tid * 16, po); ld(gv[1], rs, tid * 16, po + 4096); ld(gv[2], rs, tid * 16, po + 8192); ld(gv[3], rs, tid * 16, po + 12288); \
    ld(uv[0], rs, tid * 16, po + 16384); ld(uv[1], rs, tid * 16, po + 20480); ld(uv[2], rs, tid * 16, po + 24576);        \
    l8(cxn, rs, (l & 3) * 16 + (l & 4) * 2, OFF_CA + (sel0 >> 7) * 2048 + G * 64);                                         \
    l8(cx1, rs, (l & 3) * 16 + (l & 4) * 2, OFF_CB + (sel0 >> 7) * 2048 + G * 64);                                          \
  }
            if constexpr (s == FS1 && n == 4) {
              asm volatile("" : "=v"(cxn), "=v"(cx1));
              if (!ffn_on && w < 4) RED_LOAD(ld16u, ld8h);
            }
            if constexpr (s == FS1S && n == 0) {
              if (!ffn_on && w < 4) {
                wait_vm<SINCE(FS1)>();
                PINV4(gv); PINV4(uv);
                asm volatile("" : "+v"(cxn), "+v"(cx1)::"memory");
                for (unsigned spins = 0; !__all(max(max(max(umax4(gv[0]), umax4(gv[1])), max(umax4(gv[2]), umax4(gv[3]))),
                                                    max(max(umax4(uv[0]), umax4(uv[1])), max(max(umax4(uv[2]), max(cxn[0], cxn[1])), max(cx1[0], cx1[1])))) != POISON) && !spin_fail(spins, rs, T);) {
                  RED_LOAD(ld16u, ld8h);
                  DRAIN();
                  PINV4(gv); PINV4(uv);
                  asm volatile("" : "+v"(cxn), "+v"(cx1)::"memory");
                }
#define F4(x) __builtin_bit_cast(f32x4, x)
                ((LDS_AS f32x4*)(smem + L_SFIN))[tid] = ((F4(gv[0]) + F4(gv[1])) + (F4(gv[2]) + F4(gv[3]))) + ((F4(uv[0]) + F4(uv[1])) + F4(uv[2]));
#undef F4
                count_in(2);
                if (w == 0) {
                  while (__hip_atomic_load(qcnt + 2, __ATOMIC_RELAXED, __HIP_MEMORY_SCOPE_WORKGROUP) < 4u) {}
                  asm volatile("" ::: "memory");
                  const u32x2 ov = red_final<4>(smem, l, fkey, cxn, cx1);
                  if (l < 8) st8(unpoison2(ov), rs, OFF_XNEW + ((xcd * 4 + bi - 28) * 8 + (l & 3) * 2 + (l >> 2)) * 8, 0);
                }
              }
            }
            if constexpr (s == FXA && n == 4) { DEF4(fx); if (w < 3) row_load(rs, g, XP_OFF(), fx); }
            if constexpr (s == FXB && n == 0) {
              if (w < 3) {
                const unsigned fa = bo[0][0] + SLOT(FXT) * STAGE + w * 2048;
                u32x4 f[8];
#pragma unroll
                for (int i = 0; i < 8; ++i) f[i] = *(const LDS_AS u32x4*)(size_t)(fa + (i >> 1) * 6144 + (i & 1) * 1024);
                wait_vm<SINCE(FXA)>();
                PINV4(fx);
                ffn_xwait<XSL>(rs, T, XP_OFF(), l, fx);
                f32x4 y0 = mfma16(f[0], fx[0], f32x4{0.f, 0.f, 0.f, 0.f}), y1 = mfma16(f[1], fx[0], f32x4{0.f, 0.f, 0.f, 0.f});
#pragma unroll
                for (int i = 1; i < 4; ++i) { y0 = mfma16(f[2 * i], fx[i], y0); y1 = mfma16(f[2 * i + 1], fx[i], y1); }
                if ((l & 14) == 0) st16(unpoison4(__builtin_bit_cast(u32x4, (l & 1) ? y1 : y0)), rs, g * 16 + (l & 1) * 512, OFF_FX + ((h * 6 + 2 * w) * 8 + p) * 64);
              }
            }
            if constexpr (s == FXL && n == 4) {
              asm volatile("" : "=v"(fx[0]), "=v"(fx[1]));
              if (aff && w < 4) { ld16h<0>(fx[0], rs, l * 16, FP_OFF(0)); ld16h<0>(fx[1], rs, l * 16, FP_OFF(1)); }
            }
            if constexpr (s == FXS && n == 2) {
              if (aff && w < 4) {
                wait_vm<0>();
                asm volatile("" : "+v"(fx[0]), "+v"(fx[1])::"memory");
                if (!FX_OK(2)) {
                  unsigned spins = 0;
                  do {
                    if (spin_fail(spins, rs, T)) break;
                    fx[0] = ld16(rs, l * 16, FP_OFF(0)); fx[1] = ld16(rs, l * 16, FP_OFF(1));
                  } while (!FX_OK(2));
                }
                if (w == 3) {
                  f32x2 nx[8];
                  unpack8v(fx[0], nx); unpack8v(fx[1], nx + 4);
                  const f32x2 sm = ((nx[0] + nx[1]) + (nx[2] + nx[3])) + ((nx[4] + nx[5]) + (nx[6] + nx[7]));
                  const float mean = sum64(hadd(sm)) * (1.0f / 1024.0f);
                  const f32x2 m2 = {mean, mean};
                  f32x2 q = {0.f, 0.f};
#pragma unroll
                  for (int j = 0; j < 8; ++j) { const f32x2 d = nx[j] - m2; q = __builtin_elementwise_fma(d, d, q); }
                  const float rd = ln_rd(sum64(hadd(q)) * (1.0f / 1024.0f));
                  if (l == 0) *(LDS_AS f32x2*)(smem + L_FXL + 384) = f32x2{rd, -rbf1(mean)};
                } else {
                  tile_sum(fx[0], L_FXL);
                }
                count_in(3);
              }
            }
          }
        };
        static_for<32>([&](auto sc_) __attribute__((always_inline)) {
          constexpr int s = decltype(sc_)::value;
          if constexpr (s > 0) PIN_ACC();
          if constexpr (IT == 1) wait_vm<vm_at(IT, s, 0) - vm_a(IT, s)>();
          pin_w<s % WA>(af[s % WA][0], af[s % WA][1]);
          SB();
          static_for<6>([&](auto nc_) __attribute__((always_inline)) {
            constexpr int n = decltype(nc_)::value;
            if constexpr (s == 0) { acc[0][n] = f32x4{0.f, 0.f, 0.f, 0.f}; acc[1][n] = acc[0][n]; }
            constexpr bool on = s < QL || n >= 2;
            if constexpr (on) acc[0][n] = n < 4 ? mfma16(bfr[n].v, af[s % WA][0], acc[0][n]) : mfma16(af[s % WA][0], bfr[n].v, acc[0][n]);
            SB();
            if constexpr (n == 0) { QKV_RDB(s, 5); }
            else if constexpr (s + 1 < 32 && (s + 1 < QL || n > 2)) QKV_RDB(s + 1, n - 1);
            SB();
            if constexpr (on) acc[1][n] = n < 4 ? mfma16(bfr[n].v, af[s % WA][1], acc[1][n]) : mfma16(af[s % WA][1], bfr[n].v, acc[1][n]);
            SB();
            if constexpr (s + 1 < 32 && (s + 1 < QL || n >= 2)) QKV_RDA(s + 1, n);
            SB();
            gap(sc_, nc_);
            SB();
          });
        });
        PIN_ACC();
        frag_t qw[32 - QL][2];
        static_for<32 - QL>([&](auto c_) __attribute__((always_inline)) {
          constexpr int s = QL + decltype(c_)::value;
          QKV_RDA(s, 0); QKV_RDB(s, 0); QKV_RDA(s, 1); QKV_RDB(s, 1);
          qw[s - QL][0] = bfr[0]; qw[s - QL][1] = bfr[1];
        });
        SB();
        const bool own = aff && (int)((sel0 >> 5) & 7) == rt;
        const bool m0 = !(sel0 & 16), mq = (l & 15) == (int)(sel0 & 15), mv = g == (int)((sel0 & 15) >> 2);
        auto patch_qk = [&](int n) __attribute__((always_inline)) {
          f32x4 t = *(const LDS_AS f32x4*)(smem + L_FXL + n * 64 + g * 16);
          const f32x4 c = *(const LDS_AS f32x4*)(smem + L_CSL + n * 64 + g * 16);
          const float nb = *(const LDS_AS float*)(smem + L_FXL + 388);
#pragma unroll
          for (int e = 0; e < 4; ++e) { t[e] = __builtin_fmaf(c[e], nb, t[e]); asm volatile("" : "+v"(t[e])); }
          acc[0][n] = (mq && m0) ? t : acc[0][n];
          acc[1][n] = (mq && !m0) ? t : acc[1][n];
        };
        if (own) {
          while (__hip_atomic_load(qcnt + 3, __ATOMIC_RELAXED, __HIP_MEMORY_SCOPE_WORKGROUP) < 4u) {}
          asm volatile("" ::: "memory");
          patch_qk(2); patch_qk(3);
          const f32x2 nab = *(const LDS_AS f32x2*)(smem + L_FXL + 384);
          const LDS_AS float* fl_ = (const LDS_AS float*)(smem + L_FXL + 256) + (l & 15);
          const LDS_AS float* cl_ = (const LDS_AS float*)(smem + L_CSL + 256) + (l & 15);
          const float t4 = __builtin_fmaf(cl_[0], nab[1], fl_[0]), t5 = __builtin_fmaf(cl_[16], nab[1], fl_[16]);
          const unsigned na = __builtin_bit_cast(unsigned, (float)nab[0]);
#pragma unroll
          for (int a = 0; a < 2; ++a) {
            const bool ma = a ? !m0 : m0;
            aq[a] = (mq && ma) ? na : aq[a];
#pragma unroll
            for (int e = 0; e < 4; ++e) {
              const bool me = mv && ma && e == (int)(sel0 & 3);
              acc[a][4][e] = me ? t4 : acc[a][4][e]; acc[a][5][e] = me ? t5 : acc[a][5][e];
              av[a][e] = me ? na : av[a][e];
            }
          }
        }
        constexpr float C2 = 1.4426950408889634f * 0.17677669529663687f;
#pragma unroll
        for (int a = 0; a < 2; ++a) {
          f32x2 a2 = {asf(aq[a]), asf(aq[a])};
          qs2[a] = f32x2{asf(aq[a]) * C2, asf(aq[a]) * C2};
          asm volatile("" : "+v"(a2), "+v"(qs2[a]));
          acc[a][2] *= f32x4{a2[0], a2[1], a2[0], a2[1]}; acc[a][3] *= f32x4{a2[0], a2[1], a2[0], a2[1]};
          acc[a][4] *= __builtin_bit_cast(f32x4, av[a]); acc[a][5] *= __builtin_bit_cast(f32x4, av[a]);
        }
        k0 = pack8(acc[0][2], acc[0][3]); k1 = pack8(acc[1][2], acc[1][3]); v0 = pack8(acc[0][4], acc[1][4]); v1 = pack8(acc[0][5], acc[1][5]);
        const unsigned kvo = OFF_KV + IT * (8u << 20) + (p * 32 + h) * 32768 + rt * 4096;
        if (p < 7) { st16(k0, rs, l * 16, kvo); st16(k1, rs, l * 16 + 1024, kvo); st16(v0, rs, l * 16 + 2048, kvo); st16(v1, rs, l * 16 + 3072, kvo); }
        char* ownp = smem + (8 + rt) * 2048 + l * 16;
        *(u32x4*)(ownp + L_K) = k0;
        if (rt < 7) *(u32x4*)(ownp + L_K + 1024) = k1;
        *(u32x4*)(ownp + L_V) = v0; *(u32x4*)(ownp + L_V + 1024) = v1;
        SB();
        static_for<32 - QL>([&](auto c_) __attribute__((always_inline)) {
          constexpr int s = QL + decltype(c_)::value;
#pragma unroll
          for (int n = 0; n < 2; ++n) { acc[0][n] = mfma16(qw[s - QL][n].v, af[s % WA][0], acc[0][n]); acc[1][n] = mfma16(qw[s - QL][n].v, af[s % WA][1], acc[1][n]); }
        });
        if (own) { patch_qk(0); patch_qk(1); }
      };
      qkv_loop(it_c);
    }
#undef A_LOAD1
#undef QKV_RDA
#undef QKV_RDB
#undef QKV_BO
    {
      IDS();
      const GAS bf16 *pwo, *pwg, *pwu;
      asm volatile("s_load_dwordx2 %0, %3, 24\n\ts_load_dwordx2 %1, %3, 40\n\ts_load_dwordx2 %2, %3, 48" : "=&s"(pwo), "=&s"(pwg), "=&s"(pwu) : "s"(ka));
      const int ql = l & 15;
      const f32x4 z4 = {0.f, 0.f, 0.f, 0.f}, ninf = {-INFINITY, -INFINITY, -INFINITY, -INFINITY};
      LDS_AS unsigned* rdy = (LDS_AS unsigned*)(smem + L_RDY);
      const unsigned rtag = T[0] + it;
      u32x4 qf[2];
      f32x4 cn[2], oa[2][2], sum[2], S[2][2];
      unsigned one2 = 0x3F803F80u;
      asm volatile("" : "+v"(one2));
      const u32x4 ones = {one2, one2, one2, one2};
      auto ex8 = [&](f32x4 a, f32x4 b) __attribute__((always_inline)) {
        return u32x4{pack2(__builtin_amdgcn_exp2f(a[0]), __builtin_amdgcn_exp2f(a[1])), pack2(__builtin_amdgcn_exp2f(a[2]), __builtin_amdgcn_exp2f(a[3])),
                     pack2(__builtin_amdgcn_exp2f(b[0]), __builtin_amdgcn_exp2f(b[1])), pack2(__builtin_amdgcn_exp2f(b[2]), __builtin_amdgcn_exp2f(b[3]))};
      };
      {
#pragma unroll
        for (int a = 0; a < 2; ++a) {
          const f32x4 c4 = {qs2[a][0], qs2[a][1], qs2[a][0], qs2[a][1]};
          qf[a] = pack8(acc[a][0] * c4, acc[a][1] * c4);
        }
        f32x4 mk;
#pragma unroll
        for (int e = 0; e < 4; ++e) mk[e] = (4 * g + e <= ql) ? 0.f : -INFINITY;
        const f32x4 d0 = mfma16(k0, qf[0], mk), d1 = mfma16(k1, qf[1], mk), d2 = mfma16(k0, qf[1], z4);
        const float m0 = -max_x32(max_x16(fmaxf(fmaxf(d0[0], d0[1]), fmaxf(d0[2], d0[3]))));
        const float m1 = -max_x32(max_x16(fmaxf(fmaxf(d1[0], d1[1]), fmaxf(d1[2], d1[3]))));
        cn[0] = f32x4{m0, m0, m0, m0}; cn[1] = f32x4{m1, m1, m1, m1};
        asm volatile("" : "+v"(cn[0]), "+v"(cn[1]));
        const f32x4 e0 = d0 + cn[0];
        const u32x4 p0 = {pack2(__builtin_amdgcn_exp2f(e0[0]), __builtin_amdgcn_exp2f(e0[1])), pack2(__builtin_amdgcn_exp2f(e0[2]), __builtin_amdgcn_exp2f(e0[3])), 0u, 0u};
        const u32x4 p1 = ex8(d2 + cn[1], d1 + cn[1]);
        oa[0][0] = mfma16(v0, p0, z4); oa[0][1] = mfma16(v1, p0, z4); sum[0] = mfma16(ones, p0, z4);
        oa[1][0] = mfma16(v0, p1, z4); oa[1][1] = mfma16(v1, p1, z4); sum[1] = mfma16(ones, p1, z4);
      }
      const char* kb_ = smem + L_K + l * 16;
      const char* vb_ = smem + L_V + l * 16;
      const unsigned wov = (l >> 3) * 2048 + (((((l & 7) >> 1) ^ ((l >> 4) & 3)) * 2 + (l & 1)) << 4), fnv = (l >> 1) * (F_ * 2) + (l & 1) * 16;
      const unsigned pp = __builtin_amdgcn_readfirstlane(p > 0 ? 32 : 0), pvo = OFF_KV + it * (8u << 20) + (p * 32 + h - pp) * 32768;
      const unsigned pfo = __builtin_amdgcn_readfirstlane(F_KV + (it * 256 + p * 32 + h - pp) * cmax(8 * SV, 128));
      asm volatile("" : "+v"(oa[0][0]), "+v"(oa[0][1]), "+v"(oa[1][0]), "+v"(oa[1][1]), "+v"(sum[0]), "+v"(sum[1]));
      DRAIN();
      asm volatile("" : "+v"(tdum));
      if (l == 0) st8(T, rs, rt * SV, F_KV + (it * 256 + p * 32 + h) * cmax(8 * SV, 128));
      BAR();
      asm volatile("" : "+s"(pwo), "+s"(pwg), "+s"(pwu));
      WO_PIECE(15); WO_PIECE(14);
#define LDS16(a_) (*(const LDS_AS u32x4*)(size_t)(a_))
      u32x4 kf[2], vf[2];
      int nw = 0;
      u32x4 p0, p1;
      float e0, e1;
#define EE(v, i) { e0 = __builtin_amdgcn_exp2f(v[i]); e1 = __builtin_amdgcn_exp2f(v[i + 1]); pin2s(e0, e1); SB(); }
#define CV(P, i) { unsigned t_ = pack2(e0, e1); pin1(t_); P[i] = t_; SB(); }
      auto body = [&](unsigned ad, auto dk_) __attribute__((always_inline)) {
        constexpr int dk = decltype(dk_)::value;
        const f32x4 a0 = S[1][0], a1 = S[1][1];
        sum[0] = mfma16(ones, p0, sum[0]); SB();
        EE(a0, 0);
        lds_wait<2>(kf[0], kf[1]);
        S[0][0] = mfma16(kf[0], qf[0], cn[0]); SB();
        CV(p1, 0); EE(a0, 2);
        S[0][1] = mfma16(kf[1], qf[0], cn[0]); SB();
        CV(p1, 1); EE(a1, 0);
        lds_wait<0>(vf[0], vf[1]);
        oa[0][0] = mfma16(vf[0], p0, oa[0][0]); SB();
        CV(p1, 2); EE(a1, 2);
        oa[0][1] = mfma16(vf[1], p0, oa[0][1]); SB();
        CV(p1, 3);
        S[1][0] = mfma16(kf[0], qf[1], cn[1]); SB();
        EE(S[0][0], 0);
        S[1][1] = mfma16(kf[1], qf[1], cn[1]); SB();
        lds2<2048 + dk>(kf[0], kf[1], ad); SB();
        CV(p0, 0); EE(S[0][0], 2);
        oa[1][0] = mfma16(vf[0], p1, oa[1][0]); SB();
        CV(p0, 1); EE(S[0][1], 0);
        oa[1][1] = mfma16(vf[1], p1, oa[1][1]); SB();
        lds2<2048 + L_V>(vf[0], vf[1], ad); SB();
        CV(p0, 2); EE(S[0][1], 2);
        sum[1] = mfma16(ones, p1, sum[1]); SB();
        CV(p0, 3);
      };
      auto last = [&]() __attribute__((always_inline)) {
        sum[0] = mfma16(ones, p0, sum[0]); SB();
        EE(S[1][0], 0);
        lds_wait<0>(vf[0], vf[1]);
        pin2(kf[0], kf[1]);
        oa[0][0] = mfma16(vf[0], p0, oa[0][0]); SB();
        CV(p1, 0); EE(S[1][0], 2);
        oa[0][1] = mfma16(vf[1], p0, oa[0][1]); SB();
        CV(p1, 1); EE(S[1][1], 0);
        const float f0 = __builtin_amdgcn_exp2f(S[1][1][2]), f1 = __builtin_amdgcn_exp2f(S[1][1][3]);
        p1[2] = pack2(e0, e1); p1[3] = pack2(f0, f1);
        oa[1][0] = mfma16(vf[0], p1, oa[1][0]); oa[1][1] = mfma16(vf[1], p1, oa[1][1]); sum[1] = mfma16(ones, p1, sum[1]);
      };
      auto piece = [&]() __attribute__((always_inline)) {
        if constexpr (it == 1) { WO_PIECE(13 - nw); }
        else if (ffn_on) { FFN_DMA(nw); }
        ++nw;
      };
      if (rt > 0) {
        unsigned ad = lbase + l * 16 + (6 + rt) * 2048;
        lds2<2048>(kf[0], kf[1], ad); lds2<2048 + L_V>(vf[0], vf[1], ad);
        lds_wait<2>(kf[0], kf[1]);
        S[0][0] = mfma16(kf[0], qf[0], cn[0]); S[0][1] = mfma16(kf[1], qf[0], cn[0]); S[1][0] = mfma16(kf[0], qf[1], cn[1]); S[1][1] = mfma16(kf[1], qf[1], cn[1]);
        SB();
        lds_wait<0>(vf[0], vf[1]);
        lds2<0>(kf[0], kf[1], ad);
        p0 = ex8(S[0][0], S[0][1]);
        lds_wait<0>(kf[0], kf[1]);
#pragma unroll 1
        for (int i = rt - 1; i > 0; --i) {
          ad -= 2048;
          body(ad, std::integral_constant<int, -2048>{});
          piece();
          SB();
        }
        piece();
        last();
      }
      {
        constexpr int FW = it ? FW1 : FW0, FN = 8 / FW;
        if (w < FW) {
          const int g0 = FN * rt;
          poll_flags(rs, (l & (FN - 1)) * SV, pfo + g0 * SV, T);
#pragma unroll
          for (int j = 0; j < FN; ++j) {
            const unsigned d = lbase + (g0 + j) * 2048, o = pvo + (g0 + j) * 4096;
            dma16s<0>(rs, l * 16, o, d + L_K); dma16s<1024>(rs, l * 16, o, d + L_K);
            dma16s<0>(rs, l * 16, o + 2048, d + L_V); dma16s<1024>(rs, l * 16, o + 2048, d + L_V);
          }
        }
        if (p == 0) { cn[0] = ninf; cn[1] = ninf; }
        f32x4 mk;
#pragma unroll
        for (int e = 0; e < 4; ++e) mk[e] = (4 * g + e > ql) ? 0.f : -INFINITY;
        const f32x4 cm0 = cn[0] + mk, cm1 = cn[1] + mk;
        if (w < FW) {
          if constexpr (FN > 1) { wait_vm<2 * FN>(); if (l < FN / 2) rdy[FN * rt + l] = rtag; }
          DRAIN();
          if (l < FN) rdy[FN * rt + l] = rtag;
        }
        if constexpr (it == 1) { WO_PIECE(6); WO_PIECE(5); WO_PIECE(4); }
        else { WO_PIECE(13); WO_PIECE(12); if (ffn_on) FFN_DMA(7); }

        int have = rt - 1;
        auto need = [&](int gq) __attribute__((always_inline)) {
          if (__builtin_expect(gq > have && gq < 8, 0)) {
            do {
              const unsigned t = __hip_atomic_load(rdy + (l & 7), __ATOMIC_RELAXED, __HIP_MEMORY_SCOPE_WORKGROUP);
              have = rt - 1 + __builtin_ctz(~((unsigned)__builtin_amdgcn_ballot_w64(t == rtag) & 0xffu) >> rt);
            } while (gq > have);
            asm volatile("" ::: "memory");
          }
        };
        unsigned ad = lbase + l * 16 + rt * 2048;
        need(rt);
        lds2<0>(kf[0], kf[1], ad); lds2<L_V>(vf[0], vf[1], ad);
        lds_wait<2>(kf[0], kf[1]);
        S[0][0] = mfma16(kf[0], qf[0], cm0); S[0][1] = mfma16(kf[1], qf[0], cn[0]); S[1][0] = ninf; S[1][1] = mfma16(kf[1], qf[1], cm1);
        SB();
        lds_wait<0>(vf[0], vf[1]);
        need(rt + 1);
        lds2<2048>(kf[0], kf[1], ad);
        p0 = ex8(S[0][0], S[0][1]);
        lds_wait<0>(kf[0], kf[1]);
#pragma unroll 1
        for (int gq = rt + 1; gq < 8; ++gq) {
          need(gq + 1);
          body(ad, std::integral_constant<int, 2048>{});
          piece();
          SB();
          ad += 2048;
        }
        last();
      }
      {
        float big = 0.f;
#pragma unroll
        for (int a = 0; a < 2; ++a)
#pragma unroll
          for (int d = 0; d < 2; ++d)
            big = __builtin_elementwise_maximum(__builtin_elementwise_maximum(big, __builtin_elementwise_maximum(fabsf(oa[a][d][0]), fabsf(oa[a][d][1]))),
                                                __builtin_elementwise_maximum(fabsf(oa[a][d][2]), fabsf(oa[a][d][3])));
        bool ok = sum[0][0] >= 0.5f && sum[0][0] < 0x1p64f && sum[1][0] >= 0.5f && sum[1][0] < 0x1p64f && big < INFINITY;
        if (!__all(ok)) {
#pragma unroll
          for (int a = 0; a < 2; ++a) {
            const int qs = 2 * rt + a, qq = 256 + 16 * qs + ql, klo = max(qq - 255, p > 0 ? 0 : 256);
            auto score = [&](int t) __attribute__((always_inline)) {
              f32x4 s = mfma16(t == 31 ? k1 : *(const u32x4*)(kb_ + t * 1024), qf[a], z4);
#pragma unroll
              for (int e = 0; e < 4; ++e) { const int kk = 16 * t + 4 * g + e; s[e] = (kk <= qq && kk >= klo) ? s[e] : -INFINITY; }
              return s;
            };
            float mx = -INFINITY;
#pragma unroll 1
            for (int t = qs; t <= qs + 16; ++t) { const f32x4 s = score(t); mx = fmaxf(fmaxf(mx, fmaxf(s[0], s[1])), fmaxf(s[2], s[3])); }
            mx = max_x32(max_x16(mx));
            oa[a][0] = z4; oa[a][1] = z4; sum[a] = z4;
#pragma unroll 1
            for (int G = qs >> 1; G <= (qs + 16) >> 1; ++G) {
              const f32x4 s0 = score(2 * G), s1 = score(2 * G + 1);
              f32x4 e0, e1;
#pragma unroll
              for (int e = 0; e < 4; ++e) { e0[e] = __builtin_amdgcn_exp2f(s0[e] - mx); e1[e] = __builtin_amdgcn_exp2f(s1[e] - mx); }
              const u32x4 pf = pack8(e0, e1);
              oa[a][0] = mfma16(*(const u32x4*)(vb_ + G * 2048), pf, oa[a][0]);
              oa[a][1] = mfma16(*(const u32x4*)(vb_ + G * 2048 + 1024), pf, oa[a][1]);
              sum[a] = mfma16(ones, pf, sum[a]);
            }
          }
        }
      }
#pragma unroll
      for (int a = 0; a < 2; ++a) {
        const float inv = __builtin_amdgcn_rcpf(sum[a][0]);
        oa[a][0] *= inv; oa[a][1] *= inv;
      }
      const unsigned oo = OFF_O + it * (4u << 20) + ((p * 8 + rt) * 64 + h * 2) * 1024;
      st16(pack8(oa[0][0], oa[0][1]), rs, l * 16, oo);
      st16(pack8(oa[1][0], oa[1][1]), rs, l * 16 + 1024, oo);
      DRAIN();
      BAR();
      if (tid == 0) st8(T, rs, h * SO, F_O + (it * 8 + p) * 32 * SO);
      if constexpr (it == 0) {
        DEF4(gv); DEF4(uv);
        if (ffn_on) {
          const unsigned ro = 65536 + w * 1024 + (4 * g + ((l & 15) >> 2)) * 32 + (l & 3) * 8;
#pragma unroll
          for (int i = 0; i < 4; ++i) {
            frag_t f;
            f.hf.a = trread(smem, ro + i * 16384); f.hf.b = trread(smem, ro + i * 16384 + 512);
            gv[i] = f.v;
            f.hf.a = trread(smem, ro + i * 16384 + 8192); f.hf.b = trread(smem, ro + i * 16384 + 8704);
            uv[i] = f.v;
          }
        }
        PINV4(gv); PINV4(uv);
        {
          ARGP(px, 0);
          const unsigned xo = ((rb * 4 + (w & 3)) * 32 + (w >> 2) * 16) * 2048 + ct * 128, xw = (l >> 3) * 2048 + (((l & 7) ^ g) << 4);
          dma16(RSRC(px, 2048 * 1024 * 2), xw, xo, lbase + w * 4096); dma16(RSRC(px, 2048 * 1024 * 2), xw ^ 64, xo + 16384, lbase + w * 4096 + 1024);
        }
      }
      WO_PIECE(3); WO_PIECE(2); WO_PIECE(1); WO_PIECE(0);
      if constexpr (it == 0) { WO_PIECE(11); WO_PIECE(10); WO_PIECE(9); WO_PIECE(8); WO_PIECE(7); WO_PIECE(6); WO_PIECE(5); WO_PIECE(4); }
    }
    IDS();
    ARGP(pwr, 4); ARGP(pwd, 7); ARGP(pout, 8);
    const int kh = w >> 2, rt2 = rb * 4 + (w & 3);
    const unsigned R = rt2 * 32 + kh * 16 + (l & 15);
    const unsigned to = (rt2 * 64 + ct * 4 + kh) * 1024 + l * 16;
    const unsigned rmo = ct * 64 + ((g & 1) ? 12 + 4 * g : 4 * g);
    f32x2 v[8], wv[8];
    float wsum;
    {
      u32x4 wa[2], wrp[2], res[2];
#pragma unroll
      for (int j = 0; j < 2; ++j) {
        wrp[j] = *(const GAS u32x4*)(pwr + rmo + 32 * j);
        wa[j] = *(const GAS u32x4*)(pwr + l * 8 + j * 512);
      }
      {
        f32x2 t[8];
        unpack8v(wa[0], t); unpack8v(wa[1], t + 4);
        const f32x2 s = ((t[0] + t[1]) + (t[2] + t[3])) + ((t[4] + t[5]) + (t[6] + t[7]));
        wsum = sum64(hadd(s));
        unpack8v(trade16(wrp[0]), wv); unpack8v(trade16(wrp[1]), wv + 4);
        asm volatile("" : "+v"(wsum), "+v"(wv[0]), "+v"(wv[1]), "+v"(wv[2]), "+v"(wv[3]), "+v"(wv[4]), "+v"(wv[5]), "+v"(wv[6]), "+v"(wv[7]));
      }
      unsigned bt[4];
#pragma unroll
      for (int n = 0; n < 4; ++n) {
        bt[n] = lbase + 32768 + 65536 + kh * 4096 + (4 * g + ((l & 15) >> 2)) * 128 + (l & 3) * 8 + ((n ^ (2 * (g & 1) + ((l & 15) >> 3))) << 5);
        asm volatile("" : "+v"(bt[n]));
      }
      if (w == 0) poll_flags(rs, (l & 31) * SO, F_O + (it * 8 + (rb >> 1)) * 32 * SO, T);
      else if (it == 0 && w < 3) poll_flags2(rs, F_XN + (((w - 1) * 4 + g) * RX + bi % RX) * 256 + (l & 15) * 16, T);
      else if (it == 0 && w == 3) poll_flags(rs, ((l >> 3) * 32 + h) * cmax(8 * SV, 128) + (l & 7) * SV, F_KV, T);
      asm volatile("s_barrier" ::: "memory");
      const unsigned abase = OFF_O + it * (4u << 20) + (rt2 * 64 + 32 * kh) * 1024;
      const unsigned av0 = l * 16 + kh * 1024, av1 = l * 16 + (kh ^ 1) * 1024;
      u32x4 af[WB][2];
#define O_LOAD(s_) { ld16h<(wo_k(it, s_) & 1) * 2048>(af[(s_) % WB][0], rs, av0, abase + (wo_k(it, s_) >> 1) * 4096); ld16h<(wo_k(it, s_) & 1) * 2048>(af[(s_) % WB][1], rs, av1, abase + (wo_k(it, s_) >> 1) * 4096); }
      static_assert(WR == 4 && WB == 8, "wo_at");
      O_LOAD(0); O_LOAD(1); O_LOAD(2); O_LOAD(3);
      if constexpr (it == 1) { ld16h<0>(res[0], rs, g * 16, OFF_XNEW + ct * 128); ld16h<64>(res[1], rs, g * 16, OFF_XNEW + ct * 128); }
      SB();
      f32x4 ac[2][4];
#pragma unroll
      for (int a = 0; a < 2; ++a)
#pragma unroll
        for (int n = 0; n < 4; ++n) ac[a][n] = f32x4{0.f, 0.f, 0.f, 0.f};
      u32x4 bfr[2][4];
#define WO_RD(buf, s, n)                                                                             \
  {                                                                                                  \
    const unsigned o_ = bt[n] + (wo_k(it, s) & 7) * 8192;                                             \
    frag_t f_;                                                                                       \
    f_.hf.a = trabs(o_); f_.hf.b = trabs(o_ + 2048);                                                 \
    bfr[buf][n] = f_.v;                                                                              \
  }
#pragma unroll
      for (int n = 0; n < 4; ++n) WO_RD(0, 0, n);
      static_for<16>([&](auto sc_) __attribute__((always_inline)) {
        constexpr int s = decltype(sc_)::value, cur = s & 1;
        pin_ac(ac);
        if (s == (it == 0 || WO1 ? 3 : 11)) asm volatile("s_barrier" ::: "memory");
        if constexpr (s < 15 && ((wo_k(it, s) ^ wo_k(it, s + 1)) & 8)) {
#pragma unroll
          for (int n = 0; n < 4; ++n) { bt[n] += (wo_k(it, s) & 8) ? -65536 : 65536; pin1(bt[n]); }
        }
        if constexpr (s < WB - WR) O_LOAD(WR + 2 * s);
        if constexpr ((it == 0 || WO1) && s == 8) {
          asm volatile("s_barrier" ::: "memory");
          WD_DMA();
        }
        wait_vm<wo_at(it, s) - wo_pos(it, s)>();
        pin2(af[s % WB][0], af[s % WB][1]);
        if constexpr (it == 1 && s == 9) pin2(res[0], res[1]);
        SB();
#pragma unroll
        for (int n = 0; n < 4; ++n) {
          ac[0][n] = mfma16(bfr[cur][n], af[s % WB][0], ac[0][n]);
          ac[1][n] = mfma16(bfr[cur][n], af[s % WB][1], ac[1][n]);
          if (s + 1 < 16) WO_RD(cur ^ 1, s + 1, n);
          if (it == 0 && s == 8) {
            const u32x2 t = *(const LDS_AS u32x2*)(smem + w * 4096 + (l & 15) * 128 + (((2 * n + (g >> 1)) ^ ((l & 15) >> 1)) << 4) + (g & 1) * 8);
            res[n >> 1][2 * (n & 1)] = t[0]; res[n >> 1][2 * (n & 1) + 1] = t[1];
          }
          if (s == 9) {
            if (it == 1 && n == 0) { res[0] = R == sel0 ? res[0] : x2p[0]; res[1] = R == sel0 ? res[1] : x2p[1]; }

            v[2 * n] = f32x2{lo16(res[n >> 1][2 * (n & 1)]), hi16(res[n >> 1][2 * (n & 1)])};
            v[2 * n + 1] = f32x2{lo16(res[n >> 1][2 * (n & 1) + 1]), hi16(res[n >> 1][2 * (n & 1) + 1])};
            pin2f(v[2 * n], v[2 * n + 1]);
          }
          SB();
        }
        if constexpr (s < WB - WR) { O_LOAD(WR + 2 * s + 1); }
        else if constexpr (s + WB < 16) { O_LOAD(s + WB); }
        SB();
      });
#undef WO_RD
#undef O_LOAD
#define XCH_AT(v) ((v) == 7 ? 155648 : (v) * 4096)
#pragma unroll
      for (int n = 0; n < 4; ++n) *(f32x4*)(smem + XCH_AT(w) + n * 1024 + l * 16) = ac[1][n];
#pragma unroll
      for (int n = 0; n < 4; ++n) { v[2 * n] += f32x2{ac[0][n][0], ac[0][n][1]}; v[2 * n + 1] += f32x2{ac[0][n][2], ac[0][n][3]}; }
      BAR();
#pragma unroll
      for (int n = 0; n < 4; ++n) {
        const f32x4 o = *(const f32x4*)(smem + XCH_AT(w ^ 4) + n * 1024 + l * 16);
        v[2 * n] += f32x2{o[0], o[1]}; v[2 * n + 1] += f32x2{o[2], o[3]};
      }
    }
    {
      if (it == 0 && X1F) { st16(pack8v(v), rs, to, OFF_XN + (4u << 20)); st16(pack8v(v + 4), rs, to + 2048, OFF_XN + (4u << 20)); }
      SB();
      const f32x2 s = ((v[0] + v[1]) + (v[2] + v[3])) + ((v[4] + v[5]) + (v[6] + v[7]));
      f32x2 dp = v[0] * wv[0], q = v[0] * v[0];
#pragma unroll
      for (int j = 1; j < 8; ++j) { dp = __builtin_elementwise_fma(v[j], wv[j], dp); q = __builtin_elementwise_fma(v[j], v[j], q); }
      float mt = hadd(s), qq = hadd(q), dot = hadd(dp);
      add_g3(mt, qq, dot);
      const unsigned so = OFF_STAT + it * 524288 + rb * 32768, lrow = R & 127;
      if (g == 0) st16(unpoison4(__builtin_bit_cast(u32x4, f32x4{mt, qq, dot, 0.f})), rs, ct * 2048 + lrow * 16, so);
      SB();
      if (it == 0 && !X1F) { st16(pack8v(v), rs, to, OFF_XN + (4u << 20)); st16(pack8v(v + 4), rs, to + 2048, OFF_XN + (4u << 20)); }
      SB();
      if (it == 0) wait_vm<X1F>();
      if constexpr (it == 0) {
        volatile LDS_AS u32x2* in = (volatile LDS_AS u32x2*)(smem + L_XC);
        if (l == 0) in[w] = T;
        const u32x2 t = in[l & 7];
        if (__all(t[0] == T[0] && t[1] == T[1]) && l < RX) st8(T, rs, (l * 32 + (rb & 1) * 16 + ct) * SX, F_X1 + (rb >> 1) * RX * 32 * SX);
      }
      if (it == 1 && !WO1) { if (ffn_on) { WD_DMA(); } }
      if (it == 0) {
#pragma unroll
        for (int j = 0; j < 9; ++j) *(u32x4*)(smem + L_RING + (j / 3) * STAGE + (w * 3 + j % 3) * 1024 + l * 16) = keep[j];
      }
      __builtin_amdgcn_s_sleep(it == 0 ? SDI0 : SDIR);
      char* const skey = smem + (it == 0 ? L_SKEY : L_XC);
      u32x4 sp[4];
      ld16h<0>(sp[0], rs, g * 8192 + lrow * 16, so); ld16h<2048>(sp[1], rs, g * 8192 + lrow * 16, so);
      ld16h<0>(sp[2], rs, g * 8192 + lrow * 16, so + 4096); ld16h<2048>(sp[3], rs, g * 8192 + lrow * 16, so + 4096);
      u32x2 xf;
      unsigned k_;
      asm volatile("" : "=v"(xf), "=v"(k_));
      if (it == 0 && w == 0) { ld8h(xf, rs, (l & 31) * SX, F_X1 + (p * RX + (xcd >> 1) % RX) * 32 * SX); ld4h<0>(k_, rs, (l & 15) * SK, OFF_KEY + (bid & 15) * cmax(16 * SK, 128)); }
      DRAIN();
      asm volatile("" : "+v"(xf), "+v"(k_)::"memory");
      PINV4(sp);
      const RowFin r = fin_key(rs, T, g * 8192 + lrow * 16, so, skey, l, w, R, wsum, sp);
      if constexpr (it == 0) {
        if (w == 0) {
          const unsigned need = 0x30003u << (((PROT() + 5) & 7) * 2);
          unsigned m = (unsigned)__builtin_amdgcn_ballot_w64(xf[0] == T[0] && xf[1] == T[1]);
          for (unsigned spins = 0; (m & need) != need;) {
            if (spin_fail(spins, rs, T)) { m = ~0u; break; }
            const u32x2 f = ld8(rs, F_X1 + ((p * RX + (xcd >> 1) % RX) * 32 + (l & 31)) * SX, 0);
            k_ = key_load(rs, l, 0, bid & 15);
            m = (unsigned)__builtin_amdgcn_ballot_w64(f[0] == T[0] && f[1] == T[1]);
          }
          k_ = umax16(k_ == POISON ? 0u : k_);
          if (l == 0) *(LDS_AS u32x4*)(skey + 32) = u32x4{m, k_, POISON, 0u};
        }
        if (tid == 192) *(LDS_AS u32x4*)(smem + L_QC) = u32x4{0u, 0u, 0u, 0u};
      }
      BAR();
      if constexpr (it == 0) {
        PRE_LOAD(1, PROT() + 5, 0, 4);
        const u32x2 mk = *(const LDS_AS u32x2*)(skey + 32);
        xm0 = __builtin_amdgcn_readfirstlane(mk[0]); kg0 = __builtin_amdgcn_readfirstlane(mk[1]);
      }
      const unsigned own = fin_cand(rs, skey, it, g, rb, ct, r, v);
      if constexpr (it == 0) kg0 = max(kg0, (unsigned)__builtin_amdgcn_readfirstlane(own));
      fin_x2(r, v);
      if constexpr (it == 0) {
        x2p[0] = pack8v(v); x2p[1] = pack8v(v + 4);
        asm volatile("" : "+v"(x2p[0]), "+v"(x2p[1]));
        if ((w & 3) * 2 + (w >> 2) == ct >> 1) {
          const float f = 1.0f + r.rd, rd2 = ln_rd(r.var * f * f), mb2 = rbf1(__builtin_fmaf(r.mean - r.mb, r.rd, r.mean));
          if (g < 2 && ((l >> 3) & 1) == (ct & 1))
            st4(min(__builtin_bit_cast(unsigned, g == 0 ? f * rd2 : -__builtin_fmaf(r.mb, r.rd, mb2) * __builtin_amdgcn_rcpf(f)), POISON - 1), rs, OFF_AB + 16384 + R * 4 + g * 8192, 0);
        }
      }
    }
    if (it == 1) {
      const u32x4 op0 = trade16(pack8v(v)), op1 = trade16(pack8v(v + 4));
#define OUT_STORE()                                                                                 \
  if (R != key_row(key)) {                                                                          \
    __builtin_amdgcn_raw_buffer_store_b128(op0, rout, (R * 1024 + rmo) * 2, 0, SC1);                  \
    __builtin_amdgcn_raw_buffer_store_b128(op1, rout, (R * 1024 + rmo + 32) * 2, 0, SC1);             \
  }
      u32x4 xv[4];
      unsigned guess = 0;
      {
        __builtin_amdgcn_s_sleep(GSL);
        const unsigned k_ = key_load(rs, l, 1, bid & 15);
        guess = ffn_xoff(__builtin_amdgcn_readfirstlane(umax16(k_ == POISON ? 0u : k_)), 1, w);
        row_load(rs, g, guess, xv);
      }
      if (w == 0) {
        const unsigned k_ = key_wait(rs, T, l, 1, bid & 15, key_load(rs, l, 1, bid & 15));
        if (l == 0) *(LDS_AS unsigned*)(smem + L_SKEY + 32) = k_;
      }
      DRAIN();
      PINV4(xv);
      BAR();
      const unsigned key = __builtin_amdgcn_readfirstlane(*(LDS_AS unsigned*)(smem + L_SKEY + 32));
      if (ffn_on) {
        f32x4 ga, ua;
        s16x4 dfr[8];
        ffn_d_read(smem, l, w, dfr);
        if (guess != ffn_xoff(key, 1, w)) ld4x16(xv, rs, g * 16, ffn_xoff(key, 1, w));
        ffn_gu(gv, uv, xv, ga, ua);
        ffn_gu_check(rs, T, ffn_xoff(key, 1, w), l, gv, uv, xv, ga, ua);
        ffn_gu_store(smem, l, w, ga, ua);
        BAR();
        f32x4 ya = {0.f, 0.f, 0.f, 0.f}, yb = ya;
        u32x2 hh = ffn_h(ffn_t_read(smem, l), l);
        ffn_d_mma(dfr, hh, ya, yb);
        if ((l & 15) < 8) ffn_d_store(rs, l, 4 * w, 1, slice, ya + yb);
        SB();
        OUT_STORE();
      } else {
        OUT_STORE();
        const unsigned G = xcd * 4 + bi - 28;
        if (G == 0 && tid == 0) st8(T, rs, F_EP, 0);
        u32x4 pv[4];
        u32x2 cxn, cx1;
        __builtin_amdgcn_s_sleep(RSL);
        red_load(rs, tid, 1, G, key, pv, cxn, cx1);
        red_sum(rs, smem, T, tid, w, 1, G, key, pv, cxn, cx1);
        BAR();
        if (w == 0) {
          const u32x2 ov = red_final<8>(smem, l, key, cxn, cx1);
          if (l < 8) __builtin_amdgcn_raw_buffer_store_b64(ov, rout, (key_row(key) * 1024 + G * 32 + l * 4) * 2, 0, 0);
        }
      }
#undef OUT_STORE
    }
  };
  layer(std::integral_constant<int, 0>{});
  layer(std::integral_constant<int, 1>{});
}

extern "C" void kernel_launch(void* const* d_in, const int* in_sizes, int n_in, void* d_out, int out_size,
                              void* d_ws, size_t ws_size, hipStream_t stream) {
  (void)hipFuncSetAttribute((const void*)mega_kernel, hipFuncAttributeMaxDynamicSharedMemorySize, LDS_BYTES);
  mega_kernel<<<256, 512, LDS_BYTES, stream>>>((const bf16*)d_in[0], (char*)d_ws, (const bf16*)d_in[1], (const bf16*)d_in[2], (const bf16*)d_in[4],
                                               (const bf16*)d_in[5], (const bf16*)d_in[6], (const bf16*)d_in[7], (bf16*)d_out);
}
```

```cpp
#include <hip/hip_runtime.h>
#include <stdint.h>
#include <utility>

typedef __bf16 bf16;
typedef __bf16 bf16x8 __attribute__((ext_vector_type(8)));
typedef __bf16 bf16x2 __attribute__((ext_vector_type(2)));
typedef float f32x4 __attribute__((ext_vector_type(4)));
typedef float f32x2 __attribute__((ext_vector_type(2)));
typedef short s16x4 __attribute__((ext_vector_type(4)));
typedef unsigned int u32x4 __attribute__((ext_vector_type(4)));
typedef unsigned int u32x2 __attribute__((ext_vector_type(2)));
#define LDS_AS __attribute__((address_space(3)))
#define GAS __attribute__((address_space(1)))
typedef GAS unsigned gu32;
typedef GAS unsigned long long gu64;
#define RLX_AGENT __ATOMIC_RELAXED, __HIP_MEMORY_SCOPE_AGENT
typedef __amdgpu_buffer_rsrc_t rsrc_t;

constexpr int cmax(int a, int b) { return a > b ? a : b; }
constexpr int F_ = 3584;
constexpr int SC1 = 16;

constexpr int RX = 4;
constexpr unsigned F_EP = 0, F_TMO = 128;
constexpr unsigned F_XN = 4096;
#define STR 8, 8, 4, 8
constexpr unsigned STV[4] = {STR}, SO = STV[0], SX = STV[1], SK = STV[2], SV = STV[3];
constexpr unsigned F_O = 8u << 20;
constexpr unsigned F_X1 = F_O + 131072;
constexpr unsigned OFF_KEY = F_O + 393216;
constexpr unsigned F_KV = F_O + 524288;
constexpr unsigned OFF_AB = 147456;
constexpr unsigned OFF_XNEW = 262144;
constexpr unsigned OFF_CS = 294912;
constexpr unsigned OFF_FX = 393216;
constexpr unsigned OFF_CA = 524288;
constexpr unsigned OFF_CB = 655360;
constexpr unsigned OFF_STAT = 1u << 20;
constexpr unsigned OFF_PART = 3u << 20;
constexpr unsigned OFF_XN = 8u << 20;
constexpr unsigned OFF_O = 16u << 20;
constexpr unsigned OFF_KV = 32u << 20;
constexpr unsigned OFF_WI = 52u << 20;
constexpr unsigned WS_BYTES = 58u << 20;
constexpr unsigned POISON = 0xFFFFFFFFu;

constexpr int L_ST0 = 147456, L_CNT = 147968, L_CSL = 31872, L_FXL = 32256, L_K = 0, L_V = 32768, L_RING = 65536, L_XCH = 0, L_DN = 32768, L_TOUCH0 = 149504, L_RDY = 31744, L_QC = 31808, LDS_BYTES = 163840;
constexpr int STAGE = 24576;
#define SLOT(t) ((t) & 3)

template <typename F, int... I> __device__ __forceinline__ void static_for_impl(F&& f, std::integer_sequence<int, I...>) {
  (f(std::integral_constant<int, I>{}), ...);
}
template <int N, typename F> __device__ __forceinline__ void static_for(F&& f) { static_for_impl(f, std::make_integer_sequence<int, N>{}); }
__device__ __forceinline__ unsigned pack2(float a, float b) {
  f32x2 v = {a, b};
  return __builtin_bit_cast(unsigned, __builtin_convertvector(v, bf16x2));
}
__device__ __forceinline__ float asf(unsigned u) { return __builtin_bit_cast(float, u); }
__device__ __forceinline__ float lo16(unsigned u) { return __builtin_bit_cast(float, u << 16); }
__device__ __forceinline__ float hi16(unsigned u) { return __builtin_bit_cast(float, u & 0xffff0000u); }
__device__ __forceinline__ u32x4 pack8(const f32x4 a, const f32x4 b) {
  u32x4 o;
  o[0] = pack2(a[0], a[1]); o[1] = pack2(a[2], a[3]); o[2] = pack2(b[0], b[1]); o[3] = pack2(b[2], b[3]);
  return o;
}
__device__ __forceinline__ f32x4 mfma16(u32x4 a, u32x4 b, f32x4 c) {
  return __builtin_amdgcn_mfma_f32_16x16x32_bf16(__builtin_bit_cast(bf16x8, a), __builtin_bit_cast(bf16x8, b), c, 0, 0, 0);
}
__device__ __forceinline__ f32x4 mfma16k16(s16x4 a, s16x4 b, f32x4 c) { return __builtin_amdgcn_mfma_f32_16x16x16bf16_1k(a, b, c, 0, 0, 0); }
template <int CTRL> __device__ __forceinline__ float dppf(float v) {
  return __builtin_bit_cast(float, __builtin_amdgcn_update_dpp(0, __builtin_bit_cast(int, v), CTRL, 0xf, 0xf, true));
}
template <int CTRL> __device__ __forceinline__ unsigned dppu(unsigned v) {
  return (unsigned)__builtin_amdgcn_update_dpp(0, (int)v, CTRL, 0xf, 0xf, true);
}
__device__ __forceinline__ float add_x16(float v) {
  auto r = __builtin_amdgcn_permlane16_swap(__builtin_bit_cast(unsigned, v), __builtin_bit_cast(unsigned, v), false, false);
  return __builtin_bit_cast(float, (unsigned)r[0]) + __builtin_bit_cast(float, (unsigned)r[1]);
}
__device__ __forceinline__ float add_x32(float v) {
  auto r = __builtin_amdgcn_permlane32_swap(__builtin_bit_cast(unsigned, v), __builtin_bit_cast(unsigned, v), false, false);
  return __builtin_bit_cast(float, (unsigned)r[0]) + __builtin_bit_cast(float, (unsigned)r[1]);
}
__device__ __forceinline__ float max_x16(float v) {
  auto r = __builtin_amdgcn_permlane16_swap(__builtin_bit_cast(unsigned, v), __builtin_bit_cast(unsigned, v), false, false);
  return fmaxf(__builtin_bit_cast(float, (unsigned)r[0]), __builtin_bit_cast(float, (unsigned)r[1]));
}
__device__ __forceinline__ float max_x32(float v) {
  auto r = __builtin_amdgcn_permlane32_swap(__builtin_bit_cast(unsigned, v), __builtin_bit_cast(unsigned, v), false, false);
  return fmaxf(__builtin_bit_cast(float, (unsigned)r[0]), __builtin_bit_cast(float, (unsigned)r[1]));
}
__device__ __forceinline__ unsigned umax_x16(unsigned v) {
  auto r = __builtin_amdgcn_permlane16_swap(v, v, false, false);
  return max((unsigned)r[0], (unsigned)r[1]);
}
__device__ __forceinline__ unsigned umax_x32(unsigned v) {
  auto r = __builtin_amdgcn_permlane32_swap(v, v, false, false);
  return max((unsigned)r[0], (unsigned)r[1]);
}
__device__ __forceinline__ float sum_hi8(float v) {
  v += dppf<0x128>(v);
  v = add_x16(v);
  return add_x32(v);
}
__device__ __forceinline__ float sum32(float v) {
  v += dppf<0xB1>(v);
  v += dppf<0x4E>(v);
  v += dppf<0x124>(v);
  v += dppf<0x128>(v);
  return add_x16(v);
}
__device__ __forceinline__ float sum64(float v) {
  v += dppf<0xB1>(v);
  v += dppf<0x4E>(v);
  v += dppf<0x124>(v);
  v += dppf<0x128>(v);
  v = add_x16(v);
  return add_x32(v);
}

#define BAR() asm volatile("s_waitcnt lgkmcnt(0)\n\ts_barrier" ::: "memory")
#define DRAIN() asm volatile("s_waitcnt vmcnt(0)" ::: "memory")
#define SB() __builtin_amdgcn_sched_barrier(0)
template <int N> __device__ __forceinline__ void wait_vm() { asm volatile("s_waitcnt vmcnt(%0)" ::"n"(N) : "memory"); }

__device__ __forceinline__ void dma16(rsrc_t rs, unsigned voff, unsigned soff, unsigned lds) {
  asm volatile("s_mov_b32 m0, %0\n\ts_nop 0\n\tbuffer_load_dwordx4 %1, %2, %3 offen lds" ::"s"(lds), "v"(voff), "s"(rs), "s"(soff) : "memory");
}
template <int OFF> __device__ __forceinline__ void dma16s(rsrc_t rs, unsigned voff, unsigned soff, unsigned lds) {
  asm volatile("s_mov_b32 m0, %0\n\ts_nop 0\n\tbuffer_load_dwordx4 %1, %2, %3 offen offset:%4 sc1 lds" ::"s"(lds), "v"(voff), "s"(rs), "s"(soff), "n"(OFF) : "memory");
}
__device__ __forceinline__ void touch(unsigned& d, const GAS bf16* ptr) { asm volatile("global_load_dword %0, %1, off" : "+v"(d) : "v"(ptr) : "memory"); }
__device__ __forceinline__ void ldrows(u32x4 (&d)[4], rsrc_t rx, unsigned voff, unsigned soff) {
  asm volatile("buffer_load_dwordx4 %0, %4, %5, %6 offen\n\tbuffer_load_dwordx4 %1, %4, %5, %6 offen offset:512\n\t"
               "buffer_load_dwordx4 %2, %4, %5, %6 offen offset:1024\n\tbuffer_load_dwordx4 %3, %4, %5, %6 offen offset:1536"
               : "=&v"(d[0]), "=&v"(d[1]), "=&v"(d[2]), "=&v"(d[3]) : "v"(voff), "s"(rx), "s"(soff) : "memory");
}
__device__ __forceinline__ void pin4(f32x2* v) { asm volatile("" : "+v"(v[0]), "+v"(v[1]), "+v"(v[2]), "+v"(v[3])); }
__device__ __forceinline__ float rbf1(float x) { return __builtin_bit_cast(float, pack2(0.f, x)); }
__device__ __forceinline__ void vswap(unsigned& a, unsigned& b) { asm volatile("v_swap_b32 %0, %1" : "+v"(a), "+v"(b)); }
__device__ __forceinline__ void vswap(float& a, float& b) { asm volatile("v_swap_b32 %0, %1" : "+v"(a), "+v"(b)); }
template <typename V, int N> __device__ __forceinline__ void vswapn(V& a, V& b) {
#pragma unroll
  for (int i = 0; i < N; ++i) { auto x = a[i], y = b[i]; vswap(x, y); a[i] = x; b[i] = y; }
}
__device__ __forceinline__ u32x4 ld16(rsrc_t rs, unsigned voff, unsigned soff) { return __builtin_amdgcn_raw_buffer_load_b128(rs, voff, soff, SC1); }
template <int OFF> __device__ __forceinline__ void ld16h(u32x4& d, rsrc_t rs, unsigned voff, unsigned soff) {
  asm volatile("buffer_load_dwordx4 %0, %1, %2, %3 offen offset:%4 sc1" : "=v"(d) : "v"(voff), "s"(rs), "s"(soff), "n"(OFF) : "memory");
}
template <int I, int OFF> __device__ __forceinline__ void ld16w(u32x4& d, rsrc_t rs, unsigned voff, unsigned soff) {
  if constexpr (I == 0) asm volatile("buffer_load_dwordx4 %0, %1, %2, %3 offen offset:%4 sc1" : "={v[128:131]}"(d) : "v"(voff), "s"(rs), "s"(soff), "n"(OFF) : "memory");
  else if constexpr (I == 1) asm volatile("buffer_load_dwordx4 %0, %1, %2, %3 offen offset:%4 sc1" : "={v[132:135]}"(d) : "v"(voff), "s"(rs), "s"(soff), "n"(OFF) : "memory");
  else if constexpr (I == 2) asm volatile("buffer_load_dwordx4 %0, %1, %2, %3 offen offset:%4 sc1" : "={v[136:139]}"(d) : "v"(voff), "s"(rs), "s"(soff), "n"(OFF) : "memory");
  else if constexpr (I == 3) asm volatile("buffer_load_dwordx4 %0, %1, %2, %3 offen offset:%4 sc1" : "={v[140:143]}"(d) : "v"(voff), "s"(rs), "s"(soff), "n"(OFF) : "memory");
  else if constexpr (I == 4) asm volatile("buffer_load_dwordx4 %0, %1, %2, %3 offen offset:%4 sc1" : "={v[144:147]}"(d) : "v"(voff), "s"(rs), "s"(soff), "n"(OFF) : "memory");
  else if constexpr (I == 5) asm volatile("buffer_load_dwordx4 %0, %1, %2, %3 offen offset:%4 sc1" : "={v[148:151]}"(d) : "v"(voff), "s"(rs), "s"(soff), "n"(OFF) : "memory");
  else if constexpr (I == 6) asm volatile("buffer_load_dwordx4 %0, %1, %2, %3 offen offset:%4 sc1" : "={v[152:155]}"(d) : "v"(voff), "s"(rs), "s"(soff), "n"(OFF) : "memory");
  else if constexpr (I == 7) asm volatile("buffer_load_dwordx4 %0, %1, %2, %3 offen offset:%4 sc1" : "={v[156:159]}"(d) : "v"(voff), "s"(rs), "s"(soff), "n"(OFF) : "memory");
  else if constexpr (I == 8) asm volatile("buffer_load_dwordx4 %0, %1, %2, %3 offen offset:%4 sc1" : "={v[160:163]}"(d) : "v"(voff), "s"(rs), "s"(soff), "n"(OFF) : "memory");
  else if constexpr (I == 9) asm volatile("buffer_load_dwordx4 %0, %1, %2, %3 offen offset:%4 sc1" : "={v[164:167]}"(d) : "v"(voff), "s"(rs), "s"(soff), "n"(OFF) : "memory");
  else if constexpr (I == 10) asm volatile("buffer_load_dwordx4 %0, %1, %2, %3 offen offset:%4 sc1" : "={v[168:171]}"(d) : "v"(voff), "s"(rs), "s"(soff), "n"(OFF) : "memory");
  else if constexpr (I == 11) asm volatile("buffer_load_dwordx4 %0, %1, %2, %3 offen offset:%4 sc1" : "={v[172:175]}"(d) : "v"(voff), "s"(rs), "s"(soff), "n"(OFF) : "memory");
}
template <int K> __device__ __forceinline__ void pin_w(u32x4& a, u32x4& b) {
  if constexpr (K == 0) asm volatile("" : "+{v[128:131]}"(a), "+{v[132:135]}"(b)::"memory");
  else if constexpr (K == 1) asm volatile("" : "+{v[136:139]}"(a), "+{v[140:143]}"(b)::"memory");
  else if constexpr (K == 2) asm volatile("" : "+{v[144:147]}"(a), "+{v[148:151]}"(b)::"memory");
  else if constexpr (K == 3) asm volatile("" : "+{v[152:155]}"(a), "+{v[156:159]}"(b)::"memory");
  else if constexpr (K == 4) asm volatile("" : "+{v[160:163]}"(a), "+{v[164:167]}"(b)::"memory");
  else if constexpr (K == 5) asm volatile("" : "+{v[168:171]}"(a), "+{v[172:175]}"(b)::"memory");
}
__device__ __forceinline__ void ld8h(u32x2& d, rsrc_t rs, unsigned voff, unsigned soff) {
  asm volatile("buffer_load_dwordx2 %0, %1, %2, %3 offen sc1" : "=v"(d) : "v"(voff), "s"(rs), "s"(soff) : "memory");
}
__device__ __forceinline__ void ld16u(u32x4& d, rsrc_t rs, unsigned voff, unsigned soff) {
  asm volatile("buffer_load_dwordx4 %0, %1, %2, %3 offen sc1" : "+v"(d) : "v"(voff), "s"(rs), "s"(soff) : "memory");
}
template <int OFF> __device__ __forceinline__ void ld4h(unsigned& d, rsrc_t rs, unsigned voff, unsigned soff) {
  asm volatile("buffer_load_dword %0, %1, %2, %3 offen offset:%4 sc1" : "=v"(d) : "v"(voff), "s"(rs), "s"(soff), "n"(OFF) : "memory");
}
__device__ __forceinline__ void ld4x16(u32x4 (&d)[4], rsrc_t rs, unsigned voff, unsigned soff) {
  asm volatile("buffer_load_dwordx4 %0, %4, %5, %6 offen sc1\n\tbuffer_load_dwordx4 %1, %4, %5, %6 offen offset:64 sc1\n\t"
               "buffer_load_dwordx4 %2, %4, %5, %6 offen offset:128 sc1\n\tbuffer_load_dwordx4 %3, %4, %5, %6 offen offset:192 sc1\n\ts_waitcnt vmcnt(0)"
               : "=&v"(d[0]), "=&v"(d[1]), "=&v"(d[2]), "=&v"(d[3]) : "v"(voff), "s"(rs), "s"(soff) : "memory");
}
__device__ __forceinline__ u32x2 ld8(rsrc_t rs, unsigned voff, unsigned soff) { return __builtin_amdgcn_raw_buffer_load_b64(rs, voff, soff, SC1); }
__device__ __forceinline__ void st16(u32x4 v, rsrc_t rs, unsigned voff, unsigned soff) { __builtin_amdgcn_raw_buffer_store_b128(v, rs, voff + soff, 0, SC1); }
__device__ __forceinline__ void st8(u32x2 v, rsrc_t rs, unsigned voff, unsigned soff) { __builtin_amdgcn_raw_buffer_store_b64(v, rs, voff, soff, SC1); }

__device__ __forceinline__ unsigned ld4(rsrc_t rs, unsigned voff, unsigned soff) { return __builtin_amdgcn_raw_buffer_load_b32(rs, voff, soff, SC1); }
__device__ __forceinline__ void st4(unsigned v, rsrc_t rs, unsigned voff, unsigned soff) { __builtin_amdgcn_raw_buffer_store_b32(v, rs, voff, soff, SC1); }

constexpr unsigned kSpinCap = 1u << 16;
__device__ __forceinline__ bool spin_fail(unsigned& spins, rsrc_t rs, u32x2 T) {
  asm volatile("" ::: "memory");
  ++spins;
  if (spins > kSpinCap) { st8(T, rs, 0, F_TMO); return true; }
  if ((spins & 255u) == 0u) {
    const u32x2 v = ld8(rs, 0, F_TMO);
    if (v[0] == T[0] && v[1] == T[1]) return true;
  }
  __builtin_amdgcn_s_sleep(3);
  return false;
}
__device__ __forceinline__ void poll_flags2(rsrc_t rs, unsigned voff, u32x2 T) {
  for (unsigned spins = 0;;) {
    const u32x4 v = ld16(rs, voff, 0);
    if (__all(v[0] == T[0] && v[1] == T[1] && v[2] == T[0] && v[3] == T[1])) break;
    if (spin_fail(spins, rs, T)) break;
  }
  asm volatile("" ::: "memory");
  __builtin_amdgcn_fence(__ATOMIC_ACQUIRE, "wavefront");
}
__device__ __forceinline__ void poll_flags(rsrc_t rs, unsigned voff, unsigned soff, u32x2 T) {
  for (unsigned spins = 0;;) {
    const u32x2 v = ld8(rs, voff, soff);
    if (__all(v[0] == T[0] && v[1] == T[1])) break;
    if (spin_fail(spins, rs, T)) break;
  }
  asm volatile("" ::: "memory");
  __builtin_amdgcn_fence(__ATOMIC_ACQUIRE, "wavefront");
}

__device__ __forceinline__ unsigned toff(unsigned R, unsigned ks, unsigned g) {
  return (((R >> 5) * 64 + ks * 2 + ((R >> 4) & 1)) << 10) + ((g * 16 + (R & 15)) << 4);
}
__device__ __forceinline__ void unpack8v(const u32x4 u, f32x2* v) {
#pragma unroll
  for (int j = 0; j < 4; ++j) v[j] = f32x2{lo16(u[j]), hi16(u[j])};
}
__device__ __forceinline__ u32x4 pack8v(const f32x2* v) {
  u32x4 o;
#pragma unroll
  for (int j = 0; j < 4; ++j) o[j] = pack2(v[j][0], v[j][1]);
  return o;
}
__device__ __forceinline__ f32x2 rbf1v(f32x2 v) { return f32x2{rbf1(v[0]), rbf1(v[1])}; }
__device__ __forceinline__ float ln_rd(float var) { return __builtin_amdgcn_rcpf(rbf1(__builtin_amdgcn_sqrtf(rbf1(rbf1(var) + 1e-5f)))); }
template <int N> __device__ __forceinline__ void ln_apply(float mb, float rd, const f32x2* v, f32x2* y) {
  const f32x2 mb2 = {mb, mb}, rd2 = {rd, rd};
#pragma unroll
  for (int j = 0; j < N; ++j) y[j] = rbf1v(rbf1v(v[j] - mb2) * rd2);
}
__device__ __forceinline__ float hadd(f32x2 v) {
  float a = v[0];
  asm volatile("" : "+v"(a));
  return a + v[1];
}
__device__ __forceinline__ float add_g(float v) { return add_x32(add_x16(v)); }
__device__ __forceinline__ void add_g3(float& x, float& y, float& z) {
  auto a = __builtin_amdgcn_permlane16_swap(__builtin_bit_cast(unsigned, x), __builtin_bit_cast(unsigned, y), false, false);
  auto b = __builtin_amdgcn_permlane16_swap(__builtin_bit_cast(unsigned, z), __builtin_bit_cast(unsigned, z), false, false);
  const float r = asf(a[0]) + asf(a[1]), t = asf(b[0]) + asf(b[1]);
  auto c = __builtin_amdgcn_permlane32_swap(__builtin_bit_cast(unsigned, r), __builtin_bit_cast(unsigned, r), false, false);
  auto d = __builtin_amdgcn_permlane32_swap(__builtin_bit_cast(unsigned, t), __builtin_bit_cast(unsigned, t), false, false);
  const float u = asf(c[0]) + asf(c[1]);
  z = asf(d[0]) + asf(d[1]);
  auto e = __builtin_amdgcn_permlane16_swap(__builtin_bit_cast(unsigned, u), __builtin_bit_cast(unsigned, u), false, false);
  x = asf(e[0]); y = asf(e[1]);
}
__device__ __forceinline__ u32x4 trade16(u32x4 v) {
  auto a = __builtin_amdgcn_permlane16_swap(v[0], v[2], false, false);
  auto b = __builtin_amdgcn_permlane16_swap(v[1], v[3], false, false);
  return u32x4{(unsigned)a[0], (unsigned)b[0], (unsigned)a[1], (unsigned)b[1]};
}
__device__ __forceinline__ unsigned umax4(u32x4 v) { return max(max(v[0], v[1]), max(v[2], v[3])); }
__device__ __forceinline__ u32x2 unpoison2(u32x2 v) { return u32x2{min(v[0], POISON - 1), min(v[1], POISON - 1)}; }
__device__ __forceinline__ u32x4 unpoison4(u32x4 v) {
  return u32x4{min(v[0], POISON - 1), min(v[1], POISON - 1), min(v[2], POISON - 1), min(v[3], POISON - 1)};
}

typedef union { struct { s16x4 a, b; } hf; u32x4 v; } frag_t;
__device__ __forceinline__ s16x4 trread(const char* smem, unsigned off) {
  return __builtin_amdgcn_ds_read_tr16_b64_v4i16((LDS_AS s16x4*)(smem + off));
}
__device__ __forceinline__ s16x4 trabs(unsigned addr) { return __builtin_amdgcn_ds_read_tr16_b64_v4i16((LDS_AS s16x4*)(size_t)addr); }

constexpr int WA = 6;
#define WRV 3
#define WBV 4
constexpr int WB = WBV, WR = WRV;
#define CH 0, 4, 7, 8, 13, 15, 20, 22, 27, 29
constexpr int CHV[10] = {CH};
constexpr int FSL = CHV[1], FS0 = CHV[2], FS0D = CHV[3], FS1 = CHV[4], FS1S = CHV[5];
constexpr int FXA = CHV[6], FXB = CHV[7], FXL = CHV[8], FXS = CHV[9];
constexpr int FXT = FXB >> 2;
#define C0 27, 3, 16, 19, 22, 23, 2, 5, 4
constexpr int C0V[9] = {C0};
constexpr int IMG = C0V[0], CSD = C0V[1];
constexpr int ERQ = C0V[2], CSL = C0V[3], CSS = C0V[4];
constexpr int RWL = C0V[6], RWS = C0V[7], POI = C0V[8];
#define WTS 99
#define WTG 2
#define PBS 10
#define KP0 26
#define PS0 8
constexpr int EIN = cmax(CSD, cmax(RWS, POI)) + 3;
constexpr int ABS = 27;
constexpr int abl(int it) { return it ? 23 : C0V[5]; }
constexpr int XU = 3, XL = 2;
constexpr int x_slot(int u) { return u % XU == 0 ? 0 : u % XU == 1 ? 32768 : 49152; }
#define TCH 20, 6, 5, 27
constexpr int TCV[4] = {TCH};
constexpr int TS0 = TCV[0], TSN = TCV[1], TW = TCV[2], TST = TCV[3];
constexpr int QL = 26;
#define FDG 0
#define XSL 4
#define FW0 4
#define FW1 4
#define X1F 1
#define RWG 0
#define CSG 4
#define RXCD 5
#define THALF 0
#define WDS (17 - WB)
#define WDW 1
#define SDI0 12
#define SLP 24, 14, 56, 3
constexpr int SLV[4] = {SLP};
constexpr int SDIR = SLV[0], GSL = SLV[1], RSL = SLV[2], KSL = SLV[3];
constexpr bool tile_piece(int s) { return s >= 3 && ((s - 3) >> 2) + 3 < 8 && ((s - 3) & 3) < 3; }
constexpr int a_req(int s, int n) {
  if (!(n & 1)) return -1;
  if (s <= 1) return 8 + 3 * s + (n >> 1);
  return n < 5 && s + 5 < 32 ? 2 * (s + 5) + (n >> 1) : -1;
}
constexpr int vm_n(int it, int s, int n) {
  int c = 0;
  if (n == 0) c += tile_piece(s);
  if (n == 4) c += (s == abl(it)) + (it ? (s == FSL ? 5 : 0) : (s == ERQ) + (s >= IMG && s < IMG + 3));
  if (!it) c += (s == WTS && n == WTG) + (s < 4 && n == 2) + (s == 0 && (n == 0 || n == 4)) + ((n == 3 || n == 5) && s + XL + XU < 32);
  else c += (a_req(s, n) >= 0) + ((s == 0 || s == 4) && n == 2);
  return c;
}
constexpr int vm_at(int it, int s, int n) {
  int c = it ? 8 : 14;
  for (int u = 0; u < 6 * s + n; ++u) c += vm_n(it, u / 6, u % 6);
  return c;
}
constexpr int vm_a(int it, int k) {
  if (k < 4) return 2 * k + 2;
  for (int u = 0; u < 192; ++u)
    if (a_req(u / 6, u % 6) == 2 * k + 1) return vm_at(it, (u + 1) / 6, (u + 1) % 6);
  return 0;
}
constexpr int vm_tile(int it, int t) { return t >= 3 ? vm_at(it, 4 * t - 7, 1) : t == 1 ? vm_at(0, 0, 5) : vm_at(0, 3, 3); }
constexpr int vm_x(int u) { return u == 2 ? 9 : u < XU + XL ? 12 + 2 * (u - XU) : vm_at(0, u - XU - XL + 1, 0); }

constexpr int wo_k(int it, int s) { return it || s < 4 ? 15 - s : s < 8 ? 7 - s : 19 - s; }
constexpr int wo_pos(int it, int s) { return 2 * s + 2 + (s >= WR ? 2 * it : 0); }
constexpr int wo_at(int it, int s) { return 2 * it + (s < WB - WR ? 2 * WR + 2 + 4 * s : s < 16 - WB ? 2 * (2 * WB - WR) + 2 * (s - WB + WR) : 32); }
template <int OFF> __device__ __forceinline__ void lds_count(unsigned ad) {
  asm volatile("s_mov_b64 exec, 1\n\tds_add_u32 %0, %1 offset:%2\n\ts_mov_b64 exec, -1" ::"v"(ad), "v"(1u), "n"(OFF) : "memory");
}
__device__ __forceinline__ void lds_look(char* c, unsigned want) {
  while ((unsigned)__builtin_amdgcn_readfirstlane(__hip_atomic_load((LDS_AS unsigned*)c, __ATOMIC_RELAXED, __HIP_MEMORY_SCOPE_WORKGROUP)) < want) {}
  asm volatile("" ::: "memory");
}
__device__ __forceinline__ void pin1(unsigned& a) { asm volatile("" : "+v"(a)); }
__device__ __forceinline__ void pin2(u32x4& a, u32x4& b) { asm volatile("" : "+v"(a), "+v"(b)::"memory"); }
__device__ __forceinline__ void pin2s(float& a, float& b) { asm volatile("" : "+v"(a), "+v"(b)); }
template <int OFF> __device__ __forceinline__ void lds2(u32x4& a, u32x4& b, unsigned ad) {
  asm volatile("ds_read_b128 %0, %2 offset:%3\n\tds_read_b128 %1, %2 offset:%4" : "=&v"(a), "=&v"(b) : "v"(ad), "n"(OFF), "n"(OFF + 1024) : "memory");
}
template <int N> __device__ __forceinline__ void lds_wait(u32x4& a, u32x4& b) { asm volatile("s_waitcnt lgkmcnt(%2)" : "+v"(a), "+v"(b) : "n"(N) : "memory"); }
__device__ __forceinline__ void pin2f(f32x2& a, f32x2& b) { asm volatile("" : "+v"(a), "+v"(b)); }
__device__ __forceinline__ void pin_ac(f32x4 (&ac)[2][4]) {
  asm volatile("" : "+{v[176:179]}"(ac[0][0]), "+{v[180:183]}"(ac[0][1]), "+{v[184:187]}"(ac[0][2]), "+{v[188:191]}"(ac[0][3]),
                    "+{v[192:195]}"(ac[1][0]), "+{v[196:199]}"(ac[1][1]), "+{v[200:203]}"(ac[1][2]), "+{v[204:207]}"(ac[1][3]));
}

constexpr int L_XC = LDS_BYTES - 64, L_SKEY = 32704, L_SRED = 4096, L_SFIN = 20480;
__device__ __forceinline__ unsigned key_load(rsrc_t rs, int l, int L, int rep) { return ld4(rs, (l & 15) * SK, OFF_KEY + (L * 16 + rep) * cmax(16 * SK, 128)); }
__device__ __forceinline__ unsigned umax16(unsigned k) {
  k = max(k, dppu<0xB1>(k));
  k = max(k, dppu<0x4E>(k));
  k = max(k, dppu<0x141>(k));
  return max(k, dppu<0x140>(k));
}
__device__ __forceinline__ unsigned key_row(unsigned key) { return (0xffffu - (key & 0xffffu)) & 2047u; }
__device__ __forceinline__ unsigned key_wait(rsrc_t rs, u32x2 T, int l, int L, int rep, unsigned kv) {
  if (!__all(kv != POISON)) {
    unsigned spins = 0;
    do {
      if (spin_fail(spins, rs, T)) break;
      if (L == 0) __builtin_amdgcn_s_sleep(KSL);
      kv = key_load(rs, l, L, rep);
    } while (!__all(kv != POISON));
  }
  return __builtin_amdgcn_readfirstlane(umax16(kv));
}
__device__ __forceinline__ unsigned ffn_xoff(unsigned key, int L, int w) { return OFF_CA + L * 32768 + (key_row(key) >> 7) * 2048 + w * 256; }
__device__ __forceinline__ void row_load(rsrc_t rs, int g, unsigned xo, u32x4 (&xv)[4]) {
  ld16h<0>(xv[0], rs, g * 16, xo); ld16h<64>(xv[1], rs, g * 16, xo); ld16h<128>(xv[2], rs, g * 16, xo); ld16h<192>(xv[3], rs, g * 16, xo);
}
template <int SL = 0> __device__ __forceinline__ void ffn_xwait(rsrc_t rs, u32x2 T, unsigned xo, int l, u32x4 (&xv)[4]) {
#define XV_OK() __all(max(max(umax4(xv[0]), umax4(xv[1])), max(umax4(xv[2]), umax4(xv[3]))) != POISON)
  if (!XV_OK()) {
    unsigned spins = 0;
    do {
      if (spin_fail(spins, rs, T)) break;
      if (SL) __builtin_amdgcn_s_sleep(SL);
#pragma unroll
      for (int i = 0; i < 4; ++i) xv[i] = ld16(rs, (l >> 4) * 16 + i * 64, xo);
    } while (!XV_OK());
  }
#undef XV_OK
}
__device__ __forceinline__ void ffn_gu(const u32x4 (&gv)[4], const u32x4 (&uv)[4], const u32x4 (&xv)[4], f32x4& ga, f32x4& ua) {
  ga = mfma16(gv[0], xv[0], f32x4{0.f, 0.f, 0.f, 0.f}); ua = mfma16(uv[0], xv[0], f32x4{0.f, 0.f, 0.f, 0.f});
#pragma unroll
  for (int i = 1; i < 4; ++i) { ga = mfma16(gv[i], xv[i], ga); ua = mfma16(uv[i], xv[i], ua); }
}
__device__ __forceinline__ void ffn_gu_check(rsrc_t rs, u32x2 T, unsigned xo, int l, const u32x4 (&gv)[4], const u32x4 (&uv)[4], u32x4 (&xv)[4], f32x4& ga, f32x4& ua) {
  if (__builtin_expect(__any(ga[0] != ga[0]), 0)) {
    ffn_xwait(rs, T, xo, l, xv);
    ffn_gu(gv, uv, xv, ga, ua);
  }
}
__device__ __forceinline__ void ffn_gu_store(char* smem, int l, int w, f32x4 ga, f32x4 ua) {
  LDS_AS f32x4* sred = (LDS_AS f32x4*)(smem + L_SRED);
  if ((l & 15) == 0) { sred[w * 8 + (l >> 4)] = ga; sred[w * 8 + 4 + (l >> 4)] = ua; }
}
__device__ __forceinline__ f32x4 ffn_t_read(char* smem, int l) { return ((const LDS_AS f32x4*)(smem + L_SRED))[(l & 7) * 8 + ((l >> 3) & 1) * 4 + (l >> 4)]; }
__device__ __forceinline__ void ffn_d_read(char* smem, int l, int blk, s16x4 (&dfr)[8]) {
#pragma unroll
  for (int i = 0; i < 8; ++i) dfr[i] = trread(smem, L_DN + blk * 4096 + (l >> 4) * 1024 + ((l & 15) >> 2) * 256 + i * 32 + (l & 3) * 8);
}
__device__ __forceinline__ float ffn_d_sum(float t) { t += dppf<0xB1>(t); t += dppf<0x4E>(t); return t + dppf<0x141>(t); }
__device__ __forceinline__ u32x2 ffn_h(f32x4 t, int l) {
#pragma unroll
  for (int e = 0; e < 4; ++e) t[e] = ffn_d_sum(t[e]);
  const float a = (l & 1) ? t[1] : t[0], b = (l & 1) ? t[3] : t[2], s = (l & 2) ? b : a;
  const float gg = rbf1(s), uu = rbf1(dppf<0x128>(s));
  const float h = rbf1(gg * __builtin_amdgcn_rcpf(1.0f + __expf(-gg))) * uu;
  const unsigned h0 = pack2(h, dppf<0x55>(h)), h1 = pack2(dppf<0xAA>(h), dppf<0xFF>(h));
  return (l & 15) == 0 ? u32x2{h0, h1} : u32x2{0u, 0u};
}
__device__ __forceinline__ void ffn_d_mma(const s16x4 (&dfr)[8], u32x2& hh, f32x4& ya, f32x4& yb) {
#pragma unroll
  for (int i = 0; i < 8; ++i) {
    if (i & 1) yb = mfma16k16(dfr[i], __builtin_bit_cast(s16x4, hh), yb);
    else ya = mfma16k16(dfr[i], __builtin_bit_cast(s16x4, hh), ya);
    hh = u32x2{dppu<0x111>(hh[0]), dppu<0x111>(hh[1])};
  }
}
__device__ __forceinline__ void ffn_d_store(rsrc_t rs, int l, int G0, int L, int slice, f32x4 y) {
  const unsigned c = l & 15;
  st16(unpoison4(__builtin_bit_cast(u32x4, y)), rs, (G0 + (c >> 1)) * 32768 + slice * 128 + (c & 1) * 64 + (l >> 4) * 16, OFF_PART + L * (1u << 20));
}
__device__ __forceinline__ void red_load(rsrc_t rs, int tid, int L, unsigned G, unsigned key, u32x4 (&pv)[4], u32x2& cxn, u32x2& cx1) {
#pragma unroll
  for (int k = 0; k < 4; ++k) pv[k] = ld16(rs, min(tid + 512 * k, 1791) * 16, OFF_PART + L * (1u << 20) + G * 32768);
  cxn = ld8(rs, (tid & 3) * 16 + (tid & 4) * 2, OFF_CA + L * 32768 + (key_row(key) >> 7) * 2048 + G * 64);
  cx1 = ld8(rs, (tid & 3) * 16 + (tid & 4) * 2, OFF_CB + L * 32768 + (key_row(key) >> 7) * 2048 + G * 64);
}
__device__ __forceinline__ void red_sum(rsrc_t rs, char* smem, u32x2 T, int tid, int w, int L, unsigned G, unsigned key,
                                        u32x4 (&pv)[4], u32x2& cxn, u32x2& cx1) {
#define PV_OK() __all(max(max(max(umax4(pv[0]), umax4(pv[1])), max(umax4(pv[2]), umax4(pv[3]))), max(max(cxn[0], cxn[1]), max(cx1[0], cx1[1]))) != POISON)
  if (!PV_OK()) {
    unsigned spins = 0;
    do {
      if (spin_fail(spins, rs, T)) break;
      red_load(rs, tid, L, G, key, pv, cxn, cx1);
    } while (!PV_OK());
  }
#undef PV_OK
  f32x4 s = (__builtin_bit_cast(f32x4, pv[0]) + __builtin_bit_cast(f32x4, pv[1])) + __builtin_bit_cast(f32x4, pv[2]);
  if (w < 4) s += __builtin_bit_cast(f32x4, pv[3]);
  ((LDS_AS f32x4*)(smem + L_SFIN))[tid] = s;
}
template <int NW> __device__ __forceinline__ u32x2 red_final(char* smem, int l, unsigned key, u32x2 cxn, u32x2 cx1) {
  const LDS_AS f32x4* sf = (const LDS_AS f32x4*)(smem + L_SFIN) + l;
  f32x4 s = (sf[0] + sf[64]) + (sf[128] + sf[192]);
  if constexpr (NW == 8) s += (sf[256] + sf[320]) + (sf[384] + sf[448]);
#pragma unroll
  for (int j = 0; j < 4; ++j) s[j] = sum_hi8(s[j]);
  const unsigned k16 = key >> 16;
  const float rmax = __builtin_bit_cast(float, ((k16 & 0x8000u) ? (k16 & 0x7fffu) : (~k16 & 0xffffu)) << 16);
  const float sig = rbf1(__builtin_amdgcn_rcpf(1.0f + __expf(-rmax)));
  const float xn4[4] = {lo16(cxn[0]), hi16(cxn[0]), lo16(cxn[1]), hi16(cxn[1])};
  const float x14[4] = {lo16(cx1[0]), hi16(cx1[0]), lo16(cx1[1]), hi16(cx1[1])};
  float o[4];
#pragma unroll
  for (int j = 0; j < 4; ++j) o[j] = x14[j] + rbf1(xn4[j] + rbf1(rbf1(s[j]) * sig));
  return u32x2{pack2(o[0], o[1]), pack2(o[2], o[3])};
}

struct RowFin { float mean, var, mb, rd; unsigned key; };
__device__ __forceinline__ void fin_wait(rsrc_t rs, u32x2 T, unsigned voff, unsigned so, u32x4 (&sp)[4]) {
#define ST_OK() __all(max(max(umax4(sp[0]), umax4(sp[1])), max(umax4(sp[2]), umax4(sp[3]))) != POISON)
  if (!ST_OK()) {
    unsigned spins = 0;
    do {
      if (spin_fail(spins, rs, T)) break;
#pragma unroll
      for (int i = 0; i < 4; ++i) sp[i] = ld16(rs, voff + i * 2048, so);
    } while (!ST_OK());
  }
#undef ST_OK
}
__device__ __forceinline__ RowFin fin_key(rsrc_t rs, u32x2 T, unsigned voff, unsigned so, char* skey, int l, int w, unsigned R, float wsum, u32x4 (&sp)[4]) {
  RowFin r;
#define ST_SUM() ((__builtin_bit_cast(f32x4, sp[0]) + __builtin_bit_cast(f32x4, sp[1])) + (__builtin_bit_cast(f32x4, sp[2]) + __builtin_bit_cast(f32x4, sp[3])))
  f32x4 sf = ST_SUM();
  const float z = (sf[0] + sf[1]) + sf[2];
  if (__any(z != z)) {
    fin_wait(rs, T, voff, so, sp);
    sf = ST_SUM();
  }
#undef ST_SUM
  float s0 = sf[0], s1 = sf[1], s2 = sf[2];
  add_g3(s0, s1, s2);
  r.mean = s0 * (1.0f / 1024.0f);
  r.var = fmaxf(__builtin_fmaf(-r.mean, r.mean, s1 * (1.0f / 1024.0f)), 0.f);
  r.mb = rbf1(r.mean); r.rd = ln_rd(r.var);
  const unsigned rb16 = pack2((s2 - r.mb * wsum) * r.rd, 0.f) & 0xffffu;
  r.key = min((((rb16 & 0x8000u) ? (~rb16 & 0xffffu) : (rb16 | 0x8000u)) << 16) | (0xffffu - R), POISON - 1);
  const unsigned wk = umax16(r.key);
  if (l == 0) ((LDS_AS unsigned*)skey)[w] = wk;
  return r;
}
__device__ __forceinline__ unsigned fin_cand(rsrc_t rs, char* skey, int L, int g, int rb, int ct, const RowFin& r, const f32x2 (&v)[8]) {
  const u32x4 k0 = *(const LDS_AS u32x4*)skey, k1 = *(const LDS_AS u32x4*)(skey + 16);
  const unsigned best = max(umax4(k0), umax4(k1));
  if (r.key == best) {
    const unsigned co = L * 32768 + rb * 2048 + ct * 128 + g * 16;
    f32x2 t[8], rd2 = {r.rd, r.rd}, of2 = {-r.mb * r.rd, -r.mb * r.rd};
    asm volatile("" : "+v"(rd2), "+v"(of2));
#pragma unroll
    for (int j = 0; j < 8; ++j) t[j] = __builtin_elementwise_fma(v[j], rd2, of2);
    st16(unpoison4(pack8v(t)), rs, co, OFF_CA); st16(unpoison4(pack8v(t + 4)), rs, co + 64, OFF_CA);
    if (g == 0) st4(best, rs, OFF_KEY + (L * 16 + ct) * cmax(16 * SK, 128) + rb * SK, 0);
    st16(unpoison4(pack8v(v)), rs, co, OFF_CB); st16(unpoison4(pack8v(v + 4)), rs, co + 64, OFF_CB);
  }
  return best;
}
__device__ __forceinline__ void fin_x2(const RowFin& r, f32x2 (&v)[8]) {
  f32x2 sc2 = {1.0f + r.rd, 1.0f + r.rd}, of2 = {-r.mb * r.rd, -r.mb * r.rd};
  asm volatile("" : "+v"(sc2), "+v"(of2));
#pragma unroll
  for (int j = 0; j < 8; ++j) v[j] = __builtin_elementwise_fma(v[j], sc2, of2);
}

extern "C" __device__ unsigned long long dispatch_id() __asm("llvm.amdgcn.dispatch.id");

extern "C" __global__ __launch_bounds__(512, 2) void mega_kernel(const bf16* x, char* ws, const bf16* wqkv, const bf16* wo, const bf16* wr,
                                                                 const bf16* wg, const bf16* wu, const bf16* wd, bf16* out) {
  extern __shared__ __attribute__((aligned(16))) char smem[];
  const int w0_ = __builtin_amdgcn_readfirstlane(threadIdx.x >> 6);
  const unsigned lbase = (unsigned)(size_t)(LDS_AS char*)smem;
  {
    unsigned long long pc;
    unsigned len;
    asm volatile("s_getpc_b64 %0\n\ts_mov_b32 %1, .Ltext_end-mega_kernel\n\t.subsection 1\n.Ltext_end:\n\t.subsection 0" : "=s"(pc), "=s"(len));
    const unsigned o = ((blockIdx.x >> 3) * 64 + (threadIdx.x & 63)) * 128;
    if (w0_ == 7 && o < len)
      asm volatile("s_mov_b32 m0, %0\n\ts_nop 0\n\tglobal_load_lds_dword %1, %2" ::"s"(lbase + L_TOUCH0), "v"(o), "s"(pc & ~127ull) : "memory");
  }
  asm volatile("" : "+s"(x), "+s"(ws), "+s"(wqkv));
#define HGRP() ((xcd & 2) + (xcd >> 2))
#define IDS()                                                                                         \
  int tid = threadIdx.x, w = w0_, bid = blockIdx.x;                                                   \
  asm volatile("" : "+v"(tid));                                                                       \
  asm volatile("" : "+s"(w));                                                                         \
  asm volatile("" : "+s"(bid));                                                                       \
  const int l = tid & 63, xcd = bid & 7, bi = bid >> 3;                                               \
  const int p = (xcd & 1) * 4 + (bi & 3), h = HGRP() * 8 + (bi >> 2);         \
  const int rb = 2 * xcd + (bi & 1), ct = bi >> 1;                               \
  const int slice = (xcd - (xcd > RXCD)) * 32 + bi;                                            \
  const bool ffn_on = xcd != RXCD;                                                                    \
  const int g = l >> 4;                                                                               \
  const int rt = w < 4 ? w : 11 - w;          \
  KARGS();                                                                                            \
  (void)p; (void)h; (void)rb; (void)ct; (void)slice; (void)ffn_on; (void)g; (void)rt;
#define PROT() (2 * (p & 3) + (p >> 2))
  const rsrc_t rs = __builtin_amdgcn_make_buffer_rsrc((void*)ws, 0, WS_BYTES, 0x00020000);
  typedef const void* const __attribute__((address_space(4)))* karg_t;
#define KARGS() karg_t ka = (karg_t)__builtin_amdgcn_kernarg_segment_ptr(); asm volatile("" : "+s"(ka))
#define ARGP(name, i) const GAS bf16* name = (const GAS bf16*)ka[i]; asm volatile("" : "+s"(name))
#define RSRC(ptr, bytes) __builtin_amdgcn_make_buffer_rsrc((void*)(ptr), 0, bytes, 0x00020000)
#define rwq RSRC(pwq, 1024 * 3072 * 2)
#define rwo RSRC(pwo, 1024 * 1024 * 2)
#define rwg RSRC(pwg, 1024 * F_ * 2)
#define rwu RSRC(pwu, 1024 * F_ * 2)
#define rwd RSRC(pwd, 1024 * F_ * 2)
#define rout RSRC(pout, 2048 * 1024 * 2)

#define WQ_VOFF() ((l >> 2) * 6144 + (((l & 3) ^ ((((l >> 4) ^ (l >> 5)) & 1) << 1)) << 4))
#define WQ_PIECE(t, j, dst) dma16(rwq, wqv, ((((t) + rot) & 7) * 128 + ((j) & 7) * 16) * 6144 + ((j) >> 3) * 2048 + h * 64, lbase + (dst) + (j) * 1024)
#define WQ_BOFF(dt) ((g >> 1) * 1024 + (8 * (g & 1) + ((l & 15) >> 2)) * 64 + (l & 3) * 8 + (((dt) ^ (g & 1)) << 5))
#define X_REQ(u, hf) dma16(rx, xv[hf], (p * 256 + rt * 32 + ((u) & 1) * 16 + (hf) * 8) * 2048 + ((((u) >> 2) + rot) & 7) * 256 + (((u) >> 1) & 1) * 128, lbase + rt * 2048 + x_slot(u) + (hf) * 1024)
#define WO_PIECE(j) dma16(rwo, wov, ((w >> 2) * 64 + (w & 3) + 4 * (j)) * 16384 + ct * 128, lbase + 32768 + (j) * 8192 + w * 1024)
#define WD_DMA_OF(v) _Pragma("unroll") for (int i = 0; i < 4; ++i)                                  \
      dma16(rwd, (l >> 4) * 2048 + (l & 15) * 16, (unsigned)(slice * 16 + 4 * i) * 2048 + (v) * 256, lbase + L_DN + (v) * 4096 + i * 1024)
#define FFN_DMA(m) dma16((m) & 1 ? rwu : rwg, fnv, (unsigned)(((4 * w + ((m) >> 1)) * 32) * F_ + slice * 16) * 2, lbase + 65536 + (m) * 8192 + w * 1024)
#define POISON_BIG(L)                                                                                \
  if (w >= 2) {                                                                                      \
    unsigned pz1 = POISON;                                                                           \
    asm volatile("" : "+v"(pz1));                                                                    \
    st16(u32x4{pz1, pz1, pz1, pz1}, rs, (w >= 4 ? OFF_PART + (L) * (1u << 20) + bid * 4096 - 4096 : OFF_STAT + (L) * 524288 + (p * 32 + h) * 2048 - 2048) + tid * 16, 0); \
  }
  u32x4 gv[4], uv[4];
  u32x4 keep[9];
  u32x4 pre[4][2];
#define PRE_LOAD(it_, rot_, s0, s1)                                                                  \
  static_for<(s1) - (s0)>([&](auto c_) __attribute__((always_inline)) {                              \
    constexpr int s = (s0) + decltype(c_)::value;                                                    \
    const unsigned ab_ = OFF_XN + (it_) * (4u << 20) + (((rot_) + (s >> 2)) & 7) * 8192 + ((s >> 1) & 1) * 4096; \
    ld16w<2 * s, (s & 1) * 2048>(pre[s][0], rs, l * 16, ab_ + (p * 8 + rt) * 65536);                         \
    ld16w<2 * s + 1, (s & 1) * 2048 + 1024>(pre[s][1], rs, l * 16, ab_ + (p * 8 + rt) * 65536);                 \
  })
  u32x2 T;
  const GAS bf16* tsrc;
  unsigned xv[2], xa[2];
  u32x2 ep;
#define X_READ(u, d0, d1) { d0 = *(const LDS_AS u32x4*)(size_t)(xa[0] + x_slot(u)); d1 = *(const LDS_AS u32x4*)(size_t)(xa[1] + x_slot(u)); }
  const rsrc_t rx = RSRC(x, 2048 * 1024 * 2);
  {
    IDS();
    const int rot = PROT();
    const bf16* pwq = wqkv;
    const unsigned wqv = WQ_VOFF();
    xv[0] = (l >> 3) * 2048 + (((l & 7) ^ (l >> 4)) << 4); xv[1] = xv[0] ^ 64;
#define WQ_KMAJ(q) (((q) % 3) * 8 + (q) / 3)
    WQ_PIECE(0, WQ_KMAJ(w), L_RING);
    X_REQ(0, 0); X_REQ(0, 1); X_REQ(1, 0); X_REQ(1, 1);
    WQ_PIECE(0, WQ_KMAJ(8 + w), L_RING);
    asm volatile("s_barrier" ::: "memory");
    WQ_PIECE(0, WQ_KMAJ(16 + w), L_RING);
    X_REQ(2, 0); X_REQ(2, 1);
    asm volatile("buffer_load_dwordx2 %0, off, %1, 0 sc1" : "=v"(ep) : "s"(rs) : "memory");
    if constexpr (PBS < 0) { POISON_BIG(0); }
    {
      ARGP(pwo, 3); ARGP(pwg, 5); ARGP(pwu, 6);
      tsrc = (w < 4 ? pwg : pwu) + ((bi * 32 + ((tid & 255) >> 3)) * F_ + (slice >> 5) * 512 + (tid & 7) * 64);
      if (!ffn_on) tsrc = pwo + (bi * 512 + tid) * 64;
      asm volatile("" : "+v"(tsrc));
    }
#pragma unroll
    for (int kk = 0; kk < 2; ++kk) {
      const int r = l & 15;
      xa[kk] = lbase + rt * 2048 + (r >> 3) * 1024 + (r & 7) * 128 + (((4 * kk + g) ^ (r >> 1)) << 4);
      asm volatile("" : "+v"(xa[kk]));
    }
    if (tid == 192) *(LDS_AS u32x4*)(smem + L_QC) = u32x4{0u, 0u, 0u, 0u};
    wait_vm<4>();
    X_READ(0, pre[0][0], pre[1][0]); X_READ(1, pre[0][1], pre[1][1]);
    asm volatile("s_waitcnt lgkmcnt(0)\n\ts_barrier" : "+v"(pre[0][0]), "+v"(pre[0][1]), "+v"(pre[1][0]), "+v"(pre[1][1])::"memory");
    X_REQ(3, 0); X_REQ(3, 1); X_REQ(4, 0); X_REQ(4, 1);
  }
  unsigned sel0 = 0x7FFFFFFFu;
  u32x4 x2p[2];
  unsigned xm0 = 0, kg0 = 0;

  auto layer = [&](auto it_c) __attribute__((always_inline)) {
    constexpr int it = decltype(it_c)::value;
    f32x4 acc[2][6];
    f32x2 qs2[2];
    u32x4 k0, k1, v0, v1;
    unsigned tdum = 0;
    {
      IDS();
      bool aff = false;
      unsigned aq[2];
      u32x4 av[2];
#define PIN_ACC() asm volatile("" : "+{v[176:179]}"(acc[0][0]), "+{v[180:183]}"(acc[0][1]), "+{v[184:187]}"(acc[0][2]), "+{v[188:191]}"(acc[0][3]), "+{v[192:195]}"(acc[0][4]), "+{v[196:199]}"(acc[0][5]), \
                               "+{v[200:203]}"(acc[1][0]), "+{v[204:207]}"(acc[1][1]), "+{v[208:211]}"(acc[1][2]), "+{v[212:215]}"(acc[1][3]), "+{v[216:219]}"(acc[1][4]), "+{v[220:223]}"(acc[1][5]))
#define PINV4(x) asm volatile("" : "+v"(x[0]), "+v"(x[1]), "+v"(x[2]), "+v"(x[3])::"memory")
      auto qkv_loop = [&](auto it_c) __attribute__((always_inline)) {
        constexpr int IT = decltype(it_c)::value;
        const unsigned abase = OFF_XN + IT * (4u << 20) + (p * 8 + rt) * 65536;
        const int rot = PROT() + 5 * IT;
        unsigned fkey = 0, kv, bq[2];
        u32x4 fx[4], bv[2], af[WA][2];
        u32x2 cxn, cx1;
        f32x4 sa, sb, cs4[2];
        f32x2 b2[2];
#define AB_OFF() (OFF_AB + IT * 16384 + (p * 8 + rt) * 128)
#define A_LOAD1(k, a) ld16w<(k) % WA * 2 + (a), ((k) & 1) * 2048 + (a) * 1024>(af[(k) % WA][a], rs, l * 16, abase + ((((k) >> 2) + rot) & 7) * 8192 + (((k) >> 1) & 1) * 4096)
#pragma unroll
        for (int s = 0; s < 2 + 2 * IT; ++s) { af[s][0] = pre[s][0]; af[s][1] = pre[s][1]; }
        const bf16* pwq = wqkv;
        const int tstep = TS0 + bid % TSN;
        const unsigned wqv = WQ_VOFF();
#define QKV_DMA1(t, i)                                                                            \
  {                                                                                               \
    if constexpr (IT == 0) WQ_PIECE(t, 3 * w + (i), L_RING + SLOT(t) * STAGE);                    \
    else dma16s<(i) * 1024>(rs, l * 16, OFF_WI + (h * 8 + (((t) + rot) & 7)) * STAGE + w * 3072, lbase + L_RING + SLOT(t) * STAGE + w * 3072); \
  }
        unsigned bo[2][2];
#pragma unroll
        for (int dt = 0; dt < 2 - IT; ++dt) {
          bo[dt][0] = lbase + L_RING + (IT ? l * 16 : WQ_BOFF(dt)); bo[dt][1] = bo[dt][0] + 2 * STAGE;
          asm volatile("" : "+v"(bo[dt][0]), "+v"(bo[dt][1]));
        }
#define FRAG_T() (lbase + L_RING + (4 * g + ((l & 15) >> 2)) * 64 + (l & 3) * 8 + (((g ^ (g >> 1)) & 1) << 5))
        auto frag_t2 = [&](unsigned o) __attribute__((always_inline)) {
          frag_t t;
          t.hf.a = trabs(o); t.hf.b = trabs(o + 1024);
          return t.v;
        };
        unsigned ka[3];
        auto tile_sum = [&](u32x4 pt, int dst) __attribute__((always_inline)) {
          f32x4 c = __builtin_bit_cast(f32x4, pt);
#pragma unroll
          for (int e = 0; e < 4; ++e) { c[e] += dppf<0x124>(c[e]); c[e] += dppf<0x128>(c[e]); c[e] = add_x16(c[e]); }
          if ((l & 28) == 0) *(LDS_AS f32x4*)(smem + dst + (w * 2 + (l >> 5)) * 64 + (l & 3) * 16) = c;
        };
        frag_t bfr[6];
#define QKV_BO(s, n) (bo[(n) & 1][SLOT((s) >> 2) >> 1] + (SLOT((s) >> 2) & 1) * STAGE + ((n) >> 1) * 8192 + ((s) & 3) * 2048)
#define QKV_RDA(s, n)                                                                             \
  {                                                                                               \
    if constexpr (IT == 0) bfr[n].hf.a = trabs(QKV_BO(s, n));                                     \
    else bfr[n].v = *(const LDS_AS u32x4*)(size_t)(bo[0][SLOT((s) >> 2) >> 1] + (SLOT((s) >> 2) & 1) * STAGE + (((s) & 3) * 6 + (n)) * 1024); \
  }
#define QKV_RDB(s, n) { if constexpr (IT == 0) bfr[n].hf.b = trabs(QKV_BO(s, (n) ^ 1) + 256); }
#pragma unroll
        for (int n = 0; n < 6; ++n) { QKV_RDA(0, n); if (n < 5) QKV_RDB(0, n); }
#define XF_OFF() (l & 8 ? ((l & 7) * RX + bi % RX) * 256 + h * 8 : (p * RX + (xcd >> 1) % RX) * 256 + (4 * rt + (l & 3)) * 8)
#define XP_OFF() (OFF_XNEW + ((rot + FXT) & 7) * 256)
#define FP_OFF(i) (w == 3 ? OFF_XNEW + (i) * 1024 : OFF_FX + h * 3072 + w * 1024)
#define FX_OK(n) __all(((n) == 4 ? max(max(umax4(fx[0]), umax4(fx[1])), max(umax4(fx[2]), umax4(fx[3]))) : max(umax4(fx[0]), umax4(fx[1]))) != POISON)
#define SINCE(s0) (vm_at(IT, s, n) - vm_at(IT, s0, 5))
#define DEF4(x) asm volatile("" : "=v"(x[0]), "=v"(x[1]), "=v"(x[2]), "=v"(x[3]))
        LDS_AS unsigned* qcnt = (LDS_AS unsigned*)(smem + L_QC);
        unsigned qc;
        unsigned qadr = lbase + L_QC, qone = 1;
        asm volatile("" : "+v"(qadr), "+v"(qone));
#define count_in(i) asm volatile("s_mov_b64 exec, 1\n\tds_add_u32 %0, %1 offset:%2\n\ts_mov_b64 exec, -1" ::"v"(qadr), "v"(qone), "n"(4 * (i)) : "memory")
        auto count_wait = [&](int i, unsigned want) __attribute__((always_inline)) {
          if (__builtin_expect((unsigned)__builtin_amdgcn_readfirstlane(qc) < want, 0)) {
            while ((unsigned)__builtin_amdgcn_readfirstlane(__hip_atomic_load(qcnt + i, __ATOMIC_RELAXED, __HIP_MEMORY_SCOPE_WORKGROUP)) < want) {}
          }
          asm volatile("" ::: "memory");
        };
        unsigned long long xok = xm0;
        auto x1_need = [&](unsigned long long m) __attribute__((always_inline)) {
          if (__builtin_expect((xok & m) != m, 0)) {
            for (unsigned spins = 0;;) {
              const u32x2 f = ld8(rs, F_X1 + ((p * RX + (xcd >> 1) % RX) * 32 + (l & 31)) * SX, 0);
              xok = __builtin_amdgcn_ballot_w64(f[0] == T[0] && f[1] == T[1]);
              if ((xok & m) == m) break;
              if (spin_fail(spins, rs, T)) { xok = ~0ull; break; }
              __builtin_amdgcn_s_sleep(KSL);
            }
            asm volatile("" ::: "memory");
          }
        };
        auto gap = [&](auto sc_, auto nc_) __attribute__((always_inline)) {
          constexpr int s = decltype(sc_)::value, n = decltype(nc_)::value;
          {
            constexpr int ts = (s + 3) / 4, tc = (s + 2) / 4, t0 = 1;
            if constexpr (ts >= 3 && ts < 8 && s == 4 * ts - 3 && n == 2) { wait_vm<vm_at(IT, s, n) - vm_tile(IT, ts)>(); count_in(0); }
            if constexpr (!IT && tc >= t0 && tc < 3 && s == 4 * tc - 2 && n == 1) { wait_vm<vm_at(IT, s, n) - vm_tile(IT, tc)>(); count_in(0); }
            if constexpr (IT && ts >= t0 && ts < 3 && s == 4 * ts - 3 && n == 2) count_in(0);
            if constexpr (tc >= t0 && tc < 8 && s == 4 * tc - 2) {
              if constexpr (n == 3) qc = __hip_atomic_load(qcnt, __ATOMIC_RELAXED, __HIP_MEMORY_SCOPE_WORKGROUP);
              if constexpr (n == 5) count_wait(0, 8 * (tc - t0 + 1));
            }
          }
          if constexpr (n == 0 && tile_piece(s)) QKV_DMA1(((s - 3) >> 2) + 3, (s - 3) & 3);
          if constexpr (IT == 0) {
            if constexpr (s == 0 && !(n & 1)) QKV_DMA1(1, n >> 1);
            if constexpr (s >= 1 && s < 4 && n == 2) QKV_DMA1(2, s - 1);
            constexpr int u = s + XL;
            if constexpr (n == 1 && u < 32) {
              wait_vm<vm_at(0, s, n) - vm_x(u)>();
              X_READ(u, af[(u & ~1) % WA][u & 1], af[(u | 1) % WA][u & 1]);
            }
            if constexpr (n == 3 && u + XU < 32) { asm volatile("s_waitcnt lgkmcnt(%0)" ::"n"(s + 1 < QL ? 4 : 3) : "memory"); X_REQ(u + XU, 0); }
            if constexpr (n == 5 && u + XU < 32) X_REQ(u + XU, 1);
            if constexpr (s == WTS && n == WTG) {
              const int i = bi * 192 + w * 24 + min(l, 23);
              touch(tdum, (const GAS bf16*)pwq + (size_t)(i / 12 + 512 * (~xcd & 1)) * 3072 + ((i % 12) >> 2) * 1024 + (xcd >> 1) * 256 + (i & 3) * 64);
            }
            if constexpr (s == PBS && n == 0) { POISON_BIG(0); }
            if constexpr (s == 0 && n == 1) count_in(2);
            if constexpr (s == 0 && n == 3) qc = __hip_atomic_load(qcnt + 2, __ATOMIC_RELAXED, __HIP_MEMORY_SCOPE_WORKGROUP);
            if constexpr (s == 0 && n == 5) count_wait(2, 8);
            if constexpr (s == 1 && n == 1) {
              asm volatile("" : "+v"(ep)::"memory");
              const unsigned long long e = (((unsigned long long)(unsigned)__builtin_amdgcn_readfirstlane(ep[1]) << 32) | (unsigned)__builtin_amdgcn_readfirstlane(ep[0])) + 1ull + dispatch_id() * 0x9E3779B97F4A7C16ull;
              T = u32x2{(unsigned)e, (unsigned)(e >> 32)};
            }
            if constexpr (s == RWL && n == 4) {
              DEF4(fx);
              if (w < 4) ldrows(fx, rx, (l >> 5) * 2048 + (l & 31) * 16, (p * 256 + h * 8 + 2 * w) * 2048);
            }
            if constexpr (s == RWS && n == RWG) {
              if (w < 4) {
                wait_vm<SINCE(RWL)>();
                PINV4(fx);
                f32x2 sv[16];
#pragma unroll
                for (int i = 0; i < 4; ++i) unpack8v(fx[i], sv + 4 * i);
                f32x2 t0 = sv[0] + sv[1], t1 = sv[2] + sv[3];
#pragma unroll
                for (int j = 4; j < 16; j += 2) { t0 += sv[j]; t1 += sv[j + 1]; }
                const float m = sum32(hadd(t0 + t1)) * (1.0f / 1024.0f);
                f32x2 m2 = {m, m}, q0 = {0.f, 0.f}, q1 = {0.f, 0.f};
                asm volatile("" : "+v"(m2));
#pragma unroll
                for (int j = 0; j < 16; j += 2) {
                  const f32x2 d0 = sv[j] - m2, d1 = sv[j + 1] - m2;
                  q0 = __builtin_elementwise_fma(d0, d0, q0); q1 = __builtin_elementwise_fma(d1, d1, q1);
                }
                const float rd = ln_rd(sum32(hadd(q0 + q1)) * (1.0f / 1024.0f));
                if ((l & 30) == 0) st4(__builtin_bit_cast(unsigned, (l & 1) ? -rbf1(m) : rd), rs, (l >> 5) * 4 + (l & 1) * 8192, OFF_AB + (p * 256 + h * 8 + 2 * w) * 4);
              }
            }
            if constexpr (s == POI && n == 4) {
              if (w == 3) {
                unsigned pz1 = POISON;
                asm volatile("" : "+v"(pz1));
                const unsigned prb = 2 * p + (h >> 4), pct = h & 15, m = l - 32;
                static_assert(OFF_CB == OFF_CA + 131072, "poison addressing");
                if (l < 60) st16(u32x4{pz1, pz1, pz1, pz1}, rs, l < 32 ? OFF_CA + (l & 8) * 4096 + (l & 16) * 8192 + prb * 2048 + pct * 128 + (l & 7) * 16
                                                             : l < 56 ? OFF_FX + ((h * 6 + (m >> 2)) * 8 + p) * 64 + (l & 3) * 16
                                                                      : OFF_AB + 16384 + (p * 32 + h) * 32 + (l & 1) * 16 + (l & 2) * 4096, 0);
                if (l < 4) st4(pz1, rs, l < 2 ? OFF_XNEW + bid * 8 + l * 4 : OFF_KEY + ((l & 1) * 16 + pct) * cmax(16 * SK, 128) + prb * SK, 0);
              }
            }
            static_assert(vm_x(EIN + XL) >= vm_at(0, EIN - 3, 5) && IMG >= 1 && CSD >= 1, "first flag");
            if constexpr (s == EIN && n == 1) { if (w < 4) count_in(1); }
            if constexpr (s == EIN + 2 && n == 0) qc = __hip_atomic_load(qcnt + 1, __ATOMIC_RELAXED, __HIP_MEMORY_SCOPE_WORKGROUP);
            if constexpr (s == EIN + 2 && n == 2) {
              if (w == 0) { count_wait(1, 4); if (l < RX) st8(T, rs, F_XN + (p * RX + l) * 256 + h * 8, 0); }
            }
            if constexpr (s == ERQ && n == 4) asm volatile("buffer_load_dwordx2 %0, %1, %2, 0 offen sc1" : "=v"(cxn) : "v"(XF_OFF() + F_XN), "s"(rs) : "memory");
            if constexpr (s == CSL && n == 2) {
              wait_vm<SINCE(ERQ)>();
              asm volatile("" : "+v"(cxn)::"memory");
              if (!__all(cxn[0] == T[0] && cxn[1] == T[1])) poll_flags(rs, XF_OFF(), F_XN, T);
            }
          } else {
#define X1_OFF() (F_X1 + ((p * RX + (xcd >> 1) % RX) * 32 + (l & 31)) * SX)
            if constexpr ((s == 0 || s == 4) && n == 2) asm volatile("buffer_load_dwordx2 %0, %1, %2, 0 offen sc1" : "=v"(cxn) : "v"(X1_OFF()), "s"(rs) : "memory");
            if constexpr ((s == 3 || s == 7) && n == 0) {
              wait_vm<vm_at(1, s, 1) - vm_at(1, s - 3, 3)>();
              asm volatile("" : "+v"(cxn)::"memory");
              xok |= __builtin_amdgcn_ballot_w64(cxn[0] == T[0] && cxn[1] == T[1]);
            }
            if constexpr (a_req(s, n) >= 0 && (a_req(s, n) & 7) == 0) x1_need(0x30003ull << (((rot + a_req(s, n) / 8) & 7) * 2));
          }
          if constexpr (IT && a_req(s, n) >= 0) A_LOAD1(a_req(s, n) / 2, a_req(s, n) & 1);
          if constexpr (IT == 0 && s == CSD && n == CSG) {
            if (w < 3) {
              unsigned a0 = FRAG_T() + SLOT(s >> 2) * STAGE + w * 8192, a1 = a0 ^ 32, one2 = 0x3F803F80u;
              asm volatile("" : "+v"(a0), "+v"(a1), "+v"(one2));
              const u32x4 o4 = {one2, one2, one2, one2};
              f32x4 y0 = {0.f, 0.f, 0.f, 0.f}, y1 = y0;
#pragma unroll
              for (int i = 0; i < 4; ++i) { y0 = mfma16(frag_t2(a0 + i * 2048), o4, y0); y1 = mfma16(frag_t2(a1 + i * 2048), o4, y1); }
              if ((l & 14) == 0) st16(__builtin_bit_cast(u32x4, (l & 1) ? y1 : y0), rs, g * 16 + (l & 1) * 512, OFF_CS + ((h * 6 + 2 * w) * 8 + p) * 64);
            }
          }
          if constexpr (!IT && s == CSL && n == 4) { asm volatile("" : "=v"(fx[0])); if (w < 3) ld16h<0>(fx[0], rs, l * 16, OFF_CS + h * 3072 + w * 1024); }
          if constexpr (!IT && s == CSS && n == 2) {
            if (w < 3) {
              wait_vm<SINCE(CSL)>();
              asm volatile("" : "+v"(fx[0])::"memory");
              tile_sum(fx[0], L_CSL);
            }
          }
          if constexpr (s == abl(IT) && n == 4) ld4h<0>(aq[0], rs, (l & 31) * 4 + (l >> 5) * 8192, AB_OFF());
          if constexpr (s == ABS && n == 0) {
            wait_vm<SINCE(abl(IT))>();
            asm volatile("" : "+v"(aq[0])::"memory");
            if constexpr (IT) {
              for (unsigned spins = 0; !__all(aq[0] != POISON) && !spin_fail(spins, rs, T);) {
                ld4h<0>(aq[0], rs, (l & 31) * 4 + (l >> 5) * 8192, AB_OFF());
                asm volatile("s_waitcnt vmcnt(0)" : "+v"(aq[0])::"memory");
              }
            }
            *(LDS_AS unsigned*)(smem + L_K + (8 + rt) * 2048 + l * 4) = aq[0];
          }
          if constexpr (s == ABS && n == 2) {
#pragma unroll
            for (int a = 0; a < 2; ++a) {
              const LDS_AS char* ab = (const LDS_AS char*)(smem + L_K + (8 + rt) * 2048 + a * 64);
              aq[a] = *(const LDS_AS unsigned*)(ab + (l & 15) * 4); bq[a] = *(const LDS_AS unsigned*)(ab + 128 + (l & 15) * 4);
              av[a] = *(const LDS_AS u32x4*)(ab + g * 16); bv[a] = *(const LDS_AS u32x4*)(ab + 128 + g * 16);
            }
          }
          if constexpr (s == ABS && n == 5) {
#pragma unroll
            for (int a = 0; a < 2; ++a) { b2[a] = f32x2{asf(bq[a]), asf(bq[a])}; asm volatile("" : "+v"(b2[a])); }
          }
          if constexpr (s == ABS + 1) {
            constexpr int k = (n + 4) % 6;
            if constexpr (k < 4) cs4[k & 1] = *(const LDS_AS f32x4*)(smem + L_CSL + k * 64 + g * 16);
            else cs4[k & 1][0] = *(const LDS_AS float*)(smem + L_CSL + k * 64 + (l & 15) * 4);
          }
          if constexpr ((s == ABS + 1 && n > 0) || (s == ABS + 2 && n == 0)) {
            constexpr int k = (n + 3) % 6;
            if constexpr (k < 4) {
#pragma unroll
              for (int a = 0; a < 2; ++a) acc[a][k] = __builtin_elementwise_fma(cs4[k & 1], f32x4{b2[a][0], b2[a][1], b2[a][0], b2[a][1]}, acc[a][k]);
            } else {
              f32x2 c2 = {cs4[k & 1][0], cs4[k & 1][0]};
              asm volatile("" : "+v"(c2));
#pragma unroll
              for (int a = 0; a < 2; ++a) acc[a][k] = __builtin_elementwise_fma(f32x4{c2[0], c2[1], c2[0], c2[1]}, __builtin_bit_cast(f32x4, bv[a]), acc[a][k]);
            }
          }
          if constexpr (IT == 0) {
            if constexpr (s == KP0 - 1 && n < 3) {
              const int c = 3 * (w & 1) + n;
              ka[n] = (FRAG_T() ^ ((c & 1) << 5)) + (SLOT(5) * STAGE + (c >> 1) * 8192 + (w >> 1) * 2048);
              asm volatile("" : "+v"(ka[n]));
            }
            if constexpr (s >= KP0 && s < KP0 + 3 && (n & 1)) keep[3 * (s - KP0) + (n >> 1)] = frag_t2(ka[n >> 1] + (s - KP0) * STAGE);
            if constexpr (s >= IMG && s < IMG + 3 && n == 4) st16(keep[s - IMG], rs, l * 16 + (s - IMG) * 1024, OFF_WI + (h * 8 + ((rot + 5) & 7)) * STAGE + w * 3072);
            if constexpr (s >= TS0 && s < TS0 + TSN && n == 4) {
              if (tstep == s && w < TW && (THALF != 1 || !ffn_on)) touch(tdum, tsrc);
            }
            if constexpr (s == TST && n == 4) { if (w >= TW) touch(tdum, tsrc); }
          } else {
            if constexpr (s == 28 && n == 0) { POISON_BIG(1); }
            if constexpr (s == FSL && n == 4) { ld4h<0>(kv, rs, (l & 15) * SK, OFF_KEY + (bid & 15) * cmax(16 * SK, 128)); row_load(rs, g, ffn_xoff(kg0, 0, w), fx); }
            if constexpr (s == FS0 && n == 0) {
              wait_vm<SINCE(FSL)>();
              asm volatile("" : "+v"(kv)::"memory");
              PINV4(fx);
              if (__builtin_expect(!__all(kv != POISON), 0)) {
                LDS_AS unsigned* kw = (LDS_AS unsigned*)(smem + L_SKEY + 40);
                if (w == 0) {
                  kv = key_wait(rs, T, l, 0, bid & 15, kv);
                } else {
                  while ((kv = __hip_atomic_load(kw, __ATOMIC_RELAXED, __HIP_MEMORY_SCOPE_WORKGROUP)) == POISON) __builtin_amdgcn_s_sleep(2);
                }
              }
              fkey = __builtin_amdgcn_readfirstlane(umax16(kv));
              if (w == 0) *(LDS_AS unsigned*)(smem + L_SKEY + 40) = min(fkey, POISON - 1);
              sel0 = key_row(fkey);
              aff = (int)(sel0 >> 8) == p;
              asm volatile("" : "=v"(sa), "=v"(sb));
              if (ffn_on) {
                if (__builtin_expect(((kg0 ^ fkey) & 0x780u) != 0, 0)) ld4x16(fx, rs, g * 16, ffn_xoff(fkey, 0, w));
                ffn_gu(gv, uv, fx, sa, sb);
              }
            }
            if constexpr (s == FS0 && n == 2) {
              if (ffn_on) {
                ffn_gu_check(rs, T, ffn_xoff(fkey, 0, w), l, gv, uv, fx, sa, sb);
                ffn_gu_store(smem, l, w, sa, sb);
                count_in(1);
              }
            }
            if constexpr (s == FS0D && n == FDG) {
              if (ffn_on && w < 4) {
                s16x4 da[8], db[8];
                ffn_d_read(smem, l, 2 * w, da);
                while (__hip_atomic_load(qcnt + 1, __ATOMIC_RELAXED, __HIP_MEMORY_SCOPE_WORKGROUP) < 8u) {}
                asm volatile("" ::: "memory");
                const f32x4 t = ffn_t_read(smem, l);
                ffn_d_read(smem, l, 2 * w + 1, db);
                u32x2 ha = ffn_h(t, l), hb = {dppu<0x118>(ha[0]), dppu<0x118>(ha[1])};
                f32x4 ya = {0.f, 0.f, 0.f, 0.f}, yb = ya;
#pragma unroll
                for (int i = 0; i < 8; ++i) {
                  ya = mfma16k16(da[i], __builtin_bit_cast(s16x4, ha), ya); yb = mfma16k16(db[i], __builtin_bit_cast(s16x4, hb), yb);
                  ha = u32x2{dppu<0x111>(ha[0]), dppu<0x111>(ha[1])}; hb = u32x2{dppu<0x111>(hb[0]), dppu<0x111>(hb[1])};
                }
                ffn_d_store(rs, l, 8 * w, 0, slice, ya + yb);
              }
            }
#define RED_LOAD(ld, l8)                                                                                                    \
  {                                                                                                                         \
    const unsigned G = bi, po = OFF_PART + G * 32768;                                                        \
    ld(gv[0], rs, tid * 16, po); ld(gv[1], rs, tid * 16, po + 4096); ld(gv[2], rs, tid * 16, po + 8192); ld(gv[3], rs, tid * 16, po + 12288); \
    ld(uv[0], rs, tid * 16, po + 16384); ld(uv[1], rs, tid * 16, po + 20480); ld(uv[2], rs, tid * 16, po + 24576);        \
    l8(cxn, rs, (l & 3) * 16 + (l & 4) * 2, OFF_CA + (sel0 >> 7) * 2048 + G * 64);                                         \
    l8(cx1, rs, (l & 3) * 16 + (l & 4) * 2, OFF_CB + (sel0 >> 7) * 2048 + G * 64);                                          \
  }
            if constexpr (s == FS1 && n == 4) {
              asm volatile("" : "=v"(cxn), "=v"(cx1));
              if (!ffn_on && w < 4) RED_LOAD(ld16u, ld8h);
            }
            if constexpr (s == FS1S && n == 0) {
              if (!ffn_on && w < 4) {
                wait_vm<SINCE(FS1)>();
                PINV4(gv); PINV4(uv);
                asm volatile("" : "+v"(cxn), "+v"(cx1)::"memory");
                for (unsigned spins = 0; !__all(max(max(max(umax4(gv[0]), umax4(gv[1])), max(umax4(gv[2]), umax4(gv[3]))),
                                                    max(max(umax4(uv[0]), umax4(uv[1])), max(max(umax4(uv[2]), max(cxn[0], cxn[1])), max(cx1[0], cx1[1])))) != POISON) && !spin_fail(spins, rs, T);) {
                  RED_LOAD(ld16u, ld8h);
                  DRAIN();
                  PINV4(gv); PINV4(uv);
                  asm volatile("" : "+v"(cxn), "+v"(cx1)::"memory");
                }
#define F4(x) __builtin_bit_cast(f32x4, x)
                ((LDS_AS f32x4*)(smem + L_SFIN))[tid] = ((F4(gv[0]) + F4(gv[1])) + (F4(gv[2]) + F4(gv[3]))) + ((F4(uv[0]) + F4(uv[1])) + F4(uv[2]));
#undef F4
                count_in(2);
                if (w == 0) {
                  while (__hip_atomic_load(qcnt + 2, __ATOMIC_RELAXED, __HIP_MEMORY_SCOPE_WORKGROUP) < 4u) {}
                  asm volatile("" ::: "memory");
                  const u32x2 ov = red_final<4>(smem, l, fkey, cxn, cx1);
                  if (l < 8) st8(unpoison2(ov), rs, OFF_XNEW + (bi * 8 + (l & 3) * 2 + (l >> 2)) * 8, 0);
                }
              }
            }
            if constexpr (s == FXA && n == 4) { DEF4(fx); if (w < 3) row_load(rs, g, XP_OFF(), fx); }
            if constexpr (s == FXB && n == 0) {
              if (w < 3) {
                const unsigned fa = bo[0][0] + SLOT(FXT) * STAGE + w * 2048;
                u32x4 f[8];
#pragma unroll
                for (int i = 0; i < 8; ++i) f[i] = *(const LDS_AS u32x4*)(size_t)(fa + (i >> 1) * 6144 + (i & 1) * 1024);
                wait_vm<SINCE(FXA)>();
                PINV4(fx);
                ffn_xwait<XSL>(rs, T, XP_OFF(), l, fx);
                f32x4 y0 = mfma16(f[0], fx[0], f32x4{0.f, 0.f, 0.f, 0.f}), y1 = mfma16(f[1], fx[0], f32x4{0.f, 0.f, 0.f, 0.f});
#pragma unroll
                for (int i = 1; i < 4; ++i) { y0 = mfma16(f[2 * i], fx[i], y0); y1 = mfma16(f[2 * i + 1], fx[i], y1); }
                if ((l & 14) == 0) st16(unpoison4(__builtin_bit_cast(u32x4, (l & 1) ? y1 : y0)), rs, g * 16 + (l & 1) * 512, OFF_FX + ((h * 6 + 2 * w) * 8 + p) * 64);
              }
            }
            if constexpr (s == FXL && n == 4) {
              asm volatile("" : "=v"(fx[0]), "=v"(fx[1]));
              if (aff && w < 4) { ld16h<0>(fx[0], rs, l * 16, FP_OFF(0)); ld16h<0>(fx[1], rs, l * 16, FP_OFF(1)); }
            }
            if constexpr (s == FXS && n == 2) {
              if (aff && w < 4) {
                wait_vm<0>();
                asm volatile("" : "+v"(fx[0]), "+v"(fx[1])::"memory");
                if (!FX_OK(2)) {
                  unsigned spins = 0;
                  do {
                    if (spin_fail(spins, rs, T)) break;
                    fx[0] = ld16(rs, l * 16, FP_OFF(0)); fx[1] = ld16(rs, l * 16, FP_OFF(1));
                  } while (!FX_OK(2));
                }
                if (w == 3) {
                  f32x2 nx[8];
                  unpack8v(fx[0], nx); unpack8v(fx[1], nx + 4);
                  const f32x2 sm = ((nx[0] + nx[1]) + (nx[2] + nx[3])) + ((nx[4] + nx[5]) + (nx[6] + nx[7]));
                  const float mean = sum64(hadd(sm)) * (1.0f / 1024.0f);
                  const f32x2 m2 = {mean, mean};
                  f32x2 q = {0.f, 0.f};
#pragma unroll
                  for (int j = 0; j < 8; ++j) { const f32x2 d = nx[j] - m2; q = __builtin_elementwise_fma(d, d, q); }
                  const float rd = ln_rd(sum64(hadd(q)) * (1.0f / 1024.0f));
                  if (l == 0) *(LDS_AS f32x2*)(smem + L_FXL + 384) = f32x2{rd, -rbf1(mean)};
                } else {
                  tile_sum(fx[0], L_FXL);
                }
                count_in(3);
              }
            }
          }
        };
        static_for<32>([&](auto sc_) __attribute__((always_inline)) {
          constexpr int s = decltype(sc_)::value;
          if constexpr (s > 0) PIN_ACC();
          if constexpr (IT == 1) wait_vm<vm_at(IT, s, 0) - vm_a(IT, s)>();
          pin_w<s % WA>(af[s % WA][0], af[s % WA][1]);
          SB();
          static_for<6>([&](auto nc_) __attribute__((always_inline)) {
            constexpr int n = decltype(nc_)::value;
            if constexpr (s == 0) { acc[0][n] = f32x4{0.f, 0.f, 0.f, 0.f}; acc[1][n] = acc[0][n]; }
            constexpr bool on = s < QL || n >= 2;
            if constexpr (on) acc[0][n] = n < 4 ? mfma16(bfr[n].v, af[s % WA][0], acc[0][n]) : mfma16(af[s % WA][0], bfr[n].v, acc[0][n]);
            SB();
            if constexpr (n == 0) { QKV_RDB(s, 5); }
            else if constexpr (s + 1 < 32 && (s + 1 < QL || n > 2)) QKV_RDB(s + 1, n - 1);
            SB();
            if constexpr (on) acc[1][n] = n < 4 ? mfma16(bfr[n].v, af[s % WA][1], acc[1][n]) : mfma16(af[s % WA][1], bfr[n].v, acc[1][n]);
            SB();
            if constexpr (s + 1 < 32 && (s + 1 < QL || n >= 2)) QKV_RDA(s + 1, n);
            SB();
            gap(sc_, nc_);
            SB();
          });
        });
        PIN_ACC();
        frag_t qw[32 - QL][2];
        static_for<32 - QL>([&](auto c_) __attribute__((always_inline)) {
          constexpr int s = QL + decltype(c_)::value;
          QKV_RDA(s, 0); QKV_RDB(s, 0); QKV_RDA(s, 1); QKV_RDB(s, 1);
          qw[s - QL][0] = bfr[0]; qw[s - QL][1] = bfr[1];
        });
        SB();
        const bool own = aff && (int)((sel0 >> 5) & 7) == rt;
        const bool m0 = !(sel0 & 16), mq = (l & 15) == (int)(sel0 & 15), mv = g == (int)((sel0 & 15) >> 2);
        auto patch_qk = [&](int n) __attribute__((always_inline)) {
          f32x4 t = *(const LDS_AS f32x4*)(smem + L_FXL + n * 64 + g * 16);
          const f32x4 c = *(const LDS_AS f32x4*)(smem + L_CSL + n * 64 + g * 16);
          const float nb = *(const LDS_AS float*)(smem + L_FXL + 388);
#pragma unroll
          for (int e = 0; e < 4; ++e) { t[e] = __builtin_fmaf(c[e], nb, t[e]); asm volatile("" : "+v"(t[e])); }
          acc[0][n] = (mq && m0) ? t : acc[0][n];
          acc[1][n] = (mq && !m0) ? t : acc[1][n];
        };
        if (own) {
          while (__hip_atomic_load(qcnt + 3, __ATOMIC_RELAXED, __HIP_MEMORY_SCOPE_WORKGROUP) < 4u) {}
          asm volatile("" ::: "memory");
          patch_qk(2); patch_qk(3);
          const f32x2 nab = *(const LDS_AS f32x2*)(smem + L_FXL + 384);
          const LDS_AS float* fl_ = (const LDS_AS float*)(smem + L_FXL + 256) + (l & 15);
          const LDS_AS float* cl_ = (const LDS_AS float*)(smem + L_CSL + 256) + (l & 15);
          const float t4 = __builtin_fmaf(cl_[0], nab[1], fl_[0]), t5 = __builtin_fmaf(cl_[16], nab[1], fl_[16]);
          const unsigned na = __builtin_bit_cast(unsigned, (float)nab[0]);
#pragma unroll
          for (int a = 0; a < 2; ++a) {
            const bool ma = a ? !m0 : m0;
            aq[a] = (mq && ma) ? na : aq[a];
#pragma unroll
            for (int e = 0; e < 4; ++e) {
              const bool me = mv && ma && e == (int)(sel0 & 3);
              acc[a][4][e] = me ? t4 : acc[a][4][e]; acc[a][5][e] = me ? t5 : acc[a][5][e];
              av[a][e] = me ? na : av[a][e];
            }
          }
        }
        constexpr float C2 = 1.4426950408889634f * 0.17677669529663687f;
#pragma unroll
        for (int a = 0; a < 2; ++a) {
          f32x2 a2 = {asf(aq[a]), asf(aq[a])};
          qs2[a] = f32x2{asf(aq[a]) * C2, asf(aq[a]) * C2};
          asm volatile("" : "+v"(a2), "+v"(qs2[a]));
          acc[a][2] *= f32x4{a2[0], a2[1], a2[0], a2[1]}; acc[a][3] *= f32x4{a2[0], a2[1], a2[0], a2[1]};
          acc[a][4] *= __builtin_bit_cast(f32x4, av[a]); acc[a][5] *= __builtin_bit_cast(f32x4, av[a]);
        }
        k0 = pack8(acc[0][2], acc[0][3]); k1 = pack8(acc[1][2], acc[1][3]); v0 = pack8(acc[0][4], acc[1][4]); v1 = pack8(acc[0][5], acc[1][5]);
        const unsigned kvo = OFF_KV + IT * (8u << 20) + (p * 32 + h) * 32768 + rt * 4096;
        if (p < 7) { st16(k0, rs, l * 16, kvo); st16(k1, rs, l * 16 + 1024, kvo); st16(v0, rs, l * 16 + 2048, kvo); st16(v1, rs, l * 16 + 3072, kvo); }
        char* ownp = smem + (8 + rt) * 2048 + l * 16;
        *(u32x4*)(ownp + L_K) = k0;
        if (rt < 7) *(u32x4*)(ownp + L_K + 1024) = k1;
        *(u32x4*)(ownp + L_V) = v0; *(u32x4*)(ownp + L_V + 1024) = v1;
        SB();
        static_for<32 - QL>([&](auto c_) __attribute__((always_inline)) {
          constexpr int s = QL + decltype(c_)::value;
#pragma unroll
          for (int n = 0; n < 2; ++n) { acc[0][n] = mfma16(qw[s - QL][n].v, af[s % WA][0], acc[0][n]); acc[1][n] = mfma16(qw[s - QL][n].v, af[s % WA][1], acc[1][n]); }
        });
        if (own) { patch_qk(0); patch_qk(1); }
      };
      qkv_loop(it_c);
    }
#undef A_LOAD1
#undef QKV_RDA
#undef QKV_RDB
#undef QKV_BO
    {
      IDS();
      const GAS bf16 *pwo, *pwg, *pwu;
      asm volatile("s_load_dwordx2 %0, %3, 24\n\ts_load_dwordx2 %1, %3, 40\n\ts_load_dwordx2 %2, %3, 48" : "=&s"(pwo), "=&s"(pwg), "=&s"(pwu) : "s"(ka));
      const int ql = l & 15;
      const f32x4 z4 = {0.f, 0.f, 0.f, 0.f}, ninf = {-INFINITY, -INFINITY, -INFINITY, -INFINITY};
      LDS_AS unsigned* rdy = (LDS_AS unsigned*)(smem + L_RDY);
      const unsigned rtag = T[0] + it;
      u32x4 qf[2];
      f32x4 cn[2], oa[2][2], sum[2], S[2][2];
      unsigned one2 = 0x3F803F80u;
      asm volatile("" : "+v"(one2));
      const u32x4 ones = {one2, one2, one2, one2};
      auto ex8 = [&](f32x4 a, f32x4 b) __attribute__((always_inline)) {
        return u32x4{pack2(__builtin_amdgcn_exp2f(a[0]), __builtin_amdgcn_exp2f(a[1])), pack2(__builtin_amdgcn_exp2f(a[2]), __builtin_amdgcn_exp2f(a[3])),
                     pack2(__builtin_amdgcn_exp2f(b[0]), __builtin_amdgcn_exp2f(b[1])), pack2(__builtin_amdgcn_exp2f(b[2]), __builtin_amdgcn_exp2f(b[3]))};
      };
      {
#pragma unroll
        for (int a = 0; a < 2; ++a) {
          const f32x4 c4 = {qs2[a][0], qs2[a][1], qs2[a][0], qs2[a][1]};
          qf[a] = pack8(acc[a][0] * c4, acc[a][1] * c4);
        }
        f32x4 mk;
#pragma unroll
        for (int e = 0; e < 4; ++e) mk[e] = (4 * g + e <= ql) ? 0.f : -INFINITY;
        const f32x4 d0 = mfma16(k0, qf[0], mk), d1 = mfma16(k1, qf[1], mk), d2 = mfma16(k0, qf[1], z4);
        const float m0 = -max_x32(max_x16(fmaxf(fmaxf(d0[0], d0[1]), fmaxf(d0[2], d0[3]))));
        const float m1 = -max_x32(max_x16(fmaxf(fmaxf(d1[0], d1[1]), fmaxf(d1[2], d1[3]))));
        cn[0] = f32x4{m0, m0, m0, m0}; cn[1] = f32x4{m1, m1, m1, m1};
        asm volatile("" : "+v"(cn[0]), "+v"(cn[1]));
        const f32x4 e0 = d0 + cn[0];
        const u32x4 p0 = {pack2(__builtin_amdgcn_exp2f(e0[0]), __builtin_amdgcn_exp2f(e0[1])), pack2(__builtin_amdgcn_exp2f(e0[2]), __builtin_amdgcn_exp2f(e0[3])), 0u, 0u};
        const u32x4 p1 = ex8(d2 + cn[1], d1 + cn[1]);
        oa[0][0] = mfma16(v0, p0, z4); oa[0][1] = mfma16(v1, p0, z4); sum[0] = mfma16(ones, p0, z4);
        oa[1][0] = mfma16(v0, p1, z4); oa[1][1] = mfma16(v1, p1, z4); sum[1] = mfma16(ones, p1, z4);
      }
      const char* kb_ = smem + L_K + l * 16;
      const char* vb_ = smem + L_V + l * 16;
      const unsigned wov = (l >> 3) * 2048 + (((((l & 7) >> 1) ^ ((l >> 4) & 3)) * 2 + (l & 1)) << 4), fnv = (l >> 1) * (F_ * 2) + (l & 1) * 16;
      const unsigned pp = __builtin_amdgcn_readfirstlane(p > 0 ? 32 : 0), pvo = OFF_KV + it * (8u << 20) + (p * 32 + h - pp) * 32768;
      const unsigned pfo = __builtin_amdgcn_readfirstlane(F_KV + (it * 256 + p * 32 + h - pp) * cmax(8 * SV, 128));
      asm volatile("" : "+v"(oa[0][0]), "+v"(oa[0][1]), "+v"(oa[1][0]), "+v"(oa[1][1]), "+v"(sum[0]), "+v"(sum[1]));
      DRAIN();
      asm volatile("" : "+v"(tdum));
      if (l == 0) st8(T, rs, rt * SV, F_KV + (it * 256 + p * 32 + h) * cmax(8 * SV, 128));
      BAR();
      asm volatile("" : "+s"(pwo), "+s"(pwg), "+s"(pwu));
      WO_PIECE(15); WO_PIECE(14);
#define LDS16(a_) (*(const LDS_AS u32x4*)(size_t)(a_))
      u32x4 kf[2], vf[2];
      int nw = 0;
      u32x4 p0, p1;
      float e0, e1;
#define EE(v, i) { e0 = __builtin_amdgcn_exp2f(v[i]); e1 = __builtin_amdgcn_exp2f(v[i + 1]); pin2s(e0, e1); SB(); }
#define CV(P, i) { unsigned t_ = pack2(e0, e1); pin1(t_); P[i] = t_; SB(); }
      auto body = [&](unsigned ad, auto dk_) __attribute__((always_inline)) {
        constexpr int dk = decltype(dk_)::value;
        const f32x4 a0 = S[1][0], a1 = S[1][1];
        sum[0] = mfma16(ones, p0, sum[0]); SB();
        EE(a0, 0);
        lds_wait<2>(kf[0], kf[1]);
        S[0][0] = mfma16(kf[0], qf[0], cn[0]); SB();
        CV(p1, 0); EE(a0, 2);
        S[0][1] = mfma16(kf[1], qf[0], cn[0]); SB();
        CV(p1, 1); EE(a1, 0);
        lds_wait<0>(vf[0], vf[1]);
        oa[0][0] = mfma16(vf[0], p0, oa[0][0]); SB();
        CV(p1, 2); EE(a1, 2);
        oa[0][1] = mfma16(vf[1], p0, oa[0][1]); SB();
        CV(p1, 3);
        S[1][0] = mfma16(kf[0], qf[1], cn[1]); SB();
        EE(S[0][0], 0);
        S[1][1] = mfma16(kf[1], qf[1], cn[1]); SB();
        lds2<2048 + dk>(kf[0], kf[1], ad); SB();
        CV(p0, 0); EE(S[0][0], 2);
        oa[1][0] = mfma16(vf[0], p1, oa[1][0]); SB();
        CV(p0, 1); EE(S[0][1], 0);
        oa[1][1] = mfma16(vf[1], p1, oa[1][1]); SB();
        lds2<2048 + L_V>(vf[0], vf[1], ad); SB();
        CV(p0, 2); EE(S[0][1], 2);
        sum[1] = mfma16(ones, p1, sum[1]); SB();
        CV(p0, 3);
      };
      auto last = [&]() __attribute__((always_inline)) {
        sum[0] = mfma16(ones, p0, sum[0]); SB();
        EE(S[1][0], 0);
        lds_wait<0>(vf[0], vf[1]);
        pin2(kf[0], kf[1]);
        oa[0][0] = mfma16(vf[0], p0, oa[0][0]); SB();
        CV(p1, 0); EE(S[1][0], 2);
        oa[0][1] = mfma16(vf[1], p0, oa[0][1]); SB();
        CV(p1, 1); EE(S[1][1], 0);
        const float f0 = __builtin_amdgcn_exp2f(S[1][1][2]), f1 = __builtin_amdgcn_exp2f(S[1][1][3]);
        p1[2] = pack2(e0, e1); p1[3] = pack2(f0, f1);
        oa[1][0] = mfma16(vf[0], p1, oa[1][0]); oa[1][1] = mfma16(vf[1], p1, oa[1][1]); sum[1] = mfma16(ones, p1, sum[1]);
      };
      auto piece = [&]() __attribute__((always_inline)) {
        if constexpr (it == 1) { WO_PIECE(13 - nw); }
        else if (ffn_on) { FFN_DMA(nw); }
        ++nw;
      };
      if (rt > 0) {
        unsigned ad = lbase + l * 16 + (6 + rt) * 2048;
        lds2<2048>(kf[0], kf[1], ad); lds2<2048 + L_V>(vf[0], vf[1], ad);
        lds_wait<2>(kf[0], kf[1]);
        S[0][0] = mfma16(kf[0], qf[0], cn[0]); S[0][1] = mfma16(kf[1], qf[0], cn[0]); S[1][0] = mfma16(kf[0], qf[1], cn[1]); S[1][1] = mfma16(kf[1], qf[1], cn[1]);
        SB();
        lds_wait<0>(vf[0], vf[1]);
        lds2<0>(kf[0], kf[1], ad);
        p0 = ex8(S[0][0], S[0][1]);
        lds_wait<0>(kf[0], kf[1]);
#pragma unroll 1
        for (int i = rt - 1; i > 0; --i) {
          ad -= 2048;
          body(ad, std::integral_constant<int, -2048>{});
          piece();
          SB();
        }
        piece();
        last();
      }
      {
        constexpr int FW = it ? FW1 : FW0, FN = 8 / FW;
        if (w < FW) {
          const int g0 = FN * rt;
          poll_flags(rs, (l & (FN - 1)) * SV, pfo + g0 * SV, T);
#pragma unroll
          for (int j = 0; j < FN; ++j) {
            const unsigned d = lbase + (g0 + j) * 2048, o = pvo + (g0 + j) * 4096;
            dma16s<0>(rs, l * 16, o, d + L_K); dma16s<1024>(rs, l * 16, o, d + L_K);
            dma16s<0>(rs, l * 16, o + 2048, d + L_V); dma16s<1024>(rs, l * 16, o + 2048, d + L_V);
          }
        }
        if (p == 0) { cn[0] = ninf; cn[1] = ninf; }
        f32x4 mk;
#pragma unroll
        for (int e = 0; e < 4; ++e) mk[e] = (4 * g + e > ql) ? 0.f : -INFINITY;
        const f32x4 cm0 = cn[0] + mk, cm1 = cn[1] + mk;
        if (w < FW) {
          if constexpr (FN > 1) { wait_vm<2 * FN>(); if (l < FN / 2) rdy[FN * rt + l] = rtag; }
          DRAIN();
          if (l < FN) rdy[FN * rt + l] = rtag;
        }
        if constexpr (it == 1) { WO_PIECE(6); WO_PIECE(5); WO_PIECE(4); }
        else { WO_PIECE(13); WO_PIECE(12); if (ffn_on) FFN_DMA(7); }

        int have = rt - 1;
        auto need = [&](int gq) __attribute__((always_inline)) {
          if (__builtin_expect(gq > have && gq < 8, 0)) {
            do {
              const unsigned t = __hip_atomic_load(rdy + (l & 7), __ATOMIC_RELAXED, __HIP_MEMORY_SCOPE_WORKGROUP);
              have = rt - 1 + __builtin_ctz(~((unsigned)__builtin_amdgcn_ballot_w64(t == rtag) & 0xffu) >> rt);
            } while (gq > have);
            asm volatile("" ::: "memory");
          }
        };
        unsigned ad = lbase + l * 16 + rt * 2048;
        need(rt);
        lds2<0>(kf[0], kf[1], ad); lds2<L_V>(vf[0], vf[1], ad);
        lds_wait<2>(kf[0], kf[1]);
        S[0][0] = mfma16(kf[0], qf[0], cm0); S[0][1] = mfma16(kf[1], qf[0], cn[0]); S[1][0] = ninf; S[1][1] = mfma16(kf[1], qf[1], cm1);
        SB();
        lds_wait<0>(vf[0], vf[1]);
        need(rt + 1);
        lds2<2048>(kf[0], kf[1], ad);
        p0 = ex8(S[0][0], S[0][1]);
        lds_wait<0>(kf[0], kf[1]);
#pragma unroll 1
        for (int gq = rt + 1; gq < 8; ++gq) {
          need(gq + 1);
          body(ad, std::integral_constant<int, 2048>{});
          piece();
          SB();
          ad += 2048;
        }
        last();
      }
      {
        float big = 0.f;
#pragma unroll
        for (int a = 0; a < 2; ++a)
#pragma unroll
          for (int d = 0; d < 2; ++d)
            big = __builtin_elementwise_maximum(__builtin_elementwise_maximum(big, __builtin_elementwise_maximum(fabsf(oa[a][d][0]), fabsf(oa[a][d][1]))),
                                                __builtin_elementwise_maximum(fabsf(oa[a][d][2]), fabsf(oa[a][d][3])));
        bool ok = sum[0][0] >= 0.5f && sum[0][0] < 0x1p64f && sum[1][0] >= 0.5f && sum[1][0] < 0x1p64f && big < INFINITY;
        if (!__all(ok)) {
#pragma unroll
          for (int a = 0; a < 2; ++a) {
            const int qs = 2 * rt + a, qq = 256 + 16 * qs + ql, klo = max(qq - 255, p > 0 ? 0 : 256);
            auto score = [&](int t) __attribute__((always_inline)) {
              f32x4 s = mfma16(t == 31 ? k1 : *(const u32x4*)(kb_ + t * 1024), qf[a], z4);
#pragma unroll
              for (int e = 0; e < 4; ++e) { const int kk = 16 * t + 4 * g + e; s[e] = (kk <= qq && kk >= klo) ? s[e] : -INFINITY; }
              return s;
            };
            float mx = -INFINITY;
#pragma unroll 1
            for (int t = qs; t <= qs + 16; ++t) { const f32x4 s = score(t); mx = fmaxf(fmaxf(mx, fmaxf(s[0], s[1])), fmaxf(s[2], s[3])); }
            mx = max_x32(max_x16(mx));
            oa[a][0] = z4; oa[a][1] = z4; sum[a] = z4;
#pragma unroll 1
            for (int G = qs >> 1; G <= (qs + 16) >> 1; ++G) {
              const f32x4 s0 = score(2 * G), s1 = score(2 * G + 1);
              f32x4 e0, e1;
#pragma unroll
              for (int e = 0; e < 4; ++e) { e0[e] = __builtin_amdgcn_exp2f(s0[e] - mx); e1[e] = __builtin_amdgcn_exp2f(s1[e] - mx); }
              const u32x4 pf = pack8(e0, e1);
              oa[a][0] = mfma16(*(const u32x4*)(vb_ + G * 2048), pf, oa[a][0]);
              oa[a][1] = mfma16(*(const u32x4*)(vb_ + G * 2048 + 1024), pf, oa[a][1]);
              sum[a] = mfma16(ones, pf, sum[a]);
            }
          }
        }
      }
#pragma unroll
      for (int a = 0; a < 2; ++a) {
        const float inv = __builtin_amdgcn_rcpf(sum[a][0]);
        oa[a][0] *= inv; oa[a][1] *= inv;
      }
      const unsigned oo = OFF_O + it * (4u << 20) + ((p * 8 + rt) * 64 + h * 2) * 1024;
      st16(pack8(oa[0][0], oa[0][1]), rs, l * 16, oo);
      st16(pack8(oa[1][0], oa[1][1]), rs, l * 16 + 1024, oo);
      DRAIN();
      BAR();
      if (tid == 0) st8(T, rs, h * SO, F_O + (it * 8 + p) * 32 * SO);
      if constexpr (it == 0) {
        DEF4(gv); DEF4(uv);
        if (ffn_on) {
          const unsigned ro = 65536 + w * 1024 + (4 * g + ((l & 15) >> 2)) * 32 + (l & 3) * 8;
#pragma unroll
          for (int i = 0; i < 4; ++i) {
            frag_t f;
            f.hf.a = trread(smem, ro + i * 16384); f.hf.b = trread(smem, ro + i * 16384 + 512);
            gv[i] = f.v;
            f.hf.a = trread(smem, ro + i * 16384 + 8192); f.hf.b = trread(smem, ro + i * 16384 + 8704);
            uv[i] = f.v;
          }
        }
        PINV4(gv); PINV4(uv);
        {
          ARGP(px, 0);
          const unsigned xo = ((rb * 4 + (w & 3)) * 32 + (w >> 2) * 16) * 2048 + ct * 128, xw = (l >> 3) * 2048 + (((l & 7) ^ g) << 4);
          dma16(RSRC(px, 2048 * 1024 * 2), xw, xo, lbase + w * 4096); dma16(RSRC(px, 2048 * 1024 * 2), xw ^ 64, xo + 16384, lbase + w * 4096 + 1024);
        }
      }
      WO_PIECE(3); WO_PIECE(2); WO_PIECE(1); WO_PIECE(0);
      if constexpr (it == 0) { WO_PIECE(11); WO_PIECE(10); WO_PIECE(9); WO_PIECE(8); WO_PIECE(7); WO_PIECE(6); WO_PIECE(5); WO_PIECE(4); }
    }
    IDS();
    ARGP(pwr, 4); ARGP(pwd, 7); ARGP(pout, 8);
    const int kh = w >> 2, rt2 = rb * 4 + (w & 3);
    const unsigned R = rt2 * 32 + kh * 16 + (l & 15);
    const unsigned to = (rt2 * 64 + ct * 4 + kh) * 1024 + l * 16;
    const unsigned rmo = ct * 64 + ((g & 1) ? 12 + 4 * g : 4 * g);
    f32x2 v[8], wv[8];
    float wsum;
    {
      u32x4 wa[2], wrp[2], res[2];
#pragma unroll
      for (int j = 0; j < 2; ++j) {
        wrp[j] = *(const GAS u32x4*)(pwr + rmo + 32 * j);
        wa[j] = *(const GAS u32x4*)(pwr + l * 8 + j * 512);
      }
      {
        f32x2 t[8];
        unpack8v(wa[0], t); unpack8v(wa[1], t + 4);
        const f32x2 s = ((t[0] + t[1]) + (t[2] + t[3])) + ((t[4] + t[5]) + (t[6] + t[7]));
        wsum = sum64(hadd(s));
        unpack8v(trade16(wrp[0]), wv); unpack8v(trade16(wrp[1]), wv + 4);
        asm volatile("" : "+v"(wsum), "+v"(wv[0]), "+v"(wv[1]), "+v"(wv[2]), "+v"(wv[3]), "+v"(wv[4]), "+v"(wv[5]), "+v"(wv[6]), "+v"(wv[7]));
      }
      unsigned bt[4];
#pragma unroll
      for (int n = 0; n < 4; ++n) {
        bt[n] = lbase + 32768 + 65536 + kh * 4096 + (4 * g + ((l & 15) >> 2)) * 128 + (l & 3) * 8 + ((n ^ (2 * (g & 1) + ((l & 15) >> 3))) << 5);
        asm volatile("" : "+v"(bt[n]));
      }
      if (w == 0) poll_flags(rs, (l & 31) * SO, F_O + (it * 8 + (rb >> 1)) * 32 * SO, T);
      else if (it == 0 && w < 3) poll_flags2(rs, F_XN + (((w - 1) * 4 + g) * RX + bi % RX) * 256 + (l & 15) * 16, T);
      else if (it == 0 && w == 3) poll_flags(rs, ((l >> 3) * 32 + h) * cmax(8 * SV, 128) + (l & 7) * SV, F_KV, T);
      asm volatile("s_barrier" ::: "memory");
      const unsigned abase = OFF_O + it * (4u << 20) + (rt2 * 64 + 32 * kh) * 1024;
      const unsigned av0 = l * 16 + kh * 1024, av1 = l * 16 + (kh ^ 1) * 1024;
      u32x4 af[WB][2];
#define O_LOAD(s_) { ld16h<(wo_k(it, s_) & 1) * 2048>(af[(s_) % WB][0], rs, av0, abase + (wo_k(it, s_) >> 1) * 4096); ld16h<(wo_k(it, s_) & 1) * 2048>(af[(s_) % WB][1], rs, av1, abase + (wo_k(it, s_) >> 1) * 4096); }
      static_for<WR>([&](auto c_) __attribute__((always_inline)) { O_LOAD(decltype(c_)::value); });
      if constexpr (it == 1) { ld16h<0>(res[0], rs, g * 16, OFF_XNEW + ct * 128); ld16h<64>(res[1], rs, g * 16, OFF_XNEW + ct * 128); }
      SB();
      f32x4 ac[2][4];
#pragma unroll
      for (int a = 0; a < 2; ++a)
#pragma unroll
        for (int n = 0; n < 4; ++n) ac[a][n] = f32x4{0.f, 0.f, 0.f, 0.f};
      u32x4 bfr[2][4];
#define WO_RD(buf, s, n)                                                                             \
  {                                                                                                  \
    const unsigned o_ = bt[n] + (wo_k(it, s) & 7) * 8192;                                             \
    frag_t f_;                                                                                       \
    f_.hf.a = trabs(o_); f_.hf.b = trabs(o_ + 2048);                                                 \
    bfr[buf][n] = f_.v;                                                                              \
  }
#pragma unroll
      for (int n = 0; n < 4; ++n) WO_RD(0, 0, n);
      static_for<16>([&](auto sc_) __attribute__((always_inline)) {
        constexpr int s = decltype(sc_)::value, cur = s & 1;
        pin_ac(ac);
        if constexpr (s == 1) lds_count<0>(lbase + L_QC);
        if constexpr (s == (it == 0 ? 3 : 11)) lds_look(smem + L_QC, 64);
        if constexpr (s < 15 && ((wo_k(it, s) ^ wo_k(it, s + 1)) & 8)) {
#pragma unroll
          for (int n = 0; n < 4; ++n) { bt[n] += (wo_k(it, s) & 8) ? -65536 : 65536; pin1(bt[n]); }
        }
        if constexpr (s < WB - WR) O_LOAD(WR + 2 * s);
        if constexpr (it == 0 && s == 8) lds_count<8>(lbase + L_QC);
        if constexpr (it == 0 && s == WDS) {
          DRAIN();
          if ((w >> 2) == WDW && ffn_on) { lds_look(smem + L_QC + 8, 16); WD_DMA_OF(w); WD_DMA_OF(w ^ 4); }
        }
        if constexpr (it == 1 || s < WDS) wait_vm<wo_at(it, s) - wo_pos(it, s)>();
        pin2(af[s % WB][0], af[s % WB][1]);
        if constexpr (it == 1 && s == 9) pin2(res[0], res[1]);
        SB();
#pragma unroll
        for (int n = 0; n < 4; ++n) {
          ac[0][n] = mfma16(bfr[cur][n], af[s % WB][0], ac[0][n]);
          ac[1][n] = mfma16(bfr[cur][n], af[s % WB][1], ac[1][n]);
          if (s + 1 < 16) WO_RD(cur ^ 1, s + 1, n);
          if (it == 0 && s == 8) {
            const u32x2 t = *(const LDS_AS u32x2*)(smem + w * 4096 + (l & 15) * 128 + (((2 * n + (g >> 1)) ^ ((l & 15) >> 1)) << 4) + (g & 1) * 8);
            res[n >> 1][2 * (n & 1)] = t[0]; res[n >> 1][2 * (n & 1) + 1] = t[1];
          }
          if (s == 9) {
            if (it == 1 && n == 0) { res[0] = R == sel0 ? res[0] : x2p[0]; res[1] = R == sel0 ? res[1] : x2p[1]; }

            v[2 * n] = f32x2{lo16(res[n >> 1][2 * (n & 1)]), hi16(res[n >> 1][2 * (n & 1)])};
            v[2 * n + 1] = f32x2{lo16(res[n >> 1][2 * (n & 1) + 1]), hi16(res[n >> 1][2 * (n & 1) + 1])};
            pin2f(v[2 * n], v[2 * n + 1]);
          }
          SB();
        }
        if constexpr (s < WB - WR) { O_LOAD(WR + 2 * s + 1); }
        else if constexpr (s + WB < 16) { O_LOAD(s + WB); }
        SB();
      });
#undef WO_RD
#undef O_LOAD
#define XCH_AT(v) ((v) == 7 ? 155648 : (v) * 4096)
#pragma unroll
      for (int n = 0; n < 4; ++n) *(f32x4*)(smem + XCH_AT(w) + n * 1024 + l * 16) = ac[1][n];
#pragma unroll
      for (int n = 0; n < 4; ++n) { v[2 * n] += f32x2{ac[0][n][0], ac[0][n][1]}; v[2 * n + 1] += f32x2{ac[0][n][2], ac[0][n][3]}; }
      BAR();
#pragma unroll
      for (int n = 0; n < 4; ++n) {
        const f32x4 o = *(const f32x4*)(smem + XCH_AT(w ^ 4) + n * 1024 + l * 16);
        v[2 * n] += f32x2{o[0], o[1]}; v[2 * n + 1] += f32x2{o[2], o[3]};
      }
    }
    {
      if (it == 0 && X1F) { st16(pack8v(v), rs, to, OFF_XN + (4u << 20)); st16(pack8v(v + 4), rs, to + 2048, OFF_XN + (4u << 20)); }
      SB();
      const f32x2 s = ((v[0] + v[1]) + (v[2] + v[3])) + ((v[4] + v[5]) + (v[6] + v[7]));
      f32x2 dp = v[0] * wv[0], q = v[0] * v[0];
#pragma unroll
      for (int j = 1; j < 8; ++j) { dp = __builtin_elementwise_fma(v[j], wv[j], dp); q = __builtin_elementwise_fma(v[j], v[j], q); }
      float mt = hadd(s), qq = hadd(q), dot = hadd(dp);
      add_g3(mt, qq, dot);
      const unsigned so = OFF_STAT + it * 524288 + rb * 32768, lrow = R & 127;
      if (g == 0) st16(unpoison4(__builtin_bit_cast(u32x4, f32x4{mt, qq, dot, 0.f})), rs, ct * 2048 + lrow * 16, so);
      SB();
      if (it == 0 && !X1F) { st16(pack8v(v), rs, to, OFF_XN + (4u << 20)); st16(pack8v(v + 4), rs, to + 2048, OFF_XN + (4u << 20)); }
      SB();
      if (it == 0) wait_vm<X1F>();
      if constexpr (it == 0) {
        volatile LDS_AS u32x2* in = (volatile LDS_AS u32x2*)(smem + L_XC);
        if (l == 0) in[w] = T;
        const u32x2 t = in[l & 7];
        if (__all(t[0] == T[0] && t[1] == T[1]) && l < RX) st8(T, rs, (l * 32 + (rb & 1) * 16 + ct) * SX, F_X1 + (rb >> 1) * RX * 32 * SX);
      }
      if (it == 1) { if (ffn_on) { WD_DMA_OF(w); } }
      if (it == 0) {
#pragma unroll
        for (int j = 0; j < 9; ++j) *(u32x4*)(smem + L_RING + (j / 3) * STAGE + (w * 3 + j % 3) * 1024 + l * 16) = keep[j];
      }
      __builtin_amdgcn_s_sleep(it == 0 ? SDI0 : SDIR);
      char* const skey = smem + (it == 0 ? L_SKEY : L_XC);
      u32x4 sp[4];
      ld16h<0>(sp[0], rs, g * 8192 + lrow * 16, so); ld16h<2048>(sp[1], rs, g * 8192 + lrow * 16, so);
      ld16h<0>(sp[2], rs, g * 8192 + lrow * 16, so + 4096); ld16h<2048>(sp[3], rs, g * 8192 + lrow * 16, so + 4096);
      u32x2 xf;
      unsigned k_;
      asm volatile("" : "=v"(xf), "=v"(k_));
      if (it == 0 && w == 0) { ld8h(xf, rs, (l & 31) * SX, F_X1 + (p * RX + (xcd >> 1) % RX) * 32 * SX); ld4h<0>(k_, rs, (l & 15) * SK, OFF_KEY + (bid & 15) * cmax(16 * SK, 128)); }
      DRAIN();
      asm volatile("" : "+v"(xf), "+v"(k_)::"memory");
      PINV4(sp);
      const RowFin r = fin_key(rs, T, g * 8192 + lrow * 16, so, skey, l, w, R, wsum, sp);
      if constexpr (it == 0) {
        if (w == 0) {
          const unsigned need = 0x30003u << (((PROT() + 5) & 7) * 2);
          unsigned m = (unsigned)__builtin_amdgcn_ballot_w64(xf[0] == T[0] && xf[1] == T[1]);
          for (unsigned spins = 0; (m & need) != need;) {
            if (spin_fail(spins, rs, T)) { m = ~0u; break; }
            const u32x2 f = ld8(rs, F_X1 + ((p * RX + (xcd >> 1) % RX) * 32 + (l & 31)) * SX, 0);
            k_ = key_load(rs, l, 0, bid & 15);
            m = (unsigned)__builtin_amdgcn_ballot_w64(f[0] == T[0] && f[1] == T[1]);
          }
          k_ = umax16(k_ == POISON ? 0u : k_);
          if (l == 0) *(LDS_AS u32x4*)(skey + 32) = u32x4{m, k_, POISON, 0u};
        }
        if (tid == 192) *(LDS_AS u32x4*)(smem + L_QC) = u32x4{0u, 0u, 0u, 0u};
      }
      BAR();
      if constexpr (it == 0) {
        PRE_LOAD(1, PROT() + 5, 0, 4);
        const u32x2 mk = *(const LDS_AS u32x2*)(skey + 32);
        xm0 = __builtin_amdgcn_readfirstlane(mk[0]); kg0 = __builtin_amdgcn_readfirstlane(mk[1]);
      }
      const unsigned own = fin_cand(rs, skey, it, g, rb, ct, r, v);
      if constexpr (it == 0) kg0 = max(kg0, (unsigned)__builtin_amdgcn_readfirstlane(own));
      fin_x2(r, v);
      if constexpr (it == 0) {
        x2p[0] = pack8v(v); x2p[1] = pack8v(v + 4);
        asm volatile("" : "+v"(x2p[0]), "+v"(x2p[1]));
        if ((w & 3) * 2 + (w >> 2) == ct >> 1) {
          const float f = 1.0f + r.rd, rd2 = ln_rd(r.var * f * f), mb2 = rbf1(__builtin_fmaf(r.mean - r.mb, r.rd, r.mean));
          if (g < 2 && ((l >> 3) & 1) == (ct & 1))
            st4(min(__builtin_bit_cast(unsigned, g == 0 ? f * rd2 : -__builtin_fmaf(r.mb, r.rd, mb2) * __builtin_amdgcn_rcpf(f)), POISON - 1), rs, OFF_AB + 16384 + R * 4 + g * 8192, 0);
        }
      }
    }
    if (it == 1) {
      const u32x4 op0 = trade16(pack8v(v)), op1 = trade16(pack8v(v + 4));
#define OUT_STORE()                                                                                 \
  if (R != key_row(key)) {                                                                          \
    __builtin_amdgcn_raw_buffer_store_b128(op0, rout, (R * 1024 + rmo) * 2, 0, SC1);                  \
    __builtin_amdgcn_raw_buffer_store_b128(op1, rout, (R * 1024 + rmo + 32) * 2, 0, SC1);             \
  }
      u32x4 xv[4];
      unsigned guess = 0;
      {
        __builtin_amdgcn_s_sleep(GSL);
        const unsigned k_ = key_load(rs, l, 1, bid & 15);
        guess = ffn_xoff(__builtin_amdgcn_readfirstlane(umax16(k_ == POISON ? 0u : k_)), 1, w);
        row_load(rs, g, guess, xv);
      }
      if (w == 0) {
        const unsigned k_ = key_wait(rs, T, l, 1, bid & 15, key_load(rs, l, 1, bid & 15));
        if (l == 0) *(LDS_AS unsigned*)(smem + L_SKEY + 32) = k_;
      }
      DRAIN();
      PINV4(xv);
      BAR();
      const unsigned key = __builtin_amdgcn_readfirstlane(*(LDS_AS unsigned*)(smem + L_SKEY + 32));
      if (ffn_on) {
        f32x4 ga, ua;
        s16x4 dfr[8];
        ffn_d_read(smem, l, w, dfr);
        if (guess != ffn_xoff(key, 1, w)) ld4x16(xv, rs, g * 16, ffn_xoff(key, 1, w));
        ffn_gu(gv, uv, xv, ga, ua);
        ffn_gu_check(rs, T, ffn_xoff(key, 1, w), l, gv, uv, xv, ga, ua);
        ffn_gu_store(smem, l, w, ga, ua);
        BAR();
        f32x4 ya = {0.f, 0.f, 0.f, 0.f}, yb = ya;
        u32x2 hh = ffn_h(ffn_t_read(smem, l), l);
        ffn_d_mma(dfr, hh, ya, yb);
        if ((l & 15) < 8) ffn_d_store(rs, l, 4 * w, 1, slice, ya + yb);
        SB();
        OUT_STORE();
      } else {
        OUT_STORE();
        const unsigned G = bi;
        if (G == 0 && tid == 0) st8(T, rs, F_EP, 0);
        u32x4 pv[4];
        u32x2 cxn, cx1;
        __builtin_amdgcn_s_sleep(RSL);
        red_load(rs, tid, 1, G, key, pv, cxn, cx1);
        red_sum(rs, smem, T, tid, w, 1, G, key, pv, cxn, cx1);
        BAR();
        if (w == 0) {
          const u32x2 ov = red_final<8>(smem, l, key, cxn, cx1);
          if (l < 8) __builtin_amdgcn_raw_buffer_store_b64(ov, rout, (key_row(key) * 1024 + G * 32 + l * 4) * 2, 0, 0);
        }
      }
#undef OUT_STORE
    }
  };
  layer(std::integral_constant<int, 0>{});
  layer(std::integral_constant<int, 1>{});
}

extern "C" void kernel_launch(void* const* d_in, const int* in_sizes, int n_in, void* d_out, int out_size,
                              void* d_ws, size_t ws_size, hipStream_t stream) {
  (void)hipFuncSetAttribute((const void*)mega_kernel, hipFuncAttributeMaxDynamicSharedMemorySize, LDS_BYTES);
  mega_kernel<<<256, 512, LDS_BYTES, stream>>>((const bf16*)d_in[0], (char*)d_ws, (const bf16*)d_in[1], (const bf16*)d_in[2], (const bf16*)d_in[4],
                                               (const bf16*)d_in[5], (const bf16*)d_in[6], (const bf16*)d_in[7], (bf16*)d_out);
}
```

```cpp
#include <hip/hip_runtime.h>
#include <stdint.h>
#include <utility>

typedef __bf16 bf16;
typedef __bf16 bf16x8 __attribute__((ext_vector_type(8)));
typedef __bf16 bf16x2 __attribute__((ext_vector_type(2)));
typedef float f32x4 __attribute__((ext_vector_type(4)));
typedef float f32x2 __attribute__((ext_vector_type(2)));
typedef short s16x4 __attribute__((ext_vector_type(4)));
typedef unsigned int u32x4 __attribute__((ext_vector_type(4)));
typedef unsigned int u32x2 __attribute__((ext_vector_type(2)));
#define LDS_AS __attribute__((address_space(3)))
#define GAS __attribute__((address_space(1)))
typedef GAS unsigned gu32;
typedef GAS unsigned long long gu64;
#define RLX_AGENT __ATOMIC_RELAXED, __HIP_MEMORY_SCOPE_AGENT
typedef __amdgpu_buffer_rsrc_t rsrc_t;

constexpr int cmax(int a, int b) { return a > b ? a : b; }
constexpr int F_ = 3584;
constexpr int SC1 = 16;

constexpr int RX = 4;
constexpr unsigned F_EP = 0, F_TMO = 128;
constexpr unsigned F_XN = 4096;
#define STR 8, 8, 4, 8
constexpr unsigned STV[4] = {STR}, SO = STV[0], SX = STV[1], SK = STV[2], SV = STV[3];
constexpr unsigned F_O = 8u << 20;
constexpr unsigned F_X1 = F_O + 131072;
constexpr unsigned OFF_KEY = F_O + 393216;
constexpr unsigned F_KV = F_O + 524288;
constexpr unsigned OFF_AB = 147456;
constexpr unsigned OFF_XNEW = 262144;
constexpr unsigned OFF_CS = 294912;
constexpr unsigned OFF_FX = 393216;
constexpr unsigned OFF_CA = 524288;
constexpr unsigned OFF_CB = 655360;
constexpr unsigned OFF_STAT = 1u << 20;
constexpr unsigned OFF_PART = 3u << 20;
constexpr unsigned OFF_XN = 8u << 20;
constexpr unsigned OFF_O = 16u << 20;
constexpr unsigned OFF_KV = 32u << 20;
constexpr unsigned OFF_WI = 52u << 20;
constexpr unsigned WS_BYTES = 58u << 20;
constexpr unsigned POISON = 0xFFFFFFFFu;

constexpr int L_ST0 = 147456, L_CNT = 147968, L_CSL = 31872, L_FXL = 32256, L_K = 0, L_V = 32768, L_RING = 65536, L_XCH = 0, L_DN = 32768, L_TOUCH0 = 149504, L_RDY = 31744, L_QC = 31808, LDS_BYTES = 163840;
constexpr int STAGE = 24576;
#define SLOT(t) ((t) & 3)

template <typename F, int... I> __device__ __forceinline__ void static_for_impl(F&& f, std::integer_sequence<int, I...>) {
  (f(std::integral_constant<int, I>{}), ...);
}
template <int N, typename F> __device__ __forceinline__ void static_for(F&& f) { static_for_impl(f, std::make_integer_sequence<int, N>{}); }
__device__ __forceinline__ unsigned pack2(float a, float b) {
  f32x2 v = {a, b};
  return __builtin_bit_cast(unsigned, __builtin_convertvector(v, bf16x2));
}
__device__ __forceinline__ float asf(unsigned u) { return __builtin_bit_cast(float, u); }
__device__ __forceinline__ float lo16(unsigned u) { return __builtin_bit_cast(float, u << 16); }
__device__ __forceinline__ float hi16(unsigned u) { return __builtin_bit_cast(float, u & 0xffff0000u); }
__device__ __forceinline__ u32x4 pack8(const f32x4 a, const f32x4 b) {
  u32x4 o;
  o[0] = pack2(a[0], a[1]); o[1] = pack2(a[2], a[3]); o[2] = pack2(b[0], b[1]); o[3] = pack2(b[2], b[3]);
  return o;
}
__device__ __forceinline__ f32x4 mfma16(u32x4 a, u32x4 b, f32x4 c) {
  return __builtin_amdgcn_mfma_f32_16x16x32_bf16(__builtin_bit_cast(bf16x8, a), __builtin_bit_cast(bf16x8, b), c, 0, 0, 0);
}
__device__ __forceinline__ f32x4 mfma16k16(s16x4 a, s16x4 b, f32x4 c) { return __builtin_amdgcn_mfma_f32_16x16x16bf16_1k(a, b, c, 0, 0, 0); }
template <int CTRL> __device__ __forceinline__ float dppf(float v) {
  return __builtin_bit_cast(float, __builtin_amdgcn_update_dpp(0, __builtin_bit_cast(int, v), CTRL, 0xf, 0xf, true));
}
template <int CTRL> __device__ __forceinline__ unsigned dppu(unsigned v) {
  return (unsigned)__builtin_amdgcn_update_dpp(0, (int)v, CTRL, 0xf, 0xf, true);
}
__device__ __forceinline__ float add_x16(float v) {
  auto r = __builtin_amdgcn_permlane16_swap(__builtin_bit_cast(unsigned, v), __builtin_bit_cast(unsigned, v), false, false);
  return __builtin_bit_cast(float, (unsigned)r[0]) + __builtin_bit_cast(float, (unsigned)r[1]);
}
__device__ __forceinline__ float add_x32(float v) {
  auto r = __builtin_amdgcn_permlane32_swap(__builtin_bit_cast(unsigned, v), __builtin_bit_cast(unsigned, v), false, false);
  return __builtin_bit_cast(float, (unsigned)r[0]) + __builtin_bit_cast(float, (unsigned)r[1]);
}
__device__ __forceinline__ float max_x16(float v) {
  auto r = __builtin_amdgcn_permlane16_swap(__builtin_bit_cast(unsigned, v), __builtin_bit_cast(unsigned, v), false, false);
  return fmaxf(__builtin_bit_cast(float, (unsigned)r[0]), __builtin_bit_cast(float, (unsigned)r[1]));
}
__device__ __forceinline__ float max_x32(float v) {
  auto r = __builtin_amdgcn_permlane32_swap(__builtin_bit_cast(unsigned, v), __builtin_bit_cast(unsigned, v), false, false);
  return fmaxf(__builtin_bit_cast(float, (unsigned)r[0]), __builtin_bit_cast(float, (unsigned)r[1]));
}
__device__ __forceinline__ unsigned umax_x16(unsigned v) {
  auto r = __builtin_amdgcn_permlane16_swap(v, v, false, false);
  return max((unsigned)r[0], (unsigned)r[1]);
}
__device__ __forceinline__ unsigned umax_x32(unsigned v) {
  auto r = __builtin_amdgcn_permlane32_swap(v, v, false, false);
  return max((unsigned)r[0], (unsigned)r[1]);
}
__device__ __forceinline__ float sum_hi8(float v) {
  v += dppf<0x128>(v);
  v = add_x16(v);
  return add_x32(v);
}
__device__ __forceinline__ float sum32(float v) {
  v += dppf<0xB1>(v);
  v += dppf<0x4E>(v);
  v += dppf<0x124>(v);
  v += dppf<0x128>(v);
  return add_x16(v);
}
__device__ __forceinline__ float sum64(float v) {
  v += dppf<0xB1>(v);
  v += dppf<0x4E>(v);
  v += dppf<0x124>(v);
  v += dppf<0x128>(v);
  v = add_x16(v);
  return add_x32(v);
}

#define BAR() asm volatile("s_waitcnt lgkmcnt(0)\n\ts_barrier" ::: "memory")
#define DRAIN() asm volatile("s_waitcnt vmcnt(0)" ::: "memory")
#define SB() __builtin_amdgcn_sched_barrier(0)
template <int N> __device__ __forceinline__ void wait_vm() { asm volatile("s_waitcnt vmcnt(%0)" ::"n"(N) : "memory"); }

__device__ __forceinline__ void dma16(rsrc_t rs, unsigned voff, unsigned soff, unsigned lds) {
  asm volatile("s_mov_b32 m0, %0\n\ts_nop 0\n\tbuffer_load_dwordx4 %1, %2, %3 offen lds" ::"s"(lds), "v"(voff), "s"(rs), "s"(soff) : "memory");
}
template <int OFF> __device__ __forceinline__ void dma16s(rsrc_t rs, unsigned voff, unsigned soff, unsigned lds) {
  asm volatile("s_mov_b32 m0, %0\n\ts_nop 0\n\tbuffer_load_dwordx4 %1, %2, %3 offen offset:%4 sc1 lds" ::"s"(lds), "v"(voff), "s"(rs), "s"(soff), "n"(OFF) : "memory");
}
__device__ __forceinline__ void touch(unsigned& d, const GAS bf16* ptr) { asm volatile("global_load_dword %0, %1, off" : "+v"(d) : "v"(ptr) : "memory"); }
__device__ __forceinline__ void ldrows(u32x4 (&d)[4], rsrc_t rx, unsigned voff, unsigned soff) {
  asm volatile("buffer_load_dwordx4 %0, %4, %5, %6 offen\n\tbuffer_load_dwordx4 %1, %4, %5, %6 offen offset:512\n\t"
               "buffer_load_dwordx4 %2, %4, %5, %6 offen offset:1024\n\tbuffer_load_dwordx4 %3, %4, %5, %6 offen offset:1536"
               : "=&v"(d[0]), "=&v"(d[1]), "=&v"(d[2]), "=&v"(d[3]) : "v"(voff), "s"(rx), "s"(soff) : "memory");
}
__device__ __forceinline__ void pin4(f32x2* v) { asm volatile("" : "+v"(v[0]), "+v"(v[1]), "+v"(v[2]), "+v"(v[3])); }
__device__ __forceinline__ float rbf1(float x) { return __builtin_bit_cast(float, pack2(0.f, x)); }
__device__ __forceinline__ void vswap(unsigned& a, unsigned& b) { asm volatile("v_swap_b32 %0, %1" : "+v"(a), "+v"(b)); }
__device__ __forceinline__ void vswap(float& a, float& b) { asm volatile("v_swap_b32 %0, %1" : "+v"(a), "+v"(b)); }
template <typename V, int N> __device__ __forceinline__ void vswapn(V& a, V& b) {
#pragma unroll
  for (int i = 0; i < N; ++i) { auto x = a[i], y = b[i]; vswap(x, y); a[i] = x; b[i] = y; }
}
__device__ __forceinline__ u32x4 ld16(rsrc_t rs, unsigned voff, unsigned soff) { return __builtin_amdgcn_raw_buffer_load_b128(rs, voff, soff, SC1); }
template <int OFF> __device__ __forceinline__ void ld16h(u32x4& d, rsrc_t rs, unsigned voff, unsigned soff) {
  asm volatile("buffer_load_dwordx4 %0, %1, %2, %3 offen offset:%4 sc1" : "=v"(d) : "v"(voff), "s"(rs), "s"(soff), "n"(OFF) : "memory");
}
template <int I, int OFF> __device__ __forceinline__ void ld16w(u32x4& d, rsrc_t rs, unsigned voff, unsigned soff) {
  if constexpr (I == 0) asm volatile("buffer_load_dwordx4 %0, %1, %2, %3 offen offset:%4 sc1" : "={v[128:131]}"(d) : "v"(voff), "s"(rs), "s"(soff), "n"(OFF) : "memory");
  else if constexpr (I == 1) asm volatile("buffer_load_dwordx4 %0, %1, %2, %3 offen offset:%4 sc1" : "={v[132:135]}"(d) : "v"(voff), "s"(rs), "s"(soff), "n"(OFF) : "memory");
  else if constexpr (I == 2) asm volatile("buffer_load_dwordx4 %0, %1, %2, %3 offen offset:%4 sc1" : "={v[136:139]}"(d) : "v"(voff), "s"(rs), "s"(soff), "n"(OFF) : "memory");
  else if constexpr (I == 3) asm volatile("buffer_load_dwordx4 %0, %1, %2, %3 offen offset:%4 sc1" : "={v[140:143]}"(d) : "v"(voff), "s"(rs), "s"(soff), "n"(OFF) : "memory");
  else if constexpr (I == 4) asm volatile("buffer_load_dwordx4 %0, %1, %2, %3 offen offset:%4 sc1" : "={v[144:147]}"(d) : "v"(voff), "s"(rs), "s"(soff), "n"(OFF) : "memory");
  else if constexpr (I == 5) asm volatile("buffer_load_dwordx4 %0, %1, %2, %3 offen offset:%4 sc1" : "={v[148:151]}"(d) : "v"(voff), "s"(rs), "s"(soff), "n"(OFF) : "memory");
  else if constexpr (I == 6) asm volatile("buffer_load_dwordx4 %0, %1, %2, %3 offen offset:%4 sc1" : "={v[152:155]}"(d) : "v"(voff), "s"(rs), "s"(soff), "n"(OFF) : "memory");
  else if constexpr (I == 7) asm volatile("buffer_load_dwordx4 %0, %1, %2, %3 offen offset:%4 sc1" : "={v[156:159]}"(d) : "v"(voff), "s"(rs), "s"(soff), "n"(OFF) : "memory");
  else if constexpr (I == 8) asm volatile("buffer_load_dwordx4 %0, %1, %2, %3 offen offset:%4 sc1" : "={v[160:163]}"(d) : "v"(voff), "s"(rs), "s"(soff), "n"(OFF) : "memory");
  else if constexpr (I == 9) asm volatile("buffer_load_dwordx4 %0, %1, %2, %3 offen offset:%4 sc1" : "={v[164:167]}"(d) : "v"(voff), "s"(rs), "s"(soff), "n"(OFF) : "memory");
  else if constexpr (I == 10) asm volatile("buffer_load_dwordx4 %0, %1, %2, %3 offen offset:%4 sc1" : "={v[168:171]}"(d) : "v"(voff), "s"(rs), "s"(soff), "n"(OFF) : "memory");
  else if constexpr (I == 11) asm volatile("buffer_load_dwordx4 %0, %1, %2, %3 offen offset:%4 sc1" : "={v[172:175]}"(d) : "v"(voff), "s"(rs), "s"(soff), "n"(OFF) : "memory");
}
template <int K> __device__ __forceinline__ void pin_w(u32x4& a, u32x4& b) {
  if constexpr (K == 0) asm volatile("" : "+{v[128:131]}"(a), "+{v[132:135]}"(b)::"memory");
  else if constexpr (K == 1) asm volatile("" : "+{v[136:139]}"(a), "+{v[140:143]}"(b)::"memory");
  else if constexpr (K == 2) asm volatile("" : "+{v[144:147]}"(a), "+{v[148:151]}"(b)::"memory");
  else if constexpr (K == 3) asm volatile("" : "+{v[152:155]}"(a), "+{v[156:159]}"(b)::"memory");
  else if constexpr (K == 4) asm volatile("" : "+{v[160:163]}"(a), "+{v[164:167]}"(b)::"memory");
  else if constexpr (K == 5) asm volatile("" : "+{v[168:171]}"(a), "+{v[172:175]}"(b)::"memory");
}
__device__ __forceinline__ void ld8h(u32x2& d, rsrc_t rs, unsigned voff, unsigned soff) {
  asm volatile("buffer_load_dwordx2 %0, %1, %2, %3 offen sc1" : "=v"(d) : "v"(voff), "s"(rs), "s"(soff) : "memory");
}
__device__ __forceinline__ void ld16u(u32x4& d, rsrc_t rs, unsigned voff, unsigned soff) {
  asm volatile("buffer_load_dwordx4 %0, %1, %2, %3 offen sc1" : "+v"(d) : "v"(voff), "s"(rs), "s"(soff) : "memory");
}
template <int OFF> __device__ __forceinline__ void ld4h(unsigned& d, rsrc_t rs, unsigned voff, unsigned soff) {
  asm volatile("buffer_load_dword %0, %1, %2, %3 offen offset:%4 sc1" : "=v"(d) : "v"(voff), "s"(rs), "s"(soff), "n"(OFF) : "memory");
}
__device__ __forceinline__ void ld4x16(u32x4 (&d)[4], rsrc_t rs, unsigned voff, unsigned soff) {
  asm volatile("buffer_load_dwordx4 %0, %4, %5, %6 offen sc1\n\tbuffer_load_dwordx4 %1, %4, %5, %6 offen offset:64 sc1\n\t"
               "buffer_load_dwordx4 %2, %4, %5, %6 offen offset:128 sc1\n\tbuffer_load_dwordx4 %3, %4, %5, %6 offen offset:192 sc1\n\ts_waitcnt vmcnt(0)"
               : "=&v"(d[0]), "=&v"(d[1]), "=&v"(d[2]), "=&v"(d[3]) : "v"(voff), "s"(rs), "s"(soff) : "memory");
}
__device__ __forceinline__ u32x2 ld8(rsrc_t rs, unsigned voff, unsigned soff) { return __builtin_amdgcn_raw_buffer_load_b64(rs, voff, soff, SC1); }
__device__ __forceinline__ void st16(u32x4 v, rsrc_t rs, unsigned voff, unsigned soff) { __builtin_amdgcn_raw_buffer_store_b128(v, rs, voff + soff, 0, SC1); }
__device__ __forceinline__ void st8(u32x2 v, rsrc_t rs, unsigned voff, unsigned soff) { __builtin_amdgcn_raw_buffer_store_b64(v, rs, voff, soff, SC1); }

__device__ __forceinline__ unsigned ld4(rsrc_t rs, unsigned voff, unsigned soff) { return __builtin_amdgcn_raw_buffer_load_b32(rs, voff, soff, SC1); }
__device__ __forceinline__ void st4(unsigned v, rsrc_t rs, unsigned voff, unsigned soff) { __builtin_amdgcn_raw_buffer_store_b32(v, rs, voff, soff, SC1); }

constexpr unsigned kSpinCap = 1u << 16;
__device__ __forceinline__ bool spin_fail(unsigned& spins, rsrc_t rs, u32x2 T) {
  asm volatile("" ::: "memory");
  ++spins;
  if (spins > kSpinCap) { st8(T, rs, 0, F_TMO); return true; }
  if ((spins & 255u) == 0u) {
    const u32x2 v = ld8(rs, 0, F_TMO);
    if (v[0] == T[0] && v[1] == T[1]) return true;
  }
  __builtin_amdgcn_s_sleep(3);
  return false;
}
__device__ __forceinline__ void poll_flags2(rsrc_t rs, unsigned voff, u32x2 T) {
  for (unsigned spins = 0;;) {
    const u32x4 v = ld16(rs, voff, 0);
    if (__all(v[0] == T[0] && v[1] == T[1] && v[2] == T[0] && v[3] == T[1])) break;
    if (spin_fail(spins, rs, T)) break;
  }
  asm volatile("" ::: "memory");
  __builtin_amdgcn_fence(__ATOMIC_ACQUIRE, "wavefront");
}
__device__ __forceinline__ void poll_flags(rsrc_t rs, unsigned voff, unsigned soff, u32x2 T) {
  for (unsigned spins = 0;;) {
    const u32x2 v = ld8(rs, voff, soff);
    if (__all(v[0] == T[0] && v[1] == T[1])) break;
    if (spin_fail(spins, rs, T)) break;
  }
  asm volatile("" ::: "memory");
  __builtin_amdgcn_fence(__ATOMIC_ACQUIRE, "wavefront");
}

__device__ __forceinline__ unsigned toff(unsigned R, unsigned ks, unsigned g) {
  return (((R >> 5) * 64 + ks * 2 + ((R >> 4) & 1)) << 10) + ((g * 16 + (R & 15)) << 4);
}
__device__ __forceinline__ void unpack8v(const u32x4 u, f32x2* v) {
#pragma unroll
  for (int j = 0; j < 4; ++j) v[j] = f32x2{lo16(u[j]), hi16(u[j])};
}
__device__ __forceinline__ u32x4 pack8v(const f32x2* v) {
  u32x4 o;
#pragma unroll
  for (int j = 0; j < 4; ++j) o[j] = pack2(v[j][0], v[j][1]);
  return o;
}
__device__ __forceinline__ f32x2 rbf1v(f32x2 v) { return f32x2{rbf1(v[0]), rbf1(v[1])}; }
__device__ __forceinline__ float ln_rd(float var) { return __builtin_amdgcn_rcpf(rbf1(__builtin_amdgcn_sqrtf(rbf1(rbf1(var) + 1e-5f)))); }
template <int N> __device__ __forceinline__ void ln_apply(float mb, float rd, const f32x2* v, f32x2* y) {
  const f32x2 mb2 = {mb, mb}, rd2 = {rd, rd};
#pragma unroll
  for (int j = 0; j < N; ++j) y[j] = rbf1v(rbf1v(v[j] - mb2) * rd2);
}
__device__ __forceinline__ float hadd(f32x2 v) {
  float a = v[0];
  asm volatile("" : "+v"(a));
  return a + v[1];
}
__device__ __forceinline__ float add_g(float v) { return add_x32(add_x16(v)); }
__device__ __forceinline__ void add_g3(float& x, float& y, float& z) {
  auto a = __builtin_amdgcn_permlane16_swap(__builtin_bit_cast(unsigned, x), __builtin_bit_cast(unsigned, y), false, false);
  auto b = __builtin_amdgcn_permlane16_swap(__builtin_bit_cast(unsigned, z), __builtin_bit_cast(unsigned, z), false, false);
  const float r = asf(a[0]) + asf(a[1]), t = asf(b[0]) + asf(b[1]);
  auto c = __builtin_amdgcn_permlane32_swap(__builtin_bit_cast(unsigned, r), __builtin_bit_cast(unsigned, r), false, false);
  auto d = __builtin_amdgcn_permlane32_swap(__builtin_bit_cast(unsigned, t), __builtin_bit_cast(unsigned, t), false, false);
  const float u = asf(c[0]) + asf(c[1]);
  z = asf(d[0]) + asf(d[1]);
  auto e = __builtin_amdgcn_permlane16_swap(__builtin_bit_cast(unsigned, u), __builtin_bit_cast(unsigned, u), false, false);
  x = asf(e[0]); y = asf(e[1]);
}
__device__ __forceinline__ u32x4 trade16(u32x4 v) {
  auto a = __builtin_amdgcn_permlane16_swap(v[0], v[2], false, false);
  auto b = __builtin_amdgcn_permlane16_swap(v[1], v[3], false, false);
  return u32x4{(unsigned)a[0], (unsigned)b[0], (unsigned)a[1], (unsigned)b[1]};
}
__device__ __forceinline__ unsigned umax4(u32x4 v) { return max(max(v[0], v[1]), max(v[2], v[3])); }
__device__ __forceinline__ u32x2 unpoison2(u32x2 v) { return u32x2{min(v[0], POISON - 1), min(v[1], POISON - 1)}; }
__device__ __forceinline__ u32x4 unpoison4(u32x4 v) {
  return u32x4{min(v[0], POISON - 1), min(v[1], POISON - 1), min(v[2], POISON - 1), min(v[3], POISON - 1)};
}

typedef union { struct { s16x4 a, b; } hf; u32x4 v; } frag_t;
__device__ __forceinline__ s16x4 trread(const char* smem, unsigned off) {
  return __builtin_amdgcn_ds_read_tr16_b64_v4i16((LDS_AS s16x4*)(smem + off));
}
__device__ __forceinline__ s16x4 trabs(unsigned addr) { return __builtin_amdgcn_ds_read_tr16_b64_v4i16((LDS_AS s16x4*)(size_t)addr); }

constexpr int WA = 6;
#define WRV 3
#define WBV 4
constexpr int WB = WBV, WR = WRV;
#define CH 0, 4, 7, 8, 13, 15, 20, 22, 27, 29
constexpr int CHV[10] = {CH};
constexpr int FSL = CHV[1], FS0 = CHV[2], FS0D = CHV[3], FS1 = CHV[4], FS1S = CHV[5];
constexpr int FXA = CHV[6], FXB = CHV[7], FXL = CHV[8], FXS = CHV[9];
constexpr int FXT = FXB >> 2;
#define C0 27, 3, 16, 19, 22, 23, -1, 5, 4
constexpr int C0V[9] = {C0};
constexpr int IMG = C0V[0], CSD = C0V[1];
constexpr int ERQ = C0V[2], CSL = C0V[3], CSS = C0V[4];
constexpr int RWL = C0V[6], RWS = C0V[7], POI = C0V[8];
#define WTS 99
#define WTG 2
#define PBS 10
#define KP0 26
#define PS0 8
constexpr int EIN = cmax(CSD, cmax(RWS, POI)) + 3;
constexpr int ABS = 27;
constexpr int abl(int it) { return it ? 23 : C0V[5]; }
constexpr int XU = 3, XL = 2;
constexpr int x_slot(int u) { return u % XU == 0 ? 0 : u % XU == 1 ? 32768 : 49152; }
#define TCH 19, 7, 4, 27
constexpr int TCV[4] = {TCH};
constexpr int TS0 = TCV[0], TSN = TCV[1], TW = TCV[2], TST = TCV[3];
constexpr int QL = 26;
#define FDG 0
#define XSL 4
#define FW0 4
#define FW1 4
#define X1F 1
#define RWG 0
#define CSG 4
#define RXCD 5
#define TWR TW
#define WDS (17 - WB)
#define WDW 1
#define SDI0 16
#define SLP 24, 14, 56, 3
constexpr int SLV[4] = {SLP};
constexpr int SDIR = SLV[0], GSL = SLV[1], RSL = SLV[2], KSL = SLV[3];
constexpr bool tile_piece(int s) { return s >= 3 && ((s - 3) >> 2) + 3 < 8 && ((s - 3) & 3) < 3; }
constexpr int a_req(int s, int n) {
  if (!(n & 1)) return -1;
  if (s <= 1) return 8 + 3 * s + (n >> 1);
  return n < 5 && s + 5 < 32 ? 2 * (s + 5) + (n >> 1) : -1;
}
constexpr int vm_n(int it, int s, int n) {
  int c = 0;
  if (n == 0) c += tile_piece(s);
  if (n == 4) c += (s == abl(it)) + (it ? (s == FSL ? 5 : 0) : (s == ERQ) + (s >= IMG && s < IMG + 3));
  if (!it) c += (s == WTS && n == WTG) + (s < 4 && n == 2) + (s == 0 && (n == 0 || n == 4)) + ((n == 3 || n == 5) && s + XL + XU < 32);
  else c += (a_req(s, n) >= 0) + ((s == 0 || s == 4) && n == 2);
  return c;
}
constexpr int vm_at(int it, int s, int n) {
  int c = it ? 8 : 14;
  for (int u = 0; u < 6 * s + n; ++u) c += vm_n(it, u / 6, u % 6);
  return c;
}
constexpr int vm_a(int it, int k) {
  if (k < 4) return 2 * k + 2;
  for (int u = 0; u < 192; ++u)
    if (a_req(u / 6, u % 6) == 2 * k + 1) return vm_at(it, (u + 1) / 6, (u + 1) % 6);
  return 0;
}
constexpr int vm_tile(int it, int t) { return t >= 3 ? vm_at(it, 4 * t - 7, 1) : t == 1 ? vm_at(0, 0, 5) : vm_at(0, 3, 3); }
constexpr int vm_x(int u) { return u == 2 ? 9 : u < XU + XL ? 12 + 2 * (u - XU) : vm_at(0, u - XU - XL + 1, 0); }

constexpr int wo_k(int it, int s) { return it || s < 4 ? 15 - s : s < 8 ? 7 - s : 19 - s; }
constexpr int wo_pos(int it, int s) { return 2 * s + 2 + (s >= WR ? 2 * it : 0); }
constexpr int wo_at(int it, int s) { return 2 * it + (s < WB - WR ? 2 * WR + 2 + 4 * s : s < 16 - WB ? 2 * (2 * WB - WR) + 2 * (s - WB + WR) : 32); }
template <int OFF> __device__ __forceinline__ void lds_count(unsigned ad) {
  asm volatile("s_mov_b64 exec, 1\n\tds_add_u32 %0, %1 offset:%2\n\ts_mov_b64 exec, -1" ::"v"(ad), "v"(1u), "n"(OFF) : "memory");
}
__device__ __forceinline__ void lds_look(char* c, unsigned want) {
  while ((unsigned)__builtin_amdgcn_readfirstlane(__hip_atomic_load((LDS_AS unsigned*)c, __ATOMIC_RELAXED, __HIP_MEMORY_SCOPE_WORKGROUP)) < want) {}
  asm volatile("" ::: "memory");
}
__device__ __forceinline__ void pin1(unsigned& a) { asm volatile("" : "+v"(a)); }
__device__ __forceinline__ void pin2(u32x4& a, u32x4& b) { asm volatile("" : "+v"(a), "+v"(b)::"memory"); }
__device__ __forceinline__ void pin2s(float& a, float& b) { asm volatile("" : "+v"(a), "+v"(b)); }
template <int OFF> __device__ __forceinline__ void lds2(u32x4& a, u32x4& b, unsigned ad) {
  asm volatile("ds_read_b128 %0, %2 offset:%3\n\tds_read_b128 %1, %2 offset:%4" : "=&v"(a), "=&v"(b) : "v"(ad), "n"(OFF), "n"(OFF + 1024) : "memory");
}
template <int N> __device__ __forceinline__ void lds_wait(u32x4& a, u32x4& b) { asm volatile("s_waitcnt lgkmcnt(%2)" : "+v"(a), "+v"(b) : "n"(N) : "memory"); }
__device__ __forceinline__ void pin2f(f32x2& a, f32x2& b) { asm volatile("" : "+v"(a), "+v"(b)); }
__device__ __forceinline__ void pin_ac(f32x4 (&ac)[2][4]) {
  asm volatile("" : "+{v[176:179]}"(ac[0][0]), "+{v[180:183]}"(ac[0][1]), "+{v[184:187]}"(ac[0][2]), "+{v[188:191]}"(ac[0][3]),
                    "+{v[192:195]}"(ac[1][0]), "+{v[196:199]}"(ac[1][1]), "+{v[200:203]}"(ac[1][2]), "+{v[204:207]}"(ac[1][3]));
}

constexpr int L_XC = LDS_BYTES - 64, L_SKEY = 32704, L_SRED = 4096, L_SFIN = 20480;
__device__ __forceinline__ unsigned key_load(rsrc_t rs, int l, int L, int rep) { return ld4(rs, (l & 15) * SK, OFF_KEY + (L * 16 + rep) * cmax(16 * SK, 128)); }
__device__ __forceinline__ unsigned umax16(unsigned k) {
  k = max(k, dppu<0xB1>(k));
  k = max(k, dppu<0x4E>(k));
  k = max(k, dppu<0x141>(k));
  return max(k, dppu<0x140>(k));
}
__device__ __forceinline__ unsigned key_row(unsigned key) { return (0xffffu - (key & 0xffffu)) & 2047u; }
__device__ __forceinline__ unsigned key_wait(rsrc_t rs, u32x2 T, int l, int L, int rep, unsigned kv) {
  if (!__all(kv != POISON)) {
    unsigned spins = 0;
    do {
      if (spin_fail(spins, rs, T)) break;
      if (L == 0) __builtin_amdgcn_s_sleep(KSL);
      kv = key_load(rs, l, L, rep);
    } while (!__all(kv != POISON));
  }
  return __builtin_amdgcn_readfirstlane(umax16(kv));
}
__device__ __forceinline__ unsigned ffn_xoff(unsigned key, int L, int w) { return OFF_CA + L * 32768 + (key_row(key) >> 7) * 2048 + w * 256; }
__device__ __forceinline__ void row_load(rsrc_t rs, int g, unsigned xo, u32x4 (&xv)[4]) {
  ld16h<0>(xv[0], rs, g * 16, xo); ld16h<64>(xv[1], rs, g * 16, xo); ld16h<128>(xv[2], rs, g * 16, xo); ld16h<192>(xv[3], rs, g * 16, xo);
}
template <int SL = 0> __device__ __forceinline__ void ffn_xwait(rsrc_t rs, u32x2 T, unsigned xo, int l, u32x4 (&xv)[4]) {
#define XV_OK() __all(max(max(umax4(xv[0]), umax4(xv[1])), max(umax4(xv[2]), umax4(xv[3]))) != POISON)
  if (!XV_OK()) {
    unsigned spins = 0;
    do {
      if (spin_fail(spins, rs, T)) break;
      if (SL) __builtin_amdgcn_s_sleep(SL);
#pragma unroll
      for (int i = 0; i < 4; ++i) xv[i] = ld16(rs, (l >> 4) * 16 + i * 64, xo);
    } while (!XV_OK());
  }
#undef XV_OK
}
__device__ __forceinline__ void ffn_gu(const u32x4 (&gv)[4], const u32x4 (&uv)[4], const u32x4 (&xv)[4], f32x4& ga, f32x4& ua) {
  ga = mfma16(gv[0], xv[0], f32x4{0.f, 0.f, 0.f, 0.f}); ua = mfma16(uv[0], xv[0], f32x4{0.f, 0.f, 0.f, 0.f});
#pragma unroll
  for (int i = 1; i < 4; ++i) { ga = mfma16(gv[i], xv[i], ga); ua = mfma16(uv[i], xv[i], ua); }
}
__device__ __forceinline__ void ffn_gu_check(rsrc_t rs, u32x2 T, unsigned xo, int l, const u32x4 (&gv)[4], const u32x4 (&uv)[4], u32x4 (&xv)[4], f32x4& ga, f32x4& ua) {
  if (__builtin_expect(__any(ga[0] != ga[0]), 0)) {
    ffn_xwait(rs, T, xo, l, xv);
    ffn_gu(gv, uv, xv, ga, ua);
  }
}
__device__ __forceinline__ void ffn_gu_store(char* smem, int l, int w, f32x4 ga, f32x4 ua) {
  LDS_AS f32x4* sred = (LDS_AS f32x4*)(smem + L_SRED);
  if ((l & 15) == 0) { sred[w * 8 + (l >> 4)] = ga; sred[w * 8 + 4 + (l >> 4)] = ua; }
}
__device__ __forceinline__ f32x4 ffn_t_read(char* smem, int l) { return ((const LDS_AS f32x4*)(smem + L_SRED))[(l & 7) * 8 + ((l >> 3) & 1) * 4 + (l >> 4)]; }
__device__ __forceinline__ void ffn_d_read(char* smem, int l, int blk, s16x4 (&dfr)[8]) {
#pragma unroll
  for (int i = 0; i < 8; ++i) dfr[i] = trread(smem, L_DN + blk * 4096 + (l >> 4) * 1024 + ((l & 15) >> 2) * 256 + i * 32 + (l & 3) * 8);
}
__device__ __forceinline__ float ffn_d_sum(float t) { t += dppf<0xB1>(t); t += dppf<0x4E>(t); return t + dppf<0x141>(t); }
__device__ __forceinline__ u32x2 ffn_h(f32x4 t, int l) {
#pragma unroll
  for (int e = 0; e < 4; ++e) t[e] = ffn_d_sum(t[e]);
  const float a = (l & 1) ? t[1] : t[0], b = (l & 1) ? t[3] : t[2], s = (l & 2) ? b : a;
  const float gg = rbf1(s), uu = rbf1(dppf<0x128>(s));
  const float h = rbf1(gg * __builtin_amdgcn_rcpf(1.0f + __expf(-gg))) * uu;
  const unsigned h0 = pack2(h, dppf<0x55>(h)), h1 = pack2(dppf<0xAA>(h), dppf<0xFF>(h));
  return (l & 15) == 0 ? u32x2{h0, h1} : u32x2{0u, 0u};
}
__device__ __forceinline__ void ffn_d_mma(const s16x4 (&dfr)[8], u32x2& hh, f32x4& ya, f32x4& yb) {
#pragma unroll
  for (int i = 0; i < 8; ++i) {
    if (i & 1) yb = mfma16k16(dfr[i], __builtin_bit_cast(s16x4, hh), yb);
    else ya = mfma16k16(dfr[i], __builtin_bit_cast(s16x4, hh), ya);
    hh = u32x2{dppu<0x111>(hh[0]), dppu<0x111>(hh[1])};
  }
}
__device__ __forceinline__ void ffn_d_store(rsrc_t rs, int l, int G0, int L, int slice, f32x4 y) {
  const unsigned c = l & 15;
  st16(unpoison4(__builtin_bit_cast(u32x4, y)), rs, (G0 + (c >> 1)) * 32768 + slice * 128 + (c & 1) * 64 + (l >> 4) * 16, OFF_PART + L * (1u << 20));
}
__device__ __forceinline__ void red_load(rsrc_t rs, int tid, int L, unsigned G, unsigned key, u32x4 (&pv)[4], u32x2& cxn, u32x2& cx1) {
#pragma unroll
  for (int k = 0; k < 4; ++k) pv[k] = ld16(rs, min(tid + 512 * k, 1791) * 16, OFF_PART + L * (1u << 20) + G * 32768);
  cxn = ld8(rs, (tid & 3) * 16 + (tid & 4) * 2, OFF_CA + L * 32768 + (key_row(key) >> 7) * 2048 + G * 64);
  cx1 = ld8(rs, (tid & 3) * 16 + (tid & 4) * 2, OFF_CB + L * 32768 + (key_row(key) >> 7) * 2048 + G * 64);
}
__device__ __forceinline__ void red_sum(rsrc_t rs, char* smem, u32x2 T, int tid, int w, int L, unsigned G, unsigned key,
                                        u32x4 (&pv)[4], u32x2& cxn, u32x2& cx1) {
#define PV_OK() __all(max(max(max(umax4(pv[0]), umax4(pv[1])), max(umax4(pv[2]), umax4(pv[3]))), max(max(cxn[0], cxn[1]), max(cx1[0], cx1[1]))) != POISON)
  if (!PV_OK()) {
    unsigned spins = 0;
    do {
      if (spin_fail(spins, rs, T)) break;
      red_load(rs, tid, L, G, key, pv, cxn, cx1);
    } while (!PV_OK());
  }
#undef PV_OK
  f32x4 s = (__builtin_bit_cast(f32x4, pv[0]) + __builtin_bit_cast(f32x4, pv[1])) + __builtin_bit_cast(f32x4, pv[2]);
  if (w < 4) s += __builtin_bit_cast(f32x4, pv[3]);
  ((LDS_AS f32x4*)(smem + L_SFIN))[tid] = s;
}
template <int NW> __device__ __forceinline__ u32x2 red_final(char* smem, int l, unsigned key, u32x2 cxn, u32x2 cx1) {
  const LDS_AS f32x4* sf = (const LDS_AS f32x4*)(smem + L_SFIN) + l;
  f32x4 s = (sf[0] + sf[64]) + (sf[128] + sf[192]);
  if constexpr (NW == 8) s += (sf[256] + sf[320]) + (sf[384] + sf[448]);
#pragma unroll
  for (int j = 0; j < 4; ++j) s[j] = sum_hi8(s[j]);
  const unsigned k16 = key >> 16;
  const float rmax = __builtin_bit_cast(float, ((k16 & 0x8000u) ? (k16 & 0x7fffu) : (~k16 & 0xffffu)) << 16);
  const float sig = rbf1(__builtin_amdgcn_rcpf(1.0f + __expf(-rmax)));
  const float xn4[4] = {lo16(cxn[0]), hi16(cxn[0]), lo16(cxn[1]), hi16(cxn[1])};
  const float x14[4] = {lo16(cx1[0]), hi16(cx1[0]), lo16(cx1[1]), hi16(cx1[1])};
  float o[4];
#pragma unroll
  for (int j = 0; j < 4; ++j) o[j] = x14[j] + rbf1(xn4[j] + rbf1(rbf1(s[j]) * sig));
  return u32x2{pack2(o[0], o[1]), pack2(o[2], o[3])};
}

struct RowFin { float mean, var, mb, rd; unsigned key; };
__device__ __forceinline__ void fin_wait(rsrc_t rs, u32x2 T, unsigned voff, unsigned so, u32x4 (&sp)[4]) {
#define ST_OK() __all(max(max(umax4(sp[0]), umax4(sp[1])), max(umax4(sp[2]), umax4(sp[3]))) != POISON)
  if (!ST_OK()) {
    unsigned spins = 0;
    do {
      if (spin_fail(spins, rs, T)) break;
#pragma unroll
      for (int i = 0; i < 4; ++i) sp[i] = ld16(rs, voff + i * 2048, so);
    } while (!ST_OK());
  }
#undef ST_OK
}
__device__ __forceinline__ RowFin fin_key(rsrc_t rs, u32x2 T, unsigned voff, unsigned so, char* skey, int l, int w, unsigned R, float wsum, u32x4 (&sp)[4]) {
  RowFin r;
#define ST_SUM() ((__builtin_bit_cast(f32x4, sp[0]) + __builtin_bit_cast(f32x4, sp[1])) + (__builtin_bit_cast(f32x4, sp[2]) + __builtin_bit_cast(f32x4, sp[3])))
  f32x4 sf = ST_SUM();
  const float z = (sf[0] + sf[1]) + sf[2];
  if (__any(z != z)) {
    fin_wait(rs, T, voff, so, sp);
    sf = ST_SUM();
  }
#undef ST_SUM
  float s0 = sf[0], s1 = sf[1], s2 = sf[2];
  add_g3(s0, s1, s2);
  r.mean = s0 * (1.0f / 1024.0f);
  r.var = fmaxf(__builtin_fmaf(-r.mean, r.mean, s1 * (1.0f / 1024.0f)), 0.f);
  r.mb = rbf1(r.mean); r.rd = ln_rd(r.var);
  const unsigned rb16 = pack2((s2 - r.mb * wsum) * r.rd, 0.f) & 0xffffu;
  r.key = min((((rb16 & 0x8000u) ? (~rb16 & 0xffffu) : (rb16 | 0x8000u)) << 16) | (0xffffu - R), POISON - 1);
  const unsigned wk = umax16(r.key);
  if (l == 0) ((LDS_AS unsigned*)skey)[w] = wk;
  return r;
}
__device__ __forceinline__ unsigned fin_cand(rsrc_t rs, char* skey, int L, int g, int rb, int ct, const RowFin& r, const f32x2 (&v)[8]) {
  const u32x4 k0 = *(const LDS_AS u32x4*)skey, k1 = *(const LDS_AS u32x4*)(skey + 16);
  const unsigned best = max(umax4(k0), umax4(k1));
  if (r.key == best) {
    const unsigned co = L * 32768 + rb * 2048 + ct * 128 + g * 16;
    f32x2 t[8], rd2 = {r.rd, r.rd}, of2 = {-r.mb * r.rd, -r.mb * r.rd};
    asm volatile("" : "+v"(rd2), "+v"(of2));
#pragma unroll
    for (int j = 0; j < 8; ++j) t[j] = __builtin_elementwise_fma(v[j], rd2, of2);
    st16(unpoison4(pack8v(t)), rs, co, OFF_CA); st16(unpoison4(pack8v(t + 4)), rs, co + 64, OFF_CA);
    if (g == 0) st4(best, rs, OFF_KEY + (L * 16 + ct) * cmax(16 * SK, 128) + rb * SK, 0);
    st16(unpoison4(pack8v(v)), rs, co, OFF_CB); st16(unpoison4(pack8v(v + 4)), rs, co + 64, OFF_CB);
  }
  return best;
}
__device__ __forceinline__ void fin_x2(const RowFin& r, f32x2 (&v)[8]) {
  f32x2 sc2 = {1.0f + r.rd, 1.0f + r.rd}, of2 = {-r.mb * r.rd, -r.mb * r.rd};
  asm volatile("" : "+v"(sc2), "+v"(of2));
#pragma unroll
  for (int j = 0; j < 8; ++j) v[j] = __builtin_elementwise_fma(v[j], sc2, of2);
}

extern "C" __device__ unsigned long long dispatch_id() __asm("llvm.amdgcn.dispatch.id");

extern "C" __global__ __launch_bounds__(512, 2) void mega_kernel(const bf16* x, char* ws, const bf16* wqkv, const bf16* wo, const bf16* wr,
                                                                 const bf16* wg, const bf16* wu, const bf16* wd, bf16* out) {
  extern __shared__ __attribute__((aligned(16))) char smem[];
  const int w0_ = __builtin_amdgcn_readfirstlane(threadIdx.x >> 6);
  const unsigned lbase = (unsigned)(size_t)(LDS_AS char*)smem;
  {
    unsigned long long pc;
    unsigned len;
    asm volatile("s_getpc_b64 %0\n\ts_mov_b32 %1, .Ltext_end-mega_kernel\n\t.subsection 1\n.Ltext_end:\n\t.subsection 0" : "=s"(pc), "=s"(len));
    const unsigned o = ((blockIdx.x >> 3) * 64 + (threadIdx.x & 63)) * 128;
    if (w0_ == 7 && o < len)
      asm volatile("s_mov_b32 m0, %0\n\ts_nop 0\n\tglobal_load_lds_dword %1, %2" ::"s"(lbase + L_TOUCH0), "v"(o), "s"(pc & ~127ull) : "memory");
  }
  asm volatile("" : "+s"(x), "+s"(ws), "+s"(wqkv));
#define HGRP() ((xcd & 2) + (xcd >> 2))
#define IDS()                                                                                         \
  int tid = threadIdx.x, w = w0_, bid = blockIdx.x;                                                   \
  asm volatile("" : "+v"(tid));                                                                       \
  asm volatile("" : "+s"(w));                                                                         \
  asm volatile("" : "+s"(bid));                                                                       \
  const int l = tid & 63, xcd = bid & 7, bi = bid >> 3;                                               \
  const int p = (xcd & 1) * 4 + (bi & 3), h = HGRP() * 8 + (bi >> 2);         \
  const int rb = 2 * xcd + (bi & 1), ct = bi >> 1;                               \
  const int slice = (xcd - (xcd > RXCD)) * 32 + bi;                                            \
  const bool ffn_on = xcd != RXCD;                                                                    \
  const int g = l >> 4;                                                                               \
  const int rt = w < 4 ? w : 11 - w;          \
  KARGS();                                                                                            \
  (void)p; (void)h; (void)rb; (void)ct; (void)slice; (void)ffn_on; (void)g; (void)rt;
#define PROT() (2 * (p & 3) + (p >> 2))
  const rsrc_t rs = __builtin_amdgcn_make_buffer_rsrc((void*)ws, 0, WS_BYTES, 0x00020000);
  typedef const void* const __attribute__((address_space(4)))* karg_t;
#define KARGS() karg_t ka = (karg_t)__builtin_amdgcn_kernarg_segment_ptr(); asm volatile("" : "+s"(ka))
#define ARGP(name, i) const GAS bf16* name = (const GAS bf16*)ka[i]; asm volatile("" : "+s"(name))
#define RSRC(ptr, bytes) __builtin_amdgcn_make_buffer_rsrc((void*)(ptr), 0, bytes, 0x00020000)
#define rwq RSRC(pwq, 1024 * 3072 * 2)
#define rwo RSRC(pwo, 1024 * 1024 * 2)
#define rwg RSRC(pwg, 1024 * F_ * 2)
#define rwu RSRC(pwu, 1024 * F_ * 2)
#define rwd RSRC(pwd, 1024 * F_ * 2)
#define rout RSRC(pout, 2048 * 1024 * 2)

#define WQ_VOFF() ((l >> 2) * 6144 + (((l & 3) ^ ((((l >> 4) ^ (l >> 5)) & 1) << 1)) << 4))
#define WQ_PIECE(t, j, dst) dma16(rwq, wqv, ((((t) + rot) & 7) * 128 + ((j) & 7) * 16) * 6144 + ((j) >> 3) * 2048 + h * 64, lbase + (dst) + (j) * 1024)
#define WQ_BOFF(dt) ((g >> 1) * 1024 + (8 * (g & 1) + ((l & 15) >> 2)) * 64 + (l & 3) * 8 + (((dt) ^ (g & 1)) << 5))
#define X_REQ(u, hf) dma16(rx, xv[hf], (p * 256 + rt * 32 + ((u) & 1) * 16 + (hf) * 8) * 2048 + ((((u) >> 2) + rot) & 7) * 256 + (((u) >> 1) & 1) * 128, lbase + rt * 2048 + x_slot(u) + (hf) * 1024)
#define WO_PIECE(j) dma16(rwo, wov, ((w >> 2) * 64 + (w & 3) + 4 * (j)) * 16384 + ct * 128, lbase + 32768 + (j) * 8192 + w * 1024)
#define WD_DMA_OF(v) _Pragma("unroll") for (int i = 0; i < 4; ++i)                                  \
      dma16(rwd, (l >> 4) * 2048 + (l & 15) * 16, (unsigned)(slice * 16 + 4 * i) * 2048 + (v) * 256, lbase + L_DN + (v) * 4096 + i * 1024)
#define FFN_DMA(m) dma16((m) & 1 ? rwu : rwg, fnv, (unsigned)(((4 * w + ((m) >> 1)) * 32) * F_ + slice * 16) * 2, lbase + 65536 + (m) * 8192 + w * 1024)
#define POISON_BIG(L)                                                                                \
  if (w >= 2) {                                                                                      \
    unsigned pz1 = POISON;                                                                           \
    asm volatile("" : "+v"(pz1));                                                                    \
    st16(u32x4{pz1, pz1, pz1, pz1}, rs, (w >= 4 ? OFF_PART + (L) * (1u << 20) + bid * 4096 - 4096 : OFF_STAT + (L) * 524288 + (p * 32 + h) * 2048 - 2048) + tid * 16, 0); \
  }
  u32x4 gv[4], uv[4];
  u32x4 keep[9];
  u32x4 pre[4][2];
#define PRE_LOAD(it_, rot_, s0, s1)                                                                  \
  static_for<(s1) - (s0)>([&](auto c_) __attribute__((always_inline)) {                              \
    constexpr int s = (s0) + decltype(c_)::value;                                                    \
    const unsigned ab_ = OFF_XN + (it_) * (4u << 20) + (((rot_) + (s >> 2)) & 7) * 8192 + ((s >> 1) & 1) * 4096; \
    ld16w<2 * s, (s & 1) * 2048>(pre[s][0], rs, l * 16, ab_ + (p * 8 + rt) * 65536);                         \
    ld16w<2 * s + 1, (s & 1) * 2048 + 1024>(pre[s][1], rs, l * 16, ab_ + (p * 8 + rt) * 65536);                 \
  })
  u32x2 T;
  const GAS bf16* tsrc;
  unsigned xv[2], xa[2];
  u32x2 ep;
  u32x4 rows0[4];
#define X_READ(u, d0, d1) { d0 = *(const LDS_AS u32x4*)(size_t)(xa[0] + x_slot(u)); d1 = *(const LDS_AS u32x4*)(size_t)(xa[1] + x_slot(u)); }
  const rsrc_t rx = RSRC(x, 2048 * 1024 * 2);
  {
    IDS();
    const int rot = PROT();
    const bf16* pwq = wqkv;
    const unsigned wqv = WQ_VOFF();
    xv[0] = (l >> 3) * 2048 + (((l & 7) ^ (l >> 4)) << 4); xv[1] = xv[0] ^ 64;
#define WQ_KMAJ(q) (((q) % 3) * 8 + (q) / 3)
    WQ_PIECE(0, WQ_KMAJ(w), L_RING);
    X_REQ(0, 0); X_REQ(0, 1); X_REQ(1, 0); X_REQ(1, 1);
    WQ_PIECE(0, WQ_KMAJ(8 + w), L_RING);
    asm volatile("s_barrier" ::: "memory");
    WQ_PIECE(0, WQ_KMAJ(16 + w), L_RING);
    X_REQ(2, 0); X_REQ(2, 1);
    asm volatile("buffer_load_dwordx2 %0, off, %1, 0 sc1" : "=v"(ep) : "s"(rs) : "memory");
    if constexpr (RWL < 0) {
      asm volatile("" : "=v"(rows0[0]), "=v"(rows0[1]), "=v"(rows0[2]), "=v"(rows0[3]));
      if (w < 4) ldrows(rows0, rx, (l >> 5) * 2048 + (l & 31) * 16, (p * 256 + h * 8 + 2 * w) * 2048);
    }
    if constexpr (PBS < 0) { POISON_BIG(0); }
    {
      ARGP(pwo, 3); ARGP(pwg, 5); ARGP(pwu, 6);
      tsrc = (w < 4 ? pwg : pwu) + ((bi * 32 + ((tid & 255) >> 3)) * F_ + (slice >> 5) * 512 + (tid & 7) * 64);
      if (!ffn_on) tsrc = pwo + (bi * 512 + tid) * 64;
      asm volatile("" : "+v"(tsrc));
    }
#pragma unroll
    for (int kk = 0; kk < 2; ++kk) {
      const int r = l & 15;
      xa[kk] = lbase + rt * 2048 + (r >> 3) * 1024 + (r & 7) * 128 + (((4 * kk + g) ^ (r >> 1)) << 4);
      asm volatile("" : "+v"(xa[kk]));
    }
    if (tid == 192) *(LDS_AS u32x4*)(smem + L_QC) = u32x4{0u, 0u, 0u, 0u};
    wait_vm<4>();
    X_READ(0, pre[0][0], pre[1][0]); X_READ(1, pre[0][1], pre[1][1]);
    asm volatile("s_waitcnt lgkmcnt(0)\n\ts_barrier" : "+v"(pre[0][0]), "+v"(pre[0][1]), "+v"(pre[1][0]), "+v"(pre[1][1])::"memory");
    X_REQ(3, 0); X_REQ(3, 1); X_REQ(4, 0); X_REQ(4, 1);
  }
  unsigned sel0 = 0x7FFFFFFFu;
  u32x4 x2p[2];
  unsigned xm0 = 0, kg0 = 0;

  auto layer = [&](auto it_c) __attribute__((always_inline)) {
    constexpr int it = decltype(it_c)::value;
    f32x4 acc[2][6];
    f32x2 qs2[2];
    u32x4 k0, k1, v0, v1;
    unsigned tdum = 0;
    {
      IDS();
      bool aff = false;
      unsigned aq[2];
      u32x4 av[2];
#define PIN_ACC() asm volatile("" : "+{v[176:179]}"(acc[0][0]), "+{v[180:183]}"(acc[0][1]), "+{v[184:187]}"(acc[0][2]), "+{v[188:191]}"(acc[0][3]), "+{v[192:195]}"(acc[0][4]), "+{v[196:199]}"(acc[0][5]), \
                               "+{v[200:203]}"(acc[1][0]), "+{v[204:207]}"(acc[1][1]), "+{v[208:211]}"(acc[1][2]), "+{v[212:215]}"(acc[1][3]), "+{v[216:219]}"(acc[1][4]), "+{v[220:223]}"(acc[1][5]))
#define PINV4(x) asm volatile("" : "+v"(x[0]), "+v"(x[1]), "+v"(x[2]), "+v"(x[3])::"memory")
      auto qkv_loop = [&](auto it_c) __attribute__((always_inline)) {
        constexpr int IT = decltype(it_c)::value;
        const unsigned abase = OFF_XN + IT * (4u << 20) + (p * 8 + rt) * 65536;
        const int rot = PROT() + 5 * IT;
        unsigned fkey = 0, kv, bq[2];
        u32x4 fx[4], bv[2], af[WA][2];
        u32x2 cxn, cx1;
        f32x4 sa, sb, cs4[2];
        f32x2 b2[2];
#define AB_OFF() (OFF_AB + IT * 16384 + (p * 8 + rt) * 128)
#define A_LOAD1(k, a) ld16w<(k) % WA * 2 + (a), ((k) & 1) * 2048 + (a) * 1024>(af[(k) % WA][a], rs, l * 16, abase + ((((k) >> 2) + rot) & 7) * 8192 + (((k) >> 1) & 1) * 4096)
#pragma unroll
        for (int s = 0; s < 2 + 2 * IT; ++s) { af[s][0] = pre[s][0]; af[s][1] = pre[s][1]; }
        const bf16* pwq = wqkv;
        const int tstep = TS0 + bid % TSN;
        const unsigned wqv = WQ_VOFF();
#define QKV_DMA1(t, i)                                                                            \
  {                                                                                               \
    if constexpr (IT == 0) WQ_PIECE(t, 3 * w + (i), L_RING + SLOT(t) * STAGE);                    \
    else dma16s<(i) * 1024>(rs, l * 16, OFF_WI + (h * 8 + (((t) + rot) & 7)) * STAGE + w * 3072, lbase + L_RING + SLOT(t) * STAGE + w * 3072); \
  }
        unsigned bo[2][2];
#pragma unroll
        for (int dt = 0; dt < 2 - IT; ++dt) {
          bo[dt][0] = lbase + L_RING + (IT ? l * 16 : WQ_BOFF(dt)); bo[dt][1] = bo[dt][0] + 2 * STAGE;
          asm volatile("" : "+v"(bo[dt][0]), "+v"(bo[dt][1]));
        }
#define FRAG_T() (lbase + L_RING + (4 * g + ((l & 15) >> 2)) * 64 + (l & 3) * 8 + (((g ^ (g >> 1)) & 1) << 5))
        auto frag_t2 = [&](unsigned o) __attribute__((always_inline)) {
          frag_t t;
          t.hf.a = trabs(o); t.hf.b = trabs(o + 1024);
          return t.v;
        };
        unsigned ka[3];
        auto tile_sum = [&](u32x4 pt, int dst) __attribute__((always_inline)) {
          f32x4 c = __builtin_bit_cast(f32x4, pt);
#pragma unroll
          for (int e = 0; e < 4; ++e) { c[e] += dppf<0x124>(c[e]); c[e] += dppf<0x128>(c[e]); c[e] = add_x16(c[e]); }
          if ((l & 28) == 0) *(LDS_AS f32x4*)(smem + dst + (w * 2 + (l >> 5)) * 64 + (l & 3) * 16) = c;
        };
        frag_t bfr[6];
#define QKV_BO(s, n) (bo[(n) & 1][SLOT((s) >> 2) >> 1] + (SLOT((s) >> 2) & 1) * STAGE + ((n) >> 1) * 8192 + ((s) & 3) * 2048)
#define QKV_RDA(s, n)                                                                             \
  {                                                                                               \
    if constexpr (IT == 0) bfr[n].hf.a = trabs(QKV_BO(s, n));                                     \
    else bfr[n].v = *(const LDS_AS u32x4*)(size_t)(bo[0][SLOT((s) >> 2) >> 1] + (SLOT((s) >> 2) & 1) * STAGE + (((s) & 3) * 6 + (n)) * 1024); \
  }
#define QKV_RDB(s, n) { if constexpr (IT == 0) bfr[n].hf.b = trabs(QKV_BO(s, (n) ^ 1) + 256); }
#pragma unroll
        for (int n = 0; n < 6; ++n) { QKV_RDA(0, n); if (n < 5) QKV_RDB(0, n); }
#define XF_OFF() (l & 8 ? ((l & 7) * RX + bi % RX) * 256 + h * 8 : (p * RX + (xcd >> 1) % RX) * 256 + (4 * rt + (l & 3)) * 8)
#define XP_OFF() (OFF_XNEW + ((rot + FXT) & 7) * 256)
#define FP_OFF(i) (w == 3 ? OFF_XNEW + (i) * 1024 : OFF_FX + h * 3072 + w * 1024)
#define FX_OK(n) __all(((n) == 4 ? max(max(umax4(fx[0]), umax4(fx[1])), max(umax4(fx[2]), umax4(fx[3]))) : max(umax4(fx[0]), umax4(fx[1]))) != POISON)
#define SINCE(s0) (vm_at(IT, s, n) - vm_at(IT, s0, 5))
#define DEF4(x) asm volatile("" : "=v"(x[0]), "=v"(x[1]), "=v"(x[2]), "=v"(x[3]))
        LDS_AS unsigned* qcnt = (LDS_AS unsigned*)(smem + L_QC);
        unsigned qc;
        unsigned qadr = lbase + L_QC, qone = 1;
        asm volatile("" : "+v"(qadr), "+v"(qone));
#define count_in(i) asm volatile("s_mov_b64 exec, 1\n\tds_add_u32 %0, %1 offset:%2\n\ts_mov_b64 exec, -1" ::"v"(qadr), "v"(qone), "n"(4 * (i)) : "memory")
        auto count_wait = [&](int i, unsigned want) __attribute__((always_inline)) {
          if (__builtin_expect((unsigned)__builtin_amdgcn_readfirstlane(qc) < want, 0)) {
            while ((unsigned)__builtin_amdgcn_readfirstlane(__hip_atomic_load(qcnt + i, __ATOMIC_RELAXED, __HIP_MEMORY_SCOPE_WORKGROUP)) < want) {}
          }
          asm volatile("" ::: "memory");
        };
        unsigned long long xok = xm0;
        auto x1_need = [&](unsigned long long m) __attribute__((always_inline)) {
          if (__builtin_expect((xok & m) != m, 0)) {
            for (unsigned spins = 0;;) {
              const u32x2 f = ld8(rs, F_X1 + ((p * RX + (xcd >> 1) % RX) * 32 + (l & 31)) * SX, 0);
              xok = __builtin_amdgcn_ballot_w64(f[0] == T[0] && f[1] == T[1]);
              if ((xok & m) == m) break;
              if (spin_fail(spins, rs, T)) { xok = ~0ull; break; }
              __builtin_amdgcn_s_sleep(KSL);
            }
            asm volatile("" ::: "memory");
          }
        };
        auto gap = [&](auto sc_, auto nc_) __attribute__((always_inline)) {
          constexpr int s = decltype(sc_)::value, n = decltype(nc_)::value;
          {
            constexpr int ts = (s + 3) / 4, tc = (s + 2) / 4, t0 = 1;
            if constexpr (ts >= 3 && ts < 8 && s == 4 * ts - 3 && n == 2) { wait_vm<vm_at(IT, s, n) - vm_tile(IT, ts)>(); count_in(0); }
            if constexpr (!IT && tc >= t0 && tc < 3 && s == 4 * tc - 2 && n == 1) { wait_vm<vm_at(IT, s, n) - vm_tile(IT, tc)>(); count_in(0); }
            if constexpr (IT && ts >= t0 && ts < 3 && s == 4 * ts - 3 && n == 2) count_in(0);
            if constexpr (tc >= t0 && tc < 8 && s == 4 * tc - 2) {
              if constexpr (n == 3) qc = __hip_atomic_load(qcnt, __ATOMIC_RELAXED, __HIP_MEMORY_SCOPE_WORKGROUP);
              if constexpr (n == 5) count_wait(0, 8 * (tc - t0 + 1));
            }
          }
          if constexpr (n == 0 && tile_piece(s)) QKV_DMA1(((s - 3) >> 2) + 3, (s - 3) & 3);
          if constexpr (IT == 0) {
            if constexpr (s == 0 && !(n & 1)) QKV_DMA1(1, n >> 1);
            if constexpr (s >= 1 && s < 4 && n == 2) QKV_DMA1(2, s - 1);
            constexpr int u = s + XL;
            if constexpr (n == 1 && u < 32) {
              wait_vm<vm_at(0, s, n) - vm_x(u)>();
              X_READ(u, af[(u & ~1) % WA][u & 1], af[(u | 1) % WA][u & 1]);
            }
            if constexpr (n == 3 && u + XU < 32) { asm volatile("s_waitcnt lgkmcnt(%0)" ::"n"(s + 1 < QL ? 4 : 3) : "memory"); X_REQ(u + XU, 0); }
            if constexpr (n == 5 && u + XU < 32) X_REQ(u + XU, 1);
            if constexpr (s == WTS && n == WTG) {
              const int i = bi * 192 + w * 24 + min(l, 23);
              touch(tdum, (const GAS bf16*)pwq + (size_t)(i / 12 + 512 * (~xcd & 1)) * 3072 + ((i % 12) >> 2) * 1024 + (xcd >> 1) * 256 + (i & 3) * 64);
            }
            if constexpr (s == PBS && n == 0) { POISON_BIG(0); }
            if constexpr (s == 0 && n == 1) count_in(2);
            if constexpr (s == 0 && n == 3) qc = __hip_atomic_load(qcnt + 2, __ATOMIC_RELAXED, __HIP_MEMORY_SCOPE_WORKGROUP);
            if constexpr (s == 0 && n == 5) count_wait(2, 8);
            if constexpr (s == 1 && n == 1) {
              asm volatile("" : "+v"(ep)::"memory");
              const unsigned long long e = (((unsigned long long)(unsigned)__builtin_amdgcn_readfirstlane(ep[1]) << 32) | (unsigned)__builtin_amdgcn_readfirstlane(ep[0])) + 1ull + dispatch_id() * 0x9E3779B97F4A7C16ull;
              T = u32x2{(unsigned)e, (unsigned)(e >> 32)};
            }
            if constexpr (s == RWL && n == 4) {
              DEF4(fx);
              if (w < 4) ldrows(fx, rx, (l >> 5) * 2048 + (l & 31) * 16, (p * 256 + h * 8 + 2 * w) * 2048);
            }
            if constexpr (s == RWS && n == RWG) {
              if (w < 4) {
                if constexpr (RWL >= 0) wait_vm<SINCE(RWL < 0 ? 0 : RWL)>();
                else { fx[0] = rows0[0]; fx[1] = rows0[1]; fx[2] = rows0[2]; fx[3] = rows0[3]; }
                PINV4(fx);
                f32x2 sv[16];
#pragma unroll
                for (int i = 0; i < 4; ++i) unpack8v(fx[i], sv + 4 * i);
                f32x2 t0 = sv[0] + sv[1], t1 = sv[2] + sv[3];
#pragma unroll
                for (int j = 4; j < 16; j += 2) { t0 += sv[j]; t1 += sv[j + 1]; }
                const float m = sum32(hadd(t0 + t1)) * (1.0f / 1024.0f);
                f32x2 m2 = {m, m}, q0 = {0.f, 0.f}, q1 = {0.f, 0.f};
                asm volatile("" : "+v"(m2));
#pragma unroll
                for (int j = 0; j < 16; j += 2) {
                  const f32x2 d0 = sv[j] - m2, d1 = sv[j + 1] - m2;
                  q0 = __builtin_elementwise_fma(d0, d0, q0); q1 = __builtin_elementwise_fma(d1, d1, q1);
                }
                const float rd = ln_rd(sum32(hadd(q0 + q1)) * (1.0f / 1024.0f));
                if ((l & 30) == 0) st4(__builtin_bit_cast(unsigned, (l & 1) ? -rbf1(m) : rd), rs, (l >> 5) * 4 + (l & 1) * 8192, OFF_AB + (p * 256 + h * 8 + 2 * w) * 4);
              }
            }
            if constexpr (s == POI && n == 4) {
              if (w == 3) {
                unsigned pz1 = POISON;
                asm volatile("" : "+v"(pz1));
                const unsigned prb = 2 * p + (h >> 4), pct = h & 15, m = l - 32;
                static_assert(OFF_CB == OFF_CA + 131072, "poison addressing");
                if (l < 60) st16(u32x4{pz1, pz1, pz1, pz1}, rs, l < 32 ? OFF_CA + (l & 8) * 4096 + (l & 16) * 8192 + prb * 2048 + pct * 128 + (l & 7) * 16
                                                             : l < 56 ? OFF_FX + ((h * 6 + (m >> 2)) * 8 + p) * 64 + (l & 3) * 16
                                                                      : OFF_AB + 16384 + (p * 32 + h) * 32 + (l & 1) * 16 + (l & 2) * 4096, 0);
                if (l < 4) st4(pz1, rs, l < 2 ? OFF_XNEW + bid * 8 + l * 4 : OFF_KEY + ((l & 1) * 16 + pct) * cmax(16 * SK, 128) + prb * SK, 0);
              }
            }
            static_assert(vm_x(EIN + XL) >= vm_at(0, EIN - 3, 5) && IMG >= 1 && CSD >= 1, "first flag");
            if constexpr (s == EIN && n == 1) { if (w < 4) count_in(1); }
            if constexpr (s == EIN + 2 && n == 0) qc = __hip_atomic_load(qcnt + 1, __ATOMIC_RELAXED, __HIP_MEMORY_SCOPE_WORKGROUP);
            if constexpr (s == EIN + 2 && n == 2) {
              if (w == 0) { count_wait(1, 4); if (l < RX) st8(T, rs, F_XN + (p * RX + l) * 256 + h * 8, 0); }
            }
            if constexpr (s == ERQ && n == 4) asm volatile("buffer_load_dwordx2 %0, %1, %2, 0 offen sc1" : "=v"(cxn) : "v"(XF_OFF() + F_XN), "s"(rs) : "memory");
            if constexpr (s == CSL && n == 2) {
              wait_vm<SINCE(ERQ)>();
              asm volatile("" : "+v"(cxn)::"memory");
              if (!__all(cxn[0] == T[0] && cxn[1] == T[1])) poll_flags(rs, XF_OFF(), F_XN, T);
            }
          } else {
#define X1_OFF() (F_X1 + ((p * RX + (xcd >> 1) % RX) * 32 + (l & 31)) * SX)
            if constexpr ((s == 0 || s == 4) && n == 2) asm volatile("buffer_load_dwordx2 %0, %1, %2, 0 offen sc1" : "=v"(cxn) : "v"(X1_OFF()), "s"(rs) : "memory");
            if constexpr ((s == 3 || s == 7) && n == 0) {
              wait_vm<vm_at(1, s, 1) - vm_at(1, s - 3, 3)>();
              asm volatile("" : "+v"(cxn)::"memory");
              xok |= __builtin_amdgcn_ballot_w64(cxn[0] == T[0] && cxn[1] == T[1]);
            }
            if constexpr (a_req(s, n) >= 0 && (a_req(s, n) & 7) == 0) x1_need(0x30003ull << (((rot + a_req(s, n) / 8) & 7) * 2));
          }
          if constexpr (IT && a_req(s, n) >= 0) A_LOAD1(a_req(s, n) / 2, a_req(s, n) & 1);
          if constexpr (IT == 0 && s == CSD && n == CSG) {
            if (w < 3) {
              unsigned a0 = FRAG_T() + SLOT(s >> 2) * STAGE + w * 8192, a1 = a0 ^ 32, one2 = 0x3F803F80u;
              asm volatile("" : "+v"(a0), "+v"(a1), "+v"(one2));
              const u32x4 o4 = {one2, one2, one2, one2};
              f32x4 y0 = {0.f, 0.f, 0.f, 0.f}, y1 = y0;
#pragma unroll
              for (int i = 0; i < 4; ++i) { y0 = mfma16(frag_t2(a0 + i * 2048), o4, y0); y1 = mfma16(frag_t2(a1 + i * 2048), o4, y1); }
              if ((l & 14) == 0) st16(__builtin_bit_cast(u32x4, (l & 1) ? y1 : y0), rs, g * 16 + (l & 1) * 512, OFF_CS + ((h * 6 + 2 * w) * 8 + p) * 64);
            }
          }
          if constexpr (!IT && s == CSL && n == 4) { asm volatile("" : "=v"(fx[0])); if (w < 3) ld16h<0>(fx[0], rs, l * 16, OFF_CS + h * 3072 + w * 1024); }
          if constexpr (!IT && s == CSS && n == 2) {
            if (w < 3) {
              wait_vm<SINCE(CSL)>();
              asm volatile("" : "+v"(fx[0])::"memory");
              tile_sum(fx[0], L_CSL);
            }
          }
          if constexpr (s == abl(IT) && n == 4) ld4h<0>(aq[0], rs, (l & 31) * 4 + (l >> 5) * 8192, AB_OFF());
          if constexpr (s == ABS && n == 0) {
            wait_vm<SINCE(abl(IT))>();
            asm volatile("" : "+v"(aq[0])::"memory");
            if constexpr (IT) {
              for (unsigned spins = 0; !__all(aq[0] != POISON) && !spin_fail(spins, rs, T);) {
                ld4h<0>(aq[0], rs, (l & 31) * 4 + (l >> 5) * 8192, AB_OFF());
                asm volatile("s_waitcnt vmcnt(0)" : "+v"(aq[0])::"memory");
              }
            }
            *(LDS_AS unsigned*)(smem + L_K + (8 + rt) * 2048 + l * 4) = aq[0];
          }
          if constexpr (s == ABS && n == 2) {
#pragma unroll
            for (int a = 0; a < 2; ++a) {
              const LDS_AS char* ab = (const LDS_AS char*)(smem + L_K + (8 + rt) * 2048 + a * 64);
              aq[a] = *(const LDS_AS unsigned*)(ab + (l & 15) * 4); bq[a] = *(const LDS_AS unsigned*)(ab + 128 + (l & 15) * 4);
              av[a] = *(const LDS_AS u32x4*)(ab + g * 16); bv[a] = *(const LDS_AS u32x4*)(ab + 128 + g * 16);
            }
          }
          if constexpr (s == ABS && n == 5) {
#pragma unroll
            for (int a = 0; a < 2; ++a) { b2[a] = f32x2{asf(bq[a]), asf(bq[a])}; asm volatile("" : "+v"(b2[a])); }
          }
          if constexpr (s == ABS + 1) {
            constexpr int k = (n + 4) % 6;
            if constexpr (k < 4) cs4[k & 1] = *(const LDS_AS f32x4*)(smem + L_CSL + k * 64 + g * 16);
            else cs4[k & 1][0] = *(const LDS_AS float*)(smem + L_CSL + k * 64 + (l & 15) * 4);
          }
          if constexpr ((s == ABS + 1 && n > 0) || (s == ABS + 2 && n == 0)) {
            constexpr int k = (n + 3) % 6;
            if constexpr (k < 4) {
#pragma unroll
              for (int a = 0; a < 2; ++a) acc[a][k] = __builtin_elementwise_fma(cs4[k & 1], f32x4{b2[a][0], b2[a][1], b2[a][0], b2[a][1]}, acc[a][k]);
            } else {
              f32x2 c2 = {cs4[k & 1][0], cs4[k & 1][0]};
              asm volatile("" : "+v"(c2));
#pragma unroll
              for (int a = 0; a < 2; ++a) acc[a][k] = __builtin_elementwise_fma(f32x4{c2[0], c2[1], c2[0], c2[1]}, __builtin_bit_cast(f32x4, bv[a]), acc[a][k]);
            }
          }
          if constexpr (IT == 0) {
            if constexpr (s == KP0 - 1 && n < 3) {
              const int c = 3 * (w & 1) + n;
              ka[n] = (FRAG_T() ^ ((c & 1) << 5)) + (SLOT(5) * STAGE + (c >> 1) * 8192 + (w >> 1) * 2048);
              asm volatile("" : "+v"(ka[n]));
            }
            if constexpr (s >= KP0 && s < KP0 + 3 && (n & 1)) keep[3 * (s - KP0) + (n >> 1)] = frag_t2(ka[n >> 1] + (s - KP0) * STAGE);
            if constexpr (s >= IMG && s < IMG + 3 && n == 4) st16(keep[s - IMG], rs, l * 16 + (s - IMG) * 1024, OFF_WI + (h * 8 + ((rot + 5) & 7)) * STAGE + w * 3072);
            if constexpr (s >= TS0 && s < TS0 + TSN && n == 4) {
              if (tstep == s && w < (ffn_on ? TW : TWR)) touch(tdum, tsrc);
            }
            if constexpr (s == TST && n == 4) { if (w >= (ffn_on ? TW : TWR)) touch(tdum, tsrc); }
          } else {
            if constexpr (s == 28 && n == 0) { POISON_BIG(1); }
            if constexpr (s == FSL && n == 4) { ld4h<0>(kv, rs, (l & 15) * SK, OFF_KEY + (bid & 15) * cmax(16 * SK, 128)); row_load(rs, g, ffn_xoff(kg0, 0, w), fx); }
            if constexpr (s == FS0 && n == 0) {
              wait_vm<SINCE(FSL)>();
              asm volatile("" : "+v"(kv)::"memory");
              PINV4(fx);
              if (__builtin_expect(!__all(kv != POISON), 0)) {
                LDS_AS unsigned* kw = (LDS_AS unsigned*)(smem + L_SKEY + 40);
                if (w == 0) {
                  kv = key_wait(rs, T, l, 0, bid & 15, kv);
                } else {
                  while ((kv = __hip_atomic_load(kw, __ATOMIC_RELAXED, __HIP_MEMORY_SCOPE_WORKGROUP)) == POISON) __builtin_amdgcn_s_sleep(2);
                }
              }
              fkey = __builtin_amdgcn_readfirstlane(umax16(kv));
              if (w == 0) *(LDS_AS unsigned*)(smem + L_SKEY + 40) = min(fkey, POISON - 1);
              sel0 = key_row(fkey);
              aff = (int)(sel0 >> 8) == p;
              asm volatile("" : "=v"(sa), "=v"(sb));
              if (ffn_on) {
                if (__builtin_expect(((kg0 ^ fkey) & 0x780u) != 0, 0)) ld4x16(fx, rs, g * 16, ffn_xoff(fkey, 0, w));
                ffn_gu(gv, uv, fx, sa, sb);
              }
            }
            if constexpr (s == FS0 && n == 2) {
              if (ffn_on) {
                ffn_gu_check(rs, T, ffn_xoff(fkey, 0, w), l, gv, uv, fx, sa, sb);
                ffn_gu_store(smem, l, w, sa, sb);
                count_in(1);
              }
            }
            if constexpr (s == FS0D && n == FDG) {
              if (ffn_on && w < 4) {
                s16x4 da[8], db[8];
                ffn_d_read(smem, l, 2 * w, da);
                while (__hip_atomic_load(qcnt + 1, __ATOMIC_RELAXED, __HIP_MEMORY_SCOPE_WORKGROUP) < 8u) {}
                asm volatile("" ::: "memory");
                const f32x4 t = ffn_t_read(smem, l);
                ffn_d_read(smem, l, 2 * w + 1, db);
                u32x2 ha = ffn_h(t, l), hb = {dppu<0x118>(ha[0]), dppu<0x118>(ha[1])};
                f32x4 ya = {0.f, 0.f, 0.f, 0.f}, yb = ya;
#pragma unroll
                for (int i = 0; i < 8; ++i) {
                  ya = mfma16k16(da[i], __builtin_bit_cast(s16x4, ha), ya); yb = mfma16k16(db[i], __builtin_bit_cast(s16x4, hb), yb);
                  ha = u32x2{dppu<0x111>(ha[0]), dppu<0x111>(ha[1])}; hb = u32x2{dppu<0x111>(hb[0]), dppu<0x111>(hb[1])};
                }
                ffn_d_store(rs, l, 8 * w, 0, slice, ya + yb);
              }
            }
#define RED_LOAD(ld, l8)                                                                                                    \
  {                                                                                                                         \
    const unsigned G = bi, po = OFF_PART + G * 32768;                                                        \
    ld(gv[0], rs, tid * 16, po); ld(gv[1], rs, tid * 16, po + 4096); ld(gv[2], rs, tid * 16, po + 8192); ld(gv[3], rs, tid * 16, po + 12288); \
    ld(uv[0], rs, tid * 16, po + 16384); ld(uv[1], rs, tid * 16, po + 20480); ld(uv[2], rs, tid * 16, po + 24576);        \
    l8(cxn, rs, (l & 3) * 16 + (l & 4) * 2, OFF_CA + (sel0 >> 7) * 2048 + G * 64);                                         \
    l8(cx1, rs, (l & 3) * 16 + (l & 4) * 2, OFF_CB + (sel0 >> 7) * 2048 + G * 64);                                          \
  }
            if constexpr (s == FS1 && n == 4) {
              asm volatile("" : "=v"(cxn), "=v"(cx1));
              if (!ffn_on && w < 4) RED_LOAD(ld16u, ld8h);
            }
            if constexpr (s == FS1S && n == 0) {
              if (!ffn_on && w < 4) {
                wait_vm<SINCE(FS1)>();
                PINV4(gv); PINV4(uv);
                asm volatile("" : "+v"(cxn), "+v"(cx1)::"memory");
                for (unsigned spins = 0; !__all(max(max(max(umax4(gv[0]), umax4(gv[1])), max(umax4(gv[2]), umax4(gv[3]))),
                                                    max(max(umax4(uv[0]), umax4(uv[1])), max(max(umax4(uv[2]), max(cxn[0], cxn[1])), max(cx1[0], cx1[1])))) != POISON) && !spin_fail(spins, rs, T);) {
                  RED_LOAD(ld16u, ld8h);
                  DRAIN();
                  PINV4(gv); PINV4(uv);
                  asm volatile("" : "+v"(cxn), "+v"(cx1)::"memory");
                }
#define F4(x) __builtin_bit_cast(f32x4, x)
                ((LDS_AS f32x4*)(smem + L_SFIN))[tid] = ((F4(gv[0]) + F4(gv[1])) + (F4(gv[2]) + F4(gv[3]))) + ((F4(uv[0]) + F4(uv[1])) + F4(uv[2]));
#undef F4
                count_in(2);
                if (w == 0) {
                  while (__hip_atomic_load(qcnt + 2, __ATOMIC_RELAXED, __HIP_MEMORY_SCOPE_WORKGROUP) < 4u) {}
                  asm volatile("" ::: "memory");
                  const u32x2 ov = red_final<4>(smem, l, fkey, cxn, cx1);
                  if (l < 8) st8(unpoison2(ov), rs, OFF_XNEW + (bi * 8 + (l & 3) * 2 + (l >> 2)) * 8, 0);
                }
              }
            }
            if constexpr (s == FXA && n == 4) { DEF4(fx); if (w < 3) row_load(rs, g, XP_OFF(), fx); }
            if constexpr (s == FXB && n == 0) {
              if (w < 3) {
                const unsigned fa = bo[0][0] + SLOT(FXT) * STAGE + w * 2048;
                u32x4 f[8];
#pragma unroll
                for (int i = 0; i < 8; ++i) f[i] = *(const LDS_AS u32x4*)(size_t)(fa + (i >> 1) * 6144 + (i & 1) * 1024);
                wait_vm<SINCE(FXA)>();
                PINV4(fx);
                ffn_xwait<XSL>(rs, T, XP_OFF(), l, fx);
                f32x4 y0 = mfma16(f[0], fx[0], f32x4{0.f, 0.f, 0.f, 0.f}), y1 = mfma16(f[1], fx[0], f32x4{0.f, 0.f, 0.f, 0.f});
#pragma unroll
                for (int i = 1; i < 4; ++i) { y0 = mfma16(f[2 * i], fx[i], y0); y1 = mfma16(f[2 * i + 1], fx[i], y1); }
                if ((l & 14) == 0) st16(unpoison4(__builtin_bit_cast(u32x4, (l & 1) ? y1 : y0)), rs, g * 16 + (l & 1) * 512, OFF_FX + ((h * 6 + 2 * w) * 8 + p) * 64);
              }
            }
            if constexpr (s == FXL && n == 4) {
              asm volatile("" : "=v"(fx[0]), "=v"(fx[1]));
              if (aff && w < 4) { ld16h<0>(fx[0], rs, l * 16, FP_OFF(0)); ld16h<0>(fx[1], rs, l * 16, FP_OFF(1)); }
            }
            if constexpr (s == FXS && n == 2) {
              if (aff && w < 4) {
                wait_vm<0>();
                asm volatile("" : "+v"(fx[0]), "+v"(fx[1])::"memory");
                if (!FX_OK(2)) {
                  unsigned spins = 0;
                  do {
                    if (spin_fail(spins, rs, T)) break;
                    fx[0] = ld16(rs, l * 16, FP_OFF(0)); fx[1] = ld16(rs, l * 16, FP_OFF(1));
                  } while (!FX_OK(2));
                }
                if (w == 3) {
                  f32x2 nx[8];
                  unpack8v(fx[0], nx); unpack8v(fx[1], nx + 4);
                  const f32x2 sm = ((nx[0] + nx[1]) + (nx[2] + nx[3])) + ((nx[4] + nx[5]) + (nx[6] + nx[7]));
                  const float mean = sum64(hadd(sm)) * (1.0f / 1024.0f);
                  const f32x2 m2 = {mean, mean};
                  f32x2 q = {0.f, 0.f};
#pragma unroll
                  for (int j = 0; j < 8; ++j) { const f32x2 d = nx[j] - m2; q = __builtin_elementwise_fma(d, d, q); }
                  const float rd = ln_rd(sum64(hadd(q)) * (1.0f / 1024.0f));
                  if (l == 0) *(LDS_AS f32x2*)(smem + L_FXL + 384) = f32x2{rd, -rbf1(mean)};
                } else {
                  tile_sum(fx[0], L_FXL);
                }
                count_in(3);
              }
            }
          }
        };
        static_for<32>([&](auto sc_) __attribute__((always_inline)) {
          constexpr int s = decltype(sc_)::value;
          if constexpr (s > 0) PIN_ACC();
          if constexpr (IT == 1) wait_vm<vm_at(IT, s, 0) - vm_a(IT, s)>();
          pin_w<s % WA>(af[s % WA][0], af[s % WA][1]);
          SB();
          static_for<6>([&](auto nc_) __attribute__((always_inline)) {
            constexpr int n = decltype(nc_)::value;
            if constexpr (s == 0) { acc[0][n] = f32x4{0.f, 0.f, 0.f, 0.f}; acc[1][n] = acc[0][n]; }
            constexpr bool on = s < QL || n >= 2;
            if constexpr (on) acc[0][n] = n < 4 ? mfma16(bfr[n].v, af[s % WA][0], acc[0][n]) : mfma16(af[s % WA][0], bfr[n].v, acc[0][n]);
            SB();
            if constexpr (n == 0) { QKV_RDB(s, 5); }
            else if constexpr (s + 1 < 32 && (s + 1 < QL || n > 2)) QKV_RDB(s + 1, n - 1);
            SB();
            if constexpr (on) acc[1][n] = n < 4 ? mfma16(bfr[n].v, af[s % WA][1], acc[1][n]) : mfma16(af[s % WA][1], bfr[n].v, acc[1][n]);
            SB();
            if constexpr (s + 1 < 32 && (s + 1 < QL || n >= 2)) QKV_RDA(s + 1, n);
            SB();
            gap(sc_, nc_);
            SB();
          });
        });
        PIN_ACC();
        frag_t qw[32 - QL][2];
        static_for<32 - QL>([&](auto c_) __attribute__((always_inline)) {
          constexpr int s = QL + decltype(c_)::value;
          QKV_RDA(s, 0); QKV_RDB(s, 0); QKV_RDA(s, 1); QKV_RDB(s, 1);
          qw[s - QL][0] = bfr[0]; qw[s - QL][1] = bfr[1];
        });
        SB();
        const bool own = aff && (int)((sel0 >> 5) & 7) == rt;
        const bool m0 = !(sel0 & 16), mq = (l & 15) == (int)(sel0 & 15), mv = g == (int)((sel0 & 15) >> 2);
        auto patch_qk = [&](int n) __attribute__((always_inline)) {
          f32x4 t = *(const LDS_AS f32x4*)(smem + L_FXL + n * 64 + g * 16);
          const f32x4 c = *(const LDS_AS f32x4*)(smem + L_CSL + n * 64 + g * 16);
          const float nb = *(const LDS_AS float*)(smem + L_FXL + 388);
#pragma unroll
          for (int e = 0; e < 4; ++e) { t[e] = __builtin_fmaf(c[e], nb, t[e]); asm volatile("" : "+v"(t[e])); }
          acc[0][n] = (mq && m0) ? t : acc[0][n];
          acc[1][n] = (mq && !m0) ? t : acc[1][n];
        };
        if (own) {
          while (__hip_atomic_load(qcnt + 3, __ATOMIC_RELAXED, __HIP_MEMORY_SCOPE_WORKGROUP) < 4u) {}
          asm volatile("" ::: "memory");
          patch_qk(2); patch_qk(3);
          const f32x2 nab = *(const LDS_AS f32x2*)(smem + L_FXL + 384);
          const LDS_AS float* fl_ = (const LDS_AS float*)(smem + L_FXL + 256) + (l & 15);
          const LDS_AS float* cl_ = (const LDS_AS float*)(smem + L_CSL + 256) + (l & 15);
          const float t4 = __builtin_fmaf(cl_[0], nab[1], fl_[0]), t5 = __builtin_fmaf(cl_[16], nab[1], fl_[16]);
          const unsigned na = __builtin_bit_cast(unsigned, (float)nab[0]);
#pragma unroll
          for (int a = 0; a < 2; ++a) {
            const bool ma = a ? !m0 : m0;
            aq[a] = (mq && ma) ? na : aq[a];
#pragma unroll
            for (int e = 0; e < 4; ++e) {
              const bool me = mv && ma && e == (int)(sel0 & 3);
              acc[a][4][e] = me ? t4 : acc[a][4][e]; acc[a][5][e] = me ? t5 : acc[a][5][e];
              av[a][e] = me ? na : av[a][e];
            }
          }
        }
        constexpr float C2 = 1.4426950408889634f * 0.17677669529663687f;
#pragma unroll
        for (int a = 0; a < 2; ++a) {
          f32x2 a2 = {asf(aq[a]), asf(aq[a])};
          qs2[a] = f32x2{asf(aq[a]) * C2, asf(aq[a]) * C2};
          asm volatile("" : "+v"(a2), "+v"(qs2[a]));
          acc[a][2] *= f32x4{a2[0], a2[1], a2[0], a2[1]}; acc[a][3] *= f32x4{a2[0], a2[1], a2[0], a2[1]};
          acc[a][4] *= __builtin_bit_cast(f32x4, av[a]); acc[a][5] *= __builtin_bit_cast(f32x4, av[a]);
        }
        k0 = pack8(acc[0][2], acc[0][3]); k1 = pack8(acc[1][2], acc[1][3]); v0 = pack8(acc[0][4], acc[1][4]); v1 = pack8(acc[0][5], acc[1][5]);
        const unsigned kvo = OFF_KV + IT * (8u << 20) + (p * 32 + h) * 32768 + rt * 4096;
        if (p < 7) { st16(k0, rs, l * 16, kvo); st16(k1, rs, l * 16 + 1024, kvo); st16(v0, rs, l * 16 + 2048, kvo); st16(v1, rs, l * 16 + 3072, kvo); }
        char* ownp = smem + (8 + rt) * 2048 + l * 16;
        *(u32x4*)(ownp + L_K) = k0;
        if (rt < 7) *(u32x4*)(ownp + L_K + 1024) = k1;
        *(u32x4*)(ownp + L_V) = v0; *(u32x4*)(ownp + L_V + 1024) = v1;
        SB();
        static_for<32 - QL>([&](auto c_) __attribute__((always_inline)) {
          constexpr int s = QL + decltype(c_)::value;
#pragma unroll
          for (int n = 0; n < 2; ++n) { acc[0][n] = mfma16(qw[s - QL][n].v, af[s % WA][0], acc[0][n]); acc[1][n] = mfma16(qw[s - QL][n].v, af[s % WA][1], acc[1][n]); }
        });
        if (own) { patch_qk(0); patch_qk(1); }
      };
      qkv_loop(it_c);
    }
#undef A_LOAD1
#undef QKV_RDA
#undef QKV_RDB
#undef QKV_BO
    {
      IDS();
      const GAS bf16 *pwo, *pwg, *pwu;
      asm volatile("s_load_dwordx2 %0, %3, 24\n\ts_load_dwordx2 %1, %3, 40\n\ts_load_dwordx2 %2, %3, 48" : "=&s"(pwo), "=&s"(pwg), "=&s"(pwu) : "s"(ka));
      const int ql = l & 15;
      const f32x4 z4 = {0.f, 0.f, 0.f, 0.f}, ninf = {-INFINITY, -INFINITY, -INFINITY, -INFINITY};
      LDS_AS unsigned* rdy = (LDS_AS unsigned*)(smem + L_RDY);
      const unsigned rtag = T[0] + it;
      u32x4 qf[2];
      f32x4 cn[2], oa[2][2], sum[2], S[2][2];
      unsigned one2 = 0x3F803F80u;
      asm volatile("" : "+v"(one2));
      const u32x4 ones = {one2, one2, one2, one2};
      auto ex8 = [&](f32x4 a, f32x4 b) __attribute__((always_inline)) {
        return u32x4{pack2(__builtin_amdgcn_exp2f(a[0]), __builtin_amdgcn_exp2f(a[1])), pack2(__builtin_amdgcn_exp2f(a[2]), __builtin_amdgcn_exp2f(a[3])),
                     pack2(__builtin_amdgcn_exp2f(b[0]), __builtin_amdgcn_exp2f(b[1])), pack2(__builtin_amdgcn_exp2f(b[2]), __builtin_amdgcn_exp2f(b[3]))};
      };
      {
#pragma unroll
        for (int a = 0; a < 2; ++a) {
          const f32x4 c4 = {qs2[a][0], qs2[a][1], qs2[a][0], qs2[a][1]};
          qf[a] = pack8(acc[a][0] * c4, acc[a][1] * c4);
        }
        f32x4 mk;
#pragma unroll
        for (int e = 0; e < 4; ++e) mk[e] = (4 * g + e <= ql) ? 0.f : -INFINITY;
        const f32x4 d0 = mfma16(k0, qf[0], mk), d1 = mfma16(k1, qf[1], mk), d2 = mfma16(k0, qf[1], z4);
        const float m0 = -max_x32(max_x16(fmaxf(fmaxf(d0[0], d0[1]), fmaxf(d0[2], d0[3]))));
        const float m1 = -max_x32(max_x16(fmaxf(fmaxf(d1[0], d1[1]), fmaxf(d1[2], d1[3]))));
        cn[0] = f32x4{m0, m0, m0, m0}; cn[1] = f32x4{m1, m1, m1, m1};
        asm volatile("" : "+v"(cn[0]), "+v"(cn[1]));
        const f32x4 e0 = d0 + cn[0];
        const u32x4 p0 = {pack2(__builtin_amdgcn_exp2f(e0[0]), __builtin_amdgcn_exp2f(e0[1])), pack2(__builtin_amdgcn_exp2f(e0[2]), __builtin_amdgcn_exp2f(e0[3])), 0u, 0u};
        const u32x4 p1 = ex8(d2 + cn[1], d1 + cn[1]);
        oa[0][0] = mfma16(v0, p0, z4); oa[0][1] = mfma16(v1, p0, z4); sum[0] = mfma16(ones, p0, z4);
        oa[1][0] = mfma16(v0, p1, z4); oa[1][1] = mfma16(v1, p1, z4); sum[1] = mfma16(ones, p1, z4);
      }
      const char* kb_ = smem + L_K + l * 16;
      const char* vb_ = smem + L_V + l * 16;
      const unsigned wov = (l >> 3) * 2048 + (((((l & 7) >> 1) ^ ((l >> 4) & 3)) * 2 + (l & 1)) << 4), fnv = (l >> 1) * (F_ * 2) + (l & 1) * 16;
      const unsigned pp = __builtin_amdgcn_readfirstlane(p > 0 ? 32 : 0), pvo = OFF_KV + it * (8u << 20) + (p * 32 + h - pp) * 32768;
      const unsigned pfo = __builtin_amdgcn_readfirstlane(F_KV + (it * 256 + p * 32 + h - pp) * cmax(8 * SV, 128));
      asm volatile("" : "+v"(oa[0][0]), "+v"(oa[0][1]), "+v"(oa[1][0]), "+v"(oa[1][1]), "+v"(sum[0]), "+v"(sum[1]));
      DRAIN();
      asm volatile("" : "+v"(tdum));
      if (l == 0) st8(T, rs, rt * SV, F_KV + (it * 256 + p * 32 + h) * cmax(8 * SV, 128));
      BAR();
      asm volatile("" : "+s"(pwo), "+s"(pwg), "+s"(pwu));
      WO_PIECE(15); WO_PIECE(14);
#define LDS16(a_) (*(const LDS_AS u32x4*)(size_t)(a_))
      u32x4 kf[2], vf[2];
      int nw = 0;
      u32x4 p0, p1;
      float e0, e1;
#define EE(v, i) { e0 = __builtin_amdgcn_exp2f(v[i]); e1 = __builtin_amdgcn_exp2f(v[i + 1]); pin2s(e0, e1); SB(); }
#define CV(P, i) { unsigned t_ = pack2(e0, e1); pin1(t_); P[i] = t_; SB(); }
      auto body = [&](unsigned ad, auto dk_) __attribute__((always_inline)) {
        constexpr int dk = decltype(dk_)::value;
        const f32x4 a0 = S[1][0], a1 = S[1][1];
        sum[0] = mfma16(ones, p0, sum[0]); SB();
        EE(a0, 0);
        lds_wait<2>(kf[0], kf[1]);
        S[0][0] = mfma16(kf[0], qf[0], cn[0]); SB();
        CV(p1, 0); EE(a0, 2);
        S[0][1] = mfma16(kf[1], qf[0], cn[0]); SB();
        CV(p1, 1); EE(a1, 0);
        lds_wait<0>(vf[0], vf[1]);
        oa[0][0] = mfma16(vf[0], p0, oa[0][0]); SB();
        CV(p1, 2); EE(a1, 2);
        oa[0][1] = mfma16(vf[1], p0, oa[0][1]); SB();
        CV(p1, 3);
        S[1][0] = mfma16(kf[0], qf[1], cn[1]); SB();
        EE(S[0][0], 0);
        S[1][1] = mfma16(kf[1], qf[1], cn[1]); SB();
        lds2<2048 + dk>(kf[0], kf[1], ad); SB();
        CV(p0, 0); EE(S[0][0], 2);
        oa[1][0] = mfma16(vf[0], p1, oa[1][0]); SB();
        CV(p0, 1); EE(S[0][1], 0);
        oa[1][1] = mfma16(vf[1], p1, oa[1][1]); SB();
        lds2<2048 + L_V>(vf[0], vf[1], ad); SB();
        CV(p0, 2); EE(S[0][1], 2);
        sum[1] = mfma16(ones, p1, sum[1]); SB();
        CV(p0, 3);
      };
      auto last = [&]() __attribute__((always_inline)) {
        sum[0] = mfma16(ones, p0, sum[0]); SB();
        EE(S[1][0], 0);
        lds_wait<0>(vf[0], vf[1]);
        pin2(kf[0], kf[1]);
        oa[0][0] = mfma16(vf[0], p0, oa[0][0]); SB();
        CV(p1, 0); EE(S[1][0], 2);
        oa[0][1] = mfma16(vf[1], p0, oa[0][1]); SB();
        CV(p1, 1); EE(S[1][1], 0);
        const float f0 = __builtin_amdgcn_exp2f(S[1][1][2]), f1 = __builtin_amdgcn_exp2f(S[1][1][3]);
        p1[2] = pack2(e0, e1); p1[3] = pack2(f0, f1);
        oa[1][0] = mfma16(vf[0], p1, oa[1][0]); oa[1][1] = mfma16(vf[1], p1, oa[1][1]); sum[1] = mfma16(ones, p1, sum[1]);
      };
      auto piece = [&]() __attribute__((always_inline)) {
        if constexpr (it == 1) { WO_PIECE(13 - nw); }
        else if (ffn_on) { FFN_DMA(nw); }
        ++nw;
      };
      if (rt > 0) {
        unsigned ad = lbase + l * 16 + (6 + rt) * 2048;
        lds2<2048>(kf[0], kf[1], ad); lds2<2048 + L_V>(vf[0], vf[1], ad);
        lds_wait<2>(kf[0], kf[1]);
        S[0][0] = mfma16(kf[0], qf[0], cn[0]); S[0][1] = mfma16(kf[1], qf[0], cn[0]); S[1][0] = mfma16(kf[0], qf[1], cn[1]); S[1][1] = mfma16(kf[1], qf[1], cn[1]);
        SB();
        lds_wait<0>(vf[0], vf[1]);
        lds2<0>(kf[0], kf[1], ad);
        p0 = ex8(S[0][0], S[0][1]);
        lds_wait<0>(kf[0], kf[1]);
#pragma unroll 1
        for (int i = rt - 1; i > 0; --i) {
          ad -= 2048;
          body(ad, std::integral_constant<int, -2048>{});
          piece();
          SB();
        }
        piece();
        last();
      }
      {
        constexpr int FW = it ? FW1 : FW0, FN = 8 / FW;
        if (w < FW) {
          const int g0 = FN * rt;
          poll_flags(rs, (l & (FN - 1)) * SV, pfo + g0 * SV, T);
#pragma unroll
          for (int j = 0; j < FN; ++j) {
            const unsigned d = lbase + (g0 + j) * 2048, o = pvo + (g0 + j) * 4096;
            dma16s<0>(rs, l * 16, o, d + L_K); dma16s<1024>(rs, l * 16, o, d + L_K);
            dma16s<0>(rs, l * 16, o + 2048, d + L_V); dma16s<1024>(rs, l * 16, o + 2048, d + L_V);
          }
        }
        if (p == 0) { cn[0] = ninf; cn[1] = ninf; }
        f32x4 mk;
#pragma unroll
        for (int e = 0; e < 4; ++e) mk[e] = (4 * g + e > ql) ? 0.f : -INFINITY;
        const f32x4 cm0 = cn[0] + mk, cm1 = cn[1] + mk;
        if (w < FW) {
          if constexpr (FN > 1) { wait_vm<2 * FN>(); if (l < FN / 2) rdy[FN * rt + l] = rtag; }
          DRAIN();
          if (l < FN) rdy[FN * rt + l] = rtag;
        }
        if constexpr (it == 1) { WO_PIECE(6); WO_PIECE(5); WO_PIECE(4); }
        else { WO_PIECE(13); WO_PIECE(12); if (ffn_on) FFN_DMA(7); }

        int have = rt - 1;
        auto need = [&](int gq) __attribute__((always_inline)) {
          if (__builtin_expect(gq > have && gq < 8, 0)) {
            do {
              const unsigned t = __hip_atomic_load(rdy + (l & 7), __ATOMIC_RELAXED, __HIP_MEMORY_SCOPE_WORKGROUP);
              have = rt - 1 + __builtin_ctz(~((unsigned)__builtin_amdgcn_ballot_w64(t == rtag) & 0xffu) >> rt);
            } while (gq > have);
            asm volatile("" ::: "memory");
          }
        };
        unsigned ad = lbase + l * 16 + rt * 2048;
        need(rt);
        lds2<0>(kf[0], kf[1], ad); lds2<L_V>(vf[0], vf[1], ad);
        lds_wait<2>(kf[0], kf[1]);
        S[0][0] = mfma16(kf[0], qf[0], cm0); S[0][1] = mfma16(kf[1], qf[0], cn[0]); S[1][0] = ninf; S[1][1] = mfma16(kf[1], qf[1], cm1);
        SB();
        lds_wait<0>(vf[0], vf[1]);
        need(rt + 1);
        lds2<2048>(kf[0], kf[1], ad);
        p0 = ex8(S[0][0], S[0][1]);
        lds_wait<0>(kf[0], kf[1]);
#pragma unroll 1
        for (int gq = rt + 1; gq < 8; ++gq) {
          need(gq + 1);
          body(ad, std::integral_constant<int, 2048>{});
          piece();
          SB();
          ad += 2048;
        }
        last();
      }
      {
        float big = 0.f;
#pragma unroll
        for (int a = 0; a < 2; ++a)
#pragma unroll
          for (int d = 0; d < 2; ++d)
            big = __builtin_elementwise_maximum(__builtin_elementwise_maximum(big, __builtin_elementwise_maximum(fabsf(oa[a][d][0]), fabsf(oa[a][d][1]))),
                                                __builtin_elementwise_maximum(fabsf(oa[a][d][2]), fabsf(oa[a][d][3])));
        bool ok = sum[0][0] >= 0.5f && sum[0][0] < 0x1p64f && sum[1][0] >= 0.5f && sum[1][0] < 0x1p64f && big < INFINITY;
        if (!__all(ok)) {
#pragma unroll
          for (int a = 0; a < 2; ++a) {
            const int qs = 2 * rt + a, qq = 256 + 16 * qs + ql, klo = max(qq - 255, p > 0 ? 0 : 256);
            auto score = [&](int t) __attribute__((always_inline)) {
              f32x4 s = mfma16(t == 31 ? k1 : *(const u32x4*)(kb_ + t * 1024), qf[a], z4);
#pragma unroll
              for (int e = 0; e < 4; ++e) { const int kk = 16 * t + 4 * g + e; s[e] = (kk <= qq && kk >= klo) ? s[e] : -INFINITY; }
              return s;
            };
            float mx = -INFINITY;
#pragma unroll 1
            for (int t = qs; t <= qs + 16; ++t) { const f32x4 s = score(t); mx = fmaxf(fmaxf(mx, fmaxf(s[0], s[1])), fmaxf(s[2], s[3])); }
            mx = max_x32(max_x16(mx));
            oa[a][0] = z4; oa[a][1] = z4; sum[a] = z4;
#pragma unroll 1
            for (int G = qs >> 1; G <= (qs + 16) >> 1; ++G) {
              const f32x4 s0 = score(2 * G), s1 = score(2 * G + 1);
              f32x4 e0, e1;
#pragma unroll
              for (int e = 0; e < 4; ++e) { e0[e] = __builtin_amdgcn_exp2f(s0[e] - mx); e1[e] = __builtin_amdgcn_exp2f(s1[e] - mx); }
              const u32x4 pf = pack8(e0, e1);
              oa[a][0] = mfma16(*(const u32x4*)(vb_ + G * 2048), pf, oa[a][0]);
              oa[a][1] = mfma16(*(const u32x4*)(vb_ + G * 2048 + 1024), pf, oa[a][1]);
              sum[a] = mfma16(ones, pf, sum[a]);
            }
          }
        }
      }
#pragma unroll
      for (int a = 0; a < 2; ++a) {
        const float inv = __builtin_amdgcn_rcpf(sum[a][0]);
        oa[a][0] *= inv; oa[a][1] *= inv;
      }
      const unsigned oo = OFF_O + it * (4u << 20) + ((p * 8 + rt) * 64 + h * 2) * 1024;
      st16(pack8(oa[0][0], oa[0][1]), rs, l * 16, oo);
      st16(pack8(oa[1][0], oa[1][1]), rs, l * 16 + 1024, oo);
      DRAIN();
      BAR();
      if (tid == 0) st8(T, rs, h * SO, F_O + (it * 8 + p) * 32 * SO);
      if constexpr (it == 0) {
        DEF4(gv); DEF4(uv);
        if (ffn_on) {
          const unsigned ro = 65536 + w * 1024 + (4 * g + ((l & 15) >> 2)) * 32 + (l & 3) * 8;
#pragma unroll
          for (int i = 0; i < 4; ++i) {
            frag_t f;
            f.hf.a = trread(smem, ro + i * 16384); f.hf.b = trread(smem, ro + i * 16384 + 512);
            gv[i] = f.v;
            f.hf.a = trread(smem, ro + i * 16384 + 8192); f.hf.b = trread(smem, ro + i * 16384 + 8704);
            uv[i] = f.v;
          }
        }
        PINV4(gv); PINV4(uv);
        {
          ARGP(px, 0);
          const unsigned xo = ((rb * 4 + (w & 3)) * 32 + (w >> 2) * 16) * 2048 + ct * 128, xw = (l >> 3) * 2048 + (((l & 7) ^ g) << 4);
          dma16(RSRC(px, 2048 * 1024 * 2), xw, xo, lbase + w * 4096); dma16(RSRC(px, 2048 * 1024 * 2), xw ^ 64, xo + 16384, lbase + w * 4096 + 1024);
        }
      }
      WO_PIECE(3); WO_PIECE(2); WO_PIECE(1); WO_PIECE(0);
      if constexpr (it == 0) { WO_PIECE(11); WO_PIECE(10); WO_PIECE(9); WO_PIECE(8); WO_PIECE(7); WO_PIECE(6); WO_PIECE(5); WO_PIECE(4); }
    }
    IDS();
    ARGP(pwr, 4); ARGP(pwd, 7); ARGP(pout, 8);
    const int kh = w >> 2, rt2 = rb * 4 + (w & 3);
    const unsigned R = rt2 * 32 + kh * 16 + (l & 15);
    const unsigned to = (rt2 * 64 + ct * 4 + kh) * 1024 + l * 16;
    const unsigned rmo = ct * 64 + ((g & 1) ? 12 + 4 * g : 4 * g);
    f32x2 v[8], wv[8];
    float wsum;
    {
      u32x4 wa[2], wrp[2], res[2];
#pragma unroll
      for (int j = 0; j < 2; ++j) {
        wrp[j] = *(const GAS u32x4*)(pwr + rmo + 32 * j);
        wa[j] = *(const GAS u32x4*)(pwr + l * 8 + j * 512);
      }
      {
        f32x2 t[8];
        unpack8v(wa[0], t); unpack8v(wa[1], t + 4);
        const f32x2 s = ((t[0] + t[1]) + (t[2] + t[3])) + ((t[4] + t[5]) + (t[6] + t[7]));
        wsum = sum64(hadd(s));
        unpack8v(trade16(wrp[0]), wv); unpack8v(trade16(wrp[1]), wv + 4);
        asm volatile("" : "+v"(wsum), "+v"(wv[0]), "+v"(wv[1]), "+v"(wv[2]), "+v"(wv[3]), "+v"(wv[4]), "+v"(wv[5]), "+v"(wv[6]), "+v"(wv[7]));
      }
      unsigned bt[4];
#pragma unroll
      for (int n = 0; n < 4; ++n) {
        bt[n] = lbase + 32768 + 65536 + kh * 4096 + (4 * g + ((l & 15) >> 2)) * 128 + (l & 3) * 8 + ((n ^ (2 * (g & 1) + ((l & 15) >> 3))) << 5);
        asm volatile("" : "+v"(bt[n]));
      }
      if (w == 0) poll_flags(rs, (l & 31) * SO, F_O + (it * 8 + (rb >> 1)) * 32 * SO, T);
      else if (it == 0 && w < 3) poll_flags2(rs, F_XN + (((w - 1) * 4 + g) * RX + bi % RX) * 256 + (l & 15) * 16, T);
      else if (it == 0 && w == 3) poll_flags(rs, ((l >> 3) * 32 + h) * cmax(8 * SV, 128) + (l & 7) * SV, F_KV, T);
      asm volatile("s_barrier" ::: "memory");
      const unsigned abase = OFF_O + it * (4u << 20) + (rt2 * 64 + 32 * kh) * 1024;
      const unsigned av0 = l * 16 + kh * 1024, av1 = l * 16 + (kh ^ 1) * 1024;
      u32x4 af[WB][2];
#define O_LOAD(s_) { ld16h<(wo_k(it, s_) & 1) * 2048>(af[(s_) % WB][0], rs, av0, abase + (wo_k(it, s_) >> 1) * 4096); ld16h<(wo_k(it, s_) & 1) * 2048>(af[(s_) % WB][1], rs, av1, abase + (wo_k(it, s_) >> 1) * 4096); }
      static_for<WR>([&](auto c_) __attribute__((always_inline)) { O_LOAD(decltype(c_)::value); });
      if constexpr (it == 1) { ld16h<0>(res[0], rs, g * 16, OFF_XNEW + ct * 128); ld16h<64>(res[1], rs, g * 16, OFF_XNEW + ct * 128); }
      SB();
      f32x4 ac[2][4];
#pragma unroll
      for (int a = 0; a < 2; ++a)
#pragma unroll
        for (int n = 0; n < 4; ++n) ac[a][n] = f32x4{0.f, 0.f, 0.f, 0.f};
      u32x4 bfr[2][4];
#define WO_RD(buf, s, n)                                                                             \
  {                                                                                                  \
    const unsigned o_ = bt[n] + (wo_k(it, s) & 7) * 8192;                                             \
    frag_t f_;                                                                                       \
    f_.hf.a = trabs(o_); f_.hf.b = trabs(o_ + 2048);                                                 \
    bfr[buf][n] = f_.v;                                                                              \
  }
#pragma unroll
      for (int n = 0; n < 4; ++n) WO_RD(0, 0, n);
      static_for<16>([&](auto sc_) __attribute__((always_inline)) {
        constexpr int s = decltype(sc_)::value, cur = s & 1;
        pin_ac(ac);
        if constexpr (s == 1) lds_count<0>(lbase + L_QC);
        if constexpr (s == (it == 0 ? 3 : 11)) lds_look(smem + L_QC, 64);
        if constexpr (s < 15 && ((wo_k(it, s) ^ wo_k(it, s + 1)) & 8)) {
#pragma unroll
          for (int n = 0; n < 4; ++n) { bt[n] += (wo_k(it, s) & 8) ? -65536 : 65536; pin1(bt[n]); }
        }
        if constexpr (s < WB - WR) O_LOAD(WR + 2 * s);
        if constexpr (it == 0 && s == 8) lds_count<8>(lbase + L_QC);
        if constexpr (it == 0 && s == WDS) {
          DRAIN();
          if ((w >> 2) == WDW && ffn_on) { lds_look(smem + L_QC + 8, 16); WD_DMA_OF(w); WD_DMA_OF(w ^ 4); }
        }
        if constexpr (it == 1 || s < WDS) wait_vm<wo_at(it, s) - wo_pos(it, s)>();
        pin2(af[s % WB][0], af[s % WB][1]);
        if constexpr (it == 1 && s == 9) pin2(res[0], res[1]);
        SB();
#pragma unroll
        for (int n = 0; n < 4; ++n) {
          ac[0][n] = mfma16(bfr[cur][n], af[s % WB][0], ac[0][n]);
          ac[1][n] = mfma16(bfr[cur][n], af[s % WB][1], ac[1][n]);
          if (s + 1 < 16) WO_RD(cur ^ 1, s + 1, n);
          if (it == 0 && s == 8) {
            const u32x2 t = *(const LDS_AS u32x2*)(smem + w * 4096 + (l & 15) * 128 + (((2 * n + (g >> 1)) ^ ((l & 15) >> 1)) << 4) + (g & 1) * 8);
            res[n >> 1][2 * (n & 1)] = t[0]; res[n >> 1][2 * (n & 1) + 1] = t[1];
          }
          if (s == 9) {
            if (it == 1 && n == 0) { res[0] = R == sel0 ? res[0] : x2p[0]; res[1] = R == sel0 ? res[1] : x2p[1]; }

            v[2 * n] = f32x2{lo16(res[n >> 1][2 * (n & 1)]), hi16(res[n >> 1][2 * (n & 1)])};
            v[2 * n + 1] = f32x2{lo16(res[n >> 1][2 * (n & 1) + 1]), hi16(res[n >> 1][2 * (n & 1) + 1])};
            pin2f(v[2 * n], v[2 * n + 1]);
          }
          SB();
        }
        if constexpr (s < WB - WR) { O_LOAD(WR + 2 * s + 1); }
        else if constexpr (s + WB < 16) { O_LOAD(s + WB); }
        SB();
      });
#undef WO_RD
#undef O_LOAD
#define XCH_AT(v) ((v) == 7 ? 155648 : (v) * 4096)
#pragma unroll
      for (int n = 0; n < 4; ++n) *(f32x4*)(smem + XCH_AT(w) + n * 1024 + l * 16) = ac[1][n];
#pragma unroll
      for (int n = 0; n < 4; ++n) { v[2 * n] += f32x2{ac[0][n][0], ac[0][n][1]}; v[2 * n + 1] += f32x2{ac[0][n][2], ac[0][n][3]}; }
      BAR();
#pragma unroll
      for (int n = 0; n < 4; ++n) {
        const f32x4 o = *(const f32x4*)(smem + XCH_AT(w ^ 4) + n * 1024 + l * 16);
        v[2 * n] += f32x2{o[0], o[1]}; v[2 * n + 1] += f32x2{o[2], o[3]};
      }
    }
    {
      if (it == 0 && X1F) { st16(pack8v(v), rs, to, OFF_XN + (4u << 20)); st16(pack8v(v + 4), rs, to + 2048, OFF_XN + (4u << 20)); }
      SB();
      const f32x2 s = ((v[0] + v[1]) + (v[2] + v[3])) + ((v[4] + v[5]) + (v[6] + v[7]));
      f32x2 dp = v[0] * wv[0], q = v[0] * v[0];
#pragma unroll
      for (int j = 1; j < 8; ++j) { dp = __builtin_elementwise_fma(v[j], wv[j], dp); q = __builtin_elementwise_fma(v[j], v[j], q); }
      float mt = hadd(s), qq = hadd(q), dot = hadd(dp);
      add_g3(mt, qq, dot);
      const unsigned so = OFF_STAT + it * 524288 + rb * 32768, lrow = R & 127;
      if (g == 0) st16(unpoison4(__builtin_bit_cast(u32x4, f32x4{mt, qq, dot, 0.f})), rs, ct * 2048 + lrow * 16, so);
      SB();
      if (it == 0 && !X1F) { st16(pack8v(v), rs, to, OFF_XN + (4u << 20)); st16(pack8v(v + 4), rs, to + 2048, OFF_XN + (4u << 20)); }
      SB();
      if (it == 0) wait_vm<X1F>();
      if constexpr (it == 0) {
        volatile LDS_AS u32x2* in = (volatile LDS_AS u32x2*)(smem + L_XC);
        if (l == 0) in[w] = T;
        const u32x2 t = in[l & 7];
        if (__all(t[0] == T[0] && t[1] == T[1]) && l < RX) st8(T, rs, (l * 32 + (rb & 1) * 16 + ct) * SX, F_X1 + (rb >> 1) * RX * 32 * SX);
      }
      if (it == 1) { if (ffn_on) { WD_DMA_OF(w); } }
      if (it == 0) {
#pragma unroll
        for (int j = 0; j < 9; ++j) *(u32x4*)(smem + L_RING + (j / 3) * STAGE + (w * 3 + j % 3) * 1024 + l * 16) = keep[j];
      }
      __builtin_amdgcn_s_sleep(it == 0 ? SDI0 : SDIR);
      char* const skey = smem + (it == 0 ? L_SKEY : L_XC);
      u32x4 sp[4];
      ld16h<0>(sp[0], rs, g * 8192 + lrow * 16, so); ld16h<2048>(sp[1], rs, g * 8192 + lrow * 16, so);
      ld16h<0>(sp[2], rs, g * 8192 + lrow * 16, so + 4096); ld16h<2048>(sp[3], rs, g * 8192 + lrow * 16, so + 4096);
      u32x2 xf;
      unsigned k_;
      asm volatile("" : "=v"(xf), "=v"(k_));
      if (it == 0 && w == 0) { ld8h(xf, rs, (l & 31) * SX, F_X1 + (p * RX + (xcd >> 1) % RX) * 32 * SX); ld4h<0>(k_, rs, (l & 15) * SK, OFF_KEY + (bid & 15) * cmax(16 * SK, 128)); }
      DRAIN();
      asm volatile("" : "+v"(xf), "+v"(k_)::"memory");
      PINV4(sp);
      const RowFin r = fin_key(rs, T, g * 8192 + lrow * 16, so, skey, l, w, R, wsum, sp);
      if constexpr (it == 0) {
        if (w == 0) {
          const unsigned need = 0x30003u << (((PROT() + 5) & 7) * 2);
          unsigned m = (unsigned)__builtin_amdgcn_ballot_w64(xf[0] == T[0] && xf[1] == T[1]);
          for (unsigned spins = 0; (m & need) != need;) {
            if (spin_fail(spins, rs, T)) { m = ~0u; break; }
            const u32x2 f = ld8(rs, F_X1 + ((p * RX + (xcd >> 1) % RX) * 32 + (l & 31)) * SX, 0);
            k_ = key_load(rs, l, 0, bid & 15);
            m = (unsigned)__builtin_amdgcn_ballot_w64(f[0] == T[0] && f[1] == T[1]);
          }
          k_ = umax16(k_ == POISON ? 0u : k_);
          if (l == 0) *(LDS_AS u32x4*)(skey + 32) = u32x4{m, k_, POISON, 0u};
        }
        if (tid == 192) *(LDS_AS u32x4*)(smem + L_QC) = u32x4{0u, 0u, 0u, 0u};
      }
      BAR();
      if constexpr (it == 0) {
        PRE_LOAD(1, PROT() + 5, 0, 4);
        const u32x2 mk = *(const LDS_AS u32x2*)(skey + 32);
        xm0 = __builtin_amdgcn_readfirstlane(mk[0]); kg0 = __builtin_amdgcn_readfirstlane(mk[1]);
      }
      const unsigned own = fin_cand(rs, skey, it, g, rb, ct, r, v);
      if constexpr (it == 0) kg0 = max(kg0, (unsigned)__builtin_amdgcn_readfirstlane(own));
      fin_x2(r, v);
      if constexpr (it == 0) {
        x2p[0] = pack8v(v); x2p[1] = pack8v(v + 4);
        asm volatile("" : "+v"(x2p[0]), "+v"(x2p[1]));
        if ((w & 3) * 2 + (w >> 2) == ct >> 1) {
          const float f = 1.0f + r.rd, rd2 = ln_rd(r.var * f * f), mb2 = rbf1(__builtin_fmaf(r.mean - r.mb, r.rd, r.mean));
          if (g < 2 && ((l >> 3) & 1) == (ct & 1))
            st4(min(__builtin_bit_cast(unsigned, g == 0 ? f * rd2 : -__builtin_fmaf(r.mb, r.rd, mb2) * __builtin_amdgcn_rcpf(f)), POISON - 1), rs, OFF_AB + 16384 + R * 4 + g * 8192, 0);
        }
      }
    }
    if (it == 1) {
      const u32x4 op0 = trade16(pack8v(v)), op1 = trade16(pack8v(v + 4));
#define OUT_STORE()                                                                                 \
  if (R != key_row(key)) {                                                                          \
    __builtin_amdgcn_raw_buffer_store_b128(op0, rout, (R * 1024 + rmo) * 2, 0, SC1);                  \
    __builtin_amdgcn_raw_buffer_store_b128(op1, rout, (R * 1024 + rmo + 32) * 2, 0, SC1);             \
  }
      u32x4 xv[4];
      unsigned guess = 0;
      {
        __builtin_amdgcn_s_sleep(GSL);
        const unsigned k_ = key_load(rs, l, 1, bid & 15);
        guess = ffn_xoff(__builtin_amdgcn_readfirstlane(umax16(k_ == POISON ? 0u : k_)), 1, w);
        row_load(rs, g, guess, xv);
      }
      if (w == 0) {
        const unsigned k_ = key_wait(rs, T, l, 1, bid & 15, key_load(rs, l, 1, bid & 15));
        if (l == 0) *(LDS_AS unsigned*)(smem + L_SKEY + 32) = k_;
      }
      DRAIN();
      PINV4(xv);
      BAR();
      const unsigned key = __builtin_amdgcn_readfirstlane(*(LDS_AS unsigned*)(smem + L_SKEY + 32));
      if (ffn_on) {
        f32x4 ga, ua;
        s16x4 dfr[8];
        ffn_d_read(smem, l, w, dfr);
        if (guess != ffn_xoff(key, 1, w)) ld4x16(xv, rs, g * 16, ffn_xoff(key, 1, w));
        ffn_gu(gv, uv, xv, ga, ua);
        ffn_gu_check(rs, T, ffn_xoff(key, 1, w), l, gv, uv, xv, ga, ua);
        ffn_gu_store(smem, l, w, ga, ua);
        BAR();
        f32x4 ya = {0.f, 0.f, 0.f, 0.f}, yb = ya;
        u32x2 hh = ffn_h(ffn_t_read(smem, l), l);
        ffn_d_mma(dfr, hh, ya, yb);
        if ((l & 15) < 8) ffn_d_store(rs, l, 4 * w, 1, slice, ya + yb);
        SB();
        OUT_STORE();
      } else {
        OUT_STORE();
        const unsigned G = bi;
        if (G == 0 && tid == 0) st8(T, rs, F_EP, 0);
        u32x4 pv[4];
        u32x2 cxn, cx1;
        __builtin_amdgcn_s_sleep(RSL);
        red_load(rs, tid, 1, G, key, pv, cxn, cx1);
        red_sum(rs, smem, T, tid, w, 1, G, key, pv, cxn, cx1);
        BAR();
        if (w == 0) {
          const u32x2 ov = red_final<8>(smem, l, key, cxn, cx1);
          if (l < 8) __builtin_amdgcn_raw_buffer_store_b64(ov, rout, (key_row(key) * 1024 + G * 32 + l * 4) * 2, 0, 0);
        }
      }
#undef OUT_STORE
    }
  };
  layer(std::integral_constant<int, 0>{});
  layer(std::integral_constant<int, 1>{});
}

extern "C" void kernel_launch(void* const* d_in, const int* in_sizes, int n_in, void* d_out, int out_size,
                              void* d_ws, size_t ws_size, hipStream_t stream) {
  (void)hipFuncSetAttribute((const void*)mega_kernel, hipFuncAttributeMaxDynamicSharedMemorySize, LDS_BYTES);
  mega_kernel<<<256, 512, LDS_BYTES, stream>>>((const bf16*)d_in[0], (char*)d_ws, (const bf16*)d_in[1], (const bf16*)d_in[2], (const bf16*)d_in[4],
                                               (const bf16*)d_in[5], (const bf16*)d_in[6], (const bf16*)d_in[7], (bf16*)d_out);
}
```

```cpp
#include <hip/hip_runtime.h>
#include <stdint.h>
#include <utility>

typedef __bf16 bf16;
typedef __bf16 bf16x8 __attribute__((ext_vector_type(8)));
typedef __bf16 bf16x2 __attribute__((ext_vector_type(2)));
typedef float f32x4 __attribute__((ext_vector_type(4)));
typedef float f32x2 __attribute__((ext_vector_type(2)));
typedef short s16x4 __attribute__((ext_vector_type(4)));
typedef unsigned int u32x4 __attribute__((ext_vector_type(4)));
typedef unsigned int u32x2 __attribute__((ext_vector_type(2)));
#define LDS_AS __attribute__((address_space(3)))
#define GAS __attribute__((address_space(1)))
typedef GAS unsigned gu32;
typedef GAS unsigned long long gu64;
#define RLX_AGENT __ATOMIC_RELAXED, __HIP_MEMORY_SCOPE_AGENT
typedef __amdgpu_buffer_rsrc_t rsrc_t;

constexpr int cmax(int a, int b) { return a > b ? a : b; }
constexpr int F_ = 3584;
constexpr int SC1 = 16;

constexpr int RX = 4;
constexpr unsigned F_EP = 0, F_TMO = 128;
constexpr unsigned F_XN = 4096;
#define STR 8, 8, 4, 8
constexpr unsigned STV[4] = {STR}, SO = STV[0], SX = STV[1], SK = STV[2], SV = STV[3];
constexpr unsigned F_O = 8u << 20;
constexpr unsigned F_X1 = F_O + 131072;
constexpr unsigned OFF_KEY = F_O + 393216;
constexpr unsigned F_KV = F_O + 524288;
#define KVF(it, i) ((i) * 128 + (it) * 64)
#define OFL(h) (((h) >> 3) * 128 + ((h) & 7) * 8)
#define OFB(it, p) ((p) * 512 + (it) * 64)
constexpr unsigned OFF_AB = 147456;
constexpr unsigned OFF_XNEW = 262144;
constexpr unsigned OFF_CS = 294912;
constexpr unsigned OFF_FX = 393216;
constexpr unsigned OFF_CA = 524288;
constexpr unsigned OFF_CB = 655360;
constexpr unsigned OFF_STAT = 1u << 20;
constexpr unsigned OFF_PART = 3u << 20;
constexpr unsigned OFF_XN = 8u << 20;
constexpr unsigned OFF_O = 16u << 20;
constexpr unsigned OFF_KV = 32u << 20;
constexpr unsigned OFF_WI = 52u << 20;
#define REUSE 0
constexpr unsigned O_STEP = (REUSE & 1) ? 0 : 4u << 20, KV_STEP = (REUSE & 2) ? 0 : 8u << 20;
constexpr unsigned WS_BYTES = 58u << 20;
constexpr unsigned POISON = 0xFFFFFFFFu;

constexpr int L_ST0 = 147456, L_CNT = 147968, L_CSL = 31872, L_FXL = 32256, L_K = 0, L_V = 32768, L_RING = 65536, L_XCH = 0, L_DN = 32768, L_TOUCH0 = 149504, L_RDY = 31744, L_QC = 31808, LDS_BYTES = 163840;
constexpr int STAGE = 24576;
#define SLOT(t) ((t) & 3)

template <typename F, int... I> __device__ __forceinline__ void static_for_impl(F&& f, std::integer_sequence<int, I...>) {
  (f(std::integral_constant<int, I>{}), ...);
}
template <int N, typename F> __device__ __forceinline__ void static_for(F&& f) { static_for_impl(f, std::make_integer_sequence<int, N>{}); }
__device__ __forceinline__ unsigned pack2(float a, float b) {
  f32x2 v = {a, b};
  return __builtin_bit_cast(unsigned, __builtin_convertvector(v, bf16x2));
}
__device__ __forceinline__ float asf(unsigned u) { return __builtin_bit_cast(float, u); }
__device__ __forceinline__ float lo16(unsigned u) { return __builtin_bit_cast(float, u << 16); }
__device__ __forceinline__ float hi16(unsigned u) { return __builtin_bit_cast(float, u & 0xffff0000u); }
__device__ __forceinline__ u32x4 pack8(const f32x4 a, const f32x4 b) {
  u32x4 o;
  o[0] = pack2(a[0], a[1]); o[1] = pack2(a[2], a[3]); o[2] = pack2(b[0], b[1]); o[3] = pack2(b[2], b[3]);
  return o;
}
__device__ __forceinline__ f32x4 mfma16(u32x4 a, u32x4 b, f32x4 c) {
  return __builtin_amdgcn_mfma_f32_16x16x32_bf16(__builtin_bit_cast(bf16x8, a), __builtin_bit_cast(bf16x8, b), c, 0, 0, 0);
}
__device__ __forceinline__ f32x4 mfma16k16(s16x4 a, s16x4 b, f32x4 c) { return __builtin_amdgcn_mfma_f32_16x16x16bf16_1k(a, b, c, 0, 0, 0); }
template <int CTRL> __device__ __forceinline__ float dppf(float v) {
  return __builtin_bit_cast(float, __builtin_amdgcn_update_dpp(0, __builtin_bit_cast(int, v), CTRL, 0xf, 0xf, true));
}
template <int CTRL> __device__ __forceinline__ unsigned dppu(unsigned v) {
  return (unsigned)__builtin_amdgcn_update_dpp(0, (int)v, CTRL, 0xf, 0xf, true);
}
__device__ __forceinline__ float add_x16(float v) {
  auto r = __builtin_amdgcn_permlane16_swap(__builtin_bit_cast(unsigned, v), __builtin_bit_cast(unsigned, v), false, false);
  return __builtin_bit_cast(float, (unsigned)r[0]) + __builtin_bit_cast(float, (unsigned)r[1]);
}
__device__ __forceinline__ float add_x32(float v) {
  auto r = __builtin_amdgcn_permlane32_swap(__builtin_bit_cast(unsigned, v), __builtin_bit_cast(unsigned, v), false, false);
  return __builtin_bit_cast(float, (unsigned)r[0]) + __builtin_bit_cast(float, (unsigned)r[1]);
}
__device__ __forceinline__ float max_x16(float v) {
  auto r = __builtin_amdgcn_permlane16_swap(__builtin_bit_cast(unsigned, v), __builtin_bit_cast(unsigned, v), false, false);
  return fmaxf(__builtin_bit_cast(float, (unsigned)r[0]), __builtin_bit_cast(float, (unsigned)r[1]));
}
__device__ __forceinline__ float max_x32(float v) {
  auto r = __builtin_amdgcn_permlane32_swap(__builtin_bit_cast(unsigned, v), __builtin_bit_cast(unsigned, v), false, false);
  return fmaxf(__builtin_bit_cast(float, (unsigned)r[0]), __builtin_bit_cast(float, (unsigned)r[1]));
}
__device__ __forceinline__ unsigned umax_x16(unsigned v) {
  auto r = __builtin_amdgcn_permlane16_swap(v, v, false, false);
  return max((unsigned)r[0], (unsigned)r[1]);
}
__device__ __forceinline__ unsigned umax_x32(unsigned v) {
  auto r = __builtin_amdgcn_permlane32_swap(v, v, false, false);
  return max((unsigned)r[0], (unsigned)r[1]);
}
__device__ __forceinline__ float sum_hi8(float v) {
  v += dppf<0x128>(v);
  v = add_x16(v);
  return add_x32(v);
}
__device__ __forceinline__ float sum32(float v) {
  v += dppf<0xB1>(v);
  v += dppf<0x4E>(v);
  v += dppf<0x124>(v);
  v += dppf<0x128>(v);
  return add_x16(v);
}
__device__ __forceinline__ float sum64(float v) {
  v += dppf<0xB1>(v);
  v += dppf<0x4E>(v);
  v += dppf<0x124>(v);
  v += dppf<0x128>(v);
  v = add_x16(v);
  return add_x32(v);
}

#define BAR() asm volatile("s_waitcnt lgkmcnt(0)\n\ts_barrier" ::: "memory")
#define DRAIN() asm volatile("s_waitcnt vmcnt(0)" ::: "memory")
#define SB() __builtin_amdgcn_sched_barrier(0)
template <int N> __device__ __forceinline__ void wait_vm() { asm volatile("s_waitcnt vmcnt(%0)" ::"n"(N) : "memory"); }

__device__ __forceinline__ void dma16(rsrc_t rs, unsigned voff, unsigned soff, unsigned lds) {
  asm volatile("s_mov_b32 m0, %0\n\ts_nop 0\n\tbuffer_load_dwordx4 %1, %2, %3 offen lds" ::"s"(lds), "v"(voff), "s"(rs), "s"(soff) : "memory");
}
template <int OFF> __device__ __forceinline__ void dma16s(rsrc_t rs, unsigned voff, unsigned soff, unsigned lds) {
  asm volatile("s_mov_b32 m0, %0\n\ts_nop 0\n\tbuffer_load_dwordx4 %1, %2, %3 offen offset:%4 sc1 lds" ::"s"(lds), "v"(voff), "s"(rs), "s"(soff), "n"(OFF) : "memory");
}
__device__ __forceinline__ void touch(unsigned& d, const GAS bf16* ptr) { asm volatile("global_load_dword %0, %1, off" : "+v"(d) : "v"(ptr) : "memory"); }
__device__ __forceinline__ void ldrows(u32x4 (&d)[4], rsrc_t rx, unsigned voff, unsigned soff) {
  asm volatile("buffer_load_dwordx4 %0, %4, %5, %6 offen\n\tbuffer_load_dwordx4 %1, %4, %5, %6 offen offset:512\n\t"
               "buffer_load_dwordx4 %2, %4, %5, %6 offen offset:1024\n\tbuffer_load_dwordx4 %3, %4, %5, %6 offen offset:1536"
               : "=&v"(d[0]), "=&v"(d[1]), "=&v"(d[2]), "=&v"(d[3]) : "v"(voff), "s"(rx), "s"(soff) : "memory");
}
__device__ __forceinline__ void pin4(f32x2* v) { asm volatile("" : "+v"(v[0]), "+v"(v[1]), "+v"(v[2]), "+v"(v[3])); }
__device__ __forceinline__ void touchb(unsigned& d, rsrc_t rs, unsigned vo) { asm volatile("buffer_load_dword %0, %1, %2, 0 offen sc1" : "+v"(d) : "v"(vo), "s"(rs) : "memory"); }
__device__ __forceinline__ float rbf1(float x) { return __builtin_bit_cast(float, pack2(0.f, x)); }
__device__ __forceinline__ void vswap(unsigned& a, unsigned& b) { asm volatile("v_swap_b32 %0, %1" : "+v"(a), "+v"(b)); }
__device__ __forceinline__ void vswap(float& a, float& b) { asm volatile("v_swap_b32 %0, %1" : "+v"(a), "+v"(b)); }
template <typename V, int N> __device__ __forceinline__ void vswapn(V& a, V& b) {
#pragma unroll
  for (int i = 0; i < N; ++i) { auto x = a[i], y = b[i]; vswap(x, y); a[i] = x; b[i] = y; }
}
__device__ __forceinline__ u32x4 ld16(rsrc_t rs, unsigned voff, unsigned soff) { return __builtin_amdgcn_raw_buffer_load_b128(rs, voff, soff, SC1); }
template <int OFF> __device__ __forceinline__ void ld16h(u32x4& d, rsrc_t rs, unsigned voff, unsigned soff) {
  asm volatile("buffer_load_dwordx4 %0, %1, %2, %3 offen offset:%4 sc1" : "=v"(d) : "v"(voff), "s"(rs), "s"(soff), "n"(OFF) : "memory");
}
template <int I, int OFF> __device__ __forceinline__ void ld16w(u32x4& d, rsrc_t rs, unsigned voff, unsigned soff) {
  if constexpr (I == 0) asm volatile("buffer_load_dwordx4 %0, %1, %2, %3 offen offset:%4 sc1" : "={v[128:131]}"(d) : "v"(voff), "s"(rs), "s"(soff), "n"(OFF) : "memory");
  else if constexpr (I == 1) asm volatile("buffer_load_dwordx4 %0, %1, %2, %3 offen offset:%4 sc1" : "={v[132:135]}"(d) : "v"(voff), "s"(rs), "s"(soff), "n"(OFF) : "memory");
  else if constexpr (I == 2) asm volatile("buffer_load_dwordx4 %0, %1, %2, %3 offen offset:%4 sc1" : "={v[136:139]}"(d) : "v"(voff), "s"(rs), "s"(soff), "n"(OFF) : "memory");
  else if constexpr (I == 3) asm volatile("buffer_load_dwordx4 %0, %1, %2, %3 offen offset:%4 sc1" : "={v[140:143]}"(d) : "v"(voff), "s"(rs), "s"(soff), "n"(OFF) : "memory");
  else if constexpr (I == 4) asm volatile("buffer_load_dwordx4 %0, %1, %2, %3 offen offset:%4 sc1" : "={v[144:147]}"(d) : "v"(voff), "s"(rs), "s"(soff), "n"(OFF) : "memory");
  else if constexpr (I == 5) asm volatile("buffer_load_dwordx4 %0, %1, %2, %3 offen offset:%4 sc1" : "={v[148:151]}"(d) : "v"(voff), "s"(rs), "s"(soff), "n"(OFF) : "memory");
  else if constexpr (I == 6) asm volatile("buffer_load_dwordx4 %0, %1, %2, %3 offen offset:%4 sc1" : "={v[152:155]}"(d) : "v"(voff), "s"(rs), "s"(soff), "n"(OFF) : "memory");
  else if constexpr (I == 7) asm volatile("buffer_load_dwordx4 %0, %1, %2, %3 offen offset:%4 sc1" : "={v[156:159]}"(d) : "v"(voff), "s"(rs), "s"(soff), "n"(OFF) : "memory");
  else if constexpr (I == 8) asm volatile("buffer_load_dwordx4 %0, %1, %2, %3 offen offset:%4 sc1" : "={v[160:163]}"(d) : "v"(voff), "s"(rs), "s"(soff), "n"(OFF) : "memory");
  else if constexpr (I == 9) asm volatile("buffer_load_dwordx4 %0, %1, %2, %3 offen offset:%4 sc1" : "={v[164:167]}"(d) : "v"(voff), "s"(rs), "s"(soff), "n"(OFF) : "memory");
  else if constexpr (I == 10) asm volatile("buffer_load_dwordx4 %0, %1, %2, %3 offen offset:%4 sc1" : "={v[168:171]}"(d) : "v"(voff), "s"(rs), "s"(soff), "n"(OFF) : "memory");
  else if constexpr (I == 11) asm volatile("buffer_load_dwordx4 %0, %1, %2, %3 offen offset:%4 sc1" : "={v[172:175]}"(d) : "v"(voff), "s"(rs), "s"(soff), "n"(OFF) : "memory");
}
template <int K> __device__ __forceinline__ void pin_w(u32x4& a, u32x4& b) {
  if constexpr (K == 0) asm volatile("" : "+{v[128:131]}"(a), "+{v[132:135]}"(b)::"memory");
  else if constexpr (K == 1) asm volatile("" : "+{v[136:139]}"(a), "+{v[140:143]}"(b)::"memory");
  else if constexpr (K == 2) asm volatile("" : "+{v[144:147]}"(a), "+{v[148:151]}"(b)::"memory");
  else if constexpr (K == 3) asm volatile("" : "+{v[152:155]}"(a), "+{v[156:159]}"(b)::"memory");
  else if constexpr (K == 4) asm volatile("" : "+{v[160:163]}"(a), "+{v[164:167]}"(b)::"memory");
  else if constexpr (K == 5) asm volatile("" : "+{v[168:171]}"(a), "+{v[172:175]}"(b)::"memory");
}
__device__ __forceinline__ void ld8h(u32x2& d, rsrc_t rs, unsigned voff, unsigned soff) {
  asm volatile("buffer_load_dwordx2 %0, %1, %2, %3 offen sc1" : "=v"(d) : "v"(voff), "s"(rs), "s"(soff) : "memory");
}
__device__ __forceinline__ void ld16u(u32x4& d, rsrc_t rs, unsigned voff, unsigned soff) {
  asm volatile("buffer_load_dwordx4 %0, %1, %2, %3 offen sc1" : "+v"(d) : "v"(voff), "s"(rs), "s"(soff) : "memory");
}
template <int OFF> __device__ __forceinline__ void ld4h(unsigned& d, rsrc_t rs, unsigned voff, unsigned soff) {
  asm volatile("buffer_load_dword %0, %1, %2, %3 offen offset:%4 sc1" : "=v"(d) : "v"(voff), "s"(rs), "s"(soff), "n"(OFF) : "memory");
}
__device__ __forceinline__ void ld4x16(u32x4 (&d)[4], rsrc_t rs, unsigned voff, unsigned soff) {
  asm volatile("buffer_load_dwordx4 %0, %4, %5, %6 offen sc1\n\tbuffer_load_dwordx4 %1, %4, %5, %6 offen offset:64 sc1\n\t"
               "buffer_load_dwordx4 %2, %4, %5, %6 offen offset:128 sc1\n\tbuffer_load_dwordx4 %3, %4, %5, %6 offen offset:192 sc1\n\ts_waitcnt vmcnt(0)"
               : "=&v"(d[0]), "=&v"(d[1]), "=&v"(d[2]), "=&v"(d[3]) : "v"(voff), "s"(rs), "s"(soff) : "memory");
}
__device__ __forceinline__ u32x2 ld8(rsrc_t rs, unsigned voff, unsigned soff) { return __builtin_amdgcn_raw_buffer_load_b64(rs, voff, soff, SC1); }
__device__ __forceinline__ void st16(u32x4 v, rsrc_t rs, unsigned voff, unsigned soff) { __builtin_amdgcn_raw_buffer_store_b128(v, rs, voff + soff, 0, SC1); }
__device__ __forceinline__ void st8(u32x2 v, rsrc_t rs, unsigned voff, unsigned soff) { __builtin_amdgcn_raw_buffer_store_b64(v, rs, voff, soff, SC1); }

__device__ __forceinline__ unsigned ld4(rsrc_t rs, unsigned voff, unsigned soff) { return __builtin_amdgcn_raw_buffer_load_b32(rs, voff, soff, SC1); }
__device__ __forceinline__ void st4(unsigned v, rsrc_t rs, unsigned voff, unsigned soff) { __builtin_amdgcn_raw_buffer_store_b32(v, rs, voff, soff, SC1); }

constexpr unsigned kSpinCap = 1u << 16;
__device__ __forceinline__ bool spin_fail(unsigned& spins, rsrc_t rs, u32x2 T) {
  asm volatile("" ::: "memory");
  ++spins;
  if (spins > kSpinCap) { st8(T, rs, 0, F_TMO); return true; }
  if ((spins & 255u) == 0u) {
    const u32x2 v = ld8(rs, 0, F_TMO);
    if (v[0] == T[0] && v[1] == T[1]) return true;
  }
  __builtin_amdgcn_s_sleep(3);
  return false;
}
__device__ __forceinline__ void poll_flags2(rsrc_t rs, unsigned voff, u32x2 T) {
  for (unsigned spins = 0;;) {
    const u32x4 v = ld16(rs, voff, 0);
    if (__all(v[0] == T[0] && v[1] == T[1] && v[2] == T[0] && v[3] == T[1])) break;
    if (spin_fail(spins, rs, T)) break;
  }
  asm volatile("" ::: "memory");
  __builtin_amdgcn_fence(__ATOMIC_ACQUIRE, "wavefront");
}
template <int SL = 0> __device__ __forceinline__ void poll_flags(rsrc_t rs, unsigned voff, unsigned soff, u32x2 T) {
  for (unsigned spins = 0;;) {
    const u32x2 v = ld8(rs, voff, soff);
    if (__all(v[0] == T[0] && v[1] == T[1])) break;
    if (spin_fail(spins, rs, T)) break;
    if (SL) __builtin_amdgcn_s_sleep(SL);
  }
  asm volatile("" ::: "memory");
  __builtin_amdgcn_fence(__ATOMIC_ACQUIRE, "wavefront");
}

__device__ __forceinline__ unsigned toff(unsigned R, unsigned ks, unsigned g) {
  return (((R >> 5) * 64 + ks * 2 + ((R >> 4) & 1)) << 10) + ((g * 16 + (R & 15)) << 4);
}
__device__ __forceinline__ void unpack8v(const u32x4 u, f32x2* v) {
#pragma unroll
  for (int j = 0; j < 4; ++j) v[j] = f32x2{lo16(u[j]), hi16(u[j])};
}
__device__ __forceinline__ u32x4 pack8v(const f32x2* v) {
  u32x4 o;
#pragma unroll
  for (int j = 0; j < 4; ++j) o[j] = pack2(v[j][0], v[j][1]);
  return o;
}
__device__ __forceinline__ f32x2 rbf1v(f32x2 v) { return f32x2{rbf1(v[0]), rbf1(v[1])}; }
__device__ __forceinline__ float ln_rd(float var) { return __builtin_amdgcn_rcpf(rbf1(__builtin_amdgcn_sqrtf(rbf1(rbf1(var) + 1e-5f)))); }
template <int N> __device__ __forceinline__ void ln_apply(float mb, float rd, const f32x2* v, f32x2* y) {
  const f32x2 mb2 = {mb, mb}, rd2 = {rd, rd};
#pragma unroll
  for (int j = 0; j < N; ++j) y[j] = rbf1v(rbf1v(v[j] - mb2) * rd2);
}
__device__ __forceinline__ float hadd(f32x2 v) {
  float a = v[0];
  asm volatile("" : "+v"(a));
  return a + v[1];
}
__device__ __forceinline__ float add_g(float v) { return add_x32(add_x16(v)); }
__device__ __forceinline__ void add_g3(float& x, float& y, float& z) {
  auto a = __builtin_amdgcn_permlane16_swap(__builtin_bit_cast(unsigned, x), __builtin_bit_cast(unsigned, y), false, false);
  auto b = __builtin_amdgcn_permlane16_swap(__builtin_bit_cast(unsigned, z), __builtin_bit_cast(unsigned, z), false, false);
  const float r = asf(a[0]) + asf(a[1]), t = asf(b[0]) + asf(b[1]);
  auto c = __builtin_amdgcn_permlane32_swap(__builtin_bit_cast(unsigned, r), __builtin_bit_cast(unsigned, r), false, false);
  auto d = __builtin_amdgcn_permlane32_swap(__builtin_bit_cast(unsigned, t), __builtin_bit_cast(unsigned, t), false, false);
  const float u = asf(c[0]) + asf(c[1]);
  z = asf(d[0]) + asf(d[1]);
  auto e = __builtin_amdgcn_permlane16_swap(__builtin_bit_cast(unsigned, u), __builtin_bit_cast(unsigned, u), false, false);
  x = asf(e[0]); y = asf(e[1]);
}
__device__ __forceinline__ u32x4 trade16(u32x4 v) {
  auto a = __builtin_amdgcn_permlane16_swap(v[0], v[2], false, false);
  auto b = __builtin_amdgcn_permlane16_swap(v[1], v[3], false, false);
  return u32x4{(unsigned)a[0], (unsigned)b[0], (unsigned)a[1], (unsigned)b[1]};
}
__device__ __forceinline__ unsigned umax4(u32x4 v) { return max(max(v[0], v[1]), max(v[2], v[3])); }
__device__ __forceinline__ u32x2 unpoison2(u32x2 v) { return u32x2{min(v[0], POISON - 1), min(v[1], POISON - 1)}; }
__device__ __forceinline__ u32x4 unpoison4(u32x4 v) {
  return u32x4{min(v[0], POISON - 1), min(v[1], POISON - 1), min(v[2], POISON - 1), min(v[3], POISON - 1)};
}

typedef union { struct { s16x4 a, b; } hf; u32x4 v; } frag_t;
__device__ __forceinline__ s16x4 trread(const char* smem, unsigned off) {
  return __builtin_amdgcn_ds_read_tr16_b64_v4i16((LDS_AS s16x4*)(smem + off));
}
__device__ __forceinline__ s16x4 trabs(unsigned addr) { return __builtin_amdgcn_ds_read_tr16_b64_v4i16((LDS_AS s16x4*)(size_t)addr); }

constexpr int WA = 6;
#define WRV 3
#define WBV 4
constexpr int WB = WBV, WR = WRV;
#define CH 0, 4, 7, 8, 13, 15, 20, 22, 27, 29
constexpr int CHV[10] = {CH};
constexpr int FSL = CHV[1], FS0 = CHV[2], FS0D = CHV[3], FS1 = CHV[4], FS1S = CHV[5];
constexpr int FXA = CHV[6], FXB = CHV[7], FXL = CHV[8], FXS = CHV[9];
constexpr int FXT = FXB >> 2;
#define C0 27, 3, 16, 19, 22, 23, -1, 5, 4
constexpr int C0V[9] = {C0};
constexpr int IMG = C0V[0], CSD = C0V[1];
constexpr int ERQ = C0V[2], CSL = C0V[3], CSS = C0V[4];
constexpr int RWL = C0V[6], RWS = C0V[7], POI = C0V[8];
#define WTS 99
#define WTG 2
#define PBS 10
#define KP0 26
#define PS0 8
constexpr int EIN = cmax(CSD, cmax(RWS, POI)) + 3;
constexpr int ABS = 27;
constexpr int abl(int it) { return it ? 23 : C0V[5]; }
constexpr int XU = 3, XL = 2;
constexpr int x_slot(int u) { return u % XU == 0 ? 0 : u % XU == 1 ? 32768 : 49152; }
#define TCH 19, 7, 4, 27
constexpr int TCV[4] = {TCH};
constexpr int TS0 = TCV[0], TSN = TCV[1], TW = TCV[2], TST = TCV[3];
constexpr int QL = 26;
#define FDG 0
#define XSL 4
#define FW0 4
#define FW1 4
#define X1F 1
#define RWG 0
#define CSG 4
#define RXCD 5
#define TWR TW
#define WDS (17 - WB)
#define WDW 1
#define OSL0 0
#define OSL1 0
#define OPS 0
#define KVS 0
#define SDI0 16
#define SLP 24, 14, 56, 3
constexpr int SLV[4] = {SLP};
constexpr int SDIR = SLV[0], GSL = SLV[1], RSL = SLV[2], KSL = SLV[3];
constexpr bool tile_piece(int s) { return s >= 3 && ((s - 3) >> 2) + 3 < 8 && ((s - 3) & 3) < 3; }
constexpr int a_req(int s, int n) {
  if (!(n & 1)) return -1;
  if (s <= 1) return 8 + 3 * s + (n >> 1);
  return n < 5 && s + 5 < 32 ? 2 * (s + 5) + (n >> 1) : -1;
}
constexpr int vm_n(int it, int s, int n) {
  int c = 0;
  if (n == 0) c += tile_piece(s);
  if (n == 4) c += (s == abl(it)) + (it ? (s == FSL ? 5 : 0) : (s == ERQ) + (s >= IMG && s < IMG + 3));
  if (!it) c += (s == WTS && n == WTG) + (s < 4 && n == 2) + (s == 0 && (n == 0 || n == 4)) + ((n == 3 || n == 5) && s + XL + XU < 32);
  else c += (a_req(s, n) >= 0) + ((s == 0 || s == 4) && n == 2);
  return c;
}
constexpr int vm_at(int it, int s, int n) {
  int c = it ? 8 : 14;
  for (int u = 0; u < 6 * s + n; ++u) c += vm_n(it, u / 6, u % 6);
  return c;
}
constexpr int vm_a(int it, int k) {
  if (k < 4) return 2 * k + 2;
  for (int u = 0; u < 192; ++u)
    if (a_req(u / 6, u % 6) == 2 * k + 1) return vm_at(it, (u + 1) / 6, (u + 1) % 6);
  return 0;
}
constexpr int vm_tile(int it, int t) { return t >= 3 ? vm_at(it, 4 * t - 7, 1) : t == 1 ? vm_at(0, 0, 5) : vm_at(0, 3, 3); }
constexpr int vm_x(int u) { return u == 2 ? 9 : u < XU + XL ? 12 + 2 * (u - XU) : vm_at(0, u - XU - XL + 1, 0); }

constexpr int wo_k(int it, int s) { return it || s < 4 ? 15 - s : s < 8 ? 7 - s : 19 - s; }
constexpr int wo_pos(int it, int s) { return 2 * s + 2 + (s >= WR ? 2 * it : 0); }
constexpr int wo_at(int it, int s) { return 2 * it + (s < WB - WR ? 2 * WR + 2 + 4 * s : s < 16 - WB ? 2 * (2 * WB - WR) + 2 * (s - WB + WR) : 32); }
template <int OFF> __device__ __forceinline__ void lds_count(unsigned ad) {
  asm volatile("s_mov_b64 exec, 1\n\tds_add_u32 %0, %1 offset:%2\n\ts_mov_b64 exec, -1" ::"v"(ad), "v"(1u), "n"(OFF) : "memory");
}
__device__ __forceinline__ void lds_look(char* c, unsigned want) {
  while ((unsigned)__builtin_amdgcn_readfirstlane(__hip_atomic_load((LDS_AS unsigned*)c, __ATOMIC_RELAXED, __HIP_MEMORY_SCOPE_WORKGROUP)) < want) {}
  asm volatile("" ::: "memory");
}
__device__ __forceinline__ void pin1(unsigned& a) { asm volatile("" : "+v"(a)); }
__device__ __forceinline__ void pin2(u32x4& a, u32x4& b) { asm volatile("" : "+v"(a), "+v"(b)::"memory"); }
__device__ __forceinline__ void pin2s(float& a, float& b) { asm volatile("" : "+v"(a), "+v"(b)); }
template <int OFF> __device__ __forceinline__ void lds2(u32x4& a, u32x4& b, unsigned ad) {
  asm volatile("ds_read_b128 %0, %2 offset:%3\n\tds_read_b128 %1, %2 offset:%4" : "=&v"(a), "=&v"(b) : "v"(ad), "n"(OFF), "n"(OFF + 1024) : "memory");
}
template <int N> __device__ __forceinline__ void lds_wait(u32x4& a, u32x4& b) { asm volatile("s_waitcnt lgkmcnt(%2)" : "+v"(a), "+v"(b) : "n"(N) : "memory"); }
__device__ __forceinline__ void pin2f(f32x2& a, f32x2& b) { asm volatile("" : "+v"(a), "+v"(b)); }
__device__ __forceinline__ void pin_ac(f32x4 (&ac)[2][4]) {
  asm volatile("" : "+{v[176:179]}"(ac[0][0]), "+{v[180:183]}"(ac[0][1]), "+{v[184:187]}"(ac[0][2]), "+{v[188:191]}"(ac[0][3]),
                    "+{v[192:195]}"(ac[1][0]), "+{v[196:199]}"(ac[1][1]), "+{v[200:203]}"(ac[1][2]), "+{v[204:207]}"(ac[1][3]));
}

constexpr int L_XC = LDS_BYTES - 64, L_SKEY = 32704, L_SRED = 4096, L_SFIN = 20480;
__device__ __forceinline__ unsigned key_load(rsrc_t rs, int l, int L, int rep) { return ld4(rs, (l & 15) * SK, OFF_KEY + (L * 16 + rep) * cmax(16 * SK, 128)); }
__device__ __forceinline__ unsigned umax16(unsigned k) {
  k = max(k, dppu<0xB1>(k));
  k = max(k, dppu<0x4E>(k));
  k = max(k, dppu<0x141>(k));
  return max(k, dppu<0x140>(k));
}
__device__ __forceinline__ unsigned key_row(unsigned key) { return (0xffffu - (key & 0xffffu)) & 2047u; }
__device__ __forceinline__ unsigned key_wait(rsrc_t rs, u32x2 T, int l, int L, int rep, unsigned kv) {
  if (!__all(kv != POISON)) {
    unsigned spins = 0;
    do {
      if (spin_fail(spins, rs, T)) break;
      if (L == 0) __builtin_amdgcn_s_sleep(KSL);
      kv = key_load(rs, l, L, rep);
    } while (!__all(kv != POISON));
  }
  return __builtin_amdgcn_readfirstlane(umax16(kv));
}
__device__ __forceinline__ unsigned ffn_xoff(unsigned key, int L, int w) { return OFF_CA + L * 32768 + (key_row(key) >> 7) * 2048 + w * 256; }
__device__ __forceinline__ void row_load(rsrc_t rs, int g, unsigned xo, u32x4 (&xv)[4]) {
  ld16h<0>(xv[0], rs, g * 16, xo); ld16h<64>(xv[1], rs, g * 16, xo); ld16h<128>(xv[2], rs, g * 16, xo); ld16h<192>(xv[3], rs, g * 16, xo);
}
template <int SL = 0> __device__ __forceinline__ void ffn_xwait(rsrc_t rs, u32x2 T, unsigned xo, int l, u32x4 (&xv)[4]) {
#define XV_OK() __all(max(max(umax4(xv[0]), umax4(xv[1])), max(umax4(xv[2]), umax4(xv[3]))) != POISON)
  if (!XV_OK()) {
    unsigned spins = 0;
    do {
      if (spin_fail(spins, rs, T)) break;
      if (SL) __builtin_amdgcn_s_sleep(SL);
#pragma unroll
      for (int i = 0; i < 4; ++i) xv[i] = ld16(rs, (l >> 4) * 16 + i * 64, xo);
    } while (!XV_OK());
  }
#undef XV_OK
}
__device__ __forceinline__ void ffn_gu(const u32x4 (&gv)[4], const u32x4 (&uv)[4], const u32x4 (&xv)[4], f32x4& ga, f32x4& ua) {
  ga = mfma16(gv[0], xv[0], f32x4{0.f, 0.f, 0.f, 0.f}); ua = mfma16(uv[0], xv[0], f32x4{0.f, 0.f, 0.f, 0.f});
#pragma unroll
  for (int i = 1; i < 4; ++i) { ga = mfma16(gv[i], xv[i], ga); ua = mfma16(uv[i], xv[i], ua); }
}
__device__ __forceinline__ void ffn_gu_check(rsrc_t rs, u32x2 T, unsigned xo, int l, const u32x4 (&gv)[4], const u32x4 (&uv)[4], u32x4 (&xv)[4], f32x4& ga, f32x4& ua) {
  if (__builtin_expect(__any(ga[0] != ga[0]), 0)) {
    ffn_xwait(rs, T, xo, l, xv);
    ffn_gu(gv, uv, xv, ga, ua);
  }
}
__device__ __forceinline__ void ffn_gu_store(char* smem, int l, int w, f32x4 ga, f32x4 ua) {
  LDS_AS f32x4* sred = (LDS_AS f32x4*)(smem + L_SRED);
  if ((l & 15) == 0) { sred[w * 8 + (l >> 4)] = ga; sred[w * 8 + 4 + (l >> 4)] = ua; }
}
__device__ __forceinline__ f32x4 ffn_t_read(char* smem, int l) { return ((const LDS_AS f32x4*)(smem + L_SRED))[(l & 7) * 8 + ((l >> 3) & 1) * 4 + (l >> 4)]; }
__device__ __forceinline__ void ffn_d_read(char* smem, int l, int blk, s16x4 (&dfr)[8]) {
#pragma unroll
  for (int i = 0; i < 8; ++i) dfr[i] = trread(smem, L_DN + blk * 4096 + (l >> 4) * 1024 + ((l & 15) >> 2) * 256 + i * 32 + (l & 3) * 8);
}
__device__ __forceinline__ float ffn_d_sum(float t) { t += dppf<0xB1>(t); t += dppf<0x4E>(t); return t + dppf<0x141>(t); }
__device__ __forceinline__ u32x2 ffn_h(f32x4 t, int l) {
#pragma unroll
  for (int e = 0; e < 4; ++e) t[e] = ffn_d_sum(t[e]);
  const float a = (l & 1) ? t[1] : t[0], b = (l & 1) ? t[3] : t[2], s = (l & 2) ? b : a;
  const float gg = rbf1(s), uu = rbf1(dppf<0x128>(s));
  const float h = rbf1(gg * __builtin_amdgcn_rcpf(1.0f + __expf(-gg))) * uu;
  const unsigned h0 = pack2(h, dppf<0x55>(h)), h1 = pack2(dppf<0xAA>(h), dppf<0xFF>(h));
  return (l & 15) == 0 ? u32x2{h0, h1} : u32x2{0u, 0u};
}
__device__ __forceinline__ void ffn_d_mma(const s16x4 (&dfr)[8], u32x2& hh, f32x4& ya, f32x4& yb) {
#pragma unroll
  for (int i = 0; i < 8; ++i) {
    if (i & 1) yb = mfma16k16(dfr[i], __builtin_bit_cast(s16x4, hh), yb);
    else ya = mfma16k16(dfr[i], __builtin_bit_cast(s16x4, hh), ya);
    hh = u32x2{dppu<0x111>(hh[0]), dppu<0x111>(hh[1])};
  }
}
__device__ __forceinline__ void ffn_d_store(rsrc_t rs, int l, int G0, int L, int slice, f32x4 y) {
  const unsigned c = l & 15;
  st16(unpoison4(__builtin_bit_cast(u32x4, y)), rs, (G0 + (c >> 1)) * 32768 + slice * 128 + (c & 1) * 64 + (l >> 4) * 16, OFF_PART + L * (1u << 20));
}
__device__ __forceinline__ void red_load(rsrc_t rs, int tid, int L, unsigned G, unsigned key, u32x4 (&pv)[4], u32x2& cxn, u32x2& cx1) {
#pragma unroll
  for (int k = 0; k < 4; ++k) pv[k] = ld16(rs, min(tid + 512 * k, 1791) * 16, OFF_PART + L * (1u << 20) + G * 32768);
  cxn = ld8(rs, (tid & 3) * 16 + (tid & 4) * 2, OFF_CA + L * 32768 + (key_row(key) >> 7) * 2048 + G * 64);
  cx1 = ld8(rs, (tid & 3) * 16 + (tid & 4) * 2, OFF_CB + L * 32768 + (key_row(key) >> 7) * 2048 + G * 64);
}
__device__ __forceinline__ void red_sum(rsrc_t rs, char* smem, u32x2 T, int tid, int w, int L, unsigned G, unsigned key,
                                        u32x4 (&pv)[4], u32x2& cxn, u32x2& cx1) {
#define PV_OK() __all(max(max(max(umax4(pv[0]), umax4(pv[1])), max(umax4(pv[2]), umax4(pv[3]))), max(max(cxn[0], cxn[1]), max(cx1[0], cx1[1]))) != POISON)
  if (!PV_OK()) {
    unsigned spins = 0;
    do {
      if (spin_fail(spins, rs, T)) break;
      red_load(rs, tid, L, G, key, pv, cxn, cx1);
    } while (!PV_OK());
  }
#undef PV_OK
  f32x4 s = (__builtin_bit_cast(f32x4, pv[0]) + __builtin_bit_cast(f32x4, pv[1])) + __builtin_bit_cast(f32x4, pv[2]);
  if (w < 4) s += __builtin_bit_cast(f32x4, pv[3]);
  ((LDS_AS f32x4*)(smem + L_SFIN))[tid] = s;
}
template <int NW> __device__ __forceinline__ u32x2 red_final(char* smem, int l, unsigned key, u32x2 cxn, u32x2 cx1) {
  const LDS_AS f32x4* sf = (const LDS_AS f32x4*)(smem + L_SFIN) + l;
  f32x4 s = (sf[0] + sf[64]) + (sf[128] + sf[192]);
  if constexpr (NW == 8) s += (sf[256] + sf[320]) + (sf[384] + sf[448]);
#pragma unroll
  for (int j = 0; j < 4; ++j) s[j] = sum_hi8(s[j]);
  const unsigned k16 = key >> 16;
  const float rmax = __builtin_bit_cast(float, ((k16 & 0x8000u) ? (k16 & 0x7fffu) : (~k16 & 0xffffu)) << 16);
  const float sig = rbf1(__builtin_amdgcn_rcpf(1.0f + __expf(-rmax)));
  const float xn4[4] = {lo16(cxn[0]), hi16(cxn[0]), lo16(cxn[1]), hi16(cxn[1])};
  const float x14[4] = {lo16(cx1[0]), hi16(cx1[0]), lo16(cx1[1]), hi16(cx1[1])};
  float o[4];
#pragma unroll
  for (int j = 0; j < 4; ++j) o[j] = x14[j] + rbf1(xn4[j] + rbf1(rbf1(s[j]) * sig));
  return u32x2{pack2(o[0], o[1]), pack2(o[2], o[3])};
}

struct RowFin { float mean, var, mb, rd; unsigned key; };
__device__ __forceinline__ void fin_wait(rsrc_t rs, u32x2 T, unsigned voff, unsigned so, u32x4 (&sp)[4]) {
#define ST_OK() __all(max(max(umax4(sp[0]), umax4(sp[1])), max(umax4(sp[2]), umax4(sp[3]))) != POISON)
  if (!ST_OK()) {
    unsigned spins = 0;
    do {
      if (spin_fail(spins, rs, T)) break;
#pragma unroll
      for (int i = 0; i < 4; ++i) sp[i] = ld16(rs, voff + i * 2048, so);
    } while (!ST_OK());
  }
#undef ST_OK
}
__device__ __forceinline__ RowFin fin_key(rsrc_t rs, u32x2 T, unsigned voff, unsigned so, char* skey, int l, int w, unsigned R, float wsum, u32x4 (&sp)[4]) {
  RowFin r;
#define ST_SUM() ((__builtin_bit_cast(f32x4, sp[0]) + __builtin_bit_cast(f32x4, sp[1])) + (__builtin_bit_cast(f32x4, sp[2]) + __builtin_bit_cast(f32x4, sp[3])))
  f32x4 sf = ST_SUM();
  const float z = (sf[0] + sf[1]) + sf[2];
  if (__any(z != z)) {
    fin_wait(rs, T, voff, so, sp);
    sf = ST_SUM();
  }
#undef ST_SUM
  float s0 = sf[0], s1 = sf[1], s2 = sf[2];
  add_g3(s0, s1, s2);
  r.mean = s0 * (1.0f / 1024.0f);
  r.var = fmaxf(__builtin_fmaf(-r.mean, r.mean, s1 * (1.0f / 1024.0f)), 0.f);
  r.mb = rbf1(r.mean); r.rd = ln_rd(r.var);
  const unsigned rb16 = pack2((s2 - r.mb * wsum) * r.rd, 0.f) & 0xffffu;
  r.key = min((((rb16 & 0x8000u) ? (~rb16 & 0xffffu) : (rb16 | 0x8000u)) << 16) | (0xffffu - R), POISON - 1);
  const unsigned wk = umax16(r.key);
  if (l == 0) ((LDS_AS unsigned*)skey)[w] = wk;
  return r;
}
__device__ __forceinline__ unsigned fin_cand(rsrc_t rs, char* skey, int L, int g, int rb, int ct, const RowFin& r, const f32x2 (&v)[8]) {
  const u32x4 k0 = *(const LDS_AS u32x4*)skey, k1 = *(const LDS_AS u32x4*)(skey + 16);
  const unsigned best = max(umax4(k0), umax4(k1));
  if (r.key == best) {
    const unsigned co = L * 32768 + rb * 2048 + ct * 128 + g * 16;
    f32x2 t[8], rd2 = {r.rd, r.rd}, of2 = {-r.mb * r.rd, -r.mb * r.rd};
    asm volatile("" : "+v"(rd2), "+v"(of2));
#pragma unroll
    for (int j = 0; j < 8; ++j) t[j] = __builtin_elementwise_fma(v[j], rd2, of2);
    st16(unpoison4(pack8v(t)), rs, co, OFF_CA); st16(unpoison4(pack8v(t + 4)), rs, co + 64, OFF_CA);
    if (g == 0) st4(best, rs, OFF_KEY + (L * 16 + ct) * cmax(16 * SK, 128) + rb * SK, 0);
    st16(unpoison4(pack8v(v)), rs, co, OFF_CB); st16(unpoison4(pack8v(v + 4)), rs, co + 64, OFF_CB);
  }
  return best;
}
__device__ __forceinline__ void fin_x2(const RowFin& r, f32x2 (&v)[8]) {
  f32x2 sc2 = {1.0f + r.rd, 1.0f + r.rd}, of2 = {-r.mb * r.rd, -r.mb * r.rd};
  asm volatile("" : "+v"(sc2), "+v"(of2));
#pragma unroll
  for (int j = 0; j < 8; ++j) v[j] = __builtin_elementwise_fma(v[j], sc2, of2);
}

extern "C" __device__ unsigned long long dispatch_id() __asm("llvm.amdgcn.dispatch.id");

extern "C" __global__ __launch_bounds__(512, 2) void mega_kernel(const bf16* x, char* ws, const bf16* wqkv, const bf16* wo, const bf16* wr,
                                                                 const bf16* wg, const bf16* wu, const bf16* wd, bf16* out) {
  extern __shared__ __attribute__((aligned(16))) char smem[];
  const int w0_ = __builtin_amdgcn_readfirstlane(threadIdx.x >> 6);
  const unsigned lbase = (unsigned)(size_t)(LDS_AS char*)smem;
  {
    unsigned long long pc;
    unsigned len;
    asm volatile("s_getpc_b64 %0\n\ts_mov_b32 %1, .Ltext_end-mega_kernel\n\t.subsection 1\n.Ltext_end:\n\t.subsection 0" : "=s"(pc), "=s"(len));
    const unsigned o = ((blockIdx.x >> 3) * 64 + (threadIdx.x & 63)) * 128;
    if (w0_ == 7 && o < len)
      asm volatile("s_mov_b32 m0, %0\n\ts_nop 0\n\tglobal_load_lds_dword %1, %2" ::"s"(lbase + L_TOUCH0), "v"(o), "s"(pc & ~127ull) : "memory");
  }
  asm volatile("" : "+s"(x), "+s"(ws), "+s"(wqkv));
#define HGRP() ((xcd & 2) + (xcd >> 2))
#define IDS()                                                                                         \
  int tid = threadIdx.x, w = w0_, bid = blockIdx.x;                                                   \
  asm volatile("" : "+v"(tid));                                                                       \
  asm volatile("" : "+s"(w));                                                                         \
  asm volatile("" : "+s"(bid));                                                                       \
  const int l = tid & 63, xcd = bid & 7, bi = bid >> 3;                                               \
  const int p = (xcd & 1) * 4 + (bi & 3), h = HGRP() * 8 + (bi >> 2);         \
  const int rb = 2 * xcd + (bi & 1), ct = bi >> 1;                               \
  const int slice = (xcd - (xcd > RXCD)) * 32 + bi;                                            \
  const bool ffn_on = xcd != RXCD;                                                                    \
  const int g = l >> 4;                                                                               \
  const int rt = w < 4 ? w : 11 - w;          \
  KARGS();                                                                                            \
  (void)p; (void)h; (void)rb; (void)ct; (void)slice; (void)ffn_on; (void)g; (void)rt;
#define PROT() (2 * (p & 3) + (p >> 2))
  const rsrc_t rs = __builtin_amdgcn_make_buffer_rsrc((void*)ws, 0, WS_BYTES, 0x00020000);
  typedef const void* const __attribute__((address_space(4)))* karg_t;
#define KARGS() karg_t ka = (karg_t)__builtin_amdgcn_kernarg_segment_ptr(); asm volatile("" : "+s"(ka))
#define ARGP(name, i) const GAS bf16* name = (const GAS bf16*)ka[i]; asm volatile("" : "+s"(name))
#define RSRC(ptr, bytes) __builtin_amdgcn_make_buffer_rsrc((void*)(ptr), 0, bytes, 0x00020000)
#define rwq RSRC(pwq, 1024 * 3072 * 2)
#define rwo RSRC(pwo, 1024 * 1024 * 2)
#define rwg RSRC(pwg, 1024 * F_ * 2)
#define rwu RSRC(pwu, 1024 * F_ * 2)
#define rwd RSRC(pwd, 1024 * F_ * 2)
#define rout RSRC(pout, 2048 * 1024 * 2)

#define WQ_VOFF() ((l >> 2) * 6144 + (((l & 3) ^ ((((l >> 4) ^ (l >> 5)) & 1) << 1)) << 4))
#define WQ_PIECE(t, j, dst) dma16(rwq, wqv, ((((t) + rot) & 7) * 128 + ((j) & 7) * 16) * 6144 + ((j) >> 3) * 2048 + h * 64, lbase + (dst) + (j) * 1024)
#define WQ_BOFF(dt) ((g >> 1) * 1024 + (8 * (g & 1) + ((l & 15) >> 2)) * 64 + (l & 3) * 8 + (((dt) ^ (g & 1)) << 5))
#define X_REQ(u, hf) dma16(rx, xv[hf], (p * 256 + rt * 32 + ((u) & 1) * 16 + (hf) * 8) * 2048 + ((((u) >> 2) + rot) & 7) * 256 + (((u) >> 1) & 1) * 128, lbase + rt * 2048 + x_slot(u) + (hf) * 1024)
#define WO_PIECE(j) dma16(rwo, wov, ((w >> 2) * 64 + (w & 3) + 4 * (j)) * 16384 + ct * 128, lbase + 32768 + (j) * 8192 + w * 1024)
#define WD_DMA_OF(v) _Pragma("unroll") for (int i = 0; i < 4; ++i)                                  \
      dma16(rwd, (l >> 4) * 2048 + (l & 15) * 16, (unsigned)(slice * 16 + 4 * i) * 2048 + (v) * 256, lbase + L_DN + (v) * 4096 + i * 1024)
#define FFN_DMA(m) dma16((m) & 1 ? rwu : rwg, fnv, (unsigned)(((4 * w + ((m) >> 1)) * 32) * F_ + slice * 16) * 2, lbase + 65536 + (m) * 8192 + w * 1024)
#define POISON_BIG(L)                                                                                \
  if (w >= 2) {                                                                                      \
    unsigned pz1 = POISON;                                                                           \
    asm volatile("" : "+v"(pz1));                                                                    \
    st16(u32x4{pz1, pz1, pz1, pz1}, rs, (w >= 4 ? OFF_PART + (L) * (1u << 20) + bid * 4096 - 4096 : OFF_STAT + (L) * 524288 + (p * 32 + h) * 2048 - 2048) + tid * 16, 0); \
  }
  u32x4 gv[4], uv[4];
  u32x4 keep[9];
  u32x4 pre[4][2];
#define PRE_LOAD(it_, rot_, s0, s1)                                                                  \
  static_for<(s1) - (s0)>([&](auto c_) __attribute__((always_inline)) {                              \
    constexpr int s = (s0) + decltype(c_)::value;                                                    \
    const unsigned ab_ = OFF_XN + (it_) * (4u << 20) + (((rot_) + (s >> 2)) & 7) * 8192 + ((s >> 1) & 1) * 4096; \
    ld16w<2 * s, (s & 1) * 2048>(pre[s][0], rs, l * 16, ab_ + (p * 8 + rt) * 65536);                         \
    ld16w<2 * s + 1, (s & 1) * 2048 + 1024>(pre[s][1], rs, l * 16, ab_ + (p * 8 + rt) * 65536);                 \
  })
  u32x2 T;
  const GAS bf16* tsrc;
  unsigned xv[2], xa[2];
  u32x2 ep;
  u32x4 rows0[4];
#define X_READ(u, d0, d1) { d0 = *(const LDS_AS u32x4*)(size_t)(xa[0] + x_slot(u)); d1 = *(const LDS_AS u32x4*)(size_t)(xa[1] + x_slot(u)); }
  const rsrc_t rx = RSRC(x, 2048 * 1024 * 2);
  {
    IDS();
    const int rot = PROT();
    const bf16* pwq = wqkv;
    const unsigned wqv = WQ_VOFF();
    xv[0] = (l >> 3) * 2048 + (((l & 7) ^ (l >> 4)) << 4); xv[1] = xv[0] ^ 64;
#define WQ_KMAJ(q) (((q) % 3) * 8 + (q) / 3)
    WQ_PIECE(0, WQ_KMAJ(w), L_RING);
    X_REQ(0, 0); X_REQ(0, 1); X_REQ(1, 0); X_REQ(1, 1);
    WQ_PIECE(0, WQ_KMAJ(8 + w), L_RING);
    asm volatile("s_barrier" ::: "memory");
    WQ_PIECE(0, WQ_KMAJ(16 + w), L_RING);
    if (w == 6 && bi == 0) {
      ARGP(pwr, 4);
      asm volatile("s_mov_b32 m0, %0\n\ts_nop 0\n\tbuffer_load_dword %1, %2, 0 offen lds" ::"s"(lbase + L_TOUCH0 + 256), "v"((l & 15) * 128), "s"(RSRC(pwr, 2048)) : "memory");
    }
    X_REQ(2, 0); X_REQ(2, 1);
    asm volatile("buffer_load_dwordx2 %0, off, %1, 0 sc1" : "=v"(ep) : "s"(rs) : "memory");
    if constexpr (RWL < 0) {
      asm volatile("" : "=v"(rows0[0]), "=v"(rows0[1]), "=v"(rows0[2]), "=v"(rows0[3]));
      if (w < 4) ldrows(rows0, rx, (l >> 5) * 2048 + (l & 31) * 16, (p * 256 + h * 8 + 2 * w) * 2048);
    }
    if constexpr (PBS < 0) { POISON_BIG(0); }
    {
      ARGP(pwo, 3); ARGP(pwg, 5); ARGP(pwu, 6);
      tsrc = (w < 4 ? pwg : pwu) + ((bi * 32 + ((tid & 255) >> 3)) * F_ + (slice >> 5) * 512 + (tid & 7) * 64);
      if (!ffn_on) tsrc = pwo + (bi * 512 + tid) * 64;
      asm volatile("" : "+v"(tsrc));
    }
#pragma unroll
    for (int kk = 0; kk < 2; ++kk) {
      const int r = l & 15;
      xa[kk] = lbase + rt * 2048 + (r >> 3) * 1024 + (r & 7) * 128 + (((4 * kk + g) ^ (r >> 1)) << 4);
      asm volatile("" : "+v"(xa[kk]));
    }
    if (tid == 192) *(LDS_AS u32x4*)(smem + L_QC) = u32x4{0u, 0u, 0u, 0u};
    wait_vm<4>();
    X_READ(0, pre[0][0], pre[1][0]); X_READ(1, pre[0][1], pre[1][1]);
    asm volatile("s_waitcnt lgkmcnt(0)\n\ts_barrier" : "+v"(pre[0][0]), "+v"(pre[0][1]), "+v"(pre[1][0]), "+v"(pre[1][1])::"memory");
    X_REQ(3, 0); X_REQ(3, 1); X_REQ(4, 0); X_REQ(4, 1);
  }
  unsigned sel0 = 0x7FFFFFFFu;
  u32x4 x2p[2];
  unsigned xm0 = 0, kg0 = 0;

  auto layer = [&](auto it_c) __attribute__((always_inline)) {
    constexpr int it = decltype(it_c)::value;
    f32x4 acc[2][6];
    f32x2 qs2[2];
    u32x4 k0, k1, v0, v1;
    unsigned tdum = 0;
    {
      IDS();
      bool aff = false;
      unsigned aq[2];
      u32x4 av[2];
#define PIN_ACC() asm volatile("" : "+{v[176:179]}"(acc[0][0]), "+{v[180:183]}"(acc[0][1]), "+{v[184:187]}"(acc[0][2]), "+{v[188:191]}"(acc[0][3]), "+{v[192:195]}"(acc[0][4]), "+{v[196:199]}"(acc[0][5]), \
                               "+{v[200:203]}"(acc[1][0]), "+{v[204:207]}"(acc[1][1]), "+{v[208:211]}"(acc[1][2]), "+{v[212:215]}"(acc[1][3]), "+{v[216:219]}"(acc[1][4]), "+{v[220:223]}"(acc[1][5]))
#define PINV4(x) asm volatile("" : "+v"(x[0]), "+v"(x[1]), "+v"(x[2]), "+v"(x[3])::"memory")
      auto qkv_loop = [&](auto it_c) __attribute__((always_inline)) {
        constexpr int IT = decltype(it_c)::value;
        const unsigned abase = OFF_XN + IT * (4u << 20) + (p * 8 + rt) * 65536;
        const int rot = PROT() + 5 * IT;
        unsigned fkey = 0, kv, bq[2];
        u32x4 fx[4], bv[2], af[WA][2];
        u32x2 cxn, cx1;
        f32x4 sa, sb, cs4[2];
        f32x2 b2[2];
#define AB_OFF() (OFF_AB + IT * 16384 + (p * 8 + rt) * 128)
#define A_LOAD1(k, a) ld16w<(k) % WA * 2 + (a), ((k) & 1) * 2048 + (a) * 1024>(af[(k) % WA][a], rs, l * 16, abase + ((((k) >> 2) + rot) & 7) * 8192 + (((k) >> 1) & 1) * 4096)
#pragma unroll
        for (int s = 0; s < 2 + 2 * IT; ++s) { af[s][0] = pre[s][0]; af[s][1] = pre[s][1]; }
        const bf16* pwq = wqkv;
        const int tstep = TS0 + bid % TSN;
        const unsigned wqv = WQ_VOFF();
#define QKV_DMA1(t, i)                                                                            \
  {                                                                                               \
    if constexpr (IT == 0) WQ_PIECE(t, 3 * w + (i), L_RING + SLOT(t) * STAGE);                    \
    else dma16s<(i) * 1024>(rs, l * 16, OFF_WI + (h * 8 + (((t) + rot) & 7)) * STAGE + w * 3072, lbase + L_RING + SLOT(t) * STAGE + w * 3072); \
  }
        unsigned bo[2][2];
#pragma unroll
        for (int dt = 0; dt < 2 - IT; ++dt) {
          bo[dt][0] = lbase + L_RING + (IT ? l * 16 : WQ_BOFF(dt)); bo[dt][1] = bo[dt][0] + 2 * STAGE;
          asm volatile("" : "+v"(bo[dt][0]), "+v"(bo[dt][1]));
        }
#define FRAG_T() (lbase + L_RING + (4 * g + ((l & 15) >> 2)) * 64 + (l & 3) * 8 + (((g ^ (g >> 1)) & 1) << 5))
        auto frag_t2 = [&](unsigned o) __attribute__((always_inline)) {
          frag_t t;
          t.hf.a = trabs(o); t.hf.b = trabs(o + 1024);
          return t.v;
        };
        unsigned ka[3];
        auto tile_sum = [&](u32x4 pt, int dst) __attribute__((always_inline)) {
          f32x4 c = __builtin_bit_cast(f32x4, pt);
#pragma unroll
          for (int e = 0; e < 4; ++e) { c[e] += dppf<0x124>(c[e]); c[e] += dppf<0x128>(c[e]); c[e] = add_x16(c[e]); }
          if ((l & 28) == 0) *(LDS_AS f32x4*)(smem + dst + (w * 2 + (l >> 5)) * 64 + (l & 3) * 16) = c;
        };
        frag_t bfr[6];
#define QKV_BO(s, n) (bo[(n) & 1][SLOT((s) >> 2) >> 1] + (SLOT((s) >> 2) & 1) * STAGE + ((n) >> 1) * 8192 + ((s) & 3) * 2048)
#define QKV_RDA(s, n)                                                                             \
  {                                                                                               \
    if constexpr (IT == 0) bfr[n].hf.a = trabs(QKV_BO(s, n));                                     \
    else bfr[n].v = *(const LDS_AS u32x4*)(size_t)(bo[0][SLOT((s) >> 2) >> 1] + (SLOT((s) >> 2) & 1) * STAGE + (((s) & 3) * 6 + (n)) * 1024); \
  }
#define QKV_RDB(s, n) { if constexpr (IT == 0) bfr[n].hf.b = trabs(QKV_BO(s, (n) ^ 1) + 256); }
#pragma unroll
        for (int n = 0; n < 6; ++n) { QKV_RDA(0, n); if (n < 5) QKV_RDB(0, n); }
#define XF_OFF() (l & 8 ? ((l & 7) * RX + bi % RX) * 256 + h * 8 : (p * RX + (xcd >> 1) % RX) * 256 + (4 * rt + (l & 3)) * 8)
#define XP_OFF() (OFF_XNEW + ((rot + FXT) & 7) * 256)
#define FP_OFF(i) (w == 3 ? OFF_XNEW + (i) * 1024 : OFF_FX + h * 3072 + w * 1024)
#define FX_OK(n) __all(((n) == 4 ? max(max(umax4(fx[0]), umax4(fx[1])), max(umax4(fx[2]), umax4(fx[3]))) : max(umax4(fx[0]), umax4(fx[1]))) != POISON)
#define SINCE(s0) (vm_at(IT, s, n) - vm_at(IT, s0, 5))
#define DEF4(x) asm volatile("" : "=v"(x[0]), "=v"(x[1]), "=v"(x[2]), "=v"(x[3]))
        LDS_AS unsigned* qcnt = (LDS_AS unsigned*)(smem + L_QC);
        unsigned qc;
        unsigned qadr = lbase + L_QC, qone = 1;
        asm volatile("" : "+v"(qadr), "+v"(qone));
#define count_in(i) asm volatile("s_mov_b64 exec, 1\n\tds_add_u32 %0, %1 offset:%2\n\ts_mov_b64 exec, -1" ::"v"(qadr), "v"(qone), "n"(4 * (i)) : "memory")
        auto count_wait = [&](int i, unsigned want) __attribute__((always_inline)) {
          if (__builtin_expect((unsigned)__builtin_amdgcn_readfirstlane(qc) < want, 0)) {
            while ((unsigned)__builtin_amdgcn_readfirstlane(__hip_atomic_load(qcnt + i, __ATOMIC_RELAXED, __HIP_MEMORY_SCOPE_WORKGROUP)) < want) {}
          }
          asm volatile("" ::: "memory");
        };
        unsigned long long xok = xm0;
        auto x1_need = [&](unsigned long long m) __attribute__((always_inline)) {
          if (__builtin_expect((xok & m) != m, 0)) {
            for (unsigned spins = 0;;) {
              const u32x2 f = ld8(rs, F_X1 + ((p * RX + (xcd >> 1) % RX) * 32 + (l & 31)) * SX, 0);
              xok = __builtin_amdgcn_ballot_w64(f[0] == T[0] && f[1] == T[1]);
              if ((xok & m) == m) break;
              if (spin_fail(spins, rs, T)) { xok = ~0ull; break; }
              __builtin_amdgcn_s_sleep(KSL);
            }
            asm volatile("" ::: "memory");
          }
        };
        auto gap = [&](auto sc_, auto nc_) __attribute__((always_inline)) {
          constexpr int s = decltype(sc_)::value, n = decltype(nc_)::value;
          {
            constexpr int ts = (s + 3) / 4, tc = (s + 2) / 4, t0 = 1;
            if constexpr (ts >= 3 && ts < 8 && s == 4 * ts - 3 && n == 2) { wait_vm<vm_at(IT, s, n) - vm_tile(IT, ts)>(); count_in(0); }
            if constexpr (!IT && tc >= t0 && tc < 3 && s == 4 * tc - 2 && n == 1) { wait_vm<vm_at(IT, s, n) - vm_tile(IT, tc)>(); count_in(0); }
            if constexpr (IT && ts >= t0 && ts < 3 && s == 4 * ts - 3 && n == 2) count_in(0);
            if constexpr (tc >= t0 && tc < 8 && s == 4 * tc - 2) {
              if constexpr (n == 3) qc = __hip_atomic_load(qcnt, __ATOMIC_RELAXED, __HIP_MEMORY_SCOPE_WORKGROUP);
              if constexpr (n == 5) count_wait(0, 8 * (tc - t0 + 1));
            }
          }
          if constexpr (n == 0 && tile_piece(s)) QKV_DMA1(((s - 3) >> 2) + 3, (s - 3) & 3);
          if constexpr (IT == 0) {
            if constexpr (s == 0 && !(n & 1)) QKV_DMA1(1, n >> 1);
            if constexpr (s >= 1 && s < 4 && n == 2) QKV_DMA1(2, s - 1);
            constexpr int u = s + XL;
            if constexpr (n == 1 && u < 32) {
              wait_vm<vm_at(0, s, n) - vm_x(u)>();
              X_READ(u, af[(u & ~1) % WA][u & 1], af[(u | 1) % WA][u & 1]);
            }
            if constexpr (n == 3 && u + XU < 32) { asm volatile("s_waitcnt lgkmcnt(%0)" ::"n"(s + 1 < QL ? 4 : 3) : "memory"); X_REQ(u + XU, 0); }
            if constexpr (n == 5 && u + XU < 32) X_REQ(u + XU, 1);
            if constexpr (s == WTS && n == WTG) {
              const int i = bi * 192 + w * 24 + min(l, 23);
              touch(tdum, (const GAS bf16*)pwq + (size_t)(i / 12 + 512 * (~xcd & 1)) * 3072 + ((i % 12) >> 2) * 1024 + (xcd >> 1) * 256 + (i & 3) * 64);
            }
            if constexpr (s == PBS && n == 0) { POISON_BIG(0); }
            if constexpr (s == 0 && n == 1) count_in(2);
            if constexpr (s == 0 && n == 3) qc = __hip_atomic_load(qcnt + 2, __ATOMIC_RELAXED, __HIP_MEMORY_SCOPE_WORKGROUP);
            if constexpr (s == 0 && n == 5) count_wait(2, 8);
            if constexpr (s == 1 && n == 1) {
              asm volatile("" : "+v"(ep)::"memory");
              const unsigned long long e = (((unsigned long long)(unsigned)__builtin_amdgcn_readfirstlane(ep[1]) << 32) | (unsigned)__builtin_amdgcn_readfirstlane(ep[0])) + 1ull + dispatch_id() * 0x9E3779B97F4A7C16ull;
              T = u32x2{(unsigned)e, (unsigned)(e >> 32)};
            }
            if constexpr (s == RWL && n == 4) {
              DEF4(fx);
              if (w < 4) ldrows(fx, rx, (l >> 5) * 2048 + (l & 31) * 16, (p * 256 + h * 8 + 2 * w) * 2048);
            }
            if constexpr (s == RWS && n == RWG) {
              if (w < 4) {
                if constexpr (RWL >= 0) wait_vm<SINCE(RWL < 0 ? 0 : RWL)>();
                else { fx[0] = rows0[0]; fx[1] = rows0[1]; fx[2] = rows0[2]; fx[3] = rows0[3]; }
                PINV4(fx);
                f32x2 sv[16];
#pragma unroll
                for (int i = 0; i < 4; ++i) unpack8v(fx[i], sv + 4 * i);
                f32x2 t0 = sv[0] + sv[1], t1 = sv[2] + sv[3];
#pragma unroll
                for (int j = 4; j < 16; j += 2) { t0 += sv[j]; t1 += sv[j + 1]; }
                const float m = sum32(hadd(t0 + t1)) * (1.0f / 1024.0f);
                f32x2 m2 = {m, m}, q0 = {0.f, 0.f}, q1 = {0.f, 0.f};
                asm volatile("" : "+v"(m2));
#pragma unroll
                for (int j = 0; j < 16; j += 2) {
                  const f32x2 d0 = sv[j] - m2, d1 = sv[j + 1] - m2;
                  q0 = __builtin_elementwise_fma(d0, d0, q0); q1 = __builtin_elementwise_fma(d1, d1, q1);
                }
                const float rd = ln_rd(sum32(hadd(q0 + q1)) * (1.0f / 1024.0f));
                if ((l & 30) == 0) st4(__builtin_bit_cast(unsigned, (l & 1) ? -rbf1(m) : rd), rs, (l >> 5) * 4 + (l & 1) * 8192, OFF_AB + (p * 256 + h * 8 + 2 * w) * 4);
              }
            }
            if constexpr (s == POI && n == 4) {
              if (w == 3) {
                unsigned pz1 = POISON;
                asm volatile("" : "+v"(pz1));
                const unsigned prb = 2 * p + (h >> 4), pct = h & 15, m = l - 32;
                static_assert(OFF_CB == OFF_CA + 131072, "poison addressing");
                if (l < 60) st16(u32x4{pz1, pz1, pz1, pz1}, rs, l < 32 ? OFF_CA + (l & 8) * 4096 + (l & 16) * 8192 + prb * 2048 + pct * 128 + (l & 7) * 16
                                                             : l < 56 ? OFF_FX + ((h * 6 + (m >> 2)) * 8 + p) * 64 + (l & 3) * 16
                                                                      : OFF_AB + 16384 + (p * 32 + h) * 32 + (l & 1) * 16 + (l & 2) * 4096, 0);
                if (l < 4) st4(pz1, rs, l < 2 ? OFF_XNEW + bid * 8 + l * 4 : OFF_KEY + ((l & 1) * 16 + pct) * cmax(16 * SK, 128) + prb * SK, 0);
              }
            }
            if constexpr (s == POI && n == 2) {
              if (w == 2 && l < 6) {
                unsigned f0 = F_KV + KVF(0, p * 32 + h), f1 = F_O + OFB(0, p) + OFL(h), f2 = F_X1 + ((rb >> 1) * RX * 32 + (l & 3) * 32 + (rb & 1) * 16 + ct) * SX;
                asm volatile("" : "+v"(f0), "+v"(f1), "+v"(f2));
                const unsigned fo = l == 5 ? f0 : l == 4 ? f1 : f2;
                touchb(tdum, rs, fo);
              }
            }
            static_assert(vm_x(EIN + XL) >= vm_at(0, EIN - 3, 5) && IMG >= 1 && CSD >= 1, "first flag");
            if constexpr (s == EIN && n == 1) { if (w < 4) count_in(1); }
            if constexpr (s == EIN + 2 && n == 0) qc = __hip_atomic_load(qcnt + 1, __ATOMIC_RELAXED, __HIP_MEMORY_SCOPE_WORKGROUP);
            if constexpr (s == EIN + 2 && n == 2) {
              if (w == 0) { count_wait(1, 4); if (l < RX) st8(T, rs, F_XN + (p * RX + l) * 256 + h * 8, 0); }
            }
            if constexpr (s == ERQ && n == 4) asm volatile("buffer_load_dwordx2 %0, %1, %2, 0 offen sc1" : "=v"(cxn) : "v"(XF_OFF() + F_XN), "s"(rs) : "memory");
            if constexpr (s == CSL && n == 2) {
              wait_vm<SINCE(ERQ)>();
              asm volatile("" : "+v"(cxn)::"memory");
              if (!__all(cxn[0] == T[0] && cxn[1] == T[1])) poll_flags(rs, XF_OFF(), F_XN, T);
            }
          } else {
#define X1_OFF() (F_X1 + ((p * RX + (xcd >> 1) % RX) * 32 + (l & 31)) * SX)
            if constexpr ((s == 0 || s == 4) && n == 2) asm volatile("buffer_load_dwordx2 %0, %1, %2, 0 offen sc1" : "=v"(cxn) : "v"(X1_OFF()), "s"(rs) : "memory");
            if constexpr ((s == 3 || s == 7) && n == 0) {
              wait_vm<vm_at(1, s, 1) - vm_at(1, s - 3, 3)>();
              asm volatile("" : "+v"(cxn)::"memory");
              xok |= __builtin_amdgcn_ballot_w64(cxn[0] == T[0] && cxn[1] == T[1]);
            }
            if constexpr (a_req(s, n) >= 0 && (a_req(s, n) & 7) == 0) x1_need(0x30003ull << (((rot + a_req(s, n) / 8) & 7) * 2));
          }
          if constexpr (IT && a_req(s, n) >= 0) A_LOAD1(a_req(s, n) / 2, a_req(s, n) & 1);
          if constexpr (IT == 0 && s == CSD && n == CSG) {
            if (w < 3) {
              unsigned a0 = FRAG_T() + SLOT(s >> 2) * STAGE + w * 8192, a1 = a0 ^ 32, one2 = 0x3F803F80u;
              asm volatile("" : "+v"(a0), "+v"(a1), "+v"(one2));
              const u32x4 o4 = {one2, one2, one2, one2};
              f32x4 y0 = {0.f, 0.f, 0.f, 0.f}, y1 = y0;
#pragma unroll
              for (int i = 0; i < 4; ++i) { y0 = mfma16(frag_t2(a0 + i * 2048), o4, y0); y1 = mfma16(frag_t2(a1 + i * 2048), o4, y1); }
              if ((l & 14) == 0) st16(__builtin_bit_cast(u32x4, (l & 1) ? y1 : y0), rs, g * 16 + (l & 1) * 512, OFF_CS + ((h * 6 + 2 * w) * 8 + p) * 64);
            }
          }
          if constexpr (!IT && s == CSL && n == 4) { asm volatile("" : "=v"(fx[0])); if (w < 3) ld16h<0>(fx[0], rs, l * 16, OFF_CS + h * 3072 + w * 1024); }
          if constexpr (!IT && s == CSS && n == 2) {
            if (w < 3) {
              wait_vm<SINCE(CSL)>();
              asm volatile("" : "+v"(fx[0])::"memory");
              tile_sum(fx[0], L_CSL);
            }
          }
          if constexpr (s == abl(IT) && n == 4) ld4h<0>(aq[0], rs, (l & 31) * 4 + (l >> 5) * 8192, AB_OFF());
          if constexpr (s == ABS && n == 0) {
            wait_vm<SINCE(abl(IT))>();
            asm volatile("" : "+v"(aq[0])::"memory");
            if constexpr (IT) {
              for (unsigned spins = 0; !__all(aq[0] != POISON) && !spin_fail(spins, rs, T);) {
                ld4h<0>(aq[0], rs, (l & 31) * 4 + (l >> 5) * 8192, AB_OFF());
                asm volatile("s_waitcnt vmcnt(0)" : "+v"(aq[0])::"memory");
              }
            }
            *(LDS_AS unsigned*)(smem + L_K + (8 + rt) * 2048 + l * 4) = aq[0];
          }
          if constexpr (s == ABS && n == 2) {
#pragma unroll
            for (int a = 0; a < 2; ++a) {
              const LDS_AS char* ab = (const LDS_AS char*)(smem + L_K + (8 + rt) * 2048 + a * 64);
              aq[a] = *(const LDS_AS unsigned*)(ab + (l & 15) * 4); bq[a] = *(const LDS_AS unsigned*)(ab + 128 + (l & 15) * 4);
              av[a] = *(const LDS_AS u32x4*)(ab + g * 16); bv[a] = *(const LDS_AS u32x4*)(ab + 128 + g * 16);
            }
          }
          if constexpr (s == ABS && n == 5) {
#pragma unroll
            for (int a = 0; a < 2; ++a) { b2[a] = f32x2{asf(bq[a]), asf(bq[a])}; asm volatile("" : "+v"(b2[a])); }
          }
          if constexpr (s == ABS + 1) {
            constexpr int k = (n + 4) % 6;
            if constexpr (k < 4) cs4[k & 1] = *(const LDS_AS f32x4*)(smem + L_CSL + k * 64 + g * 16);
            else cs4[k & 1][0] = *(const LDS_AS float*)(smem + L_CSL + k * 64 + (l & 15) * 4);
          }
          if constexpr ((s == ABS + 1 && n > 0) || (s == ABS + 2 && n == 0)) {
            constexpr int k = (n + 3) % 6;
            if constexpr (k < 4) {
#pragma unroll
              for (int a = 0; a < 2; ++a) acc[a][k] = __builtin_elementwise_fma(cs4[k & 1], f32x4{b2[a][0], b2[a][1], b2[a][0], b2[a][1]}, acc[a][k]);
            } else {
              f32x2 c2 = {cs4[k & 1][0], cs4[k & 1][0]};
              asm volatile("" : "+v"(c2));
#pragma unroll
              for (int a = 0; a < 2; ++a) acc[a][k] = __builtin_elementwise_fma(f32x4{c2[0], c2[1], c2[0], c2[1]}, __builtin_bit_cast(f32x4, bv[a]), acc[a][k]);
            }
          }
          if constexpr (IT == 0) {
            if constexpr (s == KP0 - 1 && n < 3) {
              const int c = 3 * (w & 1) + n;
              ka[n] = (FRAG_T() ^ ((c & 1) << 5)) + (SLOT(5) * STAGE + (c >> 1) * 8192 + (w >> 1) * 2048);
              asm volatile("" : "+v"(ka[n]));
            }
            if constexpr (s >= KP0 && s < KP0 + 3 && (n & 1)) keep[3 * (s - KP0) + (n >> 1)] = frag_t2(ka[n >> 1] + (s - KP0) * STAGE);
            if constexpr (s >= IMG && s < IMG + 3 && n == 4) st16(keep[s - IMG], rs, l * 16 + (s - IMG) * 1024, OFF_WI + (h * 8 + ((rot + 5) & 7)) * STAGE + w * 3072);
            if constexpr (s >= TS0 && s < TS0 + TSN && n == 4) {
              if (tstep == s && w < (ffn_on ? TW : TWR)) touch(tdum, tsrc);
            }
            if constexpr (s == TST && n == 4) { if (w >= (ffn_on ? TW : TWR)) touch(tdum, tsrc); }
          } else {
            if constexpr (s == 28 && n == 0) { POISON_BIG(1); }
            if constexpr (s == FSL && n == 4) { ld4h<0>(kv, rs, (l & 15) * SK, OFF_KEY + (bid & 15) * cmax(16 * SK, 128)); row_load(rs, g, ffn_xoff(kg0, 0, w), fx); }
            if constexpr (s == FS0 && n == 0) {
              wait_vm<SINCE(FSL)>();
              asm volatile("" : "+v"(kv)::"memory");
              PINV4(fx);
              if (__builtin_expect(!__all(kv != POISON), 0)) {
                LDS_AS unsigned* kw = (LDS_AS unsigned*)(smem + L_SKEY + 40);
                if (w == 0) {
                  kv = key_wait(rs, T, l, 0, bid & 15, kv);
                } else {
                  while ((kv = __hip_atomic_load(kw, __ATOMIC_RELAXED, __HIP_MEMORY_SCOPE_WORKGROUP)) == POISON) __builtin_amdgcn_s_sleep(2);
                }
              }
              fkey = __builtin_amdgcn_readfirstlane(umax16(kv));
              if (w == 0) *(LDS_AS unsigned*)(smem + L_SKEY + 40) = min(fkey, POISON - 1);
              sel0 = key_row(fkey);
              aff = (int)(sel0 >> 8) == p;
              asm volatile("" : "=v"(sa), "=v"(sb));
              if (ffn_on) {
                if (__builtin_expect(((kg0 ^ fkey) & 0x780u) != 0, 0)) ld4x16(fx, rs, g * 16, ffn_xoff(fkey, 0, w));
                ffn_gu(gv, uv, fx, sa, sb);
              }
            }
            if constexpr (s == FS0 && n == 2) {
              if (ffn_on) {
                ffn_gu_check(rs, T, ffn_xoff(fkey, 0, w), l, gv, uv, fx, sa, sb);
                ffn_gu_store(smem, l, w, sa, sb);
                count_in(1);
              }
            }
            if constexpr (s == FS0D && n == FDG) {
              if (ffn_on && w < 4) {
                s16x4 da[8], db[8];
                ffn_d_read(smem, l, 2 * w, da);
                while (__hip_atomic_load(qcnt + 1, __ATOMIC_RELAXED, __HIP_MEMORY_SCOPE_WORKGROUP) < 8u) {}
                asm volatile("" ::: "memory");
                const f32x4 t = ffn_t_read(smem, l);
                ffn_d_read(smem, l, 2 * w + 1, db);
                u32x2 ha = ffn_h(t, l), hb = {dppu<0x118>(ha[0]), dppu<0x118>(ha[1])};
                f32x4 ya = {0.f, 0.f, 0.f, 0.f}, yb = ya;
#pragma unroll
                for (int i = 0; i < 8; ++i) {
                  ya = mfma16k16(da[i], __builtin_bit_cast(s16x4, ha), ya); yb = mfma16k16(db[i], __builtin_bit_cast(s16x4, hb), yb);
                  ha = u32x2{dppu<0x111>(ha[0]), dppu<0x111>(ha[1])}; hb = u32x2{dppu<0x111>(hb[0]), dppu<0x111>(hb[1])};
                }
                ffn_d_store(rs, l, 8 * w, 0, slice, ya + yb);
              }
            }
#define RED_LOAD(ld, l8)                                                                                                    \
  {                                                                                                                         \
    const unsigned G = bi, po = OFF_PART + G * 32768;                                                        \
    ld(gv[0], rs, tid * 16, po); ld(gv[1], rs, tid * 16, po + 4096); ld(gv[2], rs, tid * 16, po + 8192); ld(gv[3], rs, tid * 16, po + 12288); \
    ld(uv[0], rs, tid * 16, po + 16384); ld(uv[1], rs, tid * 16, po + 20480); ld(uv[2], rs, tid * 16, po + 24576);        \
    l8(cxn, rs, (l & 3) * 16 + (l & 4) * 2, OFF_CA + (sel0 >> 7) * 2048 + G * 64);                                         \
    l8(cx1, rs, (l & 3) * 16 + (l & 4) * 2, OFF_CB + (sel0 >> 7) * 2048 + G * 64);                                          \
  }
            if constexpr (s == FS1 && n == 4) {
              asm volatile("" : "=v"(cxn), "=v"(cx1));
              if (!ffn_on && w < 4) RED_LOAD(ld16u, ld8h);
            }
            if constexpr (s == FS1S && n == 0) {
              if (!ffn_on && w < 4) {
                wait_vm<SINCE(FS1)>();
                PINV4(gv); PINV4(uv);
                asm volatile("" : "+v"(cxn), "+v"(cx1)::"memory");
                for (unsigned spins = 0; !__all(max(max(max(umax4(gv[0]), umax4(gv[1])), max(umax4(gv[2]), umax4(gv[3]))),
                                                    max(max(umax4(uv[0]), umax4(uv[1])), max(max(umax4(uv[2]), max(cxn[0], cxn[1])), max(cx1[0], cx1[1])))) != POISON) && !spin_fail(spins, rs, T);) {
                  RED_LOAD(ld16u, ld8h);
                  DRAIN();
                  PINV4(gv); PINV4(uv);
                  asm volatile("" : "+v"(cxn), "+v"(cx1)::"memory");
                }
#define F4(x) __builtin_bit_cast(f32x4, x)
                ((LDS_AS f32x4*)(smem + L_SFIN))[tid] = ((F4(gv[0]) + F4(gv[1])) + (F4(gv[2]) + F4(gv[3]))) + ((F4(uv[0]) + F4(uv[1])) + F4(uv[2]));
#undef F4
                count_in(2);
                if (w == 0) {
                  while (__hip_atomic_load(qcnt + 2, __ATOMIC_RELAXED, __HIP_MEMORY_SCOPE_WORKGROUP) < 4u) {}
                  asm volatile("" ::: "memory");
                  const u32x2 ov = red_final<4>(smem, l, fkey, cxn, cx1);
                  if (l < 8) st8(unpoison2(ov), rs, OFF_XNEW + (bi * 8 + (l & 3) * 2 + (l >> 2)) * 8, 0);
                }
              }
            }
            if constexpr (s == FXA && n == 4) { DEF4(fx); if (w < 3) row_load(rs, g, XP_OFF(), fx); }
            if constexpr (s == FXB && n == 0) {
              if (w < 3) {
                const unsigned fa = bo[0][0] + SLOT(FXT) * STAGE + w * 2048;
                u32x4 f[8];
#pragma unroll
                for (int i = 0; i < 8; ++i) f[i] = *(const LDS_AS u32x4*)(size_t)(fa + (i >> 1) * 6144 + (i & 1) * 1024);
                wait_vm<SINCE(FXA)>();
                PINV4(fx);
                ffn_xwait<XSL>(rs, T, XP_OFF(), l, fx);
                f32x4 y0 = mfma16(f[0], fx[0], f32x4{0.f, 0.f, 0.f, 0.f}), y1 = mfma16(f[1], fx[0], f32x4{0.f, 0.f, 0.f, 0.f});
#pragma unroll
                for (int i = 1; i < 4; ++i) { y0 = mfma16(f[2 * i], fx[i], y0); y1 = mfma16(f[2 * i + 1], fx[i], y1); }
                if ((l & 14) == 0) st16(unpoison4(__builtin_bit_cast(u32x4, (l & 1) ? y1 : y0)), rs, g * 16 + (l & 1) * 512, OFF_FX + ((h * 6 + 2 * w) * 8 + p) * 64);
              }
            }
            if constexpr (s == FXL && n == 4) {
              asm volatile("" : "=v"(fx[0]), "=v"(fx[1]));
              if (aff && w < 4) { ld16h<0>(fx[0], rs, l * 16, FP_OFF(0)); ld16h<0>(fx[1], rs, l * 16, FP_OFF(1)); }
            }
            if constexpr (s == FXS && n == 2) {
              if (aff && w < 4) {
                wait_vm<0>();
                asm volatile("" : "+v"(fx[0]), "+v"(fx[1])::"memory");
                if (!FX_OK(2)) {
                  unsigned spins = 0;
                  do {
                    if (spin_fail(spins, rs, T)) break;
                    fx[0] = ld16(rs, l * 16, FP_OFF(0)); fx[1] = ld16(rs, l * 16, FP_OFF(1));
                  } while (!FX_OK(2));
                }
                if (w == 3) {
                  f32x2 nx[8];
                  unpack8v(fx[0], nx); unpack8v(fx[1], nx + 4);
                  const f32x2 sm = ((nx[0] + nx[1]) + (nx[2] + nx[3])) + ((nx[4] + nx[5]) + (nx[6] + nx[7]));
                  const float mean = sum64(hadd(sm)) * (1.0f / 1024.0f);
                  const f32x2 m2 = {mean, mean};
                  f32x2 q = {0.f, 0.f};
#pragma unroll
                  for (int j = 0; j < 8; ++j) { const f32x2 d = nx[j] - m2; q = __builtin_elementwise_fma(d, d, q); }
                  const float rd = ln_rd(sum64(hadd(q)) * (1.0f / 1024.0f));
                  if (l == 0) *(LDS_AS f32x2*)(smem + L_FXL + 384) = f32x2{rd, -rbf1(mean)};
                } else {
                  tile_sum(fx[0], L_FXL);
                }
                count_in(3);
              }
            }
          }
        };
        static_for<32>([&](auto sc_) __attribute__((always_inline)) {
          constexpr int s = decltype(sc_)::value;
          if constexpr (s > 0) PIN_ACC();
          if constexpr (IT == 1) wait_vm<vm_at(IT, s, 0) - vm_a(IT, s)>();
          pin_w<s % WA>(af[s % WA][0], af[s % WA][1]);
          SB();
          static_for<6>([&](auto nc_) __attribute__((always_inline)) {
            constexpr int n = decltype(nc_)::value;
            if constexpr (s == 0) { acc[0][n] = f32x4{0.f, 0.f, 0.f, 0.f}; acc[1][n] = acc[0][n]; }
            constexpr bool on = s < QL || n >= 2;
            if constexpr (on) acc[0][n] = n < 4 ? mfma16(bfr[n].v, af[s % WA][0], acc[0][n]) : mfma16(af[s % WA][0], bfr[n].v, acc[0][n]);
            SB();
            if constexpr (n == 0) { QKV_RDB(s, 5); }
            else if constexpr (s + 1 < 32 && (s + 1 < QL || n > 2)) QKV_RDB(s + 1, n - 1);
            SB();
            if constexpr (on) acc[1][n] = n < 4 ? mfma16(bfr[n].v, af[s % WA][1], acc[1][n]) : mfma16(af[s % WA][1], bfr[n].v, acc[1][n]);
            SB();
            if constexpr (s + 1 < 32 && (s + 1 < QL || n >= 2)) QKV_RDA(s + 1, n);
            SB();
            gap(sc_, nc_);
            SB();
          });
        });
        PIN_ACC();
        frag_t qw[32 - QL][2];
        static_for<32 - QL>([&](auto c_) __attribute__((always_inline)) {
          constexpr int s = QL + decltype(c_)::value;
          QKV_RDA(s, 0); QKV_RDB(s, 0); QKV_RDA(s, 1); QKV_RDB(s, 1);
          qw[s - QL][0] = bfr[0]; qw[s - QL][1] = bfr[1];
        });
        SB();
        const bool own = aff && (int)((sel0 >> 5) & 7) == rt;
        const bool m0 = !(sel0 & 16), mq = (l & 15) == (int)(sel0 & 15), mv = g == (int)((sel0 & 15) >> 2);
        auto patch_qk = [&](int n) __attribute__((always_inline)) {
          f32x4 t = *(const LDS_AS f32x4*)(smem + L_FXL + n * 64 + g * 16);
          const f32x4 c = *(const LDS_AS f32x4*)(smem + L_CSL + n * 64 + g * 16);
          const float nb = *(const LDS_AS float*)(smem + L_FXL + 388);
#pragma unroll
          for (int e = 0; e < 4; ++e) { t[e] = __builtin_fmaf(c[e], nb, t[e]); asm volatile("" : "+v"(t[e])); }
          acc[0][n] = (mq && m0) ? t : acc[0][n];
          acc[1][n] = (mq && !m0) ? t : acc[1][n];
        };
        if (own) {
          while (__hip_atomic_load(qcnt + 3, __ATOMIC_RELAXED, __HIP_MEMORY_SCOPE_WORKGROUP) < 4u) {}
          asm volatile("" ::: "memory");
          patch_qk(2); patch_qk(3);
          const f32x2 nab = *(const LDS_AS f32x2*)(smem + L_FXL + 384);
          const LDS_AS float* fl_ = (const LDS_AS float*)(smem + L_FXL + 256) + (l & 15);
          const LDS_AS float* cl_ = (const LDS_AS float*)(smem + L_CSL + 256) + (l & 15);
          const float t4 = __builtin_fmaf(cl_[0], nab[1], fl_[0]), t5 = __builtin_fmaf(cl_[16], nab[1], fl_[16]);
          const unsigned na = __builtin_bit_cast(unsigned, (float)nab[0]);
#pragma unroll
          for (int a = 0; a < 2; ++a) {
            const bool ma = a ? !m0 : m0;
            aq[a] = (mq && ma) ? na : aq[a];
#pragma unroll
            for (int e = 0; e < 4; ++e) {
              const bool me = mv && ma && e == (int)(sel0 & 3);
              acc[a][4][e] = me ? t4 : acc[a][4][e]; acc[a][5][e] = me ? t5 : acc[a][5][e];
              av[a][e] = me ? na : av[a][e];
            }
          }
        }
        constexpr float C2 = 1.4426950408889634f * 0.17677669529663687f;
#pragma unroll
        for (int a = 0; a < 2; ++a) {
          f32x2 a2 = {asf(aq[a]), asf(aq[a])};
          qs2[a] = f32x2{asf(aq[a]) * C2, asf(aq[a]) * C2};
          asm volatile("" : "+v"(a2), "+v"(qs2[a]));
          acc[a][2] *= f32x4{a2[0], a2[1], a2[0], a2[1]}; acc[a][3] *= f32x4{a2[0], a2[1], a2[0], a2[1]};
          acc[a][4] *= __builtin_bit_cast(f32x4, av[a]); acc[a][5] *= __builtin_bit_cast(f32x4, av[a]);
        }
        k0 = pack8(acc[0][2], acc[0][3]); k1 = pack8(acc[1][2], acc[1][3]); v0 = pack8(acc[0][4], acc[1][4]); v1 = pack8(acc[0][5], acc[1][5]);
        const unsigned kvo = OFF_KV + IT * KV_STEP + (p * 32 + h) * 32768 + rt * 4096;
        if (p < 7) { st16(k0, rs, l * 16, kvo); st16(k1, rs, l * 16 + 1024, kvo); st16(v0, rs, l * 16 + 2048, kvo); st16(v1, rs, l * 16 + 3072, kvo); }
        char* ownp = smem + (8 + rt) * 2048 + l * 16;
        *(u32x4*)(ownp + L_K) = k0;
        if (rt < 7) *(u32x4*)(ownp + L_K + 1024) = k1;
        *(u32x4*)(ownp + L_V) = v0; *(u32x4*)(ownp + L_V + 1024) = v1;
        SB();
        static_for<32 - QL>([&](auto c_) __attribute__((always_inline)) {
          constexpr int s = QL + decltype(c_)::value;
#pragma unroll
          for (int n = 0; n < 2; ++n) { acc[0][n] = mfma16(qw[s - QL][n].v, af[s % WA][0], acc[0][n]); acc[1][n] = mfma16(qw[s - QL][n].v, af[s % WA][1], acc[1][n]); }
        });
        if (own) { patch_qk(0); patch_qk(1); }
      };
      qkv_loop(it_c);
    }
#undef A_LOAD1
#undef QKV_RDA
#undef QKV_RDB
#undef QKV_BO
    {
      IDS();
      const GAS bf16 *pwo, *pwg, *pwu;
      asm volatile("s_load_dwordx2 %0, %3, 24\n\ts_load_dwordx2 %1, %3, 40\n\ts_load_dwordx2 %2, %3, 48" : "=&s"(pwo), "=&s"(pwg), "=&s"(pwu) : "s"(ka));
      const int ql = l & 15;
      const f32x4 z4 = {0.f, 0.f, 0.f, 0.f}, ninf = {-INFINITY, -INFINITY, -INFINITY, -INFINITY};
      LDS_AS unsigned* rdy = (LDS_AS unsigned*)(smem + L_RDY);
      const unsigned rtag = T[0] + it;
      u32x4 qf[2];
      f32x4 cn[2], oa[2][2], sum[2], S[2][2];
      unsigned one2 = 0x3F803F80u;
      asm volatile("" : "+v"(one2));
      const u32x4 ones = {one2, one2, one2, one2};
      auto ex8 = [&](f32x4 a, f32x4 b) __attribute__((always_inline)) {
        return u32x4{pack2(__builtin_amdgcn_exp2f(a[0]), __builtin_amdgcn_exp2f(a[1])), pack2(__builtin_amdgcn_exp2f(a[2]), __builtin_amdgcn_exp2f(a[3])),
                     pack2(__builtin_amdgcn_exp2f(b[0]), __builtin_amdgcn_exp2f(b[1])), pack2(__builtin_amdgcn_exp2f(b[2]), __builtin_amdgcn_exp2f(b[3]))};
      };
      {
#pragma unroll
        for (int a = 0; a < 2; ++a) {
          const f32x4 c4 = {qs2[a][0], qs2[a][1], qs2[a][0], qs2[a][1]};
          qf[a] = pack8(acc[a][0] * c4, acc[a][1] * c4);
        }
        f32x4 mk;
#pragma unroll
        for (int e = 0; e < 4; ++e) mk[e] = (4 * g + e <= ql) ? 0.f : -INFINITY;
        const f32x4 d0 = mfma16(k0, qf[0], mk), d1 = mfma16(k1, qf[1], mk), d2 = mfma16(k0, qf[1], z4);
        const float m0 = -max_x32(max_x16(fmaxf(fmaxf(d0[0], d0[1]), fmaxf(d0[2], d0[3]))));
        const float m1 = -max_x32(max_x16(fmaxf(fmaxf(d1[0], d1[1]), fmaxf(d1[2], d1[3]))));
        cn[0] = f32x4{m0, m0, m0, m0}; cn[1] = f32x4{m1, m1, m1, m1};
        asm volatile("" : "+v"(cn[0]), "+v"(cn[1]));
        const f32x4 e0 = d0 + cn[0];
        const u32x4 p0 = {pack2(__builtin_amdgcn_exp2f(e0[0]), __builtin_amdgcn_exp2f(e0[1])), pack2(__builtin_amdgcn_exp2f(e0[2]), __builtin_amdgcn_exp2f(e0[3])), 0u, 0u};
        const u32x4 p1 = ex8(d2 + cn[1], d1 + cn[1]);
        oa[0][0] = mfma16(v0, p0, z4); oa[0][1] = mfma16(v1, p0, z4); sum[0] = mfma16(ones, p0, z4);
        oa[1][0] = mfma16(v0, p1, z4); oa[1][1] = mfma16(v1, p1, z4); sum[1] = mfma16(ones, p1, z4);
      }
      const char* kb_ = smem + L_K + l * 16;
      const char* vb_ = smem + L_V + l * 16;
      const unsigned wov = (l >> 3) * 2048 + (((((l & 7) >> 1) ^ ((l >> 4) & 3)) * 2 + (l & 1)) << 4), fnv = (l >> 1) * (F_ * 2) + (l & 1) * 16;
      const unsigned pp = __builtin_amdgcn_readfirstlane(p > 0 ? 32 : 0), pvo = OFF_KV + it * KV_STEP + (p * 32 + h - pp) * 32768;
      const unsigned pfo = __builtin_amdgcn_readfirstlane(F_KV + KVF(it, p * 32 + h - pp));
      asm volatile("" : "+v"(oa[0][0]), "+v"(oa[0][1]), "+v"(oa[1][0]), "+v"(oa[1][1]), "+v"(sum[0]), "+v"(sum[1]));
      DRAIN();
      asm volatile("" : "+v"(tdum));
      if (l == 0) st8(T, rs, rt * SV, F_KV + KVF(it, p * 32 + h));
      BAR();
      asm volatile("" : "+s"(pwo), "+s"(pwg), "+s"(pwu));
      WO_PIECE(15); WO_PIECE(14);
#define LDS16(a_) (*(const LDS_AS u32x4*)(size_t)(a_))
      u32x4 kf[2], vf[2];
      int nw = 0;
      u32x4 p0, p1;
      float e0, e1;
#define EE(v, i) { e0 = __builtin_amdgcn_exp2f(v[i]); e1 = __builtin_amdgcn_exp2f(v[i + 1]); pin2s(e0, e1); SB(); }
#define CV(P, i) { unsigned t_ = pack2(e0, e1); pin1(t_); P[i] = t_; SB(); }
      auto body = [&](unsigned ad, auto dk_) __attribute__((always_inline)) {
        constexpr int dk = decltype(dk_)::value;
        const f32x4 a0 = S[1][0], a1 = S[1][1];
        sum[0] = mfma16(ones, p0, sum[0]); SB();
        EE(a0, 0);
        lds_wait<2>(kf[0], kf[1]);
        S[0][0] = mfma16(kf[0], qf[0], cn[0]); SB();
        CV(p1, 0); EE(a0, 2);
        S[0][1] = mfma16(kf[1], qf[0], cn[0]); SB();
        CV(p1, 1); EE(a1, 0);
        lds_wait<0>(vf[0], vf[1]);
        oa[0][0] = mfma16(vf[0], p0, oa[0][0]); SB();
        CV(p1, 2); EE(a1, 2);
        oa[0][1] = mfma16(vf[1], p0, oa[0][1]); SB();
        CV(p1, 3);
        S[1][0] = mfma16(kf[0], qf[1], cn[1]); SB();
        EE(S[0][0], 0);
        S[1][1] = mfma16(kf[1], qf[1], cn[1]); SB();
        lds2<2048 + dk>(kf[0], kf[1], ad); SB();
        CV(p0, 0); EE(S[0][0], 2);
        oa[1][0] = mfma16(vf[0], p1, oa[1][0]); SB();
        CV(p0, 1); EE(S[0][1], 0);
        oa[1][1] = mfma16(vf[1], p1, oa[1][1]); SB();
        lds2<2048 + L_V>(vf[0], vf[1], ad); SB();
        CV(p0, 2); EE(S[0][1], 2);
        sum[1] = mfma16(ones, p1, sum[1]); SB();
        CV(p0, 3);
      };
      auto last = [&]() __attribute__((always_inline)) {
        sum[0] = mfma16(ones, p0, sum[0]); SB();
        EE(S[1][0], 0);
        lds_wait<0>(vf[0], vf[1]);
        pin2(kf[0], kf[1]);
        oa[0][0] = mfma16(vf[0], p0, oa[0][0]); SB();
        CV(p1, 0); EE(S[1][0], 2);
        oa[0][1] = mfma16(vf[1], p0, oa[0][1]); SB();
        CV(p1, 1); EE(S[1][1], 0);
        const float f0 = __builtin_amdgcn_exp2f(S[1][1][2]), f1 = __builtin_amdgcn_exp2f(S[1][1][3]);
        p1[2] = pack2(e0, e1); p1[3] = pack2(f0, f1);
        oa[1][0] = mfma16(vf[0], p1, oa[1][0]); oa[1][1] = mfma16(vf[1], p1, oa[1][1]); sum[1] = mfma16(ones, p1, sum[1]);
      };
      auto piece = [&]() __attribute__((always_inline)) {
        if constexpr (it == 1) { WO_PIECE(13 - nw); }
        else if (ffn_on) { FFN_DMA(nw); }
        ++nw;
      };
      if (rt > 0) {
        unsigned ad = lbase + l * 16 + (6 + rt) * 2048;
        lds2<2048>(kf[0], kf[1], ad); lds2<2048 + L_V>(vf[0], vf[1], ad);
        lds_wait<2>(kf[0], kf[1]);
        S[0][0] = mfma16(kf[0], qf[0], cn[0]); S[0][1] = mfma16(kf[1], qf[0], cn[0]); S[1][0] = mfma16(kf[0], qf[1], cn[1]); S[1][1] = mfma16(kf[1], qf[1], cn[1]);
        SB();
        lds_wait<0>(vf[0], vf[1]);
        lds2<0>(kf[0], kf[1], ad);
        p0 = ex8(S[0][0], S[0][1]);
        lds_wait<0>(kf[0], kf[1]);
#pragma unroll 1
        for (int i = rt - 1; i > 0; --i) {
          ad -= 2048;
          body(ad, std::integral_constant<int, -2048>{});
          piece();
          SB();
        }
        piece();
        last();
      }
      {
        constexpr int FW = it ? FW1 : FW0, FN = 8 / FW;
        if (w < FW) {
          const int g0 = FN * rt;
          if (KVS && w == 0) __builtin_amdgcn_s_sleep(KVS);
          poll_flags(rs, (l & (FN - 1)) * SV, pfo + g0 * SV, T);
#pragma unroll
          for (int j = 0; j < FN; ++j) {
            const unsigned d = lbase + (g0 + j) * 2048, o = pvo + (g0 + j) * 4096;
            dma16s<0>(rs, l * 16, o, d + L_K); dma16s<1024>(rs, l * 16, o, d + L_K);
            dma16s<0>(rs, l * 16, o + 2048, d + L_V); dma16s<1024>(rs, l * 16, o + 2048, d + L_V);
          }
        }
        if (p == 0) { cn[0] = ninf; cn[1] = ninf; }
        f32x4 mk;
#pragma unroll
        for (int e = 0; e < 4; ++e) mk[e] = (4 * g + e > ql) ? 0.f : -INFINITY;
        const f32x4 cm0 = cn[0] + mk, cm1 = cn[1] + mk;
        if (w < FW) {
          if constexpr (FN > 1) { wait_vm<2 * FN>(); if (l < FN / 2) rdy[FN * rt + l] = rtag; }
          DRAIN();
          if (l < FN) rdy[FN * rt + l] = rtag;
        }
        if constexpr (it == 1) { WO_PIECE(6); WO_PIECE(5); WO_PIECE(4); }
        else { WO_PIECE(13); WO_PIECE(12); if (ffn_on) FFN_DMA(7); }

        int have = rt - 1;
        auto need = [&](int gq) __attribute__((always_inline)) {
          if (__builtin_expect(gq > have && gq < 8, 0)) {
            do {
              const unsigned t = __hip_atomic_load(rdy + (l & 7), __ATOMIC_RELAXED, __HIP_MEMORY_SCOPE_WORKGROUP);
              have = rt - 1 + __builtin_ctz(~((unsigned)__builtin_amdgcn_ballot_w64(t == rtag) & 0xffu) >> rt);
            } while (gq > have);
            asm volatile("" ::: "memory");
          }
        };
        unsigned ad = lbase + l * 16 + rt * 2048;
        need(rt);
        lds2<0>(kf[0], kf[1], ad); lds2<L_V>(vf[0], vf[1], ad);
        lds_wait<2>(kf[0], kf[1]);
        S[0][0] = mfma16(kf[0], qf[0], cm0); S[0][1] = mfma16(kf[1], qf[0], cn[0]); S[1][0] = ninf; S[1][1] = mfma16(kf[1], qf[1], cm1);
        SB();
        lds_wait<0>(vf[0], vf[1]);
        need(rt + 1);
        lds2<2048>(kf[0], kf[1], ad);
        p0 = ex8(S[0][0], S[0][1]);
        lds_wait<0>(kf[0], kf[1]);
#pragma unroll 1
        for (int gq = rt + 1; gq < 8; ++gq) {
          need(gq + 1);
          body(ad, std::integral_constant<int, 2048>{});
          piece();
          SB();
          ad += 2048;
        }
        last();
      }
      {
        float big = 0.f;
#pragma unroll
        for (int a = 0; a < 2; ++a)
#pragma unroll
          for (int d = 0; d < 2; ++d)
            big = __builtin_elementwise_maximum(__builtin_elementwise_maximum(big, __builtin_elementwise_maximum(fabsf(oa[a][d][0]), fabsf(oa[a][d][1]))),
                                                __builtin_elementwise_maximum(fabsf(oa[a][d][2]), fabsf(oa[a][d][3])));
        bool ok = sum[0][0] >= 0.5f && sum[0][0] < 0x1p64f && sum[1][0] >= 0.5f && sum[1][0] < 0x1p64f && big < INFINITY;
        if (!__all(ok)) {
#pragma unroll
          for (int a = 0; a < 2; ++a) {
            const int qs = 2 * rt + a, qq = 256 + 16 * qs + ql, klo = max(qq - 255, p > 0 ? 0 : 256);
            auto score = [&](int t) __attribute__((always_inline)) {
              f32x4 s = mfma16(t == 31 ? k1 : *(const u32x4*)(kb_ + t * 1024), qf[a], z4);
#pragma unroll
              for (int e = 0; e < 4; ++e) { const int kk = 16 * t + 4 * g + e; s[e] = (kk <= qq && kk >= klo) ? s[e] : -INFINITY; }
              return s;
            };
            float mx = -INFINITY;
#pragma unroll 1
            for (int t = qs; t <= qs + 16; ++t) { const f32x4 s = score(t); mx = fmaxf(fmaxf(mx, fmaxf(s[0], s[1])), fmaxf(s[2], s[3])); }
            mx = max_x32(max_x16(mx));
            oa[a][0] = z4; oa[a][1] = z4; sum[a] = z4;
#pragma unroll 1
            for (int G = qs >> 1; G <= (qs + 16) >> 1; ++G) {
              const f32x4 s0 = score(2 * G), s1 = score(2 * G + 1);
              f32x4 e0, e1;
#pragma unroll
              for (int e = 0; e < 4; ++e) { e0[e] = __builtin_amdgcn_exp2f(s0[e] - mx); e1[e] = __builtin_amdgcn_exp2f(s1[e] - mx); }
              const u32x4 pf = pack8(e0, e1);
              oa[a][0] = mfma16(*(const u32x4*)(vb_ + G * 2048), pf, oa[a][0]);
              oa[a][1] = mfma16(*(const u32x4*)(vb_ + G * 2048 + 1024), pf, oa[a][1]);
              sum[a] = mfma16(ones, pf, sum[a]);
            }
          }
        }
      }
#pragma unroll
      for (int a = 0; a < 2; ++a) {
        const float inv = __builtin_amdgcn_rcpf(sum[a][0]);
        oa[a][0] *= inv; oa[a][1] *= inv;
      }
      const unsigned oo = OFF_O + it * O_STEP + ((p * 8 + rt) * 64 + h * 2) * 1024;
      st16(pack8(oa[0][0], oa[0][1]), rs, l * 16, oo);
      st16(pack8(oa[1][0], oa[1][1]), rs, l * 16 + 1024, oo);
      DRAIN();
      BAR();
      if (tid == 0) st8(T, rs, OFL(h), F_O + OFB(it, p));
      if constexpr (it == 0) {
        DEF4(gv); DEF4(uv);
        if (ffn_on) {
          const unsigned ro = 65536 + w * 1024 + (4 * g + ((l & 15) >> 2)) * 32 + (l & 3) * 8;
#pragma unroll
          for (int i = 0; i < 4; ++i) {
            frag_t f;
            f.hf.a = trread(smem, ro + i * 16384); f.hf.b = trread(smem, ro + i * 16384 + 512);
            gv[i] = f.v;
            f.hf.a = trread(smem, ro + i * 16384 + 8192); f.hf.b = trread(smem, ro + i * 16384 + 8704);
            uv[i] = f.v;
          }
        }
        PINV4(gv); PINV4(uv);
        {
          ARGP(px, 0);
          const unsigned xo = ((rb * 4 + (w & 3)) * 32 + (w >> 2) * 16) * 2048 + ct * 128, xw = (l >> 3) * 2048 + (((l & 7) ^ g) << 4);
          dma16(RSRC(px, 2048 * 1024 * 2), xw, xo, lbase + w * 4096); dma16(RSRC(px, 2048 * 1024 * 2), xw ^ 64, xo + 16384, lbase + w * 4096 + 1024);
        }
      }
      WO_PIECE(3); WO_PIECE(2); WO_PIECE(1); WO_PIECE(0);
      if constexpr (it == 0) { WO_PIECE(11); WO_PIECE(10); WO_PIECE(9); WO_PIECE(8); WO_PIECE(7); WO_PIECE(6); WO_PIECE(5); WO_PIECE(4); }
    }
    IDS();
    ARGP(pwr, 4); ARGP(pwd, 7); ARGP(pout, 8);
    const int kh = w >> 2, rt2 = rb * 4 + (w & 3);
    const unsigned R = rt2 * 32 + kh * 16 + (l & 15);
    const unsigned to = (rt2 * 64 + ct * 4 + kh) * 1024 + l * 16;
    const unsigned rmo = ct * 64 + ((g & 1) ? 12 + 4 * g : 4 * g);
    f32x2 v[8], wv[8];
    float wsum;
    {
      u32x4 wa[2], wrp[2], res[2];
#pragma unroll
      for (int j = 0; j < 2; ++j) {
        wrp[j] = *(const GAS u32x4*)(pwr + rmo + 32 * j);
        wa[j] = *(const GAS u32x4*)(pwr + l * 8 + j * 512);
      }
      {
        f32x2 t[8];
        unpack8v(wa[0], t); unpack8v(wa[1], t + 4);
        const f32x2 s = ((t[0] + t[1]) + (t[2] + t[3])) + ((t[4] + t[5]) + (t[6] + t[7]));
        wsum = sum64(hadd(s));
        unpack8v(trade16(wrp[0]), wv); unpack8v(trade16(wrp[1]), wv + 4);
        asm volatile("" : "+v"(wsum), "+v"(wv[0]), "+v"(wv[1]), "+v"(wv[2]), "+v"(wv[3]), "+v"(wv[4]), "+v"(wv[5]), "+v"(wv[6]), "+v"(wv[7]));
      }
      unsigned bt[4];
#pragma unroll
      for (int n = 0; n < 4; ++n) {
        bt[n] = lbase + 32768 + 65536 + kh * 4096 + (4 * g + ((l & 15) >> 2)) * 128 + (l & 3) * 8 + ((n ^ (2 * (g & 1) + ((l & 15) >> 3))) << 5);
        asm volatile("" : "+v"(bt[n]));
      }
      if (w == 0) { if (it ? OSL1 : OSL0) __builtin_amdgcn_s_sleep(it ? OSL1 : OSL0); poll_flags<OPS>(rs, OFL(l & 31), F_O + OFB(it, rb >> 1), T); }
      else if (it == 0 && w < 3) poll_flags2(rs, F_XN + (((w - 1) * 4 + g) * RX + bi % RX) * 256 + (l & 15) * 16, T);
      else if (it == 0 && w == 3) poll_flags(rs, ((l >> 3) * 32 + h) * cmax(8 * SV, 128) + (l & 7) * SV, F_KV, T);
      asm volatile("s_barrier" ::: "memory");
      const unsigned abase = OFF_O + it * O_STEP + (rt2 * 64 + 32 * kh) * 1024;
      const unsigned av0 = l * 16 + kh * 1024, av1 = l * 16 + (kh ^ 1) * 1024;
      u32x4 af[WB][2];
#define O_LOAD(s_) { ld16h<(wo_k(it, s_) & 1) * 2048>(af[(s_) % WB][0], rs, av0, abase + (wo_k(it, s_) >> 1) * 4096); ld16h<(wo_k(it, s_) & 1) * 2048>(af[(s_) % WB][1], rs, av1, abase + (wo_k(it, s_) >> 1) * 4096); }
      static_for<WR>([&](auto c_) __attribute__((always_inline)) { O_LOAD(decltype(c_)::value); });
      if constexpr (it == 1) { ld16h<0>(res[0], rs, g * 16, OFF_XNEW + ct * 128); ld16h<64>(res[1], rs, g * 16, OFF_XNEW + ct * 128); }
      SB();
      f32x4 ac[2][4];
#pragma unroll
      for (int a = 0; a < 2; ++a)
#pragma unroll
        for (int n = 0; n < 4; ++n) ac[a][n] = f32x4{0.f, 0.f, 0.f, 0.f};
      u32x4 bfr[2][4];
#define WO_RD(buf, s, n)                                                                             \
  {                                                                                                  \
    const unsigned o_ = bt[n] + (wo_k(it, s) & 7) * 8192;                                             \
    frag_t f_;                                                                                       \
    f_.hf.a = trabs(o_); f_.hf.b = trabs(o_ + 2048);                                                 \
    bfr[buf][n] = f_.v;                                                                              \
  }
#pragma unroll
      for (int n = 0; n < 4; ++n) WO_RD(0, 0, n);
      static_for<16>([&](auto sc_) __attribute__((always_inline)) {
        constexpr int s = decltype(sc_)::value, cur = s & 1;
        pin_ac(ac);
        if constexpr (s == 1) lds_count<0>(lbase + L_QC);
        if constexpr (s == (it == 0 ? 3 : 11)) lds_look(smem + L_QC, 64);
        if constexpr (s < 15 && ((wo_k(it, s) ^ wo_k(it, s + 1)) & 8)) {
#pragma unroll
          for (int n = 0; n < 4; ++n) { bt[n] += (wo_k(it, s) & 8) ? -65536 : 65536; pin1(bt[n]); }
        }
        if constexpr (s < WB - WR) O_LOAD(WR + 2 * s);
        if constexpr (it == 0 && s == 8) lds_count<8>(lbase + L_QC);
        if constexpr (it == 0 && s == WDS) {
          DRAIN();
          if ((w >> 2) == WDW && ffn_on) { lds_look(smem + L_QC + 8, 16); WD_DMA_OF(w); WD_DMA_OF(w ^ 4); }
        }
        if constexpr (it == 1 || s < WDS) wait_vm<wo_at(it, s) - wo_pos(it, s)>();
        pin2(af[s % WB][0], af[s % WB][1]);
        if constexpr (it == 1 && s == 9) pin2(res[0], res[1]);
        SB();
#pragma unroll
        for (int n = 0; n < 4; ++n) {
          ac[0][n] = mfma16(bfr[cur][n], af[s % WB][0], ac[0][n]);
          ac[1][n] = mfma16(bfr[cur][n], af[s % WB][1], ac[1][n]);
          if (s + 1 < 16) WO_RD(cur ^ 1, s + 1, n);
          if (it == 0 && s == 8) {
            const u32x2 t = *(const LDS_AS u32x2*)(smem + w * 4096 + (l & 15) * 128 + (((2 * n + (g >> 1)) ^ ((l & 15) >> 1)) << 4) + (g & 1) * 8);
            res[n >> 1][2 * (n & 1)] = t[0]; res[n >> 1][2 * (n & 1) + 1] = t[1];
          }
          if (s == 9) {
            if (it == 1 && n == 0) { res[0] = R == sel0 ? res[0] : x2p[0]; res[1] = R == sel0 ? res[1] : x2p[1]; }

            v[2 * n] = f32x2{lo16(res[n >> 1][2 * (n & 1)]), hi16(res[n >> 1][2 * (n & 1)])};
            v[2 * n + 1] = f32x2{lo16(res[n >> 1][2 * (n & 1) + 1]), hi16(res[n >> 1][2 * (n & 1) + 1])};
            pin2f(v[2 * n], v[2 * n + 1]);
          }
          SB();
        }
        if constexpr (s < WB - WR) { O_LOAD(WR + 2 * s + 1); }
        else if constexpr (s + WB < 16) { O_LOAD(s + WB); }
        SB();
      });
#undef WO_RD
#undef O_LOAD
#define XCH_AT(v) ((v) == 7 ? 155648 : (v) * 4096)
#pragma unroll
      for (int n = 0; n < 4; ++n) *(f32x4*)(smem + XCH_AT(w) + n * 1024 + l * 16) = ac[1][n];
#pragma unroll
      for (int n = 0; n < 4; ++n) { v[2 * n] += f32x2{ac[0][n][0], ac[0][n][1]}; v[2 * n + 1] += f32x2{ac[0][n][2], ac[0][n][3]}; }
      BAR();
#pragma unroll
      for (int n = 0; n < 4; ++n) {
        const f32x4 o = *(const f32x4*)(smem + XCH_AT(w ^ 4) + n * 1024 + l * 16);
        v[2 * n] += f32x2{o[0], o[1]}; v[2 * n + 1] += f32x2{o[2], o[3]};
      }
    }
    {
      if (it == 0 && X1F) { st16(pack8v(v), rs, to, OFF_XN + (4u << 20)); st16(pack8v(v + 4), rs, to + 2048, OFF_XN + (4u << 20)); }
      SB();
      const f32x2 s = ((v[0] + v[1]) + (v[2] + v[3])) + ((v[4] + v[5]) + (v[6] + v[7]));
      f32x2 dp = v[0] * wv[0], q = v[0] * v[0];
#pragma unroll
      for (int j = 1; j < 8; ++j) { dp = __builtin_elementwise_fma(v[j], wv[j], dp); q = __builtin_elementwise_fma(v[j], v[j], q); }
      float mt = hadd(s), qq = hadd(q), dot = hadd(dp);
      add_g3(mt, qq, dot);
      const unsigned so = OFF_STAT + it * 524288 + rb * 32768, lrow = R & 127;
      if (g == 0) st16(unpoison4(__builtin_bit_cast(u32x4, f32x4{mt, qq, dot, 0.f})), rs, ct * 2048 + lrow * 16, so);
      SB();
      if (it == 0 && !X1F) { st16(pack8v(v), rs, to, OFF_XN + (4u << 20)); st16(pack8v(v + 4), rs, to + 2048, OFF_XN + (4u << 20)); }
      SB();
      if (it == 0) wait_vm<X1F>();
      if constexpr (it == 0) {
        volatile LDS_AS u32x2* in = (volatile LDS_AS u32x2*)(smem + L_XC);
        if (l == 0) in[w] = T;
        const u32x2 t = in[l & 7];
        if (__all(t[0] == T[0] && t[1] == T[1]) && l < RX) st8(T, rs, (l * 32 + (rb & 1) * 16 + ct) * SX, F_X1 + (rb >> 1) * RX * 32 * SX);
      }
      if (it == 1) { if (ffn_on) { WD_DMA_OF(w); } }
      if (it == 0) {
#pragma unroll
        for (int j = 0; j < 9; ++j) *(u32x4*)(smem + L_RING + (j / 3) * STAGE + (w * 3 + j % 3) * 1024 + l * 16) = keep[j];
      }
      __builtin_amdgcn_s_sleep(it == 0 ? SDI0 : SDIR);
      char* const skey = smem + (it == 0 ? L_SKEY : L_XC);
      u32x4 sp[4];
      ld16h<0>(sp[0], rs, g * 8192 + lrow * 16, so); ld16h<2048>(sp[1], rs, g * 8192 + lrow * 16, so);
      ld16h<0>(sp[2], rs, g * 8192 + lrow * 16, so + 4096); ld16h<2048>(sp[3], rs, g * 8192 + lrow * 16, so + 4096);
      u32x2 xf;
      unsigned k_;
      asm volatile("" : "=v"(xf), "=v"(k_));
      if (it == 0 && w == 0) { ld8h(xf, rs, (l & 31) * SX, F_X1 + (p * RX + (xcd >> 1) % RX) * 32 * SX); ld4h<0>(k_, rs, (l & 15) * SK, OFF_KEY + (bid & 15) * cmax(16 * SK, 128)); }
      DRAIN();
      asm volatile("" : "+v"(xf), "+v"(k_)::"memory");
      PINV4(sp);
      const RowFin r = fin_key(rs, T, g * 8192 + lrow * 16, so, skey, l, w, R, wsum, sp);
      if constexpr (it == 0) {
        if (w == 0) {
          const unsigned need = 0x30003u << (((PROT() + 5) & 7) * 2);
          unsigned m = (unsigned)__builtin_amdgcn_ballot_w64(xf[0] == T[0] && xf[1] == T[1]);
          for (unsigned spins = 0; (m & need) != need;) {
            if (spin_fail(spins, rs, T)) { m = ~0u; break; }
            const u32x2 f = ld8(rs, F_X1 + ((p * RX + (xcd >> 1) % RX) * 32 + (l & 31)) * SX, 0);
            k_ = key_load(rs, l, 0, bid & 15);
            m = (unsigned)__builtin_amdgcn_ballot_w64(f[0] == T[0] && f[1] == T[1]);
          }
          k_ = umax16(k_ == POISON ? 0u : k_);
          if (l == 0) *(LDS_AS u32x4*)(skey + 32) = u32x4{m, k_, POISON, 0u};
        }
        if (tid == 192) *(LDS_AS u32x4*)(smem + L_QC) = u32x4{0u, 0u, 0u, 0u};
      }
      BAR();
      if constexpr (it == 0) {
        PRE_LOAD(1, PROT() + 5, 0, 4);
        const u32x2 mk = *(const LDS_AS u32x2*)(skey + 32);
        xm0 = __builtin_amdgcn_readfirstlane(mk[0]); kg0 = __builtin_amdgcn_readfirstlane(mk[1]);
      }
      const unsigned own = fin_cand(rs, skey, it, g, rb, ct, r, v);
      if constexpr (it == 0) kg0 = max(kg0, (unsigned)__builtin_amdgcn_readfirstlane(own));
      fin_x2(r, v);
      if constexpr (it == 0) {
        x2p[0] = pack8v(v); x2p[1] = pack8v(v + 4);
        asm volatile("" : "+v"(x2p[0]), "+v"(x2p[1]));
        if ((w & 3) * 2 + (w >> 2) == ct >> 1) {
          const float f = 1.0f + r.rd, rd2 = ln_rd(r.var * f * f), mb2 = rbf1(__builtin_fmaf(r.mean - r.mb, r.rd, r.mean));
          if (g < 2 && ((l >> 3) & 1) == (ct & 1))
            st4(min(__builtin_bit_cast(unsigned, g == 0 ? f * rd2 : -__builtin_fmaf(r.mb, r.rd, mb2) * __builtin_amdgcn_rcpf(f)), POISON - 1), rs, OFF_AB + 16384 + R * 4 + g * 8192, 0);
        }
      }
    }
    if (it == 1) {
      const u32x4 op0 = trade16(pack8v(v)), op1 = trade16(pack8v(v + 4));
#define OUT_STORE()                                                                                 \
  if (R != key_row(key)) {                                                                          \
    __builtin_amdgcn_raw_buffer_store_b128(op0, rout, (R * 1024 + rmo) * 2, 0, SC1);                  \
    __builtin_amdgcn_raw_buffer_store_b128(op1, rout, (R * 1024 + rmo + 32) * 2, 0, SC1);             \
  }
      u32x4 xv[4];
      unsigned guess = 0;
      {
        __builtin_amdgcn_s_sleep(GSL);
        const unsigned k_ = key_load(rs, l, 1, bid & 15);
        guess = ffn_xoff(__builtin_amdgcn_readfirstlane(umax16(k_ == POISON ? 0u : k_)), 1, w);
        row_load(rs, g, guess, xv);
      }
      if (w == 0) {
        const unsigned k_ = key_wait(rs, T, l, 1, bid & 15, key_load(rs, l, 1, bid & 15));
        if (l == 0) *(LDS_AS unsigned*)(smem + L_SKEY + 32) = k_;
      }
      DRAIN();
      PINV4(xv);
      BAR();
      const unsigned key = __builtin_amdgcn_readfirstlane(*(LDS_AS unsigned*)(smem + L_SKEY + 32));
      if (ffn_on) {
        f32x4 ga, ua;
        s16x4 dfr[8];
        ffn_d_read(smem, l, w, dfr);
        if (guess != ffn_xoff(key, 1, w)) ld4x16(xv, rs, g * 16, ffn_xoff(key, 1, w));
        ffn_gu(gv, uv, xv, ga, ua);
        ffn_gu_check(rs, T, ffn_xoff(key, 1, w), l, gv, uv, xv, ga, ua);
        ffn_gu_store(smem, l, w, ga, ua);
        BAR();
        f32x4 ya = {0.f, 0.f, 0.f, 0.f}, yb = ya;
        u32x2 hh = ffn_h(ffn_t_read(smem, l), l);
        ffn_d_mma(dfr, hh, ya, yb);
        if ((l & 15) < 8) ffn_d_store(rs, l, 4 * w, 1, slice, ya + yb);
        SB();
        OUT_STORE();
      } else {
        OUT_STORE();
        const unsigned G = bi;
        if (G == 0 && tid == 0) st8(T, rs, F_EP, 0);
        u32x4 pv[4];
        u32x2 cxn, cx1;
        __builtin_amdgcn_s_sleep(RSL);
        red_load(rs, tid, 1, G, key, pv, cxn, cx1);
        red_sum(rs, smem, T, tid, w, 1, G, key, pv, cxn, cx1);
        BAR();
        if (w == 0) {
          const u32x2 ov = red_final<8>(smem, l, key, cxn, cx1);
          if (l < 8) __builtin_amdgcn_raw_buffer_store_b64(ov, rout, (key_row(key) * 1024 + G * 32 + l * 4) * 2, 0, 0);
        }
      }
#undef OUT_STORE
    }
  };
  layer(std::integral_constant<int, 0>{});
  layer(std::integral_constant<int, 1>{});
}

extern "C" void kernel_launch(void* const* d_in, const int* in_sizes, int n_in, void* d_out, int out_size,
                              void* d_ws, size_t ws_size, hipStream_t stream) {
  (void)hipFuncSetAttribute((const void*)mega_kernel, hipFuncAttributeMaxDynamicSharedMemorySize, LDS_BYTES);
  mega_kernel<<<256, 512, LDS_BYTES, stream>>>((const bf16*)d_in[0], (char*)d_ws, (const bf16*)d_in[1], (const bf16*)d_in[2], (const bf16*)d_in[4],
                                               (const bf16*)d_in[5], (const bf16*)d_in[6], (const bf16*)d_in[7], (bf16*)d_out);
}
```

```cpp
#include <hip/hip_runtime.h>
#include <stdint.h>
#include <utility>

typedef __bf16 bf16;
typedef __bf16 bf16x8 __attribute__((ext_vector_type(8)));
typedef __bf16 bf16x2 __attribute__((ext_vector_type(2)));
typedef float f32x4 __attribute__((ext_vector_type(4)));
typedef float f32x2 __attribute__((ext_vector_type(2)));
typedef short s16x4 __attribute__((ext_vector_type(4)));
typedef unsigned int u32x4 __attribute__((ext_vector_type(4)));
typedef unsigned int u32x2 __attribute__((ext_vector_type(2)));
#define LDS_AS __attribute__((address_space(3)))
#define GAS __attribute__((address_space(1)))
typedef GAS unsigned gu32;
typedef GAS unsigned long long gu64;
#define RLX_AGENT __ATOMIC_RELAXED, __HIP_MEMORY_SCOPE_AGENT
typedef __amdgpu_buffer_rsrc_t rsrc_t;

constexpr int cmax(int a, int b) { return a > b ? a : b; }
constexpr int F_ = 3584;
constexpr int SC1 = 16;

constexpr int RX = 4;
constexpr unsigned F_EP = 0, F_TMO = 128;
constexpr unsigned F_XN = 4096;
#define STR 8, 8, 4, 8
constexpr unsigned STV[4] = {STR}, SO = STV[0], SX = STV[1], SK = STV[2], SV = STV[3];
constexpr unsigned F_O = 8u << 20;
constexpr unsigned F_X1 = F_O + 131072;
constexpr unsigned OFF_KEY = F_O + 393216;
constexpr unsigned F_KV = F_O + 524288;
#define KVF(it, i) ((i) * 128 + (it) * 64)
#define OFL(h) (((h) >> 3) * 128 + ((h) & 7) * 8)
#define OFB(it, p) ((p) * 512 + (it) * 64)
constexpr unsigned OFF_AB = 147456;
constexpr unsigned OFF_XNEW = 262144;
constexpr unsigned OFF_CS = 294912;
constexpr unsigned OFF_FX = 393216;
constexpr unsigned OFF_CA = 524288;
constexpr unsigned OFF_CB = 655360;
constexpr unsigned OFF_STAT = 1u << 20;
constexpr unsigned OFF_PART = 3u << 20;
constexpr unsigned OFF_XN = 8u << 20;
constexpr unsigned OFF_O = 16u << 20;
constexpr unsigned OFF_KV = 32u << 20;
constexpr unsigned OFF_WI = 52u << 20;
#define REUSE 0
constexpr unsigned O_STEP = (REUSE & 1) ? 0 : 4u << 20, KV_STEP = (REUSE & 2) ? 0 : 8u << 20;
constexpr unsigned WS_BYTES = 58u << 20;
constexpr unsigned POISON = 0xFFFFFFFFu;

constexpr int L_ST0 = 147456, L_CNT = 147968, L_CSL = 31872, L_FXL = 32256, L_K = 0, L_V = 32768, L_RING = 65536, L_XCH = 0, L_DN = 32768, L_TOUCH0 = 149504, L_RDY = 31744, L_QC = 31808, LDS_BYTES = 163840;
constexpr int STAGE = 24576;
#define SLOT(t) ((t) & 3)

template <typename F, int... I> __device__ __forceinline__ void static_for_impl(F&& f, std::integer_sequence<int, I...>) {
  (f(std::integral_constant<int, I>{}), ...);
}
template <int N, typename F> __device__ __forceinline__ void static_for(F&& f) { static_for_impl(f, std::make_integer_sequence<int, N>{}); }
__device__ __forceinline__ unsigned pack2(float a, float b) {
  f32x2 v = {a, b};
  return __builtin_bit_cast(unsigned, __builtin_convertvector(v, bf16x2));
}
__device__ __forceinline__ float asf(unsigned u) { return __builtin_bit_cast(float, u); }
__device__ __forceinline__ float lo16(unsigned u) { return __builtin_bit_cast(float, u << 16); }
__device__ __forceinline__ float hi16(unsigned u) { return __builtin_bit_cast(float, u & 0xffff0000u); }
__device__ __forceinline__ u32x4 pack8(const f32x4 a, const f32x4 b) {
  u32x4 o;
  o[0] = pack2(a[0], a[1]); o[1] = pack2(a[2], a[3]); o[2] = pack2(b[0], b[1]); o[3] = pack2(b[2], b[3]);
  return o;
}
__device__ __forceinline__ f32x4 mfma16(u32x4 a, u32x4 b, f32x4 c) {
  return __builtin_amdgcn_mfma_f32_16x16x32_bf16(__builtin_bit_cast(bf16x8, a), __builtin_bit_cast(bf16x8, b), c, 0, 0, 0);
}
__device__ __forceinline__ f32x4 mfma16k16(s16x4 a, s16x4 b, f32x4 c) { return __builtin_amdgcn_mfma_f32_16x16x16bf16_1k(a, b, c, 0, 0, 0); }
template <int CTRL> __device__ __forceinline__ float dppf(float v) {
  return __builtin_bit_cast(float, __builtin_amdgcn_update_dpp(0, __builtin_bit_cast(int, v), CTRL, 0xf, 0xf, true));
}
template <int CTRL> __device__ __forceinline__ unsigned dppu(unsigned v) {
  return (unsigned)__builtin_amdgcn_update_dpp(0, (int)v, CTRL, 0xf, 0xf, true);
}
__device__ __forceinline__ float add_x16(float v) {
  auto r = __builtin_amdgcn_permlane16_swap(__builtin_bit_cast(unsigned, v), __builtin_bit_cast(unsigned, v), false, false);
  return __builtin_bit_cast(float, (unsigned)r[0]) + __builtin_bit_cast(float, (unsigned)r[1]);
}
__device__ __forceinline__ float add_x32(float v) {
  auto r = __builtin_amdgcn_permlane32_swap(__builtin_bit_cast(unsigned, v), __builtin_bit_cast(unsigned, v), false, false);
  return __builtin_bit_cast(float, (unsigned)r[0]) + __builtin_bit_cast(float, (unsigned)r[1]);
}
__device__ __forceinline__ float max_x16(float v) {
  auto r = __builtin_amdgcn_permlane16_swap(__builtin_bit_cast(unsigned, v), __builtin_bit_cast(unsigned, v), false, false);
  return fmaxf(__builtin_bit_cast(float, (unsigned)r[0]), __builtin_bit_cast(float, (unsigned)r[1]));
}
__device__ __forceinline__ float max_x32(float v) {
  auto r = __builtin_amdgcn_permlane32_swap(__builtin_bit_cast(unsigned, v), __builtin_bit_cast(unsigned, v), false, false);
  return fmaxf(__builtin_bit_cast(float, (unsigned)r[0]), __builtin_bit_cast(float, (unsigned)r[1]));
}
__device__ __forceinline__ unsigned umax_x16(unsigned v) {
  auto r = __builtin_amdgcn_permlane16_swap(v, v, false, false);
  return max((unsigned)r[0], (unsigned)r[1]);
}
__device__ __forceinline__ unsigned umax_x32(unsigned v) {
  auto r = __builtin_amdgcn_permlane32_swap(v, v, false, false);
  return max((unsigned)r[0], (unsigned)r[1]);
}
__device__ __forceinline__ float sum_hi8(float v) {
  v += dppf<0x128>(v);
  v = add_x16(v);
  return add_x32(v);
}
__device__ __forceinline__ float sum32(float v) {
  v += dppf<0xB1>(v);
  v += dppf<0x4E>(v);
  v += dppf<0x124>(v);
  v += dppf<0x128>(v);
  return add_x16(v);
}
__device__ __forceinline__ float sum64(float v) {
  v += dppf<0xB1>(v);
  v += dppf<0x4E>(v);
  v += dppf<0x124>(v);
  v += dppf<0x128>(v);
  v = add_x16(v);
  return add_x32(v);
}

#define BAR() asm volatile("s_waitcnt lgkmcnt(0)\n\ts_barrier" ::: "memory")
#define DRAIN() asm volatile("s_waitcnt vmcnt(0)" ::: "memory")
#define SB() __builtin_amdgcn_sched_barrier(0)
template <int N> __device__ __forceinline__ void wait_vm() { asm volatile("s_waitcnt vmcnt(%0)" ::"n"(N) : "memory"); }

__device__ __forceinline__ void dma16(rsrc_t rs, unsigned voff, unsigned soff, unsigned lds) {
  asm volatile("s_mov_b32 m0, %0\n\ts_nop 0\n\tbuffer_load_dwordx4 %1, %2, %3 offen lds" ::"s"(lds), "v"(voff), "s"(rs), "s"(soff) : "memory");
}
template <int OFF> __device__ __forceinline__ void dma16s(rsrc_t rs, unsigned voff, unsigned soff, unsigned lds) {
  asm volatile("s_mov_b32 m0, %0\n\ts_nop 0\n\tbuffer_load_dwordx4 %1, %2, %3 offen offset:%4 sc1 lds" ::"s"(lds), "v"(voff), "s"(rs), "s"(soff), "n"(OFF) : "memory");
}
__device__ __forceinline__ void touch(unsigned& d, const GAS bf16* ptr) { asm volatile("global_load_dword %0, %1, off" : "+v"(d) : "v"(ptr) : "memory"); }
__device__ __forceinline__ void ldrows(u32x4 (&d)[4], rsrc_t rx, unsigned voff, unsigned soff) {
  asm volatile("buffer_load_dwordx4 %0, %4, %5, %6 offen\n\tbuffer_load_dwordx4 %1, %4, %5, %6 offen offset:512\n\t"
               "buffer_load_dwordx4 %2, %4, %5, %6 offen offset:1024\n\tbuffer_load_dwordx4 %3, %4, %5, %6 offen offset:1536"
               : "=&v"(d[0]), "=&v"(d[1]), "=&v"(d[2]), "=&v"(d[3]) : "v"(voff), "s"(rx), "s"(soff) : "memory");
}
__device__ __forceinline__ void pin4(f32x2* v) { asm volatile("" : "+v"(v[0]), "+v"(v[1]), "+v"(v[2]), "+v"(v[3])); }
__device__ __forceinline__ void touchb(unsigned& d, rsrc_t rs, unsigned vo) { asm volatile("buffer_load_dword %0, %1, %2, 0 offen sc1" : "+v"(d) : "v"(vo), "s"(rs) : "memory"); }
__device__ __forceinline__ float rbf1(float x) { return __builtin_bit_cast(float, pack2(0.f, x)); }
__device__ __forceinline__ void vswap(unsigned& a, unsigned& b) { asm volatile("v_swap_b32 %0, %1" : "+v"(a), "+v"(b)); }
__device__ __forceinline__ void vswap(float& a, float& b) { asm volatile("v_swap_b32 %0, %1" : "+v"(a), "+v"(b)); }
template <typename V, int N> __device__ __forceinline__ void vswapn(V& a, V& b) {
#pragma unroll
  for (int i = 0; i < N; ++i) { auto x = a[i], y = b[i]; vswap(x, y); a[i] = x; b[i] = y; }
}
__device__ __forceinline__ u32x4 ld16(rsrc_t rs, unsigned voff, unsigned soff) { return __builtin_amdgcn_raw_buffer_load_b128(rs, voff, soff, SC1); }
template <int OFF> __device__ __forceinline__ void ld16h(u32x4& d, rsrc_t rs, unsigned voff, unsigned soff) {
  asm volatile("buffer_load_dwordx4 %0, %1, %2, %3 offen offset:%4 sc1" : "=v"(d) : "v"(voff), "s"(rs), "s"(soff), "n"(OFF) : "memory");
}
template <int I, int OFF> __device__ __forceinline__ void ld16w(u32x4& d, rsrc_t rs, unsigned voff, unsigned soff) {
  if constexpr (I == 0) asm volatile("buffer_load_dwordx4 %0, %1, %2, %3 offen offset:%4 sc1" : "={v[128:131]}"(d) : "v"(voff), "s"(rs), "s"(soff), "n"(OFF) : "memory");
  else if constexpr (I == 1) asm volatile("buffer_load_dwordx4 %0, %1, %2, %3 offen offset:%4 sc1" : "={v[132:135]}"(d) : "v"(voff), "s"(rs), "s"(soff), "n"(OFF) : "memory");
  else if constexpr (I == 2) asm volatile("buffer_load_dwordx4 %0, %1, %2, %3 offen offset:%4 sc1" : "={v[136:139]}"(d) : "v"(voff), "s"(rs), "s"(soff), "n"(OFF) : "memory");
  else if constexpr (I == 3) asm volatile("buffer_load_dwordx4 %0, %1, %2, %3 offen offset:%4 sc1" : "={v[140:143]}"(d) : "v"(voff), "s"(rs), "s"(soff), "n"(OFF) : "memory");
  else if constexpr (I == 4) asm volatile("buffer_load_dwordx4 %0, %1, %2, %3 offen offset:%4 sc1" : "={v[144:147]}"(d) : "v"(voff), "s"(rs), "s"(soff), "n"(OFF) : "memory");
  else if constexpr (I == 5) asm volatile("buffer_load_dwordx4 %0, %1, %2, %3 offen offset:%4 sc1" : "={v[148:151]}"(d) : "v"(voff), "s"(rs), "s"(soff), "n"(OFF) : "memory");
  else if constexpr (I == 6) asm volatile("buffer_load_dwordx4 %0, %1, %2, %3 offen offset:%4 sc1" : "={v[152:155]}"(d) : "v"(voff), "s"(rs), "s"(soff), "n"(OFF) : "memory");
  else if constexpr (I == 7) asm volatile("buffer_load_dwordx4 %0, %1, %2, %3 offen offset:%4 sc1" : "={v[156:159]}"(d) : "v"(voff), "s"(rs), "s"(soff), "n"(OFF) : "memory");
  else if constexpr (I == 8) asm volatile("buffer_load_dwordx4 %0, %1, %2, %3 offen offset:%4 sc1" : "={v[160:163]}"(d) : "v"(voff), "s"(rs), "s"(soff), "n"(OFF) : "memory");
  else if constexpr (I == 9) asm volatile("buffer_load_dwordx4 %0, %1, %2, %3 offen offset:%4 sc1" : "={v[164:167]}"(d) : "v"(voff), "s"(rs), "s"(soff), "n"(OFF) : "memory");
  else if constexpr (I == 10) asm volatile("buffer_load_dwordx4 %0, %1, %2, %3 offen offset:%4 sc1" : "={v[168:171]}"(d) : "v"(voff), "s"(rs), "s"(soff), "n"(OFF) : "memory");
  else if constexpr (I == 11) asm volatile("buffer_load_dwordx4 %0, %1, %2, %3 offen offset:%4 sc1" : "={v[172:175]}"(d) : "v"(voff), "s"(rs), "s"(soff), "n"(OFF) : "memory");
}
template <int K> __device__ __forceinline__ void pin_w(u32x4& a, u32x4& b) {
  if constexpr (K == 0) asm volatile("" : "+{v[128:131]}"(a), "+{v[132:135]}"(b)::"memory");
  else if constexpr (K == 1) asm volatile("" : "+{v[136:139]}"(a), "+{v[140:143]}"(b)::"memory");
  else if constexpr (K == 2) asm volatile("" : "+{v[144:147]}"(a), "+{v[148:151]}"(b)::"memory");
  else if constexpr (K == 3) asm volatile("" : "+{v[152:155]}"(a), "+{v[156:159]}"(b)::"memory");
  else if constexpr (K == 4) asm volatile("" : "+{v[160:163]}"(a), "+{v[164:167]}"(b)::"memory");
  else if constexpr (K == 5) asm volatile("" : "+{v[168:171]}"(a), "+{v[172:175]}"(b)::"memory");
}
__device__ __forceinline__ void ld8h(u32x2& d, rsrc_t rs, unsigned voff, unsigned soff) {
  asm volatile("buffer_load_dwordx2 %0, %1, %2, %3 offen sc1" : "=v"(d) : "v"(voff), "s"(rs), "s"(soff) : "memory");
}
__device__ __forceinline__ void ld16u(u32x4& d, rsrc_t rs, unsigned voff, unsigned soff) {
  asm volatile("buffer_load_dwordx4 %0, %1, %2, %3 offen sc1" : "+v"(d) : "v"(voff), "s"(rs), "s"(soff) : "memory");
}
template <int OFF> __device__ __forceinline__ void ld4h(unsigned& d, rsrc_t rs, unsigned voff, unsigned soff) {
  asm volatile("buffer_load_dword %0, %1, %2, %3 offen offset:%4 sc1" : "=v"(d) : "v"(voff), "s"(rs), "s"(soff), "n"(OFF) : "memory");
}
__device__ __forceinline__ void ld4x16(u32x4 (&d)[4], rsrc_t rs, unsigned voff, unsigned soff) {
  asm volatile("buffer_load_dwordx4 %0, %4, %5, %6 offen sc1\n\tbuffer_load_dwordx4 %1, %4, %5, %6 offen offset:64 sc1\n\t"
               "buffer_load_dwordx4 %2, %4, %5, %6 offen offset:128 sc1\n\tbuffer_load_dwordx4 %3, %4, %5, %6 offen offset:192 sc1\n\ts_waitcnt vmcnt(0)"
               : "=&v"(d[0]), "=&v"(d[1]), "=&v"(d[2]), "=&v"(d[3]) : "v"(voff), "s"(rs), "s"(soff) : "memory");
}
__device__ __forceinline__ u32x2 ld8(rsrc_t rs, unsigned voff, unsigned soff) { return __builtin_amdgcn_raw_buffer_load_b64(rs, voff, soff, SC1); }
__device__ __forceinline__ void st16(u32x4 v, rsrc_t rs, unsigned voff, unsigned soff) { __builtin_amdgcn_raw_buffer_store_b128(v, rs, voff + soff, 0, SC1); }
__device__ __forceinline__ void st8(u32x2 v, rsrc_t rs, unsigned voff, unsigned soff) { __builtin_amdgcn_raw_buffer_store_b64(v, rs, voff, soff, SC1); }

__device__ __forceinline__ unsigned ld4(rsrc_t rs, unsigned voff, unsigned soff) { return __builtin_amdgcn_raw_buffer_load_b32(rs, voff, soff, SC1); }
__device__ __forceinline__ void st4(unsigned v, rsrc_t rs, unsigned voff, unsigned soff) { __builtin_amdgcn_raw_buffer_store_b32(v, rs, voff, soff, SC1); }

constexpr unsigned kSpinCap = 1u << 16;
__device__ __forceinline__ bool spin_fail(unsigned& spins, rsrc_t rs, u32x2 T) {
  asm volatile("" ::: "memory");
  ++spins;
  if (spins > kSpinCap) { st8(T, rs, 0, F_TMO); return true; }
  if ((spins & 255u) == 0u) {
    const u32x2 v = ld8(rs, 0, F_TMO);
    if (v[0] == T[0] && v[1] == T[1]) return true;
  }
  __builtin_amdgcn_s_sleep(3);
  return false;
}
__device__ __forceinline__ void poll_flags2(rsrc_t rs, unsigned voff, u32x2 T) {
  for (unsigned spins = 0;;) {
    const u32x4 v = ld16(rs, voff, 0);
    if (__all(v[0] == T[0] && v[1] == T[1] && v[2] == T[0] && v[3] == T[1])) break;
    if (spin_fail(spins, rs, T)) break;
  }
  asm volatile("" ::: "memory");
  __builtin_amdgcn_fence(__ATOMIC_ACQUIRE, "wavefront");
}
template <int SL = 0> __device__ __forceinline__ void poll_flags(rsrc_t rs, unsigned voff, unsigned soff, u32x2 T) {
  for (unsigned spins = 0;;) {
    const u32x2 v = ld8(rs, voff, soff);
    if (__all(v[0] == T[0] && v[1] == T[1])) break;
    if (spin_fail(spins, rs, T)) break;
    if (SL) __builtin_amdgcn_s_sleep(SL);
  }
  asm volatile("" ::: "memory");
  __builtin_amdgcn_fence(__ATOMIC_ACQUIRE, "wavefront");
}

__device__ __forceinline__ unsigned toff(unsigned R, unsigned ks, unsigned g) {
  return (((R >> 5) * 64 + ks * 2 + ((R >> 4) & 1)) << 10) + ((g * 16 + (R & 15)) << 4);
}
__device__ __forceinline__ void unpack8v(const u32x4 u, f32x2* v) {
#pragma unroll
  for (int j = 0; j < 4; ++j) v[j] = f32x2{lo16(u[j]), hi16(u[j])};
}
__device__ __forceinline__ u32x4 pack8v(const f32x2* v) {
  u32x4 o;
#pragma unroll
  for (int j = 0; j < 4; ++j) o[j] = pack2(v[j][0], v[j][1]);
  return o;
}
__device__ __forceinline__ f32x2 rbf1v(f32x2 v) { return f32x2{rbf1(v[0]), rbf1(v[1])}; }
__device__ __forceinline__ float ln_rd(float var) { return __builtin_amdgcn_rcpf(rbf1(__builtin_amdgcn_sqrtf(rbf1(rbf1(var) + 1e-5f)))); }
template <int N> __device__ __forceinline__ void ln_apply(float mb, float rd, const f32x2* v, f32x2* y) {
  const f32x2 mb2 = {mb, mb}, rd2 = {rd, rd};
#pragma unroll
  for (int j = 0; j < N; ++j) y[j] = rbf1v(rbf1v(v[j] - mb2) * rd2);
}
__device__ __forceinline__ float hadd(f32x2 v) {
  float a = v[0];
  asm volatile("" : "+v"(a));
  return a + v[1];
}
__device__ __forceinline__ float add_g(float v) { return add_x32(add_x16(v)); }
__device__ __forceinline__ void add_g3(float& x, float& y, float& z) {
  auto a = __builtin_amdgcn_permlane16_swap(__builtin_bit_cast(unsigned, x), __builtin_bit_cast(unsigned, y), false, false);
  auto b = __builtin_amdgcn_permlane16_swap(__builtin_bit_cast(unsigned, z), __builtin_bit_cast(unsigned, z), false, false);
  const float r = asf(a[0]) + asf(a[1]), t = asf(b[0]) + asf(b[1]);
  auto c = __builtin_amdgcn_permlane32_swap(__builtin_bit_cast(unsigned, r), __builtin_bit_cast(unsigned, r), false, false);
  auto d = __builtin_amdgcn_permlane32_swap(__builtin_bit_cast(unsigned, t), __builtin_bit_cast(unsigned, t), false, false);
  const float u = asf(c[0]) + asf(c[1]);
  z = asf(d[0]) + asf(d[1]);
  auto e = __builtin_amdgcn_permlane16_swap(__builtin_bit_cast(unsigned, u), __builtin_bit_cast(unsigned, u), false, false);
  x = asf(e[0]); y = asf(e[1]);
}
__device__ __forceinline__ u32x4 trade16(u32x4 v) {
  auto a = __builtin_amdgcn_permlane16_swap(v[0], v[2], false, false);
  auto b = __builtin_amdgcn_permlane16_swap(v[1], v[3], false, false);
  return u32x4{(unsigned)a[0], (unsigned)b[0], (unsigned)a[1], (unsigned)b[1]};
}
__device__ __forceinline__ unsigned umax4(u32x4 v) { return max(max(v[0], v[1]), max(v[2], v[3])); }
__device__ __forceinline__ u32x2 unpoison2(u32x2 v) { return u32x2{min(v[0], POISON - 1), min(v[1], POISON - 1)}; }
__device__ __forceinline__ u32x4 unpoison4(u32x4 v) {
  return u32x4{min(v[0], POISON - 1), min(v[1], POISON - 1), min(v[2], POISON - 1), min(v[3], POISON - 1)};
}

typedef union { struct { s16x4 a, b; } hf; u32x4 v; } frag_t;
__device__ __forceinline__ s16x4 trread(const char* smem, unsigned off) {
  return __builtin_amdgcn_ds_read_tr16_b64_v4i16((LDS_AS s16x4*)(smem + off));
}
__device__ __forceinline__ s16x4 trabs(unsigned addr) { return __builtin_amdgcn_ds_read_tr16_b64_v4i16((LDS_AS s16x4*)(size_t)addr); }

constexpr int WA = 6;
#define WRV 3
#define WBV 4
constexpr int WB = WBV, WR = WRV;
#define CH 0, 4, 7, 8, 13, 15, 20, 22, 27, 29
constexpr int CHV[10] = {CH};
constexpr int FSL = CHV[1], FS0 = CHV[2], FS0D = CHV[3], FS1 = CHV[4], FS1S = CHV[5];
constexpr int FXA = CHV[6], FXB = CHV[7], FXL = CHV[8], FXS = CHV[9];
constexpr int FXT = FXB >> 2;
#define C0 27, 3, 16, 19, 22, 23, -1, 5, 4
constexpr int C0V[9] = {C0};
constexpr int IMG = C0V[0], CSD = C0V[1];
constexpr int ERQ = C0V[2], CSL = C0V[3], CSS = C0V[4];
constexpr int RWL = C0V[6], RWS = C0V[7], POI = C0V[8];
#define WTS 99
#define WTG 2
#define PBS 10
#define KP0 26
#define PS0 8
constexpr int EIN = cmax(CSD, cmax(RWS, POI)) + 3;
constexpr int ABS = 27;
constexpr int abl(int it) { return it ? 23 : C0V[5]; }
constexpr int XU = 3, XL = 2;
constexpr int x_slot(int u) { return u % XU == 0 ? 0 : u % XU == 1 ? 32768 : 49152; }
#define TCH 19, 7, 4, 27
constexpr int TCV[4] = {TCH};
constexpr int TS0 = TCV[0], TSN = TCV[1], TW = TCV[2], TST = TCV[3];
#define QLV 26
constexpr int QL = QLV;
#define FDG 0
#define XSL 4
#define FW0 4
#define FW1 4
#define X1F 1
#define RWG 0
#define CSG 4
#define RXCD 5
#define TWR TW
#define WDS (17 - WB)
#define WDW 1
#define OSL0 0
#define OSL1 0
#define OPS 0
#define KVS 0
#define LOP 0
#define SDI0 16
#define SLP 24, 14, 56, 3
constexpr int SLV[4] = {SLP};
constexpr int SDIR = SLV[0], GSL = SLV[1], RSL = SLV[2], KSL = SLV[3];
constexpr bool tile_piece(int s) { return s >= 3 && ((s - 3) >> 2) + 3 < 8 && ((s - 3) & 3) < 3; }
constexpr int a_req(int s, int n) {
  if (!(n & 1)) return -1;
  if (s <= 1) return 8 + 3 * s + (n >> 1);
  return n < 5 && s + 5 < 32 ? 2 * (s + 5) + (n >> 1) : -1;
}
constexpr int vm_n(int it, int s, int n) {
  int c = 0;
  if (n == 0) c += tile_piece(s);
  if (n == 4) c += (s == abl(it)) + (it ? (s == FSL ? 5 : 0) : (s == ERQ) + (s >= IMG && s < IMG + 3));
  if (!it) c += (s == WTS && n == WTG) + (s < 4 && n == 2) + (s == 0 && (n == 0 || n == 4)) + ((n == 3 || n == 5) && s + XL + XU < 32);
  else c += (a_req(s, n) >= 0) + ((s == 0 || s == 4) && n == 2);
  return c;
}
constexpr int vm_at(int it, int s, int n) {
  int c = it ? 8 : 14;
  for (int u = 0; u < 6 * s + n; ++u) c += vm_n(it, u / 6, u % 6);
  return c;
}
constexpr int vm_a(int it, int k) {
  if (k < 4) return 2 * k + 2;
  for (int u = 0; u < 192; ++u)
    if (a_req(u / 6, u % 6) == 2 * k + 1) return vm_at(it, (u + 1) / 6, (u + 1) % 6);
  return 0;
}
constexpr int vm_tile(int it, int t) { return t >= 3 ? vm_at(it, 4 * t - 7, 1) : t == 1 ? vm_at(0, 0, 5) : vm_at(0, 3, 3); }
constexpr int vm_x(int u) { return u == 2 ? 9 : u < XU + XL ? 12 + 2 * (u - XU) : vm_at(0, u - XU - XL + 1, 0); }

constexpr int wo_k(int it, int s) { return it || s < 4 ? 15 - s : s < 8 ? 7 - s : 19 - s; }
constexpr int wo_pos(int it, int s) { return 2 * s + 2 + (s >= WR ? 2 * it : 0); }
constexpr int wo_at(int it, int s) { return 2 * it + (s < WB - WR ? 2 * WR + 2 + 4 * s : s < 16 - WB ? 2 * (2 * WB - WR) + 2 * (s - WB + WR) : 32); }
template <int OFF> __device__ __forceinline__ void lds_count(unsigned ad) {
  asm volatile("s_mov_b64 exec, 1\n\tds_add_u32 %0, %1 offset:%2\n\ts_mov_b64 exec, -1" ::"v"(ad), "v"(1u), "n"(OFF) : "memory");
}
__device__ __forceinline__ void lds_look(char* c, unsigned want) {
  while ((unsigned)__builtin_amdgcn_readfirstlane(__hip_atomic_load((LDS_AS unsigned*)c, __ATOMIC_RELAXED, __HIP_MEMORY_SCOPE_WORKGROUP)) < want) {}
  asm volatile("" ::: "memory");
}
__device__ __forceinline__ void pin1(unsigned& a) { asm volatile("" : "+v"(a)); }
__device__ __forceinline__ void pin2(u32x4& a, u32x4& b) { asm volatile("" : "+v"(a), "+v"(b)::"memory"); }
__device__ __forceinline__ void pin2s(float& a, float& b) { asm volatile("" : "+v"(a), "+v"(b)); }
template <int OFF> __device__ __forceinline__ void lds2(u32x4& a, u32x4& b, unsigned ad) {
  asm volatile("ds_read_b128 %0, %2 offset:%3\n\tds_read_b128 %1, %2 offset:%4" : "=&v"(a), "=&v"(b) : "v"(ad), "n"(OFF), "n"(OFF + 1024) : "memory");
}
template <int N> __device__ __forceinline__ void lds_wait(u32x4& a, u32x4& b) { asm volatile("s_waitcnt lgkmcnt(%2)" : "+v"(a), "+v"(b) : "n"(N) : "memory"); }
__device__ __forceinline__ void pin2f(f32x2& a, f32x2& b) { asm volatile("" : "+v"(a), "+v"(b)); }
__device__ __forceinline__ void pin_ac(f32x4 (&ac)[2][4]) {
  asm volatile("" : "+{v[176:179]}"(ac[0][0]), "+{v[180:183]}"(ac[0][1]), "+{v[184:187]}"(ac[0][2]), "+{v[188:191]}"(ac[0][3]),
                    "+{v[192:195]}"(ac[1][0]), "+{v[196:199]}"(ac[1][1]), "+{v[200:203]}"(ac[1][2]), "+{v[204:207]}"(ac[1][3]));
}

constexpr int L_XC = LDS_BYTES - 64, L_SKEY = 32704, L_SRED = 4096, L_SFIN = 20480;
__device__ __forceinline__ unsigned key_load(rsrc_t rs, int l, int L, int rep) { return ld4(rs, (l & 15) * SK, OFF_KEY + (L * 16 + rep) * cmax(16 * SK, 128)); }
__device__ __forceinline__ unsigned umax16(unsigned k) {
  k = max(k, dppu<0xB1>(k));
  k = max(k, dppu<0x4E>(k));
  k = max(k, dppu<0x141>(k));
  return max(k, dppu<0x140>(k));
}
__device__ __forceinline__ unsigned key_row(unsigned key) { return (0xffffu - (key & 0xffffu)) & 2047u; }
__device__ __forceinline__ unsigned key_wait(rsrc_t rs, u32x2 T, int l, int L, int rep, unsigned kv) {
  if (!__all(kv != POISON)) {
    unsigned spins = 0;
    do {
      if (spin_fail(spins, rs, T)) break;
      if (L == 0) __builtin_amdgcn_s_sleep(KSL);
      kv = key_load(rs, l, L, rep);
    } while (!__all(kv != POISON));
  }
  return __builtin_amdgcn_readfirstlane(umax16(kv));
}
__device__ __forceinline__ unsigned ffn_xoff(unsigned key, int L, int w) { return OFF_CA + L * 32768 + (key_row(key) >> 7) * 2048 + w * 256; }
__device__ __forceinline__ void row_load(rsrc_t rs, int g, unsigned xo, u32x4 (&xv)[4]) {
  ld16h<0>(xv[0], rs, g * 16, xo); ld16h<64>(xv[1], rs, g * 16, xo); ld16h<128>(xv[2], rs, g * 16, xo); ld16h<192>(xv[3], rs, g * 16, xo);
}
template <int SL = 0> __device__ __forceinline__ void ffn_xwait(rsrc_t rs, u32x2 T, unsigned xo, int l, u32x4 (&xv)[4]) {
#define XV_OK() __all(max(max(umax4(xv[0]), umax4(xv[1])), max(umax4(xv[2]), umax4(xv[3]))) != POISON)
  if (!XV_OK()) {
    unsigned spins = 0;
    do {
      if (spin_fail(spins, rs, T)) break;
      if (SL) __builtin_amdgcn_s_sleep(SL);
#pragma unroll
      for (int i = 0; i < 4; ++i) xv[i] = ld16(rs, (l >> 4) * 16 + i * 64, xo);
    } while (!XV_OK());
  }
#undef XV_OK
}
__device__ __forceinline__ void ffn_gu(const u32x4 (&gv)[4], const u32x4 (&uv)[4], const u32x4 (&xv)[4], f32x4& ga, f32x4& ua) {
  ga = mfma16(gv[0], xv[0], f32x4{0.f, 0.f, 0.f, 0.f}); ua = mfma16(uv[0], xv[0], f32x4{0.f, 0.f, 0.f, 0.f});
#pragma unroll
  for (int i = 1; i < 4; ++i) { ga = mfma16(gv[i], xv[i], ga); ua = mfma16(uv[i], xv[i], ua); }
}
__device__ __forceinline__ void ffn_gu_check(rsrc_t rs, u32x2 T, unsigned xo, int l, const u32x4 (&gv)[4], const u32x4 (&uv)[4], u32x4 (&xv)[4], f32x4& ga, f32x4& ua) {
  if (__builtin_expect(__any(ga[0] != ga[0]), 0)) {
    ffn_xwait(rs, T, xo, l, xv);
    ffn_gu(gv, uv, xv, ga, ua);
  }
}
__device__ __forceinline__ void ffn_gu_store(char* smem, int l, int w, f32x4 ga, f32x4 ua) {
  LDS_AS f32x4* sred = (LDS_AS f32x4*)(smem + L_SRED);
  if ((l & 15) == 0) { sred[w * 8 + (l >> 4)] = ga; sred[w * 8 + 4 + (l >> 4)] = ua; }
}
__device__ __forceinline__ f32x4 ffn_t_read(char* smem, int l) { return ((const LDS_AS f32x4*)(smem + L_SRED))[(l & 7) * 8 + ((l >> 3) & 1) * 4 + (l >> 4)]; }
__device__ __forceinline__ void ffn_d_read(char* smem, int l, int blk, s16x4 (&dfr)[8]) {
#pragma unroll
  for (int i = 0; i < 8; ++i) dfr[i] = trread(smem, L_DN + blk * 4096 + (l >> 4) * 1024 + ((l & 15) >> 2) * 256 + i * 32 + (l & 3) * 8);
}
__device__ __forceinline__ float ffn_d_sum(float t) { t += dppf<0xB1>(t); t += dppf<0x4E>(t); return t + dppf<0x141>(t); }
__device__ __forceinline__ u32x2 ffn_h(f32x4 t, int l) {
#pragma unroll
  for (int e = 0; e < 4; ++e) t[e] = ffn_d_sum(t[e]);
  const float a = (l & 1) ? t[1] : t[0], b = (l & 1) ? t[3] : t[2], s = (l & 2) ? b : a;
  const float gg = rbf1(s), uu = rbf1(dppf<0x128>(s));
  const float h = rbf1(gg * __builtin_amdgcn_rcpf(1.0f + __expf(-gg))) * uu;
  const unsigned h0 = pack2(h, dppf<0x55>(h)), h1 = pack2(dppf<0xAA>(h), dppf<0xFF>(h));
  return (l & 15) == 0 ? u32x2{h0, h1} : u32x2{0u, 0u};
}
__device__ __forceinline__ void ffn_d_mma(const s16x4 (&dfr)[8], u32x2& hh, f32x4& ya, f32x4& yb) {
#pragma unroll
  for (int i = 0; i < 8; ++i) {
    if (i & 1) yb = mfma16k16(dfr[i], __builtin_bit_cast(s16x4, hh), yb);
    else ya = mfma16k16(dfr[i], __builtin_bit_cast(s16x4, hh), ya);
    hh = u32x2{dppu<0x111>(hh[0]), dppu<0x111>(hh[1])};
  }
}
__device__ __forceinline__ void ffn_d_store(rsrc_t rs, int l, int G0, int L, int slice, f32x4 y) {
  const unsigned c = l & 15;
  st16(unpoison4(__builtin_bit_cast(u32x4, y)), rs, (G0 + (c >> 1)) * 32768 + slice * 128 + (c & 1) * 64 + (l >> 4) * 16, OFF_PART + L * (1u << 20));
}
__device__ __forceinline__ void red_load(rsrc_t rs, int tid, int L, unsigned G, unsigned key, u32x4 (&pv)[4], u32x2& cxn, u32x2& cx1) {
#pragma unroll
  for (int k = 0; k < 4; ++k) pv[k] = ld16(rs, min(tid + 512 * k, 1791) * 16, OFF_PART + L * (1u << 20) + G * 32768);
  cxn = ld8(rs, (tid & 3) * 16 + (tid & 4) * 2, OFF_CA + L * 32768 + (key_row(key) >> 7) * 2048 + G * 64);
  cx1 = ld8(rs, (tid & 3) * 16 + (tid & 4) * 2, OFF_CB + L * 32768 + (key_row(key) >> 7) * 2048 + G * 64);
}
__device__ __forceinline__ void red_sum(rsrc_t rs, char* smem, u32x2 T, int tid, int w, int L, unsigned G, unsigned key,
                                        u32x4 (&pv)[4], u32x2& cxn, u32x2& cx1) {
#define PV_OK() __all(max(max(max(umax4(pv[0]), umax4(pv[1])), max(umax4(pv[2]), umax4(pv[3]))), max(max(cxn[0], cxn[1]), max(cx1[0], cx1[1]))) != POISON)
  if (!PV_OK()) {
    unsigned spins = 0;
    do {
      if (spin_fail(spins, rs, T)) break;
      red_load(rs, tid, L, G, key, pv, cxn, cx1);
    } while (!PV_OK());
  }
#undef PV_OK
  f32x4 s = (__builtin_bit_cast(f32x4, pv[0]) + __builtin_bit_cast(f32x4, pv[1])) + __builtin_bit_cast(f32x4, pv[2]);
  if (w < 4) s += __builtin_bit_cast(f32x4, pv[3]);
  ((LDS_AS f32x4*)(smem + L_SFIN))[tid] = s;
}
template <int NW> __device__ __forceinline__ u32x2 red_final(char* smem, int l, unsigned key, u32x2 cxn, u32x2 cx1) {
  const LDS_AS f32x4* sf = (const LDS_AS f32x4*)(smem + L_SFIN) + l;
  f32x4 s = (sf[0] + sf[64]) + (sf[128] + sf[192]);
  if constexpr (NW == 8) s += (sf[256] + sf[320]) + (sf[384] + sf[448]);
#pragma unroll
  for (int j = 0; j < 4; ++j) s[j] = sum_hi8(s[j]);
  const unsigned k16 = key >> 16;
  const float rmax = __builtin_bit_cast(float, ((k16 & 0x8000u) ? (k16 & 0x7fffu) : (~k16 & 0xffffu)) << 16);
  const float sig = rbf1(__builtin_amdgcn_rcpf(1.0f + __expf(-rmax)));
  const float xn4[4] = {lo16(cxn[0]), hi16(cxn[0]), lo16(cxn[1]), hi16(cxn[1])};
  const float x14[4] = {lo16(cx1[0]), hi16(cx1[0]), lo16(cx1[1]), hi16(cx1[1])};
  float o[4];
#pragma unroll
  for (int j = 0; j < 4; ++j) o[j] = x14[j] + rbf1(xn4[j] + rbf1(rbf1(s[j]) * sig));
  return u32x2{pack2(o[0], o[1]), pack2(o[2], o[3])};
}

struct RowFin { float mean, var, mb, rd; unsigned key; };
__device__ __forceinline__ void fin_wait(rsrc_t rs, u32x2 T, unsigned voff, unsigned so, u32x4 (&sp)[4]) {
#define ST_OK() __all(max(max(umax4(sp[0]), umax4(sp[1])), max(umax4(sp[2]), umax4(sp[3]))) != POISON)
  if (!ST_OK()) {
    unsigned spins = 0;
    do {
      if (spin_fail(spins, rs, T)) break;
#pragma unroll
      for (int i = 0; i < 4; ++i) sp[i] = ld16(rs, voff + i * 2048, so);
    } while (!ST_OK());
  }
#undef ST_OK
}
__device__ __forceinline__ RowFin fin_key(rsrc_t rs, u32x2 T, unsigned voff, unsigned so, char* skey, int l, int w, unsigned R, float wsum, u32x4 (&sp)[4]) {
  RowFin r;
#define ST_SUM() ((__builtin_bit_cast(f32x4, sp[0]) + __builtin_bit_cast(f32x4, sp[1])) + (__builtin_bit_cast(f32x4, sp[2]) + __builtin_bit_cast(f32x4, sp[3])))
  f32x4 sf = ST_SUM();
  const float z = (sf[0] + sf[1]) + sf[2];
  if (__any(z != z)) {
    fin_wait(rs, T, voff, so, sp);
    sf = ST_SUM();
  }
#undef ST_SUM
  float s0 = sf[0], s1 = sf[1], s2 = sf[2];
  add_g3(s0, s1, s2);
  r.mean = s0 * (1.0f / 1024.0f);
  r.var = fmaxf(__builtin_fmaf(-r.mean, r.mean, s1 * (1.0f / 1024.0f)), 0.f);
  r.mb = rbf1(r.mean); r.rd = ln_rd(r.var);
  const unsigned rb16 = pack2((s2 - r.mb * wsum) * r.rd, 0.f) & 0xffffu;
  r.key = min((((rb16 & 0x8000u) ? (~rb16 & 0xffffu) : (rb16 | 0x8000u)) << 16) | (0xffffu - R), POISON - 1);
  const unsigned wk = umax16(r.key);
  if (l == 0) ((LDS_AS unsigned*)skey)[w] = wk;
  return r;
}
__device__ __forceinline__ unsigned fin_cand(rsrc_t rs, char* skey, int L, int g, int rb, int ct, const RowFin& r, const f32x2 (&v)[8]) {
  const u32x4 k0 = *(const LDS_AS u32x4*)skey, k1 = *(const LDS_AS u32x4*)(skey + 16);
  const unsigned best = max(umax4(k0), umax4(k1));
  if (r.key == best) {
    const unsigned co = L * 32768 + rb * 2048 + ct * 128 + g * 16;
    f32x2 t[8], rd2 = {r.rd, r.rd}, of2 = {-r.mb * r.rd, -r.mb * r.rd};
    asm volatile("" : "+v"(rd2), "+v"(of2));
#pragma unroll
    for (int j = 0; j < 8; ++j) t[j] = __builtin_elementwise_fma(v[j], rd2, of2);
    st16(unpoison4(pack8v(t)), rs, co, OFF_CA); st16(unpoison4(pack8v(t + 4)), rs, co + 64, OFF_CA);
    if (g == 0) st4(best, rs, OFF_KEY + (L * 16 + ct) * cmax(16 * SK, 128) + rb * SK, 0);
    st16(unpoison4(pack8v(v)), rs, co, OFF_CB); st16(unpoison4(pack8v(v + 4)), rs, co + 64, OFF_CB);
  }
  return best;
}
__device__ __forceinline__ void fin_x2(const RowFin& r, f32x2 (&v)[8]) {
  f32x2 sc2 = {1.0f + r.rd, 1.0f + r.rd}, of2 = {-r.mb * r.rd, -r.mb * r.rd};
  asm volatile("" : "+v"(sc2), "+v"(of2));
#pragma unroll
  for (int j = 0; j < 8; ++j) v[j] = __builtin_elementwise_fma(v[j], sc2, of2);
}

extern "C" __device__ unsigned long long dispatch_id() __asm("llvm.amdgcn.dispatch.id");

extern "C" __global__ __launch_bounds__(512, 2) void mega_kernel(const bf16* x, char* ws, const bf16* wqkv, const bf16* wo, const bf16* wr,
                                                                 const bf16* wg, const bf16* wu, const bf16* wd, bf16* out) {
  extern __shared__ __attribute__((aligned(16))) char smem[];
  const int w0_ = __builtin_amdgcn_readfirstlane(threadIdx.x >> 6);
  typedef unsigned long long u64x2 __attribute__((ext_vector_type(2)));
  u64x2 a01;
  unsigned long long a2;
  asm volatile("s_load_dwordx4 %0, %2, 0\n\ts_load_dwordx2 %1, %2, 16" : "=&s"(a01), "=&s"(a2) : "s"(__builtin_amdgcn_kernarg_segment_ptr()));
  const unsigned lbase = (unsigned)(size_t)(LDS_AS char*)smem;
  {
    unsigned long long pc;
    unsigned len;
    asm volatile("s_getpc_b64 %0\n\ts_mov_b32 %1, .Ltext_end-mega_kernel\n\t.subsection 1\n.Ltext_end:\n\t.subsection 0" : "=s"(pc), "=s"(len));
    const unsigned o = ((blockIdx.x >> 3) * 64 + (threadIdx.x & 63)) * 128;
    if (w0_ == 7 && o < len)
      asm volatile("s_mov_b32 m0, %0\n\ts_nop 0\n\tglobal_load_lds_dword %1, %2" ::"s"(lbase + L_TOUCH0), "v"(o), "s"(pc & ~127ull) : "memory");
  }
#define HGRP() ((xcd & 2) + (xcd >> 2))
#define IDS()                                                                                         \
  int tid = threadIdx.x, w = w0_, bid = blockIdx.x;                                                   \
  asm volatile("" : "+v"(tid));                                                                       \
  asm volatile("" : "+s"(w));                                                                         \
  asm volatile("" : "+s"(bid));                                                                       \
  const int l = tid & 63, xcd = bid & 7, bi = bid >> 3;                                               \
  const int p = (xcd & 1) * 4 + (bi & 3), h = HGRP() * 8 + (bi >> 2);         \
  const int rb = 2 * xcd + (bi & 1), ct = bi >> 1;                               \
  const int slice = (xcd - (xcd > RXCD)) * 32 + bi;                                            \
  const bool ffn_on = xcd != RXCD;                                                                    \
  const int g = l >> 4;                                                                               \
  const int rt = w < 4 ? w : 11 - w;          \
  KARGS();                                                                                            \
  (void)p; (void)h; (void)rb; (void)ct; (void)slice; (void)ffn_on; (void)g; (void)rt;
#define PROT() (2 * (p & 3) + (p >> 2))
  typedef const void* const __attribute__((address_space(4)))* karg_t;
#define KARGS() karg_t ka = (karg_t)__builtin_amdgcn_kernarg_segment_ptr(); asm volatile("" : "+s"(ka))
#define ARGP(name, i) const GAS bf16* name = (const GAS bf16*)ka[i]; asm volatile("" : "+s"(name))
#define RSRC(ptr, bytes) __builtin_amdgcn_make_buffer_rsrc((void*)(ptr), 0, bytes, 0x00020000)
#define rwq RSRC(pwq, 1024 * 3072 * 2)
#define rwo RSRC(pwo, 1024 * 1024 * 2)
#define rwg RSRC(pwg, 1024 * F_ * 2)
#define rwu RSRC(pwu, 1024 * F_ * 2)
#define rwd RSRC(pwd, 1024 * F_ * 2)
#define rout RSRC(pout, 2048 * 1024 * 2)

#define WQ_VOFF() ((l >> 2) * 6144 + (((l & 3) ^ ((((l >> 4) ^ (l >> 5)) & 1) << 1)) << 4))
#define WQ_KMAJ(q) (((q) % 3) * 8 + (q) / 3)
#define WQ_PIECE(t, j, dst) dma16(rwq, wqv, ((((t) + rot) & 7) * 128 + ((j) & 7) * 16) * 6144 + ((j) >> 3) * 2048 + h * 64, lbase + (dst) + (j) * 1024)
#define WQ_BOFF(dt) ((g >> 1) * 1024 + (8 * (g & 1) + ((l & 15) >> 2)) * 64 + (l & 3) * 8 + (((dt) ^ (g & 1)) << 5))
#define X_REQ(u, hf) dma16(rx, xv[hf], (p * 256 + rt * 32 + ((u) & 1) * 16 + (hf) * 8) * 2048 + ((((u) >> 2) + rot) & 7) * 256 + (((u) >> 1) & 1) * 128, lbase + rt * 2048 + x_slot(u) + (hf) * 1024)
#define WO_PIECE(j) dma16(rwo, wov, ((w >> 2) * 64 + (w & 3) + 4 * (j)) * 16384 + ct * 128, lbase + 32768 + (j) * 8192 + w * 1024)
#define WD_DMA_OF(v) _Pragma("unroll") for (int i = 0; i < 4; ++i)                                  \
      dma16(rwd, (l >> 4) * 2048 + (l & 15) * 16, (unsigned)(slice * 16 + 4 * i) * 2048 + (v) * 256, lbase + L_DN + (v) * 4096 + i * 1024)
#define FFN_DMA(m) dma16((m) & 1 ? rwu : rwg, fnv, (unsigned)(((4 * w + ((m) >> 1)) * 32) * F_ + slice * 16) * 2, lbase + 65536 + (m) * 8192 + w * 1024)
#define POISON_BIG(L)                                                                                \
  if (w >= 2) {                                                                                      \
    unsigned pz1 = POISON;                                                                           \
    asm volatile("" : "+v"(pz1));                                                                    \
    st16(u32x4{pz1, pz1, pz1, pz1}, rs, (w >= 4 ? OFF_PART + (L) * (1u << 20) + bid * 4096 - 4096 : OFF_STAT + (L) * 524288 + (p * 32 + h) * 2048 - 2048) + tid * 16, 0); \
  }
  u32x4 gv[4], uv[4];
  u32x4 keep[9];
  u32x4 pre[4][2];
#define PRE_LOAD(it_, rot_, s0, s1)                                                                  \
  static_for<(s1) - (s0)>([&](auto c_) __attribute__((always_inline)) {                              \
    constexpr int s = (s0) + decltype(c_)::value;                                                    \
    const unsigned ab_ = OFF_XN + (it_) * (4u << 20) + (((rot_) + (s >> 2)) & 7) * 8192 + ((s >> 1) & 1) * 4096; \
    ld16w<2 * s, (s & 1) * 2048>(pre[s][0], rs, l * 16, ab_ + (p * 8 + rt) * 65536);                         \
    ld16w<2 * s + 1, (s & 1) * 2048 + 1024>(pre[s][1], rs, l * 16, ab_ + (p * 8 + rt) * 65536);                 \
  })
  u32x2 T;
  const GAS bf16* tsrc;
  unsigned xv[2], xa[2];
  u32x2 ep;
  u32x4 rows0[4];
#define X_READ(u, d0, d1) { d0 = *(const LDS_AS u32x4*)(size_t)(xa[0] + x_slot(u)); d1 = *(const LDS_AS u32x4*)(size_t)(xa[1] + x_slot(u)); }
  unsigned g1[5], wqv0;
  {
    IDS();
    const int rot = PROT(), qa = WQ_KMAJ(w), qb = WQ_KMAJ(8 + w);
    wqv0 = WQ_VOFF();
    xv[0] = (l >> 3) * 2048 + (((l & 7) ^ (l >> 4)) << 4); xv[1] = xv[0] ^ 64;
    g1[0] = (rot * 128 + (qa & 7) * 16) * 6144 + (qa >> 3) * 2048 + h * 64; g1[1] = lbase + L_RING + qa * 1024;
    g1[2] = (rot * 128 + (qb & 7) * 16) * 6144 + (qb >> 3) * 2048 + h * 64; g1[3] = lbase + L_RING + qb * 1024;
    g1[4] = (p * 256 + rt * 32) * 2048 + rot * 256;
    asm volatile("" : "+v"(wqv0), "+v"(xv[0]), "+v"(xv[1]));
    asm volatile("s_waitcnt lgkmcnt(0)" : "+s"(a01), "+s"(a2), "+s"(g1[0]), "+s"(g1[1]), "+s"(g1[2]), "+s"(g1[3]), "+s"(g1[4]));
  }
  x = (const bf16*)a01[0]; ws = (char*)a01[1]; wqkv = (const bf16*)a2;
  const rsrc_t rs = __builtin_amdgcn_make_buffer_rsrc((void*)ws, 0, WS_BYTES, 0x00020000);
  const rsrc_t rx = RSRC(x, 2048 * 1024 * 2);
  {
    IDS();
    const int rot = PROT();
    const bf16* pwq = wqkv;
    const unsigned wqv = wqv0;
    dma16(rwq, wqv, g1[0], g1[1]);
#pragma unroll
    for (int i = 0; i < 4; ++i) dma16(rx, xv[i & 1], g1[4] + i * 16384, lbase + rt * 2048 + x_slot(i >> 1) + (i & 1) * 1024);
    dma16(rwq, wqv, g1[2], g1[3]);
    asm volatile("s_barrier" ::: "memory");
    WQ_PIECE(0, WQ_KMAJ(16 + w), L_RING);
    if (w == 6 && bi == 0) {
      ARGP(pwr, 4);
      asm volatile("s_mov_b32 m0, %0\n\ts_nop 0\n\tbuffer_load_dword %1, %2, 0 offen lds" ::"s"(lbase + L_TOUCH0 + 256), "v"((l & 15) * 128), "s"(RSRC(pwr, 2048)) : "memory");
    }
    X_REQ(2, 0); X_REQ(2, 1);
    asm volatile("buffer_load_dwordx2 %0, off, %1, 0 sc1" : "=v"(ep) : "s"(rs) : "memory");
    if constexpr (RWL == -1) {
      asm volatile("" : "=v"(rows0[0]), "=v"(rows0[1]), "=v"(rows0[2]), "=v"(rows0[3]));
      if (w < 4) ldrows(rows0, rx, (l >> 5) * 2048 + (l & 31) * 16, (p * 256 + h * 8 + 2 * w) * 2048);
    }
    if constexpr (PBS < 0) { POISON_BIG(0); }
    {
      ARGP(pwo, 3); ARGP(pwg, 5); ARGP(pwu, 6);
      tsrc = (w < 4 ? pwg : pwu) + ((bi * 32 + ((tid & 255) >> 3)) * F_ + (slice >> 5) * 512 + (tid & 7) * 64);
      if (!ffn_on) tsrc = pwo + (bi * 512 + tid) * 64;
      asm volatile("" : "+v"(tsrc));
    }
#pragma unroll
    for (int kk = 0; kk < 2; ++kk) {
      const int r = l & 15;
      xa[kk] = lbase + rt * 2048 + (r >> 3) * 1024 + (r & 7) * 128 + (((4 * kk + g) ^ (r >> 1)) << 4);
      asm volatile("" : "+v"(xa[kk]));
    }
    if (tid == 192) *(LDS_AS u32x4*)(smem + L_QC) = u32x4{0u, 0u, 0u, 0u};
    if (RWL == -1 && w < 4) wait_vm<8>(); else wait_vm<4>();
    X_READ(0, pre[0][0], pre[1][0]); X_READ(1, pre[0][1], pre[1][1]);
    asm volatile("s_waitcnt lgkmcnt(0)\n\ts_barrier" : "+v"(pre[0][0]), "+v"(pre[0][1]), "+v"(pre[1][0]), "+v"(pre[1][1])::"memory");
    if constexpr (RWL == -2) {
      asm volatile("" : "=v"(rows0[0]), "=v"(rows0[1]), "=v"(rows0[2]), "=v"(rows0[3]));
      if (w < 4) ldrows(rows0, rx, (l >> 5) * 2048 + (l & 31) * 16, (p * 256 + h * 8 + 2 * w) * 2048);
    }
    X_REQ(3, 0); X_REQ(3, 1); X_REQ(4, 0); X_REQ(4, 1);
  }
  unsigned sel0 = 0x7FFFFFFFu;
  u32x4 x2p[2];
  unsigned xm0 = 0, kg0 = 0;

  auto layer = [&](auto it_c) __attribute__((always_inline)) {
    constexpr int it = decltype(it_c)::value;
    f32x4 acc[2][6];
    f32x2 qs2[2];
    u32x4 k0, k1, v0, v1;
    unsigned tdum = 0;
    {
      IDS();
      bool aff = false;
      unsigned aq[2];
      u32x4 av[2];
#define PIN_ACC() asm volatile("" : "+{v[176:179]}"(acc[0][0]), "+{v[180:183]}"(acc[0][1]), "+{v[184:187]}"(acc[0][2]), "+{v[188:191]}"(acc[0][3]), "+{v[192:195]}"(acc[0][4]), "+{v[196:199]}"(acc[0][5]), \
                               "+{v[200:203]}"(acc[1][0]), "+{v[204:207]}"(acc[1][1]), "+{v[208:211]}"(acc[1][2]), "+{v[212:215]}"(acc[1][3]), "+{v[216:219]}"(acc[1][4]), "+{v[220:223]}"(acc[1][5]))
#define PINV4(x) asm volatile("" : "+v"(x[0]), "+v"(x[1]), "+v"(x[2]), "+v"(x[3])::"memory")
      auto qkv_loop = [&](auto it_c) __attribute__((always_inline)) {
        constexpr int IT = decltype(it_c)::value;
        const unsigned abase = OFF_XN + IT * (4u << 20) + (p * 8 + rt) * 65536;
        const int rot = PROT() + 5 * IT;
        unsigned fkey = 0, kv, bq[2];
        u32x4 fx[4], bv[2], af[WA][2];
        u32x2 cxn, cx1;
        f32x4 sa, sb, cs4[2];
        f32x2 b2[2];
#define AB_OFF() (OFF_AB + IT * 16384 + (p * 8 + rt) * 128)
#define A_LOAD1(k, a) ld16w<(k) % WA * 2 + (a), ((k) & 1) * 2048 + (a) * 1024>(af[(k) % WA][a], rs, l * 16, abase + ((((k) >> 2) + rot) & 7) * 8192 + (((k) >> 1) & 1) * 4096)
#pragma unroll
        for (int s = 0; s < 2 + 2 * IT; ++s) { af[s][0] = pre[s][0]; af[s][1] = pre[s][1]; }
        const bf16* pwq = wqkv;
        const int tstep = TS0 + bid % TSN;
        const unsigned wqv = WQ_VOFF();
#define QKV_DMA1(t, i)                                                                            \
  {                                                                                               \
    if constexpr (IT == 0) WQ_PIECE(t, 3 * w + (i), L_RING + SLOT(t) * STAGE);                    \
    else dma16s<(i) * 1024>(rs, l * 16, OFF_WI + (h * 8 + (((t) + rot) & 7)) * STAGE + w * 3072, lbase + L_RING + SLOT(t) * STAGE + w * 3072); \
  }
        unsigned bo[2][2];
#pragma unroll
        for (int dt = 0; dt < 2 - IT; ++dt) {
          bo[dt][0] = lbase + L_RING + (IT ? l * 16 : WQ_BOFF(dt)); bo[dt][1] = bo[dt][0] + 2 * STAGE;
          asm volatile("" : "+v"(bo[dt][0]), "+v"(bo[dt][1]));
        }
#define FRAG_T() (lbase + L_RING + (4 * g + ((l & 15) >> 2)) * 64 + (l & 3) * 8 + (((g ^ (g >> 1)) & 1) << 5))
        auto frag_t2 = [&](unsigned o) __attribute__((always_inline)) {
          frag_t t;
          t.hf.a = trabs(o); t.hf.b = trabs(o + 1024);
          return t.v;
        };
        unsigned ka[3];
        auto tile_sum = [&](u32x4 pt, int dst) __attribute__((always_inline)) {
          f32x4 c = __builtin_bit_cast(f32x4, pt);
#pragma unroll
          for (int e = 0; e < 4; ++e) { c[e] += dppf<0x124>(c[e]); c[e] += dppf<0x128>(c[e]); c[e] = add_x16(c[e]); }
          if ((l & 28) == 0) *(LDS_AS f32x4*)(smem + dst + (w * 2 + (l >> 5)) * 64 + (l & 3) * 16) = c;
        };
        frag_t bfr[6];
#define QKV_BO(s, n) (bo[(n) & 1][SLOT((s) >> 2) >> 1] + (SLOT((s) >> 2) & 1) * STAGE + ((n) >> 1) * 8192 + ((s) & 3) * 2048)
#define QKV_RDA(s, n)                                                                             \
  {                                                                                               \
    if constexpr (IT == 0) bfr[n].hf.a = trabs(QKV_BO(s, n));                                     \
    else bfr[n].v = *(const LDS_AS u32x4*)(size_t)(bo[0][SLOT((s) >> 2) >> 1] + (SLOT((s) >> 2) & 1) * STAGE + (((s) & 3) * 6 + (n)) * 1024); \
  }
#define QKV_RDB(s, n) { if constexpr (IT == 0) bfr[n].hf.b = trabs(QKV_BO(s, (n) ^ 1) + 256); }
#pragma unroll
        for (int n = 0; n < 6; ++n) { QKV_RDA(0, n); if (n < 5) QKV_RDB(0, n); }
#define XF_OFF() (l & 8 ? ((l & 7) * RX + bi % RX) * 256 + h * 8 : (p * RX + (xcd >> 1) % RX) * 256 + (4 * rt + (l & 3)) * 8)
#define XP_OFF() (OFF_XNEW + ((rot + FXT) & 7) * 256)
#define FP_OFF(i) (w == 3 ? OFF_XNEW + (i) * 1024 : OFF_FX + h * 3072 + w * 1024)
#define FX_OK(n) __all(((n) == 4 ? max(max(umax4(fx[0]), umax4(fx[1])), max(umax4(fx[2]), umax4(fx[3]))) : max(umax4(fx[0]), umax4(fx[1]))) != POISON)
#define SINCE(s0) (vm_at(IT, s, n) - vm_at(IT, s0, 5))
#define DEF4(x) asm volatile("" : "=v"(x[0]), "=v"(x[1]), "=v"(x[2]), "=v"(x[3]))
        LDS_AS unsigned* qcnt = (LDS_AS unsigned*)(smem + L_QC);
        unsigned qc;
        unsigned qadr = lbase + L_QC, qone = 1;
        asm volatile("" : "+v"(qadr), "+v"(qone));
#define count_in(i) asm volatile("s_mov_b64 exec, 1\n\tds_add_u32 %0, %1 offset:%2\n\ts_mov_b64 exec, -1" ::"v"(qadr), "v"(qone), "n"(4 * (i)) : "memory")
        auto count_wait = [&](int i, unsigned want) __attribute__((always_inline)) {
          if (__builtin_expect((unsigned)__builtin_amdgcn_readfirstlane(qc) < want, 0)) {
            while ((unsigned)__builtin_amdgcn_readfirstlane(__hip_atomic_load(qcnt + i, __ATOMIC_RELAXED, __HIP_MEMORY_SCOPE_WORKGROUP)) < want) {}
          }
          asm volatile("" ::: "memory");
        };
        unsigned long long xok = xm0;
        auto x1_need = [&](unsigned long long m) __attribute__((always_inline)) {
          if (__builtin_expect((xok & m) != m, 0)) {
            for (unsigned spins = 0;;) {
              const u32x2 f = ld8(rs, F_X1 + ((p * RX + (xcd >> 1) % RX) * 32 + (l & 31)) * SX, 0);
              xok = __builtin_amdgcn_ballot_w64(f[0] == T[0] && f[1] == T[1]);
              if ((xok & m) == m) break;
              if (spin_fail(spins, rs, T)) { xok = ~0ull; break; }
              __builtin_amdgcn_s_sleep(KSL);
            }
            asm volatile("" ::: "memory");
          }
        };
        auto gap = [&](auto sc_, auto nc_) __attribute__((always_inline)) {
          constexpr int s = decltype(sc_)::value, n = decltype(nc_)::value;
          {
            constexpr int ts = (s + 3) / 4, tc = (s + 2) / 4, t0 = 1;
            if constexpr (ts >= 3 && ts < 8 && s == 4 * ts - 3 && n == 2) { wait_vm<vm_at(IT, s, n) - vm_tile(IT, ts)>(); count_in(0); }
            if constexpr (!IT && tc >= t0 && tc < 3 && s == 4 * tc - 2 && n == 1) { wait_vm<vm_at(IT, s, n) - vm_tile(IT, tc)>(); count_in(0); }
            if constexpr (IT && ts >= t0 && ts < 3 && s == 4 * ts - 3 && n == 2) count_in(0);
            if constexpr (tc >= t0 && tc < 8 && s == 4 * tc - 2) {
              if constexpr (n == 3) qc = __hip_atomic_load(qcnt, __ATOMIC_RELAXED, __HIP_MEMORY_SCOPE_WORKGROUP);
              if constexpr (n == 5) count_wait(0, 8 * (tc - t0 + 1));
            }
          }
          if constexpr (n == 0 && tile_piece(s)) QKV_DMA1(((s - 3) >> 2) + 3, (s - 3) & 3);
          if constexpr (IT == 0) {
            if constexpr (s == 0 && !(n & 1)) QKV_DMA1(1, n >> 1);
            if constexpr (s >= 1 && s < 4 && n == 2) QKV_DMA1(2, s - 1);
            constexpr int u = s + XL;
            if constexpr (n == 1 && u < 32) {
              if (s == 0 && RWL < 0 && w < 4) wait_vm<vm_at(0, s, n) - vm_x(u) + 4>(); else wait_vm<vm_at(0, s, n) - vm_x(u)>();
              X_READ(u, af[(u & ~1) % WA][u & 1], af[(u | 1) % WA][u & 1]);
            }
            if constexpr (n == 3 && u + XU < 32) { asm volatile("s_waitcnt lgkmcnt(%0)" ::"n"(s + 1 < QL ? 4 : 3) : "memory"); X_REQ(u + XU, 0); }
            if constexpr (n == 5 && u + XU < 32) X_REQ(u + XU, 1);
            if constexpr (s == WTS && n == WTG) {
              const int i = bi * 192 + w * 24 + min(l, 23);
              touch(tdum, (const GAS bf16*)pwq + (size_t)(i / 12 + 512 * (~xcd & 1)) * 3072 + ((i % 12) >> 2) * 1024 + (xcd >> 1) * 256 + (i & 3) * 64);
            }
            if constexpr (s == PBS && n == 0) { POISON_BIG(0); }
            if constexpr (s == 0 && n == 1) count_in(2);
            if constexpr (s == 0 && n == 3) qc = __hip_atomic_load(qcnt + 2, __ATOMIC_RELAXED, __HIP_MEMORY_SCOPE_WORKGROUP);
            if constexpr (s == 0 && n == 5) count_wait(2, 8);
            if constexpr (s == 1 && n == 1) {
              asm volatile("" : "+v"(ep)::"memory");
              const unsigned long long e = (((unsigned long long)(unsigned)__builtin_amdgcn_readfirstlane(ep[1]) << 32) | (unsigned)__builtin_amdgcn_readfirstlane(ep[0])) + 1ull + dispatch_id() * 0x9E3779B97F4A7C16ull;
              T = u32x2{(unsigned)e, (unsigned)(e >> 32)};
            }
            if constexpr (s == RWL && n == 4) {
              DEF4(fx);
              if (w < 4) ldrows(fx, rx, (l >> 5) * 2048 + (l & 31) * 16, (p * 256 + h * 8 + 2 * w) * 2048);
            }
            if constexpr (s == RWS && n == RWG) {
              if (w < 4) {
                if constexpr (RWL >= 0) wait_vm<SINCE(RWL < 0 ? 0 : RWL)>();
                else { fx[0] = rows0[0]; fx[1] = rows0[1]; fx[2] = rows0[2]; fx[3] = rows0[3]; }
                PINV4(fx);
                f32x2 sv[16];
#pragma unroll
                for (int i = 0; i < 4; ++i) unpack8v(fx[i], sv + 4 * i);
                f32x2 t0 = sv[0] + sv[1], t1 = sv[2] + sv[3];
#pragma unroll
                for (int j = 4; j < 16; j += 2) { t0 += sv[j]; t1 += sv[j + 1]; }
                const float m = sum32(hadd(t0 + t1)) * (1.0f / 1024.0f);
                f32x2 m2 = {m, m}, q0 = {0.f, 0.f}, q1 = {0.f, 0.f};
                asm volatile("" : "+v"(m2));
#pragma unroll
                for (int j = 0; j < 16; j += 2) {
                  const f32x2 d0 = sv[j] - m2, d1 = sv[j + 1] - m2;
                  q0 = __builtin_elementwise_fma(d0, d0, q0); q1 = __builtin_elementwise_fma(d1, d1, q1);
                }
                const float rd = ln_rd(sum32(hadd(q0 + q1)) * (1.0f / 1024.0f));
                if ((l & 30) == 0) st4(__builtin_bit_cast(unsigned, (l & 1) ? -rbf1(m) : rd), rs, (l >> 5) * 4 + (l & 1) * 8192, OFF_AB + (p * 256 + h * 8 + 2 * w) * 4);
              }
            }
            if constexpr (s == POI && n == 4) {
              if (w == 3) {
                unsigned pz1 = POISON;
                asm volatile("" : "+v"(pz1));
                const unsigned prb = 2 * p + (h >> 4), pct = h & 15, m = l - 32;
                static_assert(OFF_CB == OFF_CA + 131072, "poison addressing");
                if (l < 60) st16(u32x4{pz1, pz1, pz1, pz1}, rs, l < 32 ? OFF_CA + (l & 8) * 4096 + (l & 16) * 8192 + prb * 2048 + pct * 128 + (l & 7) * 16
                                                             : l < 56 ? OFF_FX + ((h * 6 + (m >> 2)) * 8 + p) * 64 + (l & 3) * 16
                                                                      : OFF_AB + 16384 + (p * 32 + h) * 32 + (l & 1) * 16 + (l & 2) * 4096, 0);
                if (l < 4) st4(pz1, rs, l < 2 ? OFF_XNEW + bid * 8 + l * 4 : OFF_KEY + ((l & 1) * 16 + pct) * cmax(16 * SK, 128) + prb * SK, 0);
              }
            }
            if constexpr (s == POI && n == 2) {
              if (w == 2 && l < 6) {
                unsigned f0 = F_KV + KVF(0, p * 32 + h), f1 = F_O + OFB(0, p) + OFL(h), f2 = F_X1 + ((rb >> 1) * RX * 32 + (l & 3) * 32 + (rb & 1) * 16 + ct) * SX;
                asm volatile("" : "+v"(f0), "+v"(f1), "+v"(f2));
                const unsigned fo = l == 5 ? f0 : l == 4 ? f1 : f2;
                touchb(tdum, rs, fo);
              }
            }
            static_assert(vm_x(EIN + XL) >= vm_at(0, EIN - 3, 5) && IMG >= 1 && CSD >= 1, "first flag");
            if constexpr (s == EIN && n == 1) { if (w < 4) count_in(1); }
            if constexpr (s == EIN + 2 && n == 0) qc = __hip_atomic_load(qcnt + 1, __ATOMIC_RELAXED, __HIP_MEMORY_SCOPE_WORKGROUP);
            if constexpr (s == EIN + 2 && n == 2) {
              if (w == 0) { count_wait(1, 4); if (l < RX) st8(T, rs, F_XN + (p * RX + l) * 256 + h * 8, 0); }
            }
            if constexpr (s == ERQ && n == 4) asm volatile("buffer_load_dwordx2 %0, %1, %2, 0 offen sc1" : "=v"(cxn) : "v"(XF_OFF() + F_XN), "s"(rs) : "memory");
            if constexpr (s == CSL && n == 2) {
              wait_vm<SINCE(ERQ)>();
              asm volatile("" : "+v"(cxn)::"memory");
              if (!__all(cxn[0] == T[0] && cxn[1] == T[1])) poll_flags(rs, XF_OFF(), F_XN, T);
            }
          } else {
#define X1_OFF() (F_X1 + ((p * RX + (xcd >> 1) % RX) * 32 + (l & 31)) * SX)
            if constexpr ((s == 0 || s == 4) && n == 2) asm volatile("buffer_load_dwordx2 %0, %1, %2, 0 offen sc1" : "=v"(cxn) : "v"(X1_OFF()), "s"(rs) : "memory");
            if constexpr ((s == 3 || s == 7) && n == 0) {
              wait_vm<vm_at(1, s, 1) - vm_at(1, s - 3, 3)>();
              asm volatile("" : "+v"(cxn)::"memory");
              xok |= __builtin_amdgcn_ballot_w64(cxn[0] == T[0] && cxn[1] == T[1]);
            }
            if constexpr (a_req(s, n) >= 0 && (a_req(s, n) & 7) == 0) x1_need(0x30003ull << (((rot + a_req(s, n) / 8) & 7) * 2));
          }
          if constexpr (IT && a_req(s, n) >= 0) A_LOAD1(a_req(s, n) / 2, a_req(s, n) & 1);
          if constexpr (IT == 0 && s == CSD && n == CSG) {
            if (w < 3) {
              unsigned a0 = FRAG_T() + SLOT(s >> 2) * STAGE + w * 8192, a1 = a0 ^ 32, one2 = 0x3F803F80u;
              asm volatile("" : "+v"(a0), "+v"(a1), "+v"(one2));
              const u32x4 o4 = {one2, one2, one2, one2};
              f32x4 y0 = {0.f, 0.f, 0.f, 0.f}, y1 = y0;
#pragma unroll
              for (int i = 0; i < 4; ++i) { y0 = mfma16(frag_t2(a0 + i * 2048), o4, y0); y1 = mfma16(frag_t2(a1 + i * 2048), o4, y1); }
              if ((l & 14) == 0) st16(__builtin_bit_cast(u32x4, (l & 1) ? y1 : y0), rs, g * 16 + (l & 1) * 512, OFF_CS + ((h * 6 + 2 * w) * 8 + p) * 64);
            }
          }
          if constexpr (!IT && s == CSL && n == 4) { asm volatile("" : "=v"(fx[0])); if (w < 3) ld16h<0>(fx[0], rs, l * 16, OFF_CS + h * 3072 + w * 1024); }
          if constexpr (!IT && s == CSS && n == 2) {
            if (w < 3) {
              wait_vm<SINCE(CSL)>();
              asm volatile("" : "+v"(fx[0])::"memory");
              tile_sum(fx[0], L_CSL);
            }
          }
          if constexpr (s == abl(IT) && n == 4) ld4h<0>(aq[0], rs, (l & 31) * 4 + (l >> 5) * 8192, AB_OFF());
          if constexpr (s == ABS && n == 0) {
            wait_vm<SINCE(abl(IT))>();
            asm volatile("" : "+v"(aq[0])::"memory");
            if constexpr (IT) {
              for (unsigned spins = 0; !__all(aq[0] != POISON) && !spin_fail(spins, rs, T);) {
                ld4h<0>(aq[0], rs, (l & 31) * 4 + (l >> 5) * 8192, AB_OFF());
                asm volatile("s_waitcnt vmcnt(0)" : "+v"(aq[0])::"memory");
              }
            }
            *(LDS_AS unsigned*)(smem + L_K + (8 + rt) * 2048 + l * 4) = aq[0];
          }
          if constexpr (s == ABS && n == 2) {
#pragma unroll
            for (int a = 0; a < 2; ++a) {
              const LDS_AS char* ab = (const LDS_AS char*)(smem + L_K + (8 + rt) * 2048 + a * 64);
              aq[a] = *(const LDS_AS unsigned*)(ab + (l & 15) * 4); bq[a] = *(const LDS_AS unsigned*)(ab + 128 + (l & 15) * 4);
              av[a] = *(const LDS_AS u32x4*)(ab + g * 16); bv[a] = *(const LDS_AS u32x4*)(ab + 128 + g * 16);
            }
          }
          if constexpr (s == ABS && n == 5) {
#pragma unroll
            for (int a = 0; a < 2; ++a) { b2[a] = f32x2{asf(bq[a]), asf(bq[a])}; asm volatile("" : "+v"(b2[a])); }
          }
          if constexpr (s == ABS + 1) {
            constexpr int k = (n + 4) % 6;
            if constexpr (k < 4) cs4[k & 1] = *(const LDS_AS f32x4*)(smem + L_CSL + k * 64 + g * 16);
            else cs4[k & 1][0] = *(const LDS_AS float*)(smem + L_CSL + k * 64 + (l & 15) * 4);
          }
          if constexpr ((s == ABS + 1 && n > 0) || (s == ABS + 2 && n == 0)) {
            constexpr int k = (n + 3) % 6;
            if constexpr (k < 4) {
#pragma unroll
              for (int a = 0; a < 2; ++a) acc[a][k] = __builtin_elementwise_fma(cs4[k & 1], f32x4{b2[a][0], b2[a][1], b2[a][0], b2[a][1]}, acc[a][k]);
            } else {
              f32x2 c2 = {cs4[k & 1][0], cs4[k & 1][0]};
              asm volatile("" : "+v"(c2));
#pragma unroll
              for (int a = 0; a < 2; ++a) acc[a][k] = __builtin_elementwise_fma(f32x4{c2[0], c2[1], c2[0], c2[1]}, __builtin_bit_cast(f32x4, bv[a]), acc[a][k]);
            }
          }
          if constexpr (IT == 0) {
            if constexpr (s == KP0 - 1 && n < 3) {
              const int c = 3 * (w & 1) + n;
              ka[n] = (FRAG_T() ^ ((c & 1) << 5)) + (SLOT(5) * STAGE + (c >> 1) * 8192 + (w >> 1) * 2048);
              asm volatile("" : "+v"(ka[n]));
            }
            if constexpr (s >= KP0 && s < KP0 + 3 && (n & 1)) keep[3 * (s - KP0) + (n >> 1)] = frag_t2(ka[n >> 1] + (s - KP0) * STAGE);
            if constexpr (s >= IMG && s < IMG + 3 && n == 4) st16(keep[s - IMG], rs, l * 16 + (s - IMG) * 1024, OFF_WI + (h * 8 + ((rot + 5) & 7)) * STAGE + w * 3072);
            if constexpr (s >= TS0 && s < TS0 + TSN && n == 4) {
              if (tstep == s && w < (ffn_on ? TW : TWR)) touch(tdum, tsrc);
            }
            if constexpr (s == TST && n == 4) { if (w >= (ffn_on ? TW : TWR)) touch(tdum, tsrc); }
          } else {
            if constexpr (s == 28 && n == 0) { POISON_BIG(1); }
            if constexpr (s == FSL && n == 4) { ld4h<0>(kv, rs, (l & 15) * SK, OFF_KEY + (bid & 15) * cmax(16 * SK, 128)); row_load(rs, g, ffn_xoff(kg0, 0, w), fx); }
            if constexpr (s == FS0 && n == 0) {
              wait_vm<SINCE(FSL)>();
              asm volatile("" : "+v"(kv)::"memory");
              PINV4(fx);
              if (__builtin_expect(!__all(kv != POISON), 0)) {
                LDS_AS unsigned* kw = (LDS_AS unsigned*)(smem + L_SKEY + 40);
                if (w == 0) {
                  kv = key_wait(rs, T, l, 0, bid & 15, kv);
                } else {
                  while ((kv = __hip_atomic_load(kw, __ATOMIC_RELAXED, __HIP_MEMORY_SCOPE_WORKGROUP)) == POISON) __builtin_amdgcn_s_sleep(2);
                }
              }
              fkey = __builtin_amdgcn_readfirstlane(umax16(kv));
              if (w == 0) *(LDS_AS unsigned*)(smem + L_SKEY + 40) = min(fkey, POISON - 1);
              sel0 = key_row(fkey);
              aff = (int)(sel0 >> 8) == p;
              asm volatile("" : "=v"(sa), "=v"(sb));
              if (ffn_on) {
                if (__builtin_expect(((kg0 ^ fkey) & 0x780u) != 0, 0)) ld4x16(fx, rs, g * 16, ffn_xoff(fkey, 0, w));
                ffn_gu(gv, uv, fx, sa, sb);
              }
            }
            if constexpr (s == FS0 && n == 2) {
              if (ffn_on) {
                ffn_gu_check(rs, T, ffn_xoff(fkey, 0, w), l, gv, uv, fx, sa, sb);
                ffn_gu_store(smem, l, w, sa, sb);
                count_in(1);
              }
            }
            if constexpr (s == FS0D && n == FDG) {
              if (ffn_on && w < 4) {
                s16x4 da[8], db[8];
                ffn_d_read(smem, l, 2 * w, da);
                while (__hip_atomic_load(qcnt + 1, __ATOMIC_RELAXED, __HIP_MEMORY_SCOPE_WORKGROUP) < 8u) {}
                asm volatile("" ::: "memory");
                const f32x4 t = ffn_t_read(smem, l);
                ffn_d_read(smem, l, 2 * w + 1, db);
                u32x2 ha = ffn_h(t, l), hb = {dppu<0x118>(ha[0]), dppu<0x118>(ha[1])};
                f32x4 ya = {0.f, 0.f, 0.f, 0.f}, yb = ya;
#pragma unroll
                for (int i = 0; i < 8; ++i) {
                  ya = mfma16k16(da[i], __builtin_bit_cast(s16x4, ha), ya); yb = mfma16k16(db[i], __builtin_bit_cast(s16x4, hb), yb);
                  ha = u32x2{dppu<0x111>(ha[0]), dppu<0x111>(ha[1])}; hb = u32x2{dppu<0x111>(hb[0]), dppu<0x111>(hb[1])};
                }
                ffn_d_store(rs, l, 8 * w, 0, slice, ya + yb);
              }
            }
#define RED_LOAD(ld, l8)                                                                                                    \
  {                                                                                                                         \
    const unsigned G = bi, po = OFF_PART + G * 32768;                                                        \
    ld(gv[0], rs, tid * 16, po); ld(gv[1], rs, tid * 16, po + 4096); ld(gv[2], rs, tid * 16, po + 8192); ld(gv[3], rs, tid * 16, po + 12288); \
    ld(uv[0], rs, tid * 16, po + 16384); ld(uv[1], rs, tid * 16, po + 20480); ld(uv[2], rs, tid * 16, po + 24576);        \
    l8(cxn, rs, (l & 3) * 16 + (l & 4) * 2, OFF_CA + (sel0 >> 7) * 2048 + G * 64);                                         \
    l8(cx1, rs, (l & 3) * 16 + (l & 4) * 2, OFF_CB + (sel0 >> 7) * 2048 + G * 64);                                          \
  }
            if constexpr (s == FS1 && n == 4) {
              asm volatile("" : "=v"(cxn), "=v"(cx1));
              if (!ffn_on && w < 4) RED_LOAD(ld16u, ld8h);
            }
            if constexpr (s == FS1S && n == 0) {
              if (!ffn_on && w < 4) {
                wait_vm<SINCE(FS1)>();
                PINV4(gv); PINV4(uv);
                asm volatile("" : "+v"(cxn), "+v"(cx1)::"memory");
                for (unsigned spins = 0; !__all(max(max(max(umax4(gv[0]), umax4(gv[1])), max(umax4(gv[2]), umax4(gv[3]))),
                                                    max(max(umax4(uv[0]), umax4(uv[1])), max(max(umax4(uv[2]), max(cxn[0], cxn[1])), max(cx1[0], cx1[1])))) != POISON) && !spin_fail(spins, rs, T);) {
                  RED_LOAD(ld16u, ld8h);
                  DRAIN();
                  PINV4(gv); PINV4(uv);
                  asm volatile("" : "+v"(cxn), "+v"(cx1)::"memory");
                }
#define F4(x) __builtin_bit_cast(f32x4, x)
                ((LDS_AS f32x4*)(smem + L_SFIN))[tid] = ((F4(gv[0]) + F4(gv[1])) + (F4(gv[2]) + F4(gv[3]))) + ((F4(uv[0]) + F4(uv[1])) + F4(uv[2]));
#undef F4
                count_in(2);
                if (w == 0) {
                  while (__hip_atomic_load(qcnt + 2, __ATOMIC_RELAXED, __HIP_MEMORY_SCOPE_WORKGROUP) < 4u) {}
                  asm volatile("" ::: "memory");
                  const u32x2 ov = red_final<4>(smem, l, fkey, cxn, cx1);
                  if (l < 8) st8(unpoison2(ov), rs, OFF_XNEW + (bi * 8 + (l & 3) * 2 + (l >> 2)) * 8, 0);
                }
              }
            }
            if constexpr (s == FXA && n == 4) { DEF4(fx); if (w < 3) row_load(rs, g, XP_OFF(), fx); }
            if constexpr (s == FXB && n == 0) {
              if (w < 3) {
                const unsigned fa = bo[0][0] + SLOT(FXT) * STAGE + w * 2048;
                u32x4 f[8];
#pragma unroll
                for (int i = 0; i < 8; ++i) f[i] = *(const LDS_AS u32x4*)(size_t)(fa + (i >> 1) * 6144 + (i & 1) * 1024);
                wait_vm<SINCE(FXA)>();
                PINV4(fx);
                ffn_xwait<XSL>(rs, T, XP_OFF(), l, fx);
                f32x4 y0 = mfma16(f[0], fx[0], f32x4{0.f, 0.f, 0.f, 0.f}), y1 = mfma16(f[1], fx[0], f32x4{0.f, 0.f, 0.f, 0.f});
#pragma unroll
                for (int i = 1; i < 4; ++i) { y0 = mfma16(f[2 * i], fx[i], y0); y1 = mfma16(f[2 * i + 1], fx[i], y1); }
                if ((l & 14) == 0) st16(unpoison4(__builtin_bit_cast(u32x4, (l & 1) ? y1 : y0)), rs, g * 16 + (l & 1) * 512, OFF_FX + ((h * 6 + 2 * w) * 8 + p) * 64);
              }
            }
            if constexpr (s == FXL && n == 4) {
              asm volatile("" : "=v"(fx[0]), "=v"(fx[1]));
              if (aff && w < 4) { ld16h<0>(fx[0], rs, l * 16, FP_OFF(0)); ld16h<0>(fx[1], rs, l * 16, FP_OFF(1)); }
            }
            if constexpr (s == FXS && n == 2) {
              if (aff && w < 4) {
                wait_vm<0>();
                asm volatile("" : "+v"(fx[0]), "+v"(fx[1])::"memory");
                if (!FX_OK(2)) {
                  unsigned spins = 0;
                  do {
                    if (spin_fail(spins, rs, T)) break;
                    fx[0] = ld16(rs, l * 16, FP_OFF(0)); fx[1] = ld16(rs, l * 16, FP_OFF(1));
                  } while (!FX_OK(2));
                }
                if (w == 3) {
                  f32x2 nx[8];
                  unpack8v(fx[0], nx); unpack8v(fx[1], nx + 4);
                  const f32x2 sm = ((nx[0] + nx[1]) + (nx[2] + nx[3])) + ((nx[4] + nx[5]) + (nx[6] + nx[7]));
                  const float mean = sum64(hadd(sm)) * (1.0f / 1024.0f);
                  const f32x2 m2 = {mean, mean};
                  f32x2 q = {0.f, 0.f};
#pragma unroll
                  for (int j = 0; j < 8; ++j) { const f32x2 d = nx[j] - m2; q = __builtin_elementwise_fma(d, d, q); }
                  const float rd = ln_rd(sum64(hadd(q)) * (1.0f / 1024.0f));
                  if (l == 0) *(LDS_AS f32x2*)(smem + L_FXL + 384) = f32x2{rd, -rbf1(mean)};
                } else {
                  tile_sum(fx[0], L_FXL);
                }
                count_in(3);
              }
            }
          }
        };
        static_for<32>([&](auto sc_) __attribute__((always_inline)) {
          constexpr int s = decltype(sc_)::value;
          if constexpr (s > 0) PIN_ACC();
          if constexpr (IT == 1) wait_vm<vm_at(IT, s, 0) - vm_a(IT, s)>();
          pin_w<s % WA>(af[s % WA][0], af[s % WA][1]);
          SB();
          static_for<6>([&](auto nc_) __attribute__((always_inline)) {
            constexpr int n = decltype(nc_)::value;
            if constexpr (s == 0) { acc[0][n] = f32x4{0.f, 0.f, 0.f, 0.f}; acc[1][n] = acc[0][n]; }
            constexpr bool on = s < QL || n >= 2;
            if constexpr (on) acc[0][n] = n < 4 ? mfma16(bfr[n].v, af[s % WA][0], acc[0][n]) : mfma16(af[s % WA][0], bfr[n].v, acc[0][n]);
            SB();
            if constexpr (n == 0) { QKV_RDB(s, 5); }
            else if constexpr (s + 1 < 32 && (s + 1 < QL || n > 2)) QKV_RDB(s + 1, n - 1);
            SB();
            if constexpr (on) acc[1][n] = n < 4 ? mfma16(bfr[n].v, af[s % WA][1], acc[1][n]) : mfma16(af[s % WA][1], bfr[n].v, acc[1][n]);
            SB();
            if constexpr (s + 1 < 32 && (s + 1 < QL || n >= 2)) QKV_RDA(s + 1, n);
            SB();
            gap(sc_, nc_);
            SB();
          });
        });
        PIN_ACC();
        frag_t qw[32 - QL][2];
        static_for<32 - QL>([&](auto c_) __attribute__((always_inline)) {
          constexpr int s = QL + decltype(c_)::value;
          QKV_RDA(s, 0); QKV_RDB(s, 0); QKV_RDA(s, 1); QKV_RDB(s, 1);
          qw[s - QL][0] = bfr[0]; qw[s - QL][1] = bfr[1];
        });
        SB();
        const bool own = aff && (int)((sel0 >> 5) & 7) == rt;
        const bool m0 = !(sel0 & 16), mq = (l & 15) == (int)(sel0 & 15), mv = g == (int)((sel0 & 15) >> 2);
        auto patch_qk = [&](int n) __attribute__((always_inline)) {
          f32x4 t = *(const LDS_AS f32x4*)(smem + L_FXL + n * 64 + g * 16);
          const f32x4 c = *(const LDS_AS f32x4*)(smem + L_CSL + n * 64 + g * 16);
          const float nb = *(const LDS_AS float*)(smem + L_FXL + 388);
#pragma unroll
          for (int e = 0; e < 4; ++e) { t[e] = __builtin_fmaf(c[e], nb, t[e]); asm volatile("" : "+v"(t[e])); }
          acc[0][n] = (mq && m0) ? t : acc[0][n];
          acc[1][n] = (mq && !m0) ? t : acc[1][n];
        };
        if (own) {
          while (__hip_atomic_load(qcnt + 3, __ATOMIC_RELAXED, __HIP_MEMORY_SCOPE_WORKGROUP) < 4u) {}
          asm volatile("" ::: "memory");
          patch_qk(2); patch_qk(3);
          const f32x2 nab = *(const LDS_AS f32x2*)(smem + L_FXL + 384);
          const LDS_AS float* fl_ = (const LDS_AS float*)(smem + L_FXL + 256) + (l & 15);
          const LDS_AS float* cl_ = (const LDS_AS float*)(smem + L_CSL + 256) + (l & 15);
          const float t4 = __builtin_fmaf(cl_[0], nab[1], fl_[0]), t5 = __builtin_fmaf(cl_[16], nab[1], fl_[16]);
          const unsigned na = __builtin_bit_cast(unsigned, (float)nab[0]);
#pragma unroll
          for (int a = 0; a < 2; ++a) {
            const bool ma = a ? !m0 : m0;
            aq[a] = (mq && ma) ? na : aq[a];
#pragma unroll
            for (int e = 0; e < 4; ++e) {
              const bool me = mv && ma && e == (int)(sel0 & 3);
              acc[a][4][e] = me ? t4 : acc[a][4][e]; acc[a][5][e] = me ? t5 : acc[a][5][e];
              av[a][e] = me ? na : av[a][e];
            }
          }
        }
        constexpr float C2 = 1.4426950408889634f * 0.17677669529663687f;
#pragma unroll
        for (int a = 0; a < 2; ++a) {
          f32x2 a2 = {asf(aq[a]), asf(aq[a])};
          qs2[a] = f32x2{asf(aq[a]) * C2, asf(aq[a]) * C2};
          asm volatile("" : "+v"(a2), "+v"(qs2[a]));
          acc[a][2] *= f32x4{a2[0], a2[1], a2[0], a2[1]}; acc[a][3] *= f32x4{a2[0], a2[1], a2[0], a2[1]};
          acc[a][4] *= __builtin_bit_cast(f32x4, av[a]); acc[a][5] *= __builtin_bit_cast(f32x4, av[a]);
        }
        k0 = pack8(acc[0][2], acc[0][3]); k1 = pack8(acc[1][2], acc[1][3]); v0 = pack8(acc[0][4], acc[1][4]); v1 = pack8(acc[0][5], acc[1][5]);
        const unsigned kvo = OFF_KV + IT * KV_STEP + (p * 32 + h) * 32768 + rt * 4096;
        if (p < 7) { st16(k0, rs, l * 16, kvo); st16(k1, rs, l * 16 + 1024, kvo); st16(v0, rs, l * 16 + 2048, kvo); st16(v1, rs, l * 16 + 3072, kvo); }
        char* ownp = smem + (8 + rt) * 2048 + l * 16;
        *(u32x4*)(ownp + L_K) = k0;
        if (rt < 7) *(u32x4*)(ownp + L_K + 1024) = k1;
        *(u32x4*)(ownp + L_V) = v0; *(u32x4*)(ownp + L_V + 1024) = v1;
        SB();
        static_for<32 - QL>([&](auto c_) __attribute__((always_inline)) {
          constexpr int s = QL + decltype(c_)::value;
#pragma unroll
          for (int n = 0; n < 2; ++n) { acc[0][n] = mfma16(qw[s - QL][n].v, af[s % WA][0], acc[0][n]); acc[1][n] = mfma16(qw[s - QL][n].v, af[s % WA][1], acc[1][n]); }
        });
        if (own) { patch_qk(0); patch_qk(1); }
      };
      qkv_loop(it_c);
    }
#undef A_LOAD1
#undef QKV_RDA
#undef QKV_RDB
#undef QKV_BO
    {
      IDS();
      const GAS bf16 *pwo, *pwg, *pwu;
      asm volatile("s_load_dwordx2 %0, %3, 24\n\ts_load_dwordx2 %1, %3, 40\n\ts_load_dwordx2 %2, %3, 48" : "=&s"(pwo), "=&s"(pwg), "=&s"(pwu) : "s"(ka));
      const int ql = l & 15;
      const f32x4 z4 = {0.f, 0.f, 0.f, 0.f}, ninf = {-INFINITY, -INFINITY, -INFINITY, -INFINITY};
      LDS_AS unsigned* rdy = (LDS_AS unsigned*)(smem + L_RDY);
      const unsigned rtag = T[0] + it;
      u32x4 qf[2];
      f32x4 cn[2], oa[2][2], sum[2], S[2][2];
      unsigned one2 = 0x3F803F80u;
      asm volatile("" : "+v"(one2));
      const u32x4 ones = {one2, one2, one2, one2};
      auto ex8 = [&](f32x4 a, f32x4 b) __attribute__((always_inline)) {
        return u32x4{pack2(__builtin_amdgcn_exp2f(a[0]), __builtin_amdgcn_exp2f(a[1])), pack2(__builtin_amdgcn_exp2f(a[2]), __builtin_amdgcn_exp2f(a[3])),
                     pack2(__builtin_amdgcn_exp2f(b[0]), __builtin_amdgcn_exp2f(b[1])), pack2(__builtin_amdgcn_exp2f(b[2]), __builtin_amdgcn_exp2f(b[3]))};
      };
      {
#pragma unroll
        for (int a = 0; a < 2; ++a) {
          const f32x4 c4 = {qs2[a][0], qs2[a][1], qs2[a][0], qs2[a][1]};
          qf[a] = pack8(acc[a][0] * c4, acc[a][1] * c4);
        }
        f32x4 mk;
#pragma unroll
        for (int e = 0; e < 4; ++e) mk[e] = (4 * g + e <= ql) ? 0.f : -INFINITY;
        const f32x4 d0 = mfma16(k0, qf[0], mk), d1 = mfma16(k1, qf[1], mk), d2 = mfma16(k0, qf[1], z4);
        const float m0 = -max_x32(max_x16(fmaxf(fmaxf(d0[0], d0[1]), fmaxf(d0[2], d0[3]))));
        const float m1 = -max_x32(max_x16(fmaxf(fmaxf(d1[0], d1[1]), fmaxf(d1[2], d1[3]))));
        cn[0] = f32x4{m0, m0, m0, m0}; cn[1] = f32x4{m1, m1, m1, m1};
        asm volatile("" : "+v"(cn[0]), "+v"(cn[1]));
        const f32x4 e0 = d0 + cn[0];
        const u32x4 p0 = {pack2(__builtin_amdgcn_exp2f(e0[0]), __builtin_amdgcn_exp2f(e0[1])), pack2(__builtin_amdgcn_exp2f(e0[2]), __builtin_amdgcn_exp2f(e0[3])), 0u, 0u};
        const u32x4 p1 = ex8(d2 + cn[1], d1 + cn[1]);
        oa[0][0] = mfma16(v0, p0, z4); oa[0][1] = mfma16(v1, p0, z4); sum[0] = mfma16(ones, p0, z4);
        oa[1][0] = mfma16(v0, p1, z4); oa[1][1] = mfma16(v1, p1, z4); sum[1] = mfma16(ones, p1, z4);
      }
      const char* kb_ = smem + L_K + l * 16;
      const char* vb_ = smem + L_V + l * 16;
      const unsigned wov = (l >> 3) * 2048 + (((((l & 7) >> 1) ^ ((l >> 4) & 3)) * 2 + (l & 1)) << 4), fnv = (l >> 1) * (F_ * 2) + (l & 1) * 16;
      const unsigned pp = __builtin_amdgcn_readfirstlane(p > 0 ? 32 : 0), pvo = OFF_KV + it * KV_STEP + (p * 32 + h - pp) * 32768;
      const unsigned pfo = __builtin_amdgcn_readfirstlane(F_KV + KVF(it, p * 32 + h - pp));
      asm volatile("" : "+v"(oa[0][0]), "+v"(oa[0][1]), "+v"(oa[1][0]), "+v"(oa[1][1]), "+v"(sum[0]), "+v"(sum[1]));
      DRAIN();
      asm volatile("" : "+v"(tdum));
      if (l == 0) st8(T, rs, rt * SV, F_KV + KVF(it, p * 32 + h));
      BAR();
      asm volatile("" : "+s"(pwo), "+s"(pwg), "+s"(pwu));
      if (!LOP || w >= 4) { WO_PIECE(15); WO_PIECE(14); }
#define LDS16(a_) (*(const LDS_AS u32x4*)(size_t)(a_))
      u32x4 kf[2], vf[2];
      int nw = 0;
      u32x4 p0, p1;
      float e0, e1;
#define EE(v, i) { e0 = __builtin_amdgcn_exp2f(v[i]); e1 = __builtin_amdgcn_exp2f(v[i + 1]); pin2s(e0, e1); SB(); }
#define CV(P, i) { unsigned t_ = pack2(e0, e1); pin1(t_); P[i] = t_; SB(); }
      auto body = [&](unsigned ad, auto dk_) __attribute__((always_inline)) {
        constexpr int dk = decltype(dk_)::value;
        const f32x4 a0 = S[1][0], a1 = S[1][1];
        sum[0] = mfma16(ones, p0, sum[0]); SB();
        EE(a0, 0);
        lds_wait<2>(kf[0], kf[1]);
        S[0][0] = mfma16(kf[0], qf[0], cn[0]); SB();
        CV(p1, 0); EE(a0, 2);
        S[0][1] = mfma16(kf[1], qf[0], cn[0]); SB();
        CV(p1, 1); EE(a1, 0);
        lds_wait<0>(vf[0], vf[1]);
        oa[0][0] = mfma16(vf[0], p0, oa[0][0]); SB();
        CV(p1, 2); EE(a1, 2);
        oa[0][1] = mfma16(vf[1], p0, oa[0][1]); SB();
        CV(p1, 3);
        S[1][0] = mfma16(kf[0], qf[1], cn[1]); SB();
        EE(S[0][0], 0);
        S[1][1] = mfma16(kf[1], qf[1], cn[1]); SB();
        lds2<2048 + dk>(kf[0], kf[1], ad); SB();
        CV(p0, 0); EE(S[0][0], 2);
        oa[1][0] = mfma16(vf[0], p1, oa[1][0]); SB();
        CV(p0, 1); EE(S[0][1], 0);
        oa[1][1] = mfma16(vf[1], p1, oa[1][1]); SB();
        lds2<2048 + L_V>(vf[0], vf[1], ad); SB();
        CV(p0, 2); EE(S[0][1], 2);
        sum[1] = mfma16(ones, p1, sum[1]); SB();
        CV(p0, 3);
      };
      auto last = [&]() __attribute__((always_inline)) {
        sum[0] = mfma16(ones, p0, sum[0]); SB();
        EE(S[1][0], 0);
        lds_wait<0>(vf[0], vf[1]);
        pin2(kf[0], kf[1]);
        oa[0][0] = mfma16(vf[0], p0, oa[0][0]); SB();
        CV(p1, 0); EE(S[1][0], 2);
        oa[0][1] = mfma16(vf[1], p0, oa[0][1]); SB();
        CV(p1, 1); EE(S[1][1], 0);
        const float f0 = __builtin_amdgcn_exp2f(S[1][1][2]), f1 = __builtin_amdgcn_exp2f(S[1][1][3]);
        p1[2] = pack2(e0, e1); p1[3] = pack2(f0, f1);
        oa[1][0] = mfma16(vf[0], p1, oa[1][0]); oa[1][1] = mfma16(vf[1], p1, oa[1][1]); sum[1] = mfma16(ones, p1, sum[1]);
      };
      auto piece = [&]() __attribute__((always_inline)) {
        if constexpr (it == 1) { WO_PIECE(13 - nw); }
        else if (ffn_on) { FFN_DMA(nw); }
        ++nw;
      };
      if (rt > 0) {
        unsigned ad = lbase + l * 16 + (6 + rt) * 2048;
        lds2<2048>(kf[0], kf[1], ad); lds2<2048 + L_V>(vf[0], vf[1], ad);
        lds_wait<2>(kf[0], kf[1]);
        S[0][0] = mfma16(kf[0], qf[0], cn[0]); S[0][1] = mfma16(kf[1], qf[0], cn[0]); S[1][0] = mfma16(kf[0], qf[1], cn[1]); S[1][1] = mfma16(kf[1], qf[1], cn[1]);
        SB();
        lds_wait<0>(vf[0], vf[1]);
        lds2<0>(kf[0], kf[1], ad);
        p0 = ex8(S[0][0], S[0][1]);
        lds_wait<0>(kf[0], kf[1]);
#pragma unroll 1
        for (int i = rt - 1; i > 0; --i) {
          ad -= 2048;
          body(ad, std::integral_constant<int, -2048>{});
          piece();
          SB();
        }
        piece();
        last();
      }
      {
        constexpr int FW = it ? FW1 : FW0, FN = 8 / FW;
        if (w < FW) {
          const int g0 = FN * rt;
          if (KVS && w == 0) __builtin_amdgcn_s_sleep(KVS);
          poll_flags(rs, (l & (FN - 1)) * SV, pfo + g0 * SV, T);
#pragma unroll
          for (int j = 0; j < FN; ++j) {
            const unsigned d = lbase + (g0 + j) * 2048, o = pvo + (g0 + j) * 4096;
            dma16s<0>(rs, l * 16, o, d + L_K); dma16s<1024>(rs, l * 16, o, d + L_K);
            dma16s<0>(rs, l * 16, o + 2048, d + L_V); dma16s<1024>(rs, l * 16, o + 2048, d + L_V);
          }
        }
        if (p == 0) { cn[0] = ninf; cn[1] = ninf; }
        f32x4 mk;
#pragma unroll
        for (int e = 0; e < 4; ++e) mk[e] = (4 * g + e > ql) ? 0.f : -INFINITY;
        const f32x4 cm0 = cn[0] + mk, cm1 = cn[1] + mk;
        if (w < FW) {
          if constexpr (FN > 1) { wait_vm<2 * FN>(); if (l < FN / 2) rdy[FN * rt + l] = rtag; }
          DRAIN();
          if (l < FN) rdy[FN * rt + l] = rtag;
        }
        if (LOP && w < 4) { WO_PIECE(15); WO_PIECE(14); }
        if constexpr (it == 1) { WO_PIECE(6); WO_PIECE(5); WO_PIECE(4); }
        else { WO_PIECE(13); WO_PIECE(12); if (ffn_on) FFN_DMA(7); }

        int have = rt - 1;
        auto need = [&](int gq) __attribute__((always_inline)) {
          if (__builtin_expect(gq > have && gq < 8, 0)) {
            do {
              const unsigned t = __hip_atomic_load(rdy + (l & 7), __ATOMIC_RELAXED, __HIP_MEMORY_SCOPE_WORKGROUP);
              have = rt - 1 + __builtin_ctz(~((unsigned)__builtin_amdgcn_ballot_w64(t == rtag) & 0xffu) >> rt);
            } while (gq > have);
            asm volatile("" ::: "memory");
          }
        };
        unsigned ad = lbase + l * 16 + rt * 2048;
        need(rt);
        lds2<0>(kf[0], kf[1], ad); lds2<L_V>(vf[0], vf[1], ad);
        lds_wait<2>(kf[0], kf[1]);
        S[0][0] = mfma16(kf[0], qf[0], cm0); S[0][1] = mfma16(kf[1], qf[0], cn[0]); S[1][0] = ninf; S[1][1] = mfma16(kf[1], qf[1], cm1);
        SB();
        lds_wait<0>(vf[0], vf[1]);
        need(rt + 1);
        lds2<2048>(kf[0], kf[1], ad);
        p0 = ex8(S[0][0], S[0][1]);
        lds_wait<0>(kf[0], kf[1]);
#pragma unroll 1
        for (int gq = rt + 1; gq < 8; ++gq) {
          need(gq + 1);
          body(ad, std::integral_constant<int, 2048>{});
          piece();
          SB();
          ad += 2048;
        }
        last();
      }
      {
        float big = 0.f;
#pragma unroll
        for (int a = 0; a < 2; ++a)
#pragma unroll
          for (int d = 0; d < 2; ++d)
            big = __builtin_elementwise_maximum(__builtin_elementwise_maximum(big, __builtin_elementwise_maximum(fabsf(oa[a][d][0]), fabsf(oa[a][d][1]))),
                                                __builtin_elementwise_maximum(fabsf(oa[a][d][2]), fabsf(oa[a][d][3])));
        bool ok = sum[0][0] >= 0.5f && sum[0][0] < 0x1p64f && sum[1][0] >= 0.5f && sum[1][0] < 0x1p64f && big < INFINITY;
        if (!__all(ok)) {
#pragma unroll
          for (int a = 0; a < 2; ++a) {
            const int qs = 2 * rt + a, qq = 256 + 16 * qs + ql, klo = max(qq - 255, p > 0 ? 0 : 256);
            auto score = [&](int t) __attribute__((always_inline)) {
              f32x4 s = mfma16(t == 31 ? k1 : *(const u32x4*)(kb_ + t * 1024), qf[a], z4);
#pragma unroll
              for (int e = 0; e < 4; ++e) { const int kk = 16 * t + 4 * g + e; s[e] = (kk <= qq && kk >= klo) ? s[e] : -INFINITY; }
              return s;
            };
            float mx = -INFINITY;
#pragma unroll 1
            for (int t = qs; t <= qs + 16; ++t) { const f32x4 s = score(t); mx = fmaxf(fmaxf(mx, fmaxf(s[0], s[1])), fmaxf(s[2], s[3])); }
            mx = max_x32(max_x16(mx));
            oa[a][0] = z4; oa[a][1] = z4; sum[a] = z4;
#pragma unroll 1
            for (int G = qs >> 1; G <= (qs + 16) >> 1; ++G) {
              const f32x4 s0 = score(2 * G), s1 = score(2 * G + 1);
              f32x4 e0, e1;
#pragma unroll
              for (int e = 0; e < 4; ++e) { e0[e] = __builtin_amdgcn_exp2f(s0[e] - mx); e1[e] = __builtin_amdgcn_exp2f(s1[e] - mx); }
              const u32x4 pf = pack8(e0, e1);
              oa[a][0] = mfma16(*(const u32x4*)(vb_ + G * 2048), pf, oa[a][0]);
              oa[a][1] = mfma16(*(const u32x4*)(vb_ + G * 2048 + 1024), pf, oa[a][1]);
              sum[a] = mfma16(ones, pf, sum[a]);
            }
          }
        }
      }
#pragma unroll
      for (int a = 0; a < 2; ++a) {
        const float inv = __builtin_amdgcn_rcpf(sum[a][0]);
        oa[a][0] *= inv; oa[a][1] *= inv;
      }
      const unsigned oo = OFF_O + it * O_STEP + ((p * 8 + rt) * 64 + h * 2) * 1024;
      st16(pack8(oa[0][0], oa[0][1]), rs, l * 16, oo);
      st16(pack8(oa[1][0], oa[1][1]), rs, l * 16 + 1024, oo);
      DRAIN();
      BAR();
      if (tid == 0) st8(T, rs, OFL(h), F_O + OFB(it, p));
      if constexpr (it == 0) {
        DEF4(gv); DEF4(uv);
        if (ffn_on) {
          const unsigned ro = 65536 + w * 1024 + (4 * g + ((l & 15) >> 2)) * 32 + (l & 3) * 8;
#pragma unroll
          for (int i = 0; i < 4; ++i) {
            frag_t f;
            f.hf.a = trread(smem, ro + i * 16384); f.hf.b = trread(smem, ro + i * 16384 + 512);
            gv[i] = f.v;
            f.hf.a = trread(smem, ro + i * 16384 + 8192); f.hf.b = trread(smem, ro + i * 16384 + 8704);
            uv[i] = f.v;
          }
        }
        PINV4(gv); PINV4(uv);
        {
          ARGP(px, 0);
          const unsigned xo = ((rb * 4 + (w & 3)) * 32 + (w >> 2) * 16) * 2048 + ct * 128, xw = (l >> 3) * 2048 + (((l & 7) ^ g) << 4);
          dma16(RSRC(px, 2048 * 1024 * 2), xw, xo, lbase + w * 4096); dma16(RSRC(px, 2048 * 1024 * 2), xw ^ 64, xo + 16384, lbase + w * 4096 + 1024);
        }
      }
      WO_PIECE(3); WO_PIECE(2); WO_PIECE(1); WO_PIECE(0);
      if constexpr (it == 0) { WO_PIECE(11); WO_PIECE(10); WO_PIECE(9); WO_PIECE(8); WO_PIECE(7); WO_PIECE(6); WO_PIECE(5); WO_PIECE(4); }
    }
    IDS();
    ARGP(pwr, 4); ARGP(pwd, 7); ARGP(pout, 8);
    const int kh = w >> 2, rt2 = rb * 4 + (w & 3);
    const unsigned R = rt2 * 32 + kh * 16 + (l & 15);
    const unsigned to = (rt2 * 64 + ct * 4 + kh) * 1024 + l * 16;
    const unsigned rmo = ct * 64 + ((g & 1) ? 12 + 4 * g : 4 * g);
    f32x2 v[8], wv[8];
    float wsum;
    {
      u32x4 wa[2], wrp[2], res[2];
#pragma unroll
      for (int j = 0; j < 2; ++j) {
        wrp[j] = *(const GAS u32x4*)(pwr + rmo + 32 * j);
        wa[j] = *(const GAS u32x4*)(pwr + l * 8 + j * 512);
      }
      {
        f32x2 t[8];
        unpack8v(wa[0], t); unpack8v(wa[1], t + 4);
        const f32x2 s = ((t[0] + t[1]) + (t[2] + t[3])) + ((t[4] + t[5]) + (t[6] + t[7]));
        wsum = sum64(hadd(s));
        unpack8v(trade16(wrp[0]), wv); unpack8v(trade16(wrp[1]), wv + 4);
        asm volatile("" : "+v"(wsum), "+v"(wv[0]), "+v"(wv[1]), "+v"(wv[2]), "+v"(wv[3]), "+v"(wv[4]), "+v"(wv[5]), "+v"(wv[6]), "+v"(wv[7]));
      }
      unsigned bt[4];
#pragma unroll
      for (int n = 0; n < 4; ++n) {
        bt[n] = lbase + 32768 + 65536 + kh * 4096 + (4 * g + ((l & 15) >> 2)) * 128 + (l & 3) * 8 + ((n ^ (2 * (g & 1) + ((l & 15) >> 3))) << 5);
        asm volatile("" : "+v"(bt[n]));
      }
      if (w == 0) { if (it ? OSL1 : OSL0) __builtin_amdgcn_s_sleep(it ? OSL1 : OSL0); poll_flags<OPS>(rs, OFL(l & 31), F_O + OFB(it, rb >> 1), T); }
      else if (it == 0 && w < 3) poll_flags2(rs, F_XN + (((w - 1) * 4 + g) * RX + bi % RX) * 256 + (l & 15) * 16, T);
      else if (it == 0 && w == 3) poll_flags(rs, ((l >> 3) * 32 + h) * cmax(8 * SV, 128) + (l & 7) * SV, F_KV, T);
      asm volatile("s_barrier" ::: "memory");
      const unsigned abase = OFF_O + it * O_STEP + (rt2 * 64 + 32 * kh) * 1024;
      const unsigned av0 = l * 16 + kh * 1024, av1 = l * 16 + (kh ^ 1) * 1024;
      u32x4 af[WB][2];
#define O_LOAD(s_) { ld16h<(wo_k(it, s_) & 1) * 2048>(af[(s_) % WB][0], rs, av0, abase + (wo_k(it, s_) >> 1) * 4096); ld16h<(wo_k(it, s_) & 1) * 2048>(af[(s_) % WB][1], rs, av1, abase + (wo_k(it, s_) >> 1) * 4096); }
      static_for<WR>([&](auto c_) __attribute__((always_inline)) { O_LOAD(decltype(c_)::value); });
      if constexpr (it == 1) { ld16h<0>(res[0], rs, g * 16, OFF_XNEW + ct * 128); ld16h<64>(res[1], rs, g * 16, OFF_XNEW + ct * 128); }
      SB();
      f32x4 ac[2][4];
#pragma unroll
      for (int a = 0; a < 2; ++a)
#pragma unroll
        for (int n = 0; n < 4; ++n) ac[a][n] = f32x4{0.f, 0.f, 0.f, 0.f};
      u32x4 bfr[2][4];
#define WO_RD(buf, s, n)                                                                             \
  {                                                                                                  \
    const unsigned o_ = bt[n] + (wo_k(it, s) & 7) * 8192;                                             \
    frag_t f_;                                                                                       \
    f_.hf.a = trabs(o_); f_.hf.b = trabs(o_ + 2048);                                                 \
    bfr[buf][n] = f_.v;                                                                              \
  }
#pragma unroll
      for (int n = 0; n < 4; ++n) WO_RD(0, 0, n);
      static_for<16>([&](auto sc_) __attribute__((always_inline)) {
        constexpr int s = decltype(sc_)::value, cur = s & 1;
        pin_ac(ac);
        if constexpr (s == 1) lds_count<0>(lbase + L_QC);
        if constexpr (s == (it == 0 ? 3 : 11)) lds_look(smem + L_QC, 64);
        if constexpr (s < 15 && ((wo_k(it, s) ^ wo_k(it, s + 1)) & 8)) {
#pragma unroll
          for (int n = 0; n < 4; ++n) { bt[n] += (wo_k(it, s) & 8) ? -65536 : 65536; pin1(bt[n]); }
        }
        if constexpr (s < WB - WR) O_LOAD(WR + 2 * s);
        if constexpr (it == 0 && s == 8) lds_count<8>(lbase + L_QC);
        if constexpr (it == 0 && s == WDS) {
          DRAIN();
          if ((w >> 2) == WDW && ffn_on) { lds_look(smem + L_QC + 8, 16); WD_DMA_OF(w); WD_DMA_OF(w ^ 4); }
        }
        if constexpr (it == 1 || s < WDS) wait_vm<wo_at(it, s) - wo_pos(it, s)>();
        pin2(af[s % WB][0], af[s % WB][1]);
        if constexpr (it == 1 && s == 9) pin2(res[0], res[1]);
        SB();
#pragma unroll
        for (int n = 0; n < 4; ++n) {
          ac[0][n] = mfma16(bfr[cur][n], af[s % WB][0], ac[0][n]);
          ac[1][n] = mfma16(bfr[cur][n], af[s % WB][1], ac[1][n]);
          if (s + 1 < 16) WO_RD(cur ^ 1, s + 1, n);
          if (it == 0 && s == 8) {
            const u32x2 t = *(const LDS_AS u32x2*)(smem + w * 4096 + (l & 15) * 128 + (((2 * n + (g >> 1)) ^ ((l & 15) >> 1)) << 4) + (g & 1) * 8);
            res[n >> 1][2 * (n & 1)] = t[0]; res[n >> 1][2 * (n & 1) + 1] = t[1];
          }
          if (s == 9) {
            if (it == 1 && n == 0) { res[0] = R == sel0 ? res[0] : x2p[0]; res[1] = R == sel0 ? res[1] : x2p[1]; }

            v[2 * n] = f32x2{lo16(res[n >> 1][2 * (n & 1)]), hi16(res[n >> 1][2 * (n & 1)])};
            v[2 * n + 1] = f32x2{lo16(res[n >> 1][2 * (n & 1) + 1]), hi16(res[n >> 1][2 * (n & 1) + 1])};
            pin2f(v[2 * n], v[2 * n + 1]);
          }
          SB();
        }
        if constexpr (s < WB - WR) { O_LOAD(WR + 2 * s + 1); }
        else if constexpr (s + WB < 16) { O_LOAD(s + WB); }
        SB();
      });
#undef WO_RD
#undef O_LOAD
#define XCH_AT(v) ((v) == 7 ? 155648 : (v) * 4096)
#pragma unroll
      for (int n = 0; n < 4; ++n) *(f32x4*)(smem + XCH_AT(w) + n * 1024 + l * 16) = ac[1][n];
#pragma unroll
      for (int n = 0; n < 4; ++n) { v[2 * n] += f32x2{ac[0][n][0], ac[0][n][1]}; v[2 * n + 1] += f32x2{ac[0][n][2], ac[0][n][3]}; }
      BAR();
#pragma unroll
      for (int n = 0; n < 4; ++n) {
        const f32x4 o = *(const f32x4*)(smem + XCH_AT(w ^ 4) + n * 1024 + l * 16);
        v[2 * n] += f32x2{o[0], o[1]}; v[2 * n + 1] += f32x2{o[2], o[3]};
      }
    }
    {
      if (it == 0 && X1F) { st16(pack8v(v), rs, to, OFF_XN + (4u << 20)); st16(pack8v(v + 4), rs, to + 2048, OFF_XN + (4u << 20)); }
      SB();
      const f32x2 s = ((v[0] + v[1]) + (v[2] + v[3])) + ((v[4] + v[5]) + (v[6] + v[7]));
      f32x2 dp = v[0] * wv[0], q = v[0] * v[0];
#pragma unroll
      for (int j = 1; j < 8; ++j) { dp = __builtin_elementwise_fma(v[j], wv[j], dp); q = __builtin_elementwise_fma(v[j], v[j], q); }
      float mt = hadd(s), qq = hadd(q), dot = hadd(dp);
      add_g3(mt, qq, dot);
      const unsigned so = OFF_STAT + it * 524288 + rb * 32768, lrow = R & 127;
      if (g == 0) st16(unpoison4(__builtin_bit_cast(u32x4, f32x4{mt, qq, dot, 0.f})), rs, ct * 2048 + lrow * 16, so);
      SB();
      if (it == 0 && !X1F) { st16(pack8v(v), rs, to, OFF_XN + (4u << 20)); st16(pack8v(v + 4), rs, to + 2048, OFF_XN + (4u << 20)); }
      SB();
      if (it == 0) wait_vm<X1F>();
      if constexpr (it == 0) {
        volatile LDS_AS u32x2* in = (volatile LDS_AS u32x2*)(smem + L_XC);
        if (l == 0) in[w] = T;
        const u32x2 t = in[l & 7];
        if (__all(t[0] == T[0] && t[1] == T[1]) && l < RX) st8(T, rs, (l * 32 + (rb & 1) * 16 + ct) * SX, F_X1 + (rb >> 1) * RX * 32 * SX);
      }
      if (it == 1) { if (ffn_on) { WD_DMA_OF(w); } }
      if (it == 0) {
#pragma unroll
        for (int j = 0; j < 9; ++j) *(u32x4*)(smem + L_RING + (j / 3) * STAGE + (w * 3 + j % 3) * 1024 + l * 16) = keep[j];
      }
      __builtin_amdgcn_s_sleep(it == 0 ? SDI0 : SDIR);
      char* const skey = smem + (it == 0 ? L_SKEY : L_XC);
      u32x4 sp[4];
      ld16h<0>(sp[0], rs, g * 8192 + lrow * 16, so); ld16h<2048>(sp[1], rs, g * 8192 + lrow * 16, so);
      ld16h<0>(sp[2], rs, g * 8192 + lrow * 16, so + 4096); ld16h<2048>(sp[3], rs, g * 8192 + lrow * 16, so + 4096);
      u32x2 xf;
      unsigned k_;
      asm volatile("" : "=v"(xf), "=v"(k_));
      if (it == 0 && w == 0) { ld8h(xf, rs, (l & 31) * SX, F_X1 + (p * RX + (xcd >> 1) % RX) * 32 * SX); ld4h<0>(k_, rs, (l & 15) * SK, OFF_KEY + (bid & 15) * cmax(16 * SK, 128)); }
      DRAIN();
      asm volatile("" : "+v"(xf), "+v"(k_)::"memory");
      PINV4(sp);
      const RowFin r = fin_key(rs, T, g * 8192 + lrow * 16, so, skey, l, w, R, wsum, sp);
      if constexpr (it == 0) {
        if (w == 0) {
          const unsigned need = 0x30003u << (((PROT() + 5) & 7) * 2);
          unsigned m = (unsigned)__builtin_amdgcn_ballot_w64(xf[0] == T[0] && xf[1] == T[1]);
          for (unsigned spins = 0; (m & need) != need;) {
            if (spin_fail(spins, rs, T)) { m = ~0u; break; }
            const u32x2 f = ld8(rs, F_X1 + ((p * RX + (xcd >> 1) % RX) * 32 + (l & 31)) * SX, 0);
            k_ = key_load(rs, l, 0, bid & 15);
            m = (unsigned)__builtin_amdgcn_ballot_w64(f[0] == T[0] && f[1] == T[1]);
          }
          k_ = umax16(k_ == POISON ? 0u : k_);
          if (l == 0) *(LDS_AS u32x4*)(skey + 32) = u32x4{m, k_, POISON, 0u};
        }
        if (tid == 192) *(LDS_AS u32x4*)(smem + L_QC) = u32x4{0u, 0u, 0u, 0u};
      }
      BAR();
      if constexpr (it == 0) {
        PRE_LOAD(1, PROT() + 5, 0, 4);
        const u32x2 mk = *(const LDS_AS u32x2*)(skey + 32);
        xm0 = __builtin_amdgcn_readfirstlane(mk[0]); kg0 = __builtin_amdgcn_readfirstlane(mk[1]);
      }
      const unsigned own = fin_cand(rs, skey, it, g, rb, ct, r, v);
      if constexpr (it == 0) kg0 = max(kg0, (unsigned)__builtin_amdgcn_readfirstlane(own));
      fin_x2(r, v);
      if constexpr (it == 0) {
        x2p[0] = pack8v(v); x2p[1] = pack8v(v + 4);
        asm volatile("" : "+v"(x2p[0]), "+v"(x2p[1]));
        if ((w & 3) * 2 + (w >> 2) == ct >> 1) {
          const float f = 1.0f + r.rd, rd2 = ln_rd(r.var * f * f), mb2 = rbf1(__builtin_fmaf(r.mean - r.mb, r.rd, r.mean));
          if (g < 2 && ((l >> 3) & 1) == (ct & 1))
            st4(min(__builtin_bit_cast(unsigned, g == 0 ? f * rd2 : -__builtin_fmaf(r.mb, r.rd, mb2) * __builtin_amdgcn_rcpf(f)), POISON - 1), rs, OFF_AB + 16384 + R * 4 + g * 8192, 0);
        }
      }
    }
    if (it == 1) {
      const u32x4 op0 = trade16(pack8v(v)), op1 = trade16(pack8v(v + 4));
#define OUT_STORE()                                                                                 \
  if (R != key_row(key)) {                                                                          \
    __builtin_amdgcn_raw_buffer_store_b128(op0, rout, (R * 1024 + rmo) * 2, 0, SC1);                  \
    __builtin_amdgcn_raw_buffer_store_b128(op1, rout, (R * 1024 + rmo + 32) * 2, 0, SC1);             \
  }
      u32x4 xv[4];
      unsigned guess = 0;
      {
        __builtin_amdgcn_s_sleep(GSL);
        const unsigned k_ = key_load(rs, l, 1, bid & 15);
        guess = ffn_xoff(__builtin_amdgcn_readfirstlane(umax16(k_ == POISON ? 0u : k_)), 1, w);
        row_load(rs, g, guess, xv);
      }
      if (w == 0) {
        const unsigned k_ = key_wait(rs, T, l, 1, bid & 15, key_load(rs, l, 1, bid & 15));
        if (l == 0) *(LDS_AS unsigned*)(smem + L_SKEY + 32) = k_;
      }
      DRAIN();
      PINV4(xv);
      BAR();
      const unsigned key = __builtin_amdgcn_readfirstlane(*(LDS_AS unsigned*)(smem + L_SKEY + 32));
      if (ffn_on) {
        f32x4 ga, ua;
        s16x4 dfr[8];
        ffn_d_read(smem, l, w, dfr);
        if (guess != ffn_xoff(key, 1, w)) ld4x16(xv, rs, g * 16, ffn_xoff(key, 1, w));
        ffn_gu(gv, uv, xv, ga, ua);
        ffn_gu_check(rs, T, ffn_xoff(key, 1, w), l, gv, uv, xv, ga, ua);
        ffn_gu_store(smem, l, w, ga, ua);
        BAR();
        f32x4 ya = {0.f, 0.f, 0.f, 0.f}, yb = ya;
        u32x2 hh = ffn_h(ffn_t_read(smem, l), l);
        ffn_d_mma(dfr, hh, ya, yb);
        if ((l & 15) < 8) ffn_d_store(rs, l, 4 * w, 1, slice, ya + yb);
        SB();
        OUT_STORE();
      } else {
        OUT_STORE();
        const unsigned G = bi;
        if (G == 0 && tid == 0) st8(T, rs, F_EP, 0);
        u32x4 pv[4];
        u32x2 cxn, cx1;
        __builtin_amdgcn_s_sleep(RSL);
        red_load(rs, tid, 1, G, key, pv, cxn, cx1);
        red_sum(rs, smem, T, tid, w, 1, G, key, pv, cxn, cx1);
        BAR();
        if (w == 0) {
          const u32x2 ov = red_final<8>(smem, l, key, cxn, cx1);
          if (l < 8) __builtin_amdgcn_raw_buffer_store_b64(ov, rout, (key_row(key) * 1024 + G * 32 + l * 4) * 2, 0, 0);
        }
      }
#undef OUT_STORE
    }
  };
  layer(std::integral_constant<int, 0>{});
  layer(std::integral_constant<int, 1>{});
}

extern "C" void kernel_launch(void* const* d_in, const int* in_sizes, int n_in, void* d_out, int out_size,
                              void* d_ws, size_t ws_size, hipStream_t stream) {
  (void)hipFuncSetAttribute((const void*)mega_kernel, hipFuncAttributeMaxDynamicSharedMemorySize, LDS_BYTES);
  mega_kernel<<<256, 512, LDS_BYTES, stream>>>((const bf16*)d_in[0], (char*)d_ws, (const bf16*)d_in[1], (const bf16*)d_in[2], (const bf16*)d_in[4],
                                               (const bf16*)d_in[5], (const bf16*)d_in[6], (const bf16*)d_in[7], (bf16*)d_out);
}
```

```cpp
#define OSLP 10
#define SLP 24,14,40,3
#include <hip/hip_runtime.h>
#include <stdint.h>
#include <utility>

typedef __bf16 bf16;
typedef __bf16 bf16x8 __attribute__((ext_vector_type(8)));
typedef __bf16 bf16x2 __attribute__((ext_vector_type(2)));
typedef float f32x4 __attribute__((ext_vector_type(4)));
typedef float f32x2 __attribute__((ext_vector_type(2)));
typedef short s16x4 __attribute__((ext_vector_type(4)));
typedef unsigned int u32x4 __attribute__((ext_vector_type(4)));
typedef unsigned int u32x2 __attribute__((ext_vector_type(2)));
#define LDS_AS __attribute__((address_space(3)))
#define GAS __attribute__((address_space(1)))
typedef GAS unsigned gu32;
typedef GAS unsigned long long gu64;
#define RLX_AGENT __ATOMIC_RELAXED, __HIP_MEMORY_SCOPE_AGENT
typedef __amdgpu_buffer_rsrc_t rsrc_t;

constexpr int cmax(int a, int b) { return a > b ? a : b; }
constexpr int F_ = 3584;
constexpr int SC1 = 16;

constexpr int RX = 4;
constexpr unsigned F_EP = 0, F_TMO = 128;
constexpr unsigned F_XN = 4096;
#define STR 8, 8, 4, 8
constexpr unsigned STV[4] = {STR}, SO = STV[0], SX = STV[1], SK = STV[2], SV = STV[3];
constexpr unsigned F_O = 8u << 20;
constexpr unsigned F_X1 = F_O + 131072;
constexpr unsigned OFF_KEY = F_O + 393216;
constexpr unsigned F_KV = F_O + 524288;
#define KVF(it, i) ((i) * 128 + (it) * 64)
#define OFL(h) (((h) >> 3) * 128 + ((h) & 7) * 8)
#define OFB(it, p) ((p) * 512 + (it) * 64)
constexpr unsigned OFF_AB = 147456;
constexpr unsigned OFF_XNEW = 262144;
constexpr unsigned OFF_CS = 294912;
constexpr unsigned OFF_FX = 393216;
constexpr unsigned OFF_CA = 524288;
constexpr unsigned OFF_CB = 655360;
constexpr unsigned OFF_STAT = 1u << 20;
constexpr unsigned OFF_PART = 3u << 20;
constexpr unsigned OFF_XN = 8u << 20;
constexpr unsigned OFF_O = 16u << 20;
constexpr unsigned OFF_KV = 32u << 20;
constexpr unsigned OFF_WI = 52u << 20;
#define REUSE 0
constexpr unsigned O_STEP = (REUSE & 1) ? 0 : 4u << 20, KV_STEP = (REUSE & 2) ? 0 : 8u << 20;
constexpr unsigned WS_BYTES = 58u << 20;
constexpr unsigned POISON = 0xFFFFFFFFu;

constexpr int L_ST0 = 147456, L_CNT = 147968, L_CSL = 31872, L_FXL = 32256, L_K = 0, L_V = 32768, L_RING = 65536, L_XCH = 0, L_DN = 32768, L_TOUCH0 = 149504, L_RDY = 31744, L_QC = 31808, LDS_BYTES = 163840;
constexpr int STAGE = 24576;
#define SLOT(t) ((t) & 3)

template <typename F, int... I> __device__ __forceinline__ void static_for_impl(F&& f, std::integer_sequence<int, I...>) {
  (f(std::integral_constant<int, I>{}), ...);
}
template <int N, typename F> __device__ __forceinline__ void static_for(F&& f) { static_for_impl(f, std::make_integer_sequence<int, N>{}); }
__device__ __forceinline__ unsigned pack2(float a, float b) {
  f32x2 v = {a, b};
  return __builtin_bit_cast(unsigned, __builtin_convertvector(v, bf16x2));
}
__device__ __forceinline__ float asf(unsigned u) { return __builtin_bit_cast(float, u); }
__device__ __forceinline__ float lo16(unsigned u) { return __builtin_bit_cast(float, u << 16); }
__device__ __forceinline__ float hi16(unsigned u) { return __builtin_bit_cast(float, u & 0xffff0000u); }
__device__ __forceinline__ u32x4 pack8(const f32x4 a, const f32x4 b) {
  u32x4 o;
  o[0] = pack2(a[0], a[1]); o[1] = pack2(a[2], a[3]); o[2] = pack2(b[0], b[1]); o[3] = pack2(b[2], b[3]);
  return o;
}
__device__ __forceinline__ f32x4 mfma16(u32x4 a, u32x4 b, f32x4 c) {
  return __builtin_amdgcn_mfma_f32_16x16x32_bf16(__builtin_bit_cast(bf16x8, a), __builtin_bit_cast(bf16x8, b), c, 0, 0, 0);
}
__device__ __forceinline__ f32x4 mfma16k16(s16x4 a, s16x4 b, f32x4 c) { return __builtin_amdgcn_mfma_f32_16x16x16bf16_1k(a, b, c, 0, 0, 0); }
template <int CTRL> __device__ __forceinline__ float dppf(float v) {
  return __builtin_bit_cast(float, __builtin_amdgcn_update_dpp(0, __builtin_bit_cast(int, v), CTRL, 0xf, 0xf, true));
}
template <int CTRL> __device__ __forceinline__ unsigned dppu(unsigned v) {
  return (unsigned)__builtin_amdgcn_update_dpp(0, (int)v, CTRL, 0xf, 0xf, true);
}
__device__ __forceinline__ float add_x16(float v) {
  auto r = __builtin_amdgcn_permlane16_swap(__builtin_bit_cast(unsigned, v), __builtin_bit_cast(unsigned, v), false, false);
  return __builtin_bit_cast(float, (unsigned)r[0]) + __builtin_bit_cast(float, (unsigned)r[1]);
}
__device__ __forceinline__ float add_x32(float v) {
  auto r = __builtin_amdgcn_permlane32_swap(__builtin_bit_cast(unsigned, v), __builtin_bit_cast(unsigned, v), false, false);
  return __builtin_bit_cast(float, (unsigned)r[0]) + __builtin_bit_cast(float, (unsigned)r[1]);
}
__device__ __forceinline__ float max_x16(float v) {
  auto r = __builtin_amdgcn_permlane16_swap(__builtin_bit_cast(unsigned, v), __builtin_bit_cast(unsigned, v), false, false);
  return fmaxf(__builtin_bit_cast(float, (unsigned)r[0]), __builtin_bit_cast(float, (unsigned)r[1]));
}
__device__ __forceinline__ float max_x32(float v) {
  auto r = __builtin_amdgcn_permlane32_swap(__builtin_bit_cast(unsigned, v), __builtin_bit_cast(unsigned, v), false, false);
  return fmaxf(__builtin_bit_cast(float, (unsigned)r[0]), __builtin_bit_cast(float, (unsigned)r[1]));
}
__device__ __forceinline__ unsigned umax_x16(unsigned v) {
  auto r = __builtin_amdgcn_permlane16_swap(v, v, false, false);
  return max((unsigned)r[0], (unsigned)r[1]);
}
__device__ __forceinline__ unsigned umax_x32(unsigned v) {
  auto r = __builtin_amdgcn_permlane32_swap(v, v, false, false);
  return max((unsigned)r[0], (unsigned)r[1]);
}
__device__ __forceinline__ float sum_hi8(float v) {
  v += dppf<0x128>(v);
  v = add_x16(v);
  return add_x32(v);
}
__device__ __forceinline__ float sum32(float v) {
  v += dppf<0xB1>(v);
  v += dppf<0x4E>(v);
  v += dppf<0x124>(v);
  v += dppf<0x128>(v);
  return add_x16(v);
}
__device__ __forceinline__ float sum64(float v) {
  v += dppf<0xB1>(v);
  v += dppf<0x4E>(v);
  v += dppf<0x124>(v);
  v += dppf<0x128>(v);
  v = add_x16(v);
  return add_x32(v);
}

#define BAR() asm volatile("s_waitcnt lgkmcnt(0)\n\ts_barrier" ::: "memory")
#define DRAIN() asm volatile("s_waitcnt vmcnt(0)" ::: "memory")
#define SB() __builtin_amdgcn_sched_barrier(0)
template <int N> __device__ __forceinline__ void wait_vm() { asm volatile("s_waitcnt vmcnt(%0)" ::"n"(N) : "memory"); }

__device__ __forceinline__ void dma16(rsrc_t rs, unsigned voff, unsigned soff, unsigned lds) {
  asm volatile("s_mov_b32 m0, %0\n\ts_nop 0\n\tbuffer_load_dwordx4 %1, %2, %3 offen lds" ::"s"(lds), "v"(voff), "s"(rs), "s"(soff) : "memory");
}
template <int OFF> __device__ __forceinline__ void dma16s(rsrc_t rs, unsigned voff, unsigned soff, unsigned lds) {
  asm volatile("s_mov_b32 m0, %0\n\ts_nop 0\n\tbuffer_load_dwordx4 %1, %2, %3 offen offset:%4 sc1 lds" ::"s"(lds), "v"(voff), "s"(rs), "s"(soff), "n"(OFF) : "memory");
}
__device__ __forceinline__ void touch(unsigned& d, const GAS bf16* ptr) { asm volatile("global_load_dword %0, %1, off" : "+v"(d) : "v"(ptr) : "memory"); }
__device__ __forceinline__ void ldrows(u32x4 (&d)[4], rsrc_t rx, unsigned voff, unsigned soff) {
  asm volatile("buffer_load_dwordx4 %0, %4, %5, %6 offen\n\tbuffer_load_dwordx4 %1, %4, %5, %6 offen offset:512\n\t"
               "buffer_load_dwordx4 %2, %4, %5, %6 offen offset:1024\n\tbuffer_load_dwordx4 %3, %4, %5, %6 offen offset:1536"
               : "=&v"(d[0]), "=&v"(d[1]), "=&v"(d[2]), "=&v"(d[3]) : "v"(voff), "s"(rx), "s"(soff) : "memory");
}
__device__ __forceinline__ void pin4(f32x2* v) { asm volatile("" : "+v"(v[0]), "+v"(v[1]), "+v"(v[2]), "+v"(v[3])); }
__device__ __forceinline__ void touchb(unsigned& d, rsrc_t rs, unsigned vo) { asm volatile("buffer_load_dword %0, %1, %2, 0 offen sc1" : "+v"(d) : "v"(vo), "s"(rs) : "memory"); }
__device__ __forceinline__ float rbf1(float x) { return __builtin_bit_cast(float, pack2(0.f, x)); }
__device__ __forceinline__ void vswap(unsigned& a, unsigned& b) { asm volatile("v_swap_b32 %0, %1" : "+v"(a), "+v"(b)); }
__device__ __forceinline__ void vswap(float& a, float& b) { asm volatile("v_swap_b32 %0, %1" : "+v"(a), "+v"(b)); }
template <typename V, int N> __device__ __forceinline__ void vswapn(V& a, V& b) {
#pragma unroll
  for (int i = 0; i < N; ++i) { auto x = a[i], y = b[i]; vswap(x, y); a[i] = x; b[i] = y; }
}
__device__ __forceinline__ u32x4 ld16(rsrc_t rs, unsigned voff, unsigned soff) { return __builtin_amdgcn_raw_buffer_load_b128(rs, voff, soff, SC1); }
template <int OFF> __device__ __forceinline__ void ld16h(u32x4& d, rsrc_t rs, unsigned voff, unsigned soff) {
  asm volatile("buffer_load_dwordx4 %0, %1, %2, %3 offen offset:%4 sc1" : "=v"(d) : "v"(voff), "s"(rs), "s"(soff), "n"(OFF) : "memory");
}
template <int I, int OFF> __device__ __forceinline__ void ld16w(u32x4& d, rsrc_t rs, unsigned voff, unsigned soff) {
  if constexpr (I == 0) asm volatile("buffer_load_dwordx4 %0, %1, %2, %3 offen offset:%4 sc1" : "={v[128:131]}"(d) : "v"(voff), "s"(rs), "s"(soff), "n"(OFF) : "memory");
  else if constexpr (I == 1) asm volatile("buffer_load_dwordx4 %0, %1, %2, %3 offen offset:%4 sc1" : "={v[132:135]}"(d) : "v"(voff), "s"(rs), "s"(soff), "n"(OFF) : "memory");
  else if constexpr (I == 2) asm volatile("buffer_load_dwordx4 %0, %1, %2, %3 offen offset:%4 sc1" : "={v[136:139]}"(d) : "v"(voff), "s"(rs), "s"(soff), "n"(OFF) : "memory");
  else if constexpr (I == 3) asm volatile("buffer_load_dwordx4 %0, %1, %2, %3 offen offset:%4 sc1" : "={v[140:143]}"(d) : "v"(voff), "s"(rs), "s"(soff), "n"(OFF) : "memory");
  else if constexpr (I == 4) asm volatile("buffer_load_dwordx4 %0, %1, %2, %3 offen offset:%4 sc1" : "={v[144:147]}"(d) : "v"(voff), "s"(rs), "s"(soff), "n"(OFF) : "memory");
  else if constexpr (I == 5) asm volatile("buffer_load_dwordx4 %0, %1, %2, %3 offen offset:%4 sc1" : "={v[148:151]}"(d) : "v"(voff), "s"(rs), "s"(soff), "n"(OFF) : "memory");
  else if constexpr (I == 6) asm volatile("buffer_load_dwordx4 %0, %1, %2, %3 offen offset:%4 sc1" : "={v[152:155]}"(d) : "v"(voff), "s"(rs), "s"(soff), "n"(OFF) : "memory");
  else if constexpr (I == 7) asm volatile("buffer_load_dwordx4 %0, %1, %2, %3 offen offset:%4 sc1" : "={v[156:159]}"(d) : "v"(voff), "s"(rs), "s"(soff), "n"(OFF) : "memory");
  else if constexpr (I == 8) asm volatile("buffer_load_dwordx4 %0, %1, %2, %3 offen offset:%4 sc1" : "={v[160:163]}"(d) : "v"(voff), "s"(rs), "s"(soff), "n"(OFF) : "memory");
  else if constexpr (I == 9) asm volatile("buffer_load_dwordx4 %0, %1, %2, %3 offen offset:%4 sc1" : "={v[164:167]}"(d) : "v"(voff), "s"(rs), "s"(soff), "n"(OFF) : "memory");
  else if constexpr (I == 10) asm volatile("buffer_load_dwordx4 %0, %1, %2, %3 offen offset:%4 sc1" : "={v[168:171]}"(d) : "v"(voff), "s"(rs), "s"(soff), "n"(OFF) : "memory");
  else if constexpr (I == 11) asm volatile("buffer_load_dwordx4 %0, %1, %2, %3 offen offset:%4 sc1" : "={v[172:175]}"(d) : "v"(voff), "s"(rs), "s"(soff), "n"(OFF) : "memory");
}
template <int K> __device__ __forceinline__ void pin_w(u32x4& a, u32x4& b) {
  if constexpr (K == 0) asm volatile("" : "+{v[128:131]}"(a), "+{v[132:135]}"(b)::"memory");
  else if constexpr (K == 1) asm volatile("" : "+{v[136:139]}"(a), "+{v[140:143]}"(b)::"memory");
  else if constexpr (K == 2) asm volatile("" : "+{v[144:147]}"(a), "+{v[148:151]}"(b)::"memory");
  else if constexpr (K == 3) asm volatile("" : "+{v[152:155]}"(a), "+{v[156:159]}"(b)::"memory");
  else if constexpr (K == 4) asm volatile("" : "+{v[160:163]}"(a), "+{v[164:167]}"(b)::"memory");
  else if constexpr (K == 5) asm volatile("" : "+{v[168:171]}"(a), "+{v[172:175]}"(b)::"memory");
}
__device__ __forceinline__ void ld8h(u32x2& d, rsrc_t rs, unsigned voff, unsigned soff) {
  asm volatile("buffer_load_dwordx2 %0, %1, %2, %3 offen sc1" : "=v"(d) : "v"(voff), "s"(rs), "s"(soff) : "memory");
}
__device__ __forceinline__ void ld16u(u32x4& d, rsrc_t rs, unsigned voff, unsigned soff) {
  asm volatile("buffer_load_dwordx4 %0, %1, %2, %3 offen sc1" : "+v"(d) : "v"(voff), "s"(rs), "s"(soff) : "memory");
}
template <int OFF> __device__ __forceinline__ void ld4h(unsigned& d, rsrc_t rs, unsigned voff, unsigned soff) {
  asm volatile("buffer_load_dword %0, %1, %2, %3 offen offset:%4 sc1" : "=v"(d) : "v"(voff), "s"(rs), "s"(soff), "n"(OFF) : "memory");
}
__device__ __forceinline__ void ld4x16(u32x4 (&d)[4], rsrc_t rs, unsigned voff, unsigned soff) {
  asm volatile("buffer_load_dwordx4 %0, %4, %5, %6 offen sc1\n\tbuffer_load_dwordx4 %1, %4, %5, %6 offen offset:64 sc1\n\t"
               "buffer_load_dwordx4 %2, %4, %5, %6 offen offset:128 sc1\n\tbuffer_load_dwordx4 %3, %4, %5, %6 offen offset:192 sc1\n\ts_waitcnt vmcnt(0)"
               : "=&v"(d[0]), "=&v"(d[1]), "=&v"(d[2]), "=&v"(d[3]) : "v"(voff), "s"(rs), "s"(soff) : "memory");
}
__device__ __forceinline__ u32x2 ld8(rsrc_t rs, unsigned voff, unsigned soff) { return __builtin_amdgcn_raw_buffer_load_b64(rs, voff, soff, SC1); }
__device__ __forceinline__ void st16(u32x4 v, rsrc_t rs, unsigned voff, unsigned soff) { __builtin_amdgcn_raw_buffer_store_b128(v, rs, voff + soff, 0, SC1); }
__device__ __forceinline__ void st8(u32x2 v, rsrc_t rs, unsigned voff, unsigned soff) { __builtin_amdgcn_raw_buffer_store_b64(v, rs, voff, soff, SC1); }

__device__ __forceinline__ unsigned ld4(rsrc_t rs, unsigned voff, unsigned soff) { return __builtin_amdgcn_raw_buffer_load_b32(rs, voff, soff, SC1); }
__device__ __forceinline__ void st4(unsigned v, rsrc_t rs, unsigned voff, unsigned soff) { __builtin_amdgcn_raw_buffer_store_b32(v, rs, voff, soff, SC1); }

constexpr unsigned kSpinCap = 1u << 16;
__device__ __forceinline__ bool spin_fail(unsigned& spins, rsrc_t rs, u32x2 T) {
  asm volatile("" ::: "memory");
  ++spins;
  if (spins > kSpinCap) { st8(T, rs, 0, F_TMO); return true; }
  if ((spins & 255u) == 0u) {
    const u32x2 v = ld8(rs, 0, F_TMO);
    if (v[0] == T[0] && v[1] == T[1]) return true;
  }
  __builtin_amdgcn_s_sleep(3);
  return false;
}
__device__ __forceinline__ void poll_flags2(rsrc_t rs, unsigned voff, u32x2 T) {
  for (unsigned spins = 0;;) {
    const u32x4 v = ld16(rs, voff, 0);
    if (__all(v[0] == T[0] && v[1] == T[1] && v[2] == T[0] && v[3] == T[1])) break;
    if (spin_fail(spins, rs, T)) break;
  }
  asm volatile("" ::: "memory");
  __builtin_amdgcn_fence(__ATOMIC_ACQUIRE, "wavefront");
}
template <int SL = 0> __device__ __forceinline__ void poll_flags(rsrc_t rs, unsigned voff, unsigned soff, u32x2 T) {
  for (unsigned spins = 0;;) {
    const u32x2 v = ld8(rs, voff, soff);
    if (__all(v[0] == T[0] && v[1] == T[1])) break;
    if (spin_fail(spins, rs, T)) break;
    if (SL) __builtin_amdgcn_s_sleep(SL);
  }
  asm volatile("" ::: "memory");
  __builtin_amdgcn_fence(__ATOMIC_ACQUIRE, "wavefront");
}

__device__ __forceinline__ unsigned toff(unsigned R, unsigned ks, unsigned g) {
  return (((R >> 5) * 64 + ks * 2 + ((R >> 4) & 1)) << 10) + ((g * 16 + (R & 15)) << 4);
}
__device__ __forceinline__ void unpack8v(const u32x4 u, f32x2* v) {
#pragma unroll
  for (int j = 0; j < 4; ++j) v[j] = f32x2{lo16(u[j]), hi16(u[j])};
}
__device__ __forceinline__ u32x4 pack8v(const f32x2* v) {
  u32x4 o;
#pragma unroll
  for (int j = 0; j < 4; ++j) o[j] = pack2(v[j][0], v[j][1]);
  return o;
}
__device__ __forceinline__ f32x2 rbf1v(f32x2 v) { return f32x2{rbf1(v[0]), rbf1(v[1])}; }
__device__ __forceinline__ float ln_rd(float var) { return __builtin_amdgcn_rcpf(rbf1(__builtin_amdgcn_sqrtf(rbf1(rbf1(var) + 1e-5f)))); }
template <int N> __device__ __forceinline__ void ln_apply(float mb, float rd, const f32x2* v, f32x2* y) {
  const f32x2 mb2 = {mb, mb}, rd2 = {rd, rd};
#pragma unroll
  for (int j = 0; j < N; ++j) y[j] = rbf1v(rbf1v(v[j] - mb2) * rd2);
}
__device__ __forceinline__ float hadd(f32x2 v) {
  float a = v[0];
  asm volatile("" : "+v"(a));
  return a + v[1];
}
__device__ __forceinline__ float add_g(float v) { return add_x32(add_x16(v)); }
__device__ __forceinline__ void add_g3(float& x, float& y, float& z) {
  auto a = __builtin_amdgcn_permlane16_swap(__builtin_bit_cast(unsigned, x), __builtin_bit_cast(unsigned, y), false, false);
  auto b = __builtin_amdgcn_permlane16_swap(__builtin_bit_cast(unsigned, z), __builtin_bit_cast(unsigned, z), false, false);
  const float r = asf(a[0]) + asf(a[1]), t = asf(b[0]) + asf(b[1]);
  auto c = __builtin_amdgcn_permlane32_swap(__builtin_bit_cast(unsigned, r), __builtin_bit_cast(unsigned, r), false, false);
  auto d = __builtin_amdgcn_permlane32_swap(__builtin_bit_cast(unsigned, t), __builtin_bit_cast(unsigned, t), false, false);
  const float u = asf(c[0]) + asf(c[1]);
  z = asf(d[0]) + asf(d[1]);
  auto e = __builtin_amdgcn_permlane16_swap(__builtin_bit_cast(unsigned, u), __builtin_bit_cast(unsigned, u), false, false);
  x = asf(e[0]); y = asf(e[1]);
}
__device__ __forceinline__ u32x4 trade16(u32x4 v) {
  auto a = __builtin_amdgcn_permlane16_swap(v[0], v[2], false, false);
  auto b = __builtin_amdgcn_permlane16_swap(v[1], v[3], false, false);
  return u32x4{(unsigned)a[0], (unsigned)b[0], (unsigned)a[1], (unsigned)b[1]};
}
__device__ __forceinline__ unsigned umax4(u32x4 v) { return max(max(v[0], v[1]), max(v[2], v[3])); }
__device__ __forceinline__ u32x2 unpoison2(u32x2 v) { return u32x2{min(v[0], POISON - 1), min(v[1], POISON - 1)}; }
__device__ __forceinline__ u32x4 unpoison4(u32x4 v) {
  return u32x4{min(v[0], POISON - 1), min(v[1], POISON - 1), min(v[2], POISON - 1), min(v[3], POISON - 1)};
}

typedef union { struct { s16x4 a, b; } hf; u32x4 v; } frag_t;
__device__ __forceinline__ s16x4 trread(const char* smem, unsigned off) {
  return __builtin_amdgcn_ds_read_tr16_b64_v4i16((LDS_AS s16x4*)(smem + off));
}
__device__ __forceinline__ s16x4 trabs(unsigned addr) { return __builtin_amdgcn_ds_read_tr16_b64_v4i16((LDS_AS s16x4*)(size_t)addr); }

constexpr int WA = 6;
#define WRV 3
#define WBV 4
constexpr int WB = WBV, WR = WRV;
#define CH 0, 4, 7, 8, 13, 15, 20, 22, 27, 29
constexpr int CHV[10] = {CH};
constexpr int FSL = CHV[1], FS0 = CHV[2], FS0D = CHV[3], FS1 = CHV[4], FS1S = CHV[5];
constexpr int FXA = CHV[6], FXB = CHV[7], FXL = CHV[8], FXS = CHV[9];
constexpr int FXT = FXB >> 2;
#define C0 27, 3, 16, 19, 22, 23, -1, 5, 4
constexpr int C0V[9] = {C0};
constexpr int IMG = C0V[0], CSD = C0V[1];
constexpr int ERQ = C0V[2], CSL = C0V[3], CSS = C0V[4];
constexpr int RWL = C0V[6], RWS = C0V[7], POI = C0V[8];
#define WTS 99
#define WTG 2
#define PBS 10
#define KP0 26
#define PS0 8
constexpr int EIN = cmax(CSD, cmax(RWS, POI)) + 3;
constexpr int ABS = 27;
constexpr int abl(int it) { return it ? 23 : C0V[5]; }
constexpr int XU = 3, XL = 2;
constexpr int x_slot(int u) { return u % XU == 0 ? 0 : u % XU == 1 ? 32768 : 49152; }
#define TCH 19, 7, 4, 27
constexpr int TCV[4] = {TCH};
constexpr int TS0 = TCV[0], TSN = TCV[1], TW = TCV[2], TST = TCV[3];
#define QLV 26
constexpr int QL = QLV;
#define FDG 0
#define XSL 4
#define FW0 4
#define FW1 4
#define X1F 1
#define RWG 0
#define CSG 4
#define RXCD 5
#define TWR TW
#define WDS (17 - WB)
#define WDW 1
#define OSL0 0
#define OSL1 0
#define OPS 0
#define KVS 0
#define LOP 0
#define OEA 0
#define SDI0 16
constexpr int SLV[4] = {SLP};
constexpr int SDIR = SLV[0], GSL = SLV[1], RSL = SLV[2], KSL = SLV[3];
constexpr bool tile_piece(int s) { return s >= 3 && ((s - 3) >> 2) + 3 < 8 && ((s - 3) & 3) < 3; }
constexpr int a_req(int s, int n) {
  if (!(n & 1)) return -1;
  if (s <= 1) return 8 + 3 * s + (n >> 1);
  return n < 5 && s + 5 < 32 ? 2 * (s + 5) + (n >> 1) : -1;
}
constexpr int vm_n(int it, int s, int n) {
  int c = 0;
  if (n == 0) c += tile_piece(s);
  if (n == 4) c += (s == abl(it)) + (it ? (s == FSL ? 5 : 0) : (s == ERQ) + (s >= IMG && s < IMG + 3));
  if (!it) c += (s == WTS && n == WTG) + (s < 4 && n == 2) + (s == 0 && (n == 0 || n == 4)) + ((n == 3 || n == 5) && s + XL + XU < 32);
  else c += (a_req(s, n) >= 0) + ((s == 0 || s == 4) && n == 2);
  return c;
}
constexpr int vm_at(int it, int s, int n) {
  int c = it ? 8 : 14;
  for (int u = 0; u < 6 * s + n; ++u) c += vm_n(it, u / 6, u % 6);
  return c;
}
constexpr int vm_a(int it, int k) {
  if (k < 4) return 2 * k + 2;
  for (int u = 0; u < 192; ++u)
    if (a_req(u / 6, u % 6) == 2 * k + 1) return vm_at(it, (u + 1) / 6, (u + 1) % 6);
  return 0;
}
constexpr int vm_tile(int it, int t) { return t >= 3 ? vm_at(it, 4 * t - 7, 1) : t == 1 ? vm_at(0, 0, 5) : vm_at(0, 3, 3); }
constexpr int vm_x(int u) { return u == 2 ? 9 : u < XU + XL ? 12 + 2 * (u - XU) : vm_at(0, u - XU - XL + 1, 0); }

constexpr int wo_k(int it, int s) { return it || s < 4 ? 15 - s : s < 8 ? 7 - s : 19 - s; }
constexpr int wo_pos(int it, int s) { return 2 * s + 2 + (s >= WR ? 2 * it : 0); }
constexpr int wo_at(int it, int s) { return 2 * it + (s < WB - WR ? 2 * WR + 2 + 4 * s : s < 16 - WB ? 2 * (2 * WB - WR) + 2 * (s - WB + WR) : 32); }
template <int OFF> __device__ __forceinline__ void lds_count(unsigned ad) {
  asm volatile("s_mov_b64 exec, 1\n\tds_add_u32 %0, %1 offset:%2\n\ts_mov_b64 exec, -1" ::"v"(ad), "v"(1u), "n"(OFF) : "memory");
}
__device__ __forceinline__ void lds_look(char* c, unsigned want) {
  while ((unsigned)__builtin_amdgcn_readfirstlane(__hip_atomic_load((LDS_AS unsigned*)c, __ATOMIC_RELAXED, __HIP_MEMORY_SCOPE_WORKGROUP)) < want) {}
  asm volatile("" ::: "memory");
}
__device__ __forceinline__ void pin1(unsigned& a) { asm volatile("" : "+v"(a)); }
__device__ __forceinline__ void pin2(u32x4& a, u32x4& b) { asm volatile("" : "+v"(a), "+v"(b)::"memory"); }
__device__ __forceinline__ void pin2s(float& a, float& b) { asm volatile("" : "+v"(a), "+v"(b)); }
template <int OFF> __device__ __forceinline__ void lds2(u32x4& a, u32x4& b, unsigned ad) {
  asm volatile("ds_read_b128 %0, %2 offset:%3\n\tds_read_b128 %1, %2 offset:%4" : "=&v"(a), "=&v"(b) : "v"(ad), "n"(OFF), "n"(OFF + 1024) : "memory");
}
template <int N> __device__ __forceinline__ void lds_wait(u32x4& a, u32x4& b) { asm volatile("s_waitcnt lgkmcnt(%2)" : "+v"(a), "+v"(b) : "n"(N) : "memory"); }
__device__ __forceinline__ void pin2f(f32x2& a, f32x2& b) { asm volatile("" : "+v"(a), "+v"(b)); }
__device__ __forceinline__ void pin_ac(f32x4 (&ac)[2][4]) {
  asm volatile("" : "+{v[176:179]}"(ac[0][0]), "+{v[180:183]}"(ac[0][1]), "+{v[184:187]}"(ac[0][2]), "+{v[188:191]}"(ac[0][3]),
                    "+{v[192:195]}"(ac[1][0]), "+{v[196:199]}"(ac[1][1]), "+{v[200:203]}"(ac[1][2]), "+{v[204:207]}"(ac[1][3]));
}

constexpr int L_XC = LDS_BYTES - 64, L_SKEY = 32704, L_SRED = 4096, L_SFIN = 20480;
__device__ __forceinline__ unsigned key_load(rsrc_t rs, int l, int L, int rep) { return ld4(rs, (l & 15) * SK, OFF_KEY + (L * 16 + rep) * cmax(16 * SK, 128)); }
__device__ __forceinline__ unsigned umax16(unsigned k) {
  k = max(k, dppu<0xB1>(k));
  k = max(k, dppu<0x4E>(k));
  k = max(k, dppu<0x141>(k));
  return max(k, dppu<0x140>(k));
}
__device__ __forceinline__ unsigned key_row(unsigned key) { return (0xffffu - (key & 0xffffu)) & 2047u; }
__device__ __forceinline__ unsigned key_wait(rsrc_t rs, u32x2 T, int l, int L, int rep, unsigned kv) {
  if (!__all(kv != POISON)) {
    unsigned spins = 0;
    do {
      if (spin_fail(spins, rs, T)) break;
      if (L == 0) __builtin_amdgcn_s_sleep(KSL);
      kv = key_load(rs, l, L, rep);
    } while (!__all(kv != POISON));
  }
  return __builtin_amdgcn_readfirstlane(umax16(kv));
}
__device__ __forceinline__ unsigned ffn_xoff(unsigned key, int L, int w) { return OFF_CA + L * 32768 + (key_row(key) >> 7) * 2048 + w * 256; }
__device__ __forceinline__ void row_load(rsrc_t rs, int g, unsigned xo, u32x4 (&xv)[4]) {
  ld16h<0>(xv[0], rs, g * 16, xo); ld16h<64>(xv[1], rs, g * 16, xo); ld16h<128>(xv[2], rs, g * 16, xo); ld16h<192>(xv[3], rs, g * 16, xo);
}
template <int SL = 0> __device__ __forceinline__ void ffn_xwait(rsrc_t rs, u32x2 T, unsigned xo, int l, u32x4 (&xv)[4]) {
#define XV_OK() __all(max(max(umax4(xv[0]), umax4(xv[1])), max(umax4(xv[2]), umax4(xv[3]))) != POISON)
  if (!XV_OK()) {
    unsigned spins = 0;
    do {
      if (spin_fail(spins, rs, T)) break;
      if (SL) __builtin_amdgcn_s_sleep(SL);
#pragma unroll
      for (int i = 0; i < 4; ++i) xv[i] = ld16(rs, (l >> 4) * 16 + i * 64, xo);
    } while (!XV_OK());
  }
#undef XV_OK
}
__device__ __forceinline__ void ffn_gu(const u32x4 (&gv)[4], const u32x4 (&uv)[4], const u32x4 (&xv)[4], f32x4& ga, f32x4& ua) {
  ga = mfma16(gv[0], xv[0], f32x4{0.f, 0.f, 0.f, 0.f}); ua = mfma16(uv[0], xv[0], f32x4{0.f, 0.f, 0.f, 0.f});
#pragma unroll
  for (int i = 1; i < 4; ++i) { ga = mfma16(gv[i], xv[i], ga); ua = mfma16(uv[i], xv[i], ua); }
}
__device__ __forceinline__ void ffn_gu_check(rsrc_t rs, u32x2 T, unsigned xo, int l, const u32x4 (&gv)[4], const u32x4 (&uv)[4], u32x4 (&xv)[4], f32x4& ga, f32x4& ua) {
  if (__builtin_expect(__any(ga[0] != ga[0]), 0)) {
    ffn_xwait(rs, T, xo, l, xv);
    ffn_gu(gv, uv, xv, ga, ua);
  }
}
__device__ __forceinline__ void ffn_gu_store(char* smem, int l, int w, f32x4 ga, f32x4 ua) {
  LDS_AS f32x4* sred = (LDS_AS f32x4*)(smem + L_SRED);
  if ((l & 15) == 0) { sred[w * 8 + (l >> 4)] = ga; sred[w * 8 + 4 + (l >> 4)] = ua; }
}
__device__ __forceinline__ f32x4 ffn_t_read(char* smem, int l) { return ((const LDS_AS f32x4*)(smem + L_SRED))[(l & 7) * 8 + ((l >> 3) & 1) * 4 + (l >> 4)]; }
__device__ __forceinline__ void ffn_d_read(char* smem, int l, int blk, s16x4 (&dfr)[8]) {
#pragma unroll
  for (int i = 0; i < 8; ++i) dfr[i] = trread(smem, L_DN + blk * 4096 + (l >> 4) * 1024 + ((l & 15) >> 2) * 256 + i * 32 + (l & 3) * 8);
}
__device__ __forceinline__ float ffn_d_sum(float t) { t += dppf<0xB1>(t); t += dppf<0x4E>(t); return t + dppf<0x141>(t); }
__device__ __forceinline__ u32x2 ffn_h(f32x4 t, int l) {
#pragma unroll
  for (int e = 0; e < 4; ++e) t[e] = ffn_d_sum(t[e]);
  const float a = (l & 1) ? t[1] : t[0], b = (l & 1) ? t[3] : t[2], s = (l & 2) ? b : a;
  const float gg = rbf1(s), uu = rbf1(dppf<0x128>(s));
  const float h = rbf1(gg * __builtin_amdgcn_rcpf(1.0f + __expf(-gg))) * uu;
  const unsigned h0 = pack2(h, dppf<0x55>(h)), h1 = pack2(dppf<0xAA>(h), dppf<0xFF>(h));
  return (l & 15) == 0 ? u32x2{h0, h1} : u32x2{0u, 0u};
}
__device__ __forceinline__ void ffn_d_mma(const s16x4 (&dfr)[8], u32x2& hh, f32x4& ya, f32x4& yb) {
#pragma unroll
  for (int i = 0; i < 8; ++i) {
    if (i & 1) yb = mfma16k16(dfr[i], __builtin_bit_cast(s16x4, hh), yb);
    else ya = mfma16k16(dfr[i], __builtin_bit_cast(s16x4, hh), ya);
    hh = u32x2{dppu<0x111>(hh[0]), dppu<0x111>(hh[1])};
  }
}
__device__ __forceinline__ void ffn_d_store(rsrc_t rs, int l, int G0, int L, int slice, f32x4 y) {
  const unsigned c = l & 15;
  st16(unpoison4(__builtin_bit_cast(u32x4, y)), rs, (G0 + (c >> 1)) * 32768 + slice * 128 + (c & 1) * 64 + (l >> 4) * 16, OFF_PART + L * (1u << 20));
}
__device__ __forceinline__ void red_load(rsrc_t rs, int tid, int L, unsigned G, unsigned key, u32x4 (&pv)[4], u32x2& cxn, u32x2& cx1) {
#pragma unroll
  for (int k = 0; k < 4; ++k) pv[k] = ld16(rs, min(tid + 512 * k, 1791) * 16, OFF_PART + L * (1u << 20) + G * 32768);
  cxn = ld8(rs, (tid & 3) * 16 + (tid & 4) * 2, OFF_CA + L * 32768 + (key_row(key) >> 7) * 2048 + G * 64);
  cx1 = ld8(rs, (tid & 3) * 16 + (tid & 4) * 2, OFF_CB + L * 32768 + (key_row(key) >> 7) * 2048 + G * 64);
}
__device__ __forceinline__ void red_sum(rsrc_t rs, char* smem, u32x2 T, int tid, int w, int L, unsigned G, unsigned key,
                                        u32x4 (&pv)[4], u32x2& cxn, u32x2& cx1) {
#define PV_OK() __all(max(max(max(umax4(pv[0]), umax4(pv[1])), max(umax4(pv[2]), umax4(pv[3]))), max(max(cxn[0], cxn[1]), max(cx1[0], cx1[1]))) != POISON)
  if (!PV_OK()) {
    unsigned spins = 0;
    do {
      if (spin_fail(spins, rs, T)) break;
      red_load(rs, tid, L, G, key, pv, cxn, cx1);
    } while (!PV_OK());
  }
#undef PV_OK
  f32x4 s = (__builtin_bit_cast(f32x4, pv[0]) + __builtin_bit_cast(f32x4, pv[1])) + __builtin_bit_cast(f32x4, pv[2]);
  if (w < 4) s += __builtin_bit_cast(f32x4, pv[3]);
  ((LDS_AS f32x4*)(smem + L_SFIN))[tid] = s;
}
template <int NW> __device__ __forceinline__ u32x2 red_final(char* smem, int l, unsigned key, u32x2 cxn, u32x2 cx1) {
  const LDS_AS f32x4* sf = (const LDS_AS f32x4*)(smem + L_SFIN) + l;
  f32x4 s = (sf[0] + sf[64]) + (sf[128] + sf[192]);
  if constexpr (NW == 8) s += (sf[256] + sf[320]) + (sf[384] + sf[448]);
#pragma unroll
  for (int j = 0; j < 4; ++j) s[j] = sum_hi8(s[j]);
  const unsigned k16 = key >> 16;
  const float rmax = __builtin_bit_cast(float, ((k16 & 0x8000u) ? (k16 & 0x7fffu) : (~k16 & 0xffffu)) << 16);
  const float sig = rbf1(__builtin_amdgcn_rcpf(1.0f + __expf(-rmax)));
  const float xn4[4] = {lo16(cxn[0]), hi16(cxn[0]), lo16(cxn[1]), hi16(cxn[1])};
  const float x14[4] = {lo16(cx1[0]), hi16(cx1[0]), lo16(cx1[1]), hi16(cx1[1])};
  float o[4];
#pragma unroll
  for (int j = 0; j < 4; ++j) o[j] = x14[j] + rbf1(xn4[j] + rbf1(rbf1(s[j]) * sig));
  return u32x2{pack2(o[0], o[1]), pack2(o[2], o[3])};
}

struct RowFin { float mean, var, mb, rd; unsigned key; };
__device__ __forceinline__ void fin_wait(rsrc_t rs, u32x2 T, unsigned voff, unsigned so, u32x4 (&sp)[4]) {
#define ST_OK() __all(max(max(umax4(sp[0]), umax4(sp[1])), max(umax4(sp[2]), umax4(sp[3]))) != POISON)
  if (!ST_OK()) {
    unsigned spins = 0;
    do {
      if (spin_fail(spins, rs, T)) break;
#pragma unroll
      for (int i = 0; i < 4; ++i) sp[i] = ld16(rs, voff + i * 2048, so);
    } while (!ST_OK());
  }
#undef ST_OK
}
__device__ __forceinline__ RowFin fin_key(rsrc_t rs, u32x2 T, unsigned voff, unsigned so, char* skey, int l, int w, unsigned R, float wsum, u32x4 (&sp)[4]) {
  RowFin r;
#define ST_SUM() ((__builtin_bit_cast(f32x4, sp[0]) + __builtin_bit_cast(f32x4, sp[1])) + (__builtin_bit_cast(f32x4, sp[2]) + __builtin_bit_cast(f32x4, sp[3])))
  f32x4 sf = ST_SUM();
  const float z = (sf[0] + sf[1]) + sf[2];
  if (__any(z != z)) {
    fin_wait(rs, T, voff, so, sp);
    sf = ST_SUM();
  }
#undef ST_SUM
  float s0 = sf[0], s1 = sf[1], s2 = sf[2];
  add_g3(s0, s1, s2);
  r.mean = s0 * (1.0f / 1024.0f);
  r.var = fmaxf(__builtin_fmaf(-r.mean, r.mean, s1 * (1.0f / 1024.0f)), 0.f);
  r.mb = rbf1(r.mean); r.rd = ln_rd(r.var);
  const unsigned rb16 = pack2((s2 - r.mb * wsum) * r.rd, 0.f) & 0xffffu;
  r.key = min((((rb16 & 0x8000u) ? (~rb16 & 0xffffu) : (rb16 | 0x8000u)) << 16) | (0xffffu - R), POISON - 1);
  const unsigned wk = umax16(r.key);
  if (l == 0) ((LDS_AS unsigned*)skey)[w] = wk;
  return r;
}
__device__ __forceinline__ unsigned fin_cand(rsrc_t rs, char* skey, int L, int g, int rb, int ct, const RowFin& r, const f32x2 (&v)[8]) {
  const u32x4 k0 = *(const LDS_AS u32x4*)skey, k1 = *(const LDS_AS u32x4*)(skey + 16);
  const unsigned best = max(umax4(k0), umax4(k1));
  if (r.key == best) {
    const unsigned co = L * 32768 + rb * 2048 + ct * 128 + g * 16;
    f32x2 t[8], rd2 = {r.rd, r.rd}, of2 = {-r.mb * r.rd, -r.mb * r.rd};
    asm volatile("" : "+v"(rd2), "+v"(of2));
#pragma unroll
    for (int j = 0; j < 8; ++j) t[j] = __builtin_elementwise_fma(v[j], rd2, of2);
    st16(unpoison4(pack8v(t)), rs, co, OFF_CA); st16(unpoison4(pack8v(t + 4)), rs, co + 64, OFF_CA);
    if (g == 0) st4(best, rs, OFF_KEY + (L * 16 + ct) * cmax(16 * SK, 128) + rb * SK, 0);
    st16(unpoison4(pack8v(v)), rs, co, OFF_CB); st16(unpoison4(pack8v(v + 4)), rs, co + 64, OFF_CB);
  }
  return best;
}
__device__ __forceinline__ void fin_x2(const RowFin& r, f32x2 (&v)[8]) {
  f32x2 sc2 = {1.0f + r.rd, 1.0f + r.rd}, of2 = {-r.mb * r.rd, -r.mb * r.rd};
  asm volatile("" : "+v"(sc2), "+v"(of2));
#pragma unroll
  for (int j = 0; j < 8; ++j) v[j] = __builtin_elementwise_fma(v[j], sc2, of2);
}

extern "C" __device__ unsigned long long dispatch_id() __asm("llvm.amdgcn.dispatch.id");

extern "C" __global__ __launch_bounds__(512, 2) void mega_kernel(const bf16* x, char* ws, const bf16* wqkv, const bf16* wo, const bf16* wr,
                                                                 const bf16* wg, const bf16* wu, const bf16* wd, bf16* out) {
  extern __shared__ __attribute__((aligned(16))) char smem[];
  const int w0_ = __builtin_amdgcn_readfirstlane(threadIdx.x >> 6);
  typedef unsigned long long u64x2 __attribute__((ext_vector_type(2)));
  u64x2 a01;
  unsigned long long a2;
  asm volatile("s_load_dwordx4 %0, %2, 0\n\ts_load_dwordx2 %1, %2, 16" : "=&s"(a01), "=&s"(a2) : "s"(__builtin_amdgcn_kernarg_segment_ptr()));
  const unsigned lbase = (unsigned)(size_t)(LDS_AS char*)smem;
  {
    unsigned long long pc;
    unsigned len;
    asm volatile("s_getpc_b64 %0\n\ts_mov_b32 %1, .Ltext_end-mega_kernel\n\t.subsection 1\n.Ltext_end:\n\t.subsection 0" : "=s"(pc), "=s"(len));
    const unsigned o = ((blockIdx.x >> 3) * 64 + (threadIdx.x & 63)) * 128;
    if (w0_ == 7 && o < len)
      asm volatile("s_mov_b32 m0, %0\n\ts_nop 0\n\tglobal_load_lds_dword %1, %2" ::"s"(lbase + L_TOUCH0), "v"(o), "s"(pc & ~127ull) : "memory");
  }
#define HGRP() ((xcd & 2) + (xcd >> 2))
#define IDS()                                                                                         \
  int tid = threadIdx.x, w = w0_, bid = blockIdx.x;                                                   \
  asm volatile("" : "+v"(tid));                                                                       \
  asm volatile("" : "+s"(w));                                                                         \
  asm volatile("" : "+s"(bid));                                                                       \
  const int l = tid & 63, xcd = bid & 7, bi = bid >> 3;                                               \
  const int p = (xcd & 1) * 4 + (bi & 3), h = HGRP() * 8 + (bi >> 2);         \
  const int rb = 2 * xcd + (bi & 1), ct = bi >> 1;                               \
  const int slice = (xcd - (xcd > RXCD)) * 32 + bi;                                            \
  const bool ffn_on = xcd != RXCD;                                                                    \
  const int g = l >> 4;                                                                               \
  const int rt = w < 4 ? w : 11 - w;          \
  KARGS();                                                                                            \
  (void)p; (void)h; (void)rb; (void)ct; (void)slice; (void)ffn_on; (void)g; (void)rt;
#define PROT() (2 * (p & 3) + (p >> 2))
  typedef const void* const __attribute__((address_space(4)))* karg_t;
#define KARGS() karg_t ka = (karg_t)__builtin_amdgcn_kernarg_segment_ptr(); asm volatile("" : "+s"(ka))
#define ARGP(name, i) const GAS bf16* name = (const GAS bf16*)ka[i]; asm volatile("" : "+s"(name))
#define RSRC(ptr, bytes) __builtin_amdgcn_make_buffer_rsrc((void*)(ptr), 0, bytes, 0x00020000)
#define rwq RSRC(pwq, 1024 * 3072 * 2)
#define rwo RSRC(pwo, 1024 * 1024 * 2)
#define rwg RSRC(pwg, 1024 * F_ * 2)
#define rwu RSRC(pwu, 1024 * F_ * 2)
#define rwd RSRC(pwd, 1024 * F_ * 2)
#define rout RSRC(pout, 2048 * 1024 * 2)

#define WQ_VOFF() ((l >> 2) * 6144 + (((l & 3) ^ ((((l >> 4) ^ (l >> 5)) & 1) << 1)) << 4))
#define WQ_KMAJ(q) (((q) % 3) * 8 + (q) / 3)
#define WQ_PIECE(t, j, dst) dma16(rwq, wqv, ((((t) + rot) & 7) * 128 + ((j) & 7) * 16) * 6144 + ((j) >> 3) * 2048 + h * 64, lbase + (dst) + (j) * 1024)
#define WQ_BOFF(dt) ((g >> 1) * 1024 + (8 * (g & 1) + ((l & 15) >> 2)) * 64 + (l & 3) * 8 + (((dt) ^ (g & 1)) << 5))
#define X_REQ(u, hf) dma16(rx, xv[hf], (p * 256 + rt * 32 + ((u) & 1) * 16 + (hf) * 8) * 2048 + ((((u) >> 2) + rot) & 7) * 256 + (((u) >> 1) & 1) * 128, lbase + rt * 2048 + x_slot(u) + (hf) * 1024)
#define WO_PIECE(j) dma16(rwo, wov, ((w >> 2) * 64 + (w & 3) + 4 * (j)) * 16384 + ct * 128, lbase + 32768 + (j) * 8192 + w * 1024)
#define WD_DMA_OF(v) _Pragma("unroll") for (int i = 0; i < 4; ++i)                                  \
      dma16(rwd, (l >> 4) * 2048 + (l & 15) * 16, (unsigned)(slice * 16 + 4 * i) * 2048 + (v) * 256, lbase + L_DN + (v) * 4096 + i * 1024)
#define FFN_DMA(m) dma16((m) & 1 ? rwu : rwg, fnv, (unsigned)(((4 * w + ((m) >> 1)) * 32) * F_ + slice * 16) * 2, lbase + 65536 + (m) * 8192 + w * 1024)
#define POISON_BIG(L)                                                                                \
  if (w >= 2) {                                                                                      \
    unsigned pz1 = POISON;                                                                           \
    asm volatile("" : "+v"(pz1));                                                                    \
    st16(u32x4{pz1, pz1, pz1, pz1}, rs, (w >= 4 ? OFF_PART + (L) * (1u << 20) + bid * 4096 - 4096 : OFF_STAT + (L) * 524288 + (p * 32 + h) * 2048 - 2048) + tid * 16, 0); \
  }
  u32x4 gv[4], uv[4];
  u32x4 keep[9];
  u32x4 pre[4][2];
#define PRE_LOAD(it_, rot_, s0, s1)                                                                  \
  static_for<(s1) - (s0)>([&](auto c_) __attribute__((always_inline)) {                              \
    constexpr int s = (s0) + decltype(c_)::value;                                                    \
    const unsigned ab_ = OFF_XN + (it_) * (4u << 20) + (((rot_) + (s >> 2)) & 7) * 8192 + ((s >> 1) & 1) * 4096; \
    ld16w<2 * s, (s & 1) * 2048>(pre[s][0], rs, l * 16, ab_ + (p * 8 + rt) * 65536);                         \
    ld16w<2 * s + 1, (s & 1) * 2048 + 1024>(pre[s][1], rs, l * 16, ab_ + (p * 8 + rt) * 65536);                 \
  })
  u32x2 T;
  const GAS bf16* tsrc;
  unsigned xv[2], xa[2];
  u32x2 ep;
  u32x4 rows0[4];
#define X_READ(u, d0, d1) { d0 = *(const LDS_AS u32x4*)(size_t)(xa[0] + x_slot(u)); d1 = *(const LDS_AS u32x4*)(size_t)(xa[1] + x_slot(u)); }
  unsigned g1[5], wqv0;
  {
    IDS();
    const int rot = PROT(), qa = WQ_KMAJ(w), qb = WQ_KMAJ(8 + w);
    wqv0 = WQ_VOFF();
    xv[0] = (l >> 3) * 2048 + (((l & 7) ^ (l >> 4)) << 4); xv[1] = xv[0] ^ 64;
    g1[0] = (rot * 128 + (qa & 7) * 16) * 6144 + (qa >> 3) * 2048 + h * 64; g1[1] = lbase + L_RING + qa * 1024;
    g1[2] = (rot * 128 + (qb & 7) * 16) * 6144 + (qb >> 3) * 2048 + h * 64; g1[3] = lbase + L_RING + qb * 1024;
    g1[4] = (p * 256 + rt * 32) * 2048 + rot * 256;
    asm volatile("" : "+v"(wqv0), "+v"(xv[0]), "+v"(xv[1]));
    asm volatile("s_waitcnt lgkmcnt(0)" : "+s"(a01), "+s"(a2), "+s"(g1[0]), "+s"(g1[1]), "+s"(g1[2]), "+s"(g1[3]), "+s"(g1[4]));
  }
  x = (const bf16*)a01[0]; ws = (char*)a01[1]; wqkv = (const bf16*)a2;
  const rsrc_t rs = __builtin_amdgcn_make_buffer_rsrc((void*)ws, 0, WS_BYTES, 0x00020000);
  const rsrc_t rx = RSRC(x, 2048 * 1024 * 2);
  {
    IDS();
    const int rot = PROT();
    const bf16* pwq = wqkv;
    const unsigned wqv = wqv0;
    dma16(rwq, wqv, g1[0], g1[1]);
#pragma unroll
    for (int i = 0; i < 4; ++i) dma16(rx, xv[i & 1], g1[4] + i * 16384, lbase + rt * 2048 + x_slot(i >> 1) + (i & 1) * 1024);
    dma16(rwq, wqv, g1[2], g1[3]);
    asm volatile("s_barrier" ::: "memory");
    WQ_PIECE(0, WQ_KMAJ(16 + w), L_RING);
    if (w == 6 && bi == 0) {
      ARGP(pwr, 4);
      asm volatile("s_mov_b32 m0, %0\n\ts_nop 0\n\tbuffer_load_dword %1, %2, 0 offen lds" ::"s"(lbase + L_TOUCH0 + 256), "v"((l & 15) * 128), "s"(RSRC(pwr, 2048)) : "memory");
    }
    X_REQ(2, 0); X_REQ(2, 1);
    asm volatile("buffer_load_dwordx2 %0, off, %1, 0 sc1" : "=v"(ep) : "s"(rs) : "memory");
    if constexpr (RWL == -1) {
      asm volatile("" : "=v"(rows0[0]), "=v"(rows0[1]), "=v"(rows0[2]), "=v"(rows0[3]));
      if (w < 4) ldrows(rows0, rx, (l >> 5) * 2048 + (l & 31) * 16, (p * 256 + h * 8 + 2 * w) * 2048);
    }
    if constexpr (PBS < 0) { POISON_BIG(0); }
    {
      ARGP(pwo, 3); ARGP(pwg, 5); ARGP(pwu, 6);
      tsrc = (w < 4 ? pwg : pwu) + ((bi * 32 + ((tid & 255) >> 3)) * F_ + (slice >> 5) * 512 + (tid & 7) * 64);
      if (!ffn_on) tsrc = pwo + (bi * 512 + tid) * 64;
      asm volatile("" : "+v"(tsrc));
    }
#pragma unroll
    for (int kk = 0; kk < 2; ++kk) {
      const int r = l & 15;
      xa[kk] = lbase + rt * 2048 + (r >> 3) * 1024 + (r & 7) * 128 + (((4 * kk + g) ^ (r >> 1)) << 4);
      asm volatile("" : "+v"(xa[kk]));
    }
    if (tid == 192) *(LDS_AS u32x4*)(smem + L_QC) = u32x4{0u, 0u, 0u, 0u};
    if (RWL == -1 && w < 4) wait_vm<8>(); else wait_vm<4>();
    X_READ(0, pre[0][0], pre[1][0]); X_READ(1, pre[0][1], pre[1][1]);
    asm volatile("s_waitcnt lgkmcnt(0)\n\ts_barrier" : "+v"(pre[0][0]), "+v"(pre[0][1]), "+v"(pre[1][0]), "+v"(pre[1][1])::"memory");
    if constexpr (RWL == -2) {
      asm volatile("" : "=v"(rows0[0]), "=v"(rows0[1]), "=v"(rows0[2]), "=v"(rows0[3]));
      if (w < 4) ldrows(rows0, rx, (l >> 5) * 2048 + (l & 31) * 16, (p * 256 + h * 8 + 2 * w) * 2048);
    }
    X_REQ(3, 0); X_REQ(3, 1); X_REQ(4, 0); X_REQ(4, 1);
  }
  unsigned sel0 = 0x7FFFFFFFu;
  u32x4 x2p[2];
  unsigned xm0 = 0, kg0 = 0;

  auto layer = [&](auto it_c) __attribute__((always_inline)) {
    constexpr int it = decltype(it_c)::value;
    f32x4 acc[2][6];
    f32x2 qs2[2];
    u32x4 k0, k1, v0, v1;
    unsigned tdum = 0;
    {
      IDS();
      bool aff = false;
      unsigned aq[2];
      u32x4 av[2];
#define PIN_ACC() asm volatile("" : "+{v[176:179]}"(acc[0][0]), "+{v[180:183]}"(acc[0][1]), "+{v[184:187]}"(acc[0][2]), "+{v[188:191]}"(acc[0][3]), "+{v[192:195]}"(acc[0][4]), "+{v[196:199]}"(acc[0][5]), \
                               "+{v[200:203]}"(acc[1][0]), "+{v[204:207]}"(acc[1][1]), "+{v[208:211]}"(acc[1][2]), "+{v[212:215]}"(acc[1][3]), "+{v[216:219]}"(acc[1][4]), "+{v[220:223]}"(acc[1][5]))
#define PINV4(x) asm volatile("" : "+v"(x[0]), "+v"(x[1]), "+v"(x[2]), "+v"(x[3])::"memory")
      auto qkv_loop = [&](auto it_c) __attribute__((always_inline)) {
        constexpr int IT = decltype(it_c)::value;
        const unsigned abase = OFF_XN + IT * (4u << 20) + (p * 8 + rt) * 65536;
        const int rot = PROT() + 5 * IT;
        unsigned fkey = 0, kv, bq[2];
        u32x4 fx[4], bv[2], af[WA][2];
        u32x2 cxn, cx1;
        f32x4 sa, sb, cs4[2];
        f32x2 b2[2];
#define AB_OFF() (OFF_AB + IT * 16384 + (p * 8 + rt) * 128)
#define A_LOAD1(k, a) ld16w<(k) % WA * 2 + (a), ((k) & 1) * 2048 + (a) * 1024>(af[(k) % WA][a], rs, l * 16, abase + ((((k) >> 2) + rot) & 7) * 8192 + (((k) >> 1) & 1) * 4096)
#pragma unroll
        for (int s = 0; s < 2 + 2 * IT; ++s) { af[s][0] = pre[s][0]; af[s][1] = pre[s][1]; }
        const bf16* pwq = wqkv;
        const int tstep = TS0 + bid % TSN;
        const unsigned wqv = WQ_VOFF();
#define QKV_DMA1(t, i)                                                                            \
  {                                                                                               \
    if constexpr (IT == 0) WQ_PIECE(t, 3 * w + (i), L_RING + SLOT(t) * STAGE);                    \
    else dma16s<(i) * 1024>(rs, l * 16, OFF_WI + (h * 8 + (((t) + rot) & 7)) * STAGE + w * 3072, lbase + L_RING + SLOT(t) * STAGE + w * 3072); \
  }
        unsigned bo[2][2];
#pragma unroll
        for (int dt = 0; dt < 2 - IT; ++dt) {
          bo[dt][0] = lbase + L_RING + (IT ? l * 16 : WQ_BOFF(dt)); bo[dt][1] = bo[dt][0] + 2 * STAGE;
          asm volatile("" : "+v"(bo[dt][0]), "+v"(bo[dt][1]));
        }
#define FRAG_T() (lbase + L_RING + (4 * g + ((l & 15) >> 2)) * 64 + (l & 3) * 8 + (((g ^ (g >> 1)) & 1) << 5))
        auto frag_t2 = [&](unsigned o) __attribute__((always_inline)) {
          frag_t t;
          t.hf.a = trabs(o); t.hf.b = trabs(o + 1024);
          return t.v;
        };
        unsigned ka[3];
        auto tile_sum = [&](u32x4 pt, int dst) __attribute__((always_inline)) {
          f32x4 c = __builtin_bit_cast(f32x4, pt);
#pragma unroll
          for (int e = 0; e < 4; ++e) { c[e] += dppf<0x124>(c[e]); c[e] += dppf<0x128>(c[e]); c[e] = add_x16(c[e]); }
          if ((l & 28) == 0) *(LDS_AS f32x4*)(smem + dst + (w * 2 + (l >> 5)) * 64 + (l & 3) * 16) = c;
        };
        frag_t bfr[6];
#define QKV_BO(s, n) (bo[(n) & 1][SLOT((s) >> 2) >> 1] + (SLOT((s) >> 2) & 1) * STAGE + ((n) >> 1) * 8192 + ((s) & 3) * 2048)
#define QKV_RDA(s, n)                                                                             \
  {                                                                                               \
    if constexpr (IT == 0) bfr[n].hf.a = trabs(QKV_BO(s, n));                                     \
    else bfr[n].v = *(const LDS_AS u32x4*)(size_t)(bo[0][SLOT((s) >> 2) >> 1] + (SLOT((s) >> 2) & 1) * STAGE + (((s) & 3) * 6 + (n)) * 1024); \
  }
#define QKV_RDB(s, n) { if constexpr (IT == 0) bfr[n].hf.b = trabs(QKV_BO(s, (n) ^ 1) + 256); }
#pragma unroll
        for (int n = 0; n < 6; ++n) { QKV_RDA(0, n); if (n < 5) QKV_RDB(0, n); }
#define XF_OFF() (l & 8 ? ((l & 7) * RX + bi % RX) * 256 + h * 8 : (p * RX + (xcd >> 1) % RX) * 256 + (4 * rt + (l & 3)) * 8)
#define XP_OFF() (OFF_XNEW + ((rot + FXT) & 7) * 256)
#define FP_OFF(i) (w == 3 ? OFF_XNEW + (i) * 1024 : OFF_FX + h * 3072 + w * 1024)
#define FX_OK(n) __all(((n) == 4 ? max(max(umax4(fx[0]), umax4(fx[1])), max(umax4(fx[2]), umax4(fx[3]))) : max(umax4(fx[0]), umax4(fx[1]))) != POISON)
#define SINCE(s0) (vm_at(IT, s, n) - vm_at(IT, s0, 5))
#define DEF4(x) asm volatile("" : "=v"(x[0]), "=v"(x[1]), "=v"(x[2]), "=v"(x[3]))
        LDS_AS unsigned* qcnt = (LDS_AS unsigned*)(smem + L_QC);
        unsigned qc;
        unsigned qadr = lbase + L_QC, qone = 1;
        asm volatile("" : "+v"(qadr), "+v"(qone));
#define count_in(i) asm volatile("s_mov_b64 exec, 1\n\tds_add_u32 %0, %1 offset:%2\n\ts_mov_b64 exec, -1" ::"v"(qadr), "v"(qone), "n"(4 * (i)) : "memory")
        auto count_wait = [&](int i, unsigned want) __attribute__((always_inline)) {
          if (__builtin_expect((unsigned)__builtin_amdgcn_readfirstlane(qc) < want, 0)) {
            while ((unsigned)__builtin_amdgcn_readfirstlane(__hip_atomic_load(qcnt + i, __ATOMIC_RELAXED, __HIP_MEMORY_SCOPE_WORKGROUP)) < want) {}
          }
          asm volatile("" ::: "memory");
        };
        unsigned long long xok = xm0;
        auto x1_need = [&](unsigned long long m) __attribute__((always_inline)) {
          if (__builtin_expect((xok & m) != m, 0)) {
            for (unsigned spins = 0;;) {
              const u32x2 f = ld8(rs, F_X1 + ((p * RX + (xcd >> 1) % RX) * 32 + (l & 31)) * SX, 0);
              xok = __builtin_amdgcn_ballot_w64(f[0] == T[0] && f[1] == T[1]);
              if ((xok & m) == m) break;
              if (spin_fail(spins, rs, T)) { xok = ~0ull; break; }
              __builtin_amdgcn_s_sleep(KSL);
            }
            asm volatile("" ::: "memory");
          }
        };
        auto gap = [&](auto sc_, auto nc_) __attribute__((always_inline)) {
          constexpr int s = decltype(sc_)::value, n = decltype(nc_)::value;
          {
            constexpr int ts = (s + 3) / 4, tc = (s + 2) / 4, t0 = 1;
            if constexpr (ts >= 3 && ts < 8 && s == 4 * ts - 3 && n == 2) { wait_vm<vm_at(IT, s, n) - vm_tile(IT, ts)>(); count_in(0); }
            if constexpr (!IT && tc >= t0 && tc < 3 && s == 4 * tc - 2 && n == 1) { wait_vm<vm_at(IT, s, n) - vm_tile(IT, tc)>(); count_in(0); }
            if constexpr (IT && ts >= t0 && ts < 3 && s == 4 * ts - 3 && n == 2) count_in(0);
            if constexpr (tc >= t0 && tc < 8 && s == 4 * tc - 2) {
              if constexpr (n == 3) qc = __hip_atomic_load(qcnt, __ATOMIC_RELAXED, __HIP_MEMORY_SCOPE_WORKGROUP);
              if constexpr (n == 5) count_wait(0, 8 * (tc - t0 + 1));
            }
          }
          if constexpr (n == 0 && tile_piece(s)) QKV_DMA1(((s - 3) >> 2) + 3, (s - 3) & 3);
          if constexpr (IT == 0) {
            if constexpr (s == 0 && !(n & 1)) QKV_DMA1(1, n >> 1);
            if constexpr (s >= 1 && s < 4 && n == 2) QKV_DMA1(2, s - 1);
            constexpr int u = s + XL;
            if constexpr (n == 1 && u < 32) {
              if (s == 0 && RWL < 0 && w < 4) wait_vm<vm_at(0, s, n) - vm_x(u) + 4>(); else wait_vm<vm_at(0, s, n) - vm_x(u)>();
              X_READ(u, af[(u & ~1) % WA][u & 1], af[(u | 1) % WA][u & 1]);
            }
            if constexpr (n == 3 && u + XU < 32) { asm volatile("s_waitcnt lgkmcnt(%0)" ::"n"(s + 1 < QL ? 4 : 3) : "memory"); X_REQ(u + XU, 0); }
            if constexpr (n == 5 && u + XU < 32) X_REQ(u + XU, 1);
            if constexpr (s == WTS && n == WTG) {
              const int i = bi * 192 + w * 24 + min(l, 23);
              touch(tdum, (const GAS bf16*)pwq + (size_t)(i / 12 + 512 * (~xcd & 1)) * 3072 + ((i % 12) >> 2) * 1024 + (xcd >> 1) * 256 + (i & 3) * 64);
            }
            if constexpr (s == PBS && n == 0) { POISON_BIG(0); }
            if constexpr (s == 0 && n == 1) count_in(2);
            if constexpr (s == 0 && n == 3) qc = __hip_atomic_load(qcnt + 2, __ATOMIC_RELAXED, __HIP_MEMORY_SCOPE_WORKGROUP);
            if constexpr (s == 0 && n == 5) count_wait(2, 8);
            if constexpr (s == 1 && n == 1) {
              asm volatile("" : "+v"(ep)::"memory");
              const unsigned long long e = (((unsigned long long)(unsigned)__builtin_amdgcn_readfirstlane(ep[1]) << 32) | (unsigned)__builtin_amdgcn_readfirstlane(ep[0])) + 1ull + dispatch_id() * 0x9E3779B97F4A7C16ull;
              T = u32x2{(unsigned)e, (unsigned)(e >> 32)};
            }
            if constexpr (s == RWL && n == 4) {
              DEF4(fx);
              if (w < 4) ldrows(fx, rx, (l >> 5) * 2048 + (l & 31) * 16, (p * 256 + h * 8 + 2 * w) * 2048);
            }
            if constexpr (s == RWS && n == RWG) {
              if (w < 4) {
                if constexpr (RWL >= 0) wait_vm<SINCE(RWL < 0 ? 0 : RWL)>();
                else { fx[0] = rows0[0]; fx[1] = rows0[1]; fx[2] = rows0[2]; fx[3] = rows0[3]; }
                PINV4(fx);
                f32x2 sv[16];
#pragma unroll
                for (int i = 0; i < 4; ++i) unpack8v(fx[i], sv + 4 * i);
                f32x2 t0 = sv[0] + sv[1], t1 = sv[2] + sv[3];
#pragma unroll
                for (int j = 4; j < 16; j += 2) { t0 += sv[j]; t1 += sv[j + 1]; }
                const float m = sum32(hadd(t0 + t1)) * (1.0f / 1024.0f);
                f32x2 m2 = {m, m}, q0 = {0.f, 0.f}, q1 = {0.f, 0.f};
                asm volatile("" : "+v"(m2));
#pragma unroll
                for (int j = 0; j < 16; j += 2) {
                  const f32x2 d0 = sv[j] - m2, d1 = sv[j + 1] - m2;
                  q0 = __builtin_elementwise_fma(d0, d0, q0); q1 = __builtin_elementwise_fma(d1, d1, q1);
                }
                const float rd = ln_rd(sum32(hadd(q0 + q1)) * (1.0f / 1024.0f));
                if ((l & 30) == 0) st4(__builtin_bit_cast(unsigned, (l & 1) ? -rbf1(m) : rd), rs, (l >> 5) * 4 + (l & 1) * 8192, OFF_AB + (p * 256 + h * 8 + 2 * w) * 4);
              }
            }
            if constexpr (s == POI && n == 4) {
              if (w == 3) {
                unsigned pz1 = POISON;
                asm volatile("" : "+v"(pz1));
                const unsigned prb = 2 * p + (h >> 4), pct = h & 15, m = l - 32;
                static_assert(OFF_CB == OFF_CA + 131072, "poison addressing");
                if (l < 60) st16(u32x4{pz1, pz1, pz1, pz1}, rs, l < 32 ? OFF_CA + (l & 8) * 4096 + (l & 16) * 8192 + prb * 2048 + pct * 128 + (l & 7) * 16
                                                             : l < 56 ? OFF_FX + ((h * 6 + (m >> 2)) * 8 + p) * 64 + (l & 3) * 16
                                                                      : OFF_AB + 16384 + (p * 32 + h) * 32 + (l & 1) * 16 + (l & 2) * 4096, 0);
                if (l < 4) st4(pz1, rs, l < 2 ? OFF_XNEW + bid * 8 + l * 4 : OFF_KEY + ((l & 1) * 16 + pct) * cmax(16 * SK, 128) + prb * SK, 0);
              }
            }
            if constexpr (s == POI && n == 2) {
              if (w == 2 && l < 6) {
                unsigned f0 = F_KV + KVF(0, p * 32 + h), f1 = F_O + OFB(0, p) + OFL(h), f2 = F_X1 + ((rb >> 1) * RX * 32 + (l & 3) * 32 + (rb & 1) * 16 + ct) * SX;
                asm volatile("" : "+v"(f0), "+v"(f1), "+v"(f2));
                const unsigned fo = l == 5 ? f0 : l == 4 ? f1 : f2;
                touchb(tdum, rs, fo);
              }
            }
            static_assert(vm_x(EIN + XL) >= vm_at(0, EIN - 3, 5) && IMG >= 1 && CSD >= 1, "first flag");
            if constexpr (s == EIN && n == 1) { if (w < 4) count_in(1); }
            if constexpr (s == EIN + 2 && n == 0) qc = __hip_atomic_load(qcnt + 1, __ATOMIC_RELAXED, __HIP_MEMORY_SCOPE_WORKGROUP);
            if constexpr (s == EIN + 2 && n == 2) {
              if (w == 0) { count_wait(1, 4); if (l < RX) st8(T, rs, F_XN + (p * RX + l) * 256 + h * 8, 0); }
            }
            if constexpr (s == ERQ && n == 4) asm volatile("buffer_load_dwordx2 %0, %1, %2, 0 offen sc1" : "=v"(cxn) : "v"(XF_OFF() + F_XN), "s"(rs) : "memory");
            if constexpr (s == CSL && n == 2) {
              wait_vm<SINCE(ERQ)>();
              asm volatile("" : "+v"(cxn)::"memory");
              if (!__all(cxn[0] == T[0] && cxn[1] == T[1])) poll_flags(rs, XF_OFF(), F_XN, T);
            }
          } else {
#define X1_OFF() (F_X1 + ((p * RX + (xcd >> 1) % RX) * 32 + (l & 31)) * SX)
            if constexpr ((s == 0 || s == 4) && n == 2) asm volatile("buffer_load_dwordx2 %0, %1, %2, 0 offen sc1" : "=v"(cxn) : "v"(X1_OFF()), "s"(rs) : "memory");
            if constexpr ((s == 3 || s == 7) && n == 0) {
              wait_vm<vm_at(1, s, 1) - vm_at(1, s - 3, 3)>();
              asm volatile("" : "+v"(cxn)::"memory");
              xok |= __builtin_amdgcn_ballot_w64(cxn[0] == T[0] && cxn[1] == T[1]);
            }
            if constexpr (a_req(s, n) >= 0 && (a_req(s, n) & 7) == 0) x1_need(0x30003ull << (((rot + a_req(s, n) / 8) & 7) * 2));
          }
          if constexpr (IT && a_req(s, n) >= 0) A_LOAD1(a_req(s, n) / 2, a_req(s, n) & 1);
          if constexpr (IT == 0 && s == CSD && n == CSG) {
            if (w < 3) {
              unsigned a0 = FRAG_T() + SLOT(s >> 2) * STAGE + w * 8192, a1 = a0 ^ 32, one2 = 0x3F803F80u;
              asm volatile("" : "+v"(a0), "+v"(a1), "+v"(one2));
              const u32x4 o4 = {one2, one2, one2, one2};
              f32x4 y0 = {0.f, 0.f, 0.f, 0.f}, y1 = y0;
#pragma unroll
              for (int i = 0; i < 4; ++i) { y0 = mfma16(frag_t2(a0 + i * 2048), o4, y0); y1 = mfma16(frag_t2(a1 + i * 2048), o4, y1); }
              if ((l & 14) == 0) st16(__builtin_bit_cast(u32x4, (l & 1) ? y1 : y0), rs, g * 16 + (l & 1) * 512, OFF_CS + ((h * 6 + 2 * w) * 8 + p) * 64);
            }
          }
          if constexpr (!IT && s == CSL && n == 4) { asm volatile("" : "=v"(fx[0])); if (w < 3) ld16h<0>(fx[0], rs, l * 16, OFF_CS + h * 3072 + w * 1024); }
          if constexpr (!IT && s == CSS && n == 2) {
            if (w < 3) {
              wait_vm<SINCE(CSL)>();
              asm volatile("" : "+v"(fx[0])::"memory");
              tile_sum(fx[0], L_CSL);
            }
          }
          if constexpr (s == abl(IT) && n == 4) ld4h<0>(aq[0], rs, (l & 31) * 4 + (l >> 5) * 8192, AB_OFF());
          if constexpr (s == ABS && n == 0) {
            wait_vm<SINCE(abl(IT))>();
            asm volatile("" : "+v"(aq[0])::"memory");
            if constexpr (IT) {
              for (unsigned spins = 0; !__all(aq[0] != POISON) && !spin_fail(spins, rs, T);) {
                ld4h<0>(aq[0], rs, (l & 31) * 4 + (l >> 5) * 8192, AB_OFF());
                asm volatile("s_waitcnt vmcnt(0)" : "+v"(aq[0])::"memory");
              }
            }
            *(LDS_AS unsigned*)(smem + L_K + (8 + rt) * 2048 + l * 4) = aq[0];
          }
          if constexpr (s == ABS && n == 2) {
#pragma unroll
            for (int a = 0; a < 2; ++a) {
              const LDS_AS char* ab = (const LDS_AS char*)(smem + L_K + (8 + rt) * 2048 + a * 64);
              aq[a] = *(const LDS_AS unsigned*)(ab + (l & 15) * 4); bq[a] = *(const LDS_AS unsigned*)(ab + 128 + (l & 15) * 4);
              av[a] = *(const LDS_AS u32x4*)(ab + g * 16); bv[a] = *(const LDS_AS u32x4*)(ab + 128 + g * 16);
            }
          }
          if constexpr (s == ABS && n == 5) {
#pragma unroll
            for (int a = 0; a < 2; ++a) { b2[a] = f32x2{asf(bq[a]), asf(bq[a])}; asm volatile("" : "+v"(b2[a])); }
          }
          if constexpr (s == ABS + 1) {
            constexpr int k = (n + 4) % 6;
            if constexpr (k < 4) cs4[k & 1] = *(const LDS_AS f32x4*)(smem + L_CSL + k * 64 + g * 16);
            else cs4[k & 1][0] = *(const LDS_AS float*)(smem + L_CSL + k * 64 + (l & 15) * 4);
          }
          if constexpr ((s == ABS + 1 && n > 0) || (s == ABS + 2 && n == 0)) {
            constexpr int k = (n + 3) % 6;
            if constexpr (k < 4) {
#pragma unroll
              for (int a = 0; a < 2; ++a) acc[a][k] = __builtin_elementwise_fma(cs4[k & 1], f32x4{b2[a][0], b2[a][1], b2[a][0], b2[a][1]}, acc[a][k]);
            } else {
              f32x2 c2 = {cs4[k & 1][0], cs4[k & 1][0]};
              asm volatile("" : "+v"(c2));
#pragma unroll
              for (int a = 0; a < 2; ++a) acc[a][k] = __builtin_elementwise_fma(f32x4{c2[0], c2[1], c2[0], c2[1]}, __builtin_bit_cast(f32x4, bv[a]), acc[a][k]);
            }
          }
          if constexpr (IT == 0) {
            if constexpr (s == KP0 - 1 && n < 3) {
              const int c = 3 * (w & 1) + n;
              ka[n] = (FRAG_T() ^ ((c & 1) << 5)) + (SLOT(5) * STAGE + (c >> 1) * 8192 + (w >> 1) * 2048);
              asm volatile("" : "+v"(ka[n]));
            }
            if constexpr (s >= KP0 && s < KP0 + 3 && (n & 1)) keep[3 * (s - KP0) + (n >> 1)] = frag_t2(ka[n >> 1] + (s - KP0) * STAGE);
            if constexpr (s >= IMG && s < IMG + 3 && n == 4) st16(keep[s - IMG], rs, l * 16 + (s - IMG) * 1024, OFF_WI + (h * 8 + ((rot + 5) & 7)) * STAGE + w * 3072);
            if constexpr (s >= TS0 && s < TS0 + TSN && n == 4) {
              if (tstep == s && w < (ffn_on ? TW : TWR)) touch(tdum, tsrc);
            }
            if constexpr (s == TST && n == 4) { if (w >= (ffn_on ? TW : TWR)) touch(tdum, tsrc); }
          } else {
            if constexpr (s == 28 && n == 0) { POISON_BIG(1); }
            if constexpr (s == FSL && n == 4) { ld4h<0>(kv, rs, (l & 15) * SK, OFF_KEY + (bid & 15) * cmax(16 * SK, 128)); row_load(rs, g, ffn_xoff(kg0, 0, w), fx); }
            if constexpr (s == FS0 && n == 0) {
              wait_vm<SINCE(FSL)>();
              asm volatile("" : "+v"(kv)::"memory");
              PINV4(fx);
              if (__builtin_expect(!__all(kv != POISON), 0)) {
                LDS_AS unsigned* kw = (LDS_AS unsigned*)(smem + L_SKEY + 40);
                if (w == 0) {
                  kv = key_wait(rs, T, l, 0, bid & 15, kv);
                } else {
                  while ((kv = __hip_atomic_load(kw, __ATOMIC_RELAXED, __HIP_MEMORY_SCOPE_WORKGROUP)) == POISON) __builtin_amdgcn_s_sleep(2);
                }
              }
              fkey = __builtin_amdgcn_readfirstlane(umax16(kv));
              if (w == 0) *(LDS_AS unsigned*)(smem + L_SKEY + 40) = min(fkey, POISON - 1);
              sel0 = key_row(fkey);
              aff = (int)(sel0 >> 8) == p;
              asm volatile("" : "=v"(sa), "=v"(sb));
              if (ffn_on) {
                if (__builtin_expect(((kg0 ^ fkey) & 0x780u) != 0, 0)) ld4x16(fx, rs, g * 16, ffn_xoff(fkey, 0, w));
                ffn_gu(gv, uv, fx, sa, sb);
              }
            }
            if constexpr (s == FS0 && n == 2) {
              if (ffn_on) {
                ffn_gu_check(rs, T, ffn_xoff(fkey, 0, w), l, gv, uv, fx, sa, sb);
                ffn_gu_store(smem, l, w, sa, sb);
                count_in(1);
              }
            }
            if constexpr (s == FS0D && n == FDG) {
              if (ffn_on && w < 4) {
                s16x4 da[8], db[8];
                ffn_d_read(smem, l, 2 * w, da);
                while (__hip_atomic_load(qcnt + 1, __ATOMIC_RELAXED, __HIP_MEMORY_SCOPE_WORKGROUP) < 8u) {}
                asm volatile("" ::: "memory");
                const f32x4 t = ffn_t_read(smem, l);
                ffn_d_read(smem, l, 2 * w + 1, db);
                u32x2 ha = ffn_h(t, l), hb = {dppu<0x118>(ha[0]), dppu<0x118>(ha[1])};
                f32x4 ya = {0.f, 0.f, 0.f, 0.f}, yb = ya;
#pragma unroll
                for (int i = 0; i < 8; ++i) {
                  ya = mfma16k16(da[i], __builtin_bit_cast(s16x4, ha), ya); yb = mfma16k16(db[i], __builtin_bit_cast(s16x4, hb), yb);
                  ha = u32x2{dppu<0x111>(ha[0]), dppu<0x111>(ha[1])}; hb = u32x2{dppu<0x111>(hb[0]), dppu<0x111>(hb[1])};
                }
                ffn_d_store(rs, l, 8 * w, 0, slice, ya + yb);
              }
            }
#define RED_LOAD(ld, l8)                                                                                                    \
  {                                                                                                                         \
    const unsigned G = bi, po = OFF_PART + G * 32768;                                                        \
    ld(gv[0], rs, tid * 16, po); ld(gv[1], rs, tid * 16, po + 4096); ld(gv[2], rs, tid * 16, po + 8192); ld(gv[3], rs, tid * 16, po + 12288); \
    ld(uv[0], rs, tid * 16, po + 16384); ld(uv[1], rs, tid * 16, po + 20480); ld(uv[2], rs, tid * 16, po + 24576);        \
    l8(cxn, rs, (l & 3) * 16 + (l & 4) * 2, OFF_CA + (sel0 >> 7) * 2048 + G * 64);                                         \
    l8(cx1, rs, (l & 3) * 16 + (l & 4) * 2, OFF_CB + (sel0 >> 7) * 2048 + G * 64);                                          \
  }
            if constexpr (s == FS1 && n == 4) {
              asm volatile("" : "=v"(cxn), "=v"(cx1));
              if (!ffn_on && w < 4) RED_LOAD(ld16u, ld8h);
            }
            if constexpr (s == FS1S && n == 0) {
              if (!ffn_on && w < 4) {
                wait_vm<SINCE(FS1)>();
                PINV4(gv); PINV4(uv);
                asm volatile("" : "+v"(cxn), "+v"(cx1)::"memory");
                for (unsigned spins = 0; !__all(max(max(max(umax4(gv[0]), umax4(gv[1])), max(umax4(gv[2]), umax4(gv[3]))),
                                                    max(max(umax4(uv[0]), umax4(uv[1])), max(max(umax4(uv[2]), max(cxn[0], cxn[1])), max(cx1[0], cx1[1])))) != POISON) && !spin_fail(spins, rs, T);) {
                  RED_LOAD(ld16u, ld8h);
                  DRAIN();
                  PINV4(gv); PINV4(uv);
                  asm volatile("" : "+v"(cxn), "+v"(cx1)::"memory");
                }
#define F4(x) __builtin_bit_cast(f32x4, x)
                ((LDS_AS f32x4*)(smem + L_SFIN))[tid] = ((F4(gv[0]) + F4(gv[1])) + (F4(gv[2]) + F4(gv[3]))) + ((F4(uv[0]) + F4(uv[1])) + F4(uv[2]));
#undef F4
                count_in(2);
                if (w == 0) {
                  while (__hip_atomic_load(qcnt + 2, __ATOMIC_RELAXED, __HIP_MEMORY_SCOPE_WORKGROUP) < 4u) {}
                  asm volatile("" ::: "memory");
                  const u32x2 ov = red_final<4>(smem, l, fkey, cxn, cx1);
                  if (l < 8) st8(unpoison2(ov), rs, OFF_XNEW + (bi * 8 + (l & 3) * 2 + (l >> 2)) * 8, 0);
                }
              }
            }
            if constexpr (s == FXA && n == 4) { DEF4(fx); if (w < 3) row_load(rs, g, XP_OFF(), fx); }
            if constexpr (s == FXB && n == 0) {
              if (w < 3) {
                const unsigned fa = bo[0][0] + SLOT(FXT) * STAGE + w * 2048;
                u32x4 f[8];
#pragma unroll
                for (int i = 0; i < 8; ++i) f[i] = *(const LDS_AS u32x4*)(size_t)(fa + (i >> 1) * 6144 + (i & 1) * 1024);
                wait_vm<SINCE(FXA)>();
                PINV4(fx);
                ffn_xwait<XSL>(rs, T, XP_OFF(), l, fx);
                f32x4 y0 = mfma16(f[0], fx[0], f32x4{0.f, 0.f, 0.f, 0.f}), y1 = mfma16(f[1], fx[0], f32x4{0.f, 0.f, 0.f, 0.f});
#pragma unroll
                for (int i = 1; i < 4; ++i) { y0 = mfma16(f[2 * i], fx[i], y0); y1 = mfma16(f[2 * i + 1], fx[i], y1); }
                if ((l & 14) == 0) st16(unpoison4(__builtin_bit_cast(u32x4, (l & 1) ? y1 : y0)), rs, g * 16 + (l & 1) * 512, OFF_FX + ((h * 6 + 2 * w) * 8 + p) * 64);
              }
            }
            if constexpr (s == FXL && n == 4) {
              asm volatile("" : "=v"(fx[0]), "=v"(fx[1]));
              if (aff && w < 4) { ld16h<0>(fx[0], rs, l * 16, FP_OFF(0)); ld16h<0>(fx[1], rs, l * 16, FP_OFF(1)); }
            }
            if constexpr (s == FXS && n == 2) {
              if (aff && w < 4) {
                wait_vm<0>();
                asm volatile("" : "+v"(fx[0]), "+v"(fx[1])::"memory");
                if (!FX_OK(2)) {
                  unsigned spins = 0;
                  do {
                    if (spin_fail(spins, rs, T)) break;
                    fx[0] = ld16(rs, l * 16, FP_OFF(0)); fx[1] = ld16(rs, l * 16, FP_OFF(1));
                  } while (!FX_OK(2));
                }
                if (w == 3) {
                  f32x2 nx[8];
                  unpack8v(fx[0], nx); unpack8v(fx[1], nx + 4);
                  const f32x2 sm = ((nx[0] + nx[1]) + (nx[2] + nx[3])) + ((nx[4] + nx[5]) + (nx[6] + nx[7]));
                  const float mean = sum64(hadd(sm)) * (1.0f / 1024.0f);
                  const f32x2 m2 = {mean, mean};
                  f32x2 q = {0.f, 0.f};
#pragma unroll
                  for (int j = 0; j < 8; ++j) { const f32x2 d = nx[j] - m2; q = __builtin_elementwise_fma(d, d, q); }
                  const float rd = ln_rd(sum64(hadd(q)) * (1.0f / 1024.0f));
                  if (l == 0) *(LDS_AS f32x2*)(smem + L_FXL + 384) = f32x2{rd, -rbf1(mean)};
                } else {
                  tile_sum(fx[0], L_FXL);
                }
                count_in(3);
              }
            }
          }
        };
        static_for<32>([&](auto sc_) __attribute__((always_inline)) {
          constexpr int s = decltype(sc_)::value;
          if constexpr (s > 0) PIN_ACC();
          if constexpr (IT == 1) wait_vm<vm_at(IT, s, 0) - vm_a(IT, s)>();
          pin_w<s % WA>(af[s % WA][0], af[s % WA][1]);
          SB();
          static_for<6>([&](auto nc_) __attribute__((always_inline)) {
            constexpr int n = decltype(nc_)::value;
            if constexpr (s == 0) { acc[0][n] = f32x4{0.f, 0.f, 0.f, 0.f}; acc[1][n] = acc[0][n]; }
            constexpr bool on = s < QL || n >= 2;
            if constexpr (on) acc[0][n] = n < 4 ? mfma16(bfr[n].v, af[s % WA][0], acc[0][n]) : mfma16(af[s % WA][0], bfr[n].v, acc[0][n]);
            SB();
            if constexpr (n == 0) { QKV_RDB(s, 5); }
            else if constexpr (s + 1 < 32 && (s + 1 < QL || n > 2)) QKV_RDB(s + 1, n - 1);
            SB();
            if constexpr (on) acc[1][n] = n < 4 ? mfma16(bfr[n].v, af[s % WA][1], acc[1][n]) : mfma16(af[s % WA][1], bfr[n].v, acc[1][n]);
            SB();
            if constexpr (s + 1 < 32 && (s + 1 < QL || n >= 2)) QKV_RDA(s + 1, n);
            SB();
            gap(sc_, nc_);
            SB();
          });
        });
        PIN_ACC();
        frag_t qw[32 - QL][2];
        static_for<32 - QL>([&](auto c_) __attribute__((always_inline)) {
          constexpr int s = QL + decltype(c_)::value;
          QKV_RDA(s, 0); QKV_RDB(s, 0); QKV_RDA(s, 1); QKV_RDB(s, 1);
          qw[s - QL][0] = bfr[0]; qw[s - QL][1] = bfr[1];
        });
        SB();
        const bool own = aff && (int)((sel0 >> 5) & 7) == rt;
        const bool m0 = !(sel0 & 16), mq = (l & 15) == (int)(sel0 & 15), mv = g == (int)((sel0 & 15) >> 2);
        auto patch_qk = [&](int n) __attribute__((always_inline)) {
          f32x4 t = *(const LDS_AS f32x4*)(smem + L_FXL + n * 64 + g * 16);
          const f32x4 c = *(const LDS_AS f32x4*)(smem + L_CSL + n * 64 + g * 16);
          const float nb = *(const LDS_AS float*)(smem + L_FXL + 388);
#pragma unroll
          for (int e = 0; e < 4; ++e) { t[e] = __builtin_fmaf(c[e], nb, t[e]); asm volatile("" : "+v"(t[e])); }
          acc[0][n] = (mq && m0) ? t : acc[0][n];
          acc[1][n] = (mq && !m0) ? t : acc[1][n];
        };
        if (own) {
          while (__hip_atomic_load(qcnt + 3, __ATOMIC_RELAXED, __HIP_MEMORY_SCOPE_WORKGROUP) < 4u) {}
          asm volatile("" ::: "memory");
          patch_qk(2); patch_qk(3);
          const f32x2 nab = *(const LDS_AS f32x2*)(smem + L_FXL + 384);
          const LDS_AS float* fl_ = (const LDS_AS float*)(smem + L_FXL + 256) + (l & 15);
          const LDS_AS float* cl_ = (const LDS_AS float*)(smem + L_CSL + 256) + (l & 15);
          const float t4 = __builtin_fmaf(cl_[0], nab[1], fl_[0]), t5 = __builtin_fmaf(cl_[16], nab[1], fl_[16]);
          const unsigned na = __builtin_bit_cast(unsigned, (float)nab[0]);
#pragma unroll
          for (int a = 0; a < 2; ++a) {
            const bool ma = a ? !m0 : m0;
            aq[a] = (mq && ma) ? na : aq[a];
#pragma unroll
            for (int e = 0; e < 4; ++e) {
              const bool me = mv && ma && e == (int)(sel0 & 3);
              acc[a][4][e] = me ? t4 : acc[a][4][e]; acc[a][5][e] = me ? t5 : acc[a][5][e];
              av[a][e] = me ? na : av[a][e];
            }
          }
        }
        constexpr float C2 = 1.4426950408889634f * 0.17677669529663687f;
#pragma unroll
        for (int a = 0; a < 2; ++a) {
          f32x2 a2 = {asf(aq[a]), asf(aq[a])};
          qs2[a] = f32x2{asf(aq[a]) * C2, asf(aq[a]) * C2};
          asm volatile("" : "+v"(a2), "+v"(qs2[a]));
          acc[a][2] *= f32x4{a2[0], a2[1], a2[0], a2[1]}; acc[a][3] *= f32x4{a2[0], a2[1], a2[0], a2[1]};
          acc[a][4] *= __builtin_bit_cast(f32x4, av[a]); acc[a][5] *= __builtin_bit_cast(f32x4, av[a]);
        }
        k0 = pack8(acc[0][2], acc[0][3]); k1 = pack8(acc[1][2], acc[1][3]); v0 = pack8(acc[0][4], acc[1][4]); v1 = pack8(acc[0][5], acc[1][5]);
        const unsigned kvo = OFF_KV + IT * KV_STEP + (p * 32 + h) * 32768 + rt * 4096;
        if (p < 7) { st16(k0, rs, l * 16, kvo); st16(k1, rs, l * 16 + 1024, kvo); st16(v0, rs, l * 16 + 2048, kvo); st16(v1, rs, l * 16 + 3072, kvo); }
        char* ownp = smem + (8 + rt) * 2048 + l * 16;
        *(u32x4*)(ownp + L_K) = k0;
        if (rt < 7) *(u32x4*)(ownp + L_K + 1024) = k1;
        *(u32x4*)(ownp + L_V) = v0; *(u32x4*)(ownp + L_V + 1024) = v1;
        SB();
        static_for<32 - QL>([&](auto c_) __attribute__((always_inline)) {
          constexpr int s = QL + decltype(c_)::value;
#pragma unroll
          for (int n = 0; n < 2; ++n) { acc[0][n] = mfma16(qw[s - QL][n].v, af[s % WA][0], acc[0][n]); acc[1][n] = mfma16(qw[s - QL][n].v, af[s % WA][1], acc[1][n]); }
        });
        if (own) { patch_qk(0); patch_qk(1); }
      };
      qkv_loop(it_c);
    }
#undef A_LOAD1
#undef QKV_RDA
#undef QKV_RDB
#undef QKV_BO
    {
      IDS();
      const GAS bf16 *pwo, *pwg, *pwu;
      asm volatile("s_load_dwordx2 %0, %3, 24\n\ts_load_dwordx2 %1, %3, 40\n\ts_load_dwordx2 %2, %3, 48" : "=&s"(pwo), "=&s"(pwg), "=&s"(pwu) : "s"(ka));
      const int ql = l & 15;
      const f32x4 z4 = {0.f, 0.f, 0.f, 0.f}, ninf = {-INFINITY, -INFINITY, -INFINITY, -INFINITY};
      LDS_AS unsigned* rdy = (LDS_AS unsigned*)(smem + L_RDY);
      const unsigned rtag = T[0] + it;
      u32x4 qf[2];
      f32x4 cn[2], oa[2][2], sum[2], S[2][2];
      unsigned one2 = 0x3F803F80u;
      asm volatile("" : "+v"(one2));
      const u32x4 ones = {one2, one2, one2, one2};
      auto ex8 = [&](f32x4 a, f32x4 b) __attribute__((always_inline)) {
        return u32x4{pack2(__builtin_amdgcn_exp2f(a[0]), __builtin_amdgcn_exp2f(a[1])), pack2(__builtin_amdgcn_exp2f(a[2]), __builtin_amdgcn_exp2f(a[3])),
                     pack2(__builtin_amdgcn_exp2f(b[0]), __builtin_amdgcn_exp2f(b[1])), pack2(__builtin_amdgcn_exp2f(b[2]), __builtin_amdgcn_exp2f(b[3]))};
      };
      {
#pragma unroll
        for (int a = 0; a < 2; ++a) {
          const f32x4 c4 = {qs2[a][0], qs2[a][1], qs2[a][0], qs2[a][1]};
          qf[a] = pack8(acc[a][0] * c4, acc[a][1] * c4);
        }
        f32x4 mk;
#pragma unroll
        for (int e = 0; e < 4; ++e) mk[e] = (4 * g + e <= ql) ? 0.f : -INFINITY;
        const f32x4 d0 = mfma16(k0, qf[0], mk), d1 = mfma16(k1, qf[1], mk), d2 = mfma16(k0, qf[1], z4);
        const float m0 = -max_x32(max_x16(fmaxf(fmaxf(d0[0], d0[1]), fmaxf(d0[2], d0[3]))));
        const float m1 = -max_x32(max_x16(fmaxf(fmaxf(d1[0], d1[1]), fmaxf(d1[2], d1[3]))));
        cn[0] = f32x4{m0, m0, m0, m0}; cn[1] = f32x4{m1, m1, m1, m1};
        asm volatile("" : "+v"(cn[0]), "+v"(cn[1]));
        const f32x4 e0 = d0 + cn[0];
        const u32x4 p0 = {pack2(__builtin_amdgcn_exp2f(e0[0]), __builtin_amdgcn_exp2f(e0[1])), pack2(__builtin_amdgcn_exp2f(e0[2]), __builtin_amdgcn_exp2f(e0[3])), 0u, 0u};
        const u32x4 p1 = ex8(d2 + cn[1], d1 + cn[1]);
        oa[0][0] = mfma16(v0, p0, z4); oa[0][1] = mfma16(v1, p0, z4); sum[0] = mfma16(ones, p0, z4);
        oa[1][0] = mfma16(v0, p1, z4); oa[1][1] = mfma16(v1, p1, z4); sum[1] = mfma16(ones, p1, z4);
      }
      const char* kb_ = smem + L_K + l * 16;
      const char* vb_ = smem + L_V + l * 16;
      const unsigned wov = (l >> 3) * 2048 + (((((l & 7) >> 1) ^ ((l >> 4) & 3)) * 2 + (l & 1)) << 4), fnv = (l >> 1) * (F_ * 2) + (l & 1) * 16;
      const unsigned pp = __builtin_amdgcn_readfirstlane(p > 0 ? 32 : 0), pvo = OFF_KV + it * KV_STEP + (p * 32 + h - pp) * 32768;
      const unsigned pfo = __builtin_amdgcn_readfirstlane(F_KV + KVF(it, p * 32 + h - pp));
      asm volatile("" : "+v"(oa[0][0]), "+v"(oa[0][1]), "+v"(oa[1][0]), "+v"(oa[1][1]), "+v"(sum[0]), "+v"(sum[1]));
      DRAIN();
      asm volatile("" : "+v"(tdum));
      if (l == 0) st8(T, rs, rt * SV, F_KV + KVF(it, p * 32 + h));
      BAR();
      asm volatile("" : "+s"(pwo), "+s"(pwg), "+s"(pwu));
      if (!LOP || w >= 4) { WO_PIECE(15); WO_PIECE(14); }
#define LDS16(a_) (*(const LDS_AS u32x4*)(size_t)(a_))
      u32x4 kf[2], vf[2];
      int nw = 0;
      u32x4 p0, p1;
      float e0, e1;
#define EE(v, i) { e0 = __builtin_amdgcn_exp2f(v[i]); e1 = __builtin_amdgcn_exp2f(v[i + 1]); pin2s(e0, e1); SB(); }
#define CV(P, i) { unsigned t_ = pack2(e0, e1); pin1(t_); P[i] = t_; SB(); }
      auto body = [&](unsigned ad, auto dk_) __attribute__((always_inline)) {
        constexpr int dk = decltype(dk_)::value;
        const f32x4 a0 = S[1][0], a1 = S[1][1];
        sum[0] = mfma16(ones, p0, sum[0]); SB();
        EE(a0, 0);
        lds_wait<2>(kf[0], kf[1]);
        S[0][0] = mfma16(kf[0], qf[0], cn[0]); SB();
        CV(p1, 0); EE(a0, 2);
        S[0][1] = mfma16(kf[1], qf[0], cn[0]); SB();
        CV(p1, 1); EE(a1, 0);
        lds_wait<0>(vf[0], vf[1]);
        oa[0][0] = mfma16(vf[0], p0, oa[0][0]); SB();
        CV(p1, 2); EE(a1, 2);
        oa[0][1] = mfma16(vf[1], p0, oa[0][1]); SB();
        CV(p1, 3);
        S[1][0] = mfma16(kf[0], qf[1], cn[1]); SB();
        EE(S[0][0], 0);
        S[1][1] = mfma16(kf[1], qf[1], cn[1]); SB();
        lds2<2048 + dk>(kf[0], kf[1], ad); SB();
        CV(p0, 0); EE(S[0][0], 2);
        oa[1][0] = mfma16(vf[0], p1, oa[1][0]); SB();
        CV(p0, 1); EE(S[0][1], 0);
        oa[1][1] = mfma16(vf[1], p1, oa[1][1]); SB();
        lds2<2048 + L_V>(vf[0], vf[1], ad); SB();
        CV(p0, 2); EE(S[0][1], 2);
        sum[1] = mfma16(ones, p1, sum[1]); SB();
        CV(p0, 3);
      };
      auto last = [&]() __attribute__((always_inline)) {
        sum[0] = mfma16(ones, p0, sum[0]); SB();
        EE(S[1][0], 0);
        lds_wait<0>(vf[0], vf[1]);
        pin2(kf[0], kf[1]);
        oa[0][0] = mfma16(vf[0], p0, oa[0][0]); SB();
        CV(p1, 0); EE(S[1][0], 2);
        oa[0][1] = mfma16(vf[1], p0, oa[0][1]); SB();
        CV(p1, 1); EE(S[1][1], 0);
        const float f0 = __builtin_amdgcn_exp2f(S[1][1][2]), f1 = __builtin_amdgcn_exp2f(S[1][1][3]);
        p1[2] = pack2(e0, e1); p1[3] = pack2(f0, f1);
        oa[1][0] = mfma16(vf[0], p1, oa[1][0]); oa[1][1] = mfma16(vf[1], p1, oa[1][1]); sum[1] = mfma16(ones, p1, sum[1]);
      };
      auto piece = [&]() __attribute__((always_inline)) {
        if constexpr (it == 1) { WO_PIECE(13 - nw); }
        else if (ffn_on) { FFN_DMA(nw); }
        ++nw;
      };
      if (rt > 0) {
        unsigned ad = lbase + l * 16 + (6 + rt) * 2048;
        lds2<2048>(kf[0], kf[1], ad); lds2<2048 + L_V>(vf[0], vf[1], ad);
        lds_wait<2>(kf[0], kf[1]);
        S[0][0] = mfma16(kf[0], qf[0], cn[0]); S[0][1] = mfma16(kf[1], qf[0], cn[0]); S[1][0] = mfma16(kf[0], qf[1], cn[1]); S[1][1] = mfma16(kf[1], qf[1], cn[1]);
        SB();
        lds_wait<0>(vf[0], vf[1]);
        lds2<0>(kf[0], kf[1], ad);
        p0 = ex8(S[0][0], S[0][1]);
        lds_wait<0>(kf[0], kf[1]);
#pragma unroll 1
        for (int i = rt - 1; i > 0; --i) {
          ad -= 2048;
          body(ad, std::integral_constant<int, -2048>{});
          piece();
          SB();
        }
        piece();
        last();
      }
      {
        constexpr int FW = it ? FW1 : FW0, FN = 8 / FW;
        if (w < FW) {
          const int g0 = FN * rt;
          if (KVS && w == 0) __builtin_amdgcn_s_sleep(KVS);
          poll_flags(rs, (l & (FN - 1)) * SV, pfo + g0 * SV, T);
#pragma unroll
          for (int j = 0; j < FN; ++j) {
            const unsigned d = lbase + (g0 + j) * 2048, o = pvo + (g0 + j) * 4096;
            dma16s<0>(rs, l * 16, o, d + L_K); dma16s<1024>(rs, l * 16, o, d + L_K);
            dma16s<0>(rs, l * 16, o + 2048, d + L_V); dma16s<1024>(rs, l * 16, o + 2048, d + L_V);
          }
        }
        if (p == 0) { cn[0] = ninf; cn[1] = ninf; }
        f32x4 mk;
#pragma unroll
        for (int e = 0; e < 4; ++e) mk[e] = (4 * g + e > ql) ? 0.f : -INFINITY;
        const f32x4 cm0 = cn[0] + mk, cm1 = cn[1] + mk;
        if (w < FW) {
          if constexpr (FN > 1) { wait_vm<2 * FN>(); if (l < FN / 2) rdy[FN * rt + l] = rtag; }
          DRAIN();
          if (l < FN) rdy[FN * rt + l] = rtag;
        }
        if (LOP && w < 4) { WO_PIECE(15); WO_PIECE(14); }
        if constexpr (it == 1) { WO_PIECE(6); WO_PIECE(5); WO_PIECE(4); }
        else { WO_PIECE(13); WO_PIECE(12); if (ffn_on) FFN_DMA(7); }

        int have = rt - 1;
        auto need = [&](int gq) __attribute__((always_inline)) {
          if (__builtin_expect(gq > have && gq < 8, 0)) {
            do {
              const unsigned t = __hip_atomic_load(rdy + (l & 7), __ATOMIC_RELAXED, __HIP_MEMORY_SCOPE_WORKGROUP);
              have = rt - 1 + __builtin_ctz(~((unsigned)__builtin_amdgcn_ballot_w64(t == rtag) & 0xffu) >> rt);
            } while (gq > have);
            asm volatile("" ::: "memory");
          }
        };
        unsigned ad = lbase + l * 16 + rt * 2048;
        need(rt);
        lds2<0>(kf[0], kf[1], ad); lds2<L_V>(vf[0], vf[1], ad);
        lds_wait<2>(kf[0], kf[1]);
        S[0][0] = mfma16(kf[0], qf[0], cm0); S[0][1] = mfma16(kf[1], qf[0], cn[0]); S[1][0] = ninf; S[1][1] = mfma16(kf[1], qf[1], cm1);
        SB();
        lds_wait<0>(vf[0], vf[1]);
        need(rt + 1);
        lds2<2048>(kf[0], kf[1], ad);
        p0 = ex8(S[0][0], S[0][1]);
        lds_wait<0>(kf[0], kf[1]);
#pragma unroll 1
        for (int gq = rt + 1; gq < 8; ++gq) {
          need(gq + 1);
          body(ad, std::integral_constant<int, 2048>{});
          piece();
          SB();
          ad += 2048;
        }
        last();
      }
      {
        float big = 0.f;
#pragma unroll
        for (int a = 0; a < 2; ++a)
#pragma unroll
          for (int d = 0; d < 2; ++d)
            big = __builtin_elementwise_maximum(__builtin_elementwise_maximum(big, __builtin_elementwise_maximum(fabsf(oa[a][d][0]), fabsf(oa[a][d][1]))),
                                                __builtin_elementwise_maximum(fabsf(oa[a][d][2]), fabsf(oa[a][d][3])));
        bool ok = sum[0][0] >= 0.5f && sum[0][0] < 0x1p64f && sum[1][0] >= 0.5f && sum[1][0] < 0x1p64f && big < INFINITY;
        if (!__all(ok)) {
#pragma unroll
          for (int a = 0; a < 2; ++a) {
            const int qs = 2 * rt + a, qq = 256 + 16 * qs + ql, klo = max(qq - 255, p > 0 ? 0 : 256);
            auto score = [&](int t) __attribute__((always_inline)) {
              f32x4 s = mfma16(t == 31 ? k1 : *(const u32x4*)(kb_ + t * 1024), qf[a], z4);
#pragma unroll
              for (int e = 0; e < 4; ++e) { const int kk = 16 * t + 4 * g + e; s[e] = (kk <= qq && kk >= klo) ? s[e] : -INFINITY; }
              return s;
            };
            float mx = -INFINITY;
#pragma unroll 1
            for (int t = qs; t <= qs + 16; ++t) { const f32x4 s = score(t); mx = fmaxf(fmaxf(mx, fmaxf(s[0], s[1])), fmaxf(s[2], s[3])); }
            mx = max_x32(max_x16(mx));
            oa[a][0] = z4; oa[a][1] = z4; sum[a] = z4;
#pragma unroll 1
            for (int G = qs >> 1; G <= (qs + 16) >> 1; ++G) {
              const f32x4 s0 = score(2 * G), s1 = score(2 * G + 1);
              f32x4 e0, e1;
#pragma unroll
              for (int e = 0; e < 4; ++e) { e0[e] = __builtin_amdgcn_exp2f(s0[e] - mx); e1[e] = __builtin_amdgcn_exp2f(s1[e] - mx); }
              const u32x4 pf = pack8(e0, e1);
              oa[a][0] = mfma16(*(const u32x4*)(vb_ + G * 2048), pf, oa[a][0]);
              oa[a][1] = mfma16(*(const u32x4*)(vb_ + G * 2048 + 1024), pf, oa[a][1]);
              sum[a] = mfma16(ones, pf, sum[a]);
            }
          }
        }
      }
#pragma unroll
      for (int a = 0; a < 2; ++a) {
        const float inv = __builtin_amdgcn_rcpf(sum[a][0]);
        oa[a][0] *= inv; oa[a][1] *= inv;
      }
      const unsigned oo = OFF_O + it * O_STEP + ((p * 8 + rt) * 64 + h * 2) * 1024;
      st16(pack8(oa[0][0], oa[0][1]), rs, l * 16, oo);
      st16(pack8(oa[1][0], oa[1][1]), rs, l * 16 + 1024, oo);
      DRAIN();
      BAR();
      if (tid == 0) st8(T, rs, OFL(h), F_O + OFB(it, p));
      if constexpr (it == 0) {
        DEF4(gv); DEF4(uv);
        if (ffn_on) {
          const unsigned ro = 65536 + w * 1024 + (4 * g + ((l & 15) >> 2)) * 32 + (l & 3) * 8;
#pragma unroll
          for (int i = 0; i < 4; ++i) {
            frag_t f;
            f.hf.a = trread(smem, ro + i * 16384); f.hf.b = trread(smem, ro + i * 16384 + 512);
            gv[i] = f.v;
            f.hf.a = trread(smem, ro + i * 16384 + 8192); f.hf.b = trread(smem, ro + i * 16384 + 8704);
            uv[i] = f.v;
          }
        }
        PINV4(gv); PINV4(uv);
        {
          ARGP(px, 0);
          const unsigned xo = ((rb * 4 + (w & 3)) * 32 + (w >> 2) * 16) * 2048 + ct * 128, xw = (l >> 3) * 2048 + (((l & 7) ^ g) << 4);
          dma16(RSRC(px, 2048 * 1024 * 2), xw, xo, lbase + w * 4096); dma16(RSRC(px, 2048 * 1024 * 2), xw ^ 64, xo + 16384, lbase + w * 4096 + 1024);
        }
      }
      WO_PIECE(3); WO_PIECE(2); WO_PIECE(1); WO_PIECE(0);
      if constexpr (it == 0) { WO_PIECE(11); WO_PIECE(10); WO_PIECE(9); WO_PIECE(8); WO_PIECE(7); WO_PIECE(6); WO_PIECE(5); WO_PIECE(4); }
    }
    IDS();
    ARGP(pwr, 4); ARGP(pwd, 7); ARGP(pout, 8);
    const int kh = w >> 2, rt2 = rb * 4 + (w & 3);
    const unsigned R = rt2 * 32 + kh * 16 + (l & 15);
    const unsigned to = (rt2 * 64 + ct * 4 + kh) * 1024 + l * 16;
    const unsigned rmo = ct * 64 + ((g & 1) ? 12 + 4 * g : 4 * g);
    f32x2 v[8], wv[8];
    float wsum;
    unsigned ownk;
    {
      u32x4 wa[2], wrp[2], res[2];
#pragma unroll
      for (int j = 0; j < 2; ++j) {
        wrp[j] = *(const GAS u32x4*)(pwr + rmo + 32 * j);
        wa[j] = *(const GAS u32x4*)(pwr + l * 8 + j * 512);
      }
      {
        f32x2 t[8];
        unpack8v(wa[0], t); unpack8v(wa[1], t + 4);
        const f32x2 s = ((t[0] + t[1]) + (t[2] + t[3])) + ((t[4] + t[5]) + (t[6] + t[7]));
        wsum = sum64(hadd(s));
        unpack8v(trade16(wrp[0]), wv); unpack8v(trade16(wrp[1]), wv + 4);
        asm volatile("" : "+v"(wsum), "+v"(wv[0]), "+v"(wv[1]), "+v"(wv[2]), "+v"(wv[3]), "+v"(wv[4]), "+v"(wv[5]), "+v"(wv[6]), "+v"(wv[7]));
      }
      unsigned bt[4];
#pragma unroll
      for (int n = 0; n < 4; ++n) {
        bt[n] = lbase + 32768 + 65536 + kh * 4096 + (4 * g + ((l & 15) >> 2)) * 128 + (l & 3) * 8 + ((n ^ (2 * (g & 1) + ((l & 15) >> 3))) << 5);
        asm volatile("" : "+v"(bt[n]));
      }
      if (w == 0) { if (it ? OSL1 : OSL0) __builtin_amdgcn_s_sleep(it ? OSL1 : OSL0); poll_flags<OPS>(rs, OFL(l & 31), F_O + OFB(it, rb >> 1), T); }
      else if (it == 0 && w < 3) poll_flags2(rs, F_XN + (((w - 1) * 4 + g) * RX + bi % RX) * 256 + (l & 15) * 16, T);
      else if (it == 0 && w == 3) poll_flags(rs, ((l >> 3) * 32 + h) * cmax(8 * SV, 128) + (l & 7) * SV, F_KV, T);
      asm volatile("s_barrier" ::: "memory");
      const unsigned abase = OFF_O + it * O_STEP + (rt2 * 64 + 32 * kh) * 1024;
      const unsigned av0 = l * 16 + kh * 1024, av1 = l * 16 + (kh ^ 1) * 1024;
      u32x4 af[WB][2];
#define O_LOAD(s_) { ld16h<(wo_k(it, s_) & 1) * 2048>(af[(s_) % WB][0], rs, av0, abase + (wo_k(it, s_) >> 1) * 4096); ld16h<(wo_k(it, s_) & 1) * 2048>(af[(s_) % WB][1], rs, av1, abase + (wo_k(it, s_) >> 1) * 4096); }
      static_for<WR>([&](auto c_) __attribute__((always_inline)) { O_LOAD(decltype(c_)::value); });
      if constexpr (it == 1) { ld16h<0>(res[0], rs, g * 16, OFF_XNEW + ct * 128); ld16h<64>(res[1], rs, g * 16, OFF_XNEW + ct * 128); }
      SB();
      f32x4 ac[2][4];
#pragma unroll
      for (int a = 0; a < 2; ++a)
#pragma unroll
        for (int n = 0; n < 4; ++n) ac[a][n] = f32x4{0.f, 0.f, 0.f, 0.f};
      u32x4 bfr[2][4];
#define WO_RD(buf, s, n)                                                                             \
  {                                                                                                  \
    const unsigned o_ = bt[n] + (wo_k(it, s) & 7) * 8192;                                             \
    frag_t f_;                                                                                       \
    f_.hf.a = trabs(o_); f_.hf.b = trabs(o_ + 2048);                                                 \
    bfr[buf][n] = f_.v;                                                                              \
  }
#pragma unroll
      for (int n = 0; n < 4; ++n) WO_RD(0, 0, n);
      static_for<16>([&](auto sc_) __attribute__((always_inline)) {
        constexpr int s = decltype(sc_)::value, cur = s & 1;
        pin_ac(ac);
        if constexpr (s == 1) lds_count<0>(lbase + L_QC);
        if constexpr (s == (it == 0 ? 3 : 11)) lds_look(smem + L_QC, 64);
        if constexpr (s < 15 && ((wo_k(it, s) ^ wo_k(it, s + 1)) & 8)) {
#pragma unroll
          for (int n = 0; n < 4; ++n) { bt[n] += (wo_k(it, s) & 8) ? -65536 : 65536; pin1(bt[n]); }
        }
        if constexpr (s < WB - WR) O_LOAD(WR + 2 * s);
        if constexpr (it == 0 && s == 8) lds_count<8>(lbase + L_QC);
        if constexpr (it == 0 && s == WDS) {
          DRAIN();
          if ((w >> 2) == WDW && ffn_on) { lds_look(smem + L_QC + 8, 16); WD_DMA_OF(w); WD_DMA_OF(w ^ 4); }
        }
        if constexpr (it == 1 || s < WDS) wait_vm<wo_at(it, s) - wo_pos(it, s)>();
        pin2(af[s % WB][0], af[s % WB][1]);
        if constexpr (it == 1 && s == 9) pin2(res[0], res[1]);
        SB();
#pragma unroll
        for (int n = 0; n < 4; ++n) {
          ac[0][n] = mfma16(bfr[cur][n], af[s % WB][0], ac[0][n]);
          ac[1][n] = mfma16(bfr[cur][n], af[s % WB][1], ac[1][n]);
          if (s + 1 < 16) WO_RD(cur ^ 1, s + 1, n);
          if (it == 0 && s == 8) {
            const u32x2 t = *(const LDS_AS u32x2*)(smem + w * 4096 + (l & 15) * 128 + (((2 * n + (g >> 1)) ^ ((l & 15) >> 1)) << 4) + (g & 1) * 8);
            res[n >> 1][2 * (n & 1)] = t[0]; res[n >> 1][2 * (n & 1) + 1] = t[1];
          }
          if (s == 9) {
            if (it == 1 && n == 0) { res[0] = R == sel0 ? res[0] : x2p[0]; res[1] = R == sel0 ? res[1] : x2p[1]; }

            v[2 * n] = f32x2{lo16(res[n >> 1][2 * (n & 1)]), hi16(res[n >> 1][2 * (n & 1)])};
            v[2 * n + 1] = f32x2{lo16(res[n >> 1][2 * (n & 1) + 1]), hi16(res[n >> 1][2 * (n & 1) + 1])};
            pin2f(v[2 * n], v[2 * n + 1]);
          }
          SB();
        }
        if constexpr (s < WB - WR) { O_LOAD(WR + 2 * s + 1); }
        else if constexpr (s + WB < 16) { O_LOAD(s + WB); }
        SB();
      });
#undef WO_RD
#undef O_LOAD
#define XCH_AT(v) ((v) == 7 ? 155648 : (v) * 4096)
#pragma unroll
      for (int n = 0; n < 4; ++n) *(f32x4*)(smem + XCH_AT(w) + n * 1024 + l * 16) = ac[1][n];
#pragma unroll
      for (int n = 0; n < 4; ++n) { v[2 * n] += f32x2{ac[0][n][0], ac[0][n][1]}; v[2 * n + 1] += f32x2{ac[0][n][2], ac[0][n][3]}; }
      BAR();
#pragma unroll
      for (int n = 0; n < 4; ++n) {
        const f32x4 o = *(const f32x4*)(smem + XCH_AT(w ^ 4) + n * 1024 + l * 16);
        v[2 * n] += f32x2{o[0], o[1]}; v[2 * n + 1] += f32x2{o[2], o[3]};
      }
    }
    {
      if (it == 0 && X1F) { st16(pack8v(v), rs, to, OFF_XN + (4u << 20)); st16(pack8v(v + 4), rs, to + 2048, OFF_XN + (4u << 20)); }
      SB();
      const f32x2 s = ((v[0] + v[1]) + (v[2] + v[3])) + ((v[4] + v[5]) + (v[6] + v[7]));
      f32x2 dp = v[0] * wv[0], q = v[0] * v[0];
#pragma unroll
      for (int j = 1; j < 8; ++j) { dp = __builtin_elementwise_fma(v[j], wv[j], dp); q = __builtin_elementwise_fma(v[j], v[j], q); }
      float mt = hadd(s), qq = hadd(q), dot = hadd(dp);
      add_g3(mt, qq, dot);
      const unsigned so = OFF_STAT + it * 524288 + rb * 32768, lrow = R & 127;
      if (g == 0) st16(unpoison4(__builtin_bit_cast(u32x4, f32x4{mt, qq, dot, 0.f})), rs, ct * 2048 + lrow * 16, so);
      SB();
      if (it == 0 && !X1F) { st16(pack8v(v), rs, to, OFF_XN + (4u << 20)); st16(pack8v(v + 4), rs, to + 2048, OFF_XN + (4u << 20)); }
      SB();
      if (it == 0) wait_vm<X1F>();
      if constexpr (it == 0) {
        volatile LDS_AS u32x2* in = (volatile LDS_AS u32x2*)(smem + L_XC);
        if (l == 0) in[w] = T;
        const u32x2 t = in[l & 7];
        if (__all(t[0] == T[0] && t[1] == T[1]) && l < RX) st8(T, rs, (l * 32 + (rb & 1) * 16 + ct) * SX, F_X1 + (rb >> 1) * RX * 32 * SX);
      }
      if (it == 1) { if (ffn_on) { WD_DMA_OF(w); } }
      if (it == 0) {
#pragma unroll
        for (int j = 0; j < 9; ++j) *(u32x4*)(smem + L_RING + (j / 3) * STAGE + (w * 3 + j % 3) * 1024 + l * 16) = keep[j];
      }
      __builtin_amdgcn_s_sleep(it == 0 ? SDI0 : SDIR);
      char* const skey = smem + (it == 0 ? L_SKEY : L_XC);
      u32x4 sp[4];
      ld16h<0>(sp[0], rs, g * 8192 + lrow * 16, so); ld16h<2048>(sp[1], rs, g * 8192 + lrow * 16, so);
      ld16h<0>(sp[2], rs, g * 8192 + lrow * 16, so + 4096); ld16h<2048>(sp[3], rs, g * 8192 + lrow * 16, so + 4096);
      u32x2 xf;
      unsigned k_;
      asm volatile("" : "=v"(xf), "=v"(k_));
      if (it == 0 && w == 0) { ld8h(xf, rs, (l & 31) * SX, F_X1 + (p * RX + (xcd >> 1) % RX) * 32 * SX); ld4h<0>(k_, rs, (l & 15) * SK, OFF_KEY + (bid & 15) * cmax(16 * SK, 128)); }
      DRAIN();
      asm volatile("" : "+v"(xf), "+v"(k_)::"memory");
      PINV4(sp);
      const RowFin r = fin_key(rs, T, g * 8192 + lrow * 16, so, skey, l, w, R, wsum, sp);
      if constexpr (it == 0) {
        if (w == 0) {
          const unsigned need = 0x30003u << (((PROT() + 5) & 7) * 2);
          unsigned m = (unsigned)__builtin_amdgcn_ballot_w64(xf[0] == T[0] && xf[1] == T[1]);
          for (unsigned spins = 0; (m & need) != need;) {
            if (spin_fail(spins, rs, T)) { m = ~0u; break; }
            const u32x2 f = ld8(rs, F_X1 + ((p * RX + (xcd >> 1) % RX) * 32 + (l & 31)) * SX, 0);
            k_ = key_load(rs, l, 0, bid & 15);
            m = (unsigned)__builtin_amdgcn_ballot_w64(f[0] == T[0] && f[1] == T[1]);
          }
          k_ = umax16(k_ == POISON ? 0u : k_);
          if (l == 0) *(LDS_AS u32x4*)(skey + 32) = u32x4{m, k_, POISON, 0u};
        }
        if (tid == 192) *(LDS_AS u32x4*)(smem + L_QC) = u32x4{0u, 0u, 0u, 0u};
      }
      BAR();
      if constexpr (it == 0) {
        PRE_LOAD(1, PROT() + 5, 0, 4);
        const u32x2 mk = *(const LDS_AS u32x2*)(skey + 32);
        xm0 = __builtin_amdgcn_readfirstlane(mk[0]); kg0 = __builtin_amdgcn_readfirstlane(mk[1]);
      }
      const unsigned own = fin_cand(rs, skey, it, g, rb, ct, r, v);
      if constexpr (it == 0) kg0 = max(kg0, (unsigned)__builtin_amdgcn_readfirstlane(own));
      ownk = own;
      fin_x2(r, v);
      if constexpr (it == 0) {
        x2p[0] = pack8v(v); x2p[1] = pack8v(v + 4);
        asm volatile("" : "+v"(x2p[0]), "+v"(x2p[1]));
        if ((w & 3) * 2 + (w >> 2) == ct >> 1) {
          const float f = 1.0f + r.rd, rd2 = ln_rd(r.var * f * f), mb2 = rbf1(__builtin_fmaf(r.mean - r.mb, r.rd, r.mean));
          if (g < 2 && ((l >> 3) & 1) == (ct & 1))
            st4(min(__builtin_bit_cast(unsigned, g == 0 ? f * rd2 : -__builtin_fmaf(r.mb, r.rd, mb2) * __builtin_amdgcn_rcpf(f)), POISON - 1), rs, OFF_AB + 16384 + R * 4 + g * 8192, 0);
        }
      }
    }
    if (it == 1) {
      const u32x4 op0 = trade16(pack8v(v)), op1 = trade16(pack8v(v + 4));
#define OUT_STORE()                                                                                 \
  if (R != key_row(key) && (!OEA || R == key_row(ownk))) {                                                                          \
    __builtin_amdgcn_raw_buffer_store_b128(op0, rout, (R * 1024 + rmo) * 2, 0, SC1);                  \
    __builtin_amdgcn_raw_buffer_store_b128(op1, rout, (R * 1024 + rmo + 32) * 2, 0, SC1);             \
  }
      if (OEA && R != key_row(ownk)) {
        __builtin_amdgcn_raw_buffer_store_b128(op0, rout, (R * 1024 + rmo) * 2, 0, SC1);
        __builtin_amdgcn_raw_buffer_store_b128(op1, rout, (R * 1024 + rmo + 32) * 2, 0, SC1);
      }
      u32x4 xv[4];
      unsigned guess = 0;
      {
        __builtin_amdgcn_s_sleep(GSL);
        const unsigned k_ = key_load(rs, l, 1, bid & 15);
        guess = ffn_xoff(__builtin_amdgcn_readfirstlane(umax16(k_ == POISON ? 0u : k_)), 1, w);
        row_load(rs, g, guess, xv);
      }
      if (w == 0) {
        const unsigned k_ = key_wait(rs, T, l, 1, bid & 15, key_load(rs, l, 1, bid & 15));
        if (l == 0) *(LDS_AS unsigned*)(smem + L_SKEY + 32) = k_;
      }
      DRAIN();
      PINV4(xv);
      BAR();
      const unsigned key = __builtin_amdgcn_readfirstlane(*(LDS_AS unsigned*)(smem + L_SKEY + 32));
      if (ffn_on) {
        f32x4 ga, ua;
        s16x4 dfr[8];
        ffn_d_read(smem, l, w, dfr);
        if (guess != ffn_xoff(key, 1, w)) ld4x16(xv, rs, g * 16, ffn_xoff(key, 1, w));
        ffn_gu(gv, uv, xv, ga, ua);
        ffn_gu_check(rs, T, ffn_xoff(key, 1, w), l, gv, uv, xv, ga, ua);
        ffn_gu_store(smem, l, w, ga, ua);
        BAR();
        f32x4 ya = {0.f, 0.f, 0.f, 0.f}, yb = ya;
        u32x2 hh = ffn_h(ffn_t_read(smem, l), l);
        ffn_d_mma(dfr, hh, ya, yb);
        if ((l & 15) < 8) ffn_d_store(rs, l, 4 * w, 1, slice, ya + yb);
        SB();
        if (OSLP) __builtin_amdgcn_s_sleep(OSLP);
        OUT_STORE();
      } else {
        OUT_STORE();
        const unsigned G = bi;
        if (G == 0 && tid == 0) st8(T, rs, F_EP, 0);
        u32x4 pv[4];
        u32x2 cxn, cx1;
        __builtin_amdgcn_s_sleep(RSL);
        red_load(rs, tid, 1, G, key, pv, cxn, cx1);
        red_sum(rs, smem, T, tid, w, 1, G, key, pv, cxn, cx1);
        BAR();
        if (w == 0) {
          const u32x2 ov = red_final<8>(smem, l, key, cxn, cx1);
          if (l < 8) __builtin_amdgcn_raw_buffer_store_b64(ov, rout, (key_row(key) * 1024 + G * 32 + l * 4) * 2, 0, 0);
        }
      }
#undef OUT_STORE
    }
  };
  layer(std::integral_constant<int, 0>{});
  layer(std::integral_constant<int, 1>{});
}

extern "C" void kernel_launch(void* const* d_in, const int* in_sizes, int n_in, void* d_out, int out_size,
                              void* d_ws, size_t ws_size, hipStream_t stream) {
  (void)hipFuncSetAttribute((const void*)mega_kernel, hipFuncAttributeMaxDynamicSharedMemorySize, LDS_BYTES);
  mega_kernel<<<256, 512, LDS_BYTES, stream>>>((const bf16*)d_in[0], (char*)d_ws, (const bf16*)d_in[1], (const bf16*)d_in[2], (const bf16*)d_in[4],
                                               (const bf16*)d_in[5], (const bf16*)d_in[6], (const bf16*)d_in[7], (bf16*)d_out);
}
```

```cpp
#include <hip/hip_runtime.h>
#include <stdint.h>
#include <utility>

typedef __bf16 bf16;
typedef __bf16 bf16x8 __attribute__((ext_vector_type(8)));
typedef __bf16 bf16x2 __attribute__((ext_vector_type(2)));
typedef float f32x4 __attribute__((ext_vector_type(4)));
typedef float f32x2 __attribute__((ext_vector_type(2)));
typedef short s16x4 __attribute__((ext_vector_type(4)));
typedef unsigned int u32x4 __attribute__((ext_vector_type(4)));
typedef unsigned int u32x2 __attribute__((ext_vector_type(2)));
#define LDS_AS __attribute__((address_space(3)))
#define GAS __attribute__((address_space(1)))
typedef GAS unsigned gu32;
typedef GAS unsigned long long gu64;
#define RLX_AGENT __ATOMIC_RELAXED, __HIP_MEMORY_SCOPE_AGENT
typedef __amdgpu_buffer_rsrc_t rsrc_t;

constexpr int cmax(int a, int b) { return a > b ? a : b; }
constexpr int F_ = 3584;
constexpr int SC1 = 16;

constexpr int RX = 4;
constexpr unsigned F_EP = 0, F_TMO = 128;
constexpr unsigned F_XN = 4096;
#define STR 8, 8, 4, 8
constexpr unsigned STV[4] = {STR}, SO = STV[0], SX = STV[1], SK = STV[2], SV = STV[3];
constexpr unsigned F_O = 8u << 20;
constexpr unsigned F_X1 = F_O + 131072;
constexpr unsigned OFF_KEY = F_O + 393216;
constexpr unsigned F_KV = F_O + 524288;
#define KVF(it, i) ((i) * 128 + (it) * 64)
#define OFL(h) (((h) >> 3) * 128 + ((h) & 7) * 8)
#define OFB(it, p) ((p) * 512 + (it) * 64)
constexpr unsigned OFF_AB = 147456;
constexpr unsigned OFF_XNEW = 262144;
constexpr unsigned OFF_CS = 294912;
constexpr unsigned OFF_FX = 393216;
constexpr unsigned OFF_CA = 524288;
constexpr unsigned OFF_CB = 655360;
constexpr unsigned OFF_STAT = 1u << 20;
constexpr unsigned OFF_PART = 3u << 20;
constexpr unsigned OFF_XN = 8u << 20;
constexpr unsigned OFF_O = 16u << 20;
constexpr unsigned OFF_KV = 32u << 20;
constexpr unsigned OFF_WI = 52u << 20;
#define REUSE 0
constexpr unsigned O_STEP = (REUSE & 1) ? 0 : 4u << 20, KV_STEP = (REUSE & 2) ? 0 : 8u << 20;
constexpr unsigned WS_BYTES = 58u << 20;
constexpr unsigned POISON = 0xFFFFFFFFu;

constexpr int L_ST0 = 147456, L_CNT = 147968, L_CSL = 31872, L_FXL = 32256, L_K = 0, L_V = 32768, L_RING = 65536, L_XCH = 0, L_DN = 32768, L_TOUCH0 = 149504, L_RDY = 31744, L_QC = 31808, LDS_BYTES = 163840;
constexpr int STAGE = 24576;
#define SLOT(t) ((t) & 3)

template <typename F, int... I> __device__ __forceinline__ void static_for_impl(F&& f, std::integer_sequence<int, I...>) {
  (f(std::integral_constant<int, I>{}), ...);
}
template <int N, typename F> __device__ __forceinline__ void static_for(F&& f) { static_for_impl(f, std::make_integer_sequence<int, N>{}); }
__device__ __forceinline__ unsigned pack2(float a, float b) {
  f32x2 v = {a, b};
  return __builtin_bit_cast(unsigned, __builtin_convertvector(v, bf16x2));
}
__device__ __forceinline__ float asf(unsigned u) { return __builtin_bit_cast(float, u); }
__device__ __forceinline__ float lo16(unsigned u) { return __builtin_bit_cast(float, u << 16); }
__device__ __forceinline__ float hi16(unsigned u) { return __builtin_bit_cast(float, u & 0xffff0000u); }
__device__ __forceinline__ u32x4 pack8(const f32x4 a, const f32x4 b) {
  u32x4 o;
  o[0] = pack2(a[0], a[1]); o[1] = pack2(a[2], a[3]); o[2] = pack2(b[0], b[1]); o[3] = pack2(b[2], b[3]);
  return o;
}
__device__ __forceinline__ f32x4 mfma16(u32x4 a, u32x4 b, f32x4 c) {
  return __builtin_amdgcn_mfma_f32_16x16x32_bf16(__builtin_bit_cast(bf16x8, a), __builtin_bit_cast(bf16x8, b), c, 0, 0, 0);
}
__device__ __forceinline__ f32x4 mfma16k16(s16x4 a, s16x4 b, f32x4 c) { return __builtin_amdgcn_mfma_f32_16x16x16bf16_1k(a, b, c, 0, 0, 0); }
template <int CTRL> __device__ __forceinline__ float dppf(float v) {
  return __builtin_bit_cast(float, __builtin_amdgcn_update_dpp(0, __builtin_bit_cast(int, v), CTRL, 0xf, 0xf, true));
}
template <int CTRL> __device__ __forceinline__ unsigned dppu(unsigned v) {
  return (unsigned)__builtin_amdgcn_update_dpp(0, (int)v, CTRL, 0xf, 0xf, true);
}
__device__ __forceinline__ float add_x16(float v) {
  auto r = __builtin_amdgcn_permlane16_swap(__builtin_bit_cast(unsigned, v), __builtin_bit_cast(unsigned, v), false, false);
  return __builtin_bit_cast(float, (unsigned)r[0]) + __builtin_bit_cast(float, (unsigned)r[1]);
}
__device__ __forceinline__ float add_x32(float v) {
  auto r = __builtin_amdgcn_permlane32_swap(__builtin_bit_cast(unsigned, v), __builtin_bit_cast(unsigned, v), false, false);
  return __builtin_bit_cast(float, (unsigned)r[0]) + __builtin_bit_cast(float, (unsigned)r[1]);
}
__device__ __forceinline__ float max_x16(float v) {
  auto r = __builtin_amdgcn_permlane16_swap(__builtin_bit_cast(unsigned, v), __builtin_bit_cast(unsigned, v), false, false);
  return fmaxf(__builtin_bit_cast(float, (unsigned)r[0]), __builtin_bit_cast(float, (unsigned)r[1]));
}
__device__ __forceinline__ float max_x32(float v) {
  auto r = __builtin_amdgcn_permlane32_swap(__builtin_bit_cast(unsigned, v), __builtin_bit_cast(unsigned, v), false, false);
  return fmaxf(__builtin_bit_cast(float, (unsigned)r[0]), __builtin_bit_cast(float, (unsigned)r[1]));
}
__device__ __forceinline__ unsigned umax_x16(unsigned v) {
  auto r = __builtin_amdgcn_permlane16_swap(v, v, false, false);
  return max((unsigned)r[0], (unsigned)r[1]);
}
__device__ __forceinline__ unsigned umax_x32(unsigned v) {
  auto r = __builtin_amdgcn_permlane32_swap(v, v, false, false);
  return max((unsigned)r[0], (unsigned)r[1]);
}
__device__ __forceinline__ float sum_hi8(float v) {
  v += dppf<0x128>(v);
  v = add_x16(v);
  return add_x32(v);
}
__device__ __forceinline__ float sum32(float v) {
  v += dppf<0xB1>(v);
  v += dppf<0x4E>(v);
  v += dppf<0x124>(v);
  v += dppf<0x128>(v);
  return add_x16(v);
}
__device__ __forceinline__ float sum64(float v) {
  v += dppf<0xB1>(v);
  v += dppf<0x4E>(v);
  v += dppf<0x124>(v);
  v += dppf<0x128>(v);
  v = add_x16(v);
  return add_x32(v);
}

#define BAR() asm volatile("s_waitcnt lgkmcnt(0)\n\ts_barrier" ::: "memory")
#define DRAIN() asm volatile("s_waitcnt vmcnt(0)" ::: "memory")
#define SB() __builtin_amdgcn_sched_barrier(0)
template <int N> __device__ __forceinline__ void wait_vm() { asm volatile("s_waitcnt vmcnt(%0)" ::"n"(N) : "memory"); }

__device__ __forceinline__ void dma16(rsrc_t rs, unsigned voff, unsigned soff, unsigned lds) {
  asm volatile("s_mov_b32 m0, %0\n\ts_nop 0\n\tbuffer_load_dwordx4 %1, %2, %3 offen lds" ::"s"(lds), "v"(voff), "s"(rs), "s"(soff) : "memory");
}
template <int OFF> __device__ __forceinline__ void dma16s(rsrc_t rs, unsigned voff, unsigned soff, unsigned lds) {
  asm volatile("s_mov_b32 m0, %0\n\ts_nop 0\n\tbuffer_load_dwordx4 %1, %2, %3 offen offset:%4 sc1 lds" ::"s"(lds), "v"(voff), "s"(rs), "s"(soff), "n"(OFF) : "memory");
}
__device__ __forceinline__ void touch(unsigned& d, const GAS bf16* ptr) { asm volatile("global_load_dword %0, %1, off" : "+v"(d) : "v"(ptr) : "memory"); }
__device__ __forceinline__ void ldrows(u32x4 (&d)[4], rsrc_t rx, unsigned voff, unsigned soff) {
  asm volatile("buffer_load_dwordx4 %0, %4, %5, %6 offen\n\tbuffer_load_dwordx4 %1, %4, %5, %6 offen offset:512\n\t"
               "buffer_load_dwordx4 %2, %4, %5, %6 offen offset:1024\n\tbuffer_load_dwordx4 %3, %4, %5, %6 offen offset:1536"
               : "=&v"(d[0]), "=&v"(d[1]), "=&v"(d[2]), "=&v"(d[3]) : "v"(voff), "s"(rx), "s"(soff) : "memory");
}
__device__ __forceinline__ void pin4(f32x2* v) { asm volatile("" : "+v"(v[0]), "+v"(v[1]), "+v"(v[2]), "+v"(v[3])); }
__device__ __forceinline__ void touchb(unsigned& d, rsrc_t rs, unsigned vo) { asm volatile("buffer_load_dword %0, %1, %2, 0 offen sc1" : "+v"(d) : "v"(vo), "s"(rs) : "memory"); }
__device__ __forceinline__ float rbf1(float x) { return __builtin_bit_cast(float, pack2(0.f, x)); }
__device__ __forceinline__ void vswap(unsigned& a, unsigned& b) { asm volatile("v_swap_b32 %0, %1" : "+v"(a), "+v"(b)); }
__device__ __forceinline__ void vswap(float& a, float& b) { asm volatile("v_swap_b32 %0, %1" : "+v"(a), "+v"(b)); }
template <typename V, int N> __device__ __forceinline__ void vswapn(V& a, V& b) {
#pragma unroll
  for (int i = 0; i < N; ++i) { auto x = a[i], y = b[i]; vswap(x, y); a[i] = x; b[i] = y; }
}
__device__ __forceinline__ u32x4 ld16(rsrc_t rs, unsigned voff, unsigned soff) { return __builtin_amdgcn_raw_buffer_load_b128(rs, voff, soff, SC1); }
template <int OFF> __device__ __forceinline__ void ld16h(u32x4& d, rsrc_t rs, unsigned voff, unsigned soff) {
  asm volatile("buffer_load_dwordx4 %0, %1, %2, %3 offen offset:%4 sc1" : "=v"(d) : "v"(voff), "s"(rs), "s"(soff), "n"(OFF) : "memory");
}
template <int I, int OFF> __device__ __forceinline__ void ld16w(u32x4& d, rsrc_t rs, unsigned voff, unsigned soff) {
  if constexpr (I == 0) asm volatile("buffer_load_dwordx4 %0, %1, %2, %3 offen offset:%4 sc1" : "={v[128:131]}"(d) : "v"(voff), "s"(rs), "s"(soff), "n"(OFF) : "memory");
  else if constexpr (I == 1) asm volatile("buffer_load_dwordx4 %0, %1, %2, %3 offen offset:%4 sc1" : "={v[132:135]}"(d) : "v"(voff), "s"(rs), "s"(soff), "n"(OFF) : "memory");
  else if constexpr (I == 2) asm volatile("buffer_load_dwordx4 %0, %1, %2, %3 offen offset:%4 sc1" : "={v[136:139]}"(d) : "v"(voff), "s"(rs), "s"(soff), "n"(OFF) : "memory");
  else if constexpr (I == 3) asm volatile("buffer_load_dwordx4 %0, %1, %2, %3 offen offset:%4 sc1" : "={v[140:143]}"(d) : "v"(voff), "s"(rs), "s"(soff), "n"(OFF) : "memory");
  else if constexpr (I == 4) asm volatile("buffer_load_dwordx4 %0, %1, %2, %3 offen offset:%4 sc1" : "={v[144:147]}"(d) : "v"(voff), "s"(rs), "s"(soff), "n"(OFF) : "memory");
  else if constexpr (I == 5) asm volatile("buffer_load_dwordx4 %0, %1, %2, %3 offen offset:%4 sc1" : "={v[148:151]}"(d) : "v"(voff), "s"(rs), "s"(soff), "n"(OFF) : "memory");
  else if constexpr (I == 6) asm volatile("buffer_load_dwordx4 %0, %1, %2, %3 offen offset:%4 sc1" : "={v[152:155]}"(d) : "v"(voff), "s"(rs), "s"(soff), "n"(OFF) : "memory");
  else if constexpr (I == 7) asm volatile("buffer_load_dwordx4 %0, %1, %2, %3 offen offset:%4 sc1" : "={v[156:159]}"(d) : "v"(voff), "s"(rs), "s"(soff), "n"(OFF) : "memory");
  else if constexpr (I == 8) asm volatile("buffer_load_dwordx4 %0, %1, %2, %3 offen offset:%4 sc1" : "={v[160:163]}"(d) : "v"(voff), "s"(rs), "s"(soff), "n"(OFF) : "memory");
  else if constexpr (I == 9) asm volatile("buffer_load_dwordx4 %0, %1, %2, %3 offen offset:%4 sc1" : "={v[164:167]}"(d) : "v"(voff), "s"(rs), "s"(soff), "n"(OFF) : "memory");
  else if constexpr (I == 10) asm volatile("buffer_load_dwordx4 %0, %1, %2, %3 offen offset:%4 sc1" : "={v[168:171]}"(d) : "v"(voff), "s"(rs), "s"(soff), "n"(OFF) : "memory");
  else if constexpr (I == 11) asm volatile("buffer_load_dwordx4 %0, %1, %2, %3 offen offset:%4 sc1" : "={v[172:175]}"(d) : "v"(voff), "s"(rs), "s"(soff), "n"(OFF) : "memory");
}
template <int K> __device__ __forceinline__ void pin_w(u32x4& a, u32x4& b) {
  if constexpr (K == 0) asm volatile("" : "+{v[128:131]}"(a), "+{v[132:135]}"(b)::"memory");
  else if constexpr (K == 1) asm volatile("" : "+{v[136:139]}"(a), "+{v[140:143]}"(b)::"memory");
  else if constexpr (K == 2) asm volatile("" : "+{v[144:147]}"(a), "+{v[148:151]}"(b)::"memory");
  else if constexpr (K == 3) asm volatile("" : "+{v[152:155]}"(a), "+{v[156:159]}"(b)::"memory");
  else if constexpr (K == 4) asm volatile("" : "+{v[160:163]}"(a), "+{v[164:167]}"(b)::"memory");
  else if constexpr (K == 5) asm volatile("" : "+{v[168:171]}"(a), "+{v[172:175]}"(b)::"memory");
}
__device__ __forceinline__ void ld8h(u32x2& d, rsrc_t rs, unsigned voff, unsigned soff) {
  asm volatile("buffer_load_dwordx2 %0, %1, %2, %3 offen sc1" : "=v"(d) : "v"(voff), "s"(rs), "s"(soff) : "memory");
}
__device__ __forceinline__ void ld16u(u32x4& d, rsrc_t rs, unsigned voff, unsigned soff) {
  asm volatile("buffer_load_dwordx4 %0, %1, %2, %3 offen sc1" : "+v"(d) : "v"(voff), "s"(rs), "s"(soff) : "memory");
}
template <int OFF> __device__ __forceinline__ void ld4h(unsigned& d, rsrc_t rs, unsigned voff, unsigned soff) {
  asm volatile("buffer_load_dword %0, %1, %2, %3 offen offset:%4 sc1" : "=v"(d) : "v"(voff), "s"(rs), "s"(soff), "n"(OFF) : "memory");
}
__device__ __forceinline__ void ld4x16(u32x4 (&d)[4], rsrc_t rs, unsigned voff, unsigned soff) {
  asm volatile("buffer_load_dwordx4 %0, %4, %5, %6 offen sc1\n\tbuffer_load_dwordx4 %1, %4, %5, %6 offen offset:64 sc1\n\t"
               "buffer_load_dwordx4 %2, %4, %5, %6 offen offset:128 sc1\n\tbuffer_load_dwordx4 %3, %4, %5, %6 offen offset:192 sc1\n\ts_waitcnt vmcnt(0)"
               : "=&v"(d[0]), "=&v"(d[1]), "=&v"(d[2]), "=&v"(d[3]) : "v"(voff), "s"(rs), "s"(soff) : "memory");
}
__device__ __forceinline__ u32x2 ld8(rsrc_t rs, unsigned voff, unsigned soff) { return __builtin_amdgcn_raw_buffer_load_b64(rs, voff, soff, SC1); }
__device__ __forceinline__ void st16(u32x4 v, rsrc_t rs, unsigned voff, unsigned soff) { __builtin_amdgcn_raw_buffer_store_b128(v, rs, voff + soff, 0, SC1); }
__device__ __forceinline__ void st8(u32x2 v, rsrc_t rs, unsigned voff, unsigned soff) { __builtin_amdgcn_raw_buffer_store_b64(v, rs, voff, soff, SC1); }

__device__ __forceinline__ unsigned ld4(rsrc_t rs, unsigned voff, unsigned soff) { return __builtin_amdgcn_raw_buffer_load_b32(rs, voff, soff, SC1); }
__device__ __forceinline__ void st4(unsigned v, rsrc_t rs, unsigned voff, unsigned soff) { __builtin_amdgcn_raw_buffer_store_b32(v, rs, voff, soff, SC1); }

constexpr unsigned kSpinCap = 1u << 16;
__device__ __forceinline__ bool spin_fail(unsigned& spins, rsrc_t rs, u32x2 T) {
  asm volatile("" ::: "memory");
  ++spins;
  if (spins > kSpinCap) { st8(T, rs, 0, F_TMO); return true; }
  if ((spins & 255u) == 0u) {
    const u32x2 v = ld8(rs, 0, F_TMO);
    if (v[0] == T[0] && v[1] == T[1]) return true;
  }
  __builtin_amdgcn_s_sleep(3);
  return false;
}
__device__ __forceinline__ void poll_flags2(rsrc_t rs, unsigned voff, u32x2 T) {
  for (unsigned spins = 0;;) {
    const u32x4 v = ld16(rs, voff, 0);
    if (__all(v[0] == T[0] && v[1] == T[1] && v[2] == T[0] && v[3] == T[1])) break;
    if (spin_fail(spins, rs, T)) break;
  }
  asm volatile("" ::: "memory");
  __builtin_amdgcn_fence(__ATOMIC_ACQUIRE, "wavefront");
}
template <int SL = 0> __device__ __forceinline__ void poll_flags(rsrc_t rs, unsigned voff, unsigned soff, u32x2 T) {
  for (unsigned spins = 0;;) {
    const u32x2 v = ld8(rs, voff, soff);
    if (__all(v[0] == T[0] && v[1] == T[1])) break;
    if (spin_fail(spins, rs, T)) break;
    if (SL) __builtin_amdgcn_s_sleep(SL);
  }
  asm volatile("" ::: "memory");
  __builtin_amdgcn_fence(__ATOMIC_ACQUIRE, "wavefront");
}

__device__ __forceinline__ unsigned toff(unsigned R, unsigned ks, unsigned g) {
  return (((R >> 5) * 64 + ks * 2 + ((R >> 4) & 1)) << 10) + ((g * 16 + (R & 15)) << 4);
}
__device__ __forceinline__ void unpack8v(const u32x4 u, f32x2* v) {
#pragma unroll
  for (int j = 0; j < 4; ++j) v[j] = f32x2{lo16(u[j]), hi16(u[j])};
}
__device__ __forceinline__ u32x4 pack8v(const f32x2* v) {
  u32x4 o;
#pragma unroll
  for (int j = 0; j < 4; ++j) o[j] = pack2(v[j][0], v[j][1]);
  return o;
}
__device__ __forceinline__ f32x2 rbf1v(f32x2 v) { return f32x2{rbf1(v[0]), rbf1(v[1])}; }
__device__ __forceinline__ float ln_rd(float var) { return __builtin_amdgcn_rcpf(rbf1(__builtin_amdgcn_sqrtf(rbf1(rbf1(var) + 1e-5f)))); }
template <int N> __device__ __forceinline__ void ln_apply(float mb, float rd, const f32x2* v, f32x2* y) {
  const f32x2 mb2 = {mb, mb}, rd2 = {rd, rd};
#pragma unroll
  for (int j = 0; j < N; ++j) y[j] = rbf1v(rbf1v(v[j] - mb2) * rd2);
}
__device__ __forceinline__ float hadd(f32x2 v) {
  float a = v[0];
  asm volatile("" : "+v"(a));
  return a + v[1];
}
__device__ __forceinline__ float add_g(float v) { return add_x32(add_x16(v)); }
__device__ __forceinline__ void add_g3(float& x, float& y, float& z) {
  auto a = __builtin_amdgcn_permlane16_swap(__builtin_bit_cast(unsigned, x), __builtin_bit_cast(unsigned, y), false, false);
  auto b = __builtin_amdgcn_permlane16_swap(__builtin_bit_cast(unsigned, z), __builtin_bit_cast(unsigned, z), false, false);
  const float r = asf(a[0]) + asf(a[1]), t = asf(b[0]) + asf(b[1]);
  auto c = __builtin_amdgcn_permlane32_swap(__builtin_bit_cast(unsigned, r), __builtin_bit_cast(unsigned, r), false, false);
  auto d = __builtin_amdgcn_permlane32_swap(__builtin_bit_cast(unsigned, t), __builtin_bit_cast(unsigned, t), false, false);
  const float u = asf(c[0]) + asf(c[1]);
  z = asf(d[0]) + asf(d[1]);
  auto e = __builtin_amdgcn_permlane16_swap(__builtin_bit_cast(unsigned, u), __builtin_bit_cast(unsigned, u), false, false);
  x = asf(e[0]); y = asf(e[1]);
}
__device__ __forceinline__ u32x4 trade16(u32x4 v) {
  auto a = __builtin_amdgcn_permlane16_swap(v[0], v[2], false, false);
  auto b = __builtin_amdgcn_permlane16_swap(v[1], v[3], false, false);
  return u32x4{(unsigned)a[0], (unsigned)b[0], (unsigned)a[1], (unsigned)b[1]};
}
__device__ __forceinline__ unsigned umax4(u32x4 v) { return max(max(v[0], v[1]), max(v[2], v[3])); }
__device__ __forceinline__ u32x2 unpoison2(u32x2 v) { return u32x2{min(v[0], POISON - 1), min(v[1], POISON - 1)}; }
__device__ __forceinline__ u32x4 unpoison4(u32x4 v) {
  return u32x4{min(v[0], POISON - 1), min(v[1], POISON - 1), min(v[2], POISON - 1), min(v[3], POISON - 1)};
}

typedef union { struct { s16x4 a, b; } hf; u32x4 v; } frag_t;
__device__ __forceinline__ s16x4 trread(const char* smem, unsigned off) {
  return __builtin_amdgcn_ds_read_tr16_b64_v4i16((LDS_AS s16x4*)(smem + off));
}
__device__ __forceinline__ s16x4 trabs(unsigned addr) { return __builtin_amdgcn_ds_read_tr16_b64_v4i16((LDS_AS s16x4*)(size_t)addr); }

constexpr int WA = 6;
#define WRV 3
#define WBV 4
constexpr int WB = WBV, WR = WRV;
#define CH 0, 4, 7, 8, 13, 15, 20, 22, 27, 29
constexpr int CHV[10] = {CH};
constexpr int FSL = CHV[1], FS0 = CHV[2], FS0D = CHV[3], FS1 = CHV[4], FS1S = CHV[5];
constexpr int FXA = CHV[6], FXB = CHV[7], FXL = CHV[8], FXS = CHV[9];
constexpr int FXT = FXB >> 2;
#define C0 24, 3, 16, 19, 22, 23, -1, 5, 4
constexpr int C0V[9] = {C0};
constexpr int IMG = C0V[0], CSD = C0V[1];
constexpr int ERQ = C0V[2], CSL = C0V[3], CSS = C0V[4];
constexpr int RWL = C0V[6], RWS = C0V[7], POI = C0V[8];
#define WTS 99
#define WTG 2
#define PBS 10
#define KP0 26
#define PS0 8
constexpr int EIN = cmax(CSD, cmax(RWS, POI)) + 3;
constexpr int ABS = 27;
constexpr int abl(int it) { return it ? 23 : C0V[5]; }
constexpr int XU = 3, XL = 2;
constexpr int x_slot(int u) { return u % XU == 0 ? 0 : u % XU == 1 ? 32768 : 49152; }
#define TCH 19, 7, 4, 27
constexpr int TCV[4] = {TCH};
constexpr int TS0 = TCV[0], TSN = TCV[1], TW = TCV[2], TST = TCV[3];
#define QLV 26
constexpr int QL = QLV;
#define FDG 0
#define XSL 4
#define FW0 4
#define FW1 4
#define X1F 1
#define RWG 0
#define CSG 4
#define RXCD 5
#define TWR TW
#define WDS (17 - WB)
#define WDW 1
#define OSL0 0
#define OSL1 0
#define OPS 0
#define KVS 0
#define LOP 0
#define OEA 0
#define OSLP 12
#define PB1 24
#define WD1 0
#define SDI0 16
#define SLP 24, 14, 42, 3
constexpr int SLV[4] = {SLP};
constexpr int SDIR = SLV[0], GSL = SLV[1], RSL = SLV[2], KSL = SLV[3];
constexpr bool tile_piece(int s) { return s >= 3 && ((s - 3) >> 2) + 3 < 8 && ((s - 3) & 3) < 3; }
constexpr int a_req(int s, int n) {
  if (!(n & 1)) return -1;
  if (s <= 1) return 8 + 3 * s + (n >> 1);
  return n < 5 && s + 5 < 32 ? 2 * (s + 5) + (n >> 1) : -1;
}
constexpr int vm_n(int it, int s, int n) {
  int c = 0;
  if (n == 0) c += tile_piece(s);
  if (n == 4) c += (s == abl(it)) + (it ? (s == FSL ? 5 : 0) : (s == ERQ) + (s >= IMG && s < IMG + 3));
  if (!it) c += (s == WTS && n == WTG) + (s < 4 && n == 2) + (s == 0 && (n == 0 || n == 4)) + ((n == 3 || n == 5) && s + XL + XU < 32);
  else c += (a_req(s, n) >= 0) + ((s == 0 || s == 4) && n == 2);
  return c;
}
constexpr int vm_at(int it, int s, int n) {
  int c = it ? 8 : 14;
  for (int u = 0; u < 6 * s + n; ++u) c += vm_n(it, u / 6, u % 6);
  return c;
}
constexpr int vm_a(int it, int k) {
  if (k < 4) return 2 * k + 2;
  for (int u = 0; u < 192; ++u)
    if (a_req(u / 6, u % 6) == 2 * k + 1) return vm_at(it, (u + 1) / 6, (u + 1) % 6);
  return 0;
}
constexpr int vm_tile(int it, int t) { return t >= 3 ? vm_at(it, 4 * t - 7, 1) : t == 1 ? vm_at(0, 0, 5) : vm_at(0, 3, 3); }
constexpr int vm_x(int u) { return u == 2 ? 9 : u < XU + XL ? 12 + 2 * (u - XU) : vm_at(0, u - XU - XL + 1, 0); }

constexpr int wo_k(int it, int s) { return it || s < 4 ? 15 - s : s < 8 ? 7 - s : 19 - s; }
constexpr int wo_pos(int it, int s) { return 2 * s + 2 + (s >= WR ? 2 * it : 0); }
constexpr int wo_at(int it, int s) { return 2 * it + (s < WB - WR ? 2 * WR + 2 + 4 * s : s < 16 - WB ? 2 * (2 * WB - WR) + 2 * (s - WB + WR) : 32); }
template <int OFF> __device__ __forceinline__ void lds_count(unsigned ad) {
  asm volatile("s_mov_b64 exec, 1\n\tds_add_u32 %0, %1 offset:%2\n\ts_mov_b64 exec, -1" ::"v"(ad), "v"(1u), "n"(OFF) : "memory");
}
__device__ __forceinline__ void lds_look(char* c, unsigned want) {
  while ((unsigned)__builtin_amdgcn_readfirstlane(__hip_atomic_load((LDS_AS unsigned*)c, __ATOMIC_RELAXED, __HIP_MEMORY_SCOPE_WORKGROUP)) < want) {}
  asm volatile("" ::: "memory");
}
__device__ __forceinline__ void pin1(unsigned& a) { asm volatile("" : "+v"(a)); }
__device__ __forceinline__ void pin2(u32x4& a, u32x4& b) { asm volatile("" : "+v"(a), "+v"(b)::"memory"); }
__device__ __forceinline__ void pin2s(float& a, float& b) { asm volatile("" : "+v"(a), "+v"(b)); }
template <int OFF> __device__ __forceinline__ void lds2(u32x4& a, u32x4& b, unsigned ad) {
  asm volatile("ds_read_b128 %0, %2 offset:%3\n\tds_read_b128 %1, %2 offset:%4" : "=&v"(a), "=&v"(b) : "v"(ad), "n"(OFF), "n"(OFF + 1024) : "memory");
}
template <int N> __device__ __forceinline__ void lds_wait(u32x4& a, u32x4& b) { asm volatile("s_waitcnt lgkmcnt(%2)" : "+v"(a), "+v"(b) : "n"(N) : "memory"); }
__device__ __forceinline__ void pin2f(f32x2& a, f32x2& b) { asm volatile("" : "+v"(a), "+v"(b)); }
__device__ __forceinline__ void pin_ac(f32x4 (&ac)[2][4]) {
  asm volatile("" : "+{v[176:179]}"(ac[0][0]), "+{v[180:183]}"(ac[0][1]), "+{v[184:187]}"(ac[0][2]), "+{v[188:191]}"(ac[0][3]),
                    "+{v[192:195]}"(ac[1][0]), "+{v[196:199]}"(ac[1][1]), "+{v[200:203]}"(ac[1][2]), "+{v[204:207]}"(ac[1][3]));
}

constexpr int L_XC = LDS_BYTES - 64, L_SKEY = 32704, L_SRED = 4096, L_SFIN = 20480;
__device__ __forceinline__ unsigned key_load(rsrc_t rs, int l, int L, int rep) { return ld4(rs, (l & 15) * SK, OFF_KEY + (L * 16 + rep) * cmax(16 * SK, 128)); }
__device__ __forceinline__ unsigned umax16(unsigned k) {
  k = max(k, dppu<0xB1>(k));
  k = max(k, dppu<0x4E>(k));
  k = max(k, dppu<0x141>(k));
  return max(k, dppu<0x140>(k));
}
__device__ __forceinline__ unsigned key_row(unsigned key) { return (0xffffu - (key & 0xffffu)) & 2047u; }
__device__ __forceinline__ unsigned key_wait(rsrc_t rs, u32x2 T, int l, int L, int rep, unsigned kv) {
  if (!__all(kv != POISON)) {
    unsigned spins = 0;
    do {
      if (spin_fail(spins, rs, T)) break;
      if (L == 0) __builtin_amdgcn_s_sleep(KSL);
      kv = key_load(rs, l, L, rep);
    } while (!__all(kv != POISON));
  }
  return __builtin_amdgcn_readfirstlane(umax16(kv));
}
__device__ __forceinline__ unsigned ffn_xoff(unsigned key, int L, int w) { return OFF_CA + L * 32768 + (key_row(key) >> 7) * 2048 + w * 256; }
__device__ __forceinline__ void row_load(rsrc_t rs, int g, unsigned xo, u32x4 (&xv)[4]) {
  ld16h<0>(xv[0], rs, g * 16, xo); ld16h<64>(xv[1], rs, g * 16, xo); ld16h<128>(xv[2], rs, g * 16, xo); ld16h<192>(xv[3], rs, g * 16, xo);
}
template <int SL = 0> __device__ __forceinline__ void ffn_xwait(rsrc_t rs, u32x2 T, unsigned xo, int l, u32x4 (&xv)[4]) {
#define XV_OK() __all(max(max(umax4(xv[0]), umax4(xv[1])), max(umax4(xv[2]), umax4(xv[3]))) != POISON)
  if (!XV_OK()) {
    unsigned spins = 0;
    do {
      if (spin_fail(spins, rs, T)) break;
      if (SL) __builtin_amdgcn_s_sleep(SL);
#pragma unroll
      for (int i = 0; i < 4; ++i) xv[i] = ld16(rs, (l >> 4) * 16 + i * 64, xo);
    } while (!XV_OK());
  }
#undef XV_OK
}
__device__ __forceinline__ void ffn_gu(const u32x4 (&gv)[4], const u32x4 (&uv)[4], const u32x4 (&xv)[4], f32x4& ga, f32x4& ua) {
  ga = mfma16(gv[0], xv[0], f32x4{0.f, 0.f, 0.f, 0.f}); ua = mfma16(uv[0], xv[0], f32x4{0.f, 0.f, 0.f, 0.f});
#pragma unroll
  for (int i = 1; i < 4; ++i) { ga = mfma16(gv[i], xv[i], ga); ua = mfma16(uv[i], xv[i], ua); }
}
__device__ __forceinline__ void ffn_gu_check(rsrc_t rs, u32x2 T, unsigned xo, int l, const u32x4 (&gv)[4], const u32x4 (&uv)[4], u32x4 (&xv)[4], f32x4& ga, f32x4& ua) {
  if (__builtin_expect(__any(ga[0] != ga[0]), 0)) {
    ffn_xwait(rs, T, xo, l, xv);
    ffn_gu(gv, uv, xv, ga, ua);
  }
}
__device__ __forceinline__ void ffn_gu_store(char* smem, int l, int w, f32x4 ga, f32x4 ua) {
  LDS_AS f32x4* sred = (LDS_AS f32x4*)(smem + L_SRED);
  if ((l & 15) == 0) { sred[w * 8 + (l >> 4)] = ga; sred[w * 8 + 4 + (l >> 4)] = ua; }
}
__device__ __forceinline__ f32x4 ffn_t_read(char* smem, int l) { return ((const LDS_AS f32x4*)(smem + L_SRED))[(l & 7) * 8 + ((l >> 3) & 1) * 4 + (l >> 4)]; }
__device__ __forceinline__ void ffn_d_read(char* smem, int l, int blk, s16x4 (&dfr)[8]) {
#pragma unroll
  for (int i = 0; i < 8; ++i) dfr[i] = trread(smem, L_DN + blk * 4096 + (l >> 4) * 1024 + ((l & 15) >> 2) * 256 + i * 32 + (l & 3) * 8);
}
__device__ __forceinline__ float ffn_d_sum(float t) { t += dppf<0xB1>(t); t += dppf<0x4E>(t); return t + dppf<0x141>(t); }
__device__ __forceinline__ u32x2 ffn_h(f32x4 t, int l) {
#pragma unroll
  for (int e = 0; e < 4; ++e) t[e] = ffn_d_sum(t[e]);
  const float a = (l & 1) ? t[1] : t[0], b = (l & 1) ? t[3] : t[2], s = (l & 2) ? b : a;
  const float gg = rbf1(s), uu = rbf1(dppf<0x128>(s));
  const float h = rbf1(gg * __builtin_amdgcn_rcpf(1.0f + __expf(-gg))) * uu;
  const unsigned h0 = pack2(h, dppf<0x55>(h)), h1 = pack2(dppf<0xAA>(h), dppf<0xFF>(h));
  return (l & 15) == 0 ? u32x2{h0, h1} : u32x2{0u, 0u};
}
__device__ __forceinline__ void ffn_d_mma(const s16x4 (&dfr)[8], u32x2& hh, f32x4& ya, f32x4& yb) {
#pragma unroll
  for (int i = 0; i < 8; ++i) {
    if (i & 1) yb = mfma16k16(dfr[i], __builtin_bit_cast(s16x4, hh), yb);
    else ya = mfma16k16(dfr[i], __builtin_bit_cast(s16x4, hh), ya);
    hh = u32x2{dppu<0x111>(hh[0]), dppu<0x111>(hh[1])};
  }
}
__device__ __forceinline__ void ffn_d_store(rsrc_t rs, int l, int G0, int L, int slice, f32x4 y) {
  const unsigned c = l & 15;
  st16(unpoison4(__builtin_bit_cast(u32x4, y)), rs, (G0 + (c >> 1)) * 32768 + slice * 128 + (c & 1) * 64 + (l >> 4) * 16, OFF_PART + L * (1u << 20));
}
__device__ __forceinline__ void red_load(rsrc_t rs, int tid, int L, unsigned G, unsigned key, u32x4 (&pv)[4], u32x2& cxn, u32x2& cx1) {
#pragma unroll
  for (int k = 0; k < 4; ++k) pv[k] = ld16(rs, min(tid + 512 * k, 1791) * 16, OFF_PART + L * (1u << 20) + G * 32768);
  cxn = ld8(rs, (tid & 3) * 16 + (tid & 4) * 2, OFF_CA + L * 32768 + (key_row(key) >> 7) * 2048 + G * 64);
  cx1 = ld8(rs, (tid & 3) * 16 + (tid & 4) * 2, OFF_CB + L * 32768 + (key_row(key) >> 7) * 2048 + G * 64);
}
__device__ __forceinline__ void red_sum(rsrc_t rs, char* smem, u32x2 T, int tid, int w, int L, unsigned G, unsigned key,
                                        u32x4 (&pv)[4], u32x2& cxn, u32x2& cx1) {
#define PV_OK() __all(max(max(max(umax4(pv[0]), umax4(pv[1])), max(umax4(pv[2]), umax4(pv[3]))), max(max(cxn[0], cxn[1]), max(cx1[0], cx1[1]))) != POISON)
  if (!PV_OK()) {
    unsigned spins = 0;
    do {
      if (spin_fail(spins, rs, T)) break;
      red_load(rs, tid, L, G, key, pv, cxn, cx1);
    } while (!PV_OK());
  }
#undef PV_OK
  f32x4 s = (__builtin_bit_cast(f32x4, pv[0]) + __builtin_bit_cast(f32x4, pv[1])) + __builtin_bit_cast(f32x4, pv[2]);
  if (w < 4) s += __builtin_bit_cast(f32x4, pv[3]);
  ((LDS_AS f32x4*)(smem + L_SFIN))[tid] = s;
}
template <int NW> __device__ __forceinline__ u32x2 red_final(char* smem, int l, unsigned key, u32x2 cxn, u32x2 cx1) {
  const LDS_AS f32x4* sf = (const LDS_AS f32x4*)(smem + L_SFIN) + l;
  f32x4 s = (sf[0] + sf[64]) + (sf[128] + sf[192]);
  if constexpr (NW == 8) s += (sf[256] + sf[320]) + (sf[384] + sf[448]);
#pragma unroll
  for (int j = 0; j < 4; ++j) s[j] = sum_hi8(s[j]);
  const unsigned k16 = key >> 16;
  const float rmax = __builtin_bit_cast(float, ((k16 & 0x8000u) ? (k16 & 0x7fffu) : (~k16 & 0xffffu)) << 16);
  const float sig = rbf1(__builtin_amdgcn_rcpf(1.0f + __expf(-rmax)));
  const float xn4[4] = {lo16(cxn[0]), hi16(cxn[0]), lo16(cxn[1]), hi16(cxn[1])};
  const float x14[4] = {lo16(cx1[0]), hi16(cx1[0]), lo16(cx1[1]), hi16(cx1[1])};
  float o[4];
#pragma unroll
  for (int j = 0; j < 4; ++j) o[j] = x14[j] + rbf1(xn4[j] + rbf1(rbf1(s[j]) * sig));
  return u32x2{pack2(o[0], o[1]), pack2(o[2], o[3])};
}

struct RowFin { float mean, var, mb, rd; unsigned key; };
__device__ __forceinline__ void fin_wait(rsrc_t rs, u32x2 T, unsigned voff, unsigned so, u32x4 (&sp)[4]) {
#define ST_OK() __all(max(max(umax4(sp[0]), umax4(sp[1])), max(umax4(sp[2]), umax4(sp[3]))) != POISON)
  if (!ST_OK()) {
    unsigned spins = 0;
    do {
      if (spin_fail(spins, rs, T)) break;
#pragma unroll
      for (int i = 0; i < 4; ++i) sp[i] = ld16(rs, voff + i * 2048, so);
    } while (!ST_OK());
  }
#undef ST_OK
}
__device__ __forceinline__ RowFin fin_key(rsrc_t rs, u32x2 T, unsigned voff, unsigned so, char* skey, int l, int w, unsigned R, float wsum, u32x4 (&sp)[4]) {
  RowFin r;
#define ST_SUM() ((__builtin_bit_cast(f32x4, sp[0]) + __builtin_bit_cast(f32x4, sp[1])) + (__builtin_bit_cast(f32x4, sp[2]) + __builtin_bit_cast(f32x4, sp[3])))
  f32x4 sf = ST_SUM();
  const float z = (sf[0] + sf[1]) + sf[2];
  if (__any(z != z)) {
    fin_wait(rs, T, voff, so, sp);
    sf = ST_SUM();
  }
#undef ST_SUM
  float s0 = sf[0], s1 = sf[1], s2 = sf[2];
  add_g3(s0, s1, s2);
  r.mean = s0 * (1.0f / 1024.0f);
  r.var = fmaxf(__builtin_fmaf(-r.mean, r.mean, s1 * (1.0f / 1024.0f)), 0.f);
  r.mb = rbf1(r.mean); r.rd = ln_rd(r.var);
  const unsigned rb16 = pack2((s2 - r.mb * wsum) * r.rd, 0.f) & 0xffffu;
  r.key = min((((rb16 & 0x8000u) ? (~rb16 & 0xffffu) : (rb16 | 0x8000u)) << 16) | (0xffffu - R), POISON - 1);
  const unsigned wk = umax16(r.key);
  if (l == 0) ((LDS_AS unsigned*)skey)[w] = wk;
  return r;
}
__device__ __forceinline__ unsigned fin_cand(rsrc_t rs, char* skey, int L, int g, int rb, int ct, const RowFin& r, const f32x2 (&v)[8]) {
  const u32x4 k0 = *(const LDS_AS u32x4*)skey, k1 = *(const LDS_AS u32x4*)(skey + 16);
  const unsigned best = max(umax4(k0), umax4(k1));
  if (r.key == best) {
    const unsigned co = L * 32768 + rb * 2048 + ct * 128 + g * 16;
    f32x2 t[8], rd2 = {r.rd, r.rd}, of2 = {-r.mb * r.rd, -r.mb * r.rd};
    asm volatile("" : "+v"(rd2), "+v"(of2));
#pragma unroll
    for (int j = 0; j < 8; ++j) t[j] = __builtin_elementwise_fma(v[j], rd2, of2);
    st16(unpoison4(pack8v(t)), rs, co, OFF_CA); st16(unpoison4(pack8v(t + 4)), rs, co + 64, OFF_CA);
    if (g == 0) st4(best, rs, OFF_KEY + (L * 16 + ct) * cmax(16 * SK, 128) + rb * SK, 0);
    st16(unpoison4(pack8v(v)), rs, co, OFF_CB); st16(unpoison4(pack8v(v + 4)), rs, co + 64, OFF_CB);
  }
  return best;
}
__device__ __forceinline__ void fin_x2(const RowFin& r, f32x2 (&v)[8]) {
  f32x2 sc2 = {1.0f + r.rd, 1.0f + r.rd}, of2 = {-r.mb * r.rd, -r.mb * r.rd};
  asm volatile("" : "+v"(sc2), "+v"(of2));
#pragma unroll
  for (int j = 0; j < 8; ++j) v[j] = __builtin_elementwise_fma(v[j], sc2, of2);
}

extern "C" __device__ unsigned long long dispatch_id() __asm("llvm.amdgcn.dispatch.id");

extern "C" __global__ __launch_bounds__(512, 2) void mega_kernel(const bf16* x, char* ws, const bf16* wqkv, const bf16* wo, const bf16* wr,
                                                                 const bf16* wg, const bf16* wu, const bf16* wd, bf16* out) {
  extern __shared__ __attribute__((aligned(16))) char smem[];
  const int w0_ = __builtin_amdgcn_readfirstlane(threadIdx.x >> 6);
  typedef unsigned long long u64x2 __attribute__((ext_vector_type(2)));
  u64x2 a01;
  unsigned long long a2;
  asm volatile("s_load_dwordx4 %0, %2, 0\n\ts_load_dwordx2 %1, %2, 16" : "=&s"(a01), "=&s"(a2) : "s"(__builtin_amdgcn_kernarg_segment_ptr()));
  const unsigned lbase = (unsigned)(size_t)(LDS_AS char*)smem;
  {
    unsigned long long pc;
    unsigned len;
    asm volatile("s_getpc_b64 %0\n\ts_mov_b32 %1, .Ltext_end-mega_kernel\n\t.subsection 1\n.Ltext_end:\n\t.subsection 0" : "=s"(pc), "=s"(len));
    const unsigned o = ((blockIdx.x >> 3) * 64 + (threadIdx.x & 63)) * 128;
    if (w0_ == 7 && o < len)
      asm volatile("s_mov_b32 m0, %0\n\ts_nop 0\n\tglobal_load_lds_dword %1, %2" ::"s"(lbase + L_TOUCH0), "v"(o), "s"(pc & ~127ull) : "memory");
  }
#define HGRP() ((xcd & 2) + (xcd >> 2))
#define IDS()                                                                                         \
  int tid = threadIdx.x, w = w0_, bid = blockIdx.x;                                                   \
  asm volatile("" : "+v"(tid));                                                                       \
  asm volatile("" : "+s"(w));                                                                         \
  asm volatile("" : "+s"(bid));                                                                       \
  const int l = tid & 63, xcd = bid & 7, bi = bid >> 3;                                               \
  const int p = (xcd & 1) * 4 + (bi & 3), h = HGRP() * 8 + (bi >> 2);         \
  const int rb = 2 * xcd + (bi & 1), ct = bi >> 1;                               \
  const int slice = (xcd - (xcd > RXCD)) * 32 + bi;                                            \
  const bool ffn_on = xcd != RXCD;                                                                    \
  const int g = l >> 4;                                                                               \
  const int rt = w < 4 ? w : 11 - w;          \
  KARGS();                                                                                            \
  (void)p; (void)h; (void)rb; (void)ct; (void)slice; (void)ffn_on; (void)g; (void)rt;
#define PROT() (2 * (p & 3) + (p >> 2))
  typedef const void* const __attribute__((address_space(4)))* karg_t;
#define KARGS() karg_t ka = (karg_t)__builtin_amdgcn_kernarg_segment_ptr(); asm volatile("" : "+s"(ka))
#define ARGP(name, i) const GAS bf16* name = (const GAS bf16*)ka[i]; asm volatile("" : "+s"(name))
#define RSRC(ptr, bytes) __builtin_amdgcn_make_buffer_rsrc((void*)(ptr), 0, bytes, 0x00020000)
#define rwq RSRC(pwq, 1024 * 3072 * 2)
#define rwo RSRC(pwo, 1024 * 1024 * 2)
#define rwg RSRC(pwg, 1024 * F_ * 2)
#define rwu RSRC(pwu, 1024 * F_ * 2)
#define rwd RSRC(pwd, 1024 * F_ * 2)
#define rout RSRC(pout, 2048 * 1024 * 2)

#define WQ_VOFF() ((l >> 2) * 6144 + (((l & 3) ^ ((((l >> 4) ^ (l >> 5)) & 1) << 1)) << 4))
#define WQ_KMAJ(q) (((q) % 3) * 8 + (q) / 3)
#define WQ_PIECE(t, j, dst) dma16(rwq, wqv, ((((t) + rot) & 7) * 128 + ((j) & 7) * 16) * 6144 + ((j) >> 3) * 2048 + h * 64, lbase + (dst) + (j) * 1024)
#define WQ_BOFF(dt) ((g >> 1) * 1024 + (8 * (g & 1) + ((l & 15) >> 2)) * 64 + (l & 3) * 8 + (((dt) ^ (g & 1)) << 5))
#define X_REQ(u, hf) dma16(rx, xv[hf], (p * 256 + rt * 32 + ((u) & 1) * 16 + (hf) * 8) * 2048 + ((((u) >> 2) + rot) & 7) * 256 + (((u) >> 1) & 1) * 128, lbase + rt * 2048 + x_slot(u) + (hf) * 1024)
#define WO_PIECE(j) dma16(rwo, wov, ((w >> 2) * 64 + (w & 3) + 4 * (j)) * 16384 + ct * 128, lbase + 32768 + (j) * 8192 + w * 1024)
#define WD_DMA_OF(v) _Pragma("unroll") for (int i = 0; i < 4; ++i)                                  \
      dma16(rwd, (l >> 4) * 2048 + (l & 15) * 16, (unsigned)(slice * 16 + 4 * i) * 2048 + (v) * 256, lbase + L_DN + (v) * 4096 + i * 1024)
#define FFN_DMA(m) dma16((m) & 1 ? rwu : rwg, fnv, (unsigned)(((4 * w + ((m) >> 1)) * 32) * F_ + slice * 16) * 2, lbase + 65536 + (m) * 8192 + w * 1024)
#define POISON_BIG(L)                                                                                \
  if (w >= 2) {                                                                                      \
    unsigned pz1 = POISON;                                                                           \
    asm volatile("" : "+v"(pz1));                                                                    \
    st16(u32x4{pz1, pz1, pz1, pz1}, rs, (w >= 4 ? OFF_PART + (L) * (1u << 20) + bid * 4096 - 4096 : OFF_STAT + (L) * 524288 + (p * 32 + h) * 2048 - 2048) + tid * 16, 0); \
  }
  u32x4 gv[4], uv[4];
  u32x4 keep[9];
  u32x4 pre[4][2];
#define PRE_LOAD(it_, rot_, s0, s1)                                                                  \
  static_for<(s1) - (s0)>([&](auto c_) __attribute__((always_inline)) {                              \
    constexpr int s = (s0) + decltype(c_)::value;                                                    \
    const unsigned ab_ = OFF_XN + (it_) * (4u << 20) + (((rot_) + (s >> 2)) & 7) * 8192 + ((s >> 1) & 1) * 4096; \
    ld16w<2 * s, (s & 1) * 2048>(pre[s][0], rs, l * 16, ab_ + (p * 8 + rt) * 65536);                         \
    ld16w<2 * s + 1, (s & 1) * 2048 + 1024>(pre[s][1], rs, l * 16, ab_ + (p * 8 + rt) * 65536);                 \
  })
  u32x2 T;
  const GAS bf16* tsrc;
  unsigned xv[2], xa[2];
  u32x2 ep;
  u32x4 rows0[4];
#define X_READ(u, d0, d1) { d0 = *(const LDS_AS u32x4*)(size_t)(xa[0] + x_slot(u)); d1 = *(const LDS_AS u32x4*)(size_t)(xa[1] + x_slot(u)); }
  unsigned g1[5], wqv0;
  {
    IDS();
    const int rot = PROT(), qa = WQ_KMAJ(w), qb = WQ_KMAJ(8 + w);
    wqv0 = WQ_VOFF();
    xv[0] = (l >> 3) * 2048 + (((l & 7) ^ (l >> 4)) << 4); xv[1] = xv[0] ^ 64;
    g1[0] = (rot * 128 + (qa & 7) * 16) * 6144 + (qa >> 3) * 2048 + h * 64; g1[1] = lbase + L_RING + qa * 1024;
    g1[2] = (rot * 128 + (qb & 7) * 16) * 6144 + (qb >> 3) * 2048 + h * 64; g1[3] = lbase + L_RING + qb * 1024;
    g1[4] = (p * 256 + rt * 32) * 2048 + rot * 256;
    asm volatile("" : "+v"(wqv0), "+v"(xv[0]), "+v"(xv[1]));
    asm volatile("s_waitcnt lgkmcnt(0)" : "+s"(a01), "+s"(a2), "+s"(g1[0]), "+s"(g1[1]), "+s"(g1[2]), "+s"(g1[3]), "+s"(g1[4]));
  }
  x = (const bf16*)a01[0]; ws = (char*)a01[1]; wqkv = (const bf16*)a2;
  const rsrc_t rs = __builtin_amdgcn_make_buffer_rsrc((void*)ws, 0, WS_BYTES, 0x00020000);
  const rsrc_t rx = RSRC(x, 2048 * 1024 * 2);
  {
    IDS();
    const int rot = PROT();
    const bf16* pwq = wqkv;
    const unsigned wqv = wqv0;
    dma16(rwq, wqv, g1[0], g1[1]);
#pragma unroll
    for (int i = 0; i < 4; ++i) dma16(rx, xv[i & 1], g1[4] + i * 16384, lbase + rt * 2048 + x_slot(i >> 1) + (i & 1) * 1024);
    dma16(rwq, wqv, g1[2], g1[3]);
    asm volatile("s_barrier" ::: "memory");
    WQ_PIECE(0, WQ_KMAJ(16 + w), L_RING);
    if (w == 6 && bi == 0) {
      ARGP(pwr, 4);
      asm volatile("s_mov_b32 m0, %0\n\ts_nop 0\n\tbuffer_load_dword %1, %2, 0 offen lds" ::"s"(lbase + L_TOUCH0 + 256), "v"((l & 15) * 128), "s"(RSRC(pwr, 2048)) : "memory");
    }
    X_REQ(2, 0); X_REQ(2, 1);
    asm volatile("buffer_load_dwordx2 %0, off, %1, 0 sc1" : "=v"(ep) : "s"(rs) : "memory");
    if constexpr (RWL == -1) {
      asm volatile("" : "=v"(rows0[0]), "=v"(rows0[1]), "=v"(rows0[2]), "=v"(rows0[3]));
      if (w < 4) ldrows(rows0, rx, (l >> 5) * 2048 + (l & 31) * 16, (p * 256 + h * 8 + 2 * w) * 2048);
    }
    if constexpr (PBS < 0) { POISON_BIG(0); }
    {
      ARGP(pwo, 3); ARGP(pwg, 5); ARGP(pwu, 6);
      tsrc = (w < 4 ? pwg : pwu) + ((bi * 32 + ((tid & 255) >> 3)) * F_ + (slice >> 5) * 512 + (tid & 7) * 64);
      if (!ffn_on) tsrc = pwo + (bi * 512 + tid) * 64;
      asm volatile("" : "+v"(tsrc));
    }
#pragma unroll
    for (int kk = 0; kk < 2; ++kk) {
      const int r = l & 15;
      xa[kk] = lbase + rt * 2048 + (r >> 3) * 1024 + (r & 7) * 128 + (((4 * kk + g) ^ (r >> 1)) << 4);
      asm volatile("" : "+v"(xa[kk]));
    }
    if (tid == 192) *(LDS_AS u32x4*)(smem + L_QC) = u32x4{0u, 0u, 0u, 0u};
    if (RWL == -1 && w < 4) wait_vm<8>(); else wait_vm<4>();
    X_READ(0, pre[0][0], pre[1][0]); X_READ(1, pre[0][1], pre[1][1]);
    asm volatile("s_waitcnt lgkmcnt(0)\n\ts_barrier" : "+v"(pre[0][0]), "+v"(pre[0][1]), "+v"(pre[1][0]), "+v"(pre[1][1])::"memory");
    if constexpr (RWL == -2) {
      asm volatile("" : "=v"(rows0[0]), "=v"(rows0[1]), "=v"(rows0[2]), "=v"(rows0[3]));
      if (w < 4) ldrows(rows0, rx, (l >> 5) * 2048 + (l & 31) * 16, (p * 256 + h * 8 + 2 * w) * 2048);
    }
    X_REQ(3, 0); X_REQ(3, 1); X_REQ(4, 0); X_REQ(4, 1);
  }
  unsigned sel0 = 0x7FFFFFFFu;
  u32x4 x2p[2];
  unsigned xm0 = 0, kg0 = 0;

  auto layer = [&](auto it_c) __attribute__((always_inline)) {
    constexpr int it = decltype(it_c)::value;
    f32x4 acc[2][6];
    f32x2 qs2[2];
    u32x4 k0, k1, v0, v1;
    unsigned tdum = 0;
    {
      IDS();
      bool aff = false;
      unsigned aq[2];
      u32x4 av[2];
#define PIN_ACC() asm volatile("" : "+{v[176:179]}"(acc[0][0]), "+{v[180:183]}"(acc[0][1]), "+{v[184:187]}"(acc[0][2]), "+{v[188:191]}"(acc[0][3]), "+{v[192:195]}"(acc[0][4]), "+{v[196:199]}"(acc[0][5]), \
                               "+{v[200:203]}"(acc[1][0]), "+{v[204:207]}"(acc[1][1]), "+{v[208:211]}"(acc[1][2]), "+{v[212:215]}"(acc[1][3]), "+{v[216:219]}"(acc[1][4]), "+{v[220:223]}"(acc[1][5]))
#define PINV4(x) asm volatile("" : "+v"(x[0]), "+v"(x[1]), "+v"(x[2]), "+v"(x[3])::"memory")
      auto qkv_loop = [&](auto it_c) __attribute__((always_inline)) {
        constexpr int IT = decltype(it_c)::value;
        const unsigned abase = OFF_XN + IT * (4u << 20) + (p * 8 + rt) * 65536;
        const int rot = PROT() + 5 * IT;
        unsigned fkey = 0, kv, bq[2];
        u32x4 fx[4], bv[2], af[WA][2];
        u32x2 cxn, cx1;
        f32x4 sa, sb, cs4[2];
        f32x2 b2[2];
#define AB_OFF() (OFF_AB + IT * 16384 + (p * 8 + rt) * 128)
#define A_LOAD1(k, a) ld16w<(k) % WA * 2 + (a), ((k) & 1) * 2048 + (a) * 1024>(af[(k) % WA][a], rs, l * 16, abase + ((((k) >> 2) + rot) & 7) * 8192 + (((k) >> 1) & 1) * 4096)
#pragma unroll
        for (int s = 0; s < 2 + 2 * IT; ++s) { af[s][0] = pre[s][0]; af[s][1] = pre[s][1]; }
        const bf16* pwq = wqkv;
        const int tstep = TS0 + bid % TSN;
        const unsigned wqv = WQ_VOFF();
#define QKV_DMA1(t, i)                                                                            \
  {                                                                                               \
    if constexpr (IT == 0) WQ_PIECE(t, 3 * w + (i), L_RING + SLOT(t) * STAGE);                    \
    else dma16s<(i) * 1024>(rs, l * 16, OFF_WI + (h * 8 + (((t) + rot) & 7)) * STAGE + w * 3072, lbase + L_RING + SLOT(t) * STAGE + w * 3072); \
  }
        unsigned bo[2][2];
#pragma unroll
        for (int dt = 0; dt < 2 - IT; ++dt) {
          bo[dt][0] = lbase + L_RING + (IT ? l * 16 : WQ_BOFF(dt)); bo[dt][1] = bo[dt][0] + 2 * STAGE;
          asm volatile("" : "+v"(bo[dt][0]), "+v"(bo[dt][1]));
        }
#define FRAG_T() (lbase + L_RING + (4 * g + ((l & 15) >> 2)) * 64 + (l & 3) * 8 + (((g ^ (g >> 1)) & 1) << 5))
        auto frag_t2 = [&](unsigned o) __attribute__((always_inline)) {
          frag_t t;
          t.hf.a = trabs(o); t.hf.b = trabs(o + 1024);
          return t.v;
        };
        unsigned ka[3];
        auto tile_sum = [&](u32x4 pt, int dst) __attribute__((always_inline)) {
          f32x4 c = __builtin_bit_cast(f32x4, pt);
#pragma unroll
          for (int e = 0; e < 4; ++e) { c[e] += dppf<0x124>(c[e]); c[e] += dppf<0x128>(c[e]); c[e] = add_x16(c[e]); }
          if ((l & 28) == 0) *(LDS_AS f32x4*)(smem + dst + (w * 2 + (l >> 5)) * 64 + (l & 3) * 16) = c;
        };
        frag_t bfr[6];
#define QKV_BO(s, n) (bo[(n) & 1][SLOT((s) >> 2) >> 1] + (SLOT((s) >> 2) & 1) * STAGE + ((n) >> 1) * 8192 + ((s) & 3) * 2048)
#define QKV_RDA(s, n)                                                                             \
  {                                                                                               \
    if constexpr (IT == 0) bfr[n].hf.a = trabs(QKV_BO(s, n));                                     \
    else bfr[n].v = *(const LDS_AS u32x4*)(size_t)(bo[0][SLOT((s) >> 2) >> 1] + (SLOT((s) >> 2) & 1) * STAGE + (((s) & 3) * 6 + (n)) * 1024); \
  }
#define QKV_RDB(s, n) { if constexpr (IT == 0) bfr[n].hf.b = trabs(QKV_BO(s, (n) ^ 1) + 256); }
#pragma unroll
        for (int n = 0; n < 6; ++n) { QKV_RDA(0, n); if (n < 5) QKV_RDB(0, n); }
#define XF_OFF() (l & 8 ? ((l & 7) * RX + bi % RX) * 256 + h * 8 : (p * RX + (xcd >> 1) % RX) * 256 + (4 * rt + (l & 3)) * 8)
#define XP_OFF() (OFF_XNEW + ((rot + FXT) & 7) * 256)
#define FP_OFF(i) (w == 3 ? OFF_XNEW + (i) * 1024 : OFF_FX + h * 3072 + w * 1024)
#define FX_OK(n) __all(((n) == 4 ? max(max(umax4(fx[0]), umax4(fx[1])), max(umax4(fx[2]), umax4(fx[3]))) : max(umax4(fx[0]), umax4(fx[1]))) != POISON)
#define SINCE(s0) (vm_at(IT, s, n) - vm_at(IT, s0, 5))
#define DEF4(x) asm volatile("" : "=v"(x[0]), "=v"(x[1]), "=v"(x[2]), "=v"(x[3]))
        LDS_AS unsigned* qcnt = (LDS_AS unsigned*)(smem + L_QC);
        unsigned qc;
        unsigned qadr = lbase + L_QC, qone = 1;
        asm volatile("" : "+v"(qadr), "+v"(qone));
#define count_in(i) asm volatile("s_mov_b64 exec, 1\n\tds_add_u32 %0, %1 offset:%2\n\ts_mov_b64 exec, -1" ::"v"(qadr), "v"(qone), "n"(4 * (i)) : "memory")
        auto count_wait = [&](int i, unsigned want) __attribute__((always_inline)) {
          if (__builtin_expect((unsigned)__builtin_amdgcn_readfirstlane(qc) < want, 0)) {
            while ((unsigned)__builtin_amdgcn_readfirstlane(__hip_atomic_load(qcnt + i, __ATOMIC_RELAXED, __HIP_MEMORY_SCOPE_WORKGROUP)) < want) {}
          }
          asm volatile("" ::: "memory");
        };
        unsigned long long xok = xm0;
        auto x1_need = [&](unsigned long long m) __attribute__((always_inline)) {
          if (__builtin_expect((xok & m) != m, 0)) {
            for (unsigned spins = 0;;) {
              const u32x2 f = ld8(rs, F_X1 + ((p * RX + (xcd >> 1) % RX) * 32 + (l & 31)) * SX, 0);
              xok = __builtin_amdgcn_ballot_w64(f[0] == T[0] && f[1] == T[1]);
              if ((xok & m) == m) break;
              if (spin_fail(spins, rs, T)) { xok = ~0ull; break; }
              __builtin_amdgcn_s_sleep(KSL);
            }
            asm volatile("" ::: "memory");
          }
        };
        auto gap = [&](auto sc_, auto nc_) __attribute__((always_inline)) {
          constexpr int s = decltype(sc_)::value, n = decltype(nc_)::value;
          {
            constexpr int ts = (s + 3) / 4, tc = (s + 2) / 4, t0 = 1;
            if constexpr (ts >= 3 && ts < 8 && s == 4 * ts - 3 && n == 2) { wait_vm<vm_at(IT, s, n) - vm_tile(IT, ts)>(); count_in(0); }
            if constexpr (!IT && tc >= t0 && tc < 3 && s == 4 * tc - 2 && n == 1) { wait_vm<vm_at(IT, s, n) - vm_tile(IT, tc)>(); count_in(0); }
            if constexpr (IT && ts >= t0 && ts < 3 && s == 4 * ts - 3 && n == 2) count_in(0);
            if constexpr (tc >= t0 && tc < 8 && s == 4 * tc - 2) {
              if constexpr (n == 3) qc = __hip_atomic_load(qcnt, __ATOMIC_RELAXED, __HIP_MEMORY_SCOPE_WORKGROUP);
              if constexpr (n == 5) count_wait(0, 8 * (tc - t0 + 1));
            }
          }
          if constexpr (n == 0 && tile_piece(s)) QKV_DMA1(((s - 3) >> 2) + 3, (s - 3) & 3);
          if constexpr (IT == 0) {
            if constexpr (s == 0 && !(n & 1)) QKV_DMA1(1, n >> 1);
            if constexpr (s >= 1 && s < 4 && n == 2) QKV_DMA1(2, s - 1);
            constexpr int u = s + XL;
            if constexpr (n == 1 && u < 32) {
              if (s == 0 && RWL < 0 && w < 4) wait_vm<vm_at(0, s, n) - vm_x(u) + 4>(); else wait_vm<vm_at(0, s, n) - vm_x(u)>();
              X_READ(u, af[(u & ~1) % WA][u & 1], af[(u | 1) % WA][u & 1]);
            }
            if constexpr (n == 3 && u + XU < 32) { asm volatile("s_waitcnt lgkmcnt(%0)" ::"n"(s + 1 < QL ? 4 : 3) : "memory"); X_REQ(u + XU, 0); }
            if constexpr (n == 5 && u + XU < 32) X_REQ(u + XU, 1);
            if constexpr (s == WTS && n == WTG) {
              const int i = bi * 192 + w * 24 + min(l, 23);
              touch(tdum, (const GAS bf16*)pwq + (size_t)(i / 12 + 512 * (~xcd & 1)) * 3072 + ((i % 12) >> 2) * 1024 + (xcd >> 1) * 256 + (i & 3) * 64);
            }
            if constexpr (s == PBS && n == 0) { POISON_BIG(0); }
            if constexpr (s == 0 && n == 1) count_in(2);
            if constexpr (s == 0 && n == 3) qc = __hip_atomic_load(qcnt + 2, __ATOMIC_RELAXED, __HIP_MEMORY_SCOPE_WORKGROUP);
            if constexpr (s == 0 && n == 5) count_wait(2, 8);
            if constexpr (s == 1 && n == 1) {
              asm volatile("" : "+v"(ep)::"memory");
              const unsigned long long e = (((unsigned long long)(unsigned)__builtin_amdgcn_readfirstlane(ep[1]) << 32) | (unsigned)__builtin_amdgcn_readfirstlane(ep[0])) + 1ull + dispatch_id() * 0x9E3779B97F4A7C16ull;
              T = u32x2{(unsigned)e, (unsigned)(e >> 32)};
            }
            if constexpr (s == RWL && n == 4) {
              DEF4(fx);
              if (w < 4) ldrows(fx, rx, (l >> 5) * 2048 + (l & 31) * 16, (p * 256 + h * 8 + 2 * w) * 2048);
            }
            if constexpr (s == RWS && n == RWG) {
              if (w < 4) {
                if constexpr (RWL >= 0) wait_vm<SINCE(RWL < 0 ? 0 : RWL)>();
                else { fx[0] = rows0[0]; fx[1] = rows0[1]; fx[2] = rows0[2]; fx[3] = rows0[3]; }
                PINV4(fx);
                f32x2 sv[16];
#pragma unroll
                for (int i = 0; i < 4; ++i) unpack8v(fx[i], sv + 4 * i);
                f32x2 t0 = sv[0] + sv[1], t1 = sv[2] + sv[3];
#pragma unroll
                for (int j = 4; j < 16; j += 2) { t0 += sv[j]; t1 += sv[j + 1]; }
                const float m = sum32(hadd(t0 + t1)) * (1.0f / 1024.0f);
                f32x2 m2 = {m, m}, q0 = {0.f, 0.f}, q1 = {0.f, 0.f};
                asm volatile("" : "+v"(m2));
#pragma unroll
                for (int j = 0; j < 16; j += 2) {
                  const f32x2 d0 = sv[j] - m2, d1 = sv[j + 1] - m2;
                  q0 = __builtin_elementwise_fma(d0, d0, q0); q1 = __builtin_elementwise_fma(d1, d1, q1);
                }
                const float rd = ln_rd(sum32(hadd(q0 + q1)) * (1.0f / 1024.0f));
                if ((l & 30) == 0) st4(__builtin_bit_cast(unsigned, (l & 1) ? -rbf1(m) : rd), rs, (l >> 5) * 4 + (l & 1) * 8192, OFF_AB + (p * 256 + h * 8 + 2 * w) * 4);
              }
            }
            if constexpr (s == POI && n == 4) {
              if (w == 3) {
                unsigned pz1 = POISON;
                asm volatile("" : "+v"(pz1));
                const unsigned prb = 2 * p + (h >> 4), pct = h & 15, m = l - 32;
                static_assert(OFF_CB == OFF_CA + 131072, "poison addressing");
                if (l < 60) st16(u32x4{pz1, pz1, pz1, pz1}, rs, l < 32 ? OFF_CA + (l & 8) * 4096 + (l & 16) * 8192 + prb * 2048 + pct * 128 + (l & 7) * 16
                                                             : l < 56 ? OFF_FX + ((h * 6 + (m >> 2)) * 8 + p) * 64 + (l & 3) * 16
                                                                      : OFF_AB + 16384 + (p * 32 + h) * 32 + (l & 1) * 16 + (l & 2) * 4096, 0);
                if (l < 4) st4(pz1, rs, l < 2 ? OFF_XNEW + bid * 8 + l * 4 : OFF_KEY + ((l & 1) * 16 + pct) * cmax(16 * SK, 128) + prb * SK, 0);
              }
            }
            if constexpr (s == POI && n == 2) {
              if (w == 2 && l < 6) {
                unsigned f0 = F_KV + KVF(0, p * 32 + h), f1 = F_O + OFB(0, p) + OFL(h), f2 = F_X1 + ((rb >> 1) * RX * 32 + (l & 3) * 32 + (rb & 1) * 16 + ct) * SX;
                asm volatile("" : "+v"(f0), "+v"(f1), "+v"(f2));
                const unsigned fo = l == 5 ? f0 : l == 4 ? f1 : f2;
                touchb(tdum, rs, fo);
              }
            }
            static_assert(vm_x(EIN + XL) >= vm_at(0, EIN - 3, 5) && IMG >= 1 && CSD >= 1, "first flag");
            if constexpr (s == EIN && n == 1) { if (w < 4) count_in(1); }
            if constexpr (s == EIN + 2 && n == 0) qc = __hip_atomic_load(qcnt + 1, __ATOMIC_RELAXED, __HIP_MEMORY_SCOPE_WORKGROUP);
            if constexpr (s == EIN + 2 && n == 2) {
              if (w == 0) { count_wait(1, 4); if (l < RX) st8(T, rs, F_XN + (p * RX + l) * 256 + h * 8, 0); }
            }
            if constexpr (s == ERQ && n == 4) asm volatile("buffer_load_dwordx2 %0, %1, %2, 0 offen sc1" : "=v"(cxn) : "v"(XF_OFF() + F_XN), "s"(rs) : "memory");
            if constexpr (s == CSL && n == 2) {
              wait_vm<SINCE(ERQ)>();
              asm volatile("" : "+v"(cxn)::"memory");
              if (!__all(cxn[0] == T[0] && cxn[1] == T[1])) poll_flags(rs, XF_OFF(), F_XN, T);
            }
          } else {
#define X1_OFF() (F_X1 + ((p * RX + (xcd >> 1) % RX) * 32 + (l & 31)) * SX)
            if constexpr ((s == 0 || s == 4) && n == 2) asm volatile("buffer_load_dwordx2 %0, %1, %2, 0 offen sc1" : "=v"(cxn) : "v"(X1_OFF()), "s"(rs) : "memory");
            if constexpr ((s == 3 || s == 7) && n == 0) {
              wait_vm<vm_at(1, s, 1) - vm_at(1, s - 3, 3)>();
              asm volatile("" : "+v"(cxn)::"memory");
              xok |= __builtin_amdgcn_ballot_w64(cxn[0] == T[0] && cxn[1] == T[1]);
            }
            if constexpr (a_req(s, n) >= 0 && (a_req(s, n) & 7) == 0) x1_need(0x30003ull << (((rot + a_req(s, n) / 8) & 7) * 2));
          }
          if constexpr (IT && a_req(s, n) >= 0) A_LOAD1(a_req(s, n) / 2, a_req(s, n) & 1);
          if constexpr (IT == 0 && s == CSD && n == CSG) {
            if (w < 3) {
              unsigned a0 = FRAG_T() + SLOT(s >> 2) * STAGE + w * 8192, a1 = a0 ^ 32, one2 = 0x3F803F80u;
              asm volatile("" : "+v"(a0), "+v"(a1), "+v"(one2));
              const u32x4 o4 = {one2, one2, one2, one2};
              f32x4 y0 = {0.f, 0.f, 0.f, 0.f}, y1 = y0;
#pragma unroll
              for (int i = 0; i < 4; ++i) { y0 = mfma16(frag_t2(a0 + i * 2048), o4, y0); y1 = mfma16(frag_t2(a1 + i * 2048), o4, y1); }
              if ((l & 14) == 0) st16(__builtin_bit_cast(u32x4, (l & 1) ? y1 : y0), rs, g * 16 + (l & 1) * 512, OFF_CS + ((h * 6 + 2 * w) * 8 + p) * 64);
            }
          }
          if constexpr (!IT && s == CSL && n == 4) { asm volatile("" : "=v"(fx[0])); if (w < 3) ld16h<0>(fx[0], rs, l * 16, OFF_CS + h * 3072 + w * 1024); }
          if constexpr (!IT && s == CSS && n == 2) {
            if (w < 3) {
              wait_vm<SINCE(CSL)>();
              asm volatile("" : "+v"(fx[0])::"memory");
              tile_sum(fx[0], L_CSL);
            }
          }
          if constexpr (s == abl(IT) && n == 4) ld4h<0>(aq[0], rs, (l & 31) * 4 + (l >> 5) * 8192, AB_OFF());
          if constexpr (s == ABS && n == 0) {
            wait_vm<SINCE(abl(IT))>();
            asm volatile("" : "+v"(aq[0])::"memory");
            if constexpr (IT) {
              for (unsigned spins = 0; !__all(aq[0] != POISON) && !spin_fail(spins, rs, T);) {
                ld4h<0>(aq[0], rs, (l & 31) * 4 + (l >> 5) * 8192, AB_OFF());
                asm volatile("s_waitcnt vmcnt(0)" : "+v"(aq[0])::"memory");
              }
            }
            *(LDS_AS unsigned*)(smem + L_K + (8 + rt) * 2048 + l * 4) = aq[0];
          }
          if constexpr (s == ABS && n == 2) {
#pragma unroll
            for (int a = 0; a < 2; ++a) {
              const LDS_AS char* ab = (const LDS_AS char*)(smem + L_K + (8 + rt) * 2048 + a * 64);
              aq[a] = *(const LDS_AS unsigned*)(ab + (l & 15) * 4); bq[a] = *(const LDS_AS unsigned*)(ab + 128 + (l & 15) * 4);
              av[a] = *(const LDS_AS u32x4*)(ab + g * 16); bv[a] = *(const LDS_AS u32x4*)(ab + 128 + g * 16);
            }
          }
          if constexpr (s == ABS && n == 5) {
#pragma unroll
            for (int a = 0; a < 2; ++a) { b2[a] = f32x2{asf(bq[a]), asf(bq[a])}; asm volatile("" : "+v"(b2[a])); }
          }
          if constexpr (s == ABS + 1) {
            constexpr int k = (n + 4) % 6;
            if constexpr (k < 4) cs4[k & 1] = *(const LDS_AS f32x4*)(smem + L_CSL + k * 64 + g * 16);
            else cs4[k & 1][0] = *(const LDS_AS float*)(smem + L_CSL + k * 64 + (l & 15) * 4);
          }
          if constexpr ((s == ABS + 1 && n > 0) || (s == ABS + 2 && n == 0)) {
            constexpr int k = (n + 3) % 6;
            if constexpr (k < 4) {
#pragma unroll
              for (int a = 0; a < 2; ++a) acc[a][k] = __builtin_elementwise_fma(cs4[k & 1], f32x4{b2[a][0], b2[a][1], b2[a][0], b2[a][1]}, acc[a][k]);
            } else {
              f32x2 c2 = {cs4[k & 1][0], cs4[k & 1][0]};
              asm volatile("" : "+v"(c2));
#pragma unroll
              for (int a = 0; a < 2; ++a) acc[a][k] = __builtin_elementwise_fma(f32x4{c2[0], c2[1], c2[0], c2[1]}, __builtin_bit_cast(f32x4, bv[a]), acc[a][k]);
            }
          }
          if constexpr (IT == 0) {
            if constexpr (s == IMG - 2 && n < 3) {
              const int c = 3 * (w & 1) + n;
              ka[n] = (FRAG_T() ^ ((c & 1) << 5)) + (SLOT(5) * STAGE + (c >> 1) * 8192 + (w >> 1) * 2048);
              asm volatile("" : "+v"(ka[n]));
            }
            if constexpr (s == IMG - 1 && (n & 1)) keep[n >> 1] = frag_t2(ka[n >> 1]);
            if constexpr (s > KP0 && s < KP0 + 3 && (n & 1)) keep[3 * (s - KP0) + (n >> 1)] = frag_t2(ka[n >> 1] + (s - KP0) * STAGE);
            if constexpr (s >= IMG && s < IMG + 3 && n == 4) st16(keep[s - IMG], rs, l * 16 + (s - IMG) * 1024, OFF_WI + (h * 8 + ((rot + 5) & 7)) * STAGE + w * 3072);
            if constexpr (s >= TS0 && s < TS0 + TSN && n == 4) {
              if (tstep == s && w < (ffn_on ? TW : TWR)) touch(tdum, tsrc);
            }
            if constexpr (s == TST && n == 4) { if (w >= (ffn_on ? TW : TWR)) touch(tdum, tsrc); }
          } else {
            if constexpr (s == PB1 && n == 0) { POISON_BIG(1); }
            if constexpr (s == FSL && n == 4) { ld4h<0>(kv, rs, (l & 15) * SK, OFF_KEY + (bid & 15) * cmax(16 * SK, 128)); row_load(rs, g, ffn_xoff(kg0, 0, w), fx); }
            if constexpr (s == FS0 && n == 0) {
              wait_vm<SINCE(FSL)>();
              asm volatile("" : "+v"(kv)::"memory");
              PINV4(fx);
              if (__builtin_expect(!__all(kv != POISON), 0)) {
                LDS_AS unsigned* kw = (LDS_AS unsigned*)(smem + L_SKEY + 40);
                if (w == 0) {
                  kv = key_wait(rs, T, l, 0, bid & 15, kv);
                } else {
                  while ((kv = __hip_atomic_load(kw, __ATOMIC_RELAXED, __HIP_MEMORY_SCOPE_WORKGROUP)) == POISON) __builtin_amdgcn_s_sleep(2);
                }
              }
              fkey = __builtin_amdgcn_readfirstlane(umax16(kv));
              if (w == 0) *(LDS_AS unsigned*)(smem + L_SKEY + 40) = min(fkey, POISON - 1);
              sel0 = key_row(fkey);
              aff = (int)(sel0 >> 8) == p;
              asm volatile("" : "=v"(sa), "=v"(sb));
              if (ffn_on) {
                if (__builtin_expect(((kg0 ^ fkey) & 0x780u) != 0, 0)) ld4x16(fx, rs, g * 16, ffn_xoff(fkey, 0, w));
                ffn_gu(gv, uv, fx, sa, sb);
              }
            }
            if constexpr (s == FS0 && n == 2) {
              if (ffn_on) {
                ffn_gu_check(rs, T, ffn_xoff(fkey, 0, w), l, gv, uv, fx, sa, sb);
                ffn_gu_store(smem, l, w, sa, sb);
                count_in(1);
              }
            }
            if constexpr (s == FS0D && n == FDG) {
              if (ffn_on && w < 4) {
                s16x4 da[8], db[8];
                ffn_d_read(smem, l, 2 * w, da);
                while (__hip_atomic_load(qcnt + 1, __ATOMIC_RELAXED, __HIP_MEMORY_SCOPE_WORKGROUP) < 8u) {}
                asm volatile("" ::: "memory");
                const f32x4 t = ffn_t_read(smem, l);
                ffn_d_read(smem, l, 2 * w + 1, db);
                u32x2 ha = ffn_h(t, l), hb = {dppu<0x118>(ha[0]), dppu<0x118>(ha[1])};
                f32x4 ya = {0.f, 0.f, 0.f, 0.f}, yb = ya;
#pragma unroll
                for (int i = 0; i < 8; ++i) {
                  ya = mfma16k16(da[i], __builtin_bit_cast(s16x4, ha), ya); yb = mfma16k16(db[i], __builtin_bit_cast(s16x4, hb), yb);
                  ha = u32x2{dppu<0x111>(ha[0]), dppu<0x111>(ha[1])}; hb = u32x2{dppu<0x111>(hb[0]), dppu<0x111>(hb[1])};
                }
                ffn_d_store(rs, l, 8 * w, 0, slice, ya + yb);
              }
            }
#define RED_LOAD(ld, l8)                                                                                                    \
  {                                                                                                                         \
    const unsigned G = bi, po = OFF_PART + G * 32768;                                                        \
    ld(gv[0], rs, tid * 16, po); ld(gv[1], rs, tid * 16, po + 4096); ld(gv[2], rs, tid * 16, po + 8192); ld(gv[3], rs, tid * 16, po + 12288); \
    ld(uv[0], rs, tid * 16, po + 16384); ld(uv[1], rs, tid * 16, po + 20480); ld(uv[2], rs, tid * 16, po + 24576);        \
    l8(cxn, rs, (l & 3) * 16 + (l & 4) * 2, OFF_CA + (sel0 >> 7) * 2048 + G * 64);                                         \
    l8(cx1, rs, (l & 3) * 16 + (l & 4) * 2, OFF_CB + (sel0 >> 7) * 2048 + G * 64);                                          \
  }
            if constexpr (s == FS1 && n == 4) {
              asm volatile("" : "=v"(cxn), "=v"(cx1));
              if (!ffn_on && w < 4) RED_LOAD(ld16u, ld8h);
            }
            if constexpr (s == FS1S && n == 0) {
              if (!ffn_on && w < 4) {
                wait_vm<SINCE(FS1)>();
                PINV4(gv); PINV4(uv);
                asm volatile("" : "+v"(cxn), "+v"(cx1)::"memory");
                for (unsigned spins = 0; !__all(max(max(max(umax4(gv[0]), umax4(gv[1])), max(umax4(gv[2]), umax4(gv[3]))),
                                                    max(max(umax4(uv[0]), umax4(uv[1])), max(max(umax4(uv[2]), max(cxn[0], cxn[1])), max(cx1[0], cx1[1])))) != POISON) && !spin_fail(spins, rs, T);) {
                  RED_LOAD(ld16u, ld8h);
                  DRAIN();
                  PINV4(gv); PINV4(uv);
                  asm volatile("" : "+v"(cxn), "+v"(cx1)::"memory");
                }
#define F4(x) __builtin_bit_cast(f32x4, x)
                ((LDS_AS f32x4*)(smem + L_SFIN))[tid] = ((F4(gv[0]) + F4(gv[1])) + (F4(gv[2]) + F4(gv[3]))) + ((F4(uv[0]) + F4(uv[1])) + F4(uv[2]));
#undef F4
                count_in(2);
                if (w == 0) {
                  while (__hip_atomic_load(qcnt + 2, __ATOMIC_RELAXED, __HIP_MEMORY_SCOPE_WORKGROUP) < 4u) {}
                  asm volatile("" ::: "memory");
                  const u32x2 ov = red_final<4>(smem, l, fkey, cxn, cx1);
                  if (l < 8) st8(unpoison2(ov), rs, OFF_XNEW + (bi * 8 + (l & 3) * 2 + (l >> 2)) * 8, 0);
                }
              }
            }
            if constexpr (s == FXA && n == 4) { DEF4(fx); if (w < 3) row_load(rs, g, XP_OFF(), fx); }
            if constexpr (s == FXB && n == 0) {
              if (w < 3) {
                const unsigned fa = bo[0][0] + SLOT(FXT) * STAGE + w * 2048;
                u32x4 f[8];
#pragma unroll
                for (int i = 0; i < 8; ++i) f[i] = *(const LDS_AS u32x4*)(size_t)(fa + (i >> 1) * 6144 + (i & 1) * 1024);
                wait_vm<SINCE(FXA)>();
                PINV4(fx);
                ffn_xwait<XSL>(rs, T, XP_OFF(), l, fx);
                f32x4 y0 = mfma16(f[0], fx[0], f32x4{0.f, 0.f, 0.f, 0.f}), y1 = mfma16(f[1], fx[0], f32x4{0.f, 0.f, 0.f, 0.f});
#pragma unroll
                for (int i = 1; i < 4; ++i) { y0 = mfma16(f[2 * i], fx[i], y0); y1 = mfma16(f[2 * i + 1], fx[i], y1); }
                if ((l & 14) == 0) st16(unpoison4(__builtin_bit_cast(u32x4, (l & 1) ? y1 : y0)), rs, g * 16 + (l & 1) * 512, OFF_FX + ((h * 6 + 2 * w) * 8 + p) * 64);
              }
            }
            if constexpr (s == FXL && n == 4) {
              asm volatile("" : "=v"(fx[0]), "=v"(fx[1]));
              if (aff && w < 4) { ld16h<0>(fx[0], rs, l * 16, FP_OFF(0)); ld16h<0>(fx[1], rs, l * 16, FP_OFF(1)); }
            }
            if constexpr (s == FXS && n == 2) {
              if (aff && w < 4) {
                wait_vm<0>();
                asm volatile("" : "+v"(fx[0]), "+v"(fx[1])::"memory");
                if (!FX_OK(2)) {
                  unsigned spins = 0;
                  do {
                    if (spin_fail(spins, rs, T)) break;
                    fx[0] = ld16(rs, l * 16, FP_OFF(0)); fx[1] = ld16(rs, l * 16, FP_OFF(1));
                  } while (!FX_OK(2));
                }
                if (w == 3) {
                  f32x2 nx[8];
                  unpack8v(fx[0], nx); unpack8v(fx[1], nx + 4);
                  const f32x2 sm = ((nx[0] + nx[1]) + (nx[2] + nx[3])) + ((nx[4] + nx[5]) + (nx[6] + nx[7]));
                  const float mean = sum64(hadd(sm)) * (1.0f / 1024.0f);
                  const f32x2 m2 = {mean, mean};
                  f32x2 q = {0.f, 0.f};
#pragma unroll
                  for (int j = 0; j < 8; ++j) { const f32x2 d = nx[j] - m2; q = __builtin_elementwise_fma(d, d, q); }
                  const float rd = ln_rd(sum64(hadd(q)) * (1.0f / 1024.0f));
                  if (l == 0) *(LDS_AS f32x2*)(smem + L_FXL + 384) = f32x2{rd, -rbf1(mean)};
                } else {
                  tile_sum(fx[0], L_FXL);
                }
                count_in(3);
              }
            }
          }
        };
        static_for<32>([&](auto sc_) __attribute__((always_inline)) {
          constexpr int s = decltype(sc_)::value;
          if constexpr (s > 0) PIN_ACC();
          if constexpr (IT == 1) wait_vm<vm_at(IT, s, 0) - vm_a(IT, s)>();
          pin_w<s % WA>(af[s % WA][0], af[s % WA][1]);
          SB();
          static_for<6>([&](auto nc_) __attribute__((always_inline)) {
            constexpr int n = decltype(nc_)::value;
            if constexpr (s == 0) { acc[0][n] = f32x4{0.f, 0.f, 0.f, 0.f}; acc[1][n] = acc[0][n]; }
            constexpr bool on = s < QL || n >= 2;
            if constexpr (on) acc[0][n] = n < 4 ? mfma16(bfr[n].v, af[s % WA][0], acc[0][n]) : mfma16(af[s % WA][0], bfr[n].v, acc[0][n]);
            SB();
            if constexpr (n == 0) { QKV_RDB(s, 5); }
            else if constexpr (s + 1 < 32 && (s + 1 < QL || n > 2)) QKV_RDB(s + 1, n - 1);
            SB();
            if constexpr (on) acc[1][n] = n < 4 ? mfma16(bfr[n].v, af[s % WA][1], acc[1][n]) : mfma16(af[s % WA][1], bfr[n].v, acc[1][n]);
            SB();
            if constexpr (s + 1 < 32 && (s + 1 < QL || n >= 2)) QKV_RDA(s + 1, n);
            SB();
            gap(sc_, nc_);
            SB();
          });
        });
        PIN_ACC();
        frag_t qw[32 - QL][2];
        static_for<32 - QL>([&](auto c_) __attribute__((always_inline)) {
          constexpr int s = QL + decltype(c_)::value;
          QKV_RDA(s, 0); QKV_RDB(s, 0); QKV_RDA(s, 1); QKV_RDB(s, 1);
          qw[s - QL][0] = bfr[0]; qw[s - QL][1] = bfr[1];
        });
        SB();
        const bool own = aff && (int)((sel0 >> 5) & 7) == rt;
        const bool m0 = !(sel0 & 16), mq = (l & 15) == (int)(sel0 & 15), mv = g == (int)((sel0 & 15) >> 2);
        auto patch_qk = [&](int n) __attribute__((always_inline)) {
          f32x4 t = *(const LDS_AS f32x4*)(smem + L_FXL + n * 64 + g * 16);
          const f32x4 c = *(const LDS_AS f32x4*)(smem + L_CSL + n * 64 + g * 16);
          const float nb = *(const LDS_AS float*)(smem + L_FXL + 388);
#pragma unroll
          for (int e = 0; e < 4; ++e) { t[e] = __builtin_fmaf(c[e], nb, t[e]); asm volatile("" : "+v"(t[e])); }
          acc[0][n] = (mq && m0) ? t : acc[0][n];
          acc[1][n] = (mq && !m0) ? t : acc[1][n];
        };
        if (own) {
          while (__hip_atomic_load(qcnt + 3, __ATOMIC_RELAXED, __HIP_MEMORY_SCOPE_WORKGROUP) < 4u) {}
          asm volatile("" ::: "memory");
          patch_qk(2); patch_qk(3);
          const f32x2 nab = *(const LDS_AS f32x2*)(smem + L_FXL + 384);
          const LDS_AS float* fl_ = (const LDS_AS float*)(smem + L_FXL + 256) + (l & 15);
          const LDS_AS float* cl_ = (const LDS_AS float*)(smem + L_CSL + 256) + (l & 15);
          const float t4 = __builtin_fmaf(cl_[0], nab[1], fl_[0]), t5 = __builtin_fmaf(cl_[16], nab[1], fl_[16]);
          const unsigned na = __builtin_bit_cast(unsigned, (float)nab[0]);
#pragma unroll
          for (int a = 0; a < 2; ++a) {
            const bool ma = a ? !m0 : m0;
            aq[a] = (mq && ma) ? na : aq[a];
#pragma unroll
            for (int e = 0; e < 4; ++e) {
              const bool me = mv && ma && e == (int)(sel0 & 3);
              acc[a][4][e] = me ? t4 : acc[a][4][e]; acc[a][5][e] = me ? t5 : acc[a][5][e];
              av[a][e] = me ? na : av[a][e];
            }
          }
        }
        constexpr float C2 = 1.4426950408889634f * 0.17677669529663687f;
#pragma unroll
        for (int a = 0; a < 2; ++a) {
          f32x2 a2 = {asf(aq[a]), asf(aq[a])};
          qs2[a] = f32x2{asf(aq[a]) * C2, asf(aq[a]) * C2};
          asm volatile("" : "+v"(a2), "+v"(qs2[a]));
          acc[a][2] *= f32x4{a2[0], a2[1], a2[0], a2[1]}; acc[a][3] *= f32x4{a2[0], a2[1], a2[0], a2[1]};
          acc[a][4] *= __builtin_bit_cast(f32x4, av[a]); acc[a][5] *= __builtin_bit_cast(f32x4, av[a]);
        }
        k0 = pack8(acc[0][2], acc[0][3]); k1 = pack8(acc[1][2], acc[1][3]); v0 = pack8(acc[0][4], acc[1][4]); v1 = pack8(acc[0][5], acc[1][5]);
        const unsigned kvo = OFF_KV + IT * KV_STEP + (p * 32 + h) * 32768 + rt * 4096;
        if (p < 7) { st16(k0, rs, l * 16, kvo); st16(k1, rs, l * 16 + 1024, kvo); st16(v0, rs, l * 16 + 2048, kvo); st16(v1, rs, l * 16 + 3072, kvo); }
        char* ownp = smem + (8 + rt) * 2048 + l * 16;
        *(u32x4*)(ownp + L_K) = k0;
        if (rt < 7) *(u32x4*)(ownp + L_K + 1024) = k1;
        *(u32x4*)(ownp + L_V) = v0; *(u32x4*)(ownp + L_V + 1024) = v1;
        SB();
        static_for<32 - QL>([&](auto c_) __attribute__((always_inline)) {
          constexpr int s = QL + decltype(c_)::value;
#pragma unroll
          for (int n = 0; n < 2; ++n) { acc[0][n] = mfma16(qw[s - QL][n].v, af[s % WA][0], acc[0][n]); acc[1][n] = mfma16(qw[s - QL][n].v, af[s % WA][1], acc[1][n]); }
        });
        if (own) { patch_qk(0); patch_qk(1); }
      };
      qkv_loop(it_c);
    }
#undef A_LOAD1
#undef QKV_RDA
#undef QKV_RDB
#undef QKV_BO
    {
      IDS();
      const GAS bf16 *pwo, *pwg, *pwu;
      asm volatile("s_load_dwordx2 %0, %3, 24\n\ts_load_dwordx2 %1, %3, 40\n\ts_load_dwordx2 %2, %3, 48" : "=&s"(pwo), "=&s"(pwg), "=&s"(pwu) : "s"(ka));
      const int ql = l & 15;
      const f32x4 z4 = {0.f, 0.f, 0.f, 0.f}, ninf = {-INFINITY, -INFINITY, -INFINITY, -INFINITY};
      LDS_AS unsigned* rdy = (LDS_AS unsigned*)(smem + L_RDY);
      const unsigned rtag = T[0] + it;
      u32x4 qf[2];
      f32x4 cn[2], oa[2][2], sum[2], S[2][2];
      unsigned one2 = 0x3F803F80u;
      asm volatile("" : "+v"(one2));
      const u32x4 ones = {one2, one2, one2, one2};
      auto ex8 = [&](f32x4 a, f32x4 b) __attribute__((always_inline)) {
        return u32x4{pack2(__builtin_amdgcn_exp2f(a[0]), __builtin_amdgcn_exp2f(a[1])), pack2(__builtin_amdgcn_exp2f(a[2]), __builtin_amdgcn_exp2f(a[3])),
                     pack2(__builtin_amdgcn_exp2f(b[0]), __builtin_amdgcn_exp2f(b[1])), pack2(__builtin_amdgcn_exp2f(b[2]), __builtin_amdgcn_exp2f(b[3]))};
      };
      {
#pragma unroll
        for (int a = 0; a < 2; ++a) {
          const f32x4 c4 = {qs2[a][0], qs2[a][1], qs2[a][0], qs2[a][1]};
          qf[a] = pack8(acc[a][0] * c4, acc[a][1] * c4);
        }
        f32x4 mk;
#pragma unroll
        for (int e = 0; e < 4; ++e) mk[e] = (4 * g + e <= ql) ? 0.f : -INFINITY;
        const f32x4 d0 = mfma16(k0, qf[0], mk), d1 = mfma16(k1, qf[1], mk), d2 = mfma16(k0, qf[1], z4);
        const float m0 = -max_x32(max_x16(fmaxf(fmaxf(d0[0], d0[1]), fmaxf(d0[2], d0[3]))));
        const float m1 = -max_x32(max_x16(fmaxf(fmaxf(d1[0], d1[1]), fmaxf(d1[2], d1[3]))));
        cn[0] = f32x4{m0, m0, m0, m0}; cn[1] = f32x4{m1, m1, m1, m1};
        asm volatile("" : "+v"(cn[0]), "+v"(cn[1]));
        const f32x4 e0 = d0 + cn[0];
        const u32x4 p0 = {pack2(__builtin_amdgcn_exp2f(e0[0]), __builtin_amdgcn_exp2f(e0[1])), pack2(__builtin_amdgcn_exp2f(e0[2]), __builtin_amdgcn_exp2f(e0[3])), 0u, 0u};
        const u32x4 p1 = ex8(d2 + cn[1], d1 + cn[1]);
        oa[0][0] = mfma16(v0, p0, z4); oa[0][1] = mfma16(v1, p0, z4); sum[0] = mfma16(ones, p0, z4);
        oa[1][0] = mfma16(v0, p1, z4); oa[1][1] = mfma16(v1, p1, z4); sum[1] = mfma16(ones, p1, z4);
      }
      const char* kb_ = smem + L_K + l * 16;
      const char* vb_ = smem + L_V + l * 16;
      const unsigned wov = (l >> 3) * 2048 + (((((l & 7) >> 1) ^ ((l >> 4) & 3)) * 2 + (l & 1)) << 4), fnv = (l >> 1) * (F_ * 2) + (l & 1) * 16;
      const unsigned pp = __builtin_amdgcn_readfirstlane(p > 0 ? 32 : 0), pvo = OFF_KV + it * KV_STEP + (p * 32 + h - pp) * 32768;
      const unsigned pfo = __builtin_amdgcn_readfirstlane(F_KV + KVF(it, p * 32 + h - pp));
      asm volatile("" : "+v"(oa[0][0]), "+v"(oa[0][1]), "+v"(oa[1][0]), "+v"(oa[1][1]), "+v"(sum[0]), "+v"(sum[1]));
      DRAIN();
      asm volatile("" : "+v"(tdum));
      if (l == 0) st8(T, rs, rt * SV, F_KV + KVF(it, p * 32 + h));
      BAR();
      asm volatile("" : "+s"(pwo), "+s"(pwg), "+s"(pwu));
      if (!LOP || w >= 4) { WO_PIECE(15); WO_PIECE(14); }
#define LDS16(a_) (*(const LDS_AS u32x4*)(size_t)(a_))
      u32x4 kf[2], vf[2];
      int nw = 0;
      u32x4 p0, p1;
      float e0, e1;
#define EE(v, i) { e0 = __builtin_amdgcn_exp2f(v[i]); e1 = __builtin_amdgcn_exp2f(v[i + 1]); pin2s(e0, e1); SB(); }
#define CV(P, i) { unsigned t_ = pack2(e0, e1); pin1(t_); P[i] = t_; SB(); }
      auto body = [&](unsigned ad, auto dk_) __attribute__((always_inline)) {
        constexpr int dk = decltype(dk_)::value;
        const f32x4 a0 = S[1][0], a1 = S[1][1];
        sum[0] = mfma16(ones, p0, sum[0]); SB();
        EE(a0, 0);
        lds_wait<2>(kf[0], kf[1]);
        S[0][0] = mfma16(kf[0], qf[0], cn[0]); SB();
        CV(p1, 0); EE(a0, 2);
        S[0][1] = mfma16(kf[1], qf[0], cn[0]); SB();
        CV(p1, 1); EE(a1, 0);
        lds_wait<0>(vf[0], vf[1]);
        oa[0][0] = mfma16(vf[0], p0, oa[0][0]); SB();
        CV(p1, 2); EE(a1, 2);
        oa[0][1] = mfma16(vf[1], p0, oa[0][1]); SB();
        CV(p1, 3);
        S[1][0] = mfma16(kf[0], qf[1], cn[1]); SB();
        EE(S[0][0], 0);
        S[1][1] = mfma16(kf[1], qf[1], cn[1]); SB();
        lds2<2048 + dk>(kf[0], kf[1], ad); SB();
        CV(p0, 0); EE(S[0][0], 2);
        oa[1][0] = mfma16(vf[0], p1, oa[1][0]); SB();
        CV(p0, 1); EE(S[0][1], 0);
        oa[1][1] = mfma16(vf[1], p1, oa[1][1]); SB();
        lds2<2048 + L_V>(vf[0], vf[1], ad); SB();
        CV(p0, 2); EE(S[0][1], 2);
        sum[1] = mfma16(ones, p1, sum[1]); SB();
        CV(p0, 3);
      };
      auto last = [&]() __attribute__((always_inline)) {
        sum[0] = mfma16(ones, p0, sum[0]); SB();
        EE(S[1][0], 0);
        lds_wait<0>(vf[0], vf[1]);
        pin2(kf[0], kf[1]);
        oa[0][0] = mfma16(vf[0], p0, oa[0][0]); SB();
        CV(p1, 0); EE(S[1][0], 2);
        oa[0][1] = mfma16(vf[1], p0, oa[0][1]); SB();
        CV(p1, 1); EE(S[1][1], 0);
        const float f0 = __builtin_amdgcn_exp2f(S[1][1][2]), f1 = __builtin_amdgcn_exp2f(S[1][1][3]);
        p1[2] = pack2(e0, e1); p1[3] = pack2(f0, f1);
        oa[1][0] = mfma16(vf[0], p1, oa[1][0]); oa[1][1] = mfma16(vf[1], p1, oa[1][1]); sum[1] = mfma16(ones, p1, sum[1]);
      };
      auto piece = [&]() __attribute__((always_inline)) {
        if constexpr (it == 1) { WO_PIECE(13 - nw); }
        else if (ffn_on) { FFN_DMA(nw); }
        ++nw;
      };
      if (rt > 0) {
        unsigned ad = lbase + l * 16 + (6 + rt) * 2048;
        lds2<2048>(kf[0], kf[1], ad); lds2<2048 + L_V>(vf[0], vf[1], ad);
        lds_wait<2>(kf[0], kf[1]);
        S[0][0] = mfma16(kf[0], qf[0], cn[0]); S[0][1] = mfma16(kf[1], qf[0], cn[0]); S[1][0] = mfma16(kf[0], qf[1], cn[1]); S[1][1] = mfma16(kf[1], qf[1], cn[1]);
        SB();
        lds_wait<0>(vf[0], vf[1]);
        lds2<0>(kf[0], kf[1], ad);
        p0 = ex8(S[0][0], S[0][1]);
        lds_wait<0>(kf[0], kf[1]);
#pragma unroll 1
        for (int i = rt - 1; i > 0; --i) {
          ad -= 2048;
          body(ad, std::integral_constant<int, -2048>{});
          piece();
          SB();
        }
        piece();
        last();
      }
      {
        constexpr int FW = it ? FW1 : FW0, FN = 8 / FW;
        if (w < FW) {
          const int g0 = FN * rt;
          if (KVS && w == 0) __builtin_amdgcn_s_sleep(KVS);
          poll_flags(rs, (l & (FN - 1)) * SV, pfo + g0 * SV, T);
#pragma unroll
          for (int j = 0; j < FN; ++j) {
            const unsigned d = lbase + (g0 + j) * 2048, o = pvo + (g0 + j) * 4096;
            dma16s<0>(rs, l * 16, o, d + L_K); dma16s<1024>(rs, l * 16, o, d + L_K);
            dma16s<0>(rs, l * 16, o + 2048, d + L_V); dma16s<1024>(rs, l * 16, o + 2048, d + L_V);
          }
        }
        if (p == 0) { cn[0] = ninf; cn[1] = ninf; }
        f32x4 mk;
#pragma unroll
        for (int e = 0; e < 4; ++e) mk[e] = (4 * g + e > ql) ? 0.f : -INFINITY;
        const f32x4 cm0 = cn[0] + mk, cm1 = cn[1] + mk;
        if (w < FW) {
          if constexpr (FN > 1) { wait_vm<2 * FN>(); if (l < FN / 2) rdy[FN * rt + l] = rtag; }
          DRAIN();
          if (l < FN) rdy[FN * rt + l] = rtag;
        }
        if (LOP && w < 4) { WO_PIECE(15); WO_PIECE(14); }
        if constexpr (it == 1) { WO_PIECE(6); WO_PIECE(5); WO_PIECE(4); }
        else { WO_PIECE(13); WO_PIECE(12); if (ffn_on) FFN_DMA(7); }

        int have = rt - 1;
        auto need = [&](int gq) __attribute__((always_inline)) {
          if (__builtin_expect(gq > have && gq < 8, 0)) {
            do {
              const unsigned t = __hip_atomic_load(rdy + (l & 7), __ATOMIC_RELAXED, __HIP_MEMORY_SCOPE_WORKGROUP);
              have = rt - 1 + __builtin_ctz(~((unsigned)__builtin_amdgcn_ballot_w64(t == rtag) & 0xffu) >> rt);
            } while (gq > have);
            asm volatile("" ::: "memory");
          }
        };
        unsigned ad = lbase + l * 16 + rt * 2048;
        need(rt);
        lds2<0>(kf[0], kf[1], ad); lds2<L_V>(vf[0], vf[1], ad);
        lds_wait<2>(kf[0], kf[1]);
        S[0][0] = mfma16(kf[0], qf[0], cm0); S[0][1] = mfma16(kf[1], qf[0], cn[0]); S[1][0] = ninf; S[1][1] = mfma16(kf[1], qf[1], cm1);
        SB();
        lds_wait<0>(vf[0], vf[1]);
        need(rt + 1);
        lds2<2048>(kf[0], kf[1], ad);
        p0 = ex8(S[0][0], S[0][1]);
        lds_wait<0>(kf[0], kf[1]);
#pragma unroll 1
        for (int gq = rt + 1; gq < 8; ++gq) {
          need(gq + 1);
          body(ad, std::integral_constant<int, 2048>{});
          piece();
          SB();
          ad += 2048;
        }
        last();
      }
      {
        float big = 0.f;
#pragma unroll
        for (int a = 0; a < 2; ++a)
#pragma unroll
          for (int d = 0; d < 2; ++d)
            big = __builtin_elementwise_maximum(__builtin_elementwise_maximum(big, __builtin_elementwise_maximum(fabsf(oa[a][d][0]), fabsf(oa[a][d][1]))),
                                                __builtin_elementwise_maximum(fabsf(oa[a][d][2]), fabsf(oa[a][d][3])));
        bool ok = sum[0][0] >= 0.5f && sum[0][0] < 0x1p64f && sum[1][0] >= 0.5f && sum[1][0] < 0x1p64f && big < INFINITY;
        if (!__all(ok)) {
#pragma unroll
          for (int a = 0; a < 2; ++a) {
            const int qs = 2 * rt + a, qq = 256 + 16 * qs + ql, klo = max(qq - 255, p > 0 ? 0 : 256);
            auto score = [&](int t) __attribute__((always_inline)) {
              f32x4 s = mfma16(t == 31 ? k1 : *(const u32x4*)(kb_ + t * 1024), qf[a], z4);
#pragma unroll
              for (int e = 0; e < 4; ++e) { const int kk = 16 * t + 4 * g + e; s[e] = (kk <= qq && kk >= klo) ? s[e] : -INFINITY; }
              return s;
            };
            float mx = -INFINITY;
#pragma unroll 1
            for (int t = qs; t <= qs + 16; ++t) { const f32x4 s = score(t); mx = fmaxf(fmaxf(mx, fmaxf(s[0], s[1])), fmaxf(s[2], s[3])); }
            mx = max_x32(max_x16(mx));
            oa[a][0] = z4; oa[a][1] = z4; sum[a] = z4;
#pragma unroll 1
            for (int G = qs >> 1; G <= (qs + 16) >> 1; ++G) {
              const f32x4 s0 = score(2 * G), s1 = score(2 * G + 1);
              f32x4 e0, e1;
#pragma unroll
              for (int e = 0; e < 4; ++e) { e0[e] = __builtin_amdgcn_exp2f(s0[e] - mx); e1[e] = __builtin_amdgcn_exp2f(s1[e] - mx); }
              const u32x4 pf = pack8(e0, e1);
              oa[a][0] = mfma16(*(const u32x4*)(vb_ + G * 2048), pf, oa[a][0]);
              oa[a][1] = mfma16(*(const u32x4*)(vb_ + G * 2048 + 1024), pf, oa[a][1]);
              sum[a] = mfma16(ones, pf, sum[a]);
            }
          }
        }
      }
#pragma unroll
      for (int a = 0; a < 2; ++a) {
        const float inv = __builtin_amdgcn_rcpf(sum[a][0]);
        oa[a][0] *= inv; oa[a][1] *= inv;
      }
      const unsigned oo = OFF_O + it * O_STEP + ((p * 8 + rt) * 64 + h * 2) * 1024;
      st16(pack8(oa[0][0], oa[0][1]), rs, l * 16, oo);
      st16(pack8(oa[1][0], oa[1][1]), rs, l * 16 + 1024, oo);
      DRAIN();
      BAR();
      if (tid == 0) st8(T, rs, OFL(h), F_O + OFB(it, p));
      if constexpr (it == 0) {
        DEF4(gv); DEF4(uv);
        if (ffn_on) {
          const unsigned ro = 65536 + w * 1024 + (4 * g + ((l & 15) >> 2)) * 32 + (l & 3) * 8;
#pragma unroll
          for (int i = 0; i < 4; ++i) {
            frag_t f;
            f.hf.a = trread(smem, ro + i * 16384); f.hf.b = trread(smem, ro + i * 16384 + 512);
            gv[i] = f.v;
            f.hf.a = trread(smem, ro + i * 16384 + 8192); f.hf.b = trread(smem, ro + i * 16384 + 8704);
            uv[i] = f.v;
          }
        }
        PINV4(gv); PINV4(uv);
        {
          ARGP(px, 0);
          const unsigned xo = ((rb * 4 + (w & 3)) * 32 + (w >> 2) * 16) * 2048 + ct * 128, xw = (l >> 3) * 2048 + (((l & 7) ^ g) << 4);
          dma16(RSRC(px, 2048 * 1024 * 2), xw, xo, lbase + w * 4096); dma16(RSRC(px, 2048 * 1024 * 2), xw ^ 64, xo + 16384, lbase + w * 4096 + 1024);
        }
      }
      WO_PIECE(3); WO_PIECE(2); WO_PIECE(1); WO_PIECE(0);
      if constexpr (it == 0) { WO_PIECE(11); WO_PIECE(10); WO_PIECE(9); WO_PIECE(8); WO_PIECE(7); WO_PIECE(6); WO_PIECE(5); WO_PIECE(4); }
    }
    IDS();
    ARGP(pwr, 4); ARGP(pwd, 7); ARGP(pout, 8);
    const int kh = w >> 2, rt2 = rb * 4 + (w & 3);
    const unsigned R = rt2 * 32 + kh * 16 + (l & 15);
    const unsigned to = (rt2 * 64 + ct * 4 + kh) * 1024 + l * 16;
    const unsigned rmo = ct * 64 + ((g & 1) ? 12 + 4 * g : 4 * g);
    f32x2 v[8], wv[8];
    float wsum;
    unsigned ownk;
    {
      u32x4 wa[2], wrp[2], res[2];
#pragma unroll
      for (int j = 0; j < 2; ++j) {
        wrp[j] = *(const GAS u32x4*)(pwr + rmo + 32 * j);
        wa[j] = *(const GAS u32x4*)(pwr + l * 8 + j * 512);
      }
      {
        f32x2 t[8];
        unpack8v(wa[0], t); unpack8v(wa[1], t + 4);
        const f32x2 s = ((t[0] + t[1]) + (t[2] + t[3])) + ((t[4] + t[5]) + (t[6] + t[7]));
        wsum = sum64(hadd(s));
        unpack8v(trade16(wrp[0]), wv); unpack8v(trade16(wrp[1]), wv + 4);
        asm volatile("" : "+v"(wsum), "+v"(wv[0]), "+v"(wv[1]), "+v"(wv[2]), "+v"(wv[3]), "+v"(wv[4]), "+v"(wv[5]), "+v"(wv[6]), "+v"(wv[7]));
      }
      unsigned bt[4];
#pragma unroll
      for (int n = 0; n < 4; ++n) {
        bt[n] = lbase + 32768 + 65536 + kh * 4096 + (4 * g + ((l & 15) >> 2)) * 128 + (l & 3) * 8 + ((n ^ (2 * (g & 1) + ((l & 15) >> 3))) << 5);
        asm volatile("" : "+v"(bt[n]));
      }
      if (w == 0) { if (it ? OSL1 : OSL0) __builtin_amdgcn_s_sleep(it ? OSL1 : OSL0); poll_flags<OPS>(rs, OFL(l & 31), F_O + OFB(it, rb >> 1), T); }
      else if (it == 0 && w < 3) poll_flags2(rs, F_XN + (((w - 1) * 4 + g) * RX + bi % RX) * 256 + (l & 15) * 16, T);
      else if (it == 0 && w == 3) poll_flags(rs, ((l >> 3) * 32 + h) * cmax(8 * SV, 128) + (l & 7) * SV, F_KV, T);
      asm volatile("s_barrier" ::: "memory");
      const unsigned abase = OFF_O + it * O_STEP + (rt2 * 64 + 32 * kh) * 1024;
      const unsigned av0 = l * 16 + kh * 1024, av1 = l * 16 + (kh ^ 1) * 1024;
      u32x4 af[WB][2];
#define O_LOAD(s_) { ld16h<(wo_k(it, s_) & 1) * 2048>(af[(s_) % WB][0], rs, av0, abase + (wo_k(it, s_) >> 1) * 4096); ld16h<(wo_k(it, s_) & 1) * 2048>(af[(s_) % WB][1], rs, av1, abase + (wo_k(it, s_) >> 1) * 4096); }
      static_for<WR>([&](auto c_) __attribute__((always_inline)) { O_LOAD(decltype(c_)::value); });
      if constexpr (it == 1) { ld16h<0>(res[0], rs, g * 16, OFF_XNEW + ct * 128); ld16h<64>(res[1], rs, g * 16, OFF_XNEW + ct * 128); }
      SB();
      f32x4 ac[2][4];
#pragma unroll
      for (int a = 0; a < 2; ++a)
#pragma unroll
        for (int n = 0; n < 4; ++n) ac[a][n] = f32x4{0.f, 0.f, 0.f, 0.f};
      u32x4 bfr[2][4];
#define WO_RD(buf, s, n)                                                                             \
  {                                                                                                  \
    const unsigned o_ = bt[n] + (wo_k(it, s) & 7) * 8192;                                             \
    frag_t f_;                                                                                       \
    f_.hf.a = trabs(o_); f_.hf.b = trabs(o_ + 2048);                                                 \
    bfr[buf][n] = f_.v;                                                                              \
  }
#pragma unroll
      for (int n = 0; n < 4; ++n) WO_RD(0, 0, n);
      static_for<16>([&](auto sc_) __attribute__((always_inline)) {
        constexpr int s = decltype(sc_)::value, cur = s & 1;
        pin_ac(ac);
        if constexpr (s == 1) lds_count<0>(lbase + L_QC);
        if constexpr (s == (it == 0 ? 3 : 11)) lds_look(smem + L_QC, 64);
        if constexpr (s < 15 && ((wo_k(it, s) ^ wo_k(it, s + 1)) & 8)) {
#pragma unroll
          for (int n = 0; n < 4; ++n) { bt[n] += (wo_k(it, s) & 8) ? -65536 : 65536; pin1(bt[n]); }
        }
        if constexpr (s < WB - WR) O_LOAD(WR + 2 * s);
        if constexpr (it == 0 && s == 8) lds_count<8>(lbase + L_QC);
        if constexpr (it == 0 && s == WDS) {
          DRAIN();
          if ((w >> 2) == WDW && ffn_on) { lds_look(smem + L_QC + 8, 16); WD_DMA_OF(w); WD_DMA_OF(w ^ 4); }
        }
        if constexpr (it == 1 || s < WDS) wait_vm<wo_at(it, s) - wo_pos(it, s)>();
        pin2(af[s % WB][0], af[s % WB][1]);
        if constexpr (it == 1 && s == 9) pin2(res[0], res[1]);
        SB();
#pragma unroll
        for (int n = 0; n < 4; ++n) {
          ac[0][n] = mfma16(bfr[cur][n], af[s % WB][0], ac[0][n]);
          ac[1][n] = mfma16(bfr[cur][n], af[s % WB][1], ac[1][n]);
          if (s + 1 < 16) WO_RD(cur ^ 1, s + 1, n);
          if (it == 0 && s == 8) {
            const u32x2 t = *(const LDS_AS u32x2*)(smem + w * 4096 + (l & 15) * 128 + (((2 * n + (g >> 1)) ^ ((l & 15) >> 1)) << 4) + (g & 1) * 8);
            res[n >> 1][2 * (n & 1)] = t[0]; res[n >> 1][2 * (n & 1) + 1] = t[1];
          }
          if (s == 9) {
            if (it == 1 && n == 0) { res[0] = R == sel0 ? res[0] : x2p[0]; res[1] = R == sel0 ? res[1] : x2p[1]; }

            v[2 * n] = f32x2{lo16(res[n >> 1][2 * (n & 1)]), hi16(res[n >> 1][2 * (n & 1)])};
            v[2 * n + 1] = f32x2{lo16(res[n >> 1][2 * (n & 1) + 1]), hi16(res[n >> 1][2 * (n & 1) + 1])};
            pin2f(v[2 * n], v[2 * n + 1]);
          }
          SB();
        }
        if constexpr (s < WB - WR) { O_LOAD(WR + 2 * s + 1); }
        else if constexpr (s + WB < 16) { O_LOAD(s + WB); }
        SB();
      });
#undef WO_RD
#undef O_LOAD
#define XCH_AT(v) ((v) == 7 ? 155648 : (v) * 4096)
#pragma unroll
      for (int n = 0; n < 4; ++n) *(f32x4*)(smem + XCH_AT(w) + n * 1024 + l * 16) = ac[1][n];
#pragma unroll
      for (int n = 0; n < 4; ++n) { v[2 * n] += f32x2{ac[0][n][0], ac[0][n][1]}; v[2 * n + 1] += f32x2{ac[0][n][2], ac[0][n][3]}; }
      BAR();
#pragma unroll
      for (int n = 0; n < 4; ++n) {
        const f32x4 o = *(const f32x4*)(smem + XCH_AT(w ^ 4) + n * 1024 + l * 16);
        v[2 * n] += f32x2{o[0], o[1]}; v[2 * n + 1] += f32x2{o[2], o[3]};
      }
    }
    {
      if (it == 0 && X1F) { st16(pack8v(v), rs, to, OFF_XN + (4u << 20)); st16(pack8v(v + 4), rs, to + 2048, OFF_XN + (4u << 20)); }
      SB();
      const f32x2 s = ((v[0] + v[1]) + (v[2] + v[3])) + ((v[4] + v[5]) + (v[6] + v[7]));
      f32x2 dp = v[0] * wv[0], q = v[0] * v[0];
#pragma unroll
      for (int j = 1; j < 8; ++j) { dp = __builtin_elementwise_fma(v[j], wv[j], dp); q = __builtin_elementwise_fma(v[j], v[j], q); }
      float mt = hadd(s), qq = hadd(q), dot = hadd(dp);
      add_g3(mt, qq, dot);
      const unsigned so = OFF_STAT + it * 524288 + rb * 32768, lrow = R & 127;
      if (g == 0) st16(unpoison4(__builtin_bit_cast(u32x4, f32x4{mt, qq, dot, 0.f})), rs, ct * 2048 + lrow * 16, so);
      SB();
      if (it == 0 && !X1F) { st16(pack8v(v), rs, to, OFF_XN + (4u << 20)); st16(pack8v(v + 4), rs, to + 2048, OFF_XN + (4u << 20)); }
      SB();
      if (it == 0) wait_vm<X1F>();
      if constexpr (it == 0) {
        volatile LDS_AS u32x2* in = (volatile LDS_AS u32x2*)(smem + L_XC);
        if (l == 0) in[w] = T;
        const u32x2 t = in[l & 7];
        if (__all(t[0] == T[0] && t[1] == T[1]) && l < RX) st8(T, rs, (l * 32 + (rb & 1) * 16 + ct) * SX, F_X1 + (rb >> 1) * RX * 32 * SX);
      }
      if (it == 1 && WD1 == 0) { if (ffn_on) { WD_DMA_OF(w); } }
      if (it == 0) {
#pragma unroll
        for (int j = 0; j < 9; ++j) *(u32x4*)(smem + L_RING + (j / 3) * STAGE + (w * 3 + j % 3) * 1024 + l * 16) = keep[j];
      }
      __builtin_amdgcn_s_sleep(it == 0 ? SDI0 : SDIR);
      char* const skey = smem + (it == 0 ? L_SKEY : L_XC);
      u32x4 sp[4];
      ld16h<0>(sp[0], rs, g * 8192 + lrow * 16, so); ld16h<2048>(sp[1], rs, g * 8192 + lrow * 16, so);
      ld16h<0>(sp[2], rs, g * 8192 + lrow * 16, so + 4096); ld16h<2048>(sp[3], rs, g * 8192 + lrow * 16, so + 4096);
      u32x2 xf;
      unsigned k_;
      asm volatile("" : "=v"(xf), "=v"(k_));
      if (it == 0 && w == 0) { ld8h(xf, rs, (l & 31) * SX, F_X1 + (p * RX + (xcd >> 1) % RX) * 32 * SX); ld4h<0>(k_, rs, (l & 15) * SK, OFF_KEY + (bid & 15) * cmax(16 * SK, 128)); }
      DRAIN();
      asm volatile("" : "+v"(xf), "+v"(k_)::"memory");
      PINV4(sp);
      if (it == 1 && WD1 == 1) { if (ffn_on) { WD_DMA_OF(w); } }
      const RowFin r = fin_key(rs, T, g * 8192 + lrow * 16, so, skey, l, w, R, wsum, sp);
      if constexpr (it == 0) {
        if (w == 0) {
          const unsigned need = 0x30003u << (((PROT() + 5) & 7) * 2);
          unsigned m = (unsigned)__builtin_amdgcn_ballot_w64(xf[0] == T[0] && xf[1] == T[1]);
          for (unsigned spins = 0; (m & need) != need;) {
            if (spin_fail(spins, rs, T)) { m = ~0u; break; }
            const u32x2 f = ld8(rs, F_X1 + ((p * RX + (xcd >> 1) % RX) * 32 + (l & 31)) * SX, 0);
            k_ = key_load(rs, l, 0, bid & 15);
            m = (unsigned)__builtin_amdgcn_ballot_w64(f[0] == T[0] && f[1] == T[1]);
          }
          k_ = umax16(k_ == POISON ? 0u : k_);
          if (l == 0) *(LDS_AS u32x4*)(skey + 32) = u32x4{m, k_, POISON, 0u};
        }
        if (tid == 192) *(LDS_AS u32x4*)(smem + L_QC) = u32x4{0u, 0u, 0u, 0u};
      }
      BAR();
      if constexpr (it == 0) {
        PRE_LOAD(1, PROT() + 5, 0, 4);
        const u32x2 mk = *(const LDS_AS u32x2*)(skey + 32);
        xm0 = __builtin_amdgcn_readfirstlane(mk[0]); kg0 = __builtin_amdgcn_readfirstlane(mk[1]);
      }
      const unsigned own = fin_cand(rs, skey, it, g, rb, ct, r, v);
      if constexpr (it == 0) kg0 = max(kg0, (unsigned)__builtin_amdgcn_readfirstlane(own));
      ownk = own;
      if (it == 1 && WD1 == 2) { if (ffn_on) { WD_DMA_OF(w); } }
      fin_x2(r, v);
      if constexpr (it == 0) {
        x2p[0] = pack8v(v); x2p[1] = pack8v(v + 4);
        asm volatile("" : "+v"(x2p[0]), "+v"(x2p[1]));
        if ((w & 3) * 2 + (w >> 2) == ct >> 1) {
          const float f = 1.0f + r.rd, rd2 = ln_rd(r.var * f * f), mb2 = rbf1(__builtin_fmaf(r.mean - r.mb, r.rd, r.mean));
          if (g < 2 && ((l >> 3) & 1) == (ct & 1))
            st4(min(__builtin_bit_cast(unsigned, g == 0 ? f * rd2 : -__builtin_fmaf(r.mb, r.rd, mb2) * __builtin_amdgcn_rcpf(f)), POISON - 1), rs, OFF_AB + 16384 + R * 4 + g * 8192, 0);
        }
      }
    }
    if (it == 1) {
      const u32x4 op0 = trade16(pack8v(v)), op1 = trade16(pack8v(v + 4));
#define OUT_STORE()                                                                                 \
  if (R != key_row(key) && (!OEA || R == key_row(ownk))) {                                                                          \
    __builtin_amdgcn_raw_buffer_store_b128(op0, rout, (R * 1024 + rmo) * 2, 0, SC1);                  \
    __builtin_amdgcn_raw_buffer_store_b128(op1, rout, (R * 1024 + rmo + 32) * 2, 0, SC1);             \
  }
      if (OEA && R != key_row(ownk)) {
        __builtin_amdgcn_raw_buffer_store_b128(op0, rout, (R * 1024 + rmo) * 2, 0, SC1);
        __builtin_amdgcn_raw_buffer_store_b128(op1, rout, (R * 1024 + rmo + 32) * 2, 0, SC1);
      }
      u32x4 xv[4];
      unsigned guess = 0;
      {
        __builtin_amdgcn_s_sleep(GSL);
        const unsigned k_ = key_load(rs, l, 1, bid & 15);
        guess = ffn_xoff(__builtin_amdgcn_readfirstlane(umax16(k_ == POISON ? 0u : k_)), 1, w);
        row_load(rs, g, guess, xv);
      }
      if (w == 0) {
        const unsigned k_ = key_wait(rs, T, l, 1, bid & 15, key_load(rs, l, 1, bid & 15));
        if (l == 0) *(LDS_AS unsigned*)(smem + L_SKEY + 32) = k_;
      }
      DRAIN();
      PINV4(xv);
      BAR();
      const unsigned key = __builtin_amdgcn_readfirstlane(*(LDS_AS unsigned*)(smem + L_SKEY + 32));
      if (ffn_on) {
        f32x4 ga, ua;
        s16x4 dfr[8];
        ffn_d_read(smem, l, w, dfr);
        if (guess != ffn_xoff(key, 1, w)) ld4x16(xv, rs, g * 16, ffn_xoff(key, 1, w));
        ffn_gu(gv, uv, xv, ga, ua);
        ffn_gu_check(rs, T, ffn_xoff(key, 1, w), l, gv, uv, xv, ga, ua);
        ffn_gu_store(smem, l, w, ga, ua);
        BAR();
        f32x4 ya = {0.f, 0.f, 0.f, 0.f}, yb = ya;
        u32x2 hh = ffn_h(ffn_t_read(smem, l), l);
        ffn_d_mma(dfr, hh, ya, yb);
        if ((l & 15) < 8) ffn_d_store(rs, l, 4 * w, 1, slice, ya + yb);
        SB();
        if (OSLP) __builtin_amdgcn_s_sleep(OSLP);
        OUT_STORE();
      } else {
        OUT_STORE();
        const unsigned G = bi;
        if (G == 0 && tid == 0) st8(T, rs, F_EP, 0);
        u32x4 pv[4];
        u32x2 cxn, cx1;
        __builtin_amdgcn_s_sleep(RSL);
        red_load(rs, tid, 1, G, key, pv, cxn, cx1);
        red_sum(rs, smem, T, tid, w, 1, G, key, pv, cxn, cx1);
        BAR();
        if (w == 0) {
          const u32x2 ov = red_final<8>(smem, l, key, cxn, cx1);
          if (l < 8) __builtin_amdgcn_raw_buffer_store_b64(ov, rout, (key_row(key) * 1024 + G * 32 + l * 4) * 2, 0, 0);
        }
      }
#undef OUT_STORE
    }
  };
  layer(std::integral_constant<int, 0>{});
  layer(std::integral_constant<int, 1>{});
}

extern "C" void kernel_launch(void* const* d_in, const int* in_sizes, int n_in, void* d_out, int out_size,
                              void* d_ws, size_t ws_size, hipStream_t stream) {
  (void)hipFuncSetAttribute((const void*)mega_kernel, hipFuncAttributeMaxDynamicSharedMemorySize, LDS_BYTES);
  mega_kernel<<<256, 512, LDS_BYTES, stream>>>((const bf16*)d_in[0], (char*)d_ws, (const bf16*)d_in[1], (const bf16*)d_in[2], (const bf16*)d_in[4],
                                               (const bf16*)d_in[5], (const bf16*)d_in[6], (const bf16*)d_in[7], (bf16*)d_out);
}
```

```cpp
#include <hip/hip_runtime.h>
#include <stdint.h>
#include <utility>

typedef __bf16 bf16;
typedef __bf16 bf16x8 __attribute__((ext_vector_type(8)));
typedef __bf16 bf16x2 __attribute__((ext_vector_type(2)));
typedef float f32x4 __attribute__((ext_vector_type(4)));
typedef float f32x2 __attribute__((ext_vector_type(2)));
typedef short s16x4 __attribute__((ext_vector_type(4)));
typedef unsigned int u32x4 __attribute__((ext_vector_type(4)));
typedef unsigned int u32x2 __attribute__((ext_vector_type(2)));
#define LDS_AS __attribute__((address_space(3)))
#define GAS __attribute__((address_space(1)))
typedef GAS unsigned gu32;
typedef GAS unsigned long long gu64;
#define RLX_AGENT __ATOMIC_RELAXED, __HIP_MEMORY_SCOPE_AGENT
typedef __amdgpu_buffer_rsrc_t rsrc_t;

constexpr int F_ = 3584;
constexpr int SC1 = 16;

constexpr int RX = 4;
constexpr unsigned F_EP = 0, F_TMO = 128;
constexpr unsigned F_XN = 4096;
constexpr unsigned F_O = 8u << 20;
constexpr unsigned F_X1 = F_O + 131072;
constexpr unsigned OFF_KEY = F_O + 393216;
constexpr unsigned F_KV = F_O + 524288;
#define KVF(it, i) ((i) * 128 + (it) * 64)
#define OFL(h) (((h) >> 3) * 128 + ((h) & 7) * 8)
#define OFB(it, p) ((p) * 512 + (it) * 64)
constexpr unsigned OFF_AB = 147456;
constexpr unsigned OFF_XNEW = 262144;
constexpr unsigned OFF_CS = 294912;
constexpr unsigned OFF_FX = 393216;
constexpr unsigned OFF_CA = 524288;
constexpr unsigned OFF_CB = 655360;
constexpr unsigned OFF_STAT = 1u << 20;
constexpr unsigned OFF_PART = 3u << 20;
constexpr unsigned OFF_X1 = 12u << 20;
constexpr unsigned OFF_O = 16u << 20;
constexpr unsigned OFF_KV = 32u << 20;
constexpr unsigned OFF_WI = 52u << 20;
constexpr unsigned WS_BYTES = 58u << 20;
constexpr unsigned POISON = 0xFFFFFFFFu;

constexpr int L_CSL = 31872, L_FXL = 32256, L_K = 0, L_V = 32768, L_RING = 65536, L_DN = 32768, L_TOUCH0 = 149504, L_RDY = 31744, L_QC = 31808, LDS_BYTES = 163840;
constexpr int STAGE = 24576;
#define SLOT(t) ((t) & 3)

template <typename F, int... I> __device__ __forceinline__ void static_for_impl(F&& f, std::integer_sequence<int, I...>) {
  (f(std::integral_constant<int, I>{}), ...);
}
template <int N, typename F> __device__ __forceinline__ void static_for(F&& f) { static_for_impl(f, std::make_integer_sequence<int, N>{}); }
__device__ __forceinline__ unsigned pack2(float a, float b) {
  f32x2 v = {a, b};
  return __builtin_bit_cast(unsigned, __builtin_convertvector(v, bf16x2));
}
__device__ __forceinline__ float asf(unsigned u) { return __builtin_bit_cast(float, u); }
__device__ __forceinline__ float lo16(unsigned u) { return __builtin_bit_cast(float, u << 16); }
__device__ __forceinline__ float hi16(unsigned u) { return __builtin_bit_cast(float, u & 0xffff0000u); }
__device__ __forceinline__ u32x4 pack8(const f32x4 a, const f32x4 b) {
  u32x4 o;
  o[0] = pack2(a[0], a[1]); o[1] = pack2(a[2], a[3]); o[2] = pack2(b[0], b[1]); o[3] = pack2(b[2], b[3]);
  return o;
}
__device__ __forceinline__ f32x4 mfma16(u32x4 a, u32x4 b, f32x4 c) {
  return __builtin_amdgcn_mfma_f32_16x16x32_bf16(__builtin_bit_cast(bf16x8, a), __builtin_bit_cast(bf16x8, b), c, 0, 0, 0);
}
__device__ __forceinline__ f32x4 mfma16k16(s16x4 a, s16x4 b, f32x4 c) { return __builtin_amdgcn_mfma_f32_16x16x16bf16_1k(a, b, c, 0, 0, 0); }
template <int CTRL> __device__ __forceinline__ float dppf(float v) {
  return __builtin_bit_cast(float, __builtin_amdgcn_update_dpp(0, __builtin_bit_cast(int, v), CTRL, 0xf, 0xf, true));
}
template <int CTRL> __device__ __forceinline__ unsigned dppu(unsigned v) {
  return (unsigned)__builtin_amdgcn_update_dpp(0, (int)v, CTRL, 0xf, 0xf, true);
}
__device__ __forceinline__ float add_x16(float v) {
  auto r = __builtin_amdgcn_permlane16_swap(__builtin_bit_cast(unsigned, v), __builtin_bit_cast(unsigned, v), false, false);
  return __builtin_bit_cast(float, (unsigned)r[0]) + __builtin_bit_cast(float, (unsigned)r[1]);
}
__device__ __forceinline__ float add_x32(float v) {
  auto r = __builtin_amdgcn_permlane32_swap(__builtin_bit_cast(unsigned, v), __builtin_bit_cast(unsigned, v), false, false);
  return __builtin_bit_cast(float, (unsigned)r[0]) + __builtin_bit_cast(float, (unsigned)r[1]);
}
__device__ __forceinline__ float max_x16(float v) {
  auto r = __builtin_amdgcn_permlane16_swap(__builtin_bit_cast(unsigned, v), __builtin_bit_cast(unsigned, v), false, false);
  return fmaxf(__builtin_bit_cast(float, (unsigned)r[0]), __builtin_bit_cast(float, (unsigned)r[1]));
}
__device__ __forceinline__ float max_x32(float v) {
  auto r = __builtin_amdgcn_permlane32_swap(__builtin_bit_cast(unsigned, v), __builtin_bit_cast(unsigned, v), false, false);
  return fmaxf(__builtin_bit_cast(float, (unsigned)r[0]), __builtin_bit_cast(float, (unsigned)r[1]));
}
__device__ __forceinline__ unsigned umax_x16(unsigned v) {
  auto r = __builtin_amdgcn_permlane16_swap(v, v, false, false);
  return max((unsigned)r[0], (unsigned)r[1]);
}
__device__ __forceinline__ unsigned umax_x32(unsigned v) {
  auto r = __builtin_amdgcn_permlane32_swap(v, v, false, false);
  return max((unsigned)r[0], (unsigned)r[1]);
}
__device__ __forceinline__ float sum_hi8(float v) {
  v += dppf<0x128>(v);
  v = add_x16(v);
  return add_x32(v);
}
__device__ __forceinline__ float sum32(float v) {
  v += dppf<0xB1>(v);
  v += dppf<0x4E>(v);
  v += dppf<0x124>(v);
  v += dppf<0x128>(v);
  return add_x16(v);
}
__device__ __forceinline__ float sum64(float v) {
  v += dppf<0xB1>(v);
  v += dppf<0x4E>(v);
  v += dppf<0x124>(v);
  v += dppf<0x128>(v);
  v = add_x16(v);
  return add_x32(v);
}

#define BAR() asm volatile("s_waitcnt lgkmcnt(0)\n\ts_barrier" ::: "memory")
#define DRAIN() asm volatile("s_waitcnt vmcnt(0)" ::: "memory")
#define SB() __builtin_amdgcn_sched_barrier(0)
template <int N> __device__ __forceinline__ void wait_vm() { asm volatile("s_waitcnt vmcnt(%0)" ::"n"(N) : "memory"); }

__device__ __forceinline__ void dma16(rsrc_t rs, unsigned voff, unsigned soff, unsigned lds) {
  asm volatile("s_mov_b32 m0, %0\n\ts_nop 0\n\tbuffer_load_dwordx4 %1, %2, %3 offen lds" ::"s"(lds), "v"(voff), "s"(rs), "s"(soff) : "memory");
}
template <int OFF> __device__ __forceinline__ void dma16s(rsrc_t rs, unsigned voff, unsigned soff, unsigned lds) {
  asm volatile("s_mov_b32 m0, %0\n\ts_nop 0\n\tbuffer_load_dwordx4 %1, %2, %3 offen offset:%4 sc1 lds" ::"s"(lds), "v"(voff), "s"(rs), "s"(soff), "n"(OFF) : "memory");
}
__device__ __forceinline__ void touch(unsigned& d, const GAS bf16* ptr) { asm volatile("global_load_dword %0, %1, off" : "+v"(d) : "v"(ptr) : "memory"); }
__device__ __forceinline__ void ldrows(u32x4 (&d)[4], rsrc_t rx, unsigned voff, unsigned soff) {
  asm volatile("buffer_load_dwordx4 %0, %4, %5, %6 offen\n\tbuffer_load_dwordx4 %1, %4, %5, %6 offen offset:512\n\t"
               "buffer_load_dwordx4 %2, %4, %5, %6 offen offset:1024\n\tbuffer_load_dwordx4 %3, %4, %5, %6 offen offset:1536"
               : "=&v"(d[0]), "=&v"(d[1]), "=&v"(d[2]), "=&v"(d[3]) : "v"(voff), "s"(rx), "s"(soff) : "memory");
}
__device__ __forceinline__ void touchb(unsigned& d, rsrc_t rs, unsigned vo) { asm volatile("buffer_load_dword %0, %1, %2, 0 offen sc1" : "+v"(d) : "v"(vo), "s"(rs) : "memory"); }
__device__ __forceinline__ float rbf1(float x) { return __builtin_bit_cast(float, pack2(0.f, x)); }
__device__ __forceinline__ u32x4 ld16(rsrc_t rs, unsigned voff, unsigned soff) { return __builtin_amdgcn_raw_buffer_load_b128(rs, voff, soff, SC1); }
template <int OFF> __device__ __forceinline__ void ld16h(u32x4& d, rsrc_t rs, unsigned voff, unsigned soff) {
  asm volatile("buffer_load_dwordx4 %0, %1, %2, %3 offen offset:%4 sc1" : "=v"(d) : "v"(voff), "s"(rs), "s"(soff), "n"(OFF) : "memory");
}
template <int I, int OFF> __device__ __forceinline__ void ld16w(u32x4& d, rsrc_t rs, unsigned voff, unsigned soff) {
  if constexpr (I == 0) asm volatile("buffer_load_dwordx4 %0, %1, %2, %3 offen offset:%4 sc1" : "={v[128:131]}"(d) : "v"(voff), "s"(rs), "s"(soff), "n"(OFF) : "memory");
  else if constexpr (I == 1) asm volatile("buffer_load_dwordx4 %0, %1, %2, %3 offen offset:%4 sc1" : "={v[132:135]}"(d) : "v"(voff), "s"(rs), "s"(soff), "n"(OFF) : "memory");
  else if constexpr (I == 2) asm volatile("buffer_load_dwordx4 %0, %1, %2, %3 offen offset:%4 sc1" : "={v[136:139]}"(d) : "v"(voff), "s"(rs), "s"(soff), "n"(OFF) : "memory");
  else if constexpr (I == 3) asm volatile("buffer_load_dwordx4 %0, %1, %2, %3 offen offset:%4 sc1" : "={v[140:143]}"(d) : "v"(voff), "s"(rs), "s"(soff), "n"(OFF) : "memory");
  else if constexpr (I == 4) asm volatile("buffer_load_dwordx4 %0, %1, %2, %3 offen offset:%4 sc1" : "={v[144:147]}"(d) : "v"(voff), "s"(rs), "s"(soff), "n"(OFF) : "memory");
  else if constexpr (I == 5) asm volatile("buffer_load_dwordx4 %0, %1, %2, %3 offen offset:%4 sc1" : "={v[148:151]}"(d) : "v"(voff), "s"(rs), "s"(soff), "n"(OFF) : "memory");
  else if constexpr (I == 6) asm volatile("buffer_load_dwordx4 %0, %1, %2, %3 offen offset:%4 sc1" : "={v[152:155]}"(d) : "v"(voff), "s"(rs), "s"(soff), "n"(OFF) : "memory");
  else if constexpr (I == 7) asm volatile("buffer_load_dwordx4 %0, %1, %2, %3 offen offset:%4 sc1" : "={v[156:159]}"(d) : "v"(voff), "s"(rs), "s"(soff), "n"(OFF) : "memory");
  else if constexpr (I == 8) asm volatile("buffer_load_dwordx4 %0, %1, %2, %3 offen offset:%4 sc1" : "={v[160:163]}"(d) : "v"(voff), "s"(rs), "s"(soff), "n"(OFF) : "memory");
  else if constexpr (I == 9) asm volatile("buffer_load_dwordx4 %0, %1, %2, %3 offen offset:%4 sc1" : "={v[164:167]}"(d) : "v"(voff), "s"(rs), "s"(soff), "n"(OFF) : "memory");
  else if constexpr (I == 10) asm volatile("buffer_load_dwordx4 %0, %1, %2, %3 offen offset:%4 sc1" : "={v[168:171]}"(d) : "v"(voff), "s"(rs), "s"(soff), "n"(OFF) : "memory");
  else if constexpr (I == 11) asm volatile("buffer_load_dwordx4 %0, %1, %2, %3 offen offset:%4 sc1" : "={v[172:175]}"(d) : "v"(voff), "s"(rs), "s"(soff), "n"(OFF) : "memory");
}
template <int K> __device__ __forceinline__ void pin_w(u32x4& a, u32x4& b) {
  if constexpr (K == 0) asm volatile("" : "+{v[128:131]}"(a), "+{v[132:135]}"(b)::"memory");
  else if constexpr (K == 1) asm volatile("" : "+{v[136:139]}"(a), "+{v[140:143]}"(b)::"memory");
  else if constexpr (K == 2) asm volatile("" : "+{v[144:147]}"(a), "+{v[148:151]}"(b)::"memory");
  else if constexpr (K == 3) asm volatile("" : "+{v[152:155]}"(a), "+{v[156:159]}"(b)::"memory");
  else if constexpr (K == 4) asm volatile("" : "+{v[160:163]}"(a), "+{v[164:167]}"(b)::"memory");
  else if constexpr (K == 5) asm volatile("" : "+{v[168:171]}"(a), "+{v[172:175]}"(b)::"memory");
}
__device__ __forceinline__ void ld8h(u32x2& d, rsrc_t rs, unsigned voff, unsigned soff) {
  asm volatile("buffer_load_dwordx2 %0, %1, %2, %3 offen sc1" : "=v"(d) : "v"(voff), "s"(rs), "s"(soff) : "memory");
}
__device__ __forceinline__ void ld16u(u32x4& d, rsrc_t rs, unsigned voff, unsigned soff) {
  asm volatile("buffer_load_dwordx4 %0, %1, %2, %3 offen sc1" : "+v"(d) : "v"(voff), "s"(rs), "s"(soff) : "memory");
}
template <int OFF> __device__ __forceinline__ void ld4h(unsigned& d, rsrc_t rs, unsigned voff, unsigned soff) {
  asm volatile("buffer_load_dword %0, %1, %2, %3 offen offset:%4 sc1" : "=v"(d) : "v"(voff), "s"(rs), "s"(soff), "n"(OFF) : "memory");
}
__device__ __forceinline__ void ld4x16(u32x4 (&d)[4], rsrc_t rs, unsigned voff, unsigned soff) {
  asm volatile("buffer_load_dwordx4 %0, %4, %5, %6 offen sc1\n\tbuffer_load_dwordx4 %1, %4, %5, %6 offen offset:64 sc1\n\t"
               "buffer_load_dwordx4 %2, %4, %5, %6 offen offset:128 sc1\n\tbuffer_load_dwordx4 %3, %4, %5, %6 offen offset:192 sc1\n\ts_waitcnt vmcnt(0)"
               : "=&v"(d[0]), "=&v"(d[1]), "=&v"(d[2]), "=&v"(d[3]) : "v"(voff), "s"(rs), "s"(soff) : "memory");
}
__device__ __forceinline__ u32x2 ld8(rsrc_t rs, unsigned voff, unsigned soff) { return __builtin_amdgcn_raw_buffer_load_b64(rs, voff, soff, SC1); }
__device__ __forceinline__ void st16(u32x4 v, rsrc_t rs, unsigned voff, unsigned soff) { __builtin_amdgcn_raw_buffer_store_b128(v, rs, voff + soff, 0, SC1); }
__device__ __forceinline__ void st8(u32x2 v, rsrc_t rs, unsigned voff, unsigned soff) { __builtin_amdgcn_raw_buffer_store_b64(v, rs, voff, soff, SC1); }

__device__ __forceinline__ unsigned ld4(rsrc_t rs, unsigned voff, unsigned soff) { return __builtin_amdgcn_raw_buffer_load_b32(rs, voff, soff, SC1); }
__device__ __forceinline__ void st4(unsigned v, rsrc_t rs, unsigned voff, unsigned soff) { __builtin_amdgcn_raw_buffer_store_b32(v, rs, voff, soff, SC1); }

constexpr unsigned kSpinCap = 1u << 16;
__device__ __forceinline__ bool spin_fail(unsigned& spins, rsrc_t rs, u32x2 T) {
  asm volatile("" ::: "memory");
  ++spins;
  if (spins > kSpinCap) { st8(T, rs, 0, F_TMO); return true; }
  if ((spins & 255u) == 0u) {
    const u32x2 v = ld8(rs, 0, F_TMO);
    if (v[0] == T[0] && v[1] == T[1]) return true;
  }
  __builtin_amdgcn_s_sleep(3);
  return false;
}
__device__ __forceinline__ void poll_flags2(rsrc_t rs, unsigned voff, u32x2 T) {
  for (unsigned spins = 0;;) {
    const u32x4 v = ld16(rs, voff, 0);
    if (__all(v[0] == T[0] && v[1] == T[1] && v[2] == T[0] && v[3] == T[1])) break;
    if (spin_fail(spins, rs, T)) break;
  }
  asm volatile("" ::: "memory");
  __builtin_amdgcn_fence(__ATOMIC_ACQUIRE, "wavefront");
}
__device__ __forceinline__ void poll_flags(rsrc_t rs, unsigned voff, unsigned soff, u32x2 T) {
  for (unsigned spins = 0;;) {
    const u32x2 v = ld8(rs, voff, soff);
    if (__all(v[0] == T[0] && v[1] == T[1])) break;
    if (spin_fail(spins, rs, T)) break;
  }
  asm volatile("" ::: "memory");
  __builtin_amdgcn_fence(__ATOMIC_ACQUIRE, "wavefront");
}

__device__ __forceinline__ void unpack8v(const u32x4 u, f32x2* v) {
#pragma unroll
  for (int j = 0; j < 4; ++j) v[j] = f32x2{lo16(u[j]), hi16(u[j])};
}
__device__ __forceinline__ u32x4 pack8v(const f32x2* v) {
  u32x4 o;
#pragma unroll
  for (int j = 0; j < 4; ++j) o[j] = pack2(v[j][0], v[j][1]);
  return o;
}
__device__ __forceinline__ float ln_rd(float var) { return __builtin_amdgcn_rcpf(rbf1(__builtin_amdgcn_sqrtf(rbf1(rbf1(var) + 1e-5f)))); }
__device__ __forceinline__ float hadd(f32x2 v) {
  float a = v[0];
  asm volatile("" : "+v"(a));
  return a + v[1];
}
__device__ __forceinline__ float add_g(float v) { return add_x32(add_x16(v)); }
__device__ __forceinline__ void add_g3(float& x, float& y, float& z) {
  auto a = __builtin_amdgcn_permlane16_swap(__builtin_bit_cast(unsigned, x), __builtin_bit_cast(unsigned, y), false, false);
  auto b = __builtin_amdgcn_permlane16_swap(__builtin_bit_cast(unsigned, z), __builtin_bit_cast(unsigned, z), false, false);
  const float r = asf(a[0]) + asf(a[1]), t = asf(b[0]) + asf(b[1]);
  auto c = __builtin_amdgcn_permlane32_swap(__builtin_bit_cast(unsigned, r), __builtin_bit_cast(unsigned, r), false, false);
  auto d = __builtin_amdgcn_permlane32_swap(__builtin_bit_cast(unsigned, t), __builtin_bit_cast(unsigned, t), false, false);
  const float u = asf(c[0]) + asf(c[1]);
  z = asf(d[0]) + asf(d[1]);
  auto e = __builtin_amdgcn_permlane16_swap(__builtin_bit_cast(unsigned, u), __builtin_bit_cast(unsigned, u), false, false);
  x = asf(e[0]); y = asf(e[1]);
}
__device__ __forceinline__ u32x4 trade16(u32x4 v) {
  auto a = __builtin_amdgcn_permlane16_swap(v[0], v[2], false, false);
  auto b = __builtin_amdgcn_permlane16_swap(v[1], v[3], false, false);
  return u32x4{(unsigned)a[0], (unsigned)b[0], (unsigned)a[1], (unsigned)b[1]};
}
__device__ __forceinline__ unsigned umax4(u32x4 v) { return max(max(v[0], v[1]), max(v[2], v[3])); }
__device__ __forceinline__ u32x2 unpoison2(u32x2 v) { return u32x2{min(v[0], POISON - 1), min(v[1], POISON - 1)}; }
__device__ __forceinline__ u32x4 unpoison4(u32x4 v) {
  return u32x4{min(v[0], POISON - 1), min(v[1], POISON - 1), min(v[2], POISON - 1), min(v[3], POISON - 1)};
}

typedef union { struct { s16x4 a, b; } hf; u32x4 v; } frag_t;
__device__ __forceinline__ s16x4 trread(const char* smem, unsigned off) {
  return __builtin_amdgcn_ds_read_tr16_b64_v4i16((LDS_AS s16x4*)(smem + off));
}
__device__ __forceinline__ s16x4 trabs(unsigned addr) { return __builtin_amdgcn_ds_read_tr16_b64_v4i16((LDS_AS s16x4*)(size_t)addr); }

constexpr int WA = 6;
constexpr int WB = 4, WR = 3;
#define CH 4, 7, 8, 13, 15, 20, 22, 27, 29
constexpr int CHV[9] = {CH};
constexpr int FSL = CHV[0], FS0 = CHV[1], FS0D = CHV[2], FS1 = CHV[3], FS1S = CHV[4];
constexpr int FXA = CHV[5], FXB = CHV[6], FXL = CHV[7], FXS = CHV[8];
constexpr int FXT = FXB >> 2;
constexpr int CSD = 3, POI = 4, RWS = 5, EIN = 8;
constexpr int ERQ = 16, CSL = 19, CSS = 22;
constexpr int IMG = 24, KP0 = 26;
constexpr int PB0 = 10, PB1 = 24;
constexpr int ABL = 23, ABS = 27;
constexpr int XU = 3, XL = 2;
constexpr int x_slot(int u) { return u % XU == 0 ? 0 : u % XU == 1 ? 32768 : 49152; }
constexpr int TS0 = 19, TSN = 7, TW = 4, TST = 27;
constexpr int QL = 26;
constexpr int RXCD = 5;
constexpr int WDS = 17 - WB;
constexpr int SDI0 = 16, SDIR = 24, GSL = 14, RSL = 42, KSL = 3, XSL = 4, OSL = 12;
constexpr bool tile_piece(int s) { return s >= 3 && ((s - 3) >> 2) + 3 < 8 && ((s - 3) & 3) < 3; }
constexpr int a_req(int s, int n) {
  if (!(n & 1)) return -1;
  if (s <= 1) return 8 + 3 * s + (n >> 1);
  return n < 5 && s + 5 < 32 ? 2 * (s + 5) + (n >> 1) : -1;
}
constexpr int vm_n(int it, int s, int n) {
  int c = 0;
  if (n == 0) c += tile_piece(s);
  if (n == 4) c += (s == ABL) + (it ? (s == FSL ? 5 : 0) : (s == ERQ) + (s >= IMG && s < IMG + 3));
  if (!it) c += (s < 4 && n == 2) + (s == 0 && (n == 0 || n == 4)) + ((n == 3 || n == 5) && s + XL + XU < 32);
  else c += (a_req(s, n) >= 0) + ((s == 0 || s == 4) && n == 2);
  return c;
}
constexpr int vm_at(int it, int s, int n) {
  int c = it ? 8 : 14;
  for (int u = 0; u < 6 * s + n; ++u) c += vm_n(it, u / 6, u % 6);
  return c;
}
constexpr int vm_a(int it, int k) {
  if (k < 4) return 2 * k + 2;
  for (int u = 0; u < 192; ++u)
    if (a_req(u / 6, u % 6) == 2 * k + 1) return vm_at(it, (u + 1) / 6, (u + 1) % 6);
  return 0;
}
constexpr int vm_tile(int it, int t) { return t >= 3 ? vm_at(it, 4 * t - 7, 1) : t == 1 ? vm_at(0, 0, 5) : vm_at(0, 3, 3); }
constexpr int vm_x(int u) { return u == 2 ? 9 : u < XU + XL ? 12 + 2 * (u - XU) : vm_at(0, u - XU - XL + 1, 0); }

constexpr int wo_k(int it, int s) { return it || s < 4 ? 15 - s : s < 8 ? 7 - s : 19 - s; }
constexpr int wo_pos(int it, int s) { return 2 * s + 2 + (s >= WR ? 2 * it : 0); }
constexpr int wo_at(int it, int s) { return 2 * it + (s < WB - WR ? 2 * WR + 2 + 4 * s : s < 16 - WB ? 2 * (2 * WB - WR) + 2 * (s - WB + WR) : 32); }
template <int OFF> __device__ __forceinline__ void lds_count(unsigned ad) {
  asm volatile("s_mov_b64 exec, 1\n\tds_add_u32 %0, %1 offset:%2\n\ts_mov_b64 exec, -1" ::"v"(ad), "v"(1u), "n"(OFF) : "memory");
}
__device__ __forceinline__ void lds_look(char* c, unsigned want) {
  while ((unsigned)__builtin_amdgcn_readfirstlane(__hip_atomic_load((LDS_AS unsigned*)c, __ATOMIC_RELAXED, __HIP_MEMORY_SCOPE_WORKGROUP)) < want) {}
  asm volatile("" ::: "memory");
}
__device__ __forceinline__ void pin1(unsigned& a) { asm volatile("" : "+v"(a)); }
__device__ __forceinline__ void pin2(u32x4& a, u32x4& b) { asm volatile("" : "+v"(a), "+v"(b)::"memory"); }
__device__ __forceinline__ void pin2s(float& a, float& b) { asm volatile("" : "+v"(a), "+v"(b)); }
template <int OFF> __device__ __forceinline__ void lds2(u32x4& a, u32x4& b, unsigned ad) {
  asm volatile("ds_read_b128 %0, %2 offset:%3\n\tds_read_b128 %1, %2 offset:%4" : "=&v"(a), "=&v"(b) : "v"(ad), "n"(OFF), "n"(OFF + 1024) : "memory");
}
template <int N> __device__ __forceinline__ void lds_wait(u32x4& a, u32x4& b) { asm volatile("s_waitcnt lgkmcnt(%2)" : "+v"(a), "+v"(b) : "n"(N) : "memory"); }
__device__ __forceinline__ void pin2f(f32x2& a, f32x2& b) { asm volatile("" : "+v"(a), "+v"(b)); }
__device__ __forceinline__ void pin_ac(f32x4 (&ac)[2][4]) {
  asm volatile("" : "+{v[176:179]}"(ac[0][0]), "+{v[180:183]}"(ac[0][1]), "+{v[184:187]}"(ac[0][2]), "+{v[188:191]}"(ac[0][3]),
                    "+{v[192:195]}"(ac[1][0]), "+{v[196:199]}"(ac[1][1]), "+{v[200:203]}"(ac[1][2]), "+{v[204:207]}"(ac[1][3]));
}

constexpr int L_XC = LDS_BYTES - 64, L_SKEY = 32704, L_SRED = 4096, L_SFIN = 20480;
__device__ __forceinline__ unsigned key_load(rsrc_t rs, int l, int L, int rep) { return ld4(rs, (l & 15) * 4, OFF_KEY + (L * 16 + rep) * 128); }
__device__ __forceinline__ unsigned umax16(unsigned k) {
  k = max(k, dppu<0xB1>(k));
  k = max(k, dppu<0x4E>(k));
  k = max(k, dppu<0x141>(k));
  return max(k, dppu<0x140>(k));
}
__device__ __forceinline__ unsigned key_row(unsigned key) { return (0xffffu - (key & 0xffffu)) & 2047u; }
__device__ __forceinline__ unsigned key_wait(rsrc_t rs, u32x2 T, int l, int L, int rep, unsigned kv) {
  if (!__all(kv != POISON)) {
    unsigned spins = 0;
    do {
      if (spin_fail(spins, rs, T)) break;
      if (L == 0) __builtin_amdgcn_s_sleep(KSL);
      kv = key_load(rs, l, L, rep);
    } while (!__all(kv != POISON));
  }
  return __builtin_amdgcn_readfirstlane(umax16(kv));
}
__device__ __forceinline__ unsigned ffn_xoff(unsigned key, int L, int w) { return OFF_CA + L * 32768 + (key_row(key) >> 7) * 2048 + w * 256; }
__device__ __forceinline__ void row_load(rsrc_t rs, int g, unsigned xo, u32x4 (&xv)[4]) {
  ld16h<0>(xv[0], rs, g * 16, xo); ld16h<64>(xv[1], rs, g * 16, xo); ld16h<128>(xv[2], rs, g * 16, xo); ld16h<192>(xv[3], rs, g * 16, xo);
}
template <int SL = 0> __device__ __forceinline__ void ffn_xwait(rsrc_t rs, u32x2 T, unsigned xo, int l, u32x4 (&xv)[4]) {
#define XV_OK() __all(max(max(umax4(xv[0]), umax4(xv[1])), max(umax4(xv[2]), umax4(xv[3]))) != POISON)
  if (!XV_OK()) {
    unsigned spins = 0;
    do {
      if (spin_fail(spins, rs, T)) break;
      if (SL) __builtin_amdgcn_s_sleep(SL);
#pragma unroll
      for (int i = 0; i < 4; ++i) xv[i] = ld16(rs, (l >> 4) * 16 + i * 64, xo);
    } while (!XV_OK());
  }
#undef XV_OK
}
__device__ __forceinline__ void ffn_gu(const u32x4 (&gv)[4], const u32x4 (&uv)[4], const u32x4 (&xv)[4], f32x4& ga, f32x4& ua) {
  ga = mfma16(gv[0], xv[0], f32x4{0.f, 0.f, 0.f, 0.f}); ua = mfma16(uv[0], xv[0], f32x4{0.f, 0.f, 0.f, 0.f});
#pragma unroll
  for (int i = 1; i < 4; ++i) { ga = mfma16(gv[i], xv[i], ga); ua = mfma16(uv[i], xv[i], ua); }
}
__device__ __forceinline__ void ffn_gu_check(rsrc_t rs, u32x2 T, unsigned xo, int l, const u32x4 (&gv)[4], const u32x4 (&uv)[4], u32x4 (&xv)[4], f32x4& ga, f32x4& ua) {
  if (__builtin_expect(__any(ga[0] != ga[0]), 0)) {
    ffn_xwait(rs, T, xo, l, xv);
    ffn_gu(gv, uv, xv, ga, ua);
  }
}
__device__ __forceinline__ void ffn_gu_store(char* smem, int l, int w, f32x4 ga, f32x4 ua) {
  LDS_AS f32x4* sred = (LDS_AS f32x4*)(smem + L_SRED);
  if ((l & 15) == 0) { sred[w * 8 + (l >> 4)] = ga; sred[w * 8 + 4 + (l >> 4)] = ua; }
}
__device__ __forceinline__ f32x4 ffn_t_read(char* smem, int l) { return ((const LDS_AS f32x4*)(smem + L_SRED))[(l & 7) * 8 + ((l >> 3) & 1) * 4 + (l >> 4)]; }
__device__ __forceinline__ void ffn_d_read(char* smem, int l, int blk, s16x4 (&dfr)[8]) {
#pragma unroll
  for (int i = 0; i < 8; ++i) dfr[i] = trread(smem, L_DN + blk * 4096 + (l >> 4) * 1024 + ((l & 15) >> 2) * 256 + i * 32 + (l & 3) * 8);
}
__device__ __forceinline__ float ffn_d_sum(float t) { t += dppf<0xB1>(t); t += dppf<0x4E>(t); return t + dppf<0x141>(t); }
__device__ __forceinline__ u32x2 ffn_h(f32x4 t, int l) {
#pragma unroll
  for (int e = 0; e < 4; ++e) t[e] = ffn_d_sum(t[e]);
  const float a = (l & 1) ? t[1] : t[0], b = (l & 1) ? t[3] : t[2], s = (l & 2) ? b : a;
  const float gg = rbf1(s), uu = rbf1(dppf<0x128>(s));
  const float h = rbf1(gg * __builtin_amdgcn_rcpf(1.0f + __expf(-gg))) * uu;
  const unsigned h0 = pack2(h, dppf<0x55>(h)), h1 = pack2(dppf<0xAA>(h), dppf<0xFF>(h));
  return (l & 15) == 0 ? u32x2{h0, h1} : u32x2{0u, 0u};
}
__device__ __forceinline__ void ffn_d_mma(const s16x4 (&dfr)[8], u32x2& hh, f32x4& ya, f32x4& yb) {
#pragma unroll
  for (int i = 0; i < 8; ++i) {
    if (i & 1) yb = mfma16k16(dfr[i], __builtin_bit_cast(s16x4, hh), yb);
    else ya = mfma16k16(dfr[i], __builtin_bit_cast(s16x4, hh), ya);
    hh = u32x2{dppu<0x111>(hh[0]), dppu<0x111>(hh[1])};
  }
}
__device__ __forceinline__ void ffn_d_store(rsrc_t rs, int l, int G0, int L, int slice, f32x4 y) {
  const unsigned c = l & 15;
  st16(unpoison4(__builtin_bit_cast(u32x4, y)), rs, (G0 + (c >> 1)) * 32768 + slice * 128 + (c & 1) * 64 + (l >> 4) * 16, OFF_PART + L * (1u << 20));
}
__device__ __forceinline__ void red_load(rsrc_t rs, int tid, int L, unsigned G, unsigned key, u32x4 (&pv)[4], u32x2& cxn, u32x2& cx1) {
#pragma unroll
  for (int k = 0; k < 4; ++k) pv[k] = ld16(rs, min(tid + 512 * k, 1791) * 16, OFF_PART + L * (1u << 20) + G * 32768);
  cxn = ld8(rs, (tid & 3) * 16 + (tid & 4) * 2, OFF_CA + L * 32768 + (key_row(key) >> 7) * 2048 + G * 64);
  cx1 = ld8(rs, (tid & 3) * 16 + (tid & 4) * 2, OFF_CB + L * 32768 + (key_row(key) >> 7) * 2048 + G * 64);
}
__device__ __forceinline__ void red_sum(rsrc_t rs, char* smem, u32x2 T, int tid, int w, int L, unsigned G, unsigned key,
                                        u32x4 (&pv)[4], u32x2& cxn, u32x2& cx1) {
#define PV_OK() __all(max(max(max(umax4(pv[0]), umax4(pv[1])), max(umax4(pv[2]), umax4(pv[3]))), max(max(cxn[0], cxn[1]), max(cx1[0], cx1[1]))) != POISON)
  if (!PV_OK()) {
    unsigned spins = 0;
    do {
      if (spin_fail(spins, rs, T)) break;
      red_load(rs, tid, L, G, key, pv, cxn, cx1);
    } while (!PV_OK());
  }
#undef PV_OK
  f32x4 s = (__builtin_bit_cast(f32x4, pv[0]) + __builtin_bit_cast(f32x4, pv[1])) + __builtin_bit_cast(f32x4, pv[2]);
  if (w < 4) s += __builtin_bit_cast(f32x4, pv[3]);
  ((LDS_AS f32x4*)(smem + L_SFIN))[tid] = s;
}
template <int NW> __device__ __forceinline__ u32x2 red_final(char* smem, int l, unsigned key, u32x2 cxn, u32x2 cx1) {
  const LDS_AS f32x4* sf = (const LDS_AS f32x4*)(smem + L_SFIN) + l;
  f32x4 s = (sf[0] + sf[64]) + (sf[128] + sf[192]);
  if constexpr (NW == 8) s += (sf[256] + sf[320]) + (sf[384] + sf[448]);
#pragma unroll
  for (int j = 0; j < 4; ++j) s[j] = sum_hi8(s[j]);
  const unsigned k16 = key >> 16;
  const float rmax = __builtin_bit_cast(float, ((k16 & 0x8000u) ? (k16 & 0x7fffu) : (~k16 & 0xffffu)) << 16);
  const float sig = rbf1(__builtin_amdgcn_rcpf(1.0f + __expf(-rmax)));
  const float xn4[4] = {lo16(cxn[0]), hi16(cxn[0]), lo16(cxn[1]), hi16(cxn[1])};
  const float x14[4] = {lo16(cx1[0]), hi16(cx1[0]), lo16(cx1[1]), hi16(cx1[1])};
  float o[4];
#pragma unroll
  for (int j = 0; j < 4; ++j) o[j] = x14[j] + rbf1(xn4[j] + rbf1(rbf1(s[j]) * sig));
  return u32x2{pack2(o[0], o[1]), pack2(o[2], o[3])};
}

struct RowFin { float mean, var, mb, rd; unsigned key; };
__device__ __forceinline__ void fin_wait(rsrc_t rs, u32x2 T, unsigned voff, unsigned so, u32x4 (&sp)[4]) {
#define ST_OK() __all(max(max(umax4(sp[0]), umax4(sp[1])), max(umax4(sp[2]), umax4(sp[3]))) != POISON)
  if (!ST_OK()) {
    unsigned spins = 0;
    do {
      if (spin_fail(spins, rs, T)) break;
#pragma unroll
      for (int i = 0; i < 4; ++i) sp[i] = ld16(rs, voff + i * 2048, so);
    } while (!ST_OK());
  }
#undef ST_OK
}
__device__ __forceinline__ RowFin fin_key(rsrc_t rs, u32x2 T, unsigned voff, unsigned so, char* skey, int l, int w, unsigned R, float wsum, u32x4 (&sp)[4]) {
  RowFin r;
#define ST_SUM() ((__builtin_bit_cast(f32x4, sp[0]) + __builtin_bit_cast(f32x4, sp[1])) + (__builtin_bit_cast(f32x4, sp[2]) + __builtin_bit_cast(f32x4, sp[3])))
  f32x4 sf = ST_SUM();
  const float z = (sf[0] + sf[1]) + sf[2];
  if (__any(z != z)) {
    fin_wait(rs, T, voff, so, sp);
    sf = ST_SUM();
  }
#undef ST_SUM
  float s0 = sf[0], s1 = sf[1], s2 = sf[2];
  add_g3(s0, s1, s2);
  r.mean = s0 * (1.0f / 1024.0f);
  r.var = fmaxf(__builtin_fmaf(-r.mean, r.mean, s1 * (1.0f / 1024.0f)), 0.f);
  r.mb = rbf1(r.mean); r.rd = ln_rd(r.var);
  const unsigned rb16 = pack2((s2 - r.mb * wsum) * r.rd, 0.f) & 0xffffu;
  r.key = min((((rb16 & 0x8000u) ? (~rb16 & 0xffffu) : (rb16 | 0x8000u)) << 16) | (0xffffu - R), POISON - 1);
  const unsigned wk = umax16(r.key);
  if (l == 0) ((LDS_AS unsigned*)skey)[w] = wk;
  return r;
}
__device__ __forceinline__ unsigned fin_cand(rsrc_t rs, char* skey, int L, int g, int rb, int ct, const RowFin& r, const f32x2 (&v)[8]) {
  const u32x4 k0 = *(const LDS_AS u32x4*)skey, k1 = *(const LDS_AS u32x4*)(skey + 16);
  const unsigned best = max(umax4(k0), umax4(k1));
  if (r.key == best) {
    const unsigned co = L * 32768 + rb * 2048 + ct * 128 + g * 16;
    f32x2 t[8], rd2 = {r.rd, r.rd}, of2 = {-r.mb * r.rd, -r.mb * r.rd};
    asm volatile("" : "+v"(rd2), "+v"(of2));
#pragma unroll
    for (int j = 0; j < 8; ++j) t[j] = __builtin_elementwise_fma(v[j], rd2, of2);
    st16(unpoison4(pack8v(t)), rs, co, OFF_CA); st16(unpoison4(pack8v(t + 4)), rs, co + 64, OFF_CA);
    if (g == 0) st4(best, rs, OFF_KEY + (L * 16 + ct) * 128 + rb * 4, 0);
    st16(unpoison4(pack8v(v)), rs, co, OFF_CB); st16(unpoison4(pack8v(v + 4)), rs, co + 64, OFF_CB);
  }
  return best;
}
__device__ __forceinline__ void fin_x2(const RowFin& r, f32x2 (&v)[8]) {
  f32x2 sc2 = {1.0f + r.rd, 1.0f + r.rd}, of2 = {-r.mb * r.rd, -r.mb * r.rd};
  asm volatile("" : "+v"(sc2), "+v"(of2));
#pragma unroll
  for (int j = 0; j < 8; ++j) v[j] = __builtin_elementwise_fma(v[j], sc2, of2);
}

extern "C" __device__ unsigned long long dispatch_id() __asm("llvm.amdgcn.dispatch.id");

extern "C" __global__ __launch_bounds__(512, 2) void mega_kernel(const bf16* x, char* ws, const bf16* wqkv, const bf16* wo, const bf16* wr,
                                                                 const bf16* wg, const bf16* wu, const bf16* wd, bf16* out) {
  extern __shared__ __attribute__((aligned(16))) char smem[];
  const int w0_ = __builtin_amdgcn_readfirstlane(threadIdx.x >> 6);
  typedef unsigned long long u64x2 __attribute__((ext_vector_type(2)));
  u64x2 a01;
  unsigned long long a2;
  asm volatile("s_load_dwordx4 %0, %2, 0\n\ts_load_dwordx2 %1, %2, 16" : "=&s"(a01), "=&s"(a2) : "s"(__builtin_amdgcn_kernarg_segment_ptr()));
  const unsigned lbase = (unsigned)(size_t)(LDS_AS char*)smem;
  {
    unsigned long long pc;
    unsigned len;
    asm volatile("s_getpc_b64 %0\n\ts_mov_b32 %1, .Ltext_end-mega_kernel\n\t.subsection 1\n.Ltext_end:\n\t.subsection 0" : "=s"(pc), "=s"(len));
    const unsigned o = ((blockIdx.x >> 3) * 64 + (threadIdx.x & 63)) * 128;
    if (w0_ == 7 && o < len)
      asm volatile("s_mov_b32 m0, %0\n\ts_nop 0\n\tglobal_load_lds_dword %1, %2" ::"s"(lbase + L_TOUCH0), "v"(o), "s"(pc & ~127ull) : "memory");
  }
#define HGRP() ((xcd & 2) + (xcd >> 2))
#define IDS()                                                                                         \
  int tid = threadIdx.x, w = w0_, bid = blockIdx.x;                                                   \
  asm volatile("" : "+v"(tid));                                                                       \
  asm volatile("" : "+s"(w));                                                                         \
  asm volatile("" : "+s"(bid));                                                                       \
  const int l = tid & 63, xcd = bid & 7, bi = bid >> 3;                                               \
  const int p = (xcd & 1) * 4 + (bi & 3), h = HGRP() * 8 + (bi >> 2);         \
  const int rb = 2 * xcd + (bi & 1), ct = bi >> 1;                               \
  const int slice = (xcd - (xcd > RXCD)) * 32 + bi;                                            \
  const bool ffn_on = xcd != RXCD;                                                                    \
  const int g = l >> 4;                                                                               \
  const int rt = w < 4 ? w : 11 - w;          \
  KARGS();                                                                                            \
  (void)p; (void)h; (void)rb; (void)ct; (void)slice; (void)ffn_on; (void)g; (void)rt;
#define PROT() (2 * (p & 3) + (p >> 2))
  typedef const void* const __attribute__((address_space(4)))* karg_t;
#define KARGS() karg_t ka = (karg_t)__builtin_amdgcn_kernarg_segment_ptr(); asm volatile("" : "+s"(ka))
#define ARGP(name, i) const GAS bf16* name = (const GAS bf16*)ka[i]; asm volatile("" : "+s"(name))
#define RSRC(ptr, bytes) __builtin_amdgcn_make_buffer_rsrc((void*)(ptr), 0, bytes, 0x00020000)
#define rwq RSRC(pwq, 1024 * 3072 * 2)
#define rwo RSRC(pwo, 1024 * 1024 * 2)
#define rwg RSRC(pwg, 1024 * F_ * 2)
#define rwu RSRC(pwu, 1024 * F_ * 2)
#define rwd RSRC(pwd, 1024 * F_ * 2)
#define rout RSRC(pout, 2048 * 1024 * 2)

#define WQ_VOFF() ((l >> 2) * 6144 + (((l & 3) ^ ((((l >> 4) ^ (l >> 5)) & 1) << 1)) << 4))
#define WQ_KMAJ(q) (((q) % 3) * 8 + (q) / 3)
#define WQ_PIECE(t, j, dst) dma16(rwq, wqv, ((((t) + rot) & 7) * 128 + ((j) & 7) * 16) * 6144 + ((j) >> 3) * 2048 + h * 64, lbase + (dst) + (j) * 1024)
#define WQ_BOFF(dt) ((g >> 1) * 1024 + (8 * (g & 1) + ((l & 15) >> 2)) * 64 + (l & 3) * 8 + (((dt) ^ (g & 1)) << 5))
#define X_REQ(u, hf) dma16(rx, xv[hf], (p * 256 + rt * 32 + ((u) & 1) * 16 + (hf) * 8) * 2048 + ((((u) >> 2) + rot) & 7) * 256 + (((u) >> 1) & 1) * 128, lbase + rt * 2048 + x_slot(u) + (hf) * 1024)
#define WO_PIECE(j) dma16(rwo, wov, ((w >> 2) * 64 + (w & 3) + 4 * (j)) * 16384 + ct * 128, lbase + 32768 + (j) * 8192 + w * 1024)
#define WD_DMA_OF(v) _Pragma("unroll") for (int i = 0; i < 4; ++i)                                  \
      dma16(rwd, (l >> 4) * 2048 + (l & 15) * 16, (unsigned)(slice * 16 + 4 * i) * 2048 + (v) * 256, lbase + L_DN + (v) * 4096 + i * 1024)
#define FFN_DMA(m) dma16((m) & 1 ? rwu : rwg, fnv, (unsigned)(((4 * w + ((m) >> 1)) * 32) * F_ + slice * 16) * 2, lbase + 65536 + (m) * 8192 + w * 1024)
#define POISON_BIG(L)                                                                                \
  if (w >= 2) {                                                                                      \
    unsigned pz1 = POISON;                                                                           \
    asm volatile("" : "+v"(pz1));                                                                    \
    st16(u32x4{pz1, pz1, pz1, pz1}, rs, (w >= 4 ? OFF_PART + (L) * (1u << 20) + bid * 4096 - 4096 : OFF_STAT + (L) * 524288 + (p * 32 + h) * 2048 - 2048) + tid * 16, 0); \
  }
  u32x4 gv[4], uv[4];
  u32x4 keep[9];
  u32x4 pre[4][2];
#define PRE_LOAD(it_, rot_, s0, s1)                                                                  \
  static_for<(s1) - (s0)>([&](auto c_) __attribute__((always_inline)) {                              \
    constexpr int s = (s0) + decltype(c_)::value;                                                    \
    const unsigned ab_ = OFF_X1 + (((rot_) + (s >> 2)) & 7) * 8192 + ((s >> 1) & 1) * 4096; \
    ld16w<2 * s, (s & 1) * 2048>(pre[s][0], rs, l * 16, ab_ + (p * 8 + rt) * 65536);                         \
    ld16w<2 * s + 1, (s & 1) * 2048 + 1024>(pre[s][1], rs, l * 16, ab_ + (p * 8 + rt) * 65536);                 \
  })
  u32x2 T;
  const GAS bf16* tsrc;
  unsigned xv[2], xa[2];
  u32x2 ep;
  u32x4 rows0[4];
#define X_READ(u, d0, d1) { d0 = *(const LDS_AS u32x4*)(size_t)(xa[0] + x_slot(u)); d1 = *(const LDS_AS u32x4*)(size_t)(xa[1] + x_slot(u)); }
  unsigned g1[5], wqv0;
  {
    IDS();
    const int rot = PROT(), qa = WQ_KMAJ(w), qb = WQ_KMAJ(8 + w);
    wqv0 = WQ_VOFF();
    xv[0] = (l >> 3) * 2048 + (((l & 7) ^ (l >> 4)) << 4); xv[1] = xv[0] ^ 64;
    g1[0] = (rot * 128 + (qa & 7) * 16) * 6144 + (qa >> 3) * 2048 + h * 64; g1[1] = lbase + L_RING + qa * 1024;
    g1[2] = (rot * 128 + (qb & 7) * 16) * 6144 + (qb >> 3) * 2048 + h * 64; g1[3] = lbase + L_RING + qb * 1024;
    g1[4] = (p * 256 + rt * 32) * 2048 + rot * 256;
    asm volatile("" : "+v"(wqv0), "+v"(xv[0]), "+v"(xv[1]));
    asm volatile("s_waitcnt lgkmcnt(0)" : "+s"(a01), "+s"(a2), "+s"(g1[0]), "+s"(g1[1]), "+s"(g1[2]), "+s"(g1[3]), "+s"(g1[4]));
  }
  x = (const bf16*)a01[0]; ws = (char*)a01[1]; wqkv = (const bf16*)a2;
  const rsrc_t rs = __builtin_amdgcn_make_buffer_rsrc((void*)ws, 0, WS_BYTES, 0x00020000);
  const rsrc_t rx = RSRC(x, 2048 * 1024 * 2);
  {
    IDS();
    const int rot = PROT();
    const bf16* pwq = wqkv;
    const unsigned wqv = wqv0;
    dma16(rwq, wqv, g1[0], g1[1]);
#pragma unroll
    for (int i = 0; i < 4; ++i) dma16(rx, xv[i & 1], g1[4] + i * 16384, lbase + rt * 2048 + x_slot(i >> 1) + (i & 1) * 1024);
    dma16(rwq, wqv, g1[2], g1[3]);
    asm volatile("s_barrier" ::: "memory");
    WQ_PIECE(0, WQ_KMAJ(16 + w), L_RING);
    if (w == 6 && bi == 0) {
      ARGP(pwr, 4);
      asm volatile("s_mov_b32 m0, %0\n\ts_nop 0\n\tbuffer_load_dword %1, %2, 0 offen lds" ::"s"(lbase + L_TOUCH0 + 256), "v"((l & 15) * 128), "s"(RSRC(pwr, 2048)) : "memory");
    }
    X_REQ(2, 0); X_REQ(2, 1);
    asm volatile("buffer_load_dwordx2 %0, off, %1, 0 sc1" : "=v"(ep) : "s"(rs) : "memory");
    asm volatile("" : "=v"(rows0[0]), "=v"(rows0[1]), "=v"(rows0[2]), "=v"(rows0[3]));
    if (w < 4) ldrows(rows0, rx, (l >> 5) * 2048 + (l & 31) * 16, (p * 256 + h * 8 + 2 * w) * 2048);
    {
      ARGP(pwo, 3); ARGP(pwg, 5); ARGP(pwu, 6);
      tsrc = (w < 4 ? pwg : pwu) + ((bi * 32 + ((tid & 255) >> 3)) * F_ + (slice >> 5) * 512 + (tid & 7) * 64);
      if (!ffn_on) tsrc = pwo + (bi * 512 + tid) * 64;
      asm volatile("" : "+v"(tsrc));
    }
#pragma unroll
    for (int kk = 0; kk < 2; ++kk) {
      const int r = l & 15;
      xa[kk] = lbase + rt * 2048 + (r >> 3) * 1024 + (r & 7) * 128 + (((4 * kk + g) ^ (r >> 1)) << 4);
      asm volatile("" : "+v"(xa[kk]));
    }
    if (tid == 192) *(LDS_AS u32x4*)(smem + L_QC) = u32x4{0u, 0u, 0u, 0u};
    if (w < 4) wait_vm<8>(); else wait_vm<4>();
    X_READ(0, pre[0][0], pre[1][0]); X_READ(1, pre[0][1], pre[1][1]);
    asm volatile("s_waitcnt lgkmcnt(0)\n\ts_barrier" : "+v"(pre[0][0]), "+v"(pre[0][1]), "+v"(pre[1][0]), "+v"(pre[1][1])::"memory");
    X_REQ(3, 0); X_REQ(3, 1); X_REQ(4, 0); X_REQ(4, 1);
  }
  unsigned sel0 = 0x7FFFFFFFu;
  u32x4 x2p[2];
  unsigned xm0 = 0, kg0 = 0;

  auto layer = [&](auto it_c) __attribute__((always_inline)) {
    constexpr int it = decltype(it_c)::value;
    f32x4 acc[2][6];
    f32x2 qs2[2];
    u32x4 k0, k1, v0, v1;
    unsigned tdum = 0;
    {
      IDS();
      bool aff = false;
      unsigned aq[2];
      u32x4 av[2];
#define PIN_ACC() asm volatile("" : "+{v[176:179]}"(acc[0][0]), "+{v[180:183]}"(acc[0][1]), "+{v[184:187]}"(acc[0][2]), "+{v[188:191]}"(acc[0][3]), "+{v[192:195]}"(acc[0][4]), "+{v[196:199]}"(acc[0][5]), \
                               "+{v[200:203]}"(acc[1][0]), "+{v[204:207]}"(acc[1][1]), "+{v[208:211]}"(acc[1][2]), "+{v[212:215]}"(acc[1][3]), "+{v[216:219]}"(acc[1][4]), "+{v[220:223]}"(acc[1][5]))
#define PINV4(x) asm volatile("" : "+v"(x[0]), "+v"(x[1]), "+v"(x[2]), "+v"(x[3])::"memory")
      auto qkv_loop = [&](auto it_c) __attribute__((always_inline)) {
        constexpr int IT = decltype(it_c)::value;
        const unsigned abase = OFF_X1 + (p * 8 + rt) * 65536;
        const int rot = PROT() + 5 * IT;
        unsigned fkey = 0, kv, bq[2];
        u32x4 fx[4], bv[2], af[WA][2];
        u32x2 cxn, cx1;
        f32x4 sa, sb, cs4[2];
        f32x2 b2[2];
#define AB_OFF() (OFF_AB + IT * 16384 + (p * 8 + rt) * 128)
#define A_LOAD1(k, a) ld16w<(k) % WA * 2 + (a), ((k) & 1) * 2048 + (a) * 1024>(af[(k) % WA][a], rs, l * 16, abase + ((((k) >> 2) + rot) & 7) * 8192 + (((k) >> 1) & 1) * 4096)
#pragma unroll
        for (int s = 0; s < 2 + 2 * IT; ++s) { af[s][0] = pre[s][0]; af[s][1] = pre[s][1]; }
        const bf16* pwq = wqkv;
        const int tstep = TS0 + bid % TSN;
        const unsigned wqv = WQ_VOFF();
#define QKV_DMA1(t, i)                                                                            \
  {                                                                                               \
    if constexpr (IT == 0) WQ_PIECE(t, 3 * w + (i), L_RING + SLOT(t) * STAGE);                    \
    else dma16s<(i) * 1024>(rs, l * 16, OFF_WI + (h * 8 + (((t) + rot) & 7)) * STAGE + w * 3072, lbase + L_RING + SLOT(t) * STAGE + w * 3072); \
  }
        unsigned bo[2][2];
#pragma unroll
        for (int dt = 0; dt < 2 - IT; ++dt) {
          bo[dt][0] = lbase + L_RING + (IT ? l * 16 : WQ_BOFF(dt)); bo[dt][1] = bo[dt][0] + 2 * STAGE;
          asm volatile("" : "+v"(bo[dt][0]), "+v"(bo[dt][1]));
        }
#define FRAG_T() (lbase + L_RING + (4 * g + ((l & 15) >> 2)) * 64 + (l & 3) * 8 + (((g ^ (g >> 1)) & 1) << 5))
        auto frag_t2 = [&](unsigned o) __attribute__((always_inline)) {
          frag_t t;
          t.hf.a = trabs(o); t.hf.b = trabs(o + 1024);
          return t.v;
        };
        unsigned ka[3];
        auto tile_sum = [&](u32x4 pt, int dst) __attribute__((always_inline)) {
          f32x4 c = __builtin_bit_cast(f32x4, pt);
#pragma unroll
          for (int e = 0; e < 4; ++e) { c[e] += dppf<0x124>(c[e]); c[e] += dppf<0x128>(c[e]); c[e] = add_x16(c[e]); }
          if ((l & 28) == 0) *(LDS_AS f32x4*)(smem + dst + (w * 2 + (l >> 5)) * 64 + (l & 3) * 16) = c;
        };
        frag_t bfr[6];
#define QKV_BO(s, n) (bo[(n) & 1][SLOT((s) >> 2) >> 1] + (SLOT((s) >> 2) & 1) * STAGE + ((n) >> 1) * 8192 + ((s) & 3) * 2048)
#define QKV_RDA(s, n)                                                                             \
  {                                                                                               \
    if constexpr (IT == 0) bfr[n].hf.a = trabs(QKV_BO(s, n));                                     \
    else bfr[n].v = *(const LDS_AS u32x4*)(size_t)(bo[0][SLOT((s) >> 2) >> 1] + (SLOT((s) >> 2) & 1) * STAGE + (((s) & 3) * 6 + (n)) * 1024); \
  }
#define QKV_RDB(s, n) { if constexpr (IT == 0) bfr[n].hf.b = trabs(QKV_BO(s, (n) ^ 1) + 256); }
#pragma unroll
        for (int n = 0; n < 6; ++n) { QKV_RDA(0, n); if (n < 5) QKV_RDB(0, n); }
#define XF_OFF() (l & 8 ? ((l & 7) * RX + bi % RX) * 256 + h * 8 : (p * RX + (xcd >> 1) % RX) * 256 + (4 * rt + (l & 3)) * 8)
#define XP_OFF() (OFF_XNEW + ((rot + FXT) & 7) * 256)
#define FP_OFF(i) (w == 3 ? OFF_XNEW + (i) * 1024 : OFF_FX + h * 3072 + w * 1024)
#define FX_OK(n) __all(((n) == 4 ? max(max(umax4(fx[0]), umax4(fx[1])), max(umax4(fx[2]), umax4(fx[3]))) : max(umax4(fx[0]), umax4(fx[1]))) != POISON)
#define SINCE(s0) (vm_at(IT, s, n) - vm_at(IT, s0, 5))
#define DEF4(x) asm volatile("" : "=v"(x[0]), "=v"(x[1]), "=v"(x[2]), "=v"(x[3]))
        LDS_AS unsigned* qcnt = (LDS_AS unsigned*)(smem + L_QC);
        unsigned qc;
        unsigned qadr = lbase + L_QC, qone = 1;
        asm volatile("" : "+v"(qadr), "+v"(qone));
#define count_in(i) asm volatile("s_mov_b64 exec, 1\n\tds_add_u32 %0, %1 offset:%2\n\ts_mov_b64 exec, -1" ::"v"(qadr), "v"(qone), "n"(4 * (i)) : "memory")
        auto count_wait = [&](int i, unsigned want) __attribute__((always_inline)) {
          if (__builtin_expect((unsigned)__builtin_amdgcn_readfirstlane(qc) < want, 0)) {
            while ((unsigned)__builtin_amdgcn_readfirstlane(__hip_atomic_load(qcnt + i, __ATOMIC_RELAXED, __HIP_MEMORY_SCOPE_WORKGROUP)) < want) {}
          }
          asm volatile("" ::: "memory");
        };
        unsigned long long xok = xm0;
        auto x1_need = [&](unsigned long long m) __attribute__((always_inline)) {
          if (__builtin_expect((xok & m) != m, 0)) {
            for (unsigned spins = 0;;) {
              const u32x2 f = ld8(rs, F_X1 + ((p * RX + (xcd >> 1) % RX) * 32 + (l & 31)) * 8, 0);
              xok = __builtin_amdgcn_ballot_w64(f[0] == T[0] && f[1] == T[1]);
              if ((xok & m) == m) break;
              if (spin_fail(spins, rs, T)) { xok = ~0ull; break; }
              __builtin_amdgcn_s_sleep(KSL);
            }
            asm volatile("" ::: "memory");
          }
        };
        auto gap = [&](auto sc_, auto nc_) __attribute__((always_inline)) {
          constexpr int s = decltype(sc_)::value, n = decltype(nc_)::value;
          {
            constexpr int ts = (s + 3) / 4, tc = (s + 2) / 4, t0 = 1;
            if constexpr (ts >= 3 && ts < 8 && s == 4 * ts - 3 && n == 2) { wait_vm<vm_at(IT, s, n) - vm_tile(IT, ts)>(); count_in(0); }
            if constexpr (!IT && tc >= t0 && tc < 3 && s == 4 * tc - 2 && n == 1) { wait_vm<vm_at(IT, s, n) - vm_tile(IT, tc)>(); count_in(0); }
            if constexpr (IT && ts >= t0 && ts < 3 && s == 4 * ts - 3 && n == 2) count_in(0);
            if constexpr (tc >= t0 && tc < 8 && s == 4 * tc - 2) {
              if constexpr (n == 3) qc = __hip_atomic_load(qcnt, __ATOMIC_RELAXED, __HIP_MEMORY_SCOPE_WORKGROUP);
              if constexpr (n == 5) count_wait(0, 8 * (tc - t0 + 1));
            }
          }
          if constexpr (n == 0 && tile_piece(s)) QKV_DMA1(((s - 3) >> 2) + 3, (s - 3) & 3);
          if constexpr (IT == 0) {
            if constexpr (s == 0 && !(n & 1)) QKV_DMA1(1, n >> 1);
            if constexpr (s >= 1 && s < 4 && n == 2) QKV_DMA1(2, s - 1);
            constexpr int u = s + XL;
            if constexpr (n == 1 && u < 32) {
              if (s == 0 && w < 4) wait_vm<vm_at(0, s, n) - vm_x(u) + 4>(); else wait_vm<vm_at(0, s, n) - vm_x(u)>();
              X_READ(u, af[(u & ~1) % WA][u & 1], af[(u | 1) % WA][u & 1]);
            }
            if constexpr (n == 3 && u + XU < 32) { asm volatile("s_waitcnt lgkmcnt(%0)" ::"n"(s + 1 < QL ? 4 : 3) : "memory"); X_REQ(u + XU, 0); }
            if constexpr (n == 5 && u + XU < 32) X_REQ(u + XU, 1);
            if constexpr (s == PB0 && n == 0) { POISON_BIG(0); }
            if constexpr (s == 0 && n == 1) count_in(2);
            if constexpr (s == 0 && n == 3) qc = __hip_atomic_load(qcnt + 2, __ATOMIC_RELAXED, __HIP_MEMORY_SCOPE_WORKGROUP);
            if constexpr (s == 0 && n == 5) count_wait(2, 8);
            if constexpr (s == 1 && n == 1) {
              asm volatile("" : "+v"(ep)::"memory");
              const unsigned long long e = (((unsigned long long)(unsigned)__builtin_amdgcn_readfirstlane(ep[1]) << 32) | (unsigned)__builtin_amdgcn_readfirstlane(ep[0])) + 1ull + dispatch_id() * 0x9E3779B97F4A7C16ull;
              T = u32x2{(unsigned)e, (unsigned)(e >> 32)};
            }
            if constexpr (s == RWS && n == 0) {
              if (w < 4) {
                PINV4(rows0);
                f32x2 sv[16];
#pragma unroll
                for (int i = 0; i < 4; ++i) unpack8v(rows0[i], sv + 4 * i);
                f32x2 t0 = sv[0] + sv[1], t1 = sv[2] + sv[3];
#pragma unroll
                for (int j = 4; j < 16; j += 2) { t0 += sv[j]; t1 += sv[j + 1]; }
                const float m = sum32(hadd(t0 + t1)) * (1.0f / 1024.0f);
                f32x2 m2 = {m, m}, q0 = {0.f, 0.f}, q1 = {0.f, 0.f};
                asm volatile("" : "+v"(m2));
#pragma unroll
                for (int j = 0; j < 16; j += 2) {
                  const f32x2 d0 = sv[j] - m2, d1 = sv[j + 1] - m2;
                  q0 = __builtin_elementwise_fma(d0, d0, q0); q1 = __builtin_elementwise_fma(d1, d1, q1);
                }
                const float rd = ln_rd(sum32(hadd(q0 + q1)) * (1.0f / 1024.0f));
                if ((l & 30) == 0) st4(__builtin_bit_cast(unsigned, (l & 1) ? -rbf1(m) : rd), rs, (l >> 5) * 4 + (l & 1) * 8192, OFF_AB + (p * 256 + h * 8 + 2 * w) * 4);
              }
            }
            if constexpr (s == POI && n == 4) {
              if (w == 3) {
                unsigned pz1 = POISON;
                asm volatile("" : "+v"(pz1));
                const unsigned prb = 2 * p + (h >> 4), pct = h & 15, m = l - 32;
                static_assert(OFF_CB == OFF_CA + 131072, "poison addressing");
                if (l < 60) st16(u32x4{pz1, pz1, pz1, pz1}, rs, l < 32 ? OFF_CA + (l & 8) * 4096 + (l & 16) * 8192 + prb * 2048 + pct * 128 + (l & 7) * 16
                                                             : l < 56 ? OFF_FX + ((h * 6 + (m >> 2)) * 8 + p) * 64 + (l & 3) * 16
                                                                      : OFF_AB + 16384 + (p * 32 + h) * 32 + (l & 1) * 16 + (l & 2) * 4096, 0);
                if (l < 4) st4(pz1, rs, l < 2 ? OFF_XNEW + bid * 8 + l * 4 : OFF_KEY + ((l & 1) * 16 + pct) * 128 + prb * 4, 0);
              }
            }
            if constexpr (s == POI && n == 2) {
              if (w == 2 && l < 6) {
                unsigned f0 = F_KV + KVF(0, p * 32 + h), f1 = F_O + OFB(0, p) + OFL(h), f2 = F_X1 + ((rb >> 1) * RX * 32 + (l & 3) * 32 + (rb & 1) * 16 + ct) * 8;
                asm volatile("" : "+v"(f0), "+v"(f1), "+v"(f2));
                const unsigned fo = l == 5 ? f0 : l == 4 ? f1 : f2;
                touchb(tdum, rs, fo);
              }
            }
            static_assert(vm_x(EIN + XL) >= vm_at(0, EIN - 3, 5) && IMG >= 1 && CSD >= 1, "first flag");
            if constexpr (s == EIN && n == 1) { if (w < 4) count_in(1); }
            if constexpr (s == EIN + 2 && n == 0) qc = __hip_atomic_load(qcnt + 1, __ATOMIC_RELAXED, __HIP_MEMORY_SCOPE_WORKGROUP);
            if constexpr (s == EIN + 2 && n == 2) {
              if (w == 0) { count_wait(1, 4); if (l < RX) st8(T, rs, F_XN + (p * RX + l) * 256 + h * 8, 0); }
            }
            if constexpr (s == ERQ && n == 4) asm volatile("buffer_load_dwordx2 %0, %1, %2, 0 offen sc1" : "=v"(cxn) : "v"(XF_OFF() + F_XN), "s"(rs) : "memory");
            if constexpr (s == CSL && n == 2) {
              wait_vm<SINCE(ERQ)>();
              asm volatile("" : "+v"(cxn)::"memory");
              if (!__all(cxn[0] == T[0] && cxn[1] == T[1])) poll_flags(rs, XF_OFF(), F_XN, T);
            }
          } else {
#define X1_OFF() (F_X1 + ((p * RX + (xcd >> 1) % RX) * 32 + (l & 31)) * 8)
            if constexpr ((s == 0 || s == 4) && n == 2) asm volatile("buffer_load_dwordx2 %0, %1, %2, 0 offen sc1" : "=v"(cxn) : "v"(X1_OFF()), "s"(rs) : "memory");
            if constexpr ((s == 3 || s == 7) && n == 0) {
              wait_vm<vm_at(1, s, 1) - vm_at(1, s - 3, 3)>();
              asm volatile("" : "+v"(cxn)::"memory");
              xok |= __builtin_amdgcn_ballot_w64(cxn[0] == T[0] && cxn[1] == T[1]);
            }
            if constexpr (a_req(s, n) >= 0 && (a_req(s, n) & 7) == 0) x1_need(0x30003ull << (((rot + a_req(s, n) / 8) & 7) * 2));
          }
          if constexpr (IT && a_req(s, n) >= 0) A_LOAD1(a_req(s, n) / 2, a_req(s, n) & 1);
          if constexpr (IT == 0 && s == CSD && n == 4) {
            if (w < 3) {
              unsigned a0 = FRAG_T() + SLOT(s >> 2) * STAGE + w * 8192, a1 = a0 ^ 32, one2 = 0x3F803F80u;
              asm volatile("" : "+v"(a0), "+v"(a1), "+v"(one2));
              const u32x4 o4 = {one2, one2, one2, one2};
              f32x4 y0 = {0.f, 0.f, 0.f, 0.f}, y1 = y0;
#pragma unroll
              for (int i = 0; i < 4; ++i) { y0 = mfma16(frag_t2(a0 + i * 2048), o4, y0); y1 = mfma16(frag_t2(a1 + i * 2048), o4, y1); }
              if ((l & 14) == 0) st16(__builtin_bit_cast(u32x4, (l & 1) ? y1 : y0), rs, g * 16 + (l & 1) * 512, OFF_CS + ((h * 6 + 2 * w) * 8 + p) * 64);
            }
          }
          if constexpr (!IT && s == CSL && n == 4) { asm volatile("" : "=v"(fx[0])); if (w < 3) ld16h<0>(fx[0], rs, l * 16, OFF_CS + h * 3072 + w * 1024); }
          if constexpr (!IT && s == CSS && n == 2) {
            if (w < 3) {
              wait_vm<SINCE(CSL)>();
              asm volatile("" : "+v"(fx[0])::"memory");
              tile_sum(fx[0], L_CSL);
            }
          }
          if constexpr (s == ABL && n == 4) ld4h<0>(aq[0], rs, (l & 31) * 4 + (l >> 5) * 8192, AB_OFF());
          if constexpr (s == ABS && n == 0) {
            wait_vm<SINCE(ABL)>();
            asm volatile("" : "+v"(aq[0])::"memory");
            if constexpr (IT) {
              for (unsigned spins = 0; !__all(aq[0] != POISON) && !spin_fail(spins, rs, T);) {
                ld4h<0>(aq[0], rs, (l & 31) * 4 + (l >> 5) * 8192, AB_OFF());
                asm volatile("s_waitcnt vmcnt(0)" : "+v"(aq[0])::"memory");
              }
            }
            *(LDS_AS unsigned*)(smem + L_K + (8 + rt) * 2048 + l * 4) = aq[0];
          }
          if constexpr (s == ABS && n == 2) {
#pragma unroll
            for (int a = 0; a < 2; ++a) {
              const LDS_AS char* ab = (const LDS_AS char*)(smem + L_K + (8 + rt) * 2048 + a * 64);
              aq[a] = *(const LDS_AS unsigned*)(ab + (l & 15) * 4); bq[a] = *(const LDS_AS unsigned*)(ab + 128 + (l & 15) * 4);
              av[a] = *(const LDS_AS u32x4*)(ab + g * 16); bv[a] = *(const LDS_AS u32x4*)(ab + 128 + g * 16);
            }
          }
          if constexpr (s == ABS && n == 5) {
#pragma unroll
            for (int a = 0; a < 2; ++a) { b2[a] = f32x2{asf(bq[a]), asf(bq[a])}; asm volatile("" : "+v"(b2[a])); }
          }
          if constexpr (s == ABS + 1) {
            constexpr int k = (n + 4) % 6;
            if constexpr (k < 4) cs4[k & 1] = *(const LDS_AS f32x4*)(smem + L_CSL + k * 64 + g * 16);
            else cs4[k & 1][0] = *(const LDS_AS float*)(smem + L_CSL + k * 64 + (l & 15) * 4);
          }
          if constexpr ((s == ABS + 1 && n > 0) || (s == ABS + 2 && n == 0)) {
            constexpr int k = (n + 3) % 6;
            if constexpr (k < 4) {
#pragma unroll
              for (int a = 0; a < 2; ++a) acc[a][k] = __builtin_elementwise_fma(cs4[k & 1], f32x4{b2[a][0], b2[a][1], b2[a][0], b2[a][1]}, acc[a][k]);
            } else {
              f32x2 c2 = {cs4[k & 1][0], cs4[k & 1][0]};
              asm volatile("" : "+v"(c2));
#pragma unroll
              for (int a = 0; a < 2; ++a) acc[a][k] = __builtin_elementwise_fma(f32x4{c2[0], c2[1], c2[0], c2[1]}, __builtin_bit_cast(f32x4, bv[a]), acc[a][k]);
            }
          }
          if constexpr (IT == 0) {
            if constexpr (s == IMG - 2 && n < 3) {
              const int c = 3 * (w & 1) + n;
              ka[n] = (FRAG_T() ^ ((c & 1) << 5)) + (SLOT(5) * STAGE + (c >> 1) * 8192 + (w >> 1) * 2048);
              asm volatile("" : "+v"(ka[n]));
            }
            if constexpr (s == IMG - 1 && (n & 1)) keep[n >> 1] = frag_t2(ka[n >> 1]);
            if constexpr (s > KP0 && s < KP0 + 3 && (n & 1)) keep[3 * (s - KP0) + (n >> 1)] = frag_t2(ka[n >> 1] + (s - KP0) * STAGE);
            if constexpr (s >= IMG && s < IMG + 3 && n == 4) st16(keep[s - IMG], rs, l * 16 + (s - IMG) * 1024, OFF_WI + (h * 8 + ((rot + 5) & 7)) * STAGE + w * 3072);
            if constexpr (s >= TS0 && s < TS0 + TSN && n == 4) {
              if (tstep == s && w < TW) touch(tdum, tsrc);
            }
            if constexpr (s == TST && n == 4) { if (w >= TW) touch(tdum, tsrc); }
          } else {
            if constexpr (s == PB1 && n == 0) { POISON_BIG(1); }
            if constexpr (s == FSL && n == 4) { ld4h<0>(kv, rs, (l & 15) * 4, OFF_KEY + (bid & 15) * 128); row_load(rs, g, ffn_xoff(kg0, 0, w), fx); }
            if constexpr (s == FS0 && n == 0) {
              wait_vm<SINCE(FSL)>();
              asm volatile("" : "+v"(kv)::"memory");
              PINV4(fx);
              if (__builtin_expect(!__all(kv != POISON), 0)) {
                LDS_AS unsigned* kw = (LDS_AS unsigned*)(smem + L_SKEY + 40);
                if (w == 0) {
                  kv = key_wait(rs, T, l, 0, bid & 15, kv);
                } else {
                  while ((kv = __hip_atomic_load(kw, __ATOMIC_RELAXED, __HIP_MEMORY_SCOPE_WORKGROUP)) == POISON) __builtin_amdgcn_s_sleep(2);
                }
              }
              fkey = __builtin_amdgcn_readfirstlane(umax16(kv));
              if (w == 0) *(LDS_AS unsigned*)(smem + L_SKEY + 40) = min(fkey, POISON - 1);
              sel0 = key_row(fkey);
              aff = (int)(sel0 >> 8) == p;
              asm volatile("" : "=v"(sa), "=v"(sb));
              if (ffn_on) {
                if (__builtin_expect(((kg0 ^ fkey) & 0x780u) != 0, 0)) ld4x16(fx, rs, g * 16, ffn_xoff(fkey, 0, w));
                ffn_gu(gv, uv, fx, sa, sb);
              }
            }
            if constexpr (s == FS0 && n == 2) {
              if (ffn_on) {
                ffn_gu_check(rs, T, ffn_xoff(fkey, 0, w), l, gv, uv, fx, sa, sb);
                ffn_gu_store(smem, l, w, sa, sb);
                count_in(1);
              }
            }
            if constexpr (s == FS0D && n == 0) {
              if (ffn_on && w < 4) {
                s16x4 da[8], db[8];
                ffn_d_read(smem, l, 2 * w, da);
                while (__hip_atomic_load(qcnt + 1, __ATOMIC_RELAXED, __HIP_MEMORY_SCOPE_WORKGROUP) < 8u) {}
                asm volatile("" ::: "memory");
                const f32x4 t = ffn_t_read(smem, l);
                ffn_d_read(smem, l, 2 * w + 1, db);
                u32x2 ha = ffn_h(t, l), hb = {dppu<0x118>(ha[0]), dppu<0x118>(ha[1])};
                f32x4 ya = {0.f, 0.f, 0.f, 0.f}, yb = ya;
#pragma unroll
                for (int i = 0; i < 8; ++i) {
                  ya = mfma16k16(da[i], __builtin_bit_cast(s16x4, ha), ya); yb = mfma16k16(db[i], __builtin_bit_cast(s16x4, hb), yb);
                  ha = u32x2{dppu<0x111>(ha[0]), dppu<0x111>(ha[1])}; hb = u32x2{dppu<0x111>(hb[0]), dppu<0x111>(hb[1])};
                }
                ffn_d_store(rs, l, 8 * w, 0, slice, ya + yb);
              }
            }
#define RED_LOAD(ld, l8)                                                                                                    \
  {                                                                                                                         \
    const unsigned G = bi, po = OFF_PART + G * 32768;                                                        \
    ld(gv[0], rs, tid * 16, po); ld(gv[1], rs, tid * 16, po + 4096); ld(gv[2], rs, tid * 16, po + 8192); ld(gv[3], rs, tid * 16, po + 12288); \
    ld(uv[0], rs, tid * 16, po + 16384); ld(uv[1], rs, tid * 16, po + 20480); ld(uv[2], rs, tid * 16, po + 24576);        \
    l8(cxn, rs, (l & 3) * 16 + (l & 4) * 2, OFF_CA + (sel0 >> 7) * 2048 + G * 64);                                         \
    l8(cx1, rs, (l & 3) * 16 + (l & 4) * 2, OFF_CB + (sel0 >> 7) * 2048 + G * 64);                                          \
  }
            if constexpr (s == FS1 && n == 4) {
              asm volatile("" : "=v"(cxn), "=v"(cx1));
              if (!ffn_on && w < 4) RED_LOAD(ld16u, ld8h);
            }
            if constexpr (s == FS1S && n == 0) {
              if (!ffn_on && w < 4) {
                wait_vm<SINCE(FS1)>();
                PINV4(gv); PINV4(uv);
                asm volatile("" : "+v"(cxn), "+v"(cx1)::"memory");
                for (unsigned spins = 0; !__all(max(max(max(umax4(gv[0]), umax4(gv[1])), max(umax4(gv[2]), umax4(gv[3]))),
                                                    max(max(umax4(uv[0]), umax4(uv[1])), max(max(umax4(uv[2]), max(cxn[0], cxn[1])), max(cx1[0], cx1[1])))) != POISON) && !spin_fail(spins, rs, T);) {
                  RED_LOAD(ld16u, ld8h);
                  DRAIN();
                  PINV4(gv); PINV4(uv);
                  asm volatile("" : "+v"(cxn), "+v"(cx1)::"memory");
                }
#define F4(x) __builtin_bit_cast(f32x4, x)
                ((LDS_AS f32x4*)(smem + L_SFIN))[tid] = ((F4(gv[0]) + F4(gv[1])) + (F4(gv[2]) + F4(gv[3]))) + ((F4(uv[0]) + F4(uv[1])) + F4(uv[2]));
#undef F4
                count_in(2);
                if (w == 0) {
                  while (__hip_atomic_load(qcnt + 2, __ATOMIC_RELAXED, __HIP_MEMORY_SCOPE_WORKGROUP) < 4u) {}
                  asm volatile("" ::: "memory");
                  const u32x2 ov = red_final<4>(smem, l, fkey, cxn, cx1);
                  if (l < 8) st8(unpoison2(ov), rs, OFF_XNEW + (bi * 8 + (l & 3) * 2 + (l >> 2)) * 8, 0);
                }
              }
            }
            if constexpr (s == FXA && n == 4) { DEF4(fx); if (w < 3) row_load(rs, g, XP_OFF(), fx); }
            if constexpr (s == FXB && n == 0) {
              if (w < 3) {
                const unsigned fa = bo[0][0] + SLOT(FXT) * STAGE + w * 2048;
                u32x4 f[8];
#pragma unroll
                for (int i = 0; i < 8; ++i) f[i] = *(const LDS_AS u32x4*)(size_t)(fa + (i >> 1) * 6144 + (i & 1) * 1024);
                wait_vm<SINCE(FXA)>();
                PINV4(fx);
                ffn_xwait<XSL>(rs, T, XP_OFF(), l, fx);
                f32x4 y0 = mfma16(f[0], fx[0], f32x4{0.f, 0.f, 0.f, 0.f}), y1 = mfma16(f[1], fx[0], f32x4{0.f, 0.f, 0.f, 0.f});
#pragma unroll
                for (int i = 1; i < 4; ++i) { y0 = mfma16(f[2 * i], fx[i], y0); y1 = mfma16(f[2 * i + 1], fx[i], y1); }
                if ((l & 14) == 0) st16(unpoison4(__builtin_bit_cast(u32x4, (l & 1) ? y1 : y0)), rs, g * 16 + (l & 1) * 512, OFF_FX + ((h * 6 + 2 * w) * 8 + p) * 64);
              }
            }
            if constexpr (s == FXL && n == 4) {
              asm volatile("" : "=v"(fx[0]), "=v"(fx[1]));
              if (aff && w < 4) { ld16h<0>(fx[0], rs, l * 16, FP_OFF(0)); ld16h<0>(fx[1], rs, l * 16, FP_OFF(1)); }
            }
            if constexpr (s == FXS && n == 2) {
              if (aff && w < 4) {
                wait_vm<0>();
                asm volatile("" : "+v"(fx[0]), "+v"(fx[1])::"memory");
                if (!FX_OK(2)) {
                  unsigned spins = 0;
                  do {
                    if (spin_fail(spins, rs, T)) break;
                    fx[0] = ld16(rs, l * 16, FP_OFF(0)); fx[1] = ld16(rs, l * 16, FP_OFF(1));
                  } while (!FX_OK(2));
                }
                if (w == 3) {
                  f32x2 nx[8];
                  unpack8v(fx[0], nx); unpack8v(fx[1], nx + 4);
                  const f32x2 sm = ((nx[0] + nx[1]) + (nx[2] + nx[3])) + ((nx[4] + nx[5]) + (nx[6] + nx[7]));
                  const float mean = sum64(hadd(sm)) * (1.0f / 1024.0f);
                  const f32x2 m2 = {mean, mean};
                  f32x2 q = {0.f, 0.f};
#pragma unroll
                  for (int j = 0; j < 8; ++j) { const f32x2 d = nx[j] - m2; q = __builtin_elementwise_fma(d, d, q); }
                  const float rd = ln_rd(sum64(hadd(q)) * (1.0f / 1024.0f));
                  if (l == 0) *(LDS_AS f32x2*)(smem + L_FXL + 384) = f32x2{rd, -rbf1(mean)};
                } else {
                  tile_sum(fx[0], L_FXL);
                }
                count_in(3);
              }
            }
          }
        };
        static_for<32>([&](auto sc_) __attribute__((always_inline)) {
          constexpr int s = decltype(sc_)::value;
          if constexpr (s > 0) PIN_ACC();
          if constexpr (IT == 1) wait_vm<vm_at(IT, s, 0) - vm_a(IT, s)>();
          pin_w<s % WA>(af[s % WA][0], af[s % WA][1]);
          SB();
          static_for<6>([&](auto nc_) __attribute__((always_inline)) {
            constexpr int n = decltype(nc_)::value;
            if constexpr (s == 0) { acc[0][n] = f32x4{0.f, 0.f, 0.f, 0.f}; acc[1][n] = acc[0][n]; }
            constexpr bool on = s < QL || n >= 2;
            if constexpr (on) acc[0][n] = n < 4 ? mfma16(bfr[n].v, af[s % WA][0], acc[0][n]) : mfma16(af[s % WA][0], bfr[n].v, acc[0][n]);
            SB();
            if constexpr (n == 0) { QKV_RDB(s, 5); }
            else if constexpr (s + 1 < 32 && (s + 1 < QL || n > 2)) QKV_RDB(s + 1, n - 1);
            SB();
            if constexpr (on) acc[1][n] = n < 4 ? mfma16(bfr[n].v, af[s % WA][1], acc[1][n]) : mfma16(af[s % WA][1], bfr[n].v, acc[1][n]);
            SB();
            if constexpr (s + 1 < 32 && (s + 1 < QL || n >= 2)) QKV_RDA(s + 1, n);
            SB();
            gap(sc_, nc_);
            SB();
          });
        });
        PIN_ACC();
        frag_t qw[32 - QL][2];
        static_for<32 - QL>([&](auto c_) __attribute__((always_inline)) {
          constexpr int s = QL + decltype(c_)::value;
          QKV_RDA(s, 0); QKV_RDB(s, 0); QKV_RDA(s, 1); QKV_RDB(s, 1);
          qw[s - QL][0] = bfr[0]; qw[s - QL][1] = bfr[1];
        });
        SB();
        const bool own = aff && (int)((sel0 >> 5) & 7) == rt;
        const bool m0 = !(sel0 & 16), mq = (l & 15) == (int)(sel0 & 15), mv = g == (int)((sel0 & 15) >> 2);
        auto patch_qk = [&](int n) __attribute__((always_inline)) {
          f32x4 t = *(const LDS_AS f32x4*)(smem + L_FXL + n * 64 + g * 16);
          const f32x4 c = *(const LDS_AS f32x4*)(smem + L_CSL + n * 64 + g * 16);
          const float nb = *(const LDS_AS float*)(smem + L_FXL + 388);
#pragma unroll
          for (int e = 0; e < 4; ++e) { t[e] = __builtin_fmaf(c[e], nb, t[e]); asm volatile("" : "+v"(t[e])); }
          acc[0][n] = (mq && m0) ? t : acc[0][n];
          acc[1][n] = (mq && !m0) ? t : acc[1][n];
        };
        if (own) {
          while (__hip_atomic_load(qcnt + 3, __ATOMIC_RELAXED, __HIP_MEMORY_SCOPE_WORKGROUP) < 4u) {}
          asm volatile("" ::: "memory");
          patch_qk(2); patch_qk(3);
          const f32x2 nab = *(const LDS_AS f32x2*)(smem + L_FXL + 384);
          const LDS_AS float* fl_ = (const LDS_AS float*)(smem + L_FXL + 256) + (l & 15);
          const LDS_AS float* cl_ = (const LDS_AS float*)(smem + L_CSL + 256) + (l & 15);
          const float t4 = __builtin_fmaf(cl_[0], nab[1], fl_[0]), t5 = __builtin_fmaf(cl_[16], nab[1], fl_[16]);
          const unsigned na = __builtin_bit_cast(unsigned, (float)nab[0]);
#pragma unroll
          for (int a = 0; a < 2; ++a) {
            const bool ma = a ? !m0 : m0;
            aq[a] = (mq && ma) ? na : aq[a];
#pragma unroll
            for (int e = 0; e < 4; ++e) {
              const bool me = mv && ma && e == (int)(sel0 & 3);
              acc[a][4][e] = me ? t4 : acc[a][4][e]; acc[a][5][e] = me ? t5 : acc[a][5][e];
              av[a][e] = me ? na : av[a][e];
            }
          }
        }
        constexpr float C2 = 1.4426950408889634f * 0.17677669529663687f;
#pragma unroll
        for (int a = 0; a < 2; ++a) {
          f32x2 a2 = {asf(aq[a]), asf(aq[a])};
          qs2[a] = f32x2{asf(aq[a]) * C2, asf(aq[a]) * C2};
          asm volatile("" : "+v"(a2), "+v"(qs2[a]));
          acc[a][2] *= f32x4{a2[0], a2[1], a2[0], a2[1]}; acc[a][3] *= f32x4{a2[0], a2[1], a2[0], a2[1]};
          acc[a][4] *= __builtin_bit_cast(f32x4, av[a]); acc[a][5] *= __builtin_bit_cast(f32x4, av[a]);
        }
        k0 = pack8(acc[0][2], acc[0][3]); k1 = pack8(acc[1][2], acc[1][3]); v0 = pack8(acc[0][4], acc[1][4]); v1 = pack8(acc[0][5], acc[1][5]);
        const unsigned kvo = OFF_KV + IT * (8u << 20) + (p * 32 + h) * 32768 + rt * 4096;
        if (p < 7) { st16(k0, rs, l * 16, kvo); st16(k1, rs, l * 16 + 1024, kvo); st16(v0, rs, l * 16 + 2048, kvo); st16(v1, rs, l * 16 + 3072, kvo); }
        char* ownp = smem + (8 + rt) * 2048 + l * 16;
        *(u32x4*)(ownp + L_K) = k0;
        if (rt < 7) *(u32x4*)(ownp + L_K + 1024) = k1;
        *(u32x4*)(ownp + L_V) = v0; *(u32x4*)(ownp + L_V + 1024) = v1;
        SB();
        static_for<32 - QL>([&](auto c_) __attribute__((always_inline)) {
          constexpr int s = QL + decltype(c_)::value;
#pragma unroll
          for (int n = 0; n < 2; ++n) { acc[0][n] = mfma16(qw[s - QL][n].v, af[s % WA][0], acc[0][n]); acc[1][n] = mfma16(qw[s - QL][n].v, af[s % WA][1], acc[1][n]); }
        });
        if (own) { patch_qk(0); patch_qk(1); }
      };
      qkv_loop(it_c);
    }
#undef A_LOAD1
#undef QKV_RDA
#undef QKV_RDB
#undef QKV_BO
    {
      IDS();
      const GAS bf16 *pwo, *pwg, *pwu;
      asm volatile("s_load_dwordx2 %0, %3, 24\n\ts_load_dwordx2 %1, %3, 40\n\ts_load_dwordx2 %2, %3, 48" : "=&s"(pwo), "=&s"(pwg), "=&s"(pwu) : "s"(ka));
      const int ql = l & 15;
      const f32x4 z4 = {0.f, 0.f, 0.f, 0.f}, ninf = {-INFINITY, -INFINITY, -INFINITY, -INFINITY};
      LDS_AS unsigned* rdy = (LDS_AS unsigned*)(smem + L_RDY);
      const unsigned rtag = T[0] + it;
      u32x4 qf[2];
      f32x4 cn[2], oa[2][2], sum[2], S[2][2];
      unsigned one2 = 0x3F803F80u;
      asm volatile("" : "+v"(one2));
      const u32x4 ones = {one2, one2, one2, one2};
      auto ex8 = [&](f32x4 a, f32x4 b) __attribute__((always_inline)) {
        return u32x4{pack2(__builtin_amdgcn_exp2f(a[0]), __builtin_amdgcn_exp2f(a[1])), pack2(__builtin_amdgcn_exp2f(a[2]), __builtin_amdgcn_exp2f(a[3])),
                     pack2(__builtin_amdgcn_exp2f(b[0]), __builtin_amdgcn_exp2f(b[1])), pack2(__builtin_amdgcn_exp2f(b[2]), __builtin_amdgcn_exp2f(b[3]))};
      };
      {
#pragma unroll
        for (int a = 0; a < 2; ++a) {
          const f32x4 c4 = {qs2[a][0], qs2[a][1], qs2[a][0], qs2[a][1]};
          qf[a] = pack8(acc[a][0] * c4, acc[a][1] * c4);
        }
        f32x4 mk;
#pragma unroll
        for (int e = 0; e < 4; ++e) mk[e] = (4 * g + e <= ql) ? 0.f : -INFINITY;
        const f32x4 d0 = mfma16(k0, qf[0], mk), d1 = mfma16(k1, qf[1], mk), d2 = mfma16(k0, qf[1], z4);
        const float m0 = -max_x32(max_x16(fmaxf(fmaxf(d0[0], d0[1]), fmaxf(d0[2], d0[3]))));
        const float m1 = -max_x32(max_x16(fmaxf(fmaxf(d1[0], d1[1]), fmaxf(d1[2], d1[3]))));
        cn[0] = f32x4{m0, m0, m0, m0}; cn[1] = f32x4{m1, m1, m1, m1};
        asm volatile("" : "+v"(cn[0]), "+v"(cn[1]));
        const f32x4 e0 = d0 + cn[0];
        const u32x4 p0 = {pack2(__builtin_amdgcn_exp2f(e0[0]), __builtin_amdgcn_exp2f(e0[1])), pack2(__builtin_amdgcn_exp2f(e0[2]), __builtin_amdgcn_exp2f(e0[3])), 0u, 0u};
        const u32x4 p1 = ex8(d2 + cn[1], d1 + cn[1]);
        oa[0][0] = mfma16(v0, p0, z4); oa[0][1] = mfma16(v1, p0, z4); sum[0] = mfma16(ones, p0, z4);
        oa[1][0] = mfma16(v0, p1, z4); oa[1][1] = mfma16(v1, p1, z4); sum[1] = mfma16(ones, p1, z4);
      }
      const char* kb_ = smem + L_K + l * 16;
      const char* vb_ = smem + L_V + l * 16;
      const unsigned wov = (l >> 3) * 2048 + (((((l & 7) >> 1) ^ ((l >> 4) & 3)) * 2 + (l & 1)) << 4), fnv = (l >> 1) * (F_ * 2) + (l & 1) * 16;
      const unsigned pp = __builtin_amdgcn_readfirstlane(p > 0 ? 32 : 0), pvo = OFF_KV + it * (8u << 20) + (p * 32 + h - pp) * 32768;
      const unsigned pfo = __builtin_amdgcn_readfirstlane(F_KV + KVF(it, p * 32 + h - pp));
      asm volatile("" : "+v"(oa[0][0]), "+v"(oa[0][1]), "+v"(oa[1][0]), "+v"(oa[1][1]), "+v"(sum[0]), "+v"(sum[1]));
      DRAIN();
      asm volatile("" : "+v"(tdum));
      if (l == 0) st8(T, rs, rt * 8, F_KV + KVF(it, p * 32 + h));
      BAR();
      asm volatile("" : "+s"(pwo), "+s"(pwg), "+s"(pwu));
      WO_PIECE(15); WO_PIECE(14);
      u32x4 kf[2], vf[2];
      int nw = 0;
      u32x4 p0, p1;
      float e0, e1;
#define EE(v, i) { e0 = __builtin_amdgcn_exp2f(v[i]); e1 = __builtin_amdgcn_exp2f(v[i + 1]); pin2s(e0, e1); SB(); }
#define CV(P, i) { unsigned t_ = pack2(e0, e1); pin1(t_); P[i] = t_; SB(); }
      auto body = [&](unsigned ad, auto dk_) __attribute__((always_inline)) {
        constexpr int dk = decltype(dk_)::value;
        const f32x4 a0 = S[1][0], a1 = S[1][1];
        sum[0] = mfma16(ones, p0, sum[0]); SB();
        EE(a0, 0);
        lds_wait<2>(kf[0], kf[1]);
        S[0][0] = mfma16(kf[0], qf[0], cn[0]); SB();
        CV(p1, 0); EE(a0, 2);
        S[0][1] = mfma16(kf[1], qf[0], cn[0]); SB();
        CV(p1, 1); EE(a1, 0);
        lds_wait<0>(vf[0], vf[1]);
        oa[0][0] = mfma16(vf[0], p0, oa[0][0]); SB();
        CV(p1, 2); EE(a1, 2);
        oa[0][1] = mfma16(vf[1], p0, oa[0][1]); SB();
        CV(p1, 3);
        S[1][0] = mfma16(kf[0], qf[1], cn[1]); SB();
        EE(S[0][0], 0);
        S[1][1] = mfma16(kf[1], qf[1], cn[1]); SB();
        lds2<2048 + dk>(kf[0], kf[1], ad); SB();
        CV(p0, 0); EE(S[0][0], 2);
        oa[1][0] = mfma16(vf[0], p1, oa[1][0]); SB();
        CV(p0, 1); EE(S[0][1], 0);
        oa[1][1] = mfma16(vf[1], p1, oa[1][1]); SB();
        lds2<2048 + L_V>(vf[0], vf[1], ad); SB();
        CV(p0, 2); EE(S[0][1], 2);
        sum[1] = mfma16(ones, p1, sum[1]); SB();
        CV(p0, 3);
      };
      auto last = [&]() __attribute__((always_inline)) {
        sum[0] = mfma16(ones, p0, sum[0]); SB();
        EE(S[1][0], 0);
        lds_wait<0>(vf[0], vf[1]);
        pin2(kf[0], kf[1]);
        oa[0][0] = mfma16(vf[0], p0, oa[0][0]); SB();
        CV(p1, 0); EE(S[1][0], 2);
        oa[0][1] = mfma16(vf[1], p0, oa[0][1]); SB();
        CV(p1, 1); EE(S[1][1], 0);
        const float f0 = __builtin_amdgcn_exp2f(S[1][1][2]), f1 = __builtin_amdgcn_exp2f(S[1][1][3]);
        p1[2] = pack2(e0, e1); p1[3] = pack2(f0, f1);
        oa[1][0] = mfma16(vf[0], p1, oa[1][0]); oa[1][1] = mfma16(vf[1], p1, oa[1][1]); sum[1] = mfma16(ones, p1, sum[1]);
      };
      auto piece = [&]() __attribute__((always_inline)) {
        if constexpr (it == 1) { WO_PIECE(13 - nw); }
        else if (ffn_on) { FFN_DMA(nw); }
        ++nw;
      };
      if (rt > 0) {
        unsigned ad = lbase + l * 16 + (6 + rt) * 2048;
        lds2<2048>(kf[0], kf[1], ad); lds2<2048 + L_V>(vf[0], vf[1], ad);
        lds_wait<2>(kf[0], kf[1]);
        S[0][0] = mfma16(kf[0], qf[0], cn[0]); S[0][1] = mfma16(kf[1], qf[0], cn[0]); S[1][0] = mfma16(kf[0], qf[1], cn[1]); S[1][1] = mfma16(kf[1], qf[1], cn[1]);
        SB();
        lds_wait<0>(vf[0], vf[1]);
        lds2<0>(kf[0], kf[1], ad);
        p0 = ex8(S[0][0], S[0][1]);
        lds_wait<0>(kf[0], kf[1]);
#pragma unroll 1
        for (int i = rt - 1; i > 0; --i) {
          ad -= 2048;
          body(ad, std::integral_constant<int, -2048>{});
          piece();
          SB();
        }
        piece();
        last();
      }
      {
        constexpr int FW = 4, FN = 8 / FW;
        if (w < FW) {
          const int g0 = FN * rt;
          poll_flags(rs, (l & (FN - 1)) * 8, pfo + g0 * 8, T);
#pragma unroll
          for (int j = 0; j < FN; ++j) {
            const unsigned d = lbase + (g0 + j) * 2048, o = pvo + (g0 + j) * 4096;
            dma16s<0>(rs, l * 16, o, d + L_K); dma16s<1024>(rs, l * 16, o, d + L_K);
            dma16s<0>(rs, l * 16, o + 2048, d + L_V); dma16s<1024>(rs, l * 16, o + 2048, d + L_V);
          }
        }
        if (p == 0) { cn[0] = ninf; cn[1] = ninf; }
        f32x4 mk;
#pragma unroll
        for (int e = 0; e < 4; ++e) mk[e] = (4 * g + e > ql) ? 0.f : -INFINITY;
        const f32x4 cm0 = cn[0] + mk, cm1 = cn[1] + mk;
        if (w < FW) {
          if constexpr (FN > 1) { wait_vm<2 * FN>(); if (l < FN / 2) rdy[FN * rt + l] = rtag; }
          DRAIN();
          if (l < FN) rdy[FN * rt + l] = rtag;
        }
        if constexpr (it == 1) { WO_PIECE(6); WO_PIECE(5); WO_PIECE(4); }
        else { WO_PIECE(13); WO_PIECE(12); if (ffn_on) FFN_DMA(7); }

        int have = rt - 1;
        auto need = [&](int gq) __attribute__((always_inline)) {
          if (__builtin_expect(gq > have && gq < 8, 0)) {
            do {
              const unsigned t = __hip_atomic_load(rdy + (l & 7), __ATOMIC_RELAXED, __HIP_MEMORY_SCOPE_WORKGROUP);
              have = rt - 1 + __builtin_ctz(~((unsigned)__builtin_amdgcn_ballot_w64(t == rtag) & 0xffu) >> rt);
            } while (gq > have);
            asm volatile("" ::: "memory");
          }
        };
        unsigned ad = lbase + l * 16 + rt * 2048;
        need(rt);
        lds2<0>(kf[0], kf[1], ad); lds2<L_V>(vf[0], vf[1], ad);
        lds_wait<2>(kf[0], kf[1]);
        S[0][0] = mfma16(kf[0], qf[0], cm0); S[0][1] = mfma16(kf[1], qf[0], cn[0]); S[1][0] = ninf; S[1][1] = mfma16(kf[1], qf[1], cm1);
        SB();
        lds_wait<0>(vf[0], vf[1]);
        need(rt + 1);
        lds2<2048>(kf[0], kf[1], ad);
        p0 = ex8(S[0][0], S[0][1]);
        lds_wait<0>(kf[0], kf[1]);
#pragma unroll 1
        for (int gq = rt + 1; gq < 8; ++gq) {
          need(gq + 1);
          body(ad, std::integral_constant<int, 2048>{});
          piece();
          SB();
          ad += 2048;
        }
        last();
      }
      {
        float big = 0.f;
#pragma unroll
        for (int a = 0; a < 2; ++a)
#pragma unroll
          for (int d = 0; d < 2; ++d)
            big = __builtin_elementwise_maximum(__builtin_elementwise_maximum(big, __builtin_elementwise_maximum(fabsf(oa[a][d][0]), fabsf(oa[a][d][1]))),
                                                __builtin_elementwise_maximum(fabsf(oa[a][d][2]), fabsf(oa[a][d][3])));
        bool ok = sum[0][0] >= 0.5f && sum[0][0] < 0x1p64f && sum[1][0] >= 0.5f && sum[1][0] < 0x1p64f && big < INFINITY;
        if (!__all(ok)) {
#pragma unroll
          for (int a = 0; a < 2; ++a) {
            const int qs = 2 * rt + a, qq = 256 + 16 * qs + ql, klo = max(qq - 255, p > 0 ? 0 : 256);
            auto score = [&](int t) __attribute__((always_inline)) {
              f32x4 s = mfma16(t == 31 ? k1 : *(const u32x4*)(kb_ + t * 1024), qf[a], z4);
#pragma unroll
              for (int e = 0; e < 4; ++e) { const int kk = 16 * t + 4 * g + e; s[e] = (kk <= qq && kk >= klo) ? s[e] : -INFINITY; }
              return s;
            };
            float mx = -INFINITY;
#pragma unroll 1
            for (int t = qs; t <= qs + 16; ++t) { const f32x4 s = score(t); mx = fmaxf(fmaxf(mx, fmaxf(s[0], s[1])), fmaxf(s[2], s[3])); }
            mx = max_x32(max_x16(mx));
            oa[a][0] = z4; oa[a][1] = z4; sum[a] = z4;
#pragma unroll 1
            for (int G = qs >> 1; G <= (qs + 16) >> 1; ++G) {
              const f32x4 s0 = score(2 * G), s1 = score(2 * G + 1);
              f32x4 e0, e1;
#pragma unroll
              for (int e = 0; e < 4; ++e) { e0[e] = __builtin_amdgcn_exp2f(s0[e] - mx); e1[e] = __builtin_amdgcn_exp2f(s1[e] - mx); }
              const u32x4 pf = pack8(e0, e1);
              oa[a][0] = mfma16(*(const u32x4*)(vb_ + G * 2048), pf, oa[a][0]);
              oa[a][1] = mfma16(*(const u32x4*)(vb_ + G * 2048 + 1024), pf, oa[a][1]);
              sum[a] = mfma16(ones, pf, sum[a]);
            }
          }
        }
      }
#pragma unroll
      for (int a = 0; a < 2; ++a) {
        const float inv = __builtin_amdgcn_rcpf(sum[a][0]);
        oa[a][0] *= inv; oa[a][1] *= inv;
      }
      const unsigned oo = OFF_O + it * (4u << 20) + ((p * 8 + rt) * 64 + h * 2) * 1024;
      st16(pack8(oa[0][0], oa[0][1]), rs, l * 16, oo);
      st16(pack8(oa[1][0], oa[1][1]), rs, l * 16 + 1024, oo);
      DRAIN();
      BAR();
      if (tid == 0) st8(T, rs, OFL(h), F_O + OFB(it, p));
      if constexpr (it == 0) {
        DEF4(gv); DEF4(uv);
        if (ffn_on) {
          const unsigned ro = 65536 + w * 1024 + (4 * g + ((l & 15) >> 2)) * 32 + (l & 3) * 8;
#pragma unroll
          for (int i = 0; i < 4; ++i) {
            frag_t f;
            f.hf.a = trread(smem, ro + i * 16384); f.hf.b = trread(smem, ro + i * 16384 + 512);
            gv[i] = f.v;
            f.hf.a = trread(smem, ro + i * 16384 + 8192); f.hf.b = trread(smem, ro + i * 16384 + 8704);
            uv[i] = f.v;
          }
        }
        PINV4(gv); PINV4(uv);
        {
          ARGP(px, 0);
          const unsigned xo = ((rb * 4 + (w & 3)) * 32 + (w >> 2) * 16) * 2048 + ct * 128, xw = (l >> 3) * 2048 + (((l & 7) ^ g) << 4);
          dma16(RSRC(px, 2048 * 1024 * 2), xw, xo, lbase + w * 4096); dma16(RSRC(px, 2048 * 1024 * 2), xw ^ 64, xo + 16384, lbase + w * 4096 + 1024);
        }
      }
      WO_PIECE(3); WO_PIECE(2); WO_PIECE(1); WO_PIECE(0);
      if constexpr (it == 0) { WO_PIECE(11); WO_PIECE(10); WO_PIECE(9); WO_PIECE(8); WO_PIECE(7); WO_PIECE(6); WO_PIECE(5); WO_PIECE(4); }
    }
    IDS();
    ARGP(pwr, 4); ARGP(pwd, 7); ARGP(pout, 8);
    const int kh = w >> 2, rt2 = rb * 4 + (w & 3);
    const unsigned R = rt2 * 32 + kh * 16 + (l & 15);
    const unsigned to = (rt2 * 64 + ct * 4 + kh) * 1024 + l * 16;
    const unsigned rmo = ct * 64 + ((g & 1) ? 12 + 4 * g : 4 * g);
    f32x2 v[8], wv[8];
    float wsum;
    {
      u32x4 wa[2], wrp[2], res[2];
#pragma unroll
      for (int j = 0; j < 2; ++j) {
        wrp[j] = *(const GAS u32x4*)(pwr + rmo + 32 * j);
        wa[j] = *(const GAS u32x4*)(pwr + l * 8 + j * 512);
      }
      {
        f32x2 t[8];
        unpack8v(wa[0], t); unpack8v(wa[1], t + 4);
        const f32x2 s = ((t[0] + t[1]) + (t[2] + t[3])) + ((t[4] + t[5]) + (t[6] + t[7]));
        wsum = sum64(hadd(s));
        unpack8v(trade16(wrp[0]), wv); unpack8v(trade16(wrp[1]), wv + 4);
        asm volatile("" : "+v"(wsum), "+v"(wv[0]), "+v"(wv[1]), "+v"(wv[2]), "+v"(wv[3]), "+v"(wv[4]), "+v"(wv[5]), "+v"(wv[6]), "+v"(wv[7]));
      }
      unsigned bt[4];
#pragma unroll
      for (int n = 0; n < 4; ++n) {
        bt[n] = lbase + 32768 + 65536 + kh * 4096 + (4 * g + ((l & 15) >> 2)) * 128 + (l & 3) * 8 + ((n ^ (2 * (g & 1) + ((l & 15) >> 3))) << 5);
        asm volatile("" : "+v"(bt[n]));
      }
      if (w == 0) poll_flags(rs, OFL(l & 31), F_O + OFB(it, rb >> 1), T);
      else if (it == 0 && w < 3) poll_flags2(rs, F_XN + (((w - 1) * 4 + g) * RX + bi % RX) * 256 + (l & 15) * 16, T);
      else if (it == 0 && w == 3) poll_flags(rs, ((l >> 3) * 32 + h) * 128 + (l & 7) * 8, F_KV, T);
      asm volatile("s_barrier" ::: "memory");
      const unsigned abase = OFF_O + it * (4u << 20) + (rt2 * 64 + 32 * kh) * 1024;
      const unsigned av0 = l * 16 + kh * 1024, av1 = l * 16 + (kh ^ 1) * 1024;
      u32x4 af[WB][2];
#define O_LOAD(s_) { ld16h<(wo_k(it, s_) & 1) * 2048>(af[(s_) % WB][0], rs, av0, abase + (wo_k(it, s_) >> 1) * 4096); ld16h<(wo_k(it, s_) & 1) * 2048>(af[(s_) % WB][1], rs, av1, abase + (wo_k(it, s_) >> 1) * 4096); }
      if (w >= 4) __builtin_amdgcn_s_setprio(1);
      static_for<WR>([&](auto c_) __attribute__((always_inline)) { O_LOAD(decltype(c_)::value); });
      if constexpr (it == 1) { ld16h<0>(res[0], rs, g * 16, OFF_XNEW + ct * 128); ld16h<64>(res[1], rs, g * 16, OFF_XNEW + ct * 128); }
      SB();
      f32x4 ac[2][4];
#pragma unroll
      for (int a = 0; a < 2; ++a)
#pragma unroll
        for (int n = 0; n < 4; ++n) ac[a][n] = f32x4{0.f, 0.f, 0.f, 0.f};
      u32x4 bfr[2][4];
#define WO_RD(buf, s, n)                                                                             \
  {                                                                                                  \
    const unsigned o_ = bt[n] + (wo_k(it, s) & 7) * 8192;                                             \
    frag_t f_;                                                                                       \
    f_.hf.a = trabs(o_); f_.hf.b = trabs(o_ + 2048);                                                 \
    bfr[buf][n] = f_.v;                                                                              \
  }
#pragma unroll
      for (int n = 0; n < 4; ++n) WO_RD(0, 0, n);
      static_for<16>([&](auto sc_) __attribute__((always_inline)) {
        constexpr int s = decltype(sc_)::value, cur = s & 1;
        pin_ac(ac);
        if constexpr (s == 7) __builtin_amdgcn_s_setprio(0);
        if constexpr (s == 1) lds_count<0>(lbase + L_QC);
        if constexpr (s == (it == 0 ? 3 : 11)) lds_look(smem + L_QC, 64);
        if constexpr (s < 15 && ((wo_k(it, s) ^ wo_k(it, s + 1)) & 8)) {
#pragma unroll
          for (int n = 0; n < 4; ++n) { bt[n] += (wo_k(it, s) & 8) ? -65536 : 65536; pin1(bt[n]); }
        }
        if constexpr (s < WB - WR) O_LOAD(WR + 2 * s);
        if constexpr (it == 0 && s == 8) lds_count<8>(lbase + L_QC);
        if constexpr (it == 0 && s == WDS) {
          DRAIN();
          if (ffn_on) { lds_look(smem + L_QC + 8, 16); WD_DMA_OF(w); }
        }
        if constexpr (it == 1 || s < WDS) wait_vm<wo_at(it, s) - wo_pos(it, s)>();
        pin2(af[s % WB][0], af[s % WB][1]);
        if constexpr (it == 1 && s == 9) pin2(res[0], res[1]);
        SB();
#pragma unroll
        for (int n = 0; n < 4; ++n) {
          ac[0][n] = mfma16(bfr[cur][n], af[s % WB][0], ac[0][n]);
          ac[1][n] = mfma16(bfr[cur][n], af[s % WB][1], ac[1][n]);
          if (s + 1 < 16) WO_RD(cur ^ 1, s + 1, n);
          if (it == 0 && s == 8) {
            const u32x2 t = *(const LDS_AS u32x2*)(smem + w * 4096 + (l & 15) * 128 + (((2 * n + (g >> 1)) ^ ((l & 15) >> 1)) << 4) + (g & 1) * 8);
            res[n >> 1][2 * (n & 1)] = t[0]; res[n >> 1][2 * (n & 1) + 1] = t[1];
          }
          if (s == 9) {
            if (it == 1 && n == 0) { res[0] = R == sel0 ? res[0] : x2p[0]; res[1] = R == sel0 ? res[1] : x2p[1]; }

            v[2 * n] = f32x2{lo16(res[n >> 1][2 * (n & 1)]), hi16(res[n >> 1][2 * (n & 1)])};
            v[2 * n + 1] = f32x2{lo16(res[n >> 1][2 * (n & 1) + 1]), hi16(res[n >> 1][2 * (n & 1) + 1])};
            pin2f(v[2 * n], v[2 * n + 1]);
          }
          SB();
        }
        if constexpr (s < WB - WR) { O_LOAD(WR + 2 * s + 1); }
        else if constexpr (s + WB < 16) { O_LOAD(s + WB); }
        SB();
      });
#undef WO_RD
#undef O_LOAD
#define XCH_AT(v) ((v) == 7 ? 155648 : (v) * 4096)
#pragma unroll
      for (int n = 0; n < 4; ++n) *(f32x4*)(smem + XCH_AT(w) + n * 1024 + l * 16) = ac[1][n];
#pragma unroll
      for (int n = 0; n < 4; ++n) { v[2 * n] += f32x2{ac[0][n][0], ac[0][n][1]}; v[2 * n + 1] += f32x2{ac[0][n][2], ac[0][n][3]}; }
      BAR();
#pragma unroll
      for (int n = 0; n < 4; ++n) {
        const f32x4 o = *(const f32x4*)(smem + XCH_AT(w ^ 4) + n * 1024 + l * 16);
        v[2 * n] += f32x2{o[0], o[1]}; v[2 * n + 1] += f32x2{o[2], o[3]};
      }
    }
    {
      if (it == 0) { st16(pack8v(v), rs, to, OFF_X1); st16(pack8v(v + 4), rs, to + 2048, OFF_X1); }
      SB();
      const f32x2 s = ((v[0] + v[1]) + (v[2] + v[3])) + ((v[4] + v[5]) + (v[6] + v[7]));
      f32x2 dp = v[0] * wv[0], q = v[0] * v[0];
#pragma unroll
      for (int j = 1; j < 8; ++j) { dp = __builtin_elementwise_fma(v[j], wv[j], dp); q = __builtin_elementwise_fma(v[j], v[j], q); }
      float mt = hadd(s), qq = hadd(q), dot = hadd(dp);
      add_g3(mt, qq, dot);
      const unsigned so = OFF_STAT + it * 524288 + rb * 32768, lrow = R & 127;
      if (g == 0) st16(unpoison4(__builtin_bit_cast(u32x4, f32x4{mt, qq, dot, 0.f})), rs, ct * 2048 + lrow * 16, so);
      SB();
      if (it == 0) wait_vm<1>();
      if constexpr (it == 0) {
        volatile LDS_AS u32x2* in = (volatile LDS_AS u32x2*)(smem + L_XC);
        if (l == 0) in[w] = T;
        const u32x2 t = in[l & 7];
        if (__all(t[0] == T[0] && t[1] == T[1]) && l < RX) st8(T, rs, (l * 32 + (rb & 1) * 16 + ct) * 8, F_X1 + (rb >> 1) * RX * 32 * 8);
      }
      if (it == 1) { if (ffn_on) { WD_DMA_OF(w); } }
      if (it == 0) {
#pragma unroll
        for (int j = 0; j < 9; ++j) *(u32x4*)(smem + L_RING + (j / 3) * STAGE + (w * 3 + j % 3) * 1024 + l * 16) = keep[j];
      }
      __builtin_amdgcn_s_sleep(it == 0 ? SDI0 : SDIR);
      char* const skey = smem + (it == 0 ? L_SKEY : L_XC);
      u32x4 sp[4];
      ld16h<0>(sp[0], rs, g * 8192 + lrow * 16, so); ld16h<2048>(sp[1], rs, g * 8192 + lrow * 16, so);
      ld16h<0>(sp[2], rs, g * 8192 + lrow * 16, so + 4096); ld16h<2048>(sp[3], rs, g * 8192 + lrow * 16, so + 4096);
      u32x2 xf;
      unsigned k_;
      asm volatile("" : "=v"(xf), "=v"(k_));
      if (it == 0 && w == 0) { ld8h(xf, rs, (l & 31) * 8, F_X1 + (p * RX + (xcd >> 1) % RX) * 32 * 8); ld4h<0>(k_, rs, (l & 15) * 4, OFF_KEY + (bid & 15) * 128); }
      DRAIN();
      asm volatile("" : "+v"(xf), "+v"(k_)::"memory");
      PINV4(sp);
      const RowFin r = fin_key(rs, T, g * 8192 + lrow * 16, so, skey, l, w, R, wsum, sp);
      if constexpr (it == 0) {
        if (w == 0) {
          const unsigned need = 0x30003u << (((PROT() + 5) & 7) * 2);
          unsigned m = (unsigned)__builtin_amdgcn_ballot_w64(xf[0] == T[0] && xf[1] == T[1]);
          for (unsigned spins = 0; (m & need) != need;) {
            if (spin_fail(spins, rs, T)) { m = ~0u; break; }
            const u32x2 f = ld8(rs, F_X1 + ((p * RX + (xcd >> 1) % RX) * 32 + (l & 31)) * 8, 0);
            k_ = key_load(rs, l, 0, bid & 15);
            m = (unsigned)__builtin_amdgcn_ballot_w64(f[0] == T[0] && f[1] == T[1]);
          }
          k_ = umax16(k_ == POISON ? 0u : k_);
          if (l == 0) *(LDS_AS u32x4*)(skey + 32) = u32x4{m, k_, POISON, 0u};
        }
        if (tid == 192) *(LDS_AS u32x4*)(smem + L_QC) = u32x4{0u, 0u, 0u, 0u};
      }
      BAR();
      if constexpr (it == 0) {
        PRE_LOAD(1, PROT() + 5, 0, 4);
        const u32x2 mk = *(const LDS_AS u32x2*)(skey + 32);
        xm0 = __builtin_amdgcn_readfirstlane(mk[0]); kg0 = __builtin_amdgcn_readfirstlane(mk[1]);
      }
      const unsigned own = fin_cand(rs, skey, it, g, rb, ct, r, v);
      if constexpr (it == 0) kg0 = max(kg0, (unsigned)__builtin_amdgcn_readfirstlane(own));
      fin_x2(r, v);
      if constexpr (it == 0) {
        x2p[0] = pack8v(v); x2p[1] = pack8v(v + 4);
        asm volatile("" : "+v"(x2p[0]), "+v"(x2p[1]));
        if ((w & 3) * 2 + (w >> 2) == ct >> 1) {
          const float f = 1.0f + r.rd, rd2 = ln_rd(r.var * f * f), mb2 = rbf1(__builtin_fmaf(r.mean - r.mb, r.rd, r.mean));
          if (g < 2 && ((l >> 3) & 1) == (ct & 1))
            st4(min(__builtin_bit_cast(unsigned, g == 0 ? f * rd2 : -__builtin_fmaf(r.mb, r.rd, mb2) * __builtin_amdgcn_rcpf(f)), POISON - 1), rs, OFF_AB + 16384 + R * 4 + g * 8192, 0);
        }
      }
    }
    if (it == 1) {
      const u32x4 op0 = trade16(pack8v(v)), op1 = trade16(pack8v(v + 4));
#define OUT_STORE()                                                                                 \
  if (R != key_row(key)) {                                                                          \
    __builtin_amdgcn_raw_buffer_store_b128(op0, rout, (R * 1024 + rmo) * 2, 0, SC1);                  \
    __builtin_amdgcn_raw_buffer_store_b128(op1, rout, (R * 1024 + rmo + 32) * 2, 0, SC1);             \
  }
      u32x4 xv[4];
      unsigned guess = 0;
      {
        __builtin_amdgcn_s_sleep(GSL);
        const unsigned k_ = key_load(rs, l, 1, bid & 15);
        guess = ffn_xoff(__builtin_amdgcn_readfirstlane(umax16(k_ == POISON ? 0u : k_)), 1, w);
        row_load(rs, g, guess, xv);
      }
      if (w == 0) {
        const unsigned k_ = key_wait(rs, T, l, 1, bid & 15, key_load(rs, l, 1, bid & 15));
        if (l == 0) *(LDS_AS unsigned*)(smem + L_SKEY + 32) = k_;
      }
      DRAIN();
      PINV4(xv);
      BAR();
      const unsigned key = __builtin_amdgcn_readfirstlane(*(LDS_AS unsigned*)(smem + L_SKEY + 32));
      if (ffn_on) {
        f32x4 ga, ua;
        s16x4 dfr[8];
        ffn_d_read(smem, l, w, dfr);
        if (guess != ffn_xoff(key, 1, w)) ld4x16(xv, rs, g * 16, ffn_xoff(key, 1, w));
        ffn_gu(gv, uv, xv, ga, ua);
        ffn_gu_check(rs, T, ffn_xoff(key, 1, w), l, gv, uv, xv, ga, ua);
        ffn_gu_store(smem, l, w, ga, ua);
        BAR();
        f32x4 ya = {0.f, 0.f, 0.f, 0.f}, yb = ya;
        u32x2 hh = ffn_h(ffn_t_read(smem, l), l);
        ffn_d_mma(dfr, hh, ya, yb);
        if ((l & 15) < 8) ffn_d_store(rs, l, 4 * w, 1, slice, ya + yb);
        SB();
        __builtin_amdgcn_s_sleep(OSL);
        OUT_STORE();
      } else {
        OUT_STORE();
        const unsigned G = bi;
        if (G == 0 && tid == 0) st8(T, rs, F_EP, 0);
        u32x4 pv[4];
        u32x2 cxn, cx1;
        __builtin_amdgcn_s_sleep(RSL);
        red_load(rs, tid, 1, G, key, pv, cxn, cx1);
        red_sum(rs, smem, T, tid, w, 1, G, key, pv, cxn, cx1);
        BAR();
        if (w == 0) {
          const u32x2 ov = red_final<8>(smem, l, key, cxn, cx1);
          if (l < 8) __builtin_amdgcn_raw_buffer_store_b64(ov, rout, (key_row(key) * 1024 + G * 32 + l * 4) * 2, 0, 0);
        }
      }
#undef OUT_STORE
    }
  };
  layer(std::integral_constant<int, 0>{});
  layer(std::integral_constant<int, 1>{});
}

extern "C" void kernel_launch(void* const* d_in, const int* in_sizes, int n_in, void* d_out, int out_size,
                              void* d_ws, size_t ws_size, hipStream_t stream) {
  (void)hipFuncSetAttribute((const void*)mega_kernel, hipFuncAttributeMaxDynamicSharedMemorySize, LDS_BYTES);
  mega_kernel<<<256, 512, LDS_BYTES, stream>>>((const bf16*)d_in[0], (char*)d_ws, (const bf16*)d_in[1], (const bf16*)d_in[2], (const bf16*)d_in[4],
                                               (const bf16*)d_in[5], (const bf16*)d_in[6], (const bf16*)d_in[7], (bf16*)d_out);
}
```

```cpp
#include <hip/hip_runtime.h>
#include <stdint.h>
#include <utility>

typedef __bf16 bf16;
typedef __bf16 bf16x8 __attribute__((ext_vector_type(8)));
typedef __bf16 bf16x2 __attribute__((ext_vector_type(2)));
typedef float f32x4 __attribute__((ext_vector_type(4)));
typedef float f32x2 __attribute__((ext_vector_type(2)));
typedef short s16x4 __attribute__((ext_vector_type(4)));
typedef unsigned int u32x4 __attribute__((ext_vector_type(4)));
typedef unsigned int u32x2 __attribute__((ext_vector_type(2)));
#define LDS_AS __attribute__((address_space(3)))
#define GAS __attribute__((address_space(1)))
typedef GAS unsigned gu32;
typedef GAS unsigned long long gu64;
#define RLX_AGENT __ATOMIC_RELAXED, __HIP_MEMORY_SCOPE_AGENT
typedef __amdgpu_buffer_rsrc_t rsrc_t;

constexpr int F_ = 3584;
constexpr int SC1 = 16;

constexpr int RX = 4;
constexpr unsigned F_EP = 0, F_TMO = 128;
constexpr unsigned F_XN = 4096;
constexpr unsigned F_O = 8u << 20;
constexpr unsigned F_X1 = F_O + 131072;
constexpr unsigned OFF_KEY = F_O + 393216;
constexpr unsigned F_KV = F_O + 524288;
#define KVF(it, i) ((i) * 128 + (it) * 64)
#define OFL(h) (((h) >> 3) * 128 + ((h) & 7) * 8)
#define OFB(it, p) ((p) * 512 + (it) * 64)
constexpr unsigned OFF_AB = 147456;
constexpr unsigned OFF_XNEW = 262144;
constexpr unsigned OFF_CS = 294912;
constexpr unsigned OFF_FX = 393216;
constexpr unsigned OFF_CA = 524288;
constexpr unsigned OFF_CB = 655360;
constexpr unsigned OFF_STAT = 1u << 20;
constexpr unsigned OFF_PART = 3u << 20;
constexpr unsigned OFF_X1 = 12u << 20;
constexpr unsigned OFF_O = 16u << 20;
constexpr unsigned OFF_KV = 32u << 20;
constexpr unsigned OFF_WI = 52u << 20;
constexpr unsigned WS_BYTES = 58u << 20;
constexpr unsigned POISON = 0xFFFFFFFFu;

constexpr int L_CSL = 31872, L_FXL = 32256, L_K = 0, L_V = 32768, L_RING = 65536, L_DN = 32768, L_TOUCH0 = 149504, L_RDY = 31744, L_QC = 31808, LDS_BYTES = 163840;
constexpr int STAGE = 24576;
#define SLOT(t) ((t) & 3)

template <typename F, int... I> __device__ __forceinline__ void static_for_impl(F&& f, std::integer_sequence<int, I...>) {
  (f(std::integral_constant<int, I>{}), ...);
}
template <int N, typename F> __device__ __forceinline__ void static_for(F&& f) { static_for_impl(f, std::make_integer_sequence<int, N>{}); }
__device__ __forceinline__ unsigned pack2(float a, float b) {
  f32x2 v = {a, b};
  return __builtin_bit_cast(unsigned, __builtin_convertvector(v, bf16x2));
}
__device__ __forceinline__ float asf(unsigned u) { return __builtin_bit_cast(float, u); }
__device__ __forceinline__ float lo16(unsigned u) { return __builtin_bit_cast(float, u << 16); }
__device__ __forceinline__ float hi16(unsigned u) { return __builtin_bit_cast(float, u & 0xffff0000u); }
__device__ __forceinline__ u32x4 pack8(const f32x4 a, const f32x4 b) {
  u32x4 o;
  o[0] = pack2(a[0], a[1]); o[1] = pack2(a[2], a[3]); o[2] = pack2(b[0], b[1]); o[3] = pack2(b[2], b[3]);
  return o;
}
__device__ __forceinline__ f32x4 mfma16(u32x4 a, u32x4 b, f32x4 c) {
  return __builtin_amdgcn_mfma_f32_16x16x32_bf16(__builtin_bit_cast(bf16x8, a), __builtin_bit_cast(bf16x8, b), c, 0, 0, 0);
}
__device__ __forceinline__ f32x4 mfma16k16(s16x4 a, s16x4 b, f32x4 c) { return __builtin_amdgcn_mfma_f32_16x16x16bf16_1k(a, b, c, 0, 0, 0); }
template <int CTRL> __device__ __forceinline__ float dppf(float v) {
  return __builtin_bit_cast(float, __builtin_amdgcn_update_dpp(0, __builtin_bit_cast(int, v), CTRL, 0xf, 0xf, true));
}
template <int CTRL> __device__ __forceinline__ unsigned dppu(unsigned v) {
  return (unsigned)__builtin_amdgcn_update_dpp(0, (int)v, CTRL, 0xf, 0xf, true);
}
__device__ __forceinline__ float add_x16(float v) {
  auto r = __builtin_amdgcn_permlane16_swap(__builtin_bit_cast(unsigned, v), __builtin_bit_cast(unsigned, v), false, false);
  return __builtin_bit_cast(float, (unsigned)r[0]) + __builtin_bit_cast(float, (unsigned)r[1]);
}
__device__ __forceinline__ float add_x32(float v) {
  auto r = __builtin_amdgcn_permlane32_swap(__builtin_bit_cast(unsigned, v), __builtin_bit_cast(unsigned, v), false, false);
  return __builtin_bit_cast(float, (unsigned)r[0]) + __builtin_bit_cast(float, (unsigned)r[1]);
}
__device__ __forceinline__ float max_x16(float v) {
  auto r = __builtin_amdgcn_permlane16_swap(__builtin_bit_cast(unsigned, v), __builtin_bit_cast(unsigned, v), false, false);
  return fmaxf(__builtin_bit_cast(float, (unsigned)r[0]), __builtin_bit_cast(float, (unsigned)r[1]));
}
__device__ __forceinline__ float max_x32(float v) {
  auto r = __builtin_amdgcn_permlane32_swap(__builtin_bit_cast(unsigned, v), __builtin_bit_cast(unsigned, v), false, false);
  return fmaxf(__builtin_bit_cast(float, (unsigned)r[0]), __builtin_bit_cast(float, (unsigned)r[1]));
}
__device__ __forceinline__ unsigned umax_x16(unsigned v) {
  auto r = __builtin_amdgcn_permlane16_swap(v, v, false, false);
  return max((unsigned)r[0], (unsigned)r[1]);
}
__device__ __forceinline__ unsigned umax_x32(unsigned v) {
  auto r = __builtin_amdgcn_permlane32_swap(v, v, false, false);
  return max((unsigned)r[0], (unsigned)r[1]);
}
__device__ __forceinline__ float sum_hi8(float v) {
  v += dppf<0x128>(v);
  v = add_x16(v);
  return add_x32(v);
}
__device__ __forceinline__ float sum32(float v) {
  v += dppf<0xB1>(v);
  v += dppf<0x4E>(v);
  v += dppf<0x124>(v);
  v += dppf<0x128>(v);
  return add_x16(v);
}
__device__ __forceinline__ float sum64(float v) {
  v += dppf<0xB1>(v);
  v += dppf<0x4E>(v);
  v += dppf<0x124>(v);
  v += dppf<0x128>(v);
  v = add_x16(v);
  return add_x32(v);
}

#define BAR() asm volatile("s_waitcnt lgkmcnt(0)\n\ts_barrier" ::: "memory")
#define DRAIN() asm volatile("s_waitcnt vmcnt(0)" ::: "memory")
#define SB() __builtin_amdgcn_sched_barrier(0)
template <int N> __device__ __forceinline__ void wait_vm() { asm volatile("s_waitcnt vmcnt(%0)" ::"n"(N) : "memory"); }

__device__ __forceinline__ void dma16(rsrc_t rs, unsigned voff, unsigned soff, unsigned lds) {
  asm volatile("s_mov_b32 m0, %0\n\ts_nop 0\n\tbuffer_load_dwordx4 %1, %2, %3 offen lds" ::"s"(lds), "v"(voff), "s"(rs), "s"(soff) : "memory");
}
template <int OFF> __device__ __forceinline__ void dma16s(rsrc_t rs, unsigned voff, unsigned soff, unsigned lds) {
  asm volatile("s_mov_b32 m0, %0\n\ts_nop 0\n\tbuffer_load_dwordx4 %1, %2, %3 offen offset:%4 sc1 lds" ::"s"(lds), "v"(voff), "s"(rs), "s"(soff), "n"(OFF) : "memory");
}
__device__ __forceinline__ void touch(unsigned& d, const GAS bf16* ptr) { asm volatile("global_load_dword %0, %1, off" : "+v"(d) : "v"(ptr) : "memory"); }
__device__ __forceinline__ void ldrows(u32x4 (&d)[4], rsrc_t rx, unsigned voff, unsigned soff) {
  asm volatile("buffer_load_dwordx4 %0, %4, %5, %6 offen\n\tbuffer_load_dwordx4 %1, %4, %5, %6 offen offset:512\n\t"
               "buffer_load_dwordx4 %2, %4, %5, %6 offen offset:1024\n\tbuffer_load_dwordx4 %3, %4, %5, %6 offen offset:1536"
               : "=&v"(d[0]), "=&v"(d[1]), "=&v"(d[2]), "=&v"(d[3]) : "v"(voff), "s"(rx), "s"(soff) : "memory");
}
__device__ __forceinline__ void touchb(unsigned& d, rsrc_t rs, unsigned vo) { asm volatile("buffer_load_dword %0, %1, %2, 0 offen sc1" : "+v"(d) : "v"(vo), "s"(rs) : "memory"); }
__device__ __forceinline__ float rbf1(float x) { return __builtin_bit_cast(float, pack2(0.f, x)); }
__device__ __forceinline__ u32x4 ld16(rsrc_t rs, unsigned voff, unsigned soff) { return __builtin_amdgcn_raw_buffer_load_b128(rs, voff, soff, SC1); }
template <int OFF> __device__ __forceinline__ void ld16h(u32x4& d, rsrc_t rs, unsigned voff, unsigned soff) {
  asm volatile("buffer_load_dwordx4 %0, %1, %2, %3 offen offset:%4 sc1" : "=v"(d) : "v"(voff), "s"(rs), "s"(soff), "n"(OFF) : "memory");
}
template <int I, int OFF> __device__ __forceinline__ void ld16w(u32x4& d, rsrc_t rs, unsigned voff, unsigned soff) {
  if constexpr (I == 0) asm volatile("buffer_load_dwordx4 %0, %1, %2, %3 offen offset:%4 sc1" : "={v[128:131]}"(d) : "v"(voff), "s"(rs), "s"(soff), "n"(OFF) : "memory");
  else if constexpr (I == 1) asm volatile("buffer_load_dwordx4 %0, %1, %2, %3 offen offset:%4 sc1" : "={v[132:135]}"(d) : "v"(voff), "s"(rs), "s"(soff), "n"(OFF) : "memory");
  else if constexpr (I == 2) asm volatile("buffer_load_dwordx4 %0, %1, %2, %3 offen offset:%4 sc1" : "={v[136:139]}"(d) : "v"(voff), "s"(rs), "s"(soff), "n"(OFF) : "memory");
  else if constexpr (I == 3) asm volatile("buffer_load_dwordx4 %0, %1, %2, %3 offen offset:%4 sc1" : "={v[140:143]}"(d) : "v"(voff), "s"(rs), "s"(soff), "n"(OFF) : "memory");
  else if constexpr (I == 4) asm volatile("buffer_load_dwordx4 %0, %1, %2, %3 offen offset:%4 sc1" : "={v[144:147]}"(d) : "v"(voff), "s"(rs), "s"(soff), "n"(OFF) : "memory");
  else if constexpr (I == 5) asm volatile("buffer_load_dwordx4 %0, %1, %2, %3 offen offset:%4 sc1" : "={v[148:151]}"(d) : "v"(voff), "s"(rs), "s"(soff), "n"(OFF) : "memory");
  else if constexpr (I == 6) asm volatile("buffer_load_dwordx4 %0, %1, %2, %3 offen offset:%4 sc1" : "={v[152:155]}"(d) : "v"(voff), "s"(rs), "s"(soff), "n"(OFF) : "memory");
  else if constexpr (I == 7) asm volatile("buffer_load_dwordx4 %0, %1, %2, %3 offen offset:%4 sc1" : "={v[156:159]}"(d) : "v"(voff), "s"(rs), "s"(soff), "n"(OFF) : "memory");
  else if constexpr (I == 8) asm volatile("buffer_load_dwordx4 %0, %1, %2, %3 offen offset:%4 sc1" : "={v[160:163]}"(d) : "v"(voff), "s"(rs), "s"(soff), "n"(OFF) : "memory");
  else if constexpr (I == 9) asm volatile("buffer_load_dwordx4 %0, %1, %2, %3 offen offset:%4 sc1" : "={v[164:167]}"(d) : "v"(voff), "s"(rs), "s"(soff), "n"(OFF) : "memory");
  else if constexpr (I == 10) asm volatile("buffer_load_dwordx4 %0, %1, %2, %3 offen offset:%4 sc1" : "={v[168:171]}"(d) : "v"(voff), "s"(rs), "s"(soff), "n"(OFF) : "memory");
  else if constexpr (I == 11) asm volatile("buffer_load_dwordx4 %0, %1, %2, %3 offen offset:%4 sc1" : "={v[172:175]}"(d) : "v"(voff), "s"(rs), "s"(soff), "n"(OFF) : "memory");
}
template <int K> __device__ __forceinline__ void pin_w(u32x4& a, u32x4& b) {
  if constexpr (K == 0) asm volatile("" : "+{v[128:131]}"(a), "+{v[132:135]}"(b)::"memory");
  else if constexpr (K == 1) asm volatile("" : "+{v[136:139]}"(a), "+{v[140:143]}"(b)::"memory");
  else if constexpr (K == 2) asm volatile("" : "+{v[144:147]}"(a), "+{v[148:151]}"(b)::"memory");
  else if constexpr (K == 3) asm volatile("" : "+{v[152:155]}"(a), "+{v[156:159]}"(b)::"memory");
  else if constexpr (K == 4) asm volatile("" : "+{v[160:163]}"(a), "+{v[164:167]}"(b)::"memory");
  else if constexpr (K == 5) asm volatile("" : "+{v[168:171]}"(a), "+{v[172:175]}"(b)::"memory");
}
__device__ __forceinline__ void ld8h(u32x2& d, rsrc_t rs, unsigned voff, unsigned soff) {
  asm volatile("buffer_load_dwordx2 %0, %1, %2, %3 offen sc1" : "=v"(d) : "v"(voff), "s"(rs), "s"(soff) : "memory");
}
__device__ __forceinline__ void ld16u(u32x4& d, rsrc_t rs, unsigned voff, unsigned soff) {
  asm volatile("buffer_load_dwordx4 %0, %1, %2, %3 offen sc1" : "+v"(d) : "v"(voff), "s"(rs), "s"(soff) : "memory");
}
template <int OFF> __device__ __forceinline__ void ld4h(unsigned& d, rsrc_t rs, unsigned voff, unsigned soff) {
  asm volatile("buffer_load_dword %0, %1, %2, %3 offen offset:%4 sc1" : "=v"(d) : "v"(voff), "s"(rs), "s"(soff), "n"(OFF) : "memory");
}
__device__ __forceinline__ void ld4x16(u32x4 (&d)[4], rsrc_t rs, unsigned voff, unsigned soff) {
  asm volatile("buffer_load_dwordx4 %0, %4, %5, %6 offen sc1\n\tbuffer_load_dwordx4 %1, %4, %5, %6 offen offset:64 sc1\n\t"
               "buffer_load_dwordx4 %2, %4, %5, %6 offen offset:128 sc1\n\tbuffer_load_dwordx4 %3, %4, %5, %6 offen offset:192 sc1\n\ts_waitcnt vmcnt(0)"
               : "=&v"(d[0]), "=&v"(d[1]), "=&v"(d[2]), "=&v"(d[3]) : "v"(voff), "s"(rs), "s"(soff) : "memory");
}
__device__ __forceinline__ u32x2 ld8(rsrc_t rs, unsigned voff, unsigned soff) { return __builtin_amdgcn_raw_buffer_load_b64(rs, voff, soff, SC1); }
__device__ __forceinline__ void st16(u32x4 v, rsrc_t rs, unsigned voff, unsigned soff) { __builtin_amdgcn_raw_buffer_store_b128(v, rs, voff + soff, 0, SC1); }
__device__ __forceinline__ void st8(u32x2 v, rsrc_t rs, unsigned voff, unsigned soff) { __builtin_amdgcn_raw_buffer_store_b64(v, rs, voff, soff, SC1); }

__device__ __forceinline__ unsigned ld4(rsrc_t rs, unsigned voff, unsigned soff) { return __builtin_amdgcn_raw_buffer_load_b32(rs, voff, soff, SC1); }
__device__ __forceinline__ void st4(unsigned v, rsrc_t rs, unsigned voff, unsigned soff) { __builtin_amdgcn_raw_buffer_store_b32(v, rs, voff, soff, SC1); }

constexpr unsigned kSpinCap = 1u << 16;
__device__ __forceinline__ bool spin_fail(unsigned& spins, rsrc_t rs, u32x2 T) {
  asm volatile("" ::: "memory");
  ++spins;
  if (spins > kSpinCap) { st8(T, rs, 0, F_TMO); return true; }
  if ((spins & 255u) == 0u) {
    const u32x2 v = ld8(rs, 0, F_TMO);
    if (v[0] == T[0] && v[1] == T[1]) return true;
  }
  __builtin_amdgcn_s_sleep(3);
  return false;
}
__device__ __forceinline__ void poll_flags2(rsrc_t rs, unsigned voff, u32x2 T) {
  for (unsigned spins = 0;;) {
    const u32x4 v = ld16(rs, voff, 0);
    if (__all(v[0] == T[0] && v[1] == T[1] && v[2] == T[0] && v[3] == T[1])) break;
    if (spin_fail(spins, rs, T)) break;
  }
  asm volatile("" ::: "memory");
  __builtin_amdgcn_fence(__ATOMIC_ACQUIRE, "wavefront");
}
__device__ __forceinline__ void poll_flags(rsrc_t rs, unsigned voff, unsigned soff, u32x2 T) {
  for (unsigned spins = 0;;) {
    const u32x2 v = ld8(rs, voff, soff);
    if (__all(v[0] == T[0] && v[1] == T[1])) break;
    if (spin_fail(spins, rs, T)) break;
  }
  asm volatile("" ::: "memory");
  __builtin_amdgcn_fence(__ATOMIC_ACQUIRE, "wavefront");
}

__device__ __forceinline__ void unpack8v(const u32x4 u, f32x2* v) {
#pragma unroll
  for (int j = 0; j < 4; ++j) v[j] = f32x2{lo16(u[j]), hi16(u[j])};
}
__device__ __forceinline__ u32x4 pack8v(const f32x2* v) {
  u32x4 o;
#pragma unroll
  for (int j = 0; j < 4; ++j) o[j] = pack2(v[j][0], v[j][1]);
  return o;
}
__device__ __forceinline__ float ln_rd(float var) { return __builtin_amdgcn_rcpf(rbf1(__builtin_amdgcn_sqrtf(rbf1(rbf1(var) + 1e-5f)))); }
__device__ __forceinline__ float hadd(f32x2 v) {
  float a = v[0];
  asm volatile("" : "+v"(a));
  return a + v[1];
}
__device__ __forceinline__ float add_g(float v) { return add_x32(add_x16(v)); }
__device__ __forceinline__ void add_g3(float& x, float& y, float& z) {
  auto a = __builtin_amdgcn_permlane16_swap(__builtin_bit_cast(unsigned, x), __builtin_bit_cast(unsigned, y), false, false);
  auto b = __builtin_amdgcn_permlane16_swap(__builtin_bit_cast(unsigned, z), __builtin_bit_cast(unsigned, z), false, false);
  const float r = asf(a[0]) + asf(a[1]), t = asf(b[0]) + asf(b[1]);
  auto c = __builtin_amdgcn_permlane32_swap(__builtin_bit_cast(unsigned, r), __builtin_bit_cast(unsigned, r), false, false);
  auto d = __builtin_amdgcn_permlane32_swap(__builtin_bit_cast(unsigned, t), __builtin_bit_cast(unsigned, t), false, false);
  const float u = asf(c[0]) + asf(c[1]);
  z = asf(d[0]) + asf(d[1]);
  auto e = __builtin_amdgcn_permlane16_swap(__builtin_bit_cast(unsigned, u), __builtin_bit_cast(unsigned, u), false, false);
  x = asf(e[0]); y = asf(e[1]);
}
__device__ __forceinline__ u32x4 trade16(u32x4 v) {
  auto a = __builtin_amdgcn_permlane16_swap(v[0], v[2], false, false);
  auto b = __builtin_amdgcn_permlane16_swap(v[1], v[3], false, false);
  return u32x4{(unsigned)a[0], (unsigned)b[0], (unsigned)a[1], (unsigned)b[1]};
}
__device__ __forceinline__ unsigned umax4(u32x4 v) { return max(max(v[0], v[1]), max(v[2], v[3])); }
__device__ __forceinline__ u32x2 unpoison2(u32x2 v) { return u32x2{min(v[0], POISON - 1), min(v[1], POISON - 1)}; }
__device__ __forceinline__ u32x4 unpoison4(u32x4 v) {
  return u32x4{min(v[0], POISON - 1), min(v[1], POISON - 1), min(v[2], POISON - 1), min(v[3], POISON - 1)};
}

typedef union { struct { s16x4 a, b; } hf; u32x4 v; } frag_t;
__device__ __forceinline__ s16x4 trread(const char* smem, unsigned off) {
  return __builtin_amdgcn_ds_read_tr16_b64_v4i16((LDS_AS s16x4*)(smem + off));
}
__device__ __forceinline__ s16x4 trabs(unsigned addr) { return __builtin_amdgcn_ds_read_tr16_b64_v4i16((LDS_AS s16x4*)(size_t)addr); }

constexpr int WA = 6;
constexpr int WB = 4, WR = 3;
#define CH 4, 7, 8, 13, 15, 20, 22, 27, 29
constexpr int CHV[9] = {CH};
constexpr int FSL = CHV[0], FS0 = CHV[1], FS0D = CHV[2], FS1 = CHV[3], FS1S = CHV[4];
constexpr int FXA = CHV[5], FXB = CHV[6], FXL = CHV[7], FXS = CHV[8];
constexpr int FXT = FXB >> 2;
constexpr int CSD = 3, POI = 4, RWS = 5, EIN = 8;
constexpr int ERQ = 16, CSL = 19, CSS = 22;
constexpr int IMG = 24, KP0 = 26;
constexpr int PB0 = 10, PB1 = 24;
constexpr int ABL = 23, ABS = 27;
constexpr int XU = 3, XL = 2;
constexpr int x_slot(int u) { return u % XU == 0 ? 0 : u % XU == 1 ? 32768 : 49152; }
constexpr int TS0 = 19, TSN = 7, TW = 4, TST = 27;
constexpr int QL = 26;
constexpr int RXCD = 5;
constexpr int WDS = 17 - WB;
constexpr int SDI0 = 16, SDIR = 24, GSL = 14, RSL = 38, KSL = 3, XSL = 4, OSL = 12;
constexpr bool tile_piece(int s) { return s >= 3 && ((s - 3) >> 2) + 3 < 8 && ((s - 3) & 3) < 3; }
constexpr int a_req(int s, int n) {
  if (!(n & 1)) return -1;
  if (s <= 1) return 8 + 3 * s + (n >> 1);
  return n < 5 && s + 5 < 32 ? 2 * (s + 5) + (n >> 1) : -1;
}
constexpr int vm_n(int it, int s, int n) {
  int c = 0;
  if (n == 0) c += tile_piece(s);
  if (n == 4) c += (s == ABL) + (it ? (s == FSL ? 5 : 0) : (s == ERQ) + (s >= IMG && s < IMG + 3));
  if (!it) c += (s < 4 && n == 2) + (s == 0 && (n == 0 || n == 4)) + ((n == 3 || n == 5) && s + XL + XU < 32);
  else c += (a_req(s, n) >= 0) + ((s == 0 || s == 4) && n == 2);
  return c;
}
constexpr int vm_at(int it, int s, int n) {
  int c = it ? 8 : 14;
  for (int u = 0; u < 6 * s + n; ++u) c += vm_n(it, u / 6, u % 6);
  return c;
}
constexpr int vm_a(int it, int k) {
  if (k < 4) return 2 * k + 2;
  for (int u = 0; u < 192; ++u)
    if (a_req(u / 6, u % 6) == 2 * k + 1) return vm_at(it, (u + 1) / 6, (u + 1) % 6);
  return 0;
}
constexpr int vm_tile(int it, int t) { return t >= 3 ? vm_at(it, 4 * t - 7, 1) : t == 1 ? vm_at(0, 0, 5) : vm_at(0, 3, 3); }
constexpr int vm_x(int u) { return u == 2 ? 9 : u < XU + XL ? 12 + 2 * (u - XU) : vm_at(0, u - XU - XL + 1, 0); }

constexpr int wo_k(int it, int s) { return it || s < 4 ? 15 - s : s < 8 ? 7 - s : 19 - s; }
constexpr int wo_pos(int it, int s) { return 2 * s + 2 + (s >= WR ? 2 * it : 0); }
constexpr int wo_at(int it, int s) { return 2 * it + (s < WB - WR ? 2 * WR + 2 + 4 * s : s < 16 - WB ? 2 * (2 * WB - WR) + 2 * (s - WB + WR) : 32); }
template <int OFF> __device__ __forceinline__ void lds_count(unsigned ad) {
  asm volatile("s_mov_b64 exec, 1\n\tds_add_u32 %0, %1 offset:%2\n\ts_mov_b64 exec, -1" ::"v"(ad), "v"(1u), "n"(OFF) : "memory");
}
__device__ __forceinline__ void lds_look(char* c, unsigned want) {
  while ((unsigned)__builtin_amdgcn_readfirstlane(__hip_atomic_load((LDS_AS unsigned*)c, __ATOMIC_RELAXED, __HIP_MEMORY_SCOPE_WORKGROUP)) < want) {}
  asm volatile("" ::: "memory");
}
__device__ __forceinline__ void pin1(unsigned& a) { asm volatile("" : "+v"(a)); }
__device__ __forceinline__ void pin2(u32x4& a, u32x4& b) { asm volatile("" : "+v"(a), "+v"(b)::"memory"); }
__device__ __forceinline__ void pin2s(float& a, float& b) { asm volatile("" : "+v"(a), "+v"(b)); }
template <int OFF> __device__ __forceinline__ void lds2(u32x4& a, u32x4& b, unsigned ad) {
  asm volatile("ds_read_b128 %0, %2 offset:%3\n\tds_read_b128 %1, %2 offset:%4" : "=&v"(a), "=&v"(b) : "v"(ad), "n"(OFF), "n"(OFF + 1024) : "memory");
}
template <int N> __device__ __forceinline__ void lds_wait(u32x4& a, u32x4& b) { asm volatile("s_waitcnt lgkmcnt(%2)" : "+v"(a), "+v"(b) : "n"(N) : "memory"); }
__device__ __forceinline__ void pin2f(f32x2& a, f32x2& b) { asm volatile("" : "+v"(a), "+v"(b)); }
__device__ __forceinline__ void pin_ac(f32x4 (&ac)[2][4]) {
  asm volatile("" : "+{v[176:179]}"(ac[0][0]), "+{v[180:183]}"(ac[0][1]), "+{v[184:187]}"(ac[0][2]), "+{v[188:191]}"(ac[0][3]),
                    "+{v[192:195]}"(ac[1][0]), "+{v[196:199]}"(ac[1][1]), "+{v[200:203]}"(ac[1][2]), "+{v[204:207]}"(ac[1][3]));
}

constexpr int L_XC = LDS_BYTES - 64, L_SKEY = 32704, L_SRED = 4096, L_SFIN = 20480;
__device__ __forceinline__ unsigned key_load(rsrc_t rs, int l, int L, int rep) { return ld4(rs, (l & 15) * 4, OFF_KEY + (L * 16 + rep) * 128); }
__device__ __forceinline__ unsigned umax16(unsigned k) {
  k = max(k, dppu<0xB1>(k));
  k = max(k, dppu<0x4E>(k));
  k = max(k, dppu<0x141>(k));
  return max(k, dppu<0x140>(k));
}
__device__ __forceinline__ unsigned key_row(unsigned key) { return (0xffffu - (key & 0xffffu)) & 2047u; }
__device__ __forceinline__ unsigned key_wait(rsrc_t rs, u32x2 T, int l, int L, int rep, unsigned kv) {
  if (!__all(kv != POISON)) {
    unsigned spins = 0;
    do {
      if (spin_fail(spins, rs, T)) break;
      if (L == 0) __builtin_amdgcn_s_sleep(KSL);
      kv = key_load(rs, l, L, rep);
    } while (!__all(kv != POISON));
  }
  return __builtin_amdgcn_readfirstlane(umax16(kv));
}
__device__ __forceinline__ unsigned ffn_xoff(unsigned key, int L, int w) { return OFF_CA + L * 32768 + (key_row(key) >> 7) * 2048 + w * 256; }
__device__ __forceinline__ void row_load(rsrc_t rs, int g, unsigned xo, u32x4 (&xv)[4]) {
  ld16h<0>(xv[0], rs, g * 16, xo); ld16h<64>(xv[1], rs, g * 16, xo); ld16h<128>(xv[2], rs, g * 16, xo); ld16h<192>(xv[3], rs, g * 16, xo);
}
template <int SL = 0> __device__ __forceinline__ void ffn_xwait(rsrc_t rs, u32x2 T, unsigned xo, int l, u32x4 (&xv)[4]) {
#define XV_OK() __all(max(max(umax4(xv[0]), umax4(xv[1])), max(umax4(xv[2]), umax4(xv[3]))) != POISON)
  if (!XV_OK()) {
    unsigned spins = 0;
    do {
      if (spin_fail(spins, rs, T)) break;
      if (SL) __builtin_amdgcn_s_sleep(SL);
#pragma unroll
      for (int i = 0; i < 4; ++i) xv[i] = ld16(rs, (l >> 4) * 16 + i * 64, xo);
    } while (!XV_OK());
  }
#undef XV_OK
}
__device__ __forceinline__ void ffn_gu(const u32x4 (&gv)[4], const u32x4 (&uv)[4], const u32x4 (&xv)[4], f32x4& ga, f32x4& ua) {
  ga = mfma16(gv[0], xv[0], f32x4{0.f, 0.f, 0.f, 0.f}); ua = mfma16(uv[0], xv[0], f32x4{0.f, 0.f, 0.f, 0.f});
#pragma unroll
  for (int i = 1; i < 4; ++i) { ga = mfma16(gv[i], xv[i], ga); ua = mfma16(uv[i], xv[i], ua); }
}
__device__ __forceinline__ void ffn_gu_check(rsrc_t rs, u32x2 T, unsigned xo, int l, const u32x4 (&gv)[4], const u32x4 (&uv)[4], u32x4 (&xv)[4], f32x4& ga, f32x4& ua) {
  if (__builtin_expect(__any(ga[0] != ga[0]), 0)) {
    ffn_xwait(rs, T, xo, l, xv);
    ffn_gu(gv, uv, xv, ga, ua);
  }
}
__device__ __forceinline__ void ffn_gu_store(char* smem, int l, int w, f32x4 ga, f32x4 ua) {
  LDS_AS f32x4* sred = (LDS_AS f32x4*)(smem + L_SRED);
  if ((l & 15) == 0) { sred[w * 8 + (l >> 4)] = ga; sred[w * 8 + 4 + (l >> 4)] = ua; }
}
__device__ __forceinline__ f32x4 ffn_t_read(char* smem, int l) { return ((const LDS_AS f32x4*)(smem + L_SRED))[(l & 7) * 8 + ((l >> 3) & 1) * 4 + (l >> 4)]; }
__device__ __forceinline__ void ffn_d_read(char* smem, int l, int blk, s16x4 (&dfr)[8]) {
#pragma unroll
  for (int i = 0; i < 8; ++i) dfr[i] = trread(smem, L_DN + blk * 4096 + (l >> 4) * 1024 + ((l & 15) >> 2) * 256 + i * 32 + (l & 3) * 8);
}
__device__ __forceinline__ float ffn_d_sum(float t) { t += dppf<0xB1>(t); t += dppf<0x4E>(t); return t + dppf<0x141>(t); }
__device__ __forceinline__ u32x2 ffn_h(f32x4 t, int l) {
#pragma unroll
  for (int e = 0; e < 4; ++e) t[e] = ffn_d_sum(t[e]);
  const float a = (l & 1) ? t[1] : t[0], b = (l & 1) ? t[3] : t[2], s = (l & 2) ? b : a;
  const float gg = rbf1(s), uu = rbf1(dppf<0x128>(s));
  const float h = rbf1(gg * __builtin_amdgcn_rcpf(1.0f + __expf(-gg))) * uu;
  const unsigned h0 = pack2(h, dppf<0x55>(h)), h1 = pack2(dppf<0xAA>(h), dppf<0xFF>(h));
  return (l & 15) == 0 ? u32x2{h0, h1} : u32x2{0u, 0u};
}
template <int N> __device__ __forceinline__ f32x4 ffn_d_mma(const s16x4* da, const s16x4* db, u32x2 ha) {
  u32x2 hb = {dppu<0x110 + N>(ha[0]), dppu<0x110 + N>(ha[1])};
  f32x4 ya = {0.f, 0.f, 0.f, 0.f}, yb = ya;
#pragma unroll
  for (int i = 0; i < N; ++i) {
    ya = mfma16k16(da[i], __builtin_bit_cast(s16x4, ha), ya); yb = mfma16k16(db[i], __builtin_bit_cast(s16x4, hb), yb);
    ha = u32x2{dppu<0x111>(ha[0]), dppu<0x111>(ha[1])}; hb = u32x2{dppu<0x111>(hb[0]), dppu<0x111>(hb[1])};
  }
  return ya + yb;
}
__device__ __forceinline__ void ffn_d_store(rsrc_t rs, int l, int G0, int L, int slice, f32x4 y) {
  const unsigned c = l & 15;
  st16(unpoison4(__builtin_bit_cast(u32x4, y)), rs, (G0 + (c >> 1)) * 32768 + slice * 128 + (c & 1) * 64 + (l >> 4) * 16, OFF_PART + L * (1u << 20));
}
__device__ __forceinline__ void red_load(rsrc_t rs, int tid, int L, unsigned G, unsigned key, u32x4 (&pv)[4], u32x2& cxn, u32x2& cx1) {
#pragma unroll
  for (int k = 0; k < 4; ++k) pv[k] = ld16(rs, min(tid + 512 * k, 1791) * 16, OFF_PART + L * (1u << 20) + G * 32768);
  cxn = ld8(rs, (tid & 3) * 16 + (tid & 4) * 2, OFF_CA + L * 32768 + (key_row(key) >> 7) * 2048 + G * 64);
  cx1 = ld8(rs, (tid & 3) * 16 + (tid & 4) * 2, OFF_CB + L * 32768 + (key_row(key) >> 7) * 2048 + G * 64);
}
__device__ __forceinline__ void red_sum(rsrc_t rs, char* smem, u32x2 T, int tid, int w, int L, unsigned G, unsigned key,
                                        u32x4 (&pv)[4], u32x2& cxn, u32x2& cx1) {
#define PV_OK() __all(max(max(max(umax4(pv[0]), umax4(pv[1])), max(umax4(pv[2]), umax4(pv[3]))), max(max(cxn[0], cxn[1]), max(cx1[0], cx1[1]))) != POISON)
  if (!PV_OK()) {
    unsigned spins = 0;
    do {
      if (spin_fail(spins, rs, T)) break;
      red_load(rs, tid, L, G, key, pv, cxn, cx1);
    } while (!PV_OK());
  }
#undef PV_OK
  f32x4 s = (__builtin_bit_cast(f32x4, pv[0]) + __builtin_bit_cast(f32x4, pv[1])) + __builtin_bit_cast(f32x4, pv[2]);
  if (w < 4) s += __builtin_bit_cast(f32x4, pv[3]);
  ((LDS_AS f32x4*)(smem + L_SFIN))[tid] = s;
}
__device__ __forceinline__ float key_sig(unsigned key) {
  const unsigned k16 = key >> 16;
  const float rmax = __builtin_bit_cast(float, ((k16 & 0x8000u) ? (k16 & 0x7fffu) : (~k16 & 0xffffu)) << 16);
  float sig = rbf1(__builtin_amdgcn_rcpf(1.0f + __expf(-rmax)));
  asm volatile("" : "+v"(sig));
  return sig;
}
template <int NW> __device__ __forceinline__ u32x2 red_final(char* smem, int l, float sig, u32x2 cxn, u32x2 cx1) {
  const LDS_AS f32x4* sf = (const LDS_AS f32x4*)(smem + L_SFIN) + l;
  f32x4 s = (sf[0] + sf[64]) + (sf[128] + sf[192]);
  if constexpr (NW == 8) s += (sf[256] + sf[320]) + (sf[384] + sf[448]);
#pragma unroll
  for (int j = 0; j < 4; ++j) s[j] = sum_hi8(s[j]);
  const float xn4[4] = {lo16(cxn[0]), hi16(cxn[0]), lo16(cxn[1]), hi16(cxn[1])};
  const float x14[4] = {lo16(cx1[0]), hi16(cx1[0]), lo16(cx1[1]), hi16(cx1[1])};
  float o[4];
#pragma unroll
  for (int j = 0; j < 4; ++j) o[j] = x14[j] + rbf1(xn4[j] + rbf1(rbf1(s[j]) * sig));
  return u32x2{pack2(o[0], o[1]), pack2(o[2], o[3])};
}

struct RowFin { float mean, var, mb, rd; unsigned key; };
__device__ __forceinline__ void fin_wait(rsrc_t rs, u32x2 T, unsigned voff, unsigned so, u32x4 (&sp)[4]) {
#define ST_OK() __all(max(max(umax4(sp[0]), umax4(sp[1])), max(umax4(sp[2]), umax4(sp[3]))) != POISON)
  if (!ST_OK()) {
    unsigned spins = 0;
    do {
      if (spin_fail(spins, rs, T)) break;
#pragma unroll
      for (int i = 0; i < 4; ++i) sp[i] = ld16(rs, voff + i * 2048, so);
    } while (!ST_OK());
  }
#undef ST_OK
}
__device__ __forceinline__ RowFin fin_key(rsrc_t rs, u32x2 T, unsigned voff, unsigned so, char* skey, int l, int w, unsigned R, float wsum, u32x4 (&sp)[4]) {
  RowFin r;
#define ST_SUM() ((__builtin_bit_cast(f32x4, sp[0]) + __builtin_bit_cast(f32x4, sp[1])) + (__builtin_bit_cast(f32x4, sp[2]) + __builtin_bit_cast(f32x4, sp[3])))
  f32x4 sf = ST_SUM();
  const float z = (sf[0] + sf[1]) + sf[2];
  if (__any(z != z)) {
    fin_wait(rs, T, voff, so, sp);
    sf = ST_SUM();
  }
#undef ST_SUM
  float s0 = sf[0], s1 = sf[1], s2 = sf[2];
  add_g3(s0, s1, s2);
  r.mean = s0 * (1.0f / 1024.0f);
  r.var = fmaxf(__builtin_fmaf(-r.mean, r.mean, s1 * (1.0f / 1024.0f)), 0.f);
  r.mb = rbf1(r.mean); r.rd = ln_rd(r.var);
  const unsigned rb16 = pack2((s2 - r.mb * wsum) * r.rd, 0.f) & 0xffffu;
  r.key = min((((rb16 & 0x8000u) ? (~rb16 & 0xffffu) : (rb16 | 0x8000u)) << 16) | (0xffffu - R), POISON - 1);
  const unsigned wk = umax16(r.key);
  if (l == 0) ((LDS_AS unsigned*)skey)[w] = wk;
  return r;
}
__device__ __forceinline__ void fin_pack(const RowFin& r, const f32x2 (&v)[8], u32x4 (&c)[4]) {
  f32x2 t[8], rd2 = {r.rd, r.rd}, of2 = {-r.mb * r.rd, -r.mb * r.rd};
  asm volatile("" : "+v"(rd2), "+v"(of2));
#pragma unroll
  for (int j = 0; j < 8; ++j) t[j] = __builtin_elementwise_fma(v[j], rd2, of2);
  c[0] = unpoison4(pack8v(t)); c[1] = unpoison4(pack8v(t + 4)); c[2] = unpoison4(pack8v(v)); c[3] = unpoison4(pack8v(v + 4));
  asm volatile("" : "+v"(c[0]), "+v"(c[1]), "+v"(c[2]), "+v"(c[3]));
}
__device__ __forceinline__ unsigned fin_cand(rsrc_t rs, char* skey, int L, int g, int rb, int ct, const RowFin& r, const u32x4 (&c)[4]) {
  const u32x4 k0 = *(const LDS_AS u32x4*)skey, k1 = *(const LDS_AS u32x4*)(skey + 16);
  const unsigned best = max(umax4(k0), umax4(k1));
  if (r.key == best) {
    const unsigned co = L * 32768 + rb * 2048 + ct * 128 + g * 16;
    st16(c[0], rs, co, OFF_CA); st16(c[1], rs, co + 64, OFF_CA);
    if (g == 0) st4(best, rs, OFF_KEY + (L * 16 + ct) * 128 + rb * 4, 0);
    st16(c[2], rs, co, OFF_CB); st16(c[3], rs, co + 64, OFF_CB);
  }
  return best;
}
__device__ __forceinline__ void fin_x2(const RowFin& r, f32x2 (&v)[8]) {
  f32x2 sc2 = {1.0f + r.rd, 1.0f + r.rd}, of2 = {-r.mb * r.rd, -r.mb * r.rd};
  asm volatile("" : "+v"(sc2), "+v"(of2));
#pragma unroll
  for (int j = 0; j < 8; ++j) v[j] = __builtin_elementwise_fma(v[j], sc2, of2);
}

extern "C" __device__ unsigned long long dispatch_id() __asm("llvm.amdgcn.dispatch.id");

extern "C" __global__ __launch_bounds__(512, 2) void mega_kernel(const bf16* x, char* ws, const bf16* wqkv, const bf16* wo, const bf16* wr,
                                                                 const bf16* wg, const bf16* wu, const bf16* wd, bf16* out) {
  extern __shared__ __attribute__((aligned(16))) char smem[];
  const int w0_ = __builtin_amdgcn_readfirstlane(threadIdx.x >> 6);
  typedef unsigned long long u64x2 __attribute__((ext_vector_type(2)));
  u64x2 a01;
  unsigned long long a2;
  asm volatile("s_load_dwordx4 %0, %2, 0\n\ts_load_dwordx2 %1, %2, 16" : "=&s"(a01), "=&s"(a2) : "s"(__builtin_amdgcn_kernarg_segment_ptr()));
  const unsigned lbase = (unsigned)(size_t)(LDS_AS char*)smem;
  {
    unsigned long long pc;
    unsigned len;
    asm volatile("s_getpc_b64 %0\n\ts_mov_b32 %1, .Ltext_end-mega_kernel\n\t.subsection 1\n.Ltext_end:\n\t.subsection 0" : "=s"(pc), "=s"(len));
    const unsigned o = ((blockIdx.x >> 3) * 64 + (threadIdx.x & 63)) * 128;
    if (w0_ == 7 && o < len)
      asm volatile("s_mov_b32 m0, %0\n\ts_nop 0\n\tglobal_load_lds_dword %1, %2" ::"s"(lbase + L_TOUCH0), "v"(o), "s"(pc & ~127ull) : "memory");
  }
#define HGRP() ((xcd & 2) + (xcd >> 2))
#define IDS()                                                                                         \
  int tid = threadIdx.x, w = w0_, bid = blockIdx.x;                                                   \
  asm volatile("" : "+v"(tid));                                                                       \
  asm volatile("" : "+s"(w));                                                                         \
  asm volatile("" : "+s"(bid));                                                                       \
  const int l = tid & 63, xcd = bid & 7, bi = bid >> 3;                                               \
  const int p = (xcd & 1) * 4 + (bi & 3), h = HGRP() * 8 + (bi >> 2);         \
  const int rb = 2 * xcd + (bi & 1), ct = bi >> 1;                               \
  const int slice = (xcd - (xcd > RXCD)) * 32 + bi;                                            \
  const bool ffn_on = xcd != RXCD;                                                                    \
  const int g = l >> 4;                                                                               \
  const int rt = w < 4 ? w : 11 - w;          \
  KARGS();                                                                                            \
  (void)p; (void)h; (void)rb; (void)ct; (void)slice; (void)ffn_on; (void)g; (void)rt;
#define PROT() (2 * (p & 3) + (p >> 2))
  typedef const void* const __attribute__((address_space(4)))* karg_t;
#define KARGS() karg_t ka = (karg_t)__builtin_amdgcn_kernarg_segment_ptr(); asm volatile("" : "+s"(ka))
#define ARGP(name, i) const GAS bf16* name = (const GAS bf16*)ka[i]; asm volatile("" : "+s"(name))
#define RSRC(ptr, bytes) __builtin_amdgcn_make_buffer_rsrc((void*)(ptr), 0, bytes, 0x00020000)
#define rwq RSRC(pwq, 1024 * 3072 * 2)
#define rwo RSRC(pwo, 1024 * 1024 * 2)
#define rwg RSRC(pwg, 1024 * F_ * 2)
#define rwu RSRC(pwu, 1024 * F_ * 2)
#define rwd RSRC(pwd, 1024 * F_ * 2)
#define rout RSRC(pout, 2048 * 1024 * 2)

#define WQ_VOFF() ((l >> 2) * 6144 + (((l & 3) ^ ((((l >> 4) ^ (l >> 5)) & 1) << 1)) << 4))
#define WQ_KMAJ(q) (((q) % 3) * 8 + (q) / 3)
#define WQ_PIECE(t, j, dst) dma16(rwq, wqv, ((((t) + rot) & 7) * 128 + ((j) & 7) * 16) * 6144 + ((j) >> 3) * 2048 + h * 64, lbase + (dst) + (j) * 1024)
#define WQ_BOFF(dt) ((g >> 1) * 1024 + (8 * (g & 1) + ((l & 15) >> 2)) * 64 + (l & 3) * 8 + (((dt) ^ (g & 1)) << 5))
#define X_REQ(u, hf) dma16(rx, xv[hf], (p * 256 + rt * 32 + ((u) & 1) * 16 + (hf) * 8) * 2048 + ((((u) >> 2) + rot) & 7) * 256 + (((u) >> 1) & 1) * 128, lbase + rt * 2048 + x_slot(u) + (hf) * 1024)
#define WO_PIECE(j) dma16(rwo, wov, ((w >> 2) * 64 + (w & 3) + 4 * (j)) * 16384 + ct * 128, lbase + 32768 + (j) * 8192 + w * 1024)
#define WD_DMA_OF(v) _Pragma("unroll") for (int i = 0; i < 4; ++i)                                  \
      dma16(rwd, (l >> 4) * 2048 + (l & 15) * 16, (unsigned)(slice * 16 + 4 * i) * 2048 + (v) * 256, lbase + L_DN + (v) * 4096 + i * 1024)
#define FFN_DMA(m) dma16((m) & 1 ? rwu : rwg, fnv, (unsigned)(((4 * w + ((m) >> 1)) * 32) * F_ + slice * 16) * 2, lbase + 65536 + (m) * 8192 + w * 1024)
#define POISON_BIG(L)                                                                                \
  if (w >= 2) {                                                                                      \
    unsigned pz1 = POISON;                                                                           \
    asm volatile("" : "+v"(pz1));                                                                    \
    st16(u32x4{pz1, pz1, pz1, pz1}, rs, (w >= 4 ? OFF_PART + (L) * (1u << 20) + bid * 4096 - 4096 : OFF_STAT + (L) * 524288 + (p * 32 + h) * 2048 - 2048) + tid * 16, 0); \
  }
  u32x4 gv[4], uv[4];
  u32x4 keep[9];
  u32x4 pre[4][2];
#define PRE_LOAD(it_, rot_, s0, s1)                                                                  \
  static_for<(s1) - (s0)>([&](auto c_) __attribute__((always_inline)) {                              \
    constexpr int s = (s0) + decltype(c_)::value;                                                    \
    const unsigned ab_ = OFF_X1 + (((rot_) + (s >> 2)) & 7) * 8192 + ((s >> 1) & 1) * 4096; \
    ld16w<2 * s, (s & 1) * 2048>(pre[s][0], rs, l * 16, ab_ + (p * 8 + rt) * 65536);                         \
    ld16w<2 * s + 1, (s & 1) * 2048 + 1024>(pre[s][1], rs, l * 16, ab_ + (p * 8 + rt) * 65536);                 \
  })
  u32x2 T;
  const GAS bf16* tsrc;
  unsigned xv[2], xa[2];
  u32x2 ep;
  u32x4 rows0[4];
#define X_READ(u, d0, d1) { d0 = *(const LDS_AS u32x4*)(size_t)(xa[0] + x_slot(u)); d1 = *(const LDS_AS u32x4*)(size_t)(xa[1] + x_slot(u)); }
  unsigned g1[5], wqv0;
  {
    IDS();
    const int rot = PROT(), qa = WQ_KMAJ(w), qb = WQ_KMAJ(8 + w);
    wqv0 = WQ_VOFF();
    xv[0] = (l >> 3) * 2048 + (((l & 7) ^ (l >> 4)) << 4); xv[1] = xv[0] ^ 64;
    g1[0] = (rot * 128 + (qa & 7) * 16) * 6144 + (qa >> 3) * 2048 + h * 64; g1[1] = lbase + L_RING + qa * 1024;
    g1[2] = (rot * 128 + (qb & 7) * 16) * 6144 + (qb >> 3) * 2048 + h * 64; g1[3] = lbase + L_RING + qb * 1024;
    g1[4] = (p * 256 + rt * 32) * 2048 + rot * 256;
    asm volatile("" : "+v"(wqv0), "+v"(xv[0]), "+v"(xv[1]));
    asm volatile("s_waitcnt lgkmcnt(0)" : "+s"(a01), "+s"(a2), "+s"(g1[0]), "+s"(g1[1]), "+s"(g1[2]), "+s"(g1[3]), "+s"(g1[4]));
  }
  x = (const bf16*)a01[0]; ws = (char*)a01[1]; wqkv = (const bf16*)a2;
  const rsrc_t rs = __builtin_amdgcn_make_buffer_rsrc((void*)ws, 0, WS_BYTES, 0x00020000);
  const rsrc_t rx = RSRC(x, 2048 * 1024 * 2);
  {
    IDS();
    const int rot = PROT();
    const bf16* pwq = wqkv;
    const unsigned wqv = wqv0;
    dma16(rwq, wqv, g1[0], g1[1]);
#pragma unroll
    for (int i = 0; i < 4; ++i) dma16(rx, xv[i & 1], g1[4] + i * 16384, lbase + rt * 2048 + x_slot(i >> 1) + (i & 1) * 1024);
    dma16(rwq, wqv, g1[2], g1[3]);
    asm volatile("s_barrier" ::: "memory");
    WQ_PIECE(0, WQ_KMAJ(16 + w), L_RING);
    if (w == 6 && bi == 0) {
      ARGP(pwr, 4);
      asm volatile("s_mov_b32 m0, %0\n\ts_nop 0\n\tbuffer_load_dword %1, %2, 0 offen lds" ::"s"(lbase + L_TOUCH0 + 256), "v"((l & 15) * 128), "s"(RSRC(pwr, 2048)) : "memory");
    }
    X_REQ(2, 0); X_REQ(2, 1);
    asm volatile("buffer_load_dwordx2 %0, off, %1, 0 sc1" : "=v"(ep) : "s"(rs) : "memory");
    asm volatile("" : "=v"(rows0[0]), "=v"(rows0[1]), "=v"(rows0[2]), "=v"(rows0[3]));
    if (w < 4) ldrows(rows0, rx, (l >> 5) * 2048 + (l & 31) * 16, (p * 256 + h * 8 + 2 * w) * 2048);
    {
      ARGP(pwo, 3); ARGP(pwg, 5); ARGP(pwu, 6);
      tsrc = (w < 4 ? pwg : pwu) + ((bi * 32 + ((tid & 255) >> 3)) * F_ + (slice >> 5) * 512 + (tid & 7) * 64);
      if (!ffn_on) tsrc = pwo + (bi * 512 + tid) * 64;
      asm volatile("" : "+v"(tsrc));
    }
#pragma unroll
    for (int kk = 0; kk < 2; ++kk) {
      const int r = l & 15;
      xa[kk] = lbase + rt * 2048 + (r >> 3) * 1024 + (r & 7) * 128 + (((4 * kk + g) ^ (r >> 1)) << 4);
      asm volatile("" : "+v"(xa[kk]));
    }
    if (tid == 192) *(LDS_AS u32x4*)(smem + L_QC) = u32x4{0u, 0u, 0u, 0u};
    if (w < 4) wait_vm<8>(); else wait_vm<4>();
    X_READ(0, pre[0][0], pre[1][0]); X_READ(1, pre[0][1], pre[1][1]);
    asm volatile("s_waitcnt lgkmcnt(0)\n\ts_barrier" : "+v"(pre[0][0]), "+v"(pre[0][1]), "+v"(pre[1][0]), "+v"(pre[1][1])::"memory");
    X_REQ(3, 0); X_REQ(3, 1); X_REQ(4, 0); X_REQ(4, 1);
  }
  unsigned sel0 = 0x7FFFFFFFu;
  u32x4 x2p[2];
  unsigned xm0 = 0, kg0 = 0;

  auto layer = [&](auto it_c) __attribute__((always_inline)) {
    constexpr int it = decltype(it_c)::value;
    f32x4 acc[2][6];
    f32x2 qs2[2];
    u32x4 k0, k1, v0, v1;
    unsigned tdum = 0;
    {
      IDS();
      bool aff = false;
      unsigned aq[2];
      u32x4 av[2];
#define PIN_ACC() asm volatile("" : "+{v[176:179]}"(acc[0][0]), "+{v[180:183]}"(acc[0][1]), "+{v[184:187]}"(acc[0][2]), "+{v[188:191]}"(acc[0][3]), "+{v[192:195]}"(acc[0][4]), "+{v[196:199]}"(acc[0][5]), \
                               "+{v[200:203]}"(acc[1][0]), "+{v[204:207]}"(acc[1][1]), "+{v[208:211]}"(acc[1][2]), "+{v[212:215]}"(acc[1][3]), "+{v[216:219]}"(acc[1][4]), "+{v[220:223]}"(acc[1][5]))
#define PINV4(x) asm volatile("" : "+v"(x[0]), "+v"(x[1]), "+v"(x[2]), "+v"(x[3])::"memory")
      auto qkv_loop = [&](auto it_c) __attribute__((always_inline)) {
        constexpr int IT = decltype(it_c)::value;
        const unsigned abase = OFF_X1 + (p * 8 + rt) * 65536;
        const int rot = PROT() + 5 * IT;
        unsigned fkey = 0, kv, bq[2];
        u32x4 fx[4], bv[2], af[WA][2];
        u32x2 cxn, cx1;
        f32x4 sa, sb, cs4[2];
        f32x2 b2[2];
#define AB_OFF() (OFF_AB + IT * 16384 + (p * 8 + rt) * 128)
#define A_LOAD1(k, a) ld16w<(k) % WA * 2 + (a), ((k) & 1) * 2048 + (a) * 1024>(af[(k) % WA][a], rs, l * 16, abase + ((((k) >> 2) + rot) & 7) * 8192 + (((k) >> 1) & 1) * 4096)
#pragma unroll
        for (int s = 0; s < 2 + 2 * IT; ++s) { af[s][0] = pre[s][0]; af[s][1] = pre[s][1]; }
        const bf16* pwq = wqkv;
        const int tstep = TS0 + bid % TSN;
        const unsigned wqv = WQ_VOFF();
#define QKV_DMA1(t, i)                                                                            \
  {                                                                                               \
    if constexpr (IT == 0) WQ_PIECE(t, 3 * w + (i), L_RING + SLOT(t) * STAGE);                    \
    else dma16s<(i) * 1024>(rs, l * 16, OFF_WI + (h * 8 + (((t) + rot) & 7)) * STAGE + w * 3072, lbase + L_RING + SLOT(t) * STAGE + w * 3072); \
  }
        unsigned bo[2][2];
#pragma unroll
        for (int dt = 0; dt < 2 - IT; ++dt) {
          bo[dt][0] = lbase + L_RING + (IT ? l * 16 : WQ_BOFF(dt)); bo[dt][1] = bo[dt][0] + 2 * STAGE;
          asm volatile("" : "+v"(bo[dt][0]), "+v"(bo[dt][1]));
        }
#define FRAG_T() (lbase + L_RING + (4 * g + ((l & 15) >> 2)) * 64 + (l & 3) * 8 + (((g ^ (g >> 1)) & 1) << 5))
        auto frag_t2 = [&](unsigned o) __attribute__((always_inline)) {
          frag_t t;
          t.hf.a = trabs(o); t.hf.b = trabs(o + 1024);
          return t.v;
        };
        unsigned ka[3];
        auto tile_sum = [&](u32x4 pt, int dst) __attribute__((always_inline)) {
          f32x4 c = __builtin_bit_cast(f32x4, pt);
#pragma unroll
          for (int e = 0; e < 4; ++e) { c[e] += dppf<0x124>(c[e]); c[e] += dppf<0x128>(c[e]); c[e] = add_x16(c[e]); }
          if ((l & 28) == 0) *(LDS_AS f32x4*)(smem + dst + (w * 2 + (l >> 5)) * 64 + (l & 3) * 16) = c;
        };
        frag_t bfr[6];
#define QKV_BO(s, n) (bo[(n) & 1][SLOT((s) >> 2) >> 1] + (SLOT((s) >> 2) & 1) * STAGE + ((n) >> 1) * 8192 + ((s) & 3) * 2048)
#define QKV_RDA(s, n)                                                                             \
  {                                                                                               \
    if constexpr (IT == 0) bfr[n].hf.a = trabs(QKV_BO(s, n));                                     \
    else bfr[n].v = *(const LDS_AS u32x4*)(size_t)(bo[0][SLOT((s) >> 2) >> 1] + (SLOT((s) >> 2) & 1) * STAGE + (((s) & 3) * 6 + (n)) * 1024); \
  }
#define QKV_RDB(s, n) { if constexpr (IT == 0) bfr[n].hf.b = trabs(QKV_BO(s, (n) ^ 1) + 256); }
#pragma unroll
        for (int n = 0; n < 6; ++n) { QKV_RDA(0, n); if (n < 5) QKV_RDB(0, n); }
#define XF_OFF() (l & 8 ? ((l & 7) * RX + bi % RX) * 256 + h * 8 : (p * RX + (xcd >> 1) % RX) * 256 + (4 * rt + (l & 3)) * 8)
#define XP_OFF() (OFF_XNEW + ((rot + FXT) & 7) * 256)
#define FP_OFF(i) (w == 3 ? OFF_XNEW + (i) * 1024 : OFF_FX + h * 3072 + w * 1024)
#define FX_OK(n) __all(((n) == 4 ? max(max(umax4(fx[0]), umax4(fx[1])), max(umax4(fx[2]), umax4(fx[3]))) : max(umax4(fx[0]), umax4(fx[1]))) != POISON)
#define SINCE(s0) (vm_at(IT, s, n) - vm_at(IT, s0, 5))
#define DEF4(x) asm volatile("" : "=v"(x[0]), "=v"(x[1]), "=v"(x[2]), "=v"(x[3]))
        LDS_AS unsigned* qcnt = (LDS_AS unsigned*)(smem + L_QC);
        unsigned qc;
        unsigned qadr = lbase + L_QC, qone = 1;
        asm volatile("" : "+v"(qadr), "+v"(qone));
#define count_in(i) asm volatile("s_mov_b64 exec, 1\n\tds_add_u32 %0, %1 offset:%2\n\ts_mov_b64 exec, -1" ::"v"(qadr), "v"(qone), "n"(4 * (i)) : "memory")
        auto count_wait = [&](int i, unsigned want) __attribute__((always_inline)) {
          if (__builtin_expect((unsigned)__builtin_amdgcn_readfirstlane(qc) < want, 0)) {
            while ((unsigned)__builtin_amdgcn_readfirstlane(__hip_atomic_load(qcnt + i, __ATOMIC_RELAXED, __HIP_MEMORY_SCOPE_WORKGROUP)) < want) {}
          }
          asm volatile("" ::: "memory");
        };
        unsigned long long xok = xm0;
        auto x1_need = [&](unsigned long long m) __attribute__((always_inline)) {
          if (__builtin_expect((xok & m) != m, 0)) {
            for (unsigned spins = 0;;) {
              const u32x2 f = ld8(rs, F_X1 + ((p * RX + (xcd >> 1) % RX) * 32 + (l & 31)) * 8, 0);
              xok = __builtin_amdgcn_ballot_w64(f[0] == T[0] && f[1] == T[1]);
              if ((xok & m) == m) break;
              if (spin_fail(spins, rs, T)) { xok = ~0ull; break; }
              __builtin_amdgcn_s_sleep(KSL);
            }
            asm volatile("" ::: "memory");
          }
        };
        auto gap = [&](auto sc_, auto nc_) __attribute__((always_inline)) {
          constexpr int s = decltype(sc_)::value, n = decltype(nc_)::value;
          {
            constexpr int ts = (s + 3) / 4, tc = (s + 2) / 4, t0 = 1;
            if constexpr (ts >= 3 && ts < 8 && s == 4 * ts - 3 && n == 2) { wait_vm<vm_at(IT, s, n) - vm_tile(IT, ts)>(); count_in(0); }
            if constexpr (!IT && tc >= t0 && tc < 3 && s == 4 * tc - 2 && n == 1) { wait_vm<vm_at(IT, s, n) - vm_tile(IT, tc)>(); count_in(0); }
            if constexpr (IT && ts >= t0 && ts < 3 && s == 4 * ts - 3 && n == 2) count_in(0);
            if constexpr (tc >= t0 && tc < 8 && s == 4 * tc - 2) {
              if constexpr (n == 3) qc = __hip_atomic_load(qcnt, __ATOMIC_RELAXED, __HIP_MEMORY_SCOPE_WORKGROUP);
              if constexpr (n == 5) count_wait(0, 8 * (tc - t0 + 1));
            }
          }
          if constexpr (n == 0 && tile_piece(s)) QKV_DMA1(((s - 3) >> 2) + 3, (s - 3) & 3);
          if constexpr (IT == 0) {
            if constexpr (s == 0 && !(n & 1)) QKV_DMA1(1, n >> 1);
            if constexpr (s >= 1 && s < 4 && n == 2) QKV_DMA1(2, s - 1);
            constexpr int u = s + XL;
            if constexpr (n == 1 && u < 32) {
              if (s == 0 && w < 4) wait_vm<vm_at(0, s, n) - vm_x(u) + 4>(); else wait_vm<vm_at(0, s, n) - vm_x(u)>();
              X_READ(u, af[(u & ~1) % WA][u & 1], af[(u | 1) % WA][u & 1]);
            }
            if constexpr (n == 3 && u + XU < 32) { asm volatile("s_waitcnt lgkmcnt(%0)" ::"n"(s + 1 < QL ? 4 : 3) : "memory"); X_REQ(u + XU, 0); }
            if constexpr (n == 5 && u + XU < 32) X_REQ(u + XU, 1);
            if constexpr (s == PB0 && n == 0) { POISON_BIG(0); }
            if constexpr (s == 0 && n == 1) count_in(2);
            if constexpr (s == 0 && n == 3) qc = __hip_atomic_load(qcnt + 2, __ATOMIC_RELAXED, __HIP_MEMORY_SCOPE_WORKGROUP);
            if constexpr (s == 0 && n == 5) count_wait(2, 8);
            if constexpr (s == 1 && n == 1) {
              asm volatile("" : "+v"(ep)::"memory");
              const unsigned long long e = (((unsigned long long)(unsigned)__builtin_amdgcn_readfirstlane(ep[1]) << 32) | (unsigned)__builtin_amdgcn_readfirstlane(ep[0])) + 1ull + dispatch_id() * 0x9E3779B97F4A7C16ull;
              T = u32x2{(unsigned)e, (unsigned)(e >> 32)};
            }
            if constexpr (s == RWS && n == 0) {
              if (w < 4) {
                PINV4(rows0);
                f32x2 sv[16];
#pragma unroll
                for (int i = 0; i < 4; ++i) unpack8v(rows0[i], sv + 4 * i);
                f32x2 t0 = sv[0] + sv[1], t1 = sv[2] + sv[3];
#pragma unroll
                for (int j = 4; j < 16; j += 2) { t0 += sv[j]; t1 += sv[j + 1]; }
                const float m = sum32(hadd(t0 + t1)) * (1.0f / 1024.0f);
                f32x2 m2 = {m, m}, q0 = {0.f, 0.f}, q1 = {0.f, 0.f};
                asm volatile("" : "+v"(m2));
#pragma unroll
                for (int j = 0; j < 16; j += 2) {
                  const f32x2 d0 = sv[j] - m2, d1 = sv[j + 1] - m2;
                  q0 = __builtin_elementwise_fma(d0, d0, q0); q1 = __builtin_elementwise_fma(d1, d1, q1);
                }
                const float rd = ln_rd(sum32(hadd(q0 + q1)) * (1.0f / 1024.0f));
                if ((l & 30) == 0) st4(__builtin_bit_cast(unsigned, (l & 1) ? -rbf1(m) : rd), rs, (l >> 5) * 4 + (l & 1) * 8192, OFF_AB + (p * 256 + h * 8 + 2 * w) * 4);
              }
            }
            if constexpr (s == POI && n == 4) {
              if (w == 3) {
                unsigned pz1 = POISON;
                asm volatile("" : "+v"(pz1));
                const unsigned prb = 2 * p + (h >> 4), pct = h & 15, m = l - 32;
                static_assert(OFF_CB == OFF_CA + 131072, "poison addressing");
                if (l < 60) st16(u32x4{pz1, pz1, pz1, pz1}, rs, l < 32 ? OFF_CA + (l & 8) * 4096 + (l & 16) * 8192 + prb * 2048 + pct * 128 + (l & 7) * 16
                                                             : l < 56 ? OFF_FX + ((h * 6 + (m >> 2)) * 8 + p) * 64 + (l & 3) * 16
                                                                      : OFF_AB + 16384 + (p * 32 + h) * 32 + (l & 1) * 16 + (l & 2) * 4096, 0);
                if (l < 4) st4(pz1, rs, l < 2 ? OFF_XNEW + bid * 8 + l * 4 : OFF_KEY + ((l & 1) * 16 + pct) * 128 + prb * 4, 0);
              }
            }
            if constexpr (s == POI && n == 2) {
              if (w == 2 && l < 6) {
                unsigned f0 = F_KV + KVF(0, p * 32 + h), f1 = F_O + OFB(0, p) + OFL(h), f2 = F_X1 + ((rb >> 1) * RX * 32 + (l & 3) * 32 + (rb & 1) * 16 + ct) * 8;
                asm volatile("" : "+v"(f0), "+v"(f1), "+v"(f2));
                const unsigned fo = l == 5 ? f0 : l == 4 ? f1 : f2;
                touchb(tdum, rs, fo);
              }
            }
            static_assert(vm_x(EIN + XL) >= vm_at(0, EIN - 3, 5) && IMG >= 1 && CSD >= 1, "first flag");
            if constexpr (s == EIN && n == 1) { if (w < 4) count_in(1); }
            if constexpr (s == EIN + 2 && n == 0) qc = __hip_atomic_load(qcnt + 1, __ATOMIC_RELAXED, __HIP_MEMORY_SCOPE_WORKGROUP);
            if constexpr (s == EIN + 2 && n == 2) {
              if (w == 0) { count_wait(1, 4); if (l < RX) st8(T, rs, F_XN + (p * RX + l) * 256 + h * 8, 0); }
            }
            if constexpr (s == ERQ && n == 4) asm volatile("buffer_load_dwordx2 %0, %1, %2, 0 offen sc1" : "=v"(cxn) : "v"(XF_OFF() + F_XN), "s"(rs) : "memory");
            if constexpr (s == CSL && n == 2) {
              wait_vm<SINCE(ERQ)>();
              asm volatile("" : "+v"(cxn)::"memory");
              if (!__all(cxn[0] == T[0] && cxn[1] == T[1])) poll_flags(rs, XF_OFF(), F_XN, T);
            }
          } else {
#define X1_OFF() (F_X1 + ((p * RX + (xcd >> 1) % RX) * 32 + (l & 31)) * 8)
            if constexpr ((s == 0 || s == 4) && n == 2) asm volatile("buffer_load_dwordx2 %0, %1, %2, 0 offen sc1" : "=v"(cxn) : "v"(X1_OFF()), "s"(rs) : "memory");
            if constexpr ((s == 3 || s == 7) && n == 0) {
              wait_vm<vm_at(1, s, 1) - vm_at(1, s - 3, 3)>();
              asm volatile("" : "+v"(cxn)::"memory");
              xok |= __builtin_amdgcn_ballot_w64(cxn[0] == T[0] && cxn[1] == T[1]);
            }
            if constexpr (a_req(s, n) >= 0 && (a_req(s, n) & 7) == 0) x1_need(0x30003ull << (((rot + a_req(s, n) / 8) & 7) * 2));
          }
          if constexpr (IT && a_req(s, n) >= 0) A_LOAD1(a_req(s, n) / 2, a_req(s, n) & 1);
          if constexpr (IT == 0 && s == CSD && n == 4) {
            if (w < 3) {
              unsigned a0 = FRAG_T() + SLOT(s >> 2) * STAGE + w * 8192, a1 = a0 ^ 32, one2 = 0x3F803F80u;
              asm volatile("" : "+v"(a0), "+v"(a1), "+v"(one2));
              const u32x4 o4 = {one2, one2, one2, one2};
              f32x4 y0 = {0.f, 0.f, 0.f, 0.f}, y1 = y0;
#pragma unroll
              for (int i = 0; i < 4; ++i) { y0 = mfma16(frag_t2(a0 + i * 2048), o4, y0); y1 = mfma16(frag_t2(a1 + i * 2048), o4, y1); }
              if ((l & 14) == 0) st16(__builtin_bit_cast(u32x4, (l & 1) ? y1 : y0), rs, g * 16 + (l & 1) * 512, OFF_CS + ((h * 6 + 2 * w) * 8 + p) * 64);
            }
          }
          if constexpr (!IT && s == CSL && n == 4) { asm volatile("" : "=v"(fx[0])); if (w < 3) ld16h<0>(fx[0], rs, l * 16, OFF_CS + h * 3072 + w * 1024); }
          if constexpr (!IT && s == CSS && n == 2) {
            if (w < 3) {
              wait_vm<SINCE(CSL)>();
              asm volatile("" : "+v"(fx[0])::"memory");
              tile_sum(fx[0], L_CSL);
            }
          }
          if constexpr (s == ABL && n == 4) ld4h<0>(aq[0], rs, (l & 31) * 4 + (l >> 5) * 8192, AB_OFF());
          if constexpr (s == ABS && n == 0) {
            wait_vm<SINCE(ABL)>();
            asm volatile("" : "+v"(aq[0])::"memory");
            if constexpr (IT) {
              for (unsigned spins = 0; !__all(aq[0] != POISON) && !spin_fail(spins, rs, T);) {
                ld4h<0>(aq[0], rs, (l & 31) * 4 + (l >> 5) * 8192, AB_OFF());
                asm volatile("s_waitcnt vmcnt(0)" : "+v"(aq[0])::"memory");
              }
            }
            *(LDS_AS unsigned*)(smem + L_K + (8 + rt) * 2048 + l * 4) = aq[0];
          }
          if constexpr (s == ABS && n == 2) {
#pragma unroll
            for (int a = 0; a < 2; ++a) {
              const LDS_AS char* ab = (const LDS_AS char*)(smem + L_K + (8 + rt) * 2048 + a * 64);
              aq[a] = *(const LDS_AS unsigned*)(ab + (l & 15) * 4); bq[a] = *(const LDS_AS unsigned*)(ab + 128 + (l & 15) * 4);
              av[a] = *(const LDS_AS u32x4*)(ab + g * 16); bv[a] = *(const LDS_AS u32x4*)(ab + 128 + g * 16);
            }
          }
          if constexpr (s == ABS && n == 5) {
#pragma unroll
            for (int a = 0; a < 2; ++a) { b2[a] = f32x2{asf(bq[a]), asf(bq[a])}; asm volatile("" : "+v"(b2[a])); }
          }
          if constexpr (s == ABS + 1) {
            constexpr int k = (n + 4) % 6;
            if constexpr (k < 4) cs4[k & 1] = *(const LDS_AS f32x4*)(smem + L_CSL + k * 64 + g * 16);
            else cs4[k & 1][0] = *(const LDS_AS float*)(smem + L_CSL + k * 64 + (l & 15) * 4);
          }
          if constexpr ((s == ABS + 1 && n > 0) || (s == ABS + 2 && n == 0)) {
            constexpr int k = (n + 3) % 6;
            if constexpr (k < 4) {
#pragma unroll
              for (int a = 0; a < 2; ++a) acc[a][k] = __builtin_elementwise_fma(cs4[k & 1], f32x4{b2[a][0], b2[a][1], b2[a][0], b2[a][1]}, acc[a][k]);
            } else {
              f32x2 c2 = {cs4[k & 1][0], cs4[k & 1][0]};
              asm volatile("" : "+v"(c2));
#pragma unroll
              for (int a = 0; a < 2; ++a) acc[a][k] = __builtin_elementwise_fma(f32x4{c2[0], c2[1], c2[0], c2[1]}, __builtin_bit_cast(f32x4, bv[a]), acc[a][k]);
            }
          }
          if constexpr (IT == 0) {
            if constexpr (s == IMG - 2 && n < 3) {
              const int c = 3 * (w & 1) + n;
              ka[n] = (FRAG_T() ^ ((c & 1) << 5)) + (SLOT(5) * STAGE + (c >> 1) * 8192 + (w >> 1) * 2048);
              asm volatile("" : "+v"(ka[n]));
            }
            if constexpr (s == IMG - 1 && (n & 1)) keep[n >> 1] = frag_t2(ka[n >> 1]);
            if constexpr (s > KP0 && s < KP0 + 3 && (n & 1)) keep[3 * (s - KP0) + (n >> 1)] = frag_t2(ka[n >> 1] + (s - KP0) * STAGE);
            if constexpr (s >= IMG && s < IMG + 3 && n == 4) st16(keep[s - IMG], rs, l * 16 + (s - IMG) * 1024, OFF_WI + (h * 8 + ((rot + 5) & 7)) * STAGE + w * 3072);
            if constexpr (s >= TS0 && s < TS0 + TSN && n == 4) {
              if (tstep == s && w < TW) touch(tdum, tsrc);
            }
            if constexpr (s == TST && n == 4) { if (w >= TW) touch(tdum, tsrc); }
          } else {
            if constexpr (s == PB1 && n == 0) { POISON_BIG(1); }
            if constexpr (s == FSL && n == 4) { ld4h<0>(kv, rs, (l & 15) * 4, OFF_KEY + (bid & 15) * 128); row_load(rs, g, ffn_xoff(kg0, 0, w), fx); }
            if constexpr (s == FS0 && n == 0) {
              wait_vm<SINCE(FSL)>();
              asm volatile("" : "+v"(kv)::"memory");
              PINV4(fx);
              if (__builtin_expect(!__all(kv != POISON), 0)) {
                LDS_AS unsigned* kw = (LDS_AS unsigned*)(smem + L_SKEY + 40);
                if (w == 0) {
                  kv = key_wait(rs, T, l, 0, bid & 15, kv);
                } else {
                  while ((kv = __hip_atomic_load(kw, __ATOMIC_RELAXED, __HIP_MEMORY_SCOPE_WORKGROUP)) == POISON) __builtin_amdgcn_s_sleep(2);
                }
              }
              fkey = __builtin_amdgcn_readfirstlane(umax16(kv));
              if (w == 0) *(LDS_AS unsigned*)(smem + L_SKEY + 40) = min(fkey, POISON - 1);
              sel0 = key_row(fkey);
              aff = (int)(sel0 >> 8) == p;
              asm volatile("" : "=v"(sa), "=v"(sb));
              if (ffn_on) {
                if (__builtin_expect(((kg0 ^ fkey) & 0x780u) != 0, 0)) ld4x16(fx, rs, g * 16, ffn_xoff(fkey, 0, w));
                ffn_gu(gv, uv, fx, sa, sb);
              }
            }
            if constexpr (s == FS0 && n == 2) {
              if (ffn_on) {
                ffn_gu_check(rs, T, ffn_xoff(fkey, 0, w), l, gv, uv, fx, sa, sb);
                ffn_gu_store(smem, l, w, sa, sb);
                count_in(1);
              }
            }
            if constexpr (s == FS0D && n == 0) {
              if (ffn_on && w < 4) {
                s16x4 da[8], db[8];
                ffn_d_read(smem, l, 2 * w, da);
                while (__hip_atomic_load(qcnt + 1, __ATOMIC_RELAXED, __HIP_MEMORY_SCOPE_WORKGROUP) < 8u) {}
                asm volatile("" ::: "memory");
                const f32x4 t = ffn_t_read(smem, l);
                ffn_d_read(smem, l, 2 * w + 1, db);
                ffn_d_store(rs, l, 8 * w, 0, slice, ffn_d_mma<8>(da, db, ffn_h(t, l)));
              }
            }
#define RED_LOAD(ld, l8)                                                                                                    \
  {                                                                                                                         \
    const unsigned G = bi, po = OFF_PART + G * 32768;                                                        \
    ld(gv[0], rs, tid * 16, po); ld(gv[1], rs, tid * 16, po + 4096); ld(gv[2], rs, tid * 16, po + 8192); ld(gv[3], rs, tid * 16, po + 12288); \
    ld(uv[0], rs, tid * 16, po + 16384); ld(uv[1], rs, tid * 16, po + 20480); ld(uv[2], rs, tid * 16, po + 24576);        \
    l8(cxn, rs, (l & 3) * 16 + (l & 4) * 2, OFF_CA + (sel0 >> 7) * 2048 + G * 64);                                         \
    l8(cx1, rs, (l & 3) * 16 + (l & 4) * 2, OFF_CB + (sel0 >> 7) * 2048 + G * 64);                                          \
  }
            if constexpr (s == FS1 && n == 4) {
              asm volatile("" : "=v"(cxn), "=v"(cx1));
              if (!ffn_on && w < 4) RED_LOAD(ld16u, ld8h);
            }
            if constexpr (s == FS1S && n == 0) {
              if (!ffn_on && w < 4) {
                wait_vm<SINCE(FS1)>();
                PINV4(gv); PINV4(uv);
                asm volatile("" : "+v"(cxn), "+v"(cx1)::"memory");
                for (unsigned spins = 0; !__all(max(max(max(umax4(gv[0]), umax4(gv[1])), max(umax4(gv[2]), umax4(gv[3]))),
                                                    max(max(umax4(uv[0]), umax4(uv[1])), max(max(umax4(uv[2]), max(cxn[0], cxn[1])), max(cx1[0], cx1[1])))) != POISON) && !spin_fail(spins, rs, T);) {
                  RED_LOAD(ld16u, ld8h);
                  DRAIN();
                  PINV4(gv); PINV4(uv);
                  asm volatile("" : "+v"(cxn), "+v"(cx1)::"memory");
                }
#define F4(x) __builtin_bit_cast(f32x4, x)
                ((LDS_AS f32x4*)(smem + L_SFIN))[tid] = ((F4(gv[0]) + F4(gv[1])) + (F4(gv[2]) + F4(gv[3]))) + ((F4(uv[0]) + F4(uv[1])) + F4(uv[2]));
#undef F4
                count_in(2);
                if (w == 0) {
                  while (__hip_atomic_load(qcnt + 2, __ATOMIC_RELAXED, __HIP_MEMORY_SCOPE_WORKGROUP) < 4u) {}
                  asm volatile("" ::: "memory");
                  const u32x2 ov = red_final<4>(smem, l, key_sig(fkey), cxn, cx1);
                  if (l < 8) st8(unpoison2(ov), rs, OFF_XNEW + (bi * 8 + (l & 3) * 2 + (l >> 2)) * 8, 0);
                }
              }
            }
            if constexpr (s == FXA && n == 4) { DEF4(fx); if (w < 3) row_load(rs, g, XP_OFF(), fx); }
            if constexpr (s == FXB && n == 0) {
              if (w < 3) {
                const unsigned fa = bo[0][0] + SLOT(FXT) * STAGE + w * 2048;
                u32x4 f[8];
#pragma unroll
                for (int i = 0; i < 8; ++i) f[i] = *(const LDS_AS u32x4*)(size_t)(fa + (i >> 1) * 6144 + (i & 1) * 1024);
                wait_vm<SINCE(FXA)>();
                PINV4(fx);
                ffn_xwait<XSL>(rs, T, XP_OFF(), l, fx);
                f32x4 y0 = mfma16(f[0], fx[0], f32x4{0.f, 0.f, 0.f, 0.f}), y1 = mfma16(f[1], fx[0], f32x4{0.f, 0.f, 0.f, 0.f});
#pragma unroll
                for (int i = 1; i < 4; ++i) { y0 = mfma16(f[2 * i], fx[i], y0); y1 = mfma16(f[2 * i + 1], fx[i], y1); }
                if ((l & 14) == 0) st16(unpoison4(__builtin_bit_cast(u32x4, (l & 1) ? y1 : y0)), rs, g * 16 + (l & 1) * 512, OFF_FX + ((h * 6 + 2 * w) * 8 + p) * 64);
              }
            }
            if constexpr (s == FXL && n == 4) {
              asm volatile("" : "=v"(fx[0]), "=v"(fx[1]));
              if (aff && w < 4) { ld16h<0>(fx[0], rs, l * 16, FP_OFF(0)); ld16h<0>(fx[1], rs, l * 16, FP_OFF(1)); }
            }
            if constexpr (s == FXS && n == 2) {
              if (aff && w < 4) {
                wait_vm<0>();
                asm volatile("" : "+v"(fx[0]), "+v"(fx[1])::"memory");
                if (!FX_OK(2)) {
                  unsigned spins = 0;
                  do {
                    if (spin_fail(spins, rs, T)) break;
                    fx[0] = ld16(rs, l * 16, FP_OFF(0)); fx[1] = ld16(rs, l * 16, FP_OFF(1));
                  } while (!FX_OK(2));
                }
                if (w == 3) {
                  f32x2 nx[8];
                  unpack8v(fx[0], nx); unpack8v(fx[1], nx + 4);
                  const f32x2 sm = ((nx[0] + nx[1]) + (nx[2] + nx[3])) + ((nx[4] + nx[5]) + (nx[6] + nx[7]));
                  const float mean = sum64(hadd(sm)) * (1.0f / 1024.0f);
                  const f32x2 m2 = {mean, mean};
                  f32x2 q = {0.f, 0.f};
#pragma unroll
                  for (int j = 0; j < 8; ++j) { const f32x2 d = nx[j] - m2; q = __builtin_elementwise_fma(d, d, q); }
                  const float rd = ln_rd(sum64(hadd(q)) * (1.0f / 1024.0f));
                  if (l == 0) *(LDS_AS f32x2*)(smem + L_FXL + 384) = f32x2{rd, -rbf1(mean)};
                } else {
                  tile_sum(fx[0], L_FXL);
                }
                count_in(3);
              }
            }
          }
        };
        static_for<32>([&](auto sc_) __attribute__((always_inline)) {
          constexpr int s = decltype(sc_)::value;
          if constexpr (s > 0) PIN_ACC();
          if constexpr (IT == 1) wait_vm<vm_at(IT, s, 0) - vm_a(IT, s)>();
          pin_w<s % WA>(af[s % WA][0], af[s % WA][1]);
          SB();
          static_for<6>([&](auto nc_) __attribute__((always_inline)) {
            constexpr int n = decltype(nc_)::value;
            if constexpr (s == 0) { acc[0][n] = f32x4{0.f, 0.f, 0.f, 0.f}; acc[1][n] = acc[0][n]; }
            constexpr bool on = s < QL || n >= 2;
            if constexpr (on) acc[0][n] = n < 4 ? mfma16(bfr[n].v, af[s % WA][0], acc[0][n]) : mfma16(af[s % WA][0], bfr[n].v, acc[0][n]);
            SB();
            if constexpr (n == 0) { QKV_RDB(s, 5); }
            else if constexpr (s + 1 < 32 && (s + 1 < QL || n > 2)) QKV_RDB(s + 1, n - 1);
            SB();
            if constexpr (on) acc[1][n] = n < 4 ? mfma16(bfr[n].v, af[s % WA][1], acc[1][n]) : mfma16(af[s % WA][1], bfr[n].v, acc[1][n]);
            SB();
            if constexpr (s + 1 < 32 && (s + 1 < QL || n >= 2)) QKV_RDA(s + 1, n);
            SB();
            gap(sc_, nc_);
            SB();
          });
        });
        PIN_ACC();
        frag_t qw[32 - QL][2];
        static_for<32 - QL>([&](auto c_) __attribute__((always_inline)) {
          constexpr int s = QL + decltype(c_)::value;
          QKV_RDA(s, 0); QKV_RDB(s, 0); QKV_RDA(s, 1); QKV_RDB(s, 1);
          qw[s - QL][0] = bfr[0]; qw[s - QL][1] = bfr[1];
        });
        SB();
        const bool own = aff && (int)((sel0 >> 5) & 7) == rt;
        const bool m0 = !(sel0 & 16), mq = (l & 15) == (int)(sel0 & 15), mv = g == (int)((sel0 & 15) >> 2);
        auto patch_qk = [&](int n) __attribute__((always_inline)) {
          f32x4 t = *(const LDS_AS f32x4*)(smem + L_FXL + n * 64 + g * 16);
          const f32x4 c = *(const LDS_AS f32x4*)(smem + L_CSL + n * 64 + g * 16);
          const float nb = *(const LDS_AS float*)(smem + L_FXL + 388);
#pragma unroll
          for (int e = 0; e < 4; ++e) { t[e] = __builtin_fmaf(c[e], nb, t[e]); asm volatile("" : "+v"(t[e])); }
          acc[0][n] = (mq && m0) ? t : acc[0][n];
          acc[1][n] = (mq && !m0) ? t : acc[1][n];
        };
        if (own) {
          while (__hip_atomic_load(qcnt + 3, __ATOMIC_RELAXED, __HIP_MEMORY_SCOPE_WORKGROUP) < 4u) {}
          asm volatile("" ::: "memory");
          patch_qk(2); patch_qk(3);
          const f32x2 nab = *(const LDS_AS f32x2*)(smem + L_FXL + 384);
          const LDS_AS float* fl_ = (const LDS_AS float*)(smem + L_FXL + 256) + (l & 15);
          const LDS_AS float* cl_ = (const LDS_AS float*)(smem + L_CSL + 256) + (l & 15);
          const float t4 = __builtin_fmaf(cl_[0], nab[1], fl_[0]), t5 = __builtin_fmaf(cl_[16], nab[1], fl_[16]);
          const unsigned na = __builtin_bit_cast(unsigned, (float)nab[0]);
#pragma unroll
          for (int a = 0; a < 2; ++a) {
            const bool ma = a ? !m0 : m0;
            aq[a] = (mq && ma) ? na : aq[a];
#pragma unroll
            for (int e = 0; e < 4; ++e) {
              const bool me = mv && ma && e == (int)(sel0 & 3);
              acc[a][4][e] = me ? t4 : acc[a][4][e]; acc[a][5][e] = me ? t5 : acc[a][5][e];
              av[a][e] = me ? na : av[a][e];
            }
          }
        }
        constexpr float C2 = 1.4426950408889634f * 0.17677669529663687f;
#pragma unroll
        for (int a = 0; a < 2; ++a) {
          f32x2 a2 = {asf(aq[a]), asf(aq[a])};
          qs2[a] = f32x2{asf(aq[a]) * C2, asf(aq[a]) * C2};
          asm volatile("" : "+v"(a2), "+v"(qs2[a]));
          acc[a][2] *= f32x4{a2[0], a2[1], a2[0], a2[1]}; acc[a][3] *= f32x4{a2[0], a2[1], a2[0], a2[1]};
          acc[a][4] *= __builtin_bit_cast(f32x4, av[a]); acc[a][5] *= __builtin_bit_cast(f32x4, av[a]);
        }
        k0 = pack8(acc[0][2], acc[0][3]); k1 = pack8(acc[1][2], acc[1][3]); v0 = pack8(acc[0][4], acc[1][4]); v1 = pack8(acc[0][5], acc[1][5]);
        const unsigned kvo = OFF_KV + IT * (8u << 20) + (p * 32 + h) * 32768 + rt * 4096;
        if (p < 7) { st16(k0, rs, l * 16, kvo); st16(k1, rs, l * 16 + 1024, kvo); st16(v0, rs, l * 16 + 2048, kvo); st16(v1, rs, l * 16 + 3072, kvo); }
        char* ownp = smem + (8 + rt) * 2048 + l * 16;
        *(u32x4*)(ownp + L_K) = k0;
        if (rt < 7) *(u32x4*)(ownp + L_K + 1024) = k1;
        *(u32x4*)(ownp + L_V) = v0; *(u32x4*)(ownp + L_V + 1024) = v1;
        SB();
        static_for<32 - QL>([&](auto c_) __attribute__((always_inline)) {
          constexpr int s = QL + decltype(c_)::value;
#pragma unroll
          for (int n = 0; n < 2; ++n) { acc[0][n] = mfma16(qw[s - QL][n].v, af[s % WA][0], acc[0][n]); acc[1][n] = mfma16(qw[s - QL][n].v, af[s % WA][1], acc[1][n]); }
        });
        if (own) { patch_qk(0); patch_qk(1); }
      };
      qkv_loop(it_c);
    }
#undef A_LOAD1
#undef QKV_RDA
#undef QKV_RDB
#undef QKV_BO
    {
      IDS();
      const GAS bf16 *pwo, *pwg, *pwu;
      asm volatile("s_load_dwordx2 %0, %3, 24\n\ts_load_dwordx2 %1, %3, 40\n\ts_load_dwordx2 %2, %3, 48" : "=&s"(pwo), "=&s"(pwg), "=&s"(pwu) : "s"(ka));
      const int ql = l & 15;
      const f32x4 z4 = {0.f, 0.f, 0.f, 0.f}, ninf = {-INFINITY, -INFINITY, -INFINITY, -INFINITY};
      LDS_AS unsigned* rdy = (LDS_AS unsigned*)(smem + L_RDY);
      const unsigned rtag = T[0] + it;
      u32x4 qf[2];
      f32x4 cn[2], oa[2][2], sum[2], S[2][2];
      unsigned one2 = 0x3F803F80u;
      asm volatile("" : "+v"(one2));
      const u32x4 ones = {one2, one2, one2, one2};
      auto ex8 = [&](f32x4 a, f32x4 b) __attribute__((always_inline)) {
        return u32x4{pack2(__builtin_amdgcn_exp2f(a[0]), __builtin_amdgcn_exp2f(a[1])), pack2(__builtin_amdgcn_exp2f(a[2]), __builtin_amdgcn_exp2f(a[3])),
                     pack2(__builtin_amdgcn_exp2f(b[0]), __builtin_amdgcn_exp2f(b[1])), pack2(__builtin_amdgcn_exp2f(b[2]), __builtin_amdgcn_exp2f(b[3]))};
      };
      {
#pragma unroll
        for (int a = 0; a < 2; ++a) {
          const f32x4 c4 = {qs2[a][0], qs2[a][1], qs2[a][0], qs2[a][1]};
          qf[a] = pack8(acc[a][0] * c4, acc[a][1] * c4);
        }
        f32x4 mk;
#pragma unroll
        for (int e = 0; e < 4; ++e) mk[e] = (4 * g + e <= ql) ? 0.f : -INFINITY;
        const f32x4 d0 = mfma16(k0, qf[0], mk), d1 = mfma16(k1, qf[1], mk), d2 = mfma16(k0, qf[1], z4);
        const float m0 = -max_x32(max_x16(fmaxf(fmaxf(d0[0], d0[1]), fmaxf(d0[2], d0[3]))));
        const float m1 = -max_x32(max_x16(fmaxf(fmaxf(d1[0], d1[1]), fmaxf(d1[2], d1[3]))));
        cn[0] = f32x4{m0, m0, m0, m0}; cn[1] = f32x4{m1, m1, m1, m1};
        asm volatile("" : "+v"(cn[0]), "+v"(cn[1]));
        const f32x4 e0 = d0 + cn[0];
        const u32x4 p0 = {pack2(__builtin_amdgcn_exp2f(e0[0]), __builtin_amdgcn_exp2f(e0[1])), pack2(__builtin_amdgcn_exp2f(e0[2]), __builtin_amdgcn_exp2f(e0[3])), 0u, 0u};
        const u32x4 p1 = ex8(d2 + cn[1], d1 + cn[1]);
        oa[0][0] = mfma16(v0, p0, z4); oa[0][1] = mfma16(v1, p0, z4); sum[0] = mfma16(ones, p0, z4);
        oa[1][0] = mfma16(v0, p1, z4); oa[1][1] = mfma16(v1, p1, z4); sum[1] = mfma16(ones, p1, z4);
      }
      const char* kb_ = smem + L_K + l * 16;
      const char* vb_ = smem + L_V + l * 16;
      const unsigned wov = (l >> 3) * 2048 + (((((l & 7) >> 1) ^ ((l >> 4) & 3)) * 2 + (l & 1)) << 4), fnv = (l >> 1) * (F_ * 2) + (l & 1) * 16;
      const unsigned pp = __builtin_amdgcn_readfirstlane(p > 0 ? 32 : 0), pvo = OFF_KV + it * (8u << 20) + (p * 32 + h - pp) * 32768;
      const unsigned pfo = __builtin_amdgcn_readfirstlane(F_KV + KVF(it, p * 32 + h - pp));
      asm volatile("" : "+v"(oa[0][0]), "+v"(oa[0][1]), "+v"(oa[1][0]), "+v"(oa[1][1]), "+v"(sum[0]), "+v"(sum[1]));
      DRAIN();
      asm volatile("" : "+v"(tdum));
      if (l == 0) st8(T, rs, rt * 8, F_KV + KVF(it, p * 32 + h));
      BAR();
      asm volatile("" : "+s"(pwo), "+s"(pwg), "+s"(pwu));
      WO_PIECE(15); WO_PIECE(14);
      u32x4 kf[2], vf[2];
      int nw = 0;
      u32x4 p0, p1;
      float e0, e1;
#define EE(v, i) { e0 = __builtin_amdgcn_exp2f(v[i]); e1 = __builtin_amdgcn_exp2f(v[i + 1]); pin2s(e0, e1); SB(); }
#define CV(P, i) { unsigned t_ = pack2(e0, e1); pin1(t_); P[i] = t_; SB(); }
      auto body = [&](unsigned ad, auto dk_) __attribute__((always_inline)) {
        constexpr int dk = decltype(dk_)::value;
        const f32x4 a0 = S[1][0], a1 = S[1][1];
        sum[0] = mfma16(ones, p0, sum[0]); SB();
        EE(a0, 0);
        lds_wait<2>(kf[0], kf[1]);
        S[0][0] = mfma16(kf[0], qf[0], cn[0]); SB();
        CV(p1, 0); EE(a0, 2);
        S[0][1] = mfma16(kf[1], qf[0], cn[0]); SB();
        CV(p1, 1); EE(a1, 0);
        lds_wait<0>(vf[0], vf[1]);
        oa[0][0] = mfma16(vf[0], p0, oa[0][0]); SB();
        CV(p1, 2); EE(a1, 2);
        oa[0][1] = mfma16(vf[1], p0, oa[0][1]); SB();
        CV(p1, 3);
        S[1][0] = mfma16(kf[0], qf[1], cn[1]); SB();
        EE(S[0][0], 0);
        S[1][1] = mfma16(kf[1], qf[1], cn[1]); SB();
        lds2<2048 + dk>(kf[0], kf[1], ad); SB();
        CV(p0, 0); EE(S[0][0], 2);
        oa[1][0] = mfma16(vf[0], p1, oa[1][0]); SB();
        CV(p0, 1); EE(S[0][1], 0);
        oa[1][1] = mfma16(vf[1], p1, oa[1][1]); SB();
        lds2<2048 + L_V>(vf[0], vf[1], ad); SB();
        CV(p0, 2); EE(S[0][1], 2);
        sum[1] = mfma16(ones, p1, sum[1]); SB();
        CV(p0, 3);
      };
      auto last = [&]() __attribute__((always_inline)) {
        sum[0] = mfma16(ones, p0, sum[0]); SB();
        EE(S[1][0], 0);
        lds_wait<0>(vf[0], vf[1]);
        pin2(kf[0], kf[1]);
        oa[0][0] = mfma16(vf[0], p0, oa[0][0]); SB();
        CV(p1, 0); EE(S[1][0], 2);
        oa[0][1] = mfma16(vf[1], p0, oa[0][1]); SB();
        CV(p1, 1); EE(S[1][1], 0);
        const float f0 = __builtin_amdgcn_exp2f(S[1][1][2]), f1 = __builtin_amdgcn_exp2f(S[1][1][3]);
        p1[2] = pack2(e0, e1); p1[3] = pack2(f0, f1);
        oa[1][0] = mfma16(vf[0], p1, oa[1][0]); oa[1][1] = mfma16(vf[1], p1, oa[1][1]); sum[1] = mfma16(ones, p1, sum[1]);
      };
      auto piece = [&]() __attribute__((always_inline)) {
        if constexpr (it == 1) { WO_PIECE(13 - nw); }
        else if (ffn_on) { FFN_DMA(nw); }
        ++nw;
      };
      if (rt > 0) {
        unsigned ad = lbase + l * 16 + (6 + rt) * 2048;
        lds2<2048>(kf[0], kf[1], ad); lds2<2048 + L_V>(vf[0], vf[1], ad);
        lds_wait<2>(kf[0], kf[1]);
        S[0][0] = mfma16(kf[0], qf[0], cn[0]); S[0][1] = mfma16(kf[1], qf[0], cn[0]); S[1][0] = mfma16(kf[0], qf[1], cn[1]); S[1][1] = mfma16(kf[1], qf[1], cn[1]);
        SB();
        lds_wait<0>(vf[0], vf[1]);
        lds2<0>(kf[0], kf[1], ad);
        p0 = ex8(S[0][0], S[0][1]);
        lds_wait<0>(kf[0], kf[1]);
#pragma unroll 1
        for (int i = rt - 1; i > 0; --i) {
          ad -= 2048;
          body(ad, std::integral_constant<int, -2048>{});
          piece();
          SB();
        }
        piece();
        last();
      }
      {
        constexpr int FW = 4, FN = 8 / FW;
        if (w < FW) {
          const int g0 = FN * rt;
          poll_flags(rs, (l & (FN - 1)) * 8, pfo + g0 * 8, T);
#pragma unroll
          for (int j = 0; j < FN; ++j) {
            const unsigned d = lbase + (g0 + j) * 2048, o = pvo + (g0 + j) * 4096;
            dma16s<0>(rs, l * 16, o, d + L_K); dma16s<1024>(rs, l * 16, o, d + L_K);
            dma16s<0>(rs, l * 16, o + 2048, d + L_V); dma16s<1024>(rs, l * 16, o + 2048, d + L_V);
          }
        }
        if (p == 0) { cn[0] = ninf; cn[1] = ninf; }
        f32x4 mk;
#pragma unroll
        for (int e = 0; e < 4; ++e) mk[e] = (4 * g + e > ql) ? 0.f : -INFINITY;
        const f32x4 cm0 = cn[0] + mk, cm1 = cn[1] + mk;
        if (w < FW) {
          if constexpr (FN > 1) { wait_vm<2 * FN>(); if (l < FN / 2) rdy[FN * rt + l] = rtag; }
          DRAIN();
          if (l < FN) rdy[FN * rt + l] = rtag;
        }
        if constexpr (it == 1) { WO_PIECE(6); WO_PIECE(5); WO_PIECE(4); }
        else { WO_PIECE(13); WO_PIECE(12); if (ffn_on) FFN_DMA(7); }

        int have = rt - 1;
        auto need = [&](int gq) __attribute__((always_inline)) {
          if (__builtin_expect(gq > have && gq < 8, 0)) {
            do {
              const unsigned t = __hip_atomic_load(rdy + (l & 7), __ATOMIC_RELAXED, __HIP_MEMORY_SCOPE_WORKGROUP);
              have = rt - 1 + __builtin_ctz(~((unsigned)__builtin_amdgcn_ballot_w64(t == rtag) & 0xffu) >> rt);
            } while (gq > have);
            asm volatile("" ::: "memory");
          }
        };
        unsigned ad = lbase + l * 16 + rt * 2048;
        need(rt);
        lds2<0>(kf[0], kf[1], ad); lds2<L_V>(vf[0], vf[1], ad);
        lds_wait<2>(kf[0], kf[1]);
        S[0][0] = mfma16(kf[0], qf[0], cm0); S[0][1] = mfma16(kf[1], qf[0], cn[0]); S[1][0] = ninf; S[1][1] = mfma16(kf[1], qf[1], cm1);
        SB();
        lds_wait<0>(vf[0], vf[1]);
        need(rt + 1);
        lds2<2048>(kf[0], kf[1], ad);
        p0 = ex8(S[0][0], S[0][1]);
        lds_wait<0>(kf[0], kf[1]);
#pragma unroll 1
        for (int gq = rt + 1; gq < 8; ++gq) {
          need(gq + 1);
          body(ad, std::integral_constant<int, 2048>{});
          piece();
          SB();
          ad += 2048;
        }
        last();
      }
      {
        float big = 0.f;
#pragma unroll
        for (int a = 0; a < 2; ++a)
#pragma unroll
          for (int d = 0; d < 2; ++d)
            big = __builtin_elementwise_maximum(__builtin_elementwise_maximum(big, __builtin_elementwise_maximum(fabsf(oa[a][d][0]), fabsf(oa[a][d][1]))),
                                                __builtin_elementwise_maximum(fabsf(oa[a][d][2]), fabsf(oa[a][d][3])));
        bool ok = sum[0][0] >= 0.5f && sum[0][0] < 0x1p64f && sum[1][0] >= 0.5f && sum[1][0] < 0x1p64f && big < INFINITY;
        if (!__all(ok)) {
#pragma unroll
          for (int a = 0; a < 2; ++a) {
            const int qs = 2 * rt + a, qq = 256 + 16 * qs + ql, klo = max(qq - 255, p > 0 ? 0 : 256);
            auto score = [&](int t) __attribute__((always_inline)) {
              f32x4 s = mfma16(t == 31 ? k1 : *(const u32x4*)(kb_ + t * 1024), qf[a], z4);
#pragma unroll
              for (int e = 0; e < 4; ++e) { const int kk = 16 * t + 4 * g + e; s[e] = (kk <= qq && kk >= klo) ? s[e] : -INFINITY; }
              return s;
            };
            float mx = -INFINITY;
#pragma unroll 1
            for (int t = qs; t <= qs + 16; ++t) { const f32x4 s = score(t); mx = fmaxf(fmaxf(mx, fmaxf(s[0], s[1])), fmaxf(s[2], s[3])); }
            mx = max_x32(max_x16(mx));
            oa[a][0] = z4; oa[a][1] = z4; sum[a] = z4;
#pragma unroll 1
            for (int G = qs >> 1; G <= (qs + 16) >> 1; ++G) {
              const f32x4 s0 = score(2 * G), s1 = score(2 * G + 1);
              f32x4 e0, e1;
#pragma unroll
              for (int e = 0; e < 4; ++e) { e0[e] = __builtin_amdgcn_exp2f(s0[e] - mx); e1[e] = __builtin_amdgcn_exp2f(s1[e] - mx); }
              const u32x4 pf = pack8(e0, e1);
              oa[a][0] = mfma16(*(const u32x4*)(vb_ + G * 2048), pf, oa[a][0]);
              oa[a][1] = mfma16(*(const u32x4*)(vb_ + G * 2048 + 1024), pf, oa[a][1]);
              sum[a] = mfma16(ones, pf, sum[a]);
            }
          }
        }
      }
#pragma unroll
      for (int a = 0; a < 2; ++a) {
        const float inv = __builtin_amdgcn_rcpf(sum[a][0]);
        oa[a][0] *= inv; oa[a][1] *= inv;
      }
      const unsigned oo = OFF_O + it * (4u << 20) + ((p * 8 + rt) * 64 + h * 2) * 1024;
      st16(pack8(oa[0][0], oa[0][1]), rs, l * 16, oo);
      st16(pack8(oa[1][0], oa[1][1]), rs, l * 16 + 1024, oo);
      DRAIN();
      BAR();
      if (tid == 0) st8(T, rs, OFL(h), F_O + OFB(it, p));
      if constexpr (it == 0) {
        DEF4(gv); DEF4(uv);
        if (ffn_on) {
          const unsigned ro = 65536 + w * 1024 + (4 * g + ((l & 15) >> 2)) * 32 + (l & 3) * 8;
#pragma unroll
          for (int i = 0; i < 4; ++i) {
            frag_t f;
            f.hf.a = trread(smem, ro + i * 16384); f.hf.b = trread(smem, ro + i * 16384 + 512);
            gv[i] = f.v;
            f.hf.a = trread(smem, ro + i * 16384 + 8192); f.hf.b = trread(smem, ro + i * 16384 + 8704);
            uv[i] = f.v;
          }
        }
        PINV4(gv); PINV4(uv);
        {
          ARGP(px, 0);
          const unsigned xo = ((rb * 4 + (w & 3)) * 32 + (w >> 2) * 16) * 2048 + ct * 128, xw = (l >> 3) * 2048 + (((l & 7) ^ g) << 4);
          dma16(RSRC(px, 2048 * 1024 * 2), xw, xo, lbase + w * 4096); dma16(RSRC(px, 2048 * 1024 * 2), xw ^ 64, xo + 16384, lbase + w * 4096 + 1024);
        }
      }
      WO_PIECE(3); WO_PIECE(2); WO_PIECE(1); WO_PIECE(0);
      if constexpr (it == 0) { WO_PIECE(11); WO_PIECE(10); WO_PIECE(9); WO_PIECE(8); WO_PIECE(7); WO_PIECE(6); WO_PIECE(5); WO_PIECE(4); }
    }
    IDS();
    ARGP(pwr, 4); ARGP(pwd, 7); ARGP(pout, 8);
    const int kh = w >> 2, rt2 = rb * 4 + (w & 3);
    const unsigned R = rt2 * 32 + kh * 16 + (l & 15);
    const unsigned to = (rt2 * 64 + ct * 4 + kh) * 1024 + l * 16;
    const unsigned rmo = ct * 64 + ((g & 1) ? 12 + 4 * g : 4 * g);
    f32x2 v[8], wv[8];
    float wsum;
    {
      u32x4 wa[2], wrp[2], res[2];
#pragma unroll
      for (int j = 0; j < 2; ++j) {
        wrp[j] = *(const GAS u32x4*)(pwr + rmo + 32 * j);
        wa[j] = *(const GAS u32x4*)(pwr + l * 8 + j * 512);
      }
      {
        f32x2 t[8];
        unpack8v(wa[0], t); unpack8v(wa[1], t + 4);
        const f32x2 s = ((t[0] + t[1]) + (t[2] + t[3])) + ((t[4] + t[5]) + (t[6] + t[7]));
        wsum = sum64(hadd(s));
        unpack8v(trade16(wrp[0]), wv); unpack8v(trade16(wrp[1]), wv + 4);
        asm volatile("" : "+v"(wsum), "+v"(wv[0]), "+v"(wv[1]), "+v"(wv[2]), "+v"(wv[3]), "+v"(wv[4]), "+v"(wv[5]), "+v"(wv[6]), "+v"(wv[7]));
      }
      unsigned bt[4];
#pragma unroll
      for (int n = 0; n < 4; ++n) {
        bt[n] = lbase + 32768 + 65536 + kh * 4096 + (4 * g + ((l & 15) >> 2)) * 128 + (l & 3) * 8 + ((n ^ (2 * (g & 1) + ((l & 15) >> 3))) << 5);
        asm volatile("" : "+v"(bt[n]));
      }
      if (w == 0) poll_flags(rs, OFL(l & 31), F_O + OFB(it, rb >> 1), T);
      else if (it == 0 && w < 3) poll_flags2(rs, F_XN + (((w - 1) * 4 + g) * RX + bi % RX) * 256 + (l & 15) * 16, T);
      else if (it == 0 && w == 3) poll_flags(rs, ((l >> 3) * 32 + h) * 128 + (l & 7) * 8, F_KV, T);
      asm volatile("s_barrier" ::: "memory");
      const unsigned abase = OFF_O + it * (4u << 20) + (rt2 * 64 + 32 * kh) * 1024;
      const unsigned av0 = l * 16 + kh * 1024, av1 = l * 16 + (kh ^ 1) * 1024;
      u32x4 af[WB][2];
#define O_LOAD(s_) { ld16h<(wo_k(it, s_) & 1) * 2048>(af[(s_) % WB][0], rs, av0, abase + (wo_k(it, s_) >> 1) * 4096); ld16h<(wo_k(it, s_) & 1) * 2048>(af[(s_) % WB][1], rs, av1, abase + (wo_k(it, s_) >> 1) * 4096); }
      if (w >= 4) __builtin_amdgcn_s_setprio(1);
      static_for<WR>([&](auto c_) __attribute__((always_inline)) { O_LOAD(decltype(c_)::value); });
      if constexpr (it == 1) { ld16h<0>(res[0], rs, g * 16, OFF_XNEW + ct * 128); ld16h<64>(res[1], rs, g * 16, OFF_XNEW + ct * 128); }
      SB();
      f32x4 ac[2][4];
#pragma unroll
      for (int a = 0; a < 2; ++a)
#pragma unroll
        for (int n = 0; n < 4; ++n) ac[a][n] = f32x4{0.f, 0.f, 0.f, 0.f};
      u32x4 bfr[2][4];
#define WO_RD(buf, s, n)                                                                             \
  {                                                                                                  \
    const unsigned o_ = bt[n] + (wo_k(it, s) & 7) * 8192;                                             \
    frag_t f_;                                                                                       \
    f_.hf.a = trabs(o_); f_.hf.b = trabs(o_ + 2048);                                                 \
    bfr[buf][n] = f_.v;                                                                              \
  }
#pragma unroll
      for (int n = 0; n < 4; ++n) WO_RD(0, 0, n);
      static_for<16>([&](auto sc_) __attribute__((always_inline)) {
        constexpr int s = decltype(sc_)::value, cur = s & 1;
        pin_ac(ac);
        if constexpr (s == 7) __builtin_amdgcn_s_setprio(0);
        if constexpr (s == 1) lds_count<0>(lbase + L_QC);
        if constexpr (s == (it == 0 ? 3 : 11)) lds_look(smem + L_QC, 64);
        if constexpr (s < 15 && ((wo_k(it, s) ^ wo_k(it, s + 1)) & 8)) {
#pragma unroll
          for (int n = 0; n < 4; ++n) { bt[n] += (wo_k(it, s) & 8) ? -65536 : 65536; pin1(bt[n]); }
        }
        if constexpr (s < WB - WR) O_LOAD(WR + 2 * s);
        if constexpr (it == 0 && s == 8) lds_count<8>(lbase + L_QC);
        if constexpr (it == 0 && s == WDS) {
          DRAIN();
          if (ffn_on) { lds_look(smem + L_QC + 8, 16); WD_DMA_OF(w); }
        }
        if constexpr (it == 1 || s < WDS) wait_vm<wo_at(it, s) - wo_pos(it, s)>();
        pin2(af[s % WB][0], af[s % WB][1]);
        if constexpr (it == 1 && s == 9) pin2(res[0], res[1]);
        SB();
#pragma unroll
        for (int n = 0; n < 4; ++n) {
          ac[0][n] = mfma16(bfr[cur][n], af[s % WB][0], ac[0][n]);
          ac[1][n] = mfma16(bfr[cur][n], af[s % WB][1], ac[1][n]);
          if (s + 1 < 16) WO_RD(cur ^ 1, s + 1, n);
          if (it == 0 && s == 8) {
            const u32x2 t = *(const LDS_AS u32x2*)(smem + w * 4096 + (l & 15) * 128 + (((2 * n + (g >> 1)) ^ ((l & 15) >> 1)) << 4) + (g & 1) * 8);
            res[n >> 1][2 * (n & 1)] = t[0]; res[n >> 1][2 * (n & 1) + 1] = t[1];
          }
          if (s == 9) {
            if (it == 1 && n == 0) { res[0] = R == sel0 ? res[0] : x2p[0]; res[1] = R == sel0 ? res[1] : x2p[1]; }

            v[2 * n] = f32x2{lo16(res[n >> 1][2 * (n & 1)]), hi16(res[n >> 1][2 * (n & 1)])};
            v[2 * n + 1] = f32x2{lo16(res[n >> 1][2 * (n & 1) + 1]), hi16(res[n >> 1][2 * (n & 1) + 1])};
            pin2f(v[2 * n], v[2 * n + 1]);
          }
          SB();
        }
        if constexpr (s < WB - WR) { O_LOAD(WR + 2 * s + 1); }
        else if constexpr (s + WB < 16) { O_LOAD(s + WB); }
        SB();
      });
#undef WO_RD
#undef O_LOAD
#define XCH_AT(v) ((v) == 7 ? 155648 : (v) * 4096)
#pragma unroll
      for (int n = 0; n < 4; ++n) *(f32x4*)(smem + XCH_AT(w) + n * 1024 + l * 16) = ac[1][n];
#pragma unroll
      for (int n = 0; n < 4; ++n) { v[2 * n] += f32x2{ac[0][n][0], ac[0][n][1]}; v[2 * n + 1] += f32x2{ac[0][n][2], ac[0][n][3]}; }
      BAR();
#pragma unroll
      for (int n = 0; n < 4; ++n) {
        const f32x4 o = *(const f32x4*)(smem + XCH_AT(w ^ 4) + n * 1024 + l * 16);
        v[2 * n] += f32x2{o[0], o[1]}; v[2 * n + 1] += f32x2{o[2], o[3]};
      }
    }
    {
      if (it == 0) { st16(pack8v(v), rs, to, OFF_X1); st16(pack8v(v + 4), rs, to + 2048, OFF_X1); }
      SB();
      const f32x2 s = ((v[0] + v[1]) + (v[2] + v[3])) + ((v[4] + v[5]) + (v[6] + v[7]));
      f32x2 dp = v[0] * wv[0], q = v[0] * v[0];
#pragma unroll
      for (int j = 1; j < 8; ++j) { dp = __builtin_elementwise_fma(v[j], wv[j], dp); q = __builtin_elementwise_fma(v[j], v[j], q); }
      float mt = hadd(s), qq = hadd(q), dot = hadd(dp);
      add_g3(mt, qq, dot);
      const unsigned so = OFF_STAT + it * 524288 + rb * 32768, lrow = R & 127;
      if (g == 0) st16(unpoison4(__builtin_bit_cast(u32x4, f32x4{mt, qq, dot, 0.f})), rs, ct * 2048 + lrow * 16, so);
      SB();
      if (it == 0) wait_vm<1>();
      if constexpr (it == 0) {
        volatile LDS_AS u32x2* in = (volatile LDS_AS u32x2*)(smem + L_XC);
        if (l == 0) in[w] = T;
        const u32x2 t = in[l & 7];
        if (__all(t[0] == T[0] && t[1] == T[1]) && l < RX) st8(T, rs, (l * 32 + (rb & 1) * 16 + ct) * 8, F_X1 + (rb >> 1) * RX * 32 * 8);
      }
      if (it == 1) { if (ffn_on) { WD_DMA_OF(w); } }
      if (it == 0) {
#pragma unroll
        for (int j = 0; j < 9; ++j) *(u32x4*)(smem + L_RING + (j / 3) * STAGE + (w * 3 + j % 3) * 1024 + l * 16) = keep[j];
      }
      __builtin_amdgcn_s_sleep(it == 0 ? SDI0 : SDIR);
      char* const skey = smem + (it == 0 ? L_SKEY : L_XC);
      u32x4 sp[4];
      ld16h<0>(sp[0], rs, g * 8192 + lrow * 16, so); ld16h<2048>(sp[1], rs, g * 8192 + lrow * 16, so);
      ld16h<0>(sp[2], rs, g * 8192 + lrow * 16, so + 4096); ld16h<2048>(sp[3], rs, g * 8192 + lrow * 16, so + 4096);
      u32x2 xf;
      unsigned k_;
      asm volatile("" : "=v"(xf), "=v"(k_));
      if (it == 0 && w == 0) { ld8h(xf, rs, (l & 31) * 8, F_X1 + (p * RX + (xcd >> 1) % RX) * 32 * 8); ld4h<0>(k_, rs, (l & 15) * 4, OFF_KEY + (bid & 15) * 128); }
      DRAIN();
      asm volatile("" : "+v"(xf), "+v"(k_)::"memory");
      PINV4(sp);
      const RowFin r = fin_key(rs, T, g * 8192 + lrow * 16, so, skey, l, w, R, wsum, sp);
      u32x4 cp[4];
      fin_pack(r, v, cp);
      if constexpr (it == 0) {
        if (w == 0) {
          const unsigned need = 0x30003u << (((PROT() + 5) & 7) * 2);
          unsigned m = (unsigned)__builtin_amdgcn_ballot_w64(xf[0] == T[0] && xf[1] == T[1]);
          for (unsigned spins = 0; (m & need) != need;) {
            if (spin_fail(spins, rs, T)) { m = ~0u; break; }
            const u32x2 f = ld8(rs, F_X1 + ((p * RX + (xcd >> 1) % RX) * 32 + (l & 31)) * 8, 0);
            k_ = key_load(rs, l, 0, bid & 15);
            m = (unsigned)__builtin_amdgcn_ballot_w64(f[0] == T[0] && f[1] == T[1]);
          }
          k_ = umax16(k_ == POISON ? 0u : k_);
          if (l == 0) *(LDS_AS u32x4*)(skey + 32) = u32x4{m, k_, POISON, 0u};
        }
        if (tid == 192) *(LDS_AS u32x4*)(smem + L_QC) = u32x4{0u, 0u, 0u, 0u};
      }
      BAR();
      if constexpr (it == 0) {
        PRE_LOAD(1, PROT() + 5, 0, 4);
        const u32x2 mk = *(const LDS_AS u32x2*)(skey + 32);
        xm0 = __builtin_amdgcn_readfirstlane(mk[0]); kg0 = __builtin_amdgcn_readfirstlane(mk[1]);
      }
      const unsigned own = fin_cand(rs, skey, it, g, rb, ct, r, cp);
      if constexpr (it == 0) kg0 = max(kg0, (unsigned)__builtin_amdgcn_readfirstlane(own));
      fin_x2(r, v);
      if constexpr (it == 0) {
        x2p[0] = pack8v(v); x2p[1] = pack8v(v + 4);
        asm volatile("" : "+v"(x2p[0]), "+v"(x2p[1]));
        if ((w & 3) * 2 + (w >> 2) == ct >> 1) {
          const float f = 1.0f + r.rd, rd2 = ln_rd(r.var * f * f), mb2 = rbf1(__builtin_fmaf(r.mean - r.mb, r.rd, r.mean));
          if (g < 2 && ((l >> 3) & 1) == (ct & 1))
            st4(min(__builtin_bit_cast(unsigned, g == 0 ? f * rd2 : -__builtin_fmaf(r.mb, r.rd, mb2) * __builtin_amdgcn_rcpf(f)), POISON - 1), rs, OFF_AB + 16384 + R * 4 + g * 8192, 0);
        }
      }
    }
    if (it == 1) {
      const u32x4 op0 = trade16(pack8v(v)), op1 = trade16(pack8v(v + 4));
#define OUT_STORE()                                                                                 \
  if (R != key_row(key)) {                                                                          \
    __builtin_amdgcn_raw_buffer_store_b128(op0, rout, (R * 1024 + rmo) * 2, 0, SC1);                  \
    __builtin_amdgcn_raw_buffer_store_b128(op1, rout, (R * 1024 + rmo + 32) * 2, 0, SC1);             \
  }
      u32x4 xv[4];
      unsigned guess = 0;
      {
        __builtin_amdgcn_s_sleep(GSL);
        const unsigned k_ = key_load(rs, l, 1, bid & 15);
        guess = ffn_xoff(__builtin_amdgcn_readfirstlane(umax16(k_ == POISON ? 0u : k_)), 1, w);
        row_load(rs, g, guess, xv);
      }
      if (w == 0) {
        const unsigned k_ = key_wait(rs, T, l, 1, bid & 15, key_load(rs, l, 1, bid & 15));
        if (l == 0) *(LDS_AS unsigned*)(smem + L_SKEY + 32) = k_;
      }
      DRAIN();
      PINV4(xv);
      BAR();
      const unsigned key = __builtin_amdgcn_readfirstlane(*(LDS_AS unsigned*)(smem + L_SKEY + 32));
      if (ffn_on) {
        f32x4 ga, ua;
        s16x4 dfr[8];
        ffn_d_read(smem, l, w, dfr);
        if (guess != ffn_xoff(key, 1, w)) ld4x16(xv, rs, g * 16, ffn_xoff(key, 1, w));
        ffn_gu(gv, uv, xv, ga, ua);
        ffn_gu_check(rs, T, ffn_xoff(key, 1, w), l, gv, uv, xv, ga, ua);
        ffn_gu_store(smem, l, w, ga, ua);
        BAR();
        const f32x4 y = ffn_d_mma<4>(dfr, dfr + 4, ffn_h(ffn_t_read(smem, l), l));
        if ((l & 15) < 8) ffn_d_store(rs, l, 4 * w, 1, slice, y);
        SB();
        __builtin_amdgcn_s_sleep(OSL);
        OUT_STORE();
      } else {
        OUT_STORE();
        const unsigned G = bi;
        if (G == 0 && tid == 0) st8(T, rs, F_EP, 0);
        u32x4 pv[4];
        u32x2 cxn, cx1;
        const float sig = key_sig(key);
        __builtin_amdgcn_s_sleep(RSL);
        red_load(rs, tid, 1, G, key, pv, cxn, cx1);
        red_sum(rs, smem, T, tid, w, 1, G, key, pv, cxn, cx1);
        BAR();
        if (w == 0) {
          const u32x2 ov = red_final<8>(smem, l, sig, cxn, cx1);
          if (l < 8) __builtin_amdgcn_raw_buffer_store_b64(ov, rout, (key_row(key) * 1024 + G * 32 + l * 4) * 2, 0, 0);
        }
      }
#undef OUT_STORE
    }
  };
  layer(std::integral_constant<int, 0>{});
  layer(std::integral_constant<int, 1>{});
}

extern "C" void kernel_launch(void* const* d_in, const int* in_sizes, int n_in, void* d_out, int out_size,
                              void* d_ws, size_t ws_size, hipStream_t stream) {
  (void)hipFuncSetAttribute((const void*)mega_kernel, hipFuncAttributeMaxDynamicSharedMemorySize, LDS_BYTES);
  mega_kernel<<<256, 512, LDS_BYTES, stream>>>((const bf16*)d_in[0], (char*)d_ws, (const bf16*)d_in[1], (const bf16*)d_in[2], (const bf16*)d_in[4],
                                               (const bf16*)d_in[5], (const bf16*)d_in[6], (const bf16*)d_in[7], (bf16*)d_out);
}
```

```cpp
#include <hip/hip_runtime.h>
#include <stdint.h>
#include <utility>

typedef __bf16 bf16;
typedef __bf16 bf16x8 __attribute__((ext_vector_type(8)));
typedef __bf16 bf16x2 __attribute__((ext_vector_type(2)));
typedef float f32x4 __attribute__((ext_vector_type(4)));
typedef float f32x2 __attribute__((ext_vector_type(2)));
typedef short s16x4 __attribute__((ext_vector_type(4)));
typedef unsigned int u32x4 __attribute__((ext_vector_type(4)));
typedef unsigned int u32x2 __attribute__((ext_vector_type(2)));
#define LDS_AS __attribute__((address_space(3)))
#define GAS __attribute__((address_space(1)))
typedef GAS unsigned gu32;
typedef GAS unsigned long long gu64;
#define RLX_AGENT __ATOMIC_RELAXED, __HIP_MEMORY_SCOPE_AGENT
typedef __amdgpu_buffer_rsrc_t rsrc_t;

constexpr int F_ = 3584;
constexpr int SC1 = 16;

constexpr int RX = 4;
constexpr unsigned F_EP = 0, F_TMO = 128;
constexpr unsigned F_XN = 4096;
constexpr unsigned F_O = 8u << 20;
constexpr unsigned F_X1 = F_O + 131072;
constexpr unsigned OFF_KEY = F_O + 393216;
constexpr unsigned F_KV = F_O + 524288;
#define KVF(it, i) ((i) * 128 + (it) * 64)
#define OFL(h) (((h) >> 3) * 128 + ((h) & 7) * 8)
#define OFB(it, p) ((p) * 512 + (it) * 64)
constexpr unsigned OFF_AB = 147456;
constexpr unsigned OFF_XNEW = 262144;
constexpr unsigned OFF_CS = 294912;
constexpr unsigned OFF_FX = 393216;
constexpr unsigned OFF_CA = 524288;
constexpr unsigned OFF_CB = 655360;
constexpr unsigned OFF_STAT = 1u << 20;
constexpr unsigned OFF_PART = 3u << 20;
constexpr unsigned OFF_X1 = 12u << 20;
constexpr unsigned OFF_O = 16u << 20;
constexpr unsigned OFF_KV = 32u << 20;
constexpr unsigned OFF_WI = 52u << 20;
constexpr unsigned WS_BYTES = 58u << 20;
constexpr unsigned POISON = 0xFFFFFFFFu;

constexpr int L_CSL = 31872, L_FXL = 32256, L_K = 0, L_V = 32768, L_RING = 65536, L_DN = 32768, L_TOUCH0 = 149504, L_RDY = 31744, L_QC = 31808, LDS_BYTES = 163840;
constexpr int STAGE = 24576;
#define SLOT(t) ((t) & 3)

template <typename F, int... I> __device__ __forceinline__ void static_for_impl(F&& f, std::integer_sequence<int, I...>) {
  (f(std::integral_constant<int, I>{}), ...);
}
template <int N, typename F> __device__ __forceinline__ void static_for(F&& f) { static_for_impl(f, std::make_integer_sequence<int, N>{}); }
__device__ __forceinline__ unsigned pack2(float a, float b) {
  f32x2 v = {a, b};
  return __builtin_bit_cast(unsigned, __builtin_convertvector(v, bf16x2));
}
__device__ __forceinline__ float asf(unsigned u) { return __builtin_bit_cast(float, u); }
__device__ __forceinline__ float lo16(unsigned u) { return __builtin_bit_cast(float, u << 16); }
__device__ __forceinline__ float hi16(unsigned u) { return __builtin_bit_cast(float, u & 0xffff0000u); }
__device__ __forceinline__ u32x4 pack8(const f32x4 a, const f32x4 b) {
  u32x4 o;
  o[0] = pack2(a[0], a[1]); o[1] = pack2(a[2], a[3]); o[2] = pack2(b[0], b[1]); o[3] = pack2(b[2], b[3]);
  return o;
}
__device__ __forceinline__ f32x4 mfma16(u32x4 a, u32x4 b, f32x4 c) {
  return __builtin_amdgcn_mfma_f32_16x16x32_bf16(__builtin_bit_cast(bf16x8, a), __builtin_bit_cast(bf16x8, b), c, 0, 0, 0);
}
__device__ __forceinline__ f32x4 mfma16k16(s16x4 a, s16x4 b, f32x4 c) { return __builtin_amdgcn_mfma_f32_16x16x16bf16_1k(a, b, c, 0, 0, 0); }
template <int CTRL> __device__ __forceinline__ float dppf(float v) {
  return __builtin_bit_cast(float, __builtin_amdgcn_update_dpp(0, __builtin_bit_cast(int, v), CTRL, 0xf, 0xf, true));
}
template <int CTRL> __device__ __forceinline__ unsigned dppu(unsigned v) {
  return (unsigned)__builtin_amdgcn_update_dpp(0, (int)v, CTRL, 0xf, 0xf, true);
}
__device__ __forceinline__ float add_x16(float v) {
  auto r = __builtin_amdgcn_permlane16_swap(__builtin_bit_cast(unsigned, v), __builtin_bit_cast(unsigned, v), false, false);
  return __builtin_bit_cast(float, (unsigned)r[0]) + __builtin_bit_cast(float, (unsigned)r[1]);
}
__device__ __forceinline__ float add_x32(float v) {
  auto r = __builtin_amdgcn_permlane32_swap(__builtin_bit_cast(unsigned, v), __builtin_bit_cast(unsigned, v), false, false);
  return __builtin_bit_cast(float, (unsigned)r[0]) + __builtin_bit_cast(float, (unsigned)r[1]);
}
__device__ __forceinline__ float max_x16(float v) {
  auto r = __builtin_amdgcn_permlane16_swap(__builtin_bit_cast(unsigned, v), __builtin_bit_cast(unsigned, v), false, false);
  return fmaxf(__builtin_bit_cast(float, (unsigned)r[0]), __builtin_bit_cast(float, (unsigned)r[1]));
}
__device__ __forceinline__ float max_x32(float v) {
  auto r = __builtin_amdgcn_permlane32_swap(__builtin_bit_cast(unsigned, v), __builtin_bit_cast(unsigned, v), false, false);
  return fmaxf(__builtin_bit_cast(float, (unsigned)r[0]), __builtin_bit_cast(float, (unsigned)r[1]));
}
__device__ __forceinline__ unsigned umax_x16(unsigned v) {
  auto r = __builtin_amdgcn_permlane16_swap(v, v, false, false);
  return max((unsigned)r[0], (unsigned)r[1]);
}
__device__ __forceinline__ unsigned umax_x32(unsigned v) {
  auto r = __builtin_amdgcn_permlane32_swap(v, v, false, false);
  return max((unsigned)r[0], (unsigned)r[1]);
}
__device__ __forceinline__ float sum_hi8(float v) {
  v += dppf<0x128>(v);
  v = add_x16(v);
  return add_x32(v);
}
__device__ __forceinline__ float sum32(float v) {
  v += dppf<0xB1>(v);
  v += dppf<0x4E>(v);
  v += dppf<0x124>(v);
  v += dppf<0x128>(v);
  return add_x16(v);
}
__device__ __forceinline__ float sum64(float v) {
  v += dppf<0xB1>(v);
  v += dppf<0x4E>(v);
  v += dppf<0x124>(v);
  v += dppf<0x128>(v);
  v = add_x16(v);
  return add_x32(v);
}

#define BAR() asm volatile("s_waitcnt lgkmcnt(0)\n\ts_barrier" ::: "memory")
#define DRAIN() asm volatile("s_waitcnt vmcnt(0)" ::: "memory")
#define SB() __builtin_amdgcn_sched_barrier(0)
template <int N> __device__ __forceinline__ void wait_vm() { asm volatile("s_waitcnt vmcnt(%0)" ::"n"(N) : "memory"); }

__device__ __forceinline__ void dma16(rsrc_t rs, unsigned voff, unsigned soff, unsigned lds) {
  asm volatile("s_mov_b32 m0, %0\n\ts_nop 0\n\tbuffer_load_dwordx4 %1, %2, %3 offen lds" ::"s"(lds), "v"(voff), "s"(rs), "s"(soff) : "memory");
}
template <int OFF> __device__ __forceinline__ void dma16s(rsrc_t rs, unsigned voff, unsigned soff, unsigned lds) {
  asm volatile("s_mov_b32 m0, %0\n\ts_nop 0\n\tbuffer_load_dwordx4 %1, %2, %3 offen offset:%4 sc1 lds" ::"s"(lds), "v"(voff), "s"(rs), "s"(soff), "n"(OFF) : "memory");
}
__device__ __forceinline__ void touch(unsigned& d, const GAS bf16* ptr) { asm volatile("global_load_dword %0, %1, off" : "+v"(d) : "v"(ptr) : "memory"); }
__device__ __forceinline__ void ldrows(u32x4 (&d)[4], rsrc_t rx, unsigned voff, unsigned soff) {
  asm volatile("buffer_load_dwordx4 %0, %4, %5, %6 offen\n\tbuffer_load_dwordx4 %1, %4, %5, %6 offen offset:512\n\t"
               "buffer_load_dwordx4 %2, %4, %5, %6 offen offset:1024\n\tbuffer_load_dwordx4 %3, %4, %5, %6 offen offset:1536"
               : "=&v"(d[0]), "=&v"(d[1]), "=&v"(d[2]), "=&v"(d[3]) : "v"(voff), "s"(rx), "s"(soff) : "memory");
}
__device__ __forceinline__ void touchb(unsigned& d, rsrc_t rs, unsigned vo) { asm volatile("buffer_load_dword %0, %1, %2, 0 offen sc1" : "+v"(d) : "v"(vo), "s"(rs) : "memory"); }
__device__ __forceinline__ float rbf1(float x) { return __builtin_bit_cast(float, pack2(0.f, x)); }
__device__ __forceinline__ u32x4 ld16(rsrc_t rs, unsigned voff, unsigned soff) { return __builtin_amdgcn_raw_buffer_load_b128(rs, voff, soff, SC1); }
template <int OFF> __device__ __forceinline__ void ld16h(u32x4& d, rsrc_t rs, unsigned voff, unsigned soff) {
  asm volatile("buffer_load_dwordx4 %0, %1, %2, %3 offen offset:%4 sc1" : "=v"(d) : "v"(voff), "s"(rs), "s"(soff), "n"(OFF) : "memory");
}
template <int I, int OFF> __device__ __forceinline__ void ld16w(u32x4& d, rsrc_t rs, unsigned voff, unsigned soff) {
  if constexpr (I == 0) asm volatile("buffer_load_dwordx4 %0, %1, %2, %3 offen offset:%4 sc1" : "={v[128:131]}"(d) : "v"(voff), "s"(rs), "s"(soff), "n"(OFF) : "memory");
  else if constexpr (I == 1) asm volatile("buffer_load_dwordx4 %0, %1, %2, %3 offen offset:%4 sc1" : "={v[132:135]}"(d) : "v"(voff), "s"(rs), "s"(soff), "n"(OFF) : "memory");
  else if constexpr (I == 2) asm volatile("buffer_load_dwordx4 %0, %1, %2, %3 offen offset:%4 sc1" : "={v[136:139]}"(d) : "v"(voff), "s"(rs), "s"(soff), "n"(OFF) : "memory");
  else if constexpr (I == 3) asm volatile("buffer_load_dwordx4 %0, %1, %2, %3 offen offset:%4 sc1" : "={v[140:143]}"(d) : "v"(voff), "s"(rs), "s"(soff), "n"(OFF) : "memory");
  else if constexpr (I == 4) asm volatile("buffer_load_dwordx4 %0, %1, %2, %3 offen offset:%4 sc1" : "={v[144:147]}"(d) : "v"(voff), "s"(rs), "s"(soff), "n"(OFF) : "memory");
  else if constexpr (I == 5) asm volatile("buffer_load_dwordx4 %0, %1, %2, %3 offen offset:%4 sc1" : "={v[148:151]}"(d) : "v"(voff), "s"(rs), "s"(soff), "n"(OFF) : "memory");
  else if constexpr (I == 6) asm volatile("buffer_load_dwordx4 %0, %1, %2, %3 offen offset:%4 sc1" : "={v[152:155]}"(d) : "v"(voff), "s"(rs), "s"(soff), "n"(OFF) : "memory");
  else if constexpr (I == 7) asm volatile("buffer_load_dwordx4 %0, %1, %2, %3 offen offset:%4 sc1" : "={v[156:159]}"(d) : "v"(voff), "s"(rs), "s"(soff), "n"(OFF) : "memory");
  else if constexpr (I == 8) asm volatile("buffer_load_dwordx4 %0, %1, %2, %3 offen offset:%4 sc1" : "={v[160:163]}"(d) : "v"(voff), "s"(rs), "s"(soff), "n"(OFF) : "memory");
  else if constexpr (I == 9) asm volatile("buffer_load_dwordx4 %0, %1, %2, %3 offen offset:%4 sc1" : "={v[164:167]}"(d) : "v"(voff), "s"(rs), "s"(soff), "n"(OFF) : "memory");
  else if constexpr (I == 10) asm volatile("buffer_load_dwordx4 %0, %1, %2, %3 offen offset:%4 sc1" : "={v[168:171]}"(d) : "v"(voff), "s"(rs), "s"(soff), "n"(OFF) : "memory");
  else if constexpr (I == 11) asm volatile("buffer_load_dwordx4 %0, %1, %2, %3 offen offset:%4 sc1" : "={v[172:175]}"(d) : "v"(voff), "s"(rs), "s"(soff), "n"(OFF) : "memory");
}
template <int K> __device__ __forceinline__ void pin_w(u32x4& a, u32x4& b) {
  if constexpr (K == 0) asm volatile("" : "+{v[128:131]}"(a), "+{v[132:135]}"(b)::"memory");
  else if constexpr (K == 1) asm volatile("" : "+{v[136:139]}"(a), "+{v[140:143]}"(b)::"memory");
  else if constexpr (K == 2) asm volatile("" : "+{v[144:147]}"(a), "+{v[148:151]}"(b)::"memory");
  else if constexpr (K == 3) asm volatile("" : "+{v[152:155]}"(a), "+{v[156:159]}"(b)::"memory");
  else if constexpr (K == 4) asm volatile("" : "+{v[160:163]}"(a), "+{v[164:167]}"(b)::"memory");
  else if constexpr (K == 5) asm volatile("" : "+{v[168:171]}"(a), "+{v[172:175]}"(b)::"memory");
}
__device__ __forceinline__ void ld8h(u32x2& d, rsrc_t rs, unsigned voff, unsigned soff) {
  asm volatile("buffer_load_dwordx2 %0, %1, %2, %3 offen sc1" : "=v"(d) : "v"(voff), "s"(rs), "s"(soff) : "memory");
}
__device__ __forceinline__ void ld16u(u32x4& d, rsrc_t rs, unsigned voff, unsigned soff) {
  asm volatile("buffer_load_dwordx4 %0, %1, %2, %3 offen sc1" : "+v"(d) : "v"(voff), "s"(rs), "s"(soff) : "memory");
}
template <int OFF> __device__ __forceinline__ void ld4h(unsigned& d, rsrc_t rs, unsigned voff, unsigned soff) {
  asm volatile("buffer_load_dword %0, %1, %2, %3 offen offset:%4 sc1" : "=v"(d) : "v"(voff), "s"(rs), "s"(soff), "n"(OFF) : "memory");
}
__device__ __forceinline__ void ld4x16(u32x4 (&d)[4], rsrc_t rs, unsigned voff, unsigned soff) {
  asm volatile("buffer_load_dwordx4 %0, %4, %5, %6 offen sc1\n\tbuffer_load_dwordx4 %1, %4, %5, %6 offen offset:64 sc1\n\t"
               "buffer_load_dwordx4 %2, %4, %5, %6 offen offset:128 sc1\n\tbuffer_load_dwordx4 %3, %4, %5, %6 offen offset:192 sc1\n\ts_waitcnt vmcnt(0)"
               : "=&v"(d[0]), "=&v"(d[1]), "=&v"(d[2]), "=&v"(d[3]) : "v"(voff), "s"(rs), "s"(soff) : "memory");
}
__device__ __forceinline__ u32x2 ld8(rsrc_t rs, unsigned voff, unsigned soff) { return __builtin_amdgcn_raw_buffer_load_b64(rs, voff, soff, SC1); }
__device__ __forceinline__ void st16(u32x4 v, rsrc_t rs, unsigned voff, unsigned soff) { __builtin_amdgcn_raw_buffer_store_b128(v, rs, voff + soff, 0, SC1); }
__device__ __forceinline__ void st8(u32x2 v, rsrc_t rs, unsigned voff, unsigned soff) { __builtin_amdgcn_raw_buffer_store_b64(v, rs, voff, soff, SC1); }

__device__ __forceinline__ unsigned ld4(rsrc_t rs, unsigned voff, unsigned soff) { return __builtin_amdgcn_raw_buffer_load_b32(rs, voff, soff, SC1); }
__device__ __forceinline__ void st4(unsigned v, rsrc_t rs, unsigned voff, unsigned soff) { __builtin_amdgcn_raw_buffer_store_b32(v, rs, voff, soff, SC1); }

constexpr unsigned kSpinCap = 1u << 16;
__device__ __forceinline__ bool spin_fail(unsigned& spins, rsrc_t rs, u32x2 T) {
  asm volatile("" ::: "memory");
  ++spins;
  if (spins > kSpinCap) { st8(T, rs, 0, F_TMO); return true; }
  if ((spins & 255u) == 0u) {
    const u32x2 v = ld8(rs, 0, F_TMO);
    if (v[0] == T[0] && v[1] == T[1]) return true;
  }
  __builtin_amdgcn_s_sleep(3);
  return false;
}
__device__ __forceinline__ void poll_flags2(rsrc_t rs, unsigned voff, u32x2 T) {
  for (unsigned spins = 0;;) {
    const u32x4 v = ld16(rs, voff, 0);
    if (__all(v[0] == T[0] && v[1] == T[1] && v[2] == T[0] && v[3] == T[1])) break;
    if (spin_fail(spins, rs, T)) break;
  }
  asm volatile("" ::: "memory");
  __builtin_amdgcn_fence(__ATOMIC_ACQUIRE, "wavefront");
}
__device__ __forceinline__ void poll_flags(rsrc_t rs, unsigned voff, unsigned soff, u32x2 T) {
  for (unsigned spins = 0;;) {
    const u32x2 v = ld8(rs, voff, soff);
    if (__all(v[0] == T[0] && v[1] == T[1])) break;
    if (spin_fail(spins, rs, T)) break;
  }
  asm volatile("" ::: "memory");
  __builtin_amdgcn_fence(__ATOMIC_ACQUIRE, "wavefront");
}

__device__ __forceinline__ void unpack8v(const u32x4 u, f32x2* v) {
#pragma unroll
  for (int j = 0; j < 4; ++j) v[j] = f32x2{lo16(u[j]), hi16(u[j])};
}
__device__ __forceinline__ u32x4 pack8v(const f32x2* v) {
  u32x4 o;
#pragma unroll
  for (int j = 0; j < 4; ++j) o[j] = pack2(v[j][0], v[j][1]);
  return o;
}
__device__ __forceinline__ float ln_rd(float var) { return __builtin_amdgcn_rcpf(rbf1(__builtin_amdgcn_sqrtf(rbf1(rbf1(var) + 1e-5f)))); }
__device__ __forceinline__ float hadd(f32x2 v) {
  float a = v[0];
  asm volatile("" : "+v"(a));
  return a + v[1];
}
__device__ __forceinline__ float add_g(float v) { return add_x32(add_x16(v)); }
__device__ __forceinline__ void add_g3(float& x, float& y, float& z) {
  auto a = __builtin_amdgcn_permlane16_swap(__builtin_bit_cast(unsigned, x), __builtin_bit_cast(unsigned, y), false, false);
  auto b = __builtin_amdgcn_permlane16_swap(__builtin_bit_cast(unsigned, z), __builtin_bit_cast(unsigned, z), false, false);
  const float r = asf(a[0]) + asf(a[1]), t = asf(b[0]) + asf(b[1]);
  auto c = __builtin_amdgcn_permlane32_swap(__builtin_bit_cast(unsigned, r), __builtin_bit_cast(unsigned, r), false, false);
  auto d = __builtin_amdgcn_permlane32_swap(__builtin_bit_cast(unsigned, t), __builtin_bit_cast(unsigned, t), false, false);
  const float u = asf(c[0]) + asf(c[1]);
  z = asf(d[0]) + asf(d[1]);
  auto e = __builtin_amdgcn_permlane16_swap(__builtin_bit_cast(unsigned, u), __builtin_bit_cast(unsigned, u), false, false);
  x = asf(e[0]); y = asf(e[1]);
}
__device__ __forceinline__ u32x4 trade16(u32x4 v) {
  auto a = __builtin_amdgcn_permlane16_swap(v[0], v[2], false, false);
  auto b = __builtin_amdgcn_permlane16_swap(v[1], v[3], false, false);
  return u32x4{(unsigned)a[0], (unsigned)b[0], (unsigned)a[1], (unsigned)b[1]};
}
__device__ __forceinline__ unsigned umax4(u32x4 v) { return max(max(v[0], v[1]), max(v[2], v[3])); }
__device__ __forceinline__ u32x2 unpoison2(u32x2 v) { return u32x2{min(v[0], POISON - 1), min(v[1], POISON - 1)}; }
__device__ __forceinline__ u32x4 unpoison4(u32x4 v) {
  return u32x4{min(v[0], POISON - 1), min(v[1], POISON - 1), min(v[2], POISON - 1), min(v[3], POISON - 1)};
}

typedef union { struct { s16x4 a, b; } hf; u32x4 v; } frag_t;
__device__ __forceinline__ s16x4 trread(const char* smem, unsigned off) {
  return __builtin_amdgcn_ds_read_tr16_b64_v4i16((LDS_AS s16x4*)(smem + off));
}
__device__ __forceinline__ s16x4 trabs(unsigned addr) { return __builtin_amdgcn_ds_read_tr16_b64_v4i16((LDS_AS s16x4*)(size_t)addr); }

constexpr int WA = 6;
constexpr int WB = 4, WR = 3;
#define CH 4, 7, 8, 13, 15, 20, 22, 27, 29
constexpr int CHV[9] = {CH};
constexpr int FSL = CHV[0], FS0 = CHV[1], FS0D = CHV[2], FS1 = CHV[3], FS1S = CHV[4];
constexpr int FXA = CHV[5], FXB = CHV[6], FXL = CHV[7], FXS = CHV[8];
constexpr int FXT = FXB >> 2;
constexpr int CSD = 3, POI = 4, RWS = 5, EIN = 8;
constexpr int ERQ = 16, CSL = 19, CSS = 22;
#define IMV 24
constexpr int IMG = IMV, KP0 = 26;
constexpr int PB0 = 10, PB1 = 24;
constexpr int ABL = 23, ABS = 27;
constexpr int XU = 3, XL = 2;
constexpr int x_slot(int u) { return u % XU == 0 ? 0 : u % XU == 1 ? 32768 : 49152; }
#define TCH 19, 7, 4, 27
constexpr int TCV[4] = {TCH}, TS0 = TCV[0], TSN = TCV[1], TW = TCV[2], TST = TCV[3];
constexpr int QL = 26;
constexpr int RXCD = 5;
constexpr int WDS = 17 - WB;
#define SLP 16, 24, 14, 42, 3, 4, 12
constexpr int SLV[7] = {SLP}, SDI0 = SLV[0], SDIR = SLV[1], GSL = SLV[2], RSL = SLV[3], KSL = SLV[4], XSL = SLV[5], OSL = SLV[6];
constexpr bool tile_piece(int s) { return s >= 3 && ((s - 3) >> 2) + 3 < 8 && ((s - 3) & 3) < 3; }
constexpr int a_req(int s, int n) {
  if (!(n & 1)) return -1;
  if (s <= 1) return 8 + 3 * s + (n >> 1);
  return n < 5 && s + 5 < 32 ? 2 * (s + 5) + (n >> 1) : -1;
}
constexpr int vm_n(int it, int s, int n) {
  int c = 0;
  if (n == 0) c += tile_piece(s);
  if (n == 4) c += (s == ABL) + (it ? (s == FSL ? 5 : 0) : (s == ERQ) + (s >= IMG && s < IMG + 3));
  if (!it) c += (s < 4 && n == 2) + (s == 0 && (n == 0 || n == 4)) + ((n == 3 || n == 5) && s + XL + XU < 32);
  else c += (a_req(s, n) >= 0) + ((s == 0 || s == 4) && n == 2);
  return c;
}
constexpr int vm_at(int it, int s, int n) {
  int c = it ? 8 : 14;
  for (int u = 0; u < 6 * s + n; ++u) c += vm_n(it, u / 6, u % 6);
  return c;
}
constexpr int vm_a(int it, int k) {
  if (k < 4) return 2 * k + 2;
  for (int u = 0; u < 192; ++u)
    if (a_req(u / 6, u % 6) == 2 * k + 1) return vm_at(it, (u + 1) / 6, (u + 1) % 6);
  return 0;
}
constexpr int vm_tile(int it, int t) { return t >= 3 ? vm_at(it, 4 * t - 7, 1) : t == 1 ? vm_at(0, 0, 5) : vm_at(0, 3, 3); }
constexpr int vm_x(int u) { return u == 2 ? 9 : u < XU + XL ? 12 + 2 * (u - XU) : vm_at(0, u - XU - XL + 1, 0); }

constexpr int wo_k(int it, int s) { return it || s < 4 ? 15 - s : s < 8 ? 7 - s : 19 - s; }
constexpr int wo_pos(int it, int s) { return 2 * s + 2 + (s >= WR ? 2 * it : 0); }
constexpr int wo_at(int it, int s) { return 2 * it + (s < WB - WR ? 2 * WR + 2 + 4 * s : s < 16 - WB ? 2 * (2 * WB - WR) + 2 * (s - WB + WR) : 32); }
template <int OFF> __device__ __forceinline__ void lds_count(unsigned ad) {
  asm volatile("s_mov_b64 exec, 1\n\tds_add_u32 %0, %1 offset:%2\n\ts_mov_b64 exec, -1" ::"v"(ad), "v"(1u), "n"(OFF) : "memory");
}
__device__ __forceinline__ void lds_look(char* c, unsigned want) {
  while ((unsigned)__builtin_amdgcn_readfirstlane(__hip_atomic_load((LDS_AS unsigned*)c, __ATOMIC_RELAXED, __HIP_MEMORY_SCOPE_WORKGROUP)) < want) {}
  asm volatile("" ::: "memory");
}
__device__ __forceinline__ void pin1(unsigned& a) { asm volatile("" : "+v"(a)); }
__device__ __forceinline__ void pin2(u32x4& a, u32x4& b) { asm volatile("" : "+v"(a), "+v"(b)::"memory"); }
__device__ __forceinline__ void pin2s(float& a, float& b) { asm volatile("" : "+v"(a), "+v"(b)); }
template <int OFF> __device__ __forceinline__ void lds2(u32x4& a, u32x4& b, unsigned ad) {
  asm volatile("ds_read_b128 %0, %2 offset:%3\n\tds_read_b128 %1, %2 offset:%4" : "=&v"(a), "=&v"(b) : "v"(ad), "n"(OFF), "n"(OFF + 1024) : "memory");
}
template <int N> __device__ __forceinline__ void lds_wait(u32x4& a, u32x4& b) { asm volatile("s_waitcnt lgkmcnt(%2)" : "+v"(a), "+v"(b) : "n"(N) : "memory"); }
__device__ __forceinline__ void pin2f(f32x2& a, f32x2& b) { asm volatile("" : "+v"(a), "+v"(b)); }
__device__ __forceinline__ void pin_ac(f32x4 (&ac)[2][4]) {
  asm volatile("" : "+{v[176:179]}"(ac[0][0]), "+{v[180:183]}"(ac[0][1]), "+{v[184:187]}"(ac[0][2]), "+{v[188:191]}"(ac[0][3]),
                    "+{v[192:195]}"(ac[1][0]), "+{v[196:199]}"(ac[1][1]), "+{v[200:203]}"(ac[1][2]), "+{v[204:207]}"(ac[1][3]));
}

constexpr int L_XC = LDS_BYTES - 64, L_SKEY = 32704, L_SRED = 4096, L_SFIN = 20480;
__device__ __forceinline__ unsigned key_load(rsrc_t rs, int l, int L, int rep) { return ld4(rs, (l & 15) * 4, OFF_KEY + (L * 16 + rep) * 128); }
__device__ __forceinline__ unsigned umax16(unsigned k) {
  k = max(k, dppu<0xB1>(k));
  k = max(k, dppu<0x4E>(k));
  k = max(k, dppu<0x141>(k));
  return max(k, dppu<0x140>(k));
}
__device__ __forceinline__ unsigned key_row(unsigned key) { return (0xffffu - (key & 0xffffu)) & 2047u; }
__device__ __forceinline__ unsigned key_wait(rsrc_t rs, u32x2 T, int l, int L, int rep, unsigned kv) {
  if (!__all(kv != POISON)) {
    unsigned spins = 0;
    do {
      if (spin_fail(spins, rs, T)) break;
      if (L == 0) __builtin_amdgcn_s_sleep(KSL);
      kv = key_load(rs, l, L, rep);
    } while (!__all(kv != POISON));
  }
  return __builtin_amdgcn_readfirstlane(umax16(kv));
}
__device__ __forceinline__ unsigned ffn_xoff(unsigned key, int L, int w) { return OFF_CA + L * 32768 + (key_row(key) >> 7) * 2048 + w * 256; }
__device__ __forceinline__ void row_load(rsrc_t rs, int g, unsigned xo, u32x4 (&xv)[4]) {
  ld16h<0>(xv[0], rs, g * 16, xo); ld16h<64>(xv[1], rs, g * 16, xo); ld16h<128>(xv[2], rs, g * 16, xo); ld16h<192>(xv[3], rs, g * 16, xo);
}
template <int SL = 0> __device__ __forceinline__ void ffn_xwait(rsrc_t rs, u32x2 T, unsigned xo, int l, u32x4 (&xv)[4]) {
#define XV_OK() __all(max(max(umax4(xv[0]), umax4(xv[1])), max(umax4(xv[2]), umax4(xv[3]))) != POISON)
  if (!XV_OK()) {
    unsigned spins = 0;
    do {
      if (spin_fail(spins, rs, T)) break;
      if (SL) __builtin_amdgcn_s_sleep(SL);
#pragma unroll
      for (int i = 0; i < 4; ++i) xv[i] = ld16(rs, (l >> 4) * 16 + i * 64, xo);
    } while (!XV_OK());
  }
#undef XV_OK
}
__device__ __forceinline__ void ffn_gu(const u32x4 (&gv)[4], const u32x4 (&uv)[4], const u32x4 (&xv)[4], f32x4& ga, f32x4& ua) {
  ga = mfma16(gv[0], xv[0], f32x4{0.f, 0.f, 0.f, 0.f}); ua = mfma16(uv[0], xv[0], f32x4{0.f, 0.f, 0.f, 0.f});
#pragma unroll
  for (int i = 1; i < 4; ++i) { ga = mfma16(gv[i], xv[i], ga); ua = mfma16(uv[i], xv[i], ua); }
}
__device__ __forceinline__ void ffn_gu_check(rsrc_t rs, u32x2 T, unsigned xo, int l, const u32x4 (&gv)[4], const u32x4 (&uv)[4], u32x4 (&xv)[4], f32x4& ga, f32x4& ua) {
  if (__builtin_expect(__any(ga[0] != ga[0]), 0)) {
    ffn_xwait(rs, T, xo, l, xv);
    ffn_gu(gv, uv, xv, ga, ua);
  }
}
__device__ __forceinline__ void ffn_gu_store(char* smem, int l, int w, f32x4 ga, f32x4 ua) {
  LDS_AS f32x4* sred = (LDS_AS f32x4*)(smem + L_SRED);
  if ((l & 15) == 0) { sred[w * 8 + (l >> 4)] = ga; sred[w * 8 + 4 + (l >> 4)] = ua; }
}
__device__ __forceinline__ f32x4 ffn_t_read(char* smem, int l) { return ((const LDS_AS f32x4*)(smem + L_SRED))[(l & 7) * 8 + ((l >> 3) & 1) * 4 + (l >> 4)]; }
__device__ __forceinline__ void ffn_d_read(char* smem, int l, int blk, s16x4 (&dfr)[8]) {
#pragma unroll
  for (int i = 0; i < 8; ++i) dfr[i] = trread(smem, L_DN + blk * 4096 + (l >> 4) * 1024 + ((l & 15) >> 2) * 256 + i * 32 + (l & 3) * 8);
}
__device__ __forceinline__ float ffn_d_sum(float t) { t += dppf<0xB1>(t); t += dppf<0x4E>(t); return t + dppf<0x141>(t); }
__device__ __forceinline__ u32x2 ffn_h(f32x4 t, int l) {
#pragma unroll
  for (int e = 0; e < 4; ++e) t[e] = ffn_d_sum(t[e]);
  const float a = (l & 1) ? t[1] : t[0], b = (l & 1) ? t[3] : t[2], s = (l & 2) ? b : a;
  const float gg = rbf1(s), uu = rbf1(dppf<0x128>(s));
  const float h = rbf1(gg * __builtin_amdgcn_rcpf(1.0f + __expf(-gg))) * uu;
  const unsigned h0 = pack2(h, dppf<0x55>(h)), h1 = pack2(dppf<0xAA>(h), dppf<0xFF>(h));
  return (l & 15) == 0 ? u32x2{h0, h1} : u32x2{0u, 0u};
}
__device__ __forceinline__ void ffn_d_mma(const s16x4 (&dfr)[8], u32x2& hh, f32x4& ya, f32x4& yb) {
#pragma unroll
  for (int i = 0; i < 8; ++i) {
    if (i & 1) yb = mfma16k16(dfr[i], __builtin_bit_cast(s16x4, hh), yb);
    else ya = mfma16k16(dfr[i], __builtin_bit_cast(s16x4, hh), ya);
    hh = u32x2{dppu<0x111>(hh[0]), dppu<0x111>(hh[1])};
  }
}
__device__ __forceinline__ void ffn_d_store(rsrc_t rs, int l, int G0, int L, int slice, f32x4 y) {
  const unsigned c = l & 15;
  st16(unpoison4(__builtin_bit_cast(u32x4, y)), rs, (G0 + (c >> 1)) * 32768 + slice * 128 + (c & 1) * 64 + (l >> 4) * 16, OFF_PART + L * (1u << 20));
}
__device__ __forceinline__ void red_load(rsrc_t rs, int tid, int L, unsigned G, unsigned key, u32x4 (&pv)[4], u32x2& cxn, u32x2& cx1) {
#pragma unroll
  for (int k = 0; k < 4; ++k) pv[k] = ld16(rs, min(tid + 512 * k, 1791) * 16, OFF_PART + L * (1u << 20) + G * 32768);
  cxn = ld8(rs, (tid & 3) * 16 + (tid & 4) * 2, OFF_CA + L * 32768 + (key_row(key) >> 7) * 2048 + G * 64);
  cx1 = ld8(rs, (tid & 3) * 16 + (tid & 4) * 2, OFF_CB + L * 32768 + (key_row(key) >> 7) * 2048 + G * 64);
}
__device__ __forceinline__ void red_sum(rsrc_t rs, char* smem, u32x2 T, int tid, int w, int L, unsigned G, unsigned key,
                                        u32x4 (&pv)[4], u32x2& cxn, u32x2& cx1) {
#define PV_OK() __all(max(max(max(umax4(pv[0]), umax4(pv[1])), max(umax4(pv[2]), umax4(pv[3]))), max(max(cxn[0], cxn[1]), max(cx1[0], cx1[1]))) != POISON)
  if (!PV_OK()) {
    unsigned spins = 0;
    do {
      if (spin_fail(spins, rs, T)) break;
      red_load(rs, tid, L, G, key, pv, cxn, cx1);
    } while (!PV_OK());
  }
#undef PV_OK
  f32x4 s = (__builtin_bit_cast(f32x4, pv[0]) + __builtin_bit_cast(f32x4, pv[1])) + __builtin_bit_cast(f32x4, pv[2]);
  if (w < 4) s += __builtin_bit_cast(f32x4, pv[3]);
  ((LDS_AS f32x4*)(smem + L_SFIN))[tid] = s;
}
template <int NW> __device__ __forceinline__ u32x2 red_final(char* smem, int l, unsigned key, u32x2 cxn, u32x2 cx1) {
  const LDS_AS f32x4* sf = (const LDS_AS f32x4*)(smem + L_SFIN) + l;
  f32x4 s = (sf[0] + sf[64]) + (sf[128] + sf[192]);
  if constexpr (NW == 8) s += (sf[256] + sf[320]) + (sf[384] + sf[448]);
#pragma unroll
  for (int j = 0; j < 4; ++j) s[j] = sum_hi8(s[j]);
  const unsigned k16 = key >> 16;
  const float rmax = __builtin_bit_cast(float, ((k16 & 0x8000u) ? (k16 & 0x7fffu) : (~k16 & 0xffffu)) << 16);
  const float sig = rbf1(__builtin_amdgcn_rcpf(1.0f + __expf(-rmax)));
  const float xn4[4] = {lo16(cxn[0]), hi16(cxn[0]), lo16(cxn[1]), hi16(cxn[1])};
  const float x14[4] = {lo16(cx1[0]), hi16(cx1[0]), lo16(cx1[1]), hi16(cx1[1])};
  float o[4];
#pragma unroll
  for (int j = 0; j < 4; ++j) o[j] = x14[j] + rbf1(xn4[j] + rbf1(rbf1(s[j]) * sig));
  return u32x2{pack2(o[0], o[1]), pack2(o[2], o[3])};
}

struct RowFin { float mean, var, mb, rd; unsigned key; };
__device__ __forceinline__ void fin_wait(rsrc_t rs, u32x2 T, unsigned voff, unsigned so, u32x4 (&sp)[4]) {
#define ST_OK() __all(max(max(umax4(sp[0]), umax4(sp[1])), max(umax4(sp[2]), umax4(sp[3]))) != POISON)
  if (!ST_OK()) {
    unsigned spins = 0;
    do {
      if (spin_fail(spins, rs, T)) break;
#pragma unroll
      for (int i = 0; i < 4; ++i) sp[i] = ld16(rs, voff + i * 2048, so);
    } while (!ST_OK());
  }
#undef ST_OK
}
__device__ __forceinline__ RowFin fin_key(rsrc_t rs, u32x2 T, unsigned voff, unsigned so, char* skey, int l, int w, unsigned R, float wsum, u32x4 (&sp)[4]) {
  RowFin r;
#define ST_SUM() ((__builtin_bit_cast(f32x4, sp[0]) + __builtin_bit_cast(f32x4, sp[1])) + (__builtin_bit_cast(f32x4, sp[2]) + __builtin_bit_cast(f32x4, sp[3])))
  f32x4 sf = ST_SUM();
  const float z = (sf[0] + sf[1]) + sf[2];
  if (__any(z != z)) {
    fin_wait(rs, T, voff, so, sp);
    sf = ST_SUM();
  }
#undef ST_SUM
  float s0 = sf[0], s1 = sf[1], s2 = sf[2];
  add_g3(s0, s1, s2);
  r.mean = s0 * (1.0f / 1024.0f);
  r.var = fmaxf(__builtin_fmaf(-r.mean, r.mean, s1 * (1.0f / 1024.0f)), 0.f);
  r.mb = rbf1(r.mean); r.rd = ln_rd(r.var);
  const unsigned rb16 = pack2((s2 - r.mb * wsum) * r.rd, 0.f) & 0xffffu;
  r.key = min((((rb16 & 0x8000u) ? (~rb16 & 0xffffu) : (rb16 | 0x8000u)) << 16) | (0xffffu - R), POISON - 1);
  const unsigned wk = umax16(r.key);
  if (l == 0) ((LDS_AS unsigned*)skey)[w] = wk;
  return r;
}
__device__ __forceinline__ unsigned fin_cand(rsrc_t rs, char* skey, int L, int g, int rb, int ct, const RowFin& r, const f32x2 (&v)[8]) {
  const u32x4 k0 = *(const LDS_AS u32x4*)skey, k1 = *(const LDS_AS u32x4*)(skey + 16);
  const unsigned best = max(umax4(k0), umax4(k1));
  if (r.key == best) {
    const unsigned co = L * 32768 + rb * 2048 + ct * 128 + g * 16;
    f32x2 t[8], rd2 = {r.rd, r.rd}, of2 = {-r.mb * r.rd, -r.mb * r.rd};
    asm volatile("" : "+v"(rd2), "+v"(of2));
#pragma unroll
    for (int j = 0; j < 8; ++j) t[j] = __builtin_elementwise_fma(v[j], rd2, of2);
    st16(unpoison4(pack8v(t)), rs, co, OFF_CA); st16(unpoison4(pack8v(t + 4)), rs, co + 64, OFF_CA);
    if (g == 0) st4(best, rs, OFF_KEY + (L * 16 + ct) * 128 + rb * 4, 0);
    st16(unpoison4(pack8v(v)), rs, co, OFF_CB); st16(unpoison4(pack8v(v + 4)), rs, co + 64, OFF_CB);
  }
  return best;
}
__device__ __forceinline__ void fin_x2(const RowFin& r, f32x2 (&v)[8]) {
  f32x2 sc2 = {1.0f + r.rd, 1.0f + r.rd}, of2 = {-r.mb * r.rd, -r.mb * r.rd};
  asm volatile("" : "+v"(sc2), "+v"(of2));
#pragma unroll
  for (int j = 0; j < 8; ++j) v[j] = __builtin_elementwise_fma(v[j], sc2, of2);
}

extern "C" __device__ unsigned long long dispatch_id() __asm("llvm.amdgcn.dispatch.id");

extern "C" __global__ __launch_bounds__(512, 2) void mega_kernel(const bf16* x, char* ws, const bf16* wqkv, const bf16* wo, const bf16* wr,
                                                                 const bf16* wg, const bf16* wu, const bf16* wd, bf16* out) {
  extern __shared__ __attribute__((aligned(16))) char smem[];
  const int w0_ = __builtin_amdgcn_readfirstlane(threadIdx.x >> 6);
  typedef unsigned long long u64x2 __attribute__((ext_vector_type(2)));
  u64x2 a01;
  unsigned long long a2;
  asm volatile("s_load_dwordx4 %0, %2, 0\n\ts_load_dwordx2 %1, %2, 16" : "=&s"(a01), "=&s"(a2) : "s"(__builtin_amdgcn_kernarg_segment_ptr()));
  const unsigned lbase = (unsigned)(size_t)(LDS_AS char*)smem;
  {
    unsigned long long pc;
    unsigned len;
    asm volatile("s_getpc_b64 %0\n\ts_mov_b32 %1, .Ltext_end-mega_kernel\n\t.subsection 1\n.Ltext_end:\n\t.subsection 0" : "=s"(pc), "=s"(len));
    const unsigned o = ((blockIdx.x >> 3) * 64 + (threadIdx.x & 63)) * 128;
    if (w0_ == 7 && o < len)
      asm volatile("s_mov_b32 m0, %0\n\ts_nop 0\n\tglobal_load_lds_dword %1, %2" ::"s"(lbase + L_TOUCH0), "v"(o), "s"(pc & ~127ull) : "memory");
  }
#define HGRP() ((xcd & 2) + (xcd >> 2))
#define IDS()                                                                                         \
  int tid = threadIdx.x, w = w0_, bid = blockIdx.x;                                                   \
  asm volatile("" : "+v"(tid));                                                                       \
  asm volatile("" : "+s"(w));                                                                         \
  asm volatile("" : "+s"(bid));                                                                       \
  const int l = tid & 63, xcd = bid & 7, bi = bid >> 3;                                               \
  const int p = (xcd & 1) * 4 + (bi & 3), h = HGRP() * 8 + (bi >> 2);         \
  const int rb = 2 * xcd + (bi & 1), ct = bi >> 1;                               \
  const int slice = (xcd - (xcd > RXCD)) * 32 + bi;                                            \
  const bool ffn_on = xcd != RXCD;                                                                    \
  const int g = l >> 4;                                                                               \
  const int rt = w < 4 ? w : 11 - w;          \
  KARGS();                                                                                            \
  (void)p; (void)h; (void)rb; (void)ct; (void)slice; (void)ffn_on; (void)g; (void)rt;
#define PROT() (2 * (p & 3) + (p >> 2))
  typedef const void* const __attribute__((address_space(4)))* karg_t;
#define KARGS() karg_t ka = (karg_t)__builtin_amdgcn_kernarg_segment_ptr(); asm volatile("" : "+s"(ka))
#define ARGP(name, i) const GAS bf16* name = (const GAS bf16*)ka[i]; asm volatile("" : "+s"(name))
#define RSRC(ptr, bytes) __builtin_amdgcn_make_buffer_rsrc((void*)(ptr), 0, bytes, 0x00020000)
#define rwq RSRC(pwq, 1024 * 3072 * 2)
#define rwo RSRC(pwo, 1024 * 1024 * 2)
#define rwg RSRC(pwg, 1024 * F_ * 2)
#define rwu RSRC(pwu, 1024 * F_ * 2)
#define rwd RSRC(pwd, 1024 * F_ * 2)
#define rout RSRC(pout, 2048 * 1024 * 2)

#define WQ_VOFF() ((l >> 2) * 6144 + (((l & 3) ^ ((((l >> 4) ^ (l >> 5)) & 1) << 1)) << 4))
#define WQ_KMAJ(q) (((q) % 3) * 8 + (q) / 3)
#define WQ_PIECE(t, j, dst) dma16(rwq, wqv, ((((t) + rot) & 7) * 128 + ((j) & 7) * 16) * 6144 + ((j) >> 3) * 2048 + h * 64, lbase + (dst) + (j) * 1024)
#define WQ_BOFF(dt) ((g >> 1) * 1024 + (8 * (g & 1) + ((l & 15) >> 2)) * 64 + (l & 3) * 8 + (((dt) ^ (g & 1)) << 5))
#define X_REQ(u, hf) dma16(rx, xv[hf], (p * 256 + rt * 32 + ((u) & 1) * 16 + (hf) * 8) * 2048 + ((((u) >> 2) + rot) & 7) * 256 + (((u) >> 1) & 1) * 128, lbase + rt * 2048 + x_slot(u) + (hf) * 1024)
#define WO_PIECE(j) dma16(rwo, wov, ((w >> 2) * 64 + (w & 3) + 4 * (j)) * 16384 + ct * 128, lbase + 32768 + (j) * 8192 + w * 1024)
#define WD_DMA_OF(v) _Pragma("unroll") for (int i = 0; i < 4; ++i)                                  \
      dma16(rwd, (l >> 4) * 2048 + (l & 15) * 16, (unsigned)(slice * 16 + 4 * i) * 2048 + (v) * 256, lbase + L_DN + (v) * 4096 + i * 1024)
#define FFN_DMA(m) dma16((m) & 1 ? rwu : rwg, fnv, (unsigned)(((4 * w + ((m) >> 1)) * 32) * F_ + slice * 16) * 2, lbase + 65536 + (m) * 8192 + w * 1024)
#define POISON_BIG(L)                                                                                \
  if (w >= 2) {                                                                                      \
    unsigned pz1 = POISON;                                                                           \
    asm volatile("" : "+v"(pz1));                                                                    \
    st16(u32x4{pz1, pz1, pz1, pz1}, rs, (w >= 4 ? OFF_PART + (L) * (1u << 20) + bid * 4096 - 4096 : OFF_STAT + (L) * 524288 + (p * 32 + h) * 2048 - 2048) + tid * 16, 0); \
  }
  u32x4 gv[4], uv[4];
  u32x4 keep[9];
  u32x4 pre[4][2];
#define PRE_LOAD(it_, rot_, s0, s1)                                                                  \
  static_for<(s1) - (s0)>([&](auto c_) __attribute__((always_inline)) {                              \
    constexpr int s = (s0) + decltype(c_)::value;                                                    \
    const unsigned ab_ = OFF_X1 + (((rot_) + (s >> 2)) & 7) * 8192 + ((s >> 1) & 1) * 4096; \
    ld16w<2 * s, (s & 1) * 2048>(pre[s][0], rs, l * 16, ab_ + (p * 8 + rt) * 65536);                         \
    ld16w<2 * s + 1, (s & 1) * 2048 + 1024>(pre[s][1], rs, l * 16, ab_ + (p * 8 + rt) * 65536);                 \
  })
  u32x2 T;
  const GAS bf16* tsrc;
  unsigned xv[2], xa[2];
  u32x2 ep;
  u32x4 rows0[4];
#define X_READ(u, d0, d1) { d0 = *(const LDS_AS u32x4*)(size_t)(xa[0] + x_slot(u)); d1 = *(const LDS_AS u32x4*)(size_t)(xa[1] + x_slot(u)); }
  unsigned g1[5], wqv0;
  {
    IDS();
    const int rot = PROT(), qa = WQ_KMAJ(w), qb = WQ_KMAJ(8 + w);
    wqv0 = WQ_VOFF();
    xv[0] = (l >> 3) * 2048 + (((l & 7) ^ (l >> 4)) << 4); xv[1] = xv[0] ^ 64;
    g1[0] = (rot * 128 + (qa & 7) * 16) * 6144 + (qa >> 3) * 2048 + h * 64; g1[1] = lbase + L_RING + qa * 1024;
    g1[2] = (rot * 128 + (qb & 7) * 16) * 6144 + (qb >> 3) * 2048 + h * 64; g1[3] = lbase + L_RING + qb * 1024;
    g1[4] = (p * 256 + rt * 32) * 2048 + rot * 256;
    asm volatile("" : "+v"(wqv0), "+v"(xv[0]), "+v"(xv[1]));
    asm volatile("s_waitcnt lgkmcnt(0)" : "+s"(a01), "+s"(a2), "+s"(g1[0]), "+s"(g1[1]), "+s"(g1[2]), "+s"(g1[3]), "+s"(g1[4]));
  }
  x = (const bf16*)a01[0]; ws = (char*)a01[1]; wqkv = (const bf16*)a2;
  const rsrc_t rs = __builtin_amdgcn_make_buffer_rsrc((void*)ws, 0, WS_BYTES, 0x00020000);
  const rsrc_t rx = RSRC(x, 2048 * 1024 * 2);
  {
    IDS();
    const int rot = PROT();
    const bf16* pwq = wqkv;
    const unsigned wqv = wqv0;
    dma16(rwq, wqv, g1[0], g1[1]);
#pragma unroll
    for (int i = 0; i < 4; ++i) dma16(rx, xv[i & 1], g1[4] + i * 16384, lbase + rt * 2048 + x_slot(i >> 1) + (i & 1) * 1024);
    dma16(rwq, wqv, g1[2], g1[3]);
    asm volatile("s_barrier" ::: "memory");
    WQ_PIECE(0, WQ_KMAJ(16 + w), L_RING);
    if (w == 6 && bi == 0) {
      ARGP(pwr, 4);
      asm volatile("s_mov_b32 m0, %0\n\ts_nop 0\n\tbuffer_load_dword %1, %2, 0 offen lds" ::"s"(lbase + L_TOUCH0 + 256), "v"((l & 15) * 128), "s"(RSRC(pwr, 2048)) : "memory");
    }
    X_REQ(2, 0); X_REQ(2, 1);
    asm volatile("buffer_load_dwordx2 %0, off, %1, 0 sc1" : "=v"(ep) : "s"(rs) : "memory");
    asm volatile("" : "=v"(rows0[0]), "=v"(rows0[1]), "=v"(rows0[2]), "=v"(rows0[3]));
    if (w < 4) ldrows(rows0, rx, (l >> 5) * 2048 + (l & 31) * 16, (p * 256 + h * 8 + 2 * w) * 2048);
    {
      ARGP(pwo, 3); ARGP(pwg, 5); ARGP(pwu, 6);
      tsrc = (w < 4 ? pwg : pwu) + ((bi * 32 + ((tid & 255) >> 3)) * F_ + (slice >> 5) * 512 + (tid & 7) * 64);
      if (!ffn_on) tsrc = pwo + (bi * 512 + tid) * 64;
      asm volatile("" : "+v"(tsrc));
    }
#pragma unroll
    for (int kk = 0; kk < 2; ++kk) {
      const int r = l & 15;
      xa[kk] = lbase + rt * 2048 + (r >> 3) * 1024 + (r & 7) * 128 + (((4 * kk + g) ^ (r >> 1)) << 4);
      asm volatile("" : "+v"(xa[kk]));
    }
    if (tid == 192) *(LDS_AS u32x4*)(smem + L_QC) = u32x4{0u, 0u, 0u, 0u};
    if (w < 4) wait_vm<8>(); else wait_vm<4>();
    X_READ(0, pre[0][0], pre[1][0]); X_READ(1, pre[0][1], pre[1][1]);
    asm volatile("s_waitcnt lgkmcnt(0)\n\ts_barrier" : "+v"(pre[0][0]), "+v"(pre[0][1]), "+v"(pre[1][0]), "+v"(pre[1][1])::"memory");
    X_REQ(3, 0); X_REQ(3, 1); X_REQ(4, 0); X_REQ(4, 1);
  }
  unsigned sel0 = 0x7FFFFFFFu;
  u32x4 x2p[2];
  unsigned xm0 = 0, kg0 = 0;

  auto layer = [&](auto it_c) __attribute__((always_inline)) {
    constexpr int it = decltype(it_c)::value;
    f32x4 acc[2][6];
    f32x2 qs2[2];
    u32x4 k0, k1, v0, v1;
    unsigned tdum = 0;
    {
      IDS();
      bool aff = false;
      unsigned aq[2];
      u32x4 av[2];
#define PIN_ACC() asm volatile("" : "+{v[176:179]}"(acc[0][0]), "+{v[180:183]}"(acc[0][1]), "+{v[184:187]}"(acc[0][2]), "+{v[188:191]}"(acc[0][3]), "+{v[192:195]}"(acc[0][4]), "+{v[196:199]}"(acc[0][5]), \
                               "+{v[200:203]}"(acc[1][0]), "+{v[204:207]}"(acc[1][1]), "+{v[208:211]}"(acc[1][2]), "+{v[212:215]}"(acc[1][3]), "+{v[216:219]}"(acc[1][4]), "+{v[220:223]}"(acc[1][5]))
#define PINV4(x) asm volatile("" : "+v"(x[0]), "+v"(x[1]), "+v"(x[2]), "+v"(x[3])::"memory")
      auto qkv_loop = [&](auto it_c) __attribute__((always_inline)) {
        constexpr int IT = decltype(it_c)::value;
        const unsigned abase = OFF_X1 + (p * 8 + rt) * 65536;
        const int rot = PROT() + 5 * IT;
        unsigned fkey = 0, kv, bq[2];
        u32x4 fx[4], bv[2], af[WA][2];
        u32x2 cxn, cx1;
        f32x4 sa, sb, cs4[2];
        f32x2 b2[2];
#define AB_OFF() (OFF_AB + IT * 16384 + (p * 8 + rt) * 128)
#define A_LOAD1(k, a) ld16w<(k) % WA * 2 + (a), ((k) & 1) * 2048 + (a) * 1024>(af[(k) % WA][a], rs, l * 16, abase + ((((k) >> 2) + rot) & 7) * 8192 + (((k) >> 1) & 1) * 4096)
#pragma unroll
        for (int s = 0; s < 2 + 2 * IT; ++s) { af[s][0] = pre[s][0]; af[s][1] = pre[s][1]; }
        const bf16* pwq = wqkv;
        const int tstep = TS0 + bid % TSN;
        const unsigned wqv = WQ_VOFF();
#define QKV_DMA1(t, i)                                                                            \
  {                                                                                               \
    if constexpr (IT == 0) WQ_PIECE(t, 3 * w + (i), L_RING + SLOT(t) * STAGE);                    \
    else dma16s<(i) * 1024>(rs, l * 16, OFF_WI + (h * 8 + (((t) + rot) & 7)) * STAGE + w * 3072, lbase + L_RING + SLOT(t) * STAGE + w * 3072); \
  }
        unsigned bo[2][2];
#pragma unroll
        for (int dt = 0; dt < 2 - IT; ++dt) {
          bo[dt][0] = lbase + L_RING + (IT ? l * 16 : WQ_BOFF(dt)); bo[dt][1] = bo[dt][0] + 2 * STAGE;
          asm volatile("" : "+v"(bo[dt][0]), "+v"(bo[dt][1]));
        }
#define FRAG_T() (lbase + L_RING + (4 * g + ((l & 15) >> 2)) * 64 + (l & 3) * 8 + (((g ^ (g >> 1)) & 1) << 5))
        auto frag_t2 = [&](unsigned o) __attribute__((always_inline)) {
          frag_t t;
          t.hf.a = trabs(o); t.hf.b = trabs(o + 1024);
          return t.v;
        };
        unsigned ka[3];
        auto tile_sum = [&](u32x4 pt, int dst) __attribute__((always_inline)) {
          f32x4 c = __builtin_bit_cast(f32x4, pt);
#pragma unroll
          for (int e = 0; e < 4; ++e) { c[e] += dppf<0x124>(c[e]); c[e] += dppf<0x128>(c[e]); c[e] = add_x16(c[e]); }
          if ((l & 28) == 0) *(LDS_AS f32x4*)(smem + dst + (w * 2 + (l >> 5)) * 64 + (l & 3) * 16) = c;
        };
        frag_t bfr[6];
#define QKV_BO(s, n) (bo[(n) & 1][SLOT((s) >> 2) >> 1] + (SLOT((s) >> 2) & 1) * STAGE + ((n) >> 1) * 8192 + ((s) & 3) * 2048)
#define QKV_RDA(s, n)                                                                             \
  {                                                                                               \
    if constexpr (IT == 0) bfr[n].hf.a = trabs(QKV_BO(s, n));                                     \
    else bfr[n].v = *(const LDS_AS u32x4*)(size_t)(bo[0][SLOT((s) >> 2) >> 1] + (SLOT((s) >> 2) & 1) * STAGE + (((s) & 3) * 6 + (n)) * 1024); \
  }
#define QKV_RDB(s, n) { if constexpr (IT == 0) bfr[n].hf.b = trabs(QKV_BO(s, (n) ^ 1) + 256); }
#pragma unroll
        for (int n = 0; n < 6; ++n) { QKV_RDA(0, n); if (n < 5) QKV_RDB(0, n); }
#define XF_OFF() (l & 8 ? ((l & 7) * RX + bi % RX) * 256 + h * 8 : (p * RX + (xcd >> 1) % RX) * 256 + (4 * rt + (l & 3)) * 8)
#define XP_OFF() (OFF_XNEW + ((rot + FXT) & 7) * 256)
#define FP_OFF(i) (w == 3 ? OFF_XNEW + (i) * 1024 : OFF_FX + h * 3072 + w * 1024)
#define FX_OK(n) __all(((n) == 4 ? max(max(umax4(fx[0]), umax4(fx[1])), max(umax4(fx[2]), umax4(fx[3]))) : max(umax4(fx[0]), umax4(fx[1]))) != POISON)
#define SINCE(s0) (vm_at(IT, s, n) - vm_at(IT, s0, 5))
#define DEF4(x) asm volatile("" : "=v"(x[0]), "=v"(x[1]), "=v"(x[2]), "=v"(x[3]))
        LDS_AS unsigned* qcnt = (LDS_AS unsigned*)(smem + L_QC);
        unsigned qc;
        unsigned qadr = lbase + L_QC, qone = 1;
        asm volatile("" : "+v"(qadr), "+v"(qone));
#define count_in(i) asm volatile("s_mov_b64 exec, 1\n\tds_add_u32 %0, %1 offset:%2\n\ts_mov_b64 exec, -1" ::"v"(qadr), "v"(qone), "n"(4 * (i)) : "memory")
        auto count_wait = [&](int i, unsigned want) __attribute__((always_inline)) {
          if (__builtin_expect((unsigned)__builtin_amdgcn_readfirstlane(qc) < want, 0)) {
            while ((unsigned)__builtin_amdgcn_readfirstlane(__hip_atomic_load(qcnt + i, __ATOMIC_RELAXED, __HIP_MEMORY_SCOPE_WORKGROUP)) < want) {}
          }
          asm volatile("" ::: "memory");
        };
        unsigned long long xok = xm0;
        auto x1_need = [&](unsigned long long m) __attribute__((always_inline)) {
          if (__builtin_expect((xok & m) != m, 0)) {
            for (unsigned spins = 0;;) {
              const u32x2 f = ld8(rs, F_X1 + ((p * RX + (xcd >> 1) % RX) * 32 + (l & 31)) * 8, 0);
              xok = __builtin_amdgcn_ballot_w64(f[0] == T[0] && f[1] == T[1]);
              if ((xok & m) == m) break;
              if (spin_fail(spins, rs, T)) { xok = ~0ull; break; }
              __builtin_amdgcn_s_sleep(KSL);
            }
            asm volatile("" ::: "memory");
          }
        };
        auto gap = [&](auto sc_, auto nc_) __attribute__((always_inline)) {
          constexpr int s = decltype(sc_)::value, n = decltype(nc_)::value;
          {
            constexpr int ts = (s + 3) / 4, tc = (s + 2) / 4, t0 = 1;
            if constexpr (ts >= 3 && ts < 8 && s == 4 * ts - 3 && n == 2) { wait_vm<vm_at(IT, s, n) - vm_tile(IT, ts)>(); count_in(0); }
            if constexpr (!IT && tc >= t0 && tc < 3 && s == 4 * tc - 2 && n == 1) { wait_vm<vm_at(IT, s, n) - vm_tile(IT, tc)>(); count_in(0); }
            if constexpr (IT && ts >= t0 && ts < 3 && s == 4 * ts - 3 && n == 2) count_in(0);
            if constexpr (tc >= t0 && tc < 8 && s == 4 * tc - 2) {
              if constexpr (n == 3) qc = __hip_atomic_load(qcnt, __ATOMIC_RELAXED, __HIP_MEMORY_SCOPE_WORKGROUP);
              if constexpr (n == 5) count_wait(0, 8 * (tc - t0 + 1));
            }
          }
          if constexpr (n == 0 && tile_piece(s)) QKV_DMA1(((s - 3) >> 2) + 3, (s - 3) & 3);
          if constexpr (IT == 0) {
            if constexpr (s == 0 && !(n & 1)) QKV_DMA1(1, n >> 1);
            if constexpr (s >= 1 && s < 4 && n == 2) QKV_DMA1(2, s - 1);
            constexpr int u = s + XL;
            if constexpr (n == 1 && u < 32) {
              if (s == 0 && w < 4) wait_vm<vm_at(0, s, n) - vm_x(u) + 4>();
              else if (s > TST && w >= TW) wait_vm<vm_at(0, s, n) - vm_x(u) + 1>();
              else wait_vm<vm_at(0, s, n) - vm_x(u)>();
              X_READ(u, af[(u & ~1) % WA][u & 1], af[(u | 1) % WA][u & 1]);
            }
            if constexpr (n == 3 && u + XU < 32) { asm volatile("s_waitcnt lgkmcnt(%0)" ::"n"(s + 1 < QL ? 4 : 3) : "memory"); X_REQ(u + XU, 0); }
            if constexpr (n == 5 && u + XU < 32) X_REQ(u + XU, 1);
            if constexpr (s == PB0 && n == 0) { POISON_BIG(0); }
            if constexpr (s == 0 && n == 1) count_in(2);
            if constexpr (s == 0 && n == 3) qc = __hip_atomic_load(qcnt + 2, __ATOMIC_RELAXED, __HIP_MEMORY_SCOPE_WORKGROUP);
            if constexpr (s == 0 && n == 5) count_wait(2, 8);
            if constexpr (s == 1 && n == 1) {
              asm volatile("" : "+v"(ep)::"memory");
              const unsigned long long e = (((unsigned long long)(unsigned)__builtin_amdgcn_readfirstlane(ep[1]) << 32) | (unsigned)__builtin_amdgcn_readfirstlane(ep[0])) + 1ull + dispatch_id() * 0x9E3779B97F4A7C16ull;
              T = u32x2{(unsigned)e, (unsigned)(e >> 32)};
            }
            if constexpr (s == RWS && n == 0) {
              if (w < 4) {
                PINV4(rows0);
                f32x2 sv[16];
#pragma unroll
                for (int i = 0; i < 4; ++i) unpack8v(rows0[i], sv + 4 * i);
                f32x2 t0 = sv[0] + sv[1], t1 = sv[2] + sv[3];
#pragma unroll
                for (int j = 4; j < 16; j += 2) { t0 += sv[j]; t1 += sv[j + 1]; }
                const float m = sum32(hadd(t0 + t1)) * (1.0f / 1024.0f);
                f32x2 m2 = {m, m}, q0 = {0.f, 0.f}, q1 = {0.f, 0.f};
                asm volatile("" : "+v"(m2));
#pragma unroll
                for (int j = 0; j < 16; j += 2) {
                  const f32x2 d0 = sv[j] - m2, d1 = sv[j + 1] - m2;
                  q0 = __builtin_elementwise_fma(d0, d0, q0); q1 = __builtin_elementwise_fma(d1, d1, q1);
                }
                const float rd = ln_rd(sum32(hadd(q0 + q1)) * (1.0f / 1024.0f));
                if ((l & 30) == 0) st4(__builtin_bit_cast(unsigned, (l & 1) ? -rbf1(m) : rd), rs, (l >> 5) * 4 + (l & 1) * 8192, OFF_AB + (p * 256 + h * 8 + 2 * w) * 4);
              }
            }
            if constexpr (s == POI && n == 4) {
              if (w == 3) {
                unsigned pz1 = POISON;
                asm volatile("" : "+v"(pz1));
                const unsigned prb = 2 * p + (h >> 4), pct = h & 15, m = l - 32;
                static_assert(OFF_CB == OFF_CA + 131072, "poison addressing");
                if (l < 60) st16(u32x4{pz1, pz1, pz1, pz1}, rs, l < 32 ? OFF_CA + (l & 8) * 4096 + (l & 16) * 8192 + prb * 2048 + pct * 128 + (l & 7) * 16
                                                             : l < 56 ? OFF_FX + ((h * 6 + (m >> 2)) * 8 + p) * 64 + (l & 3) * 16
                                                                      : OFF_AB + 16384 + (p * 32 + h) * 32 + (l & 1) * 16 + (l & 2) * 4096, 0);
                if (l < 4) st4(pz1, rs, l < 2 ? OFF_XNEW + bid * 8 + l * 4 : OFF_KEY + ((l & 1) * 16 + pct) * 128 + prb * 4, 0);
              }
            }
            if constexpr (s == POI && n == 2) {
              if (w == 2 && l < 6) {
                unsigned f0 = F_KV + KVF(0, p * 32 + h), f1 = F_O + OFB(0, p) + OFL(h), f2 = F_X1 + ((rb >> 1) * RX * 32 + (l & 3) * 32 + (rb & 1) * 16 + ct) * 8;
                asm volatile("" : "+v"(f0), "+v"(f1), "+v"(f2));
                const unsigned fo = l == 5 ? f0 : l == 4 ? f1 : f2;
                touchb(tdum, rs, fo);
              }
            }
            static_assert(vm_x(EIN + XL) >= vm_at(0, EIN - 3, 5) && IMG >= 1 && CSD >= 1, "first flag");
            if constexpr (s == EIN && n == 1) { if (w < 4) count_in(1); }
            if constexpr (s == EIN + 2 && n == 0) qc = __hip_atomic_load(qcnt + 1, __ATOMIC_RELAXED, __HIP_MEMORY_SCOPE_WORKGROUP);
            if constexpr (s == EIN + 2 && n == 2) {
              if (w == 0) { count_wait(1, 4); if (l < RX) st8(T, rs, F_XN + (p * RX + l) * 256 + h * 8, 0); }
            }
            if constexpr (s == ERQ && n == 4) asm volatile("buffer_load_dwordx2 %0, %1, %2, 0 offen sc1" : "=v"(cxn) : "v"(XF_OFF() + F_XN), "s"(rs) : "memory");
            if constexpr (s == CSL && n == 2) {
              wait_vm<SINCE(ERQ)>();
              asm volatile("" : "+v"(cxn)::"memory");
              if (!__all(cxn[0] == T[0] && cxn[1] == T[1])) poll_flags(rs, XF_OFF(), F_XN, T);
            }
          } else {
#define X1_OFF() (F_X1 + ((p * RX + (xcd >> 1) % RX) * 32 + (l & 31)) * 8)
            if constexpr ((s == 0 || s == 4) && n == 2) asm volatile("buffer_load_dwordx2 %0, %1, %2, 0 offen sc1" : "=v"(cxn) : "v"(X1_OFF()), "s"(rs) : "memory");
            if constexpr ((s == 3 || s == 7) && n == 0) {
              wait_vm<vm_at(1, s, 1) - vm_at(1, s - 3, 3)>();
              asm volatile("" : "+v"(cxn)::"memory");
              xok |= __builtin_amdgcn_ballot_w64(cxn[0] == T[0] && cxn[1] == T[1]);
            }
            if constexpr (a_req(s, n) >= 0 && (a_req(s, n) & 7) == 0) x1_need(0x30003ull << (((rot + a_req(s, n) / 8) & 7) * 2));
          }
          if constexpr (IT && a_req(s, n) >= 0) A_LOAD1(a_req(s, n) / 2, a_req(s, n) & 1);
          if constexpr (IT == 0 && s == CSD && n == 4) {
            if (w < 3) {
              unsigned a0 = FRAG_T() + SLOT(s >> 2) * STAGE + w * 8192, a1 = a0 ^ 32, one2 = 0x3F803F80u;
              asm volatile("" : "+v"(a0), "+v"(a1), "+v"(one2));
              const u32x4 o4 = {one2, one2, one2, one2};
              f32x4 y0 = {0.f, 0.f, 0.f, 0.f}, y1 = y0;
#pragma unroll
              for (int i = 0; i < 4; ++i) { y0 = mfma16(frag_t2(a0 + i * 2048), o4, y0); y1 = mfma16(frag_t2(a1 + i * 2048), o4, y1); }
              if ((l & 14) == 0) st16(__builtin_bit_cast(u32x4, (l & 1) ? y1 : y0), rs, g * 16 + (l & 1) * 512, OFF_CS + ((h * 6 + 2 * w) * 8 + p) * 64);
            }
          }
          if constexpr (!IT && s == CSL && n == 4) { asm volatile("" : "=v"(fx[0])); if (w < 3) ld16h<0>(fx[0], rs, l * 16, OFF_CS + h * 3072 + w * 1024); }
          if constexpr (!IT && s == CSS && n == 2) {
            if (w < 3) {
              wait_vm<SINCE(CSL)>();
              asm volatile("" : "+v"(fx[0])::"memory");
              tile_sum(fx[0], L_CSL);
            }
          }
          if constexpr (s == ABL && n == 4) ld4h<0>(aq[0], rs, (l & 31) * 4 + (l >> 5) * 8192, AB_OFF());
          if constexpr (s == ABS && n == 0) {
            wait_vm<SINCE(ABL)>();
            asm volatile("" : "+v"(aq[0])::"memory");
            if constexpr (IT) {
              for (unsigned spins = 0; !__all(aq[0] != POISON) && !spin_fail(spins, rs, T);) {
                ld4h<0>(aq[0], rs, (l & 31) * 4 + (l >> 5) * 8192, AB_OFF());
                asm volatile("s_waitcnt vmcnt(0)" : "+v"(aq[0])::"memory");
              }
            }
            *(LDS_AS unsigned*)(smem + L_K + (8 + rt) * 2048 + l * 4) = aq[0];
          }
          if constexpr (s == ABS && n == 2) {
#pragma unroll
            for (int a = 0; a < 2; ++a) {
              const LDS_AS char* ab = (const LDS_AS char*)(smem + L_K + (8 + rt) * 2048 + a * 64);
              aq[a] = *(const LDS_AS unsigned*)(ab + (l & 15) * 4); bq[a] = *(const LDS_AS unsigned*)(ab + 128 + (l & 15) * 4);
              av[a] = *(const LDS_AS u32x4*)(ab + g * 16); bv[a] = *(const LDS_AS u32x4*)(ab + 128 + g * 16);
            }
          }
          if constexpr (s == ABS && n == 5) {
#pragma unroll
            for (int a = 0; a < 2; ++a) { b2[a] = f32x2{asf(bq[a]), asf(bq[a])}; asm volatile("" : "+v"(b2[a])); }
          }
          if constexpr (s == ABS + 1) {
            constexpr int k = (n + 4) % 6;
            if constexpr (k < 4) cs4[k & 1] = *(const LDS_AS f32x4*)(smem + L_CSL + k * 64 + g * 16);
            else cs4[k & 1][0] = *(const LDS_AS float*)(smem + L_CSL + k * 64 + (l & 15) * 4);
          }
          if constexpr ((s == ABS + 1 && n > 0) || (s == ABS + 2 && n == 0)) {
            constexpr int k = (n + 3) % 6;
            if constexpr (k < 4) {
#pragma unroll
              for (int a = 0; a < 2; ++a) acc[a][k] = __builtin_elementwise_fma(cs4[k & 1], f32x4{b2[a][0], b2[a][1], b2[a][0], b2[a][1]}, acc[a][k]);
            } else {
              f32x2 c2 = {cs4[k & 1][0], cs4[k & 1][0]};
              asm volatile("" : "+v"(c2));
#pragma unroll
              for (int a = 0; a < 2; ++a) acc[a][k] = __builtin_elementwise_fma(f32x4{c2[0], c2[1], c2[0], c2[1]}, __builtin_bit_cast(f32x4, bv[a]), acc[a][k]);
            }
          }
          if constexpr (IT == 0) {
            if constexpr (s == IMG - 2 && n < 3) {
              const int c = 3 * (w & 1) + n;
              ka[n] = (FRAG_T() ^ ((c & 1) << 5)) + (SLOT(5) * STAGE + (c >> 1) * 8192 + (w >> 1) * 2048);
              asm volatile("" : "+v"(ka[n]));
            }
            if constexpr (s == IMG - 1 && (n & 1)) keep[n >> 1] = frag_t2(ka[n >> 1]);
            if constexpr (s > KP0 && s < KP0 + 3 && (n & 1)) keep[3 * (s - KP0) + (n >> 1)] = frag_t2(ka[n >> 1] + (s - KP0) * STAGE);
            if constexpr (s >= IMG && s < IMG + 3 && n == 4) st16(keep[s - IMG], rs, l * 16 + (s - IMG) * 1024, OFF_WI + (h * 8 + ((rot + 5) & 7)) * STAGE + w * 3072);
            if constexpr (s >= TS0 && s < TS0 + TSN && n == 4) {
              if (tstep == s && w < TW) touch(tdum, tsrc);
            }
            if constexpr (s == TST && n == 4) { if (w >= TW) touch(tdum, tsrc); }
          } else {
            if constexpr (s == PB1 && n == 0) { POISON_BIG(1); }
            if constexpr (s == FSL && n == 4) { ld4h<0>(kv, rs, (l & 15) * 4, OFF_KEY + (bid & 15) * 128); row_load(rs, g, ffn_xoff(kg0, 0, w), fx); }
            if constexpr (s == FS0 && n == 0) {
              wait_vm<SINCE(FSL)>();
              asm volatile("" : "+v"(kv)::"memory");
              PINV4(fx);
              if (__builtin_expect(!__all(kv != POISON), 0)) {
                LDS_AS unsigned* kw = (LDS_AS unsigned*)(smem + L_SKEY + 40);
                if (w == 0) {
                  kv = key_wait(rs, T, l, 0, bid & 15, kv);
                } else {
                  while ((kv = __hip_atomic_load(kw, __ATOMIC_RELAXED, __HIP_MEMORY_SCOPE_WORKGROUP)) == POISON) __builtin_amdgcn_s_sleep(2);
                }
              }
              fkey = __builtin_amdgcn_readfirstlane(umax16(kv));
              if (w == 0) *(LDS_AS unsigned*)(smem + L_SKEY + 40) = min(fkey, POISON - 1);
              sel0 = key_row(fkey);
              aff = (int)(sel0 >> 8) == p;
              asm volatile("" : "=v"(sa), "=v"(sb));
              if (ffn_on) {
                if (__builtin_expect(((kg0 ^ fkey) & 0x780u) != 0, 0)) ld4x16(fx, rs, g * 16, ffn_xoff(fkey, 0, w));
                ffn_gu(gv, uv, fx, sa, sb);
              }
            }
            if constexpr (s == FS0 && n == 2) {
              if (ffn_on) {
                ffn_gu_check(rs, T, ffn_xoff(fkey, 0, w), l, gv, uv, fx, sa, sb);
                ffn_gu_store(smem, l, w, sa, sb);
                count_in(1);
              }
            }
            if constexpr (s == FS0D && n == 0) {
              if (ffn_on && w < 4) {
                s16x4 da[8], db[8];
                ffn_d_read(smem, l, 2 * w, da);
                while (__hip_atomic_load(qcnt + 1, __ATOMIC_RELAXED, __HIP_MEMORY_SCOPE_WORKGROUP) < 8u) {}
                asm volatile("" ::: "memory");
                const f32x4 t = ffn_t_read(smem, l);
                ffn_d_read(smem, l, 2 * w + 1, db);
                u32x2 ha = ffn_h(t, l), hb = {dppu<0x118>(ha[0]), dppu<0x118>(ha[1])};
                f32x4 ya = {0.f, 0.f, 0.f, 0.f}, yb = ya;
#pragma unroll
                for (int i = 0; i < 8; ++i) {
                  ya = mfma16k16(da[i], __builtin_bit_cast(s16x4, ha), ya); yb = mfma16k16(db[i], __builtin_bit_cast(s16x4, hb), yb);
                  ha = u32x2{dppu<0x111>(ha[0]), dppu<0x111>(ha[1])}; hb = u32x2{dppu<0x111>(hb[0]), dppu<0x111>(hb[1])};
                }
                ffn_d_store(rs, l, 8 * w, 0, slice, ya + yb);
              }
            }
#define RED_LOAD(ld, l8)                                                                                                    \
  {                                                                                                                         \
    const unsigned G = bi, po = OFF_PART + G * 32768;                                                        \
    ld(gv[0], rs, tid * 16, po); ld(gv[1], rs, tid * 16, po + 4096); ld(gv[2], rs, tid * 16, po + 8192); ld(gv[3], rs, tid * 16, po + 12288); \
    ld(uv[0], rs, tid * 16, po + 16384); ld(uv[1], rs, tid * 16, po + 20480); ld(uv[2], rs, tid * 16, po + 24576);        \
    l8(cxn, rs, (l & 3) * 16 + (l & 4) * 2, OFF_CA + (sel0 >> 7) * 2048 + G * 64);                                         \
    l8(cx1, rs, (l & 3) * 16 + (l & 4) * 2, OFF_CB + (sel0 >> 7) * 2048 + G * 64);                                          \
  }
            if constexpr (s == FS1 && n == 4) {
              asm volatile("" : "=v"(cxn), "=v"(cx1));
              if (!ffn_on && w < 4) RED_LOAD(ld16u, ld8h);
            }
            if constexpr (s == FS1S && n == 0) {
              if (!ffn_on && w < 4) {
                wait_vm<SINCE(FS1)>();
                PINV4(gv); PINV4(uv);
                asm volatile("" : "+v"(cxn), "+v"(cx1)::"memory");
                for (unsigned spins = 0; !__all(max(max(max(umax4(gv[0]), umax4(gv[1])), max(umax4(gv[2]), umax4(gv[3]))),
                                                    max(max(umax4(uv[0]), umax4(uv[1])), max(max(umax4(uv[2]), max(cxn[0], cxn[1])), max(cx1[0], cx1[1])))) != POISON) && !spin_fail(spins, rs, T);) {
                  RED_LOAD(ld16u, ld8h);
                  DRAIN();
                  PINV4(gv); PINV4(uv);
                  asm volatile("" : "+v"(cxn), "+v"(cx1)::"memory");
                }
#define F4(x) __builtin_bit_cast(f32x4, x)
                ((LDS_AS f32x4*)(smem + L_SFIN))[tid] = ((F4(gv[0]) + F4(gv[1])) + (F4(gv[2]) + F4(gv[3]))) + ((F4(uv[0]) + F4(uv[1])) + F4(uv[2]));
#undef F4
                count_in(2);
                if (w == 0) {
                  while (__hip_atomic_load(qcnt + 2, __ATOMIC_RELAXED, __HIP_MEMORY_SCOPE_WORKGROUP) < 4u) {}
                  asm volatile("" ::: "memory");
                  const u32x2 ov = red_final<4>(smem, l, fkey, cxn, cx1);
                  if (l < 8) st8(unpoison2(ov), rs, OFF_XNEW + (bi * 8 + (l & 3) * 2 + (l >> 2)) * 8, 0);
                }
              }
            }
            if constexpr (s == FXA && n == 4) { DEF4(fx); if (w < 3) row_load(rs, g, XP_OFF(), fx); }
            if constexpr (s == FXB && n == 0) {
              if (w < 3) {
                const unsigned fa = bo[0][0] + SLOT(FXT) * STAGE + w * 2048;
                u32x4 f[8];
#pragma unroll
                for (int i = 0; i < 8; ++i) f[i] = *(const LDS_AS u32x4*)(size_t)(fa + (i >> 1) * 6144 + (i & 1) * 1024);
                wait_vm<SINCE(FXA)>();
                PINV4(fx);
                ffn_xwait<XSL>(rs, T, XP_OFF(), l, fx);
                f32x4 y0 = mfma16(f[0], fx[0], f32x4{0.f, 0.f, 0.f, 0.f}), y1 = mfma16(f[1], fx[0], f32x4{0.f, 0.f, 0.f, 0.f});
#pragma unroll
                for (int i = 1; i < 4; ++i) { y0 = mfma16(f[2 * i], fx[i], y0); y1 = mfma16(f[2 * i + 1], fx[i], y1); }
                if ((l & 14) == 0) st16(unpoison4(__builtin_bit_cast(u32x4, (l & 1) ? y1 : y0)), rs, g * 16 + (l & 1) * 512, OFF_FX + ((h * 6 + 2 * w) * 8 + p) * 64);
              }
            }
            if constexpr (s == FXL && n == 4) {
              asm volatile("" : "=v"(fx[0]), "=v"(fx[1]));
              if (aff && w < 4) { ld16h<0>(fx[0], rs, l * 16, FP_OFF(0)); ld16h<0>(fx[1], rs, l * 16, FP_OFF(1)); }
            }
            if constexpr (s == FXS && n == 2) {
              if (aff && w < 4) {
                wait_vm<0>();
                asm volatile("" : "+v"(fx[0]), "+v"(fx[1])::"memory");
                if (!FX_OK(2)) {
                  unsigned spins = 0;
                  do {
                    if (spin_fail(spins, rs, T)) break;
                    fx[0] = ld16(rs, l * 16, FP_OFF(0)); fx[1] = ld16(rs, l * 16, FP_OFF(1));
                  } while (!FX_OK(2));
                }
                if (w == 3) {
                  f32x2 nx[8];
                  unpack8v(fx[0], nx); unpack8v(fx[1], nx + 4);
                  const f32x2 sm = ((nx[0] + nx[1]) + (nx[2] + nx[3])) + ((nx[4] + nx[5]) + (nx[6] + nx[7]));
                  const float mean = sum64(hadd(sm)) * (1.0f / 1024.0f);
                  const f32x2 m2 = {mean, mean};
                  f32x2 q = {0.f, 0.f};
#pragma unroll
                  for (int j = 0; j < 8; ++j) { const f32x2 d = nx[j] - m2; q = __builtin_elementwise_fma(d, d, q); }
                  const float rd = ln_rd(sum64(hadd(q)) * (1.0f / 1024.0f));
                  if (l == 0) *(LDS_AS f32x2*)(smem + L_FXL + 384) = f32x2{rd, -rbf1(mean)};
                } else {
                  tile_sum(fx[0], L_FXL);
                }
                count_in(3);
              }
            }
          }
        };
        static_for<32>([&](auto sc_) __attribute__((always_inline)) {
          constexpr int s = decltype(sc_)::value;
          if constexpr (s > 0) PIN_ACC();
          if constexpr (IT == 1) wait_vm<vm_at(IT, s, 0) - vm_a(IT, s)>();
          pin_w<s % WA>(af[s % WA][0], af[s % WA][1]);
          SB();
          static_for<6>([&](auto nc_) __attribute__((always_inline)) {
            constexpr int n = decltype(nc_)::value;
            if constexpr (s == 0) { acc[0][n] = f32x4{0.f, 0.f, 0.f, 0.f}; acc[1][n] = acc[0][n]; }
            constexpr bool on = s < QL || n >= 2;
            if constexpr (on) acc[0][n] = n < 4 ? mfma16(bfr[n].v, af[s % WA][0], acc[0][n]) : mfma16(af[s % WA][0], bfr[n].v, acc[0][n]);
            SB();
            if constexpr (n == 0) { QKV_RDB(s, 5); }
            else if constexpr (s + 1 < 32 && (s + 1 < QL || n > 2)) QKV_RDB(s + 1, n - 1);
            SB();
            if constexpr (on) acc[1][n] = n < 4 ? mfma16(bfr[n].v, af[s % WA][1], acc[1][n]) : mfma16(af[s % WA][1], bfr[n].v, acc[1][n]);
            SB();
            if constexpr (s + 1 < 32 && (s + 1 < QL || n >= 2)) QKV_RDA(s + 1, n);
            SB();
            gap(sc_, nc_);
            SB();
          });
        });
        PIN_ACC();
        frag_t qw[32 - QL][2];
        static_for<32 - QL>([&](auto c_) __attribute__((always_inline)) {
          constexpr int s = QL + decltype(c_)::value;
          QKV_RDA(s, 0); QKV_RDB(s, 0); QKV_RDA(s, 1); QKV_RDB(s, 1);
          qw[s - QL][0] = bfr[0]; qw[s - QL][1] = bfr[1];
        });
        SB();
        const bool own = aff && (int)((sel0 >> 5) & 7) == rt;
        const bool m0 = !(sel0 & 16), mq = (l & 15) == (int)(sel0 & 15), mv = g == (int)((sel0 & 15) >> 2);
        auto patch_qk = [&](int n) __attribute__((always_inline)) {
          f32x4 t = *(const LDS_AS f32x4*)(smem + L_FXL + n * 64 + g * 16);
          const f32x4 c = *(const LDS_AS f32x4*)(smem + L_CSL + n * 64 + g * 16);
          const float nb = *(const LDS_AS float*)(smem + L_FXL + 388);
#pragma unroll
          for (int e = 0; e < 4; ++e) { t[e] = __builtin_fmaf(c[e], nb, t[e]); asm volatile("" : "+v"(t[e])); }
          acc[0][n] = (mq && m0) ? t : acc[0][n];
          acc[1][n] = (mq && !m0) ? t : acc[1][n];
        };
        if (own) {
          while (__hip_atomic_load(qcnt + 3, __ATOMIC_RELAXED, __HIP_MEMORY_SCOPE_WORKGROUP) < 4u) {}
          asm volatile("" ::: "memory");
          patch_qk(2); patch_qk(3);
          const f32x2 nab = *(const LDS_AS f32x2*)(smem + L_FXL + 384);
          const LDS_AS float* fl_ = (const LDS_AS float*)(smem + L_FXL + 256) + (l & 15);
          const LDS_AS float* cl_ = (const LDS_AS float*)(smem + L_CSL + 256) + (l & 15);
          const float t4 = __builtin_fmaf(cl_[0], nab[1], fl_[0]), t5 = __builtin_fmaf(cl_[16], nab[1], fl_[16]);
          const unsigned na = __builtin_bit_cast(unsigned, (float)nab[0]);
#pragma unroll
          for (int a = 0; a < 2; ++a) {
            const bool ma = a ? !m0 : m0;
            aq[a] = (mq && ma) ? na : aq[a];
#pragma unroll
            for (int e = 0; e < 4; ++e) {
              const bool me = mv && ma && e == (int)(sel0 & 3);
              acc[a][4][e] = me ? t4 : acc[a][4][e]; acc[a][5][e] = me ? t5 : acc[a][5][e];
              av[a][e] = me ? na : av[a][e];
            }
          }
        }
        constexpr float C2 = 1.4426950408889634f * 0.17677669529663687f;
#pragma unroll
        for (int a = 0; a < 2; ++a) {
          f32x2 a2 = {asf(aq[a]), asf(aq[a])};
          qs2[a] = f32x2{asf(aq[a]) * C2, asf(aq[a]) * C2};
          asm volatile("" : "+v"(a2), "+v"(qs2[a]));
          acc[a][2] *= f32x4{a2[0], a2[1], a2[0], a2[1]}; acc[a][3] *= f32x4{a2[0], a2[1], a2[0], a2[1]};
          acc[a][4] *= __builtin_bit_cast(f32x4, av[a]); acc[a][5] *= __builtin_bit_cast(f32x4, av[a]);
        }
        k0 = pack8(acc[0][2], acc[0][3]); k1 = pack8(acc[1][2], acc[1][3]); v0 = pack8(acc[0][4], acc[1][4]); v1 = pack8(acc[0][5], acc[1][5]);
        const unsigned kvo = OFF_KV + IT * (8u << 20) + (p * 32 + h) * 32768 + rt * 4096;
        if (p < 7) { st16(k0, rs, l * 16, kvo); st16(k1, rs, l * 16 + 1024, kvo); st16(v0, rs, l * 16 + 2048, kvo); st16(v1, rs, l * 16 + 3072, kvo); }
        char* ownp = smem + (8 + rt) * 2048 + l * 16;
        *(u32x4*)(ownp + L_K) = k0;
        if (rt < 7) *(u32x4*)(ownp + L_K + 1024) = k1;
        *(u32x4*)(ownp + L_V) = v0; *(u32x4*)(ownp + L_V + 1024) = v1;
        SB();
        static_for<32 - QL>([&](auto c_) __attribute__((always_inline)) {
          constexpr int s = QL + decltype(c_)::value;
#pragma unroll
          for (int n = 0; n < 2; ++n) { acc[0][n] = mfma16(qw[s - QL][n].v, af[s % WA][0], acc[0][n]); acc[1][n] = mfma16(qw[s - QL][n].v, af[s % WA][1], acc[1][n]); }
        });
        if (own) { patch_qk(0); patch_qk(1); }
      };
      qkv_loop(it_c);
    }
#undef A_LOAD1
#undef QKV_RDA
#undef QKV_RDB
#undef QKV_BO
    {
      IDS();
      const GAS bf16 *pwo, *pwg, *pwu;
      asm volatile("s_load_dwordx2 %0, %3, 24\n\ts_load_dwordx2 %1, %3, 40\n\ts_load_dwordx2 %2, %3, 48" : "=&s"(pwo), "=&s"(pwg), "=&s"(pwu) : "s"(ka));
      const int ql = l & 15;
      const f32x4 z4 = {0.f, 0.f, 0.f, 0.f}, ninf = {-INFINITY, -INFINITY, -INFINITY, -INFINITY};
      LDS_AS unsigned* rdy = (LDS_AS unsigned*)(smem + L_RDY);
      const unsigned rtag = T[0] + it;
      u32x4 qf[2];
      f32x4 cn[2], oa[2][2], sum[2], S[2][2];
      unsigned one2 = 0x3F803F80u;
      asm volatile("" : "+v"(one2));
      const u32x4 ones = {one2, one2, one2, one2};
      auto ex8 = [&](f32x4 a, f32x4 b) __attribute__((always_inline)) {
        return u32x4{pack2(__builtin_amdgcn_exp2f(a[0]), __builtin_amdgcn_exp2f(a[1])), pack2(__builtin_amdgcn_exp2f(a[2]), __builtin_amdgcn_exp2f(a[3])),
                     pack2(__builtin_amdgcn_exp2f(b[0]), __builtin_amdgcn_exp2f(b[1])), pack2(__builtin_amdgcn_exp2f(b[2]), __builtin_amdgcn_exp2f(b[3]))};
      };
      {
#pragma unroll
        for (int a = 0; a < 2; ++a) {
          const f32x4 c4 = {qs2[a][0], qs2[a][1], qs2[a][0], qs2[a][1]};
          qf[a] = pack8(acc[a][0] * c4, acc[a][1] * c4);
        }
        f32x4 mk;
#pragma unroll
        for (int e = 0; e < 4; ++e) mk[e] = (4 * g + e <= ql) ? 0.f : -INFINITY;
        const f32x4 d0 = mfma16(k0, qf[0], mk), d1 = mfma16(k1, qf[1], mk), d2 = mfma16(k0, qf[1], z4);
        const float m0 = -max_x32(max_x16(fmaxf(fmaxf(d0[0], d0[1]), fmaxf(d0[2], d0[3]))));
        const float m1 = -max_x32(max_x16(fmaxf(fmaxf(d1[0], d1[1]), fmaxf(d1[2], d1[3]))));
        cn[0] = f32x4{m0, m0, m0, m0}; cn[1] = f32x4{m1, m1, m1, m1};
        asm volatile("" : "+v"(cn[0]), "+v"(cn[1]));
        const f32x4 e0 = d0 + cn[0];
        const u32x4 p0 = {pack2(__builtin_amdgcn_exp2f(e0[0]), __builtin_amdgcn_exp2f(e0[1])), pack2(__builtin_amdgcn_exp2f(e0[2]), __builtin_amdgcn_exp2f(e0[3])), 0u, 0u};
        const u32x4 p1 = ex8(d2 + cn[1], d1 + cn[1]);
        oa[0][0] = mfma16(v0, p0, z4); oa[0][1] = mfma16(v1, p0, z4); sum[0] = mfma16(ones, p0, z4);
        oa[1][0] = mfma16(v0, p1, z4); oa[1][1] = mfma16(v1, p1, z4); sum[1] = mfma16(ones, p1, z4);
      }
      const char* kb_ = smem + L_K + l * 16;
      const char* vb_ = smem + L_V + l * 16;
      const unsigned wov = (l >> 3) * 2048 + (((((l & 7) >> 1) ^ ((l >> 4) & 3)) * 2 + (l & 1)) << 4), fnv = (l >> 1) * (F_ * 2) + (l & 1) * 16;
      const unsigned pp = __builtin_amdgcn_readfirstlane(p > 0 ? 32 : 0), pvo = OFF_KV + it * (8u << 20) + (p * 32 + h - pp) * 32768;
      const unsigned pfo = __builtin_amdgcn_readfirstlane(F_KV + KVF(it, p * 32 + h - pp));
      asm volatile("" : "+v"(oa[0][0]), "+v"(oa[0][1]), "+v"(oa[1][0]), "+v"(oa[1][1]), "+v"(sum[0]), "+v"(sum[1]));
      DRAIN();
      asm volatile("" : "+v"(tdum));
      if (l == 0) st8(T, rs, rt * 8, F_KV + KVF(it, p * 32 + h));
      BAR();
      asm volatile("" : "+s"(pwo), "+s"(pwg), "+s"(pwu));
      WO_PIECE(15); WO_PIECE(14);
      u32x4 kf[2], vf[2];
      int nw = 0;
      u32x4 p0, p1;
      float e0, e1;
#define EE(v, i) { e0 = __builtin_amdgcn_exp2f(v[i]); e1 = __builtin_amdgcn_exp2f(v[i + 1]); pin2s(e0, e1); SB(); }
#define CV(P, i) { unsigned t_ = pack2(e0, e1); pin1(t_); P[i] = t_; SB(); }
      auto body = [&](unsigned ad, auto dk_) __attribute__((always_inline)) {
        constexpr int dk = decltype(dk_)::value;
        const f32x4 a0 = S[1][0], a1 = S[1][1];
        sum[0] = mfma16(ones, p0, sum[0]); SB();
        EE(a0, 0);
        lds_wait<2>(kf[0], kf[1]);
        S[0][0] = mfma16(kf[0], qf[0], cn[0]); SB();
        CV(p1, 0); EE(a0, 2);
        S[0][1] = mfma16(kf[1], qf[0], cn[0]); SB();
        CV(p1, 1); EE(a1, 0);
        lds_wait<0>(vf[0], vf[1]);
        oa[0][0] = mfma16(vf[0], p0, oa[0][0]); SB();
        CV(p1, 2); EE(a1, 2);
        oa[0][1] = mfma16(vf[1], p0, oa[0][1]); SB();
        CV(p1, 3);
        S[1][0] = mfma16(kf[0], qf[1], cn[1]); SB();
        EE(S[0][0], 0);
        S[1][1] = mfma16(kf[1], qf[1], cn[1]); SB();
        lds2<2048 + dk>(kf[0], kf[1], ad); SB();
        CV(p0, 0); EE(S[0][0], 2);
        oa[1][0] = mfma16(vf[0], p1, oa[1][0]); SB();
        CV(p0, 1); EE(S[0][1], 0);
        oa[1][1] = mfma16(vf[1], p1, oa[1][1]); SB();
        lds2<2048 + L_V>(vf[0], vf[1], ad); SB();
        CV(p0, 2); EE(S[0][1], 2);
        sum[1] = mfma16(ones, p1, sum[1]); SB();
        CV(p0, 3);
      };
      auto last = [&]() __attribute__((always_inline)) {
        sum[0] = mfma16(ones, p0, sum[0]); SB();
        EE(S[1][0], 0);
        lds_wait<0>(vf[0], vf[1]);
        pin2(kf[0], kf[1]);
        oa[0][0] = mfma16(vf[0], p0, oa[0][0]); SB();
        CV(p1, 0); EE(S[1][0], 2);
        oa[0][1] = mfma16(vf[1], p0, oa[0][1]); SB();
        CV(p1, 1); EE(S[1][1], 0);
        const float f0 = __builtin_amdgcn_exp2f(S[1][1][2]), f1 = __builtin_amdgcn_exp2f(S[1][1][3]);
        p1[2] = pack2(e0, e1); p1[3] = pack2(f0, f1);
        oa[1][0] = mfma16(vf[0], p1, oa[1][0]); oa[1][1] = mfma16(vf[1], p1, oa[1][1]); sum[1] = mfma16(ones, p1, sum[1]);
      };
      auto piece = [&]() __attribute__((always_inline)) {
        if constexpr (it == 1) { WO_PIECE(13 - nw); }
        else if (ffn_on) { FFN_DMA(nw); }
        ++nw;
      };
      if (rt > 0) {
        unsigned ad = lbase + l * 16 + (6 + rt) * 2048;
        lds2<2048>(kf[0], kf[1], ad); lds2<2048 + L_V>(vf[0], vf[1], ad);
        lds_wait<2>(kf[0], kf[1]);
        S[0][0] = mfma16(kf[0], qf[0], cn[0]); S[0][1] = mfma16(kf[1], qf[0], cn[0]); S[1][0] = mfma16(kf[0], qf[1], cn[1]); S[1][1] = mfma16(kf[1], qf[1], cn[1]);
        SB();
        lds_wait<0>(vf[0], vf[1]);
        lds2<0>(kf[0], kf[1], ad);
        p0 = ex8(S[0][0], S[0][1]);
        lds_wait<0>(kf[0], kf[1]);
#pragma unroll 1
        for (int i = rt - 1; i > 0; --i) {
          ad -= 2048;
          body(ad, std::integral_constant<int, -2048>{});
          piece();
          SB();
        }
        piece();
        last();
      }
      {
        constexpr int FW = 4, FN = 8 / FW;
        if (w < FW) {
          const int g0 = FN * rt;
          poll_flags(rs, (l & (FN - 1)) * 8, pfo + g0 * 8, T);
#pragma unroll
          for (int j = 0; j < FN; ++j) {
            const unsigned d = lbase + (g0 + j) * 2048, o = pvo + (g0 + j) * 4096;
            dma16s<0>(rs, l * 16, o, d + L_K); dma16s<1024>(rs, l * 16, o, d + L_K);
            dma16s<0>(rs, l * 16, o + 2048, d + L_V); dma16s<1024>(rs, l * 16, o + 2048, d + L_V);
          }
        }
        if (p == 0) { cn[0] = ninf; cn[1] = ninf; }
        f32x4 mk;
#pragma unroll
        for (int e = 0; e < 4; ++e) mk[e] = (4 * g + e > ql) ? 0.f : -INFINITY;
        const f32x4 cm0 = cn[0] + mk, cm1 = cn[1] + mk;
        if (w < FW) {
          if constexpr (FN > 1) { wait_vm<2 * FN>(); if (l < FN / 2) rdy[FN * rt + l] = rtag; }
          DRAIN();
          if (l < FN) rdy[FN * rt + l] = rtag;
        }
        if constexpr (it == 1) { WO_PIECE(6); WO_PIECE(5); WO_PIECE(4); }
        else { WO_PIECE(13); WO_PIECE(12); if (ffn_on) FFN_DMA(7); }

        int have = rt - 1;
        auto need = [&](int gq) __attribute__((always_inline)) {
          if (__builtin_expect(gq > have && gq < 8, 0)) {
            do {
              const unsigned t = __hip_atomic_load(rdy + (l & 7), __ATOMIC_RELAXED, __HIP_MEMORY_SCOPE_WORKGROUP);
              have = rt - 1 + __builtin_ctz(~((unsigned)__builtin_amdgcn_ballot_w64(t == rtag) & 0xffu) >> rt);
            } while (gq > have);
            asm volatile("" ::: "memory");
          }
        };
        unsigned ad = lbase + l * 16 + rt * 2048;
        need(rt);
        lds2<0>(kf[0], kf[1], ad); lds2<L_V>(vf[0], vf[1], ad);
        lds_wait<2>(kf[0], kf[1]);
        S[0][0] = mfma16(kf[0], qf[0], cm0); S[0][1] = mfma16(kf[1], qf[0], cn[0]); S[1][0] = ninf; S[1][1] = mfma16(kf[1], qf[1], cm1);
        SB();
        lds_wait<0>(vf[0], vf[1]);
        need(rt + 1);
        lds2<2048>(kf[0], kf[1], ad);
        p0 = ex8(S[0][0], S[0][1]);
        lds_wait<0>(kf[0], kf[1]);
#pragma unroll 1
        for (int gq = rt + 1; gq < 8; ++gq) {
          need(gq + 1);
          body(ad, std::integral_constant<int, 2048>{});
          piece();
          SB();
          ad += 2048;
        }
        last();
      }
      {
        float big = 0.f;
#pragma unroll
        for (int a = 0; a < 2; ++a)
#pragma unroll
          for (int d = 0; d < 2; ++d)
            big = __builtin_elementwise_maximum(__builtin_elementwise_maximum(big, __builtin_elementwise_maximum(fabsf(oa[a][d][0]), fabsf(oa[a][d][1]))),
                                                __builtin_elementwise_maximum(fabsf(oa[a][d][2]), fabsf(oa[a][d][3])));
        bool ok = sum[0][0] >= 0.5f && sum[0][0] < 0x1p64f && sum[1][0] >= 0.5f && sum[1][0] < 0x1p64f && big < INFINITY;
        if (!__all(ok)) {
#pragma unroll
          for (int a = 0; a < 2; ++a) {
            const int qs = 2 * rt + a, qq = 256 + 16 * qs + ql, klo = max(qq - 255, p > 0 ? 0 : 256);
            auto score = [&](int t) __attribute__((always_inline)) {
              f32x4 s = mfma16(t == 31 ? k1 : *(const u32x4*)(kb_ + t * 1024), qf[a], z4);
#pragma unroll
              for (int e = 0; e < 4; ++e) { const int kk = 16 * t + 4 * g + e; s[e] = (kk <= qq && kk >= klo) ? s[e] : -INFINITY; }
              return s;
            };
            float mx = -INFINITY;
#pragma unroll 1
            for (int t = qs; t <= qs + 16; ++t) { const f32x4 s = score(t); mx = fmaxf(fmaxf(mx, fmaxf(s[0], s[1])), fmaxf(s[2], s[3])); }
            mx = max_x32(max_x16(mx));
            oa[a][0] = z4; oa[a][1] = z4; sum[a] = z4;
#pragma unroll 1
            for (int G = qs >> 1; G <= (qs + 16) >> 1; ++G) {
              const f32x4 s0 = score(2 * G), s1 = score(2 * G + 1);
              f32x4 e0, e1;
#pragma unroll
              for (int e = 0; e < 4; ++e) { e0[e] = __builtin_amdgcn_exp2f(s0[e] - mx); e1[e] = __builtin_amdgcn_exp2f(s1[e] - mx); }
              const u32x4 pf = pack8(e0, e1);
              oa[a][0] = mfma16(*(const u32x4*)(vb_ + G * 2048), pf, oa[a][0]);
              oa[a][1] = mfma16(*(const u32x4*)(vb_ + G * 2048 + 1024), pf, oa[a][1]);
              sum[a] = mfma16(ones, pf, sum[a]);
            }
          }
        }
      }
#pragma unroll
      for (int a = 0; a < 2; ++a) {
        const float inv = __builtin_amdgcn_rcpf(sum[a][0]);
        oa[a][0] *= inv; oa[a][1] *= inv;
      }
      const unsigned oo = OFF_O + it * (4u << 20) + ((p * 8 + rt) * 64 + h * 2) * 1024;
      st16(pack8(oa[0][0], oa[0][1]), rs, l * 16, oo);
      st16(pack8(oa[1][0], oa[1][1]), rs, l * 16 + 1024, oo);
      DRAIN();
      BAR();
      if (tid == 0) st8(T, rs, OFL(h), F_O + OFB(it, p));
      if constexpr (it == 0) {
        DEF4(gv); DEF4(uv);
        if (ffn_on) {
          const unsigned ro = 65536 + w * 1024 + (4 * g + ((l & 15) >> 2)) * 32 + (l & 3) * 8;
#pragma unroll
          for (int i = 0; i < 4; ++i) {
            frag_t f;
            f.hf.a = trread(smem, ro + i * 16384); f.hf.b = trread(smem, ro + i * 16384 + 512);
            gv[i] = f.v;
            f.hf.a = trread(smem, ro + i * 16384 + 8192); f.hf.b = trread(smem, ro + i * 16384 + 8704);
            uv[i] = f.v;
          }
        }
        PINV4(gv); PINV4(uv);
        {
          ARGP(px, 0);
          const unsigned xo = ((rb * 4 + (w & 3)) * 32 + (w >> 2) * 16) * 2048 + ct * 128, xw = (l >> 3) * 2048 + (((l & 7) ^ g) << 4);
          dma16(RSRC(px, 2048 * 1024 * 2), xw, xo, lbase + w * 4096); dma16(RSRC(px, 2048 * 1024 * 2), xw ^ 64, xo + 16384, lbase + w * 4096 + 1024);
        }
      }
      WO_PIECE(3); WO_PIECE(2); WO_PIECE(1); WO_PIECE(0);
      if constexpr (it == 0) { WO_PIECE(11); WO_PIECE(10); WO_PIECE(9); WO_PIECE(8); WO_PIECE(7); WO_PIECE(6); WO_PIECE(5); WO_PIECE(4); }
    }
    IDS();
    ARGP(pwr, 4); ARGP(pwd, 7); ARGP(pout, 8);
    const int kh = w >> 2, rt2 = rb * 4 + (w & 3);
    const unsigned R = rt2 * 32 + kh * 16 + (l & 15);
    const unsigned to = (rt2 * 64 + ct * 4 + kh) * 1024 + l * 16;
    const unsigned rmo = ct * 64 + ((g & 1) ? 12 + 4 * g : 4 * g);
    f32x2 v[8], wv[8];
    float wsum;
    {
      u32x4 wa[2], wrp[2], res[2];
#pragma unroll
      for (int j = 0; j < 2; ++j) {
        wrp[j] = *(const GAS u32x4*)(pwr + rmo + 32 * j);
        wa[j] = *(const GAS u32x4*)(pwr + l * 8 + j * 512);
      }
      {
        f32x2 t[8];
        unpack8v(wa[0], t); unpack8v(wa[1], t + 4);
        const f32x2 s = ((t[0] + t[1]) + (t[2] + t[3])) + ((t[4] + t[5]) + (t[6] + t[7]));
        wsum = sum64(hadd(s));
        unpack8v(trade16(wrp[0]), wv); unpack8v(trade16(wrp[1]), wv + 4);
        asm volatile("" : "+v"(wsum), "+v"(wv[0]), "+v"(wv[1]), "+v"(wv[2]), "+v"(wv[3]), "+v"(wv[4]), "+v"(wv[5]), "+v"(wv[6]), "+v"(wv[7]));
      }
      unsigned bt[4];
#pragma unroll
      for (int n = 0; n < 4; ++n) {
        bt[n] = lbase + 32768 + 65536 + kh * 4096 + (4 * g + ((l & 15) >> 2)) * 128 + (l & 3) * 8 + ((n ^ (2 * (g & 1) + ((l & 15) >> 3))) << 5);
        asm volatile("" : "+v"(bt[n]));
      }
      if (w == 0) poll_flags(rs, OFL(l & 31), F_O + OFB(it, rb >> 1), T);
      else if (it == 0 && w < 3) poll_flags2(rs, F_XN + (((w - 1) * 4 + g) * RX + bi % RX) * 256 + (l & 15) * 16, T);
      else if (it == 0 && w == 3) poll_flags(rs, ((l >> 3) * 32 + h) * 128 + (l & 7) * 8, F_KV, T);
      asm volatile("s_barrier" ::: "memory");
      const unsigned abase = OFF_O + it * (4u << 20) + (rt2 * 64 + 32 * kh) * 1024;
      const unsigned av0 = l * 16 + kh * 1024, av1 = l * 16 + (kh ^ 1) * 1024;
      u32x4 af[WB][2];
#define O_LOAD(s_) { ld16h<(wo_k(it, s_) & 1) * 2048>(af[(s_) % WB][0], rs, av0, abase + (wo_k(it, s_) >> 1) * 4096); ld16h<(wo_k(it, s_) & 1) * 2048>(af[(s_) % WB][1], rs, av1, abase + (wo_k(it, s_) >> 1) * 4096); }
      if (w >= 4) __builtin_amdgcn_s_setprio(1);
      static_for<WR>([&](auto c_) __attribute__((always_inline)) { O_LOAD(decltype(c_)::value); });
      if constexpr (it == 1) { ld16h<0>(res[0], rs, g * 16, OFF_XNEW + ct * 128); ld16h<64>(res[1], rs, g * 16, OFF_XNEW + ct * 128); }
      SB();
      f32x4 ac[2][4];
#pragma unroll
      for (int a = 0; a < 2; ++a)
#pragma unroll
        for (int n = 0; n < 4; ++n) ac[a][n] = f32x4{0.f, 0.f, 0.f, 0.f};
      u32x4 bfr[2][4];
#define WO_RD(buf, s, n)                                                                             \
  {                                                                                                  \
    const unsigned o_ = bt[n] + (wo_k(it, s) & 7) * 8192;                                             \
    frag_t f_;                                                                                       \
    f_.hf.a = trabs(o_); f_.hf.b = trabs(o_ + 2048);                                                 \
    bfr[buf][n] = f_.v;                                                                              \
  }
#pragma unroll
      for (int n = 0; n < 4; ++n) WO_RD(0, 0, n);
      static_for<16>([&](auto sc_) __attribute__((always_inline)) {
        constexpr int s = decltype(sc_)::value, cur = s & 1;
        pin_ac(ac);
        if constexpr (s == 7) __builtin_amdgcn_s_setprio(0);
        if constexpr (s == 1) lds_count<0>(lbase + L_QC);
        if constexpr (s == (it == 0 ? 3 : 11)) lds_look(smem + L_QC, 64);
        if constexpr (s < 15 && ((wo_k(it, s) ^ wo_k(it, s + 1)) & 8)) {
#pragma unroll
          for (int n = 0; n < 4; ++n) { bt[n] += (wo_k(it, s) & 8) ? -65536 : 65536; pin1(bt[n]); }
        }
        if constexpr (s < WB - WR) O_LOAD(WR + 2 * s);
        if constexpr (it == 0 && s == 8) lds_count<8>(lbase + L_QC);
        if constexpr (it == 0 && s == WDS) {
          DRAIN();
          if (ffn_on) { lds_look(smem + L_QC + 8, 16); WD_DMA_OF(w); }
        }
        if constexpr (it == 1 || s < WDS) wait_vm<wo_at(it, s) - wo_pos(it, s)>();
        pin2(af[s % WB][0], af[s % WB][1]);
        if constexpr (it == 1 && s == 9) pin2(res[0], res[1]);
        SB();
#pragma unroll
        for (int n = 0; n < 4; ++n) {
          ac[0][n] = mfma16(bfr[cur][n], af[s % WB][0], ac[0][n]);
          ac[1][n] = mfma16(bfr[cur][n], af[s % WB][1], ac[1][n]);
          if (s + 1 < 16) WO_RD(cur ^ 1, s + 1, n);
          if (it == 0 && s == 8) {
            const u32x2 t = *(const LDS_AS u32x2*)(smem + w * 4096 + (l & 15) * 128 + (((2 * n + (g >> 1)) ^ ((l & 15) >> 1)) << 4) + (g & 1) * 8);
            res[n >> 1][2 * (n & 1)] = t[0]; res[n >> 1][2 * (n & 1) + 1] = t[1];
          }
          if (s == 9) {
            if (it == 1 && n == 0) { res[0] = R == sel0 ? res[0] : x2p[0]; res[1] = R == sel0 ? res[1] : x2p[1]; }

            v[2 * n] = f32x2{lo16(res[n >> 1][2 * (n & 1)]), hi16(res[n >> 1][2 * (n & 1)])};
            v[2 * n + 1] = f32x2{lo16(res[n >> 1][2 * (n & 1) + 1]), hi16(res[n >> 1][2 * (n & 1) + 1])};
            pin2f(v[2 * n], v[2 * n + 1]);
          }
          SB();
        }
        if constexpr (s < WB - WR) { O_LOAD(WR + 2 * s + 1); }
        else if constexpr (s + WB < 16) { O_LOAD(s + WB); }
        SB();
      });
#undef WO_RD
#undef O_LOAD
#define XCH_AT(v) ((v) == 7 ? 155648 : (v) * 4096)
#pragma unroll
      for (int n = 0; n < 4; ++n) *(f32x4*)(smem + XCH_AT(w) + n * 1024 + l * 16) = ac[1][n];
#pragma unroll
      for (int n = 0; n < 4; ++n) { v[2 * n] += f32x2{ac[0][n][0], ac[0][n][1]}; v[2 * n + 1] += f32x2{ac[0][n][2], ac[0][n][3]}; }
      BAR();
#pragma unroll
      for (int n = 0; n < 4; ++n) {
        const f32x4 o = *(const f32x4*)(smem + XCH_AT(w ^ 4) + n * 1024 + l * 16);
        v[2 * n] += f32x2{o[0], o[1]}; v[2 * n + 1] += f32x2{o[2], o[3]};
      }
    }
    {
      if (it == 0) { st16(pack8v(v), rs, to, OFF_X1); st16(pack8v(v + 4), rs, to + 2048, OFF_X1); }
      SB();
      const f32x2 s = ((v[0] + v[1]) + (v[2] + v[3])) + ((v[4] + v[5]) + (v[6] + v[7]));
      f32x2 dp = v[0] * wv[0], q = v[0] * v[0];
#pragma unroll
      for (int j = 1; j < 8; ++j) { dp = __builtin_elementwise_fma(v[j], wv[j], dp); q = __builtin_elementwise_fma(v[j], v[j], q); }
      float mt = hadd(s), qq = hadd(q), dot = hadd(dp);
      add_g3(mt, qq, dot);
      const unsigned so = OFF_STAT + it * 524288 + rb * 32768, lrow = R & 127;
      if (g == 0) st16(unpoison4(__builtin_bit_cast(u32x4, f32x4{mt, qq, dot, 0.f})), rs, ct * 2048 + lrow * 16, so);
      SB();
      if (it == 0) wait_vm<1>();
      if constexpr (it == 0) {
        volatile LDS_AS u32x2* in = (volatile LDS_AS u32x2*)(smem + L_XC);
        if (l == 0) in[w] = T;
        const u32x2 t = in[l & 7];
        if (__all(t[0] == T[0] && t[1] == T[1]) && l < RX) st8(T, rs, (l * 32 + (rb & 1) * 16 + ct) * 8, F_X1 + (rb >> 1) * RX * 32 * 8);
      }
      if (it == 1) { if (ffn_on) { WD_DMA_OF(w); } }
      if (it == 0) {
#pragma unroll
        for (int j = 0; j < 9; ++j) *(u32x4*)(smem + L_RING + (j / 3) * STAGE + (w * 3 + j % 3) * 1024 + l * 16) = keep[j];
      }
      __builtin_amdgcn_s_sleep(it == 0 ? SDI0 : SDIR);
      char* const skey = smem + (it == 0 ? L_SKEY : L_XC);
      u32x4 sp[4];
      ld16h<0>(sp[0], rs, g * 8192 + lrow * 16, so); ld16h<2048>(sp[1], rs, g * 8192 + lrow * 16, so);
      ld16h<0>(sp[2], rs, g * 8192 + lrow * 16, so + 4096); ld16h<2048>(sp[3], rs, g * 8192 + lrow * 16, so + 4096);
      u32x2 xf;
      unsigned k_;
      asm volatile("" : "=v"(xf), "=v"(k_));
      if (it == 0 && w == 0) { ld8h(xf, rs, (l & 31) * 8, F_X1 + (p * RX + (xcd >> 1) % RX) * 32 * 8); ld4h<0>(k_, rs, (l & 15) * 4, OFF_KEY + (bid & 15) * 128); }
      DRAIN();
      asm volatile("" : "+v"(xf), "+v"(k_)::"memory");
      PINV4(sp);
      const RowFin r = fin_key(rs, T, g * 8192 + lrow * 16, so, skey, l, w, R, wsum, sp);
      if constexpr (it == 0) {
        if (w == 0) {
          const unsigned need = 0x30003u << (((PROT() + 5) & 7) * 2);
          unsigned m = (unsigned)__builtin_amdgcn_ballot_w64(xf[0] == T[0] && xf[1] == T[1]);
          for (unsigned spins = 0; (m & need) != need;) {
            if (spin_fail(spins, rs, T)) { m = ~0u; break; }
            const u32x2 f = ld8(rs, F_X1 + ((p * RX + (xcd >> 1) % RX) * 32 + (l & 31)) * 8, 0);
            k_ = key_load(rs, l, 0, bid & 15);
            m = (unsigned)__builtin_amdgcn_ballot_w64(f[0] == T[0] && f[1] == T[1]);
          }
          k_ = umax16(k_ == POISON ? 0u : k_);
          if (l == 0) *(LDS_AS u32x4*)(skey + 32) = u32x4{m, k_, POISON, 0u};
        }
        if (tid == 192) *(LDS_AS u32x4*)(smem + L_QC) = u32x4{0u, 0u, 0u, 0u};
      }
      BAR();
      if constexpr (it == 0) {
        PRE_LOAD(1, PROT() + 5, 0, 4);
        const u32x2 mk = *(const LDS_AS u32x2*)(skey + 32);
        xm0 = __builtin_amdgcn_readfirstlane(mk[0]); kg0 = __builtin_amdgcn_readfirstlane(mk[1]);
      }
      const unsigned own = fin_cand(rs, skey, it, g, rb, ct, r, v);
      if constexpr (it == 0) kg0 = max(kg0, (unsigned)__builtin_amdgcn_readfirstlane(own));
      fin_x2(r, v);
      if constexpr (it == 0) {
        x2p[0] = pack8v(v); x2p[1] = pack8v(v + 4);
        asm volatile("" : "+v"(x2p[0]), "+v"(x2p[1]));
        if ((w & 3) * 2 + (w >> 2) == ct >> 1) {
          const float f = 1.0f + r.rd, rd2 = ln_rd(r.var * f * f), mb2 = rbf1(__builtin_fmaf(r.mean - r.mb, r.rd, r.mean));
          if (g < 2 && ((l >> 3) & 1) == (ct & 1))
            st4(min(__builtin_bit_cast(unsigned, g == 0 ? f * rd2 : -__builtin_fmaf(r.mb, r.rd, mb2) * __builtin_amdgcn_rcpf(f)), POISON - 1), rs, OFF_AB + 16384 + R * 4 + g * 8192, 0);
        }
      }
    }
    if (it == 1) {
      const u32x4 op0 = trade16(pack8v(v)), op1 = trade16(pack8v(v + 4));
#define OUT_STORE()                                                                                 \
  if (R != key_row(key)) {                                                                          \
    __builtin_amdgcn_raw_buffer_store_b128(op0, rout, (R * 1024 + rmo) * 2, 0, SC1);                  \
    __builtin_amdgcn_raw_buffer_store_b128(op1, rout, (R * 1024 + rmo + 32) * 2, 0, SC1);             \
  }
      u32x4 xv[4];
      unsigned guess = 0;
      {
        __builtin_amdgcn_s_sleep(GSL);
        const unsigned k_ = key_load(rs, l, 1, bid & 15);
        guess = ffn_xoff(__builtin_amdgcn_readfirstlane(umax16(k_ == POISON ? 0u : k_)), 1, w);
        row_load(rs, g, guess, xv);
      }
      if (w == 0) {
        const unsigned k_ = key_wait(rs, T, l, 1, bid & 15, key_load(rs, l, 1, bid & 15));
        if (l == 0) *(LDS_AS unsigned*)(smem + L_SKEY + 32) = k_;
      }
      DRAIN();
      PINV4(xv);
      BAR();
      const unsigned key = __builtin_amdgcn_readfirstlane(*(LDS_AS unsigned*)(smem + L_SKEY + 32));
      if (ffn_on) {
        f32x4 ga, ua;
        s16x4 dfr[8];
        ffn_d_read(smem, l, w, dfr);
        if (guess != ffn_xoff(key, 1, w)) ld4x16(xv, rs, g * 16, ffn_xoff(key, 1, w));
        ffn_gu(gv, uv, xv, ga, ua);
        ffn_gu_check(rs, T, ffn_xoff(key, 1, w), l, gv, uv, xv, ga, ua);
        ffn_gu_store(smem, l, w, ga, ua);
        BAR();
        f32x4 ya = {0.f, 0.f, 0.f, 0.f}, yb = ya;
        u32x2 hh = ffn_h(ffn_t_read(smem, l), l);
        ffn_d_mma(dfr, hh, ya, yb);
        if ((l & 15) < 8) ffn_d_store(rs, l, 4 * w, 1, slice, ya + yb);
        SB();
        __builtin_amdgcn_s_sleep(OSL);
        OUT_STORE();
      } else {
        OUT_STORE();
        const unsigned G = bi;
        if (G == 0 && tid == 0) st8(T, rs, F_EP, 0);
        u32x4 pv[4];
        u32x2 cxn, cx1;
        __builtin_amdgcn_s_sleep(RSL);
        red_load(rs, tid, 1, G, key, pv, cxn, cx1);
        red_sum(rs, smem, T, tid, w, 1, G, key, pv, cxn, cx1);
        BAR();
        if (w == 0) {
          const u32x2 ov = red_final<8>(smem, l, key, cxn, cx1);
          if (l < 8) __builtin_amdgcn_raw_buffer_store_b64(ov, rout, (key_row(key) * 1024 + G * 32 + l * 4) * 2, 0, 0);
        }
      }
#undef OUT_STORE
    }
  };
  layer(std::integral_constant<int, 0>{});
  layer(std::integral_constant<int, 1>{});
}

extern "C" void kernel_launch(void* const* d_in, const int* in_sizes, int n_in, void* d_out, int out_size,
                              void* d_ws, size_t ws_size, hipStream_t stream) {
  (void)hipFuncSetAttribute((const void*)mega_kernel, hipFuncAttributeMaxDynamicSharedMemorySize, LDS_BYTES);
  mega_kernel<<<256, 512, LDS_BYTES, stream>>>((const bf16*)d_in[0], (char*)d_ws, (const bf16*)d_in[1], (const bf16*)d_in[2], (const bf16*)d_in[4],
                                               (const bf16*)d_in[5], (const bf16*)d_in[6], (const bf16*)d_in[7], (bf16*)d_out);
}
```

```cpp
#include <hip/hip_runtime.h>
#include <stdint.h>
#include <utility>

typedef __bf16 bf16;
typedef __bf16 bf16x8 __attribute__((ext_vector_type(8)));
typedef __bf16 bf16x2 __attribute__((ext_vector_type(2)));
typedef float f32x4 __attribute__((ext_vector_type(4)));
typedef float f32x2 __attribute__((ext_vector_type(2)));
typedef short s16x4 __attribute__((ext_vector_type(4)));
typedef unsigned int u32x4 __attribute__((ext_vector_type(4)));
typedef unsigned int u32x2 __attribute__((ext_vector_type(2)));
#define LDS_AS __attribute__((address_space(3)))
#define GAS __attribute__((address_space(1)))
typedef GAS unsigned gu32;
typedef GAS unsigned long long gu64;
#define RLX_AGENT __ATOMIC_RELAXED, __HIP_MEMORY_SCOPE_AGENT
typedef __amdgpu_buffer_rsrc_t rsrc_t;

constexpr int F_ = 3584;
constexpr int SC1 = 16;

constexpr int RX = 4;
constexpr unsigned F_EP = 0, F_TMO = 128;
constexpr unsigned F_XN = 4096;
constexpr unsigned F_O = 8u << 20;
constexpr unsigned F_X1 = F_O + 131072;
constexpr unsigned OFF_KEY = F_O + 393216;
constexpr unsigned F_KV = F_O + 524288;
#define KVF(it, i) ((i) * 128 + (it) * 64)
#define OFL(h) (((h) >> 3) * 128 + ((h) & 7) * 8)
#define OFB(it, p) ((p) * 512 + (it) * 64)
constexpr unsigned OFF_AB = 147456;
constexpr unsigned OFF_XNEW = 262144;
constexpr unsigned OFF_CS = 294912;
constexpr unsigned OFF_FX = 393216;
constexpr unsigned OFF_CA = 524288;
constexpr unsigned OFF_CB = 655360;
constexpr unsigned OFF_STAT = 1u << 20;
constexpr unsigned OFF_PART = 3u << 20;
constexpr unsigned OFF_X1 = 12u << 20;
constexpr unsigned OFF_O = 16u << 20;
constexpr unsigned OFF_KV = 32u << 20;
constexpr unsigned OFF_WI = 52u << 20;
constexpr unsigned WS_BYTES = 58u << 20;
constexpr unsigned POISON = 0xFFFFFFFFu;

constexpr int L_CSL = 31872, L_FXL = 32256, L_K = 0, L_V = 32768, L_RING = 65536, L_DN = 32768, L_TOUCH0 = 149504, L_RDY = 31744, L_QC = 31808, LDS_BYTES = 163840;
constexpr int STAGE = 24576;
#define SLOT(t) ((t) & 3)

template <typename F, int... I> __device__ __forceinline__ void static_for_impl(F&& f, std::integer_sequence<int, I...>) {
  (f(std::integral_constant<int, I>{}), ...);
}
template <int N, typename F> __device__ __forceinline__ void static_for(F&& f) { static_for_impl(f, std::make_integer_sequence<int, N>{}); }
__device__ __forceinline__ unsigned pack2(float a, float b) {
  f32x2 v = {a, b};
  return __builtin_bit_cast(unsigned, __builtin_convertvector(v, bf16x2));
}
__device__ __forceinline__ float asf(unsigned u) { return __builtin_bit_cast(float, u); }
__device__ __forceinline__ float lo16(unsigned u) { return __builtin_bit_cast(float, u << 16); }
__device__ __forceinline__ float hi16(unsigned u) { return __builtin_bit_cast(float, u & 0xffff0000u); }
__device__ __forceinline__ u32x4 pack8(const f32x4 a, const f32x4 b) {
  u32x4 o;
  o[0] = pack2(a[0], a[1]); o[1] = pack2(a[2], a[3]); o[2] = pack2(b[0], b[1]); o[3] = pack2(b[2], b[3]);
  return o;
}
__device__ __forceinline__ f32x4 mfma16(u32x4 a, u32x4 b, f32x4 c) {
  return __builtin_amdgcn_mfma_f32_16x16x32_bf16(__builtin_bit_cast(bf16x8, a), __builtin_bit_cast(bf16x8, b), c, 0, 0, 0);
}
__device__ __forceinline__ f32x4 mfma16k16(s16x4 a, s16x4 b, f32x4 c) { return __builtin_amdgcn_mfma_f32_16x16x16bf16_1k(a, b, c, 0, 0, 0); }
template <int CTRL> __device__ __forceinline__ float dppf(float v) {
  return __builtin_bit_cast(float, __builtin_amdgcn_update_dpp(0, __builtin_bit_cast(int, v), CTRL, 0xf, 0xf, true));
}
template <int CTRL> __device__ __forceinline__ unsigned dppu(unsigned v) {
  return (unsigned)__builtin_amdgcn_update_dpp(0, (int)v, CTRL, 0xf, 0xf, true);
}
__device__ __forceinline__ float add_x16(float v) {
  auto r = __builtin_amdgcn_permlane16_swap(__builtin_bit_cast(unsigned, v), __builtin_bit_cast(unsigned, v), false, false);
  return __builtin_bit_cast(float, (unsigned)r[0]) + __builtin_bit_cast(float, (unsigned)r[1]);
}
__device__ __forceinline__ float add_x32(float v) {
  auto r = __builtin_amdgcn_permlane32_swap(__builtin_bit_cast(unsigned, v), __builtin_bit_cast(unsigned, v), false, false);
  return __builtin_bit_cast(float, (unsigned)r[0]) + __builtin_bit_cast(float, (unsigned)r[1]);
}
__device__ __forceinline__ float max_x16(float v) {
  auto r = __builtin_amdgcn_permlane16_swap(__builtin_bit_cast(unsigned, v), __builtin_bit_cast(unsigned, v), false, false);
  return fmaxf(__builtin_bit_cast(float, (unsigned)r[0]), __builtin_bit_cast(float, (unsigned)r[1]));
}
__device__ __forceinline__ float max_x32(float v) {
  auto r = __builtin_amdgcn_permlane32_swap(__builtin_bit_cast(unsigned, v), __builtin_bit_cast(unsigned, v), false, false);
  return fmaxf(__builtin_bit_cast(float, (unsigned)r[0]), __builtin_bit_cast(float, (unsigned)r[1]));
}
__device__ __forceinline__ unsigned umax_x16(unsigned v) {
  auto r = __builtin_amdgcn_permlane16_swap(v, v, false, false);
  return max((unsigned)r[0], (unsigned)r[1]);
}
__device__ __forceinline__ unsigned umax_x32(unsigned v) {
  auto r = __builtin_amdgcn_permlane32_swap(v, v, false, false);
  return max((unsigned)r[0], (unsigned)r[1]);
}
__device__ __forceinline__ float sum_hi8(float v) {
  v += dppf<0x128>(v);
  v = add_x16(v);
  return add_x32(v);
}
__device__ __forceinline__ float sum32(float v) {
  v += dppf<0xB1>(v);
  v += dppf<0x4E>(v);
  v += dppf<0x124>(v);
  v += dppf<0x128>(v);
  return add_x16(v);
}
__device__ __forceinline__ float sum64(float v) {
  v += dppf<0xB1>(v);
  v += dppf<0x4E>(v);
  v += dppf<0x124>(v);
  v += dppf<0x128>(v);
  v = add_x16(v);
  return add_x32(v);
}

#define BAR() asm volatile("s_waitcnt lgkmcnt(0)\n\ts_barrier" ::: "memory")
#define DRAIN() asm volatile("s_waitcnt vmcnt(0)" ::: "memory")
#define SB() __builtin_amdgcn_sched_barrier(0)
template <int N> __device__ __forceinline__ void wait_vm() { asm volatile("s_waitcnt vmcnt(%0)" ::"n"(N) : "memory"); }

__device__ __forceinline__ void dma16(rsrc_t rs, unsigned voff, unsigned soff, unsigned lds) {
  asm volatile("s_mov_b32 m0, %0\n\ts_nop 0\n\tbuffer_load_dwordx4 %1, %2, %3 offen lds" ::"s"(lds), "v"(voff), "s"(rs), "s"(soff) : "memory");
}
template <int OFF> __device__ __forceinline__ void dma16s(rsrc_t rs, unsigned voff, unsigned soff, unsigned lds) {
  asm volatile("s_mov_b32 m0, %0\n\ts_nop 0\n\tbuffer_load_dwordx4 %1, %2, %3 offen offset:%4 sc1 lds" ::"s"(lds), "v"(voff), "s"(rs), "s"(soff), "n"(OFF) : "memory");
}
__device__ __forceinline__ void touch(unsigned& d, const GAS bf16* ptr) { asm volatile("global_load_dword %0, %1, off" : "+v"(d) : "v"(ptr) : "memory"); }
__device__ __forceinline__ void ldrows(u32x4 (&d)[4], rsrc_t rx, unsigned voff, unsigned soff) {
  asm volatile("buffer_load_dwordx4 %0, %4, %5, %6 offen\n\tbuffer_load_dwordx4 %1, %4, %5, %6 offen offset:512\n\t"
               "buffer_load_dwordx4 %2, %4, %5, %6 offen offset:1024\n\tbuffer_load_dwordx4 %3, %4, %5, %6 offen offset:1536"
               : "=&v"(d[0]), "=&v"(d[1]), "=&v"(d[2]), "=&v"(d[3]) : "v"(voff), "s"(rx), "s"(soff) : "memory");
}
__device__ __forceinline__ void touchb(unsigned& d, rsrc_t rs, unsigned vo) { asm volatile("buffer_load_dword %0, %1, %2, 0 offen sc1" : "+v"(d) : "v"(vo), "s"(rs) : "memory"); }
__device__ __forceinline__ float rbf1(float x) { return __builtin_bit_cast(float, pack2(0.f, x)); }
__device__ __forceinline__ u32x4 ld16(rsrc_t rs, unsigned voff, unsigned soff) { return __builtin_amdgcn_raw_buffer_load_b128(rs, voff, soff, SC1); }
template <int OFF> __device__ __forceinline__ void ld16h(u32x4& d, rsrc_t rs, unsigned voff, unsigned soff) {
  asm volatile("buffer_load_dwordx4 %0, %1, %2, %3 offen offset:%4 sc1" : "=v"(d) : "v"(voff), "s"(rs), "s"(soff), "n"(OFF) : "memory");
}
template <int I, int OFF> __device__ __forceinline__ void ld16w(u32x4& d, rsrc_t rs, unsigned voff, unsigned soff) {
  if constexpr (I == 0) asm volatile("buffer_load_dwordx4 %0, %1, %2, %3 offen offset:%4 sc1" : "={v[128:131]}"(d) : "v"(voff), "s"(rs), "s"(soff), "n"(OFF) : "memory");
  else if constexpr (I == 1) asm volatile("buffer_load_dwordx4 %0, %1, %2, %3 offen offset:%4 sc1" : "={v[132:135]}"(d) : "v"(voff), "s"(rs), "s"(soff), "n"(OFF) : "memory");
  else if constexpr (I == 2) asm volatile("buffer_load_dwordx4 %0, %1, %2, %3 offen offset:%4 sc1" : "={v[136:139]}"(d) : "v"(voff), "s"(rs), "s"(soff), "n"(OFF) : "memory");
  else if constexpr (I == 3) asm volatile("buffer_load_dwordx4 %0, %1, %2, %3 offen offset:%4 sc1" : "={v[140:143]}"(d) : "v"(voff), "s"(rs), "s"(soff), "n"(OFF) : "memory");
  else if constexpr (I == 4) asm volatile("buffer_load_dwordx4 %0, %1, %2, %3 offen offset:%4 sc1" : "={v[144:147]}"(d) : "v"(voff), "s"(rs), "s"(soff), "n"(OFF) : "memory");
  else if constexpr (I == 5) asm volatile("buffer_load_dwordx4 %0, %1, %2, %3 offen offset:%4 sc1" : "={v[148:151]}"(d) : "v"(voff), "s"(rs), "s"(soff), "n"(OFF) : "memory");
  else if constexpr (I == 6) asm volatile("buffer_load_dwordx4 %0, %1, %2, %3 offen offset:%4 sc1" : "={v[152:155]}"(d) : "v"(voff), "s"(rs), "s"(soff), "n"(OFF) : "memory");
  else if constexpr (I == 7) asm volatile("buffer_load_dwordx4 %0, %1, %2, %3 offen offset:%4 sc1" : "={v[156:159]}"(d) : "v"(voff), "s"(rs), "s"(soff), "n"(OFF) : "memory");
  else if constexpr (I == 8) asm volatile("buffer_load_dwordx4 %0, %1, %2, %3 offen offset:%4 sc1" : "={v[160:163]}"(d) : "v"(voff), "s"(rs), "s"(soff), "n"(OFF) : "memory");
  else if constexpr (I == 9) asm volatile("buffer_load_dwordx4 %0, %1, %2, %3 offen offset:%4 sc1" : "={v[164:167]}"(d) : "v"(voff), "s"(rs), "s"(soff), "n"(OFF) : "memory");
  else if constexpr (I == 10) asm volatile("buffer_load_dwordx4 %0, %1, %2, %3 offen offset:%4 sc1" : "={v[168:171]}"(d) : "v"(voff), "s"(rs), "s"(soff), "n"(OFF) : "memory");
  else if constexpr (I == 11) asm volatile("buffer_load_dwordx4 %0, %1, %2, %3 offen offset:%4 sc1" : "={v[172:175]}"(d) : "v"(voff), "s"(rs), "s"(soff), "n"(OFF) : "memory");
}
template <int K> __device__ __forceinline__ void pin_w(u32x4& a, u32x4& b) {
  if constexpr (K == 0) asm volatile("" : "+{v[128:131]}"(a), "+{v[132:135]}"(b)::"memory");
  else if constexpr (K == 1) asm volatile("" : "+{v[136:139]}"(a), "+{v[140:143]}"(b)::"memory");
  else if constexpr (K == 2) asm volatile("" : "+{v[144:147]}"(a), "+{v[148:151]}"(b)::"memory");
  else if constexpr (K == 3) asm volatile("" : "+{v[152:155]}"(a), "+{v[156:159]}"(b)::"memory");
  else if constexpr (K == 4) asm volatile("" : "+{v[160:163]}"(a), "+{v[164:167]}"(b)::"memory");
  else if constexpr (K == 5) asm volatile("" : "+{v[168:171]}"(a), "+{v[172:175]}"(b)::"memory");
}
__device__ __forceinline__ void ld8h(u32x2& d, rsrc_t rs, unsigned voff, unsigned soff) {
  asm volatile("buffer_load_dwordx2 %0, %1, %2, %3 offen sc1" : "=v"(d) : "v"(voff), "s"(rs), "s"(soff) : "memory");
}
__device__ __forceinline__ void ld16u(u32x4& d, rsrc_t rs, unsigned voff, unsigned soff) {
  asm volatile("buffer_load_dwordx4 %0, %1, %2, %3 offen sc1" : "+v"(d) : "v"(voff), "s"(rs), "s"(soff) : "memory");
}
template <int OFF> __device__ __forceinline__ void ld4h(unsigned& d, rsrc_t rs, unsigned voff, unsigned soff) {
  asm volatile("buffer_load_dword %0, %1, %2, %3 offen offset:%4 sc1" : "=v"(d) : "v"(voff), "s"(rs), "s"(soff), "n"(OFF) : "memory");
}
__device__ __forceinline__ void ld4x16(u32x4 (&d)[4], rsrc_t rs, unsigned voff, unsigned soff) {
  asm volatile("buffer_load_dwordx4 %0, %4, %5, %6 offen sc1\n\tbuffer_load_dwordx4 %1, %4, %5, %6 offen offset:64 sc1\n\t"
               "buffer_load_dwordx4 %2, %4, %5, %6 offen offset:128 sc1\n\tbuffer_load_dwordx4 %3, %4, %5, %6 offen offset:192 sc1\n\ts_waitcnt vmcnt(0)"
               : "=&v"(d[0]), "=&v"(d[1]), "=&v"(d[2]), "=&v"(d[3]) : "v"(voff), "s"(rs), "s"(soff) : "memory");
}
__device__ __forceinline__ u32x2 ld8(rsrc_t rs, unsigned voff, unsigned soff) { return __builtin_amdgcn_raw_buffer_load_b64(rs, voff, soff, SC1); }
__device__ __forceinline__ void st16(u32x4 v, rsrc_t rs, unsigned voff, unsigned soff) { __builtin_amdgcn_raw_buffer_store_b128(v, rs, voff + soff, 0, SC1); }
__device__ __forceinline__ void st8(u32x2 v, rsrc_t rs, unsigned voff, unsigned soff) { __builtin_amdgcn_raw_buffer_store_b64(v, rs, voff, soff, SC1); }

__device__ __forceinline__ unsigned ld4(rsrc_t rs, unsigned voff, unsigned soff) { return __builtin_amdgcn_raw_buffer_load_b32(rs, voff, soff, SC1); }
__device__ __forceinline__ void st4(unsigned v, rsrc_t rs, unsigned voff, unsigned soff) { __builtin_amdgcn_raw_buffer_store_b32(v, rs, voff, soff, SC1); }

constexpr unsigned kSpinCap = 1u << 16;
__device__ __forceinline__ bool spin_fail(unsigned& spins, rsrc_t rs, u32x2 T) {
  asm volatile("" ::: "memory");
  ++spins;
  if (spins > kSpinCap) { st8(T, rs, 0, F_TMO); return true; }
  if ((spins & 255u) == 0u) {
    const u32x2 v = ld8(rs, 0, F_TMO);
    if (v[0] == T[0] && v[1] == T[1]) return true;
  }
  __builtin_amdgcn_s_sleep(3);
  return false;
}
__device__ __forceinline__ void poll_flags2(rsrc_t rs, unsigned voff, u32x2 T) {
  for (unsigned spins = 0;;) {
    const u32x4 v = ld16(rs, voff, 0);
    if (__all(v[0] == T[0] && v[1] == T[1] && v[2] == T[0] && v[3] == T[1])) break;
    if (spin_fail(spins, rs, T)) break;
  }
  asm volatile("" ::: "memory");
  __builtin_amdgcn_fence(__ATOMIC_ACQUIRE, "wavefront");
}
__device__ __forceinline__ void poll_flags(rsrc_t rs, unsigned voff, unsigned soff, u32x2 T) {
  for (unsigned spins = 0;;) {
    const u32x2 v = ld8(rs, voff, soff);
    if (__all(v[0] == T[0] && v[1] == T[1])) break;
    if (spin_fail(spins, rs, T)) break;
  }
  asm volatile("" ::: "memory");
  __builtin_amdgcn_fence(__ATOMIC_ACQUIRE, "wavefront");
}

__device__ __forceinline__ void unpack8v(const u32x4 u, f32x2* v) {
#pragma unroll
  for (int j = 0; j < 4; ++j) v[j] = f32x2{lo16(u[j]), hi16(u[j])};
}
__device__ __forceinline__ u32x4 pack8v(const f32x2* v) {
  u32x4 o;
#pragma unroll
  for (int j = 0; j < 4; ++j) o[j] = pack2(v[j][0], v[j][1]);
  return o;
}
__device__ __forceinline__ float ln_rd(float var) { return __builtin_amdgcn_rcpf(rbf1(__builtin_amdgcn_sqrtf(rbf1(rbf1(var) + 1e-5f)))); }
__device__ __forceinline__ float hadd(f32x2 v) {
  float a = v[0];
  asm volatile("" : "+v"(a));
  return a + v[1];
}
__device__ __forceinline__ float add_g(float v) { return add_x32(add_x16(v)); }
__device__ __forceinline__ void add_g3(float& x, float& y, float& z) {
  auto a = __builtin_amdgcn_permlane16_swap(__builtin_bit_cast(unsigned, x), __builtin_bit_cast(unsigned, y), false, false);
  auto b = __builtin_amdgcn_permlane16_swap(__builtin_bit_cast(unsigned, z), __builtin_bit_cast(unsigned, z), false, false);
  const float r = asf(a[0]) + asf(a[1]), t = asf(b[0]) + asf(b[1]);
  auto c = __builtin_amdgcn_permlane32_swap(__builtin_bit_cast(unsigned, r), __builtin_bit_cast(unsigned, r), false, false);
  auto d = __builtin_amdgcn_permlane32_swap(__builtin_bit_cast(unsigned, t), __builtin_bit_cast(unsigned, t), false, false);
  const float u = asf(c[0]) + asf(c[1]);
  z = asf(d[0]) + asf(d[1]);
  auto e = __builtin_amdgcn_permlane16_swap(__builtin_bit_cast(unsigned, u), __builtin_bit_cast(unsigned, u), false, false);
  x = asf(e[0]); y = asf(e[1]);
}
__device__ __forceinline__ u32x4 trade16(u32x4 v) {
  auto a = __builtin_amdgcn_permlane16_swap(v[0], v[2], false, false);
  auto b = __builtin_amdgcn_permlane16_swap(v[1], v[3], false, false);
  return u32x4{(unsigned)a[0], (unsigned)b[0], (unsigned)a[1], (unsigned)b[1]};
}
__device__ __forceinline__ unsigned umax4(u32x4 v) { return max(max(v[0], v[1]), max(v[2], v[3])); }
__device__ __forceinline__ u32x2 unpoison2(u32x2 v) { return u32x2{min(v[0], POISON - 1), min(v[1], POISON - 1)}; }
__device__ __forceinline__ u32x4 unpoison4(u32x4 v) {
  return u32x4{min(v[0], POISON - 1), min(v[1], POISON - 1), min(v[2], POISON - 1), min(v[3], POISON - 1)};
}

typedef union { struct { s16x4 a, b; } hf; u32x4 v; } frag_t;
__device__ __forceinline__ s16x4 trread(const char* smem, unsigned off) {
  return __builtin_amdgcn_ds_read_tr16_b64_v4i16((LDS_AS s16x4*)(smem + off));
}
__device__ __forceinline__ s16x4 trabs(unsigned addr) { return __builtin_amdgcn_ds_read_tr16_b64_v4i16((LDS_AS s16x4*)(size_t)addr); }

constexpr int WA = 6;
constexpr int WB = 4, WR = 3;
#define CH 4, 7, 8, 13, 15, 20, 22, 27, 29
constexpr int CHV[9] = {CH};
constexpr int FSL = CHV[0], FS0 = CHV[1], FS0D = CHV[2], FS1 = CHV[3], FS1S = CHV[4];
constexpr int FXA = CHV[5], FXB = CHV[6], FXL = CHV[7], FXS = CHV[8];
constexpr int FXT = FXB >> 2;
constexpr int CSD = 3, POI = 4, RWS = 5, EIN = 8;
constexpr int ERQ = 16, CSL = 19, CSS = 22;
#define IMV 24
constexpr int IMG = IMV, KP0 = 26;
constexpr int PB0 = 10, PB1 = 24;
constexpr int ABL = 23, ABS = 27;
constexpr int XU = 3, XL = 2;
constexpr int x_slot(int u) { return u % XU == 0 ? 0 : u % XU == 1 ? 32768 : 49152; }
#define TCH 19, 7, 4, 27
constexpr int TCV[4] = {TCH}, TS0 = TCV[0], TSN = TCV[1], TW = TCV[2], TST = TCV[3];
constexpr int QL = 26;
constexpr int RXCD = 5;
constexpr int WDS = 17 - WB;
#define SLP 16, 24, 14, 42, 3, 4, 12
constexpr int SLV[7] = {SLP}, SDI0 = SLV[0], SDIR = SLV[1], GSL = SLV[2], RSL = SLV[3], KSL = SLV[4], XSL = SLV[5], OSL = SLV[6];
constexpr bool tile_piece(int s) { return s >= 3 && ((s - 3) >> 2) + 3 < 8 && ((s - 3) & 3) < 3; }
constexpr int a_req(int s, int n) {
  if (!(n & 1)) return -1;
  if (s <= 1) return 8 + 3 * s + (n >> 1);
  return n < 5 && s + 5 < 32 ? 2 * (s + 5) + (n >> 1) : -1;
}
constexpr int vm_n(int it, int s, int n) {
  int c = 0;
  if (n == 0) c += tile_piece(s);
  if (n == 4) c += (s == ABL) + (it ? (s == FSL ? 5 : 0) : (s == ERQ) + (s >= IMG && s < IMG + 3));
  if (!it) c += (s < 4 && n == 2) + (s == 0 && (n == 0 || n == 4)) + ((n == 3 || n == 5) && s + XL + XU < 32);
  else c += (a_req(s, n) >= 0) + ((s == 0 || s == 4) && n == 2);
  return c;
}
constexpr int vm_at(int it, int s, int n) {
  int c = it ? 8 : 14;
  for (int u = 0; u < 6 * s + n; ++u) c += vm_n(it, u / 6, u % 6);
  return c;
}
constexpr int vm_a(int it, int k) {
  if (k < 4) return 2 * k + 2;
  for (int u = 0; u < 192; ++u)
    if (a_req(u / 6, u % 6) == 2 * k + 1) return vm_at(it, (u + 1) / 6, (u + 1) % 6);
  return 0;
}
constexpr int vm_tile(int it, int t) { return t >= 3 ? vm_at(it, 4 * t - 7, 1) : t == 1 ? vm_at(0, 0, 5) : vm_at(0, 3, 3); }
constexpr int vm_x(int u) { return u == 2 ? 9 : u < XU + XL ? 12 + 2 * (u - XU) : vm_at(0, u - XU - XL + 1, 0); }

constexpr int wo_k(int it, int s) { return it || s < 4 ? 15 - s : s < 8 ? 7 - s : 19 - s; }
constexpr int wo_pos(int it, int s) { return 2 * s + 2 + (s >= WR ? 2 * it : 0); }
constexpr int wo_at(int it, int s) { return 2 * it + (s < WB - WR ? 2 * WR + 2 + 4 * s : s < 16 - WB ? 2 * (2 * WB - WR) + 2 * (s - WB + WR) : 32); }
template <int OFF> __device__ __forceinline__ void lds_count(unsigned ad) {
  asm volatile("s_mov_b64 exec, 1\n\tds_add_u32 %0, %1 offset:%2\n\ts_mov_b64 exec, -1" ::"v"(ad), "v"(1u), "n"(OFF) : "memory");
}
__device__ __forceinline__ void lds_look(char* c, unsigned want) {
  while ((unsigned)__builtin_amdgcn_readfirstlane(__hip_atomic_load((LDS_AS unsigned*)c, __ATOMIC_RELAXED, __HIP_MEMORY_SCOPE_WORKGROUP)) < want) {}
  asm volatile("" ::: "memory");
}
__device__ __forceinline__ void pin1(unsigned& a) { asm volatile("" : "+v"(a)); }
__device__ __forceinline__ void pin2(u32x4& a, u32x4& b) { asm volatile("" : "+v"(a), "+v"(b)::"memory"); }
__device__ __forceinline__ void pin2s(float& a, float& b) { asm volatile("" : "+v"(a), "+v"(b)); }
template <int OFF> __device__ __forceinline__ void lds2(u32x4& a, u32x4& b, unsigned ad) {
  asm volatile("ds_read_b128 %0, %2 offset:%3\n\tds_read_b128 %1, %2 offset:%4" : "=&v"(a), "=&v"(b) : "v"(ad), "n"(OFF), "n"(OFF + 1024) : "memory");
}
template <int N> __device__ __forceinline__ void lds_wait(u32x4& a, u32x4& b) { asm volatile("s_waitcnt lgkmcnt(%2)" : "+v"(a), "+v"(b) : "n"(N) : "memory"); }
__device__ __forceinline__ void pin2f(f32x2& a, f32x2& b) { asm volatile("" : "+v"(a), "+v"(b)); }
__device__ __forceinline__ void pin_ac(f32x4 (&ac)[2][4]) {
  asm volatile("" : "+{v[176:179]}"(ac[0][0]), "+{v[180:183]}"(ac[0][1]), "+{v[184:187]}"(ac[0][2]), "+{v[188:191]}"(ac[0][3]),
                    "+{v[192:195]}"(ac[1][0]), "+{v[196:199]}"(ac[1][1]), "+{v[200:203]}"(ac[1][2]), "+{v[204:207]}"(ac[1][3]));
}

constexpr int L_XC = LDS_BYTES - 64, L_SKEY = 32704, L_SRED = 4096, L_SFIN = 20480;
__device__ __forceinline__ unsigned key_load(rsrc_t rs, int l, int L, int rep) { return ld4(rs, (l & 15) * 4, OFF_KEY + (L * 16 + rep) * 128); }
__device__ __forceinline__ unsigned umax16(unsigned k) {
  k = max(k, dppu<0xB1>(k));
  k = max(k, dppu<0x4E>(k));
  k = max(k, dppu<0x141>(k));
  return max(k, dppu<0x140>(k));
}
__device__ __forceinline__ unsigned key_row(unsigned key) { return (0xffffu - (key & 0xffffu)) & 2047u; }
__device__ __forceinline__ unsigned key_wait(rsrc_t rs, u32x2 T, int l, int L, int rep, unsigned kv) {
  if (!__all(kv != POISON)) {
    unsigned spins = 0;
    do {
      if (spin_fail(spins, rs, T)) break;
      if (L == 0) __builtin_amdgcn_s_sleep(KSL);
      kv = key_load(rs, l, L, rep);
    } while (!__all(kv != POISON));
  }
  return __builtin_amdgcn_readfirstlane(umax16(kv));
}
__device__ __forceinline__ unsigned ffn_xoff(unsigned key, int L, int w) { return OFF_CA + L * 32768 + (key_row(key) >> 7) * 2048 + w * 256; }
__device__ __forceinline__ void row_load(rsrc_t rs, int g, unsigned xo, u32x4 (&xv)[4]) {
  ld16h<0>(xv[0], rs, g * 16, xo); ld16h<64>(xv[1], rs, g * 16, xo); ld16h<128>(xv[2], rs, g * 16, xo); ld16h<192>(xv[3], rs, g * 16, xo);
}
template <int SL = 0> __device__ __forceinline__ void ffn_xwait(rsrc_t rs, u32x2 T, unsigned xo, int l, u32x4 (&xv)[4]) {
#define XV_OK() __all(max(max(umax4(xv[0]), umax4(xv[1])), max(umax4(xv[2]), umax4(xv[3]))) != POISON)
  if (!XV_OK()) {
    unsigned spins = 0;
    do {
      if (spin_fail(spins, rs, T)) break;
      if (SL) __builtin_amdgcn_s_sleep(SL);
#pragma unroll
      for (int i = 0; i < 4; ++i) xv[i] = ld16(rs, (l >> 4) * 16 + i * 64, xo);
    } while (!XV_OK());
  }
#undef XV_OK
}
__device__ __forceinline__ void ffn_gu(const u32x4 (&gv)[4], const u32x4 (&uv)[4], const u32x4 (&xv)[4], f32x4& ga, f32x4& ua) {
  ga = mfma16(gv[0], xv[0], f32x4{0.f, 0.f, 0.f, 0.f}); ua = mfma16(uv[0], xv[0], f32x4{0.f, 0.f, 0.f, 0.f});
#pragma unroll
  for (int i = 1; i < 4; ++i) { ga = mfma16(gv[i], xv[i], ga); ua = mfma16(uv[i], xv[i], ua); }
}
__device__ __forceinline__ void ffn_gu_check(rsrc_t rs, u32x2 T, unsigned xo, int l, const u32x4 (&gv)[4], const u32x4 (&uv)[4], u32x4 (&xv)[4], f32x4& ga, f32x4& ua) {
  if (__builtin_expect(__any(ga[0] != ga[0]), 0)) {
    ffn_xwait(rs, T, xo, l, xv);
    ffn_gu(gv, uv, xv, ga, ua);
  }
}
__device__ __forceinline__ void ffn_gu_store(char* smem, int l, int w, f32x4 ga, f32x4 ua) {
  LDS_AS f32x4* sred = (LDS_AS f32x4*)(smem + L_SRED);
  if ((l & 15) == 0) { sred[w * 8 + (l >> 4)] = ga; sred[w * 8 + 4 + (l >> 4)] = ua; }
}
__device__ __forceinline__ f32x4 ffn_t_read(char* smem, int l) { return ((const LDS_AS f32x4*)(smem + L_SRED))[(l & 7) * 8 + ((l >> 3) & 1) * 4 + (l >> 4)]; }
__device__ __forceinline__ void ffn_d_read(char* smem, int l, int blk, s16x4 (&dfr)[8]) {
#pragma unroll
  for (int i = 0; i < 8; ++i) dfr[i] = trread(smem, L_DN + blk * 4096 + (l >> 4) * 1024 + ((l & 15) >> 2) * 256 + i * 32 + (l & 3) * 8);
}
__device__ __forceinline__ float ffn_d_sum(float t) { t += dppf<0xB1>(t); t += dppf<0x4E>(t); return t + dppf<0x141>(t); }
__device__ __forceinline__ u32x2 ffn_h(f32x4 t, int l) {
#pragma unroll
  for (int e = 0; e < 4; ++e) t[e] = ffn_d_sum(t[e]);
  const float a = (l & 1) ? t[1] : t[0], b = (l & 1) ? t[3] : t[2], s = (l & 2) ? b : a;
  const float gg = rbf1(s), uu = rbf1(dppf<0x128>(s));
  const float h = rbf1(gg * __builtin_amdgcn_rcpf(1.0f + __expf(-gg))) * uu;
  const unsigned h0 = pack2(h, dppf<0x55>(h)), h1 = pack2(dppf<0xAA>(h), dppf<0xFF>(h));
  return (l & 15) == 0 ? u32x2{h0, h1} : u32x2{0u, 0u};
}
__device__ __forceinline__ void ffn_d_mma(const s16x4 (&dfr)[8], u32x2& hh, f32x4& ya, f32x4& yb) {
#pragma unroll
  for (int i = 0; i < 8; ++i) {
    if (i & 1) yb = mfma16k16(dfr[i], __builtin_bit_cast(s16x4, hh), yb);
    else ya = mfma16k16(dfr[i], __builtin_bit_cast(s16x4, hh), ya);
    hh = u32x2{dppu<0x111>(hh[0]), dppu<0x111>(hh[1])};
  }
}
__device__ __forceinline__ void ffn_d_store(rsrc_t rs, int l, int G0, int L, int slice, f32x4 y) {
  const unsigned c = l & 15;
  st16(unpoison4(__builtin_bit_cast(u32x4, y)), rs, (G0 + (c >> 1)) * 32768 + slice * 128 + (c & 1) * 64 + (l >> 4) * 16, OFF_PART + L * (1u << 20));
}
__device__ __forceinline__ void red_load(rsrc_t rs, int tid, int L, unsigned G, unsigned key, u32x4 (&pv)[4], u32x2& cxn, u32x2& cx1) {
#pragma unroll
  for (int k = 0; k < 4; ++k) pv[k] = ld16(rs, min(tid + 512 * k, 1791) * 16, OFF_PART + L * (1u << 20) + G * 32768);
  cxn = ld8(rs, (tid & 3) * 16 + (tid & 4) * 2, OFF_CA + L * 32768 + (key_row(key) >> 7) * 2048 + G * 64);
  cx1 = ld8(rs, (tid & 3) * 16 + (tid & 4) * 2, OFF_CB + L * 32768 + (key_row(key) >> 7) * 2048 + G * 64);
}
__device__ __forceinline__ void red_sum(rsrc_t rs, char* smem, u32x2 T, int tid, int w, int L, unsigned G, unsigned key,
                                        u32x4 (&pv)[4], u32x2& cxn, u32x2& cx1) {
#define PV_OK() __all(max(max(max(umax4(pv[0]), umax4(pv[1])), max(umax4(pv[2]), umax4(pv[3]))), max(max(cxn[0], cxn[1]), max(cx1[0], cx1[1]))) != POISON)
  if (!PV_OK()) {
    unsigned spins = 0;
    do {
      if (spin_fail(spins, rs, T)) break;
      red_load(rs, tid, L, G, key, pv, cxn, cx1);
    } while (!PV_OK());
  }
#undef PV_OK
  f32x4 s = (__builtin_bit_cast(f32x4, pv[0]) + __builtin_bit_cast(f32x4, pv[1])) + __builtin_bit_cast(f32x4, pv[2]);
  if (w < 4) s += __builtin_bit_cast(f32x4, pv[3]);
  ((LDS_AS f32x4*)(smem + L_SFIN))[tid] = s;
}
template <int NW> __device__ __forceinline__ u32x2 red_final(char* smem, int l, unsigned key, u32x2 cxn, u32x2 cx1) {
  const LDS_AS f32x4* sf = (const LDS_AS f32x4*)(smem + L_SFIN) + l;
  f32x4 s = (sf[0] + sf[64]) + (sf[128] + sf[192]);
  if constexpr (NW == 8) s += (sf[256] + sf[320]) + (sf[384] + sf[448]);
#pragma unroll
  for (int j = 0; j < 4; ++j) s[j] = sum_hi8(s[j]);
  const unsigned k16 = key >> 16;
  const float rmax = __builtin_bit_cast(float, ((k16 & 0x8000u) ? (k16 & 0x7fffu) : (~k16 & 0xffffu)) << 16);
  const float sig = rbf1(__builtin_amdgcn_rcpf(1.0f + __expf(-rmax)));
  const float xn4[4] = {lo16(cxn[0]), hi16(cxn[0]), lo16(cxn[1]), hi16(cxn[1])};
  const float x14[4] = {lo16(cx1[0]), hi16(cx1[0]), lo16(cx1[1]), hi16(cx1[1])};
  float o[4];
#pragma unroll
  for (int j = 0; j < 4; ++j) o[j] = x14[j] + rbf1(xn4[j] + rbf1(rbf1(s[j]) * sig));
  return u32x2{pack2(o[0], o[1]), pack2(o[2], o[3])};
}

struct RowFin { float mean, var, mb, rd; unsigned key; };
__device__ __forceinline__ void fin_wait(rsrc_t rs, u32x2 T, unsigned voff, unsigned so, u32x4 (&sp)[4]) {
#define ST_OK() __all(max(max(umax4(sp[0]), umax4(sp[1])), max(umax4(sp[2]), umax4(sp[3]))) != POISON)
  if (!ST_OK()) {
    unsigned spins = 0;
    do {
      if (spin_fail(spins, rs, T)) break;
#pragma unroll
      for (int i = 0; i < 4; ++i) sp[i] = ld16(rs, voff + i * 2048, so);
    } while (!ST_OK());
  }
#undef ST_OK
}
__device__ __forceinline__ RowFin fin_key(rsrc_t rs, u32x2 T, unsigned voff, unsigned so, char* skey, int l, int w, unsigned R, float wsum, u32x4 (&sp)[4]) {
  RowFin r;
#define ST_SUM() ((__builtin_bit_cast(f32x4, sp[0]) + __builtin_bit_cast(f32x4, sp[1])) + (__builtin_bit_cast(f32x4, sp[2]) + __builtin_bit_cast(f32x4, sp[3])))
  f32x4 sf = ST_SUM();
  const float z = (sf[0] + sf[1]) + sf[2];
  if (__any(z != z)) {
    fin_wait(rs, T, voff, so, sp);
    sf = ST_SUM();
  }
#undef ST_SUM
  float s0 = sf[0], s1 = sf[1], s2 = sf[2];
  add_g3(s0, s1, s2);
  r.mean = s0 * (1.0f / 1024.0f);
  r.var = fmaxf(__builtin_fmaf(-r.mean, r.mean, s1 * (1.0f / 1024.0f)), 0.f);
  r.mb = rbf1(r.mean); r.rd = ln_rd(r.var);
  const unsigned rb16 = pack2((s2 - r.mb * wsum) * r.rd, 0.f) & 0xffffu;
  r.key = min((((rb16 & 0x8000u) ? (~rb16 & 0xffffu) : (rb16 | 0x8000u)) << 16) | (0xffffu - R), POISON - 1);
  const unsigned wk = umax16(r.key);
  if (l == 0) ((LDS_AS unsigned*)skey)[w] = wk;
  return r;
}
__device__ __forceinline__ unsigned fin_cand(rsrc_t rs, char* skey, int L, int g, int rb, int ct, const RowFin& r, const f32x2 (&v)[8]) {
  const u32x4 k0 = *(const LDS_AS u32x4*)skey, k1 = *(const LDS_AS u32x4*)(skey + 16);
  const unsigned best = max(umax4(k0), umax4(k1));
  if (r.key == best) {
    const unsigned co = L * 32768 + rb * 2048 + ct * 128 + g * 16;
    f32x2 t[8], rd2 = {r.rd, r.rd}, of2 = {-r.mb * r.rd, -r.mb * r.rd};
    asm volatile("" : "+v"(rd2), "+v"(of2));
#pragma unroll
    for (int j = 0; j < 8; ++j) t[j] = __builtin_elementwise_fma(v[j], rd2, of2);
    st16(unpoison4(pack8v(t)), rs, co, OFF_CA); st16(unpoison4(pack8v(t + 4)), rs, co + 64, OFF_CA);
    if (g == 0) st4(best, rs, OFF_KEY + (L * 16 + ct) * 128 + rb * 4, 0);
    st16(unpoison4(pack8v(v)), rs, co, OFF_CB); st16(unpoison4(pack8v(v + 4)), rs, co + 64, OFF_CB);
  }
  return best;
}
__device__ __forceinline__ void fin_x2(const RowFin& r, f32x2 (&v)[8]) {
  f32x2 sc2 = {1.0f + r.rd, 1.0f + r.rd}, of2 = {-r.mb * r.rd, -r.mb * r.rd};
  asm volatile("" : "+v"(sc2), "+v"(of2));
#pragma unroll
  for (int j = 0; j < 8; ++j) v[j] = __builtin_elementwise_fma(v[j], sc2, of2);
}

extern "C" __device__ unsigned long long dispatch_id() __asm("llvm.amdgcn.dispatch.id");

extern "C" __global__ __launch_bounds__(512, 2) void mega_kernel(const bf16* x, char* ws, const bf16* wqkv, const bf16* wo, const bf16* wr,
                                                                 const bf16* wg, const bf16* wu, const bf16* wd, bf16* out) {
  extern __shared__ __attribute__((aligned(16))) char smem[];
  const int w0_ = __builtin_amdgcn_readfirstlane(threadIdx.x >> 6);
  typedef unsigned long long u64x2 __attribute__((ext_vector_type(2)));
  u64x2 a01;
  unsigned long long a2;
  asm volatile("s_load_dwordx4 %0, %2, 0\n\ts_load_dwordx2 %1, %2, 16" : "=&s"(a01), "=&s"(a2) : "s"(__builtin_amdgcn_kernarg_segment_ptr()));
  const unsigned lbase = (unsigned)(size_t)(LDS_AS char*)smem;
  {
    unsigned long long pc;
    unsigned len;
    asm volatile("s_getpc_b64 %0\n\ts_mov_b32 %1, .Ltext_end-mega_kernel\n\t.subsection 1\n.Ltext_end:\n\t.subsection 0" : "=s"(pc), "=s"(len));
    const unsigned o = ((blockIdx.x >> 3) * 64 + (threadIdx.x & 63)) * 128;
    if (w0_ == 7 && o < len)
      asm volatile("s_mov_b32 m0, %0\n\ts_nop 0\n\tglobal_load_lds_dword %1, %2" ::"s"(lbase + L_TOUCH0), "v"(o), "s"(pc & ~127ull) : "memory");
  }
#define HGRP() ((xcd & 2) + (xcd >> 2))
#define IDS()                                                                                         \
  int tid = threadIdx.x, w = w0_, bid = blockIdx.x;                                                   \
  asm volatile("" : "+v"(tid));                                                                       \
  asm volatile("" : "+s"(w));                                                                         \
  asm volatile("" : "+s"(bid));                                                                       \
  const int l = tid & 63, xcd = bid & 7, bi = bid >> 3;                                               \
  const int p = (xcd & 1) * 4 + (bi & 3), h = HGRP() * 8 + (bi >> 2);         \
  const int rb = 2 * xcd + (bi & 1), ct = bi >> 1;                               \
  const int slice = (xcd - (xcd > RXCD)) * 32 + bi;                                            \
  const bool ffn_on = xcd != RXCD;                                                                    \
  const int g = l >> 4;                                                                               \
  const int rt = w < 4 ? w : 11 - w;          \
  KARGS();                                                                                            \
  (void)p; (void)h; (void)rb; (void)ct; (void)slice; (void)ffn_on; (void)g; (void)rt;
#define PROT() (2 * (p & 3) + (p >> 2))
  typedef const void* const __attribute__((address_space(4)))* karg_t;
#define KARGS() karg_t ka = (karg_t)__builtin_amdgcn_kernarg_segment_ptr(); asm volatile("" : "+s"(ka))
#define ARGP(name, i) const GAS bf16* name = (const GAS bf16*)ka[i]; asm volatile("" : "+s"(name))
#define RSRC(ptr, bytes) __builtin_amdgcn_make_buffer_rsrc((void*)(ptr), 0, bytes, 0x00020000)
#define rwq RSRC(pwq, 1024 * 3072 * 2)
#define rwo RSRC(pwo, 1024 * 1024 * 2)
#define rwg RSRC(pwg, 1024 * F_ * 2)
#define rwu RSRC(pwu, 1024 * F_ * 2)
#define rwd RSRC(pwd, 1024 * F_ * 2)
#define rout RSRC(pout, 2048 * 1024 * 2)

#define WQ_VOFF() ((l >> 2) * 6144 + (((l & 3) ^ ((((l >> 4) ^ (l >> 5)) & 1) << 1)) << 4))
#define WQ_KMAJ(q) (((q) % 3) * 8 + (q) / 3)
#define WQ_PIECE(t, j, dst) dma16(rwq, wqv, ((((t) + rot) & 7) * 128 + ((j) & 7) * 16) * 6144 + ((j) >> 3) * 2048 + h * 64, lbase + (dst) + (j) * 1024)
#define WQ_BOFF(dt) ((g >> 1) * 1024 + (8 * (g & 1) + ((l & 15) >> 2)) * 64 + (l & 3) * 8 + (((dt) ^ (g & 1)) << 5))
#define X_REQ(u, hf) dma16(rx, xv[hf], (p * 256 + rt * 32 + ((u) & 1) * 16 + (hf) * 8) * 2048 + ((((u) >> 2) + rot) & 7) * 256 + (((u) >> 1) & 1) * 128, lbase + rt * 2048 + x_slot(u) + (hf) * 1024)
#define WO_PIECE_OF(w, j) dma16(rwo, wov, (((w) >> 2) * 64 + ((w) & 3) + 4 * (j)) * 16384 + ct * 128, lbase + 32768 + (j) * 8192 + (w) * 1024)
#define WO_PIECE(j) WO_PIECE_OF(w, j)
#define WD_DMA_OF(v) _Pragma("unroll") for (int i = 0; i < 4; ++i)                                  \
      dma16(rwd, (l >> 4) * 2048 + (l & 15) * 16, (unsigned)(slice * 16 + 4 * i) * 2048 + (v) * 256, lbase + L_DN + (v) * 4096 + i * 1024)
#define FFN_DMA(m) dma16((m) & 1 ? rwu : rwg, fnv, (unsigned)(((4 * w + ((m) >> 1)) * 32) * F_ + slice * 16) * 2, lbase + 65536 + (m) * 8192 + w * 1024)
#define POISON_BIG(L)                                                                                \
  if (w >= 2) {                                                                                      \
    unsigned pz1 = POISON;                                                                           \
    asm volatile("" : "+v"(pz1));                                                                    \
    st16(u32x4{pz1, pz1, pz1, pz1}, rs, (w >= 4 ? OFF_PART + (L) * (1u << 20) + bid * 4096 - 4096 : OFF_STAT + (L) * 524288 + (p * 32 + h) * 2048 - 2048) + tid * 16, 0); \
  }
  u32x4 gv[4], uv[4];
  u32x4 keep[9];
  u32x4 pre[4][2];
#define PRE_LOAD(it_, rot_, s0, s1)                                                                  \
  static_for<(s1) - (s0)>([&](auto c_) __attribute__((always_inline)) {                              \
    constexpr int s = (s0) + decltype(c_)::value;                                                    \
    const unsigned ab_ = OFF_X1 + (((rot_) + (s >> 2)) & 7) * 8192 + ((s >> 1) & 1) * 4096; \
    ld16w<2 * s, (s & 1) * 2048>(pre[s][0], rs, l * 16, ab_ + (p * 8 + rt) * 65536);                         \
    ld16w<2 * s + 1, (s & 1) * 2048 + 1024>(pre[s][1], rs, l * 16, ab_ + (p * 8 + rt) * 65536);                 \
  })
  u32x2 T;
  const GAS bf16* tsrc;
  unsigned xv[2], xa[2];
  u32x2 ep;
  u32x4 rows0[4];
#define X_READ(u, d0, d1) { d0 = *(const LDS_AS u32x4*)(size_t)(xa[0] + x_slot(u)); d1 = *(const LDS_AS u32x4*)(size_t)(xa[1] + x_slot(u)); }
  unsigned g1[5], wqv0;
  {
    IDS();
    const int rot = PROT(), qa = WQ_KMAJ(w), qb = WQ_KMAJ(8 + w);
    wqv0 = WQ_VOFF();
    xv[0] = (l >> 3) * 2048 + (((l & 7) ^ (l >> 4)) << 4); xv[1] = xv[0] ^ 64;
    g1[0] = (rot * 128 + (qa & 7) * 16) * 6144 + (qa >> 3) * 2048 + h * 64; g1[1] = lbase + L_RING + qa * 1024;
    g1[2] = (rot * 128 + (qb & 7) * 16) * 6144 + (qb >> 3) * 2048 + h * 64; g1[3] = lbase + L_RING + qb * 1024;
    g1[4] = (p * 256 + rt * 32) * 2048 + rot * 256;
    asm volatile("" : "+v"(wqv0), "+v"(xv[0]), "+v"(xv[1]));
    asm volatile("s_waitcnt lgkmcnt(0)" : "+s"(a01), "+s"(a2), "+s"(g1[0]), "+s"(g1[1]), "+s"(g1[2]), "+s"(g1[3]), "+s"(g1[4]));
  }
  x = (const bf16*)a01[0]; ws = (char*)a01[1]; wqkv = (const bf16*)a2;
  const rsrc_t rs = __builtin_amdgcn_make_buffer_rsrc((void*)ws, 0, WS_BYTES, 0x00020000);
  const rsrc_t rx = RSRC(x, 2048 * 1024 * 2);
  {
    IDS();
    const int rot = PROT();
    const bf16* pwq = wqkv;
    const unsigned wqv = wqv0;
    dma16(rwq, wqv, g1[0], g1[1]);
#pragma unroll
    for (int i = 0; i < 4; ++i) dma16(rx, xv[i & 1], g1[4] + i * 16384, lbase + rt * 2048 + x_slot(i >> 1) + (i & 1) * 1024);
    dma16(rwq, wqv, g1[2], g1[3]);
    asm volatile("s_barrier" ::: "memory");
    WQ_PIECE(0, WQ_KMAJ(16 + w), L_RING);
    if (w == 6 && bi == 0) {
      ARGP(pwr, 4);
      asm volatile("s_mov_b32 m0, %0\n\ts_nop 0\n\tbuffer_load_dword %1, %2, 0 offen lds" ::"s"(lbase + L_TOUCH0 + 256), "v"((l & 15) * 128), "s"(RSRC(pwr, 2048)) : "memory");
    }
    X_REQ(2, 0); X_REQ(2, 1);
    asm volatile("buffer_load_dwordx2 %0, off, %1, 0 sc1" : "=v"(ep) : "s"(rs) : "memory");
    asm volatile("" : "=v"(rows0[0]), "=v"(rows0[1]), "=v"(rows0[2]), "=v"(rows0[3]));
    if (w < 4) ldrows(rows0, rx, (l >> 5) * 2048 + (l & 31) * 16, (p * 256 + h * 8 + 2 * w) * 2048);
    {
      ARGP(pwo, 3); ARGP(pwg, 5); ARGP(pwu, 6);
      tsrc = (w < 4 ? pwg : pwu) + ((bi * 32 + ((tid & 255) >> 3)) * F_ + (slice >> 5) * 512 + (tid & 7) * 64);
      if (!ffn_on) tsrc = pwo + (bi * 512 + tid) * 64;
      asm volatile("" : "+v"(tsrc));
    }
#pragma unroll
    for (int kk = 0; kk < 2; ++kk) {
      const int r = l & 15;
      xa[kk] = lbase + rt * 2048 + (r >> 3) * 1024 + (r & 7) * 128 + (((4 * kk + g) ^ (r >> 1)) << 4);
      asm volatile("" : "+v"(xa[kk]));
    }
    if (tid == 192) *(LDS_AS u32x4*)(smem + L_QC) = u32x4{0u, 0u, 0u, 0u};
    if (w < 4) wait_vm<8>(); else wait_vm<4>();
    X_READ(0, pre[0][0], pre[1][0]); X_READ(1, pre[0][1], pre[1][1]);
    asm volatile("s_waitcnt lgkmcnt(0)\n\ts_barrier" : "+v"(pre[0][0]), "+v"(pre[0][1]), "+v"(pre[1][0]), "+v"(pre[1][1])::"memory");
    X_REQ(3, 0); X_REQ(3, 1); X_REQ(4, 0); X_REQ(4, 1);
  }
  unsigned sel0 = 0x7FFFFFFFu;
  u32x4 x2p[2];
  unsigned xm0 = 0, kg0 = 0;

  auto layer = [&](auto it_c) __attribute__((always_inline)) {
    constexpr int it = decltype(it_c)::value;
    f32x4 acc[2][6];
    f32x2 qs2[2];
    u32x4 k0, k1, v0, v1;
    unsigned tdum = 0;
    {
      IDS();
      bool aff = false;
      unsigned aq[2];
      u32x4 av[2];
#define PIN_ACC() asm volatile("" : "+{v[176:179]}"(acc[0][0]), "+{v[180:183]}"(acc[0][1]), "+{v[184:187]}"(acc[0][2]), "+{v[188:191]}"(acc[0][3]), "+{v[192:195]}"(acc[0][4]), "+{v[196:199]}"(acc[0][5]), \
                               "+{v[200:203]}"(acc[1][0]), "+{v[204:207]}"(acc[1][1]), "+{v[208:211]}"(acc[1][2]), "+{v[212:215]}"(acc[1][3]), "+{v[216:219]}"(acc[1][4]), "+{v[220:223]}"(acc[1][5]))
#define PINV4(x) asm volatile("" : "+v"(x[0]), "+v"(x[1]), "+v"(x[2]), "+v"(x[3])::"memory")
      auto qkv_loop = [&](auto it_c) __attribute__((always_inline)) {
        constexpr int IT = decltype(it_c)::value;
        const unsigned abase = OFF_X1 + (p * 8 + rt) * 65536;
        const int rot = PROT() + 5 * IT;
        unsigned fkey = 0, kv, bq[2];
        u32x4 fx[4], bv[2], af[WA][2];
        u32x2 cxn, cx1;
        f32x4 sa, sb, cs4[2];
        f32x2 b2[2];
#define AB_OFF() (OFF_AB + IT * 16384 + (p * 8 + rt) * 128)
#define A_LOAD1(k, a) ld16w<(k) % WA * 2 + (a), ((k) & 1) * 2048 + (a) * 1024>(af[(k) % WA][a], rs, l * 16, abase + ((((k) >> 2) + rot) & 7) * 8192 + (((k) >> 1) & 1) * 4096)
#pragma unroll
        for (int s = 0; s < 2 + 2 * IT; ++s) { af[s][0] = pre[s][0]; af[s][1] = pre[s][1]; }
        const bf16* pwq = wqkv;
        const int tstep = TS0 + bid % TSN;
        const unsigned wqv = WQ_VOFF();
#define QKV_DMA1(t, i)                                                                            \
  {                                                                                               \
    if constexpr (IT == 0) WQ_PIECE(t, 3 * w + (i), L_RING + SLOT(t) * STAGE);                    \
    else dma16s<(i) * 1024>(rs, l * 16, OFF_WI + (h * 8 + (((t) + rot) & 7)) * STAGE + w * 3072, lbase + L_RING + SLOT(t) * STAGE + w * 3072); \
  }
        unsigned bo[2][2];
#pragma unroll
        for (int dt = 0; dt < 2 - IT; ++dt) {
          bo[dt][0] = lbase + L_RING + (IT ? l * 16 : WQ_BOFF(dt)); bo[dt][1] = bo[dt][0] + 2 * STAGE;
          asm volatile("" : "+v"(bo[dt][0]), "+v"(bo[dt][1]));
        }
#define FRAG_T() (lbase + L_RING + (4 * g + ((l & 15) >> 2)) * 64 + (l & 3) * 8 + (((g ^ (g >> 1)) & 1) << 5))
        auto frag_t2 = [&](unsigned o) __attribute__((always_inline)) {
          frag_t t;
          t.hf.a = trabs(o); t.hf.b = trabs(o + 1024);
          return t.v;
        };
        unsigned ka[3];
        auto tile_sum = [&](u32x4 pt, int dst) __attribute__((always_inline)) {
          f32x4 c = __builtin_bit_cast(f32x4, pt);
#pragma unroll
          for (int e = 0; e < 4; ++e) { c[e] += dppf<0x124>(c[e]); c[e] += dppf<0x128>(c[e]); c[e] = add_x16(c[e]); }
          if ((l & 28) == 0) *(LDS_AS f32x4*)(smem + dst + (w * 2 + (l >> 5)) * 64 + (l & 3) * 16) = c;
        };
        frag_t bfr[6];
#define QKV_BO(s, n) (bo[(n) & 1][SLOT((s) >> 2) >> 1] + (SLOT((s) >> 2) & 1) * STAGE + ((n) >> 1) * 8192 + ((s) & 3) * 2048)
#define QKV_RDA(s, n)                                                                             \
  {                                                                                               \
    if constexpr (IT == 0) bfr[n].hf.a = trabs(QKV_BO(s, n));                                     \
    else bfr[n].v = *(const LDS_AS u32x4*)(size_t)(bo[0][SLOT((s) >> 2) >> 1] + (SLOT((s) >> 2) & 1) * STAGE + (((s) & 3) * 6 + (n)) * 1024); \
  }
#define QKV_RDB(s, n) { if constexpr (IT == 0) bfr[n].hf.b = trabs(QKV_BO(s, (n) ^ 1) + 256); }
#pragma unroll
        for (int n = 0; n < 6; ++n) { QKV_RDA(0, n); if (n < 5) QKV_RDB(0, n); }
#define XF_OFF() (l & 8 ? ((l & 7) * RX + bi % RX) * 256 + h * 8 : (p * RX + (xcd >> 1) % RX) * 256 + (4 * rt + (l & 3)) * 8)
#define XP_OFF() (OFF_XNEW + ((rot + FXT) & 7) * 256)
#define FP_OFF(i) (w == 3 ? OFF_XNEW + (i) * 1024 : OFF_FX + h * 3072 + w * 1024)
#define FX_OK(n) __all(((n) == 4 ? max(max(umax4(fx[0]), umax4(fx[1])), max(umax4(fx[2]), umax4(fx[3]))) : max(umax4(fx[0]), umax4(fx[1]))) != POISON)
#define SINCE(s0) (vm_at(IT, s, n) - vm_at(IT, s0, 5))
#define DEF4(x) asm volatile("" : "=v"(x[0]), "=v"(x[1]), "=v"(x[2]), "=v"(x[3]))
        LDS_AS unsigned* qcnt = (LDS_AS unsigned*)(smem + L_QC);
        unsigned qc;
        unsigned qadr = lbase + L_QC, qone = 1;
        asm volatile("" : "+v"(qadr), "+v"(qone));
#define count_in(i) asm volatile("s_mov_b64 exec, 1\n\tds_add_u32 %0, %1 offset:%2\n\ts_mov_b64 exec, -1" ::"v"(qadr), "v"(qone), "n"(4 * (i)) : "memory")
        auto count_wait = [&](int i, unsigned want) __attribute__((always_inline)) {
          if (__builtin_expect((unsigned)__builtin_amdgcn_readfirstlane(qc) < want, 0)) {
            while ((unsigned)__builtin_amdgcn_readfirstlane(__hip_atomic_load(qcnt + i, __ATOMIC_RELAXED, __HIP_MEMORY_SCOPE_WORKGROUP)) < want) {}
          }
          asm volatile("" ::: "memory");
        };
        unsigned long long xok = xm0;
        auto x1_need = [&](unsigned long long m) __attribute__((always_inline)) {
          if (__builtin_expect((xok & m) != m, 0)) {
            for (unsigned spins = 0;;) {
              const u32x2 f = ld8(rs, F_X1 + ((p * RX + (xcd >> 1) % RX) * 32 + (l & 31)) * 8, 0);
              xok = __builtin_amdgcn_ballot_w64(f[0] == T[0] && f[1] == T[1]);
              if ((xok & m) == m) break;
              if (spin_fail(spins, rs, T)) { xok = ~0ull; break; }
              __builtin_amdgcn_s_sleep(KSL);
            }
            asm volatile("" ::: "memory");
          }
        };
        auto gap = [&](auto sc_, auto nc_) __attribute__((always_inline)) {
          constexpr int s = decltype(sc_)::value, n = decltype(nc_)::value;
          {
            constexpr int ts = (s + 3) / 4, tc = (s + 2) / 4, t0 = 1;
            if constexpr (ts >= 3 && ts < 8 && s == 4 * ts - 3 && n == 2) { wait_vm<vm_at(IT, s, n) - vm_tile(IT, ts)>(); count_in(0); }
            if constexpr (!IT && tc >= t0 && tc < 3 && s == 4 * tc - 2 && n == 1) { wait_vm<vm_at(IT, s, n) - vm_tile(IT, tc)>(); count_in(0); }
            if constexpr (IT && ts >= t0 && ts < 3 && s == 4 * ts - 3 && n == 2) count_in(0);
            if constexpr (tc >= t0 && tc < 8 && s == 4 * tc - 2) {
              if constexpr (n == 3) qc = __hip_atomic_load(qcnt, __ATOMIC_RELAXED, __HIP_MEMORY_SCOPE_WORKGROUP);
              if constexpr (n == 5) count_wait(0, 8 * (tc - t0 + 1));
            }
          }
          if constexpr (n == 0 && tile_piece(s)) QKV_DMA1(((s - 3) >> 2) + 3, (s - 3) & 3);
          if constexpr (IT == 0) {
            if constexpr (s == 0 && !(n & 1)) QKV_DMA1(1, n >> 1);
            if constexpr (s >= 1 && s < 4 && n == 2) QKV_DMA1(2, s - 1);
            constexpr int u = s + XL;
            if constexpr (n == 1 && u < 32) {
              if (s == 0 && w < 4) wait_vm<vm_at(0, s, n) - vm_x(u) + 4>();
              else if (s > TST && w >= TW) wait_vm<vm_at(0, s, n) - vm_x(u) + 1>();
              else wait_vm<vm_at(0, s, n) - vm_x(u)>();
              X_READ(u, af[(u & ~1) % WA][u & 1], af[(u | 1) % WA][u & 1]);
            }
            if constexpr (n == 3 && u + XU < 32) { asm volatile("s_waitcnt lgkmcnt(%0)" ::"n"(s + 1 < QL ? 4 : 3) : "memory"); X_REQ(u + XU, 0); }
            if constexpr (n == 5 && u + XU < 32) X_REQ(u + XU, 1);
            if constexpr (s == PB0 && n == 0) { POISON_BIG(0); }
            if constexpr (s == 0 && n == 1) count_in(2);
            if constexpr (s == 0 && n == 3) qc = __hip_atomic_load(qcnt + 2, __ATOMIC_RELAXED, __HIP_MEMORY_SCOPE_WORKGROUP);
            if constexpr (s == 0 && n == 5) count_wait(2, 8);
            if constexpr (s == 1 && n == 1) {
              asm volatile("" : "+v"(ep)::"memory");
              const unsigned long long e = (((unsigned long long)(unsigned)__builtin_amdgcn_readfirstlane(ep[1]) << 32) | (unsigned)__builtin_amdgcn_readfirstlane(ep[0])) + 1ull + dispatch_id() * 0x9E3779B97F4A7C16ull;
              T = u32x2{(unsigned)e, (unsigned)(e >> 32)};
            }
            if constexpr (s == RWS && n == 0) {
              if (w < 4) {
                PINV4(rows0);
                f32x2 sv[16];
#pragma unroll
                for (int i = 0; i < 4; ++i) unpack8v(rows0[i], sv + 4 * i);
                f32x2 t0 = sv[0] + sv[1], t1 = sv[2] + sv[3];
#pragma unroll
                for (int j = 4; j < 16; j += 2) { t0 += sv[j]; t1 += sv[j + 1]; }
                const float m = sum32(hadd(t0 + t1)) * (1.0f / 1024.0f);
                f32x2 m2 = {m, m}, q0 = {0.f, 0.f}, q1 = {0.f, 0.f};
                asm volatile("" : "+v"(m2));
#pragma unroll
                for (int j = 0; j < 16; j += 2) {
                  const f32x2 d0 = sv[j] - m2, d1 = sv[j + 1] - m2;
                  q0 = __builtin_elementwise_fma(d0, d0, q0); q1 = __builtin_elementwise_fma(d1, d1, q1);
                }
                const float rd = ln_rd(sum32(hadd(q0 + q1)) * (1.0f / 1024.0f));
                if ((l & 30) == 0) st4(__builtin_bit_cast(unsigned, (l & 1) ? -rbf1(m) : rd), rs, (l >> 5) * 4 + (l & 1) * 8192, OFF_AB + (p * 256 + h * 8 + 2 * w) * 4);
              }
            }
            if constexpr (s == POI && n == 4) {
              if (w == 3) {
                unsigned pz1 = POISON;
                asm volatile("" : "+v"(pz1));
                const unsigned prb = 2 * p + (h >> 4), pct = h & 15, m = l - 32;
                static_assert(OFF_CB == OFF_CA + 131072, "poison addressing");
                if (l < 60) st16(u32x4{pz1, pz1, pz1, pz1}, rs, l < 32 ? OFF_CA + (l & 8) * 4096 + (l & 16) * 8192 + prb * 2048 + pct * 128 + (l & 7) * 16
                                                             : l < 56 ? OFF_FX + ((h * 6 + (m >> 2)) * 8 + p) * 64 + (l & 3) * 16
                                                                      : OFF_AB + 16384 + (p * 32 + h) * 32 + (l & 1) * 16 + (l & 2) * 4096, 0);
                if (l < 4) st4(pz1, rs, l < 2 ? OFF_XNEW + bid * 8 + l * 4 : OFF_KEY + ((l & 1) * 16 + pct) * 128 + prb * 4, 0);
              }
            }
            if constexpr (s == POI && n == 2) {
              if (w == 2 && l < 6) {
                unsigned f0 = F_KV + KVF(0, p * 32 + h), f1 = F_O + OFB(0, p) + OFL(h), f2 = F_X1 + ((rb >> 1) * RX * 32 + (l & 3) * 32 + (rb & 1) * 16 + ct) * 8;
                asm volatile("" : "+v"(f0), "+v"(f1), "+v"(f2));
                const unsigned fo = l == 5 ? f0 : l == 4 ? f1 : f2;
                touchb(tdum, rs, fo);
              }
            }
            static_assert(vm_x(EIN + XL) >= vm_at(0, EIN - 3, 5) && IMG >= 1 && CSD >= 1, "first flag");
            if constexpr (s == EIN && n == 1) { if (w < 4) count_in(1); }
            if constexpr (s == EIN + 2 && n == 0) qc = __hip_atomic_load(qcnt + 1, __ATOMIC_RELAXED, __HIP_MEMORY_SCOPE_WORKGROUP);
            if constexpr (s == EIN + 2 && n == 2) {
              if (w == 0) { count_wait(1, 4); if (l < RX) st8(T, rs, F_XN + (p * RX + l) * 256 + h * 8, 0); }
            }
            if constexpr (s == ERQ && n == 4) asm volatile("buffer_load_dwordx2 %0, %1, %2, 0 offen sc1" : "=v"(cxn) : "v"(XF_OFF() + F_XN), "s"(rs) : "memory");
            if constexpr (s == CSL && n == 2) {
              wait_vm<SINCE(ERQ)>();
              asm volatile("" : "+v"(cxn)::"memory");
              if (!__all(cxn[0] == T[0] && cxn[1] == T[1])) poll_flags(rs, XF_OFF(), F_XN, T);
            }
          } else {
#define X1_OFF() (F_X1 + ((p * RX + (xcd >> 1) % RX) * 32 + (l & 31)) * 8)
            if constexpr ((s == 0 || s == 4) && n == 2) asm volatile("buffer_load_dwordx2 %0, %1, %2, 0 offen sc1" : "=v"(cxn) : "v"(X1_OFF()), "s"(rs) : "memory");
            if constexpr ((s == 3 || s == 7) && n == 0) {
              wait_vm<vm_at(1, s, 1) - vm_at(1, s - 3, 3)>();
              asm volatile("" : "+v"(cxn)::"memory");
              xok |= __builtin_amdgcn_ballot_w64(cxn[0] == T[0] && cxn[1] == T[1]);
            }
            if constexpr (a_req(s, n) >= 0 && (a_req(s, n) & 7) == 0) x1_need(0x30003ull << (((rot + a_req(s, n) / 8) & 7) * 2));
          }
          if constexpr (IT && a_req(s, n) >= 0) A_LOAD1(a_req(s, n) / 2, a_req(s, n) & 1);
          if constexpr (IT == 0 && s == CSD && n == 4) {
            if (w < 3) {
              unsigned a0 = FRAG_T() + SLOT(s >> 2) * STAGE + w * 8192, a1 = a0 ^ 32, one2 = 0x3F803F80u;
              asm volatile("" : "+v"(a0), "+v"(a1), "+v"(one2));
              const u32x4 o4 = {one2, one2, one2, one2};
              f32x4 y0 = {0.f, 0.f, 0.f, 0.f}, y1 = y0;
#pragma unroll
              for (int i = 0; i < 4; ++i) { y0 = mfma16(frag_t2(a0 + i * 2048), o4, y0); y1 = mfma16(frag_t2(a1 + i * 2048), o4, y1); }
              if ((l & 14) == 0) st16(__builtin_bit_cast(u32x4, (l & 1) ? y1 : y0), rs, g * 16 + (l & 1) * 512, OFF_CS + ((h * 6 + 2 * w) * 8 + p) * 64);
            }
          }
          if constexpr (!IT && s == CSL && n == 4) { asm volatile("" : "=v"(fx[0])); if (w < 3) ld16h<0>(fx[0], rs, l * 16, OFF_CS + h * 3072 + w * 1024); }
          if constexpr (!IT && s == CSS && n == 2) {
            if (w < 3) {
              wait_vm<SINCE(CSL)>();
              asm volatile("" : "+v"(fx[0])::"memory");
              tile_sum(fx[0], L_CSL);
            }
          }
          if constexpr (s == ABL && n == 4) ld4h<0>(aq[0], rs, (l & 31) * 4 + (l >> 5) * 8192, AB_OFF());
          if constexpr (s == ABS && n == 0) {
            wait_vm<SINCE(ABL)>();
            asm volatile("" : "+v"(aq[0])::"memory");
            if constexpr (IT) {
              for (unsigned spins = 0; !__all(aq[0] != POISON) && !spin_fail(spins, rs, T);) {
                ld4h<0>(aq[0], rs, (l & 31) * 4 + (l >> 5) * 8192, AB_OFF());
                asm volatile("s_waitcnt vmcnt(0)" : "+v"(aq[0])::"memory");
              }
            }
            *(LDS_AS unsigned*)(smem + L_K + (8 + rt) * 2048 + l * 4) = aq[0];
          }
          if constexpr (s == ABS && n == 2) {
#pragma unroll
            for (int a = 0; a < 2; ++a) {
              const LDS_AS char* ab = (const LDS_AS char*)(smem + L_K + (8 + rt) * 2048 + a * 64);
              aq[a] = *(const LDS_AS unsigned*)(ab + (l & 15) * 4); bq[a] = *(const LDS_AS unsigned*)(ab + 128 + (l & 15) * 4);
              av[a] = *(const LDS_AS u32x4*)(ab + g * 16); bv[a] = *(const LDS_AS u32x4*)(ab + 128 + g * 16);
            }
          }
          if constexpr (s == ABS && n == 5) {
#pragma unroll
            for (int a = 0; a < 2; ++a) { b2[a] = f32x2{asf(bq[a]), asf(bq[a])}; asm volatile("" : "+v"(b2[a])); }
          }
          if constexpr (s == ABS + 1) {
            constexpr int k = (n + 4) % 6;
            if constexpr (k < 4) cs4[k & 1] = *(const LDS_AS f32x4*)(smem + L_CSL + k * 64 + g * 16);
            else cs4[k & 1][0] = *(const LDS_AS float*)(smem + L_CSL + k * 64 + (l & 15) * 4);
          }
          if constexpr ((s == ABS + 1 && n > 0) || (s == ABS + 2 && n == 0)) {
            constexpr int k = (n + 3) % 6;
            if constexpr (k < 4) {
#pragma unroll
              for (int a = 0; a < 2; ++a) acc[a][k] = __builtin_elementwise_fma(cs4[k & 1], f32x4{b2[a][0], b2[a][1], b2[a][0], b2[a][1]}, acc[a][k]);
            } else {
              f32x2 c2 = {cs4[k & 1][0], cs4[k & 1][0]};
              asm volatile("" : "+v"(c2));
#pragma unroll
              for (int a = 0; a < 2; ++a) acc[a][k] = __builtin_elementwise_fma(f32x4{c2[0], c2[1], c2[0], c2[1]}, __builtin_bit_cast(f32x4, bv[a]), acc[a][k]);
            }
          }
          if constexpr (IT == 0) {
            if constexpr (s == IMG - 2 && n < 3) {
              const int c = 3 * (w & 1) + n;
              ka[n] = (FRAG_T() ^ ((c & 1) << 5)) + (SLOT(5) * STAGE + (c >> 1) * 8192 + (w >> 1) * 2048);
              asm volatile("" : "+v"(ka[n]));
            }
            if constexpr (s == IMG - 1 && (n & 1)) keep[n >> 1] = frag_t2(ka[n >> 1]);
            if constexpr (s > KP0 && s < KP0 + 3 && (n & 1)) keep[3 * (s - KP0) + (n >> 1)] = frag_t2(ka[n >> 1] + (s - KP0) * STAGE);
            if constexpr (s >= IMG && s < IMG + 3 && n == 4) st16(keep[s - IMG], rs, l * 16 + (s - IMG) * 1024, OFF_WI + (h * 8 + ((rot + 5) & 7)) * STAGE + w * 3072);
            if constexpr (s >= TS0 && s < TS0 + TSN && n == 4) {
              if (tstep == s && w < TW) touch(tdum, tsrc);
            }
            if constexpr (s == TST && n == 4) { if (w >= TW) touch(tdum, tsrc); }
          } else {
            if constexpr (s == PB1 && n == 0) { POISON_BIG(1); }
            if constexpr (s == FSL && n == 4) { ld4h<0>(kv, rs, (l & 15) * 4, OFF_KEY + (bid & 15) * 128); row_load(rs, g, ffn_xoff(kg0, 0, w), fx); }
            if constexpr (s == FS0 && n == 0) {
              wait_vm<SINCE(FSL)>();
              asm volatile("" : "+v"(kv)::"memory");
              PINV4(fx);
              if (__builtin_expect(!__all(kv != POISON), 0)) {
                LDS_AS unsigned* kw = (LDS_AS unsigned*)(smem + L_SKEY + 40);
                if (w == 0) {
                  kv = key_wait(rs, T, l, 0, bid & 15, kv);
                } else {
                  while ((kv = __hip_atomic_load(kw, __ATOMIC_RELAXED, __HIP_MEMORY_SCOPE_WORKGROUP)) == POISON) __builtin_amdgcn_s_sleep(2);
                }
              }
              fkey = __builtin_amdgcn_readfirstlane(umax16(kv));
              if (w == 0) *(LDS_AS unsigned*)(smem + L_SKEY + 40) = min(fkey, POISON - 1);
              sel0 = key_row(fkey);
              aff = (int)(sel0 >> 8) == p;
              asm volatile("" : "=v"(sa), "=v"(sb));
              if (ffn_on) {
                if (__builtin_expect(((kg0 ^ fkey) & 0x780u) != 0, 0)) ld4x16(fx, rs, g * 16, ffn_xoff(fkey, 0, w));
                ffn_gu(gv, uv, fx, sa, sb);
              }
            }
            if constexpr (s == FS0 && n == 2) {
              if (ffn_on) {
                ffn_gu_check(rs, T, ffn_xoff(fkey, 0, w), l, gv, uv, fx, sa, sb);
                ffn_gu_store(smem, l, w, sa, sb);
                count_in(1);
              }
            }
            if constexpr (s == FS0D && n == 0) {
              if (ffn_on && w < 4) {
                s16x4 da[8], db[8];
                ffn_d_read(smem, l, 2 * w, da);
                while (__hip_atomic_load(qcnt + 1, __ATOMIC_RELAXED, __HIP_MEMORY_SCOPE_WORKGROUP) < 8u) {}
                asm volatile("" ::: "memory");
                const f32x4 t = ffn_t_read(smem, l);
                ffn_d_read(smem, l, 2 * w + 1, db);
                u32x2 ha = ffn_h(t, l), hb = {dppu<0x118>(ha[0]), dppu<0x118>(ha[1])};
                f32x4 ya = {0.f, 0.f, 0.f, 0.f}, yb = ya;
#pragma unroll
                for (int i = 0; i < 8; ++i) {
                  ya = mfma16k16(da[i], __builtin_bit_cast(s16x4, ha), ya); yb = mfma16k16(db[i], __builtin_bit_cast(s16x4, hb), yb);
                  ha = u32x2{dppu<0x111>(ha[0]), dppu<0x111>(ha[1])}; hb = u32x2{dppu<0x111>(hb[0]), dppu<0x111>(hb[1])};
                }
                ffn_d_store(rs, l, 8 * w, 0, slice, ya + yb);
              }
            }
#define RED_LOAD(ld, l8)                                                                                                    \
  {                                                                                                                         \
    const unsigned G = bi, po = OFF_PART + G * 32768;                                                        \
    ld(gv[0], rs, tid * 16, po); ld(gv[1], rs, tid * 16, po + 4096); ld(gv[2], rs, tid * 16, po + 8192); ld(gv[3], rs, tid * 16, po + 12288); \
    ld(uv[0], rs, tid * 16, po + 16384); ld(uv[1], rs, tid * 16, po + 20480); ld(uv[2], rs, tid * 16, po + 24576);        \
    l8(cxn, rs, (l & 3) * 16 + (l & 4) * 2, OFF_CA + (sel0 >> 7) * 2048 + G * 64);                                         \
    l8(cx1, rs, (l & 3) * 16 + (l & 4) * 2, OFF_CB + (sel0 >> 7) * 2048 + G * 64);                                          \
  }
            if constexpr (s == FS1 && n == 4) {
              asm volatile("" : "=v"(cxn), "=v"(cx1));
              if (!ffn_on && w < 4) RED_LOAD(ld16u, ld8h);
            }
            if constexpr (s == FS1S && n == 0) {
              if (!ffn_on && w < 4) {
                wait_vm<SINCE(FS1)>();
                PINV4(gv); PINV4(uv);
                asm volatile("" : "+v"(cxn), "+v"(cx1)::"memory");
                for (unsigned spins = 0; !__all(max(max(max(umax4(gv[0]), umax4(gv[1])), max(umax4(gv[2]), umax4(gv[3]))),
                                                    max(max(umax4(uv[0]), umax4(uv[1])), max(max(umax4(uv[2]), max(cxn[0], cxn[1])), max(cx1[0], cx1[1])))) != POISON) && !spin_fail(spins, rs, T);) {
                  RED_LOAD(ld16u, ld8h);
                  DRAIN();
                  PINV4(gv); PINV4(uv);
                  asm volatile("" : "+v"(cxn), "+v"(cx1)::"memory");
                }
#define F4(x) __builtin_bit_cast(f32x4, x)
                ((LDS_AS f32x4*)(smem + L_SFIN))[tid] = ((F4(gv[0]) + F4(gv[1])) + (F4(gv[2]) + F4(gv[3]))) + ((F4(uv[0]) + F4(uv[1])) + F4(uv[2]));
#undef F4
                count_in(2);
                if (w == 0) {
                  while (__hip_atomic_load(qcnt + 2, __ATOMIC_RELAXED, __HIP_MEMORY_SCOPE_WORKGROUP) < 4u) {}
                  asm volatile("" ::: "memory");
                  const u32x2 ov = red_final<4>(smem, l, fkey, cxn, cx1);
                  if (l < 8) st8(unpoison2(ov), rs, OFF_XNEW + (bi * 8 + (l & 3) * 2 + (l >> 2)) * 8, 0);
                }
              }
            }
            if constexpr (s == FXA && n == 4) { DEF4(fx); if (w < 3) row_load(rs, g, XP_OFF(), fx); }
            if constexpr (s == FXB && n == 0) {
              if (w < 3) {
                const unsigned fa = bo[0][0] + SLOT(FXT) * STAGE + w * 2048;
                u32x4 f[8];
#pragma unroll
                for (int i = 0; i < 8; ++i) f[i] = *(const LDS_AS u32x4*)(size_t)(fa + (i >> 1) * 6144 + (i & 1) * 1024);
                wait_vm<SINCE(FXA)>();
                PINV4(fx);
                ffn_xwait<XSL>(rs, T, XP_OFF(), l, fx);
                f32x4 y0 = mfma16(f[0], fx[0], f32x4{0.f, 0.f, 0.f, 0.f}), y1 = mfma16(f[1], fx[0], f32x4{0.f, 0.f, 0.f, 0.f});
#pragma unroll
                for (int i = 1; i < 4; ++i) { y0 = mfma16(f[2 * i], fx[i], y0); y1 = mfma16(f[2 * i + 1], fx[i], y1); }
                if ((l & 14) == 0) st16(unpoison4(__builtin_bit_cast(u32x4, (l & 1) ? y1 : y0)), rs, g * 16 + (l & 1) * 512, OFF_FX + ((h * 6 + 2 * w) * 8 + p) * 64);
              }
            }
            if constexpr (s == FXL && n == 4) {
              asm volatile("" : "=v"(fx[0]), "=v"(fx[1]));
              if (aff && w < 4) { ld16h<0>(fx[0], rs, l * 16, FP_OFF(0)); ld16h<0>(fx[1], rs, l * 16, FP_OFF(1)); }
            }
            if constexpr (s == FXS && n == 2) {
              if (aff && w < 4) {
                wait_vm<0>();
                asm volatile("" : "+v"(fx[0]), "+v"(fx[1])::"memory");
                if (!FX_OK(2)) {
                  unsigned spins = 0;
                  do {
                    if (spin_fail(spins, rs, T)) break;
                    fx[0] = ld16(rs, l * 16, FP_OFF(0)); fx[1] = ld16(rs, l * 16, FP_OFF(1));
                  } while (!FX_OK(2));
                }
                if (w == 3) {
                  f32x2 nx[8];
                  unpack8v(fx[0], nx); unpack8v(fx[1], nx + 4);
                  const f32x2 sm = ((nx[0] + nx[1]) + (nx[2] + nx[3])) + ((nx[4] + nx[5]) + (nx[6] + nx[7]));
                  const float mean = sum64(hadd(sm)) * (1.0f / 1024.0f);
                  const f32x2 m2 = {mean, mean};
                  f32x2 q = {0.f, 0.f};
#pragma unroll
                  for (int j = 0; j < 8; ++j) { const f32x2 d = nx[j] - m2; q = __builtin_elementwise_fma(d, d, q); }
                  const float rd = ln_rd(sum64(hadd(q)) * (1.0f / 1024.0f));
                  if (l == 0) *(LDS_AS f32x2*)(smem + L_FXL + 384) = f32x2{rd, -rbf1(mean)};
                } else {
                  tile_sum(fx[0], L_FXL);
                }
                count_in(3);
              }
            }
          }
        };
        static_for<32>([&](auto sc_) __attribute__((always_inline)) {
          constexpr int s = decltype(sc_)::value;
          if constexpr (s > 0) PIN_ACC();
          if constexpr (IT == 1) wait_vm<vm_at(IT, s, 0) - vm_a(IT, s)>();
          pin_w<s % WA>(af[s % WA][0], af[s % WA][1]);
          SB();
          static_for<6>([&](auto nc_) __attribute__((always_inline)) {
            constexpr int n = decltype(nc_)::value;
            if constexpr (s == 0) { acc[0][n] = f32x4{0.f, 0.f, 0.f, 0.f}; acc[1][n] = acc[0][n]; }
            constexpr bool on = s < QL || n >= 2;
            if constexpr (on) acc[0][n] = n < 4 ? mfma16(bfr[n].v, af[s % WA][0], acc[0][n]) : mfma16(af[s % WA][0], bfr[n].v, acc[0][n]);
            SB();
            if constexpr (n == 0) { QKV_RDB(s, 5); }
            else if constexpr (s + 1 < 32 && (s + 1 < QL || n > 2)) QKV_RDB(s + 1, n - 1);
            SB();
            if constexpr (on) acc[1][n] = n < 4 ? mfma16(bfr[n].v, af[s % WA][1], acc[1][n]) : mfma16(af[s % WA][1], bfr[n].v, acc[1][n]);
            SB();
            if constexpr (s + 1 < 32 && (s + 1 < QL || n >= 2)) QKV_RDA(s + 1, n);
            SB();
            gap(sc_, nc_);
            SB();
          });
        });
        PIN_ACC();
        frag_t qw[32 - QL][2];
        static_for<32 - QL>([&](auto c_) __attribute__((always_inline)) {
          constexpr int s = QL + decltype(c_)::value;
          QKV_RDA(s, 0); QKV_RDB(s, 0); QKV_RDA(s, 1); QKV_RDB(s, 1);
          qw[s - QL][0] = bfr[0]; qw[s - QL][1] = bfr[1];
        });
        SB();
        const bool own = aff && (int)((sel0 >> 5) & 7) == rt;
        const bool m0 = !(sel0 & 16), mq = (l & 15) == (int)(sel0 & 15), mv = g == (int)((sel0 & 15) >> 2);
        auto patch_qk = [&](int n) __attribute__((always_inline)) {
          f32x4 t = *(const LDS_AS f32x4*)(smem + L_FXL + n * 64 + g * 16);
          const f32x4 c = *(const LDS_AS f32x4*)(smem + L_CSL + n * 64 + g * 16);
          const float nb = *(const LDS_AS float*)(smem + L_FXL + 388);
#pragma unroll
          for (int e = 0; e < 4; ++e) { t[e] = __builtin_fmaf(c[e], nb, t[e]); asm volatile("" : "+v"(t[e])); }
          acc[0][n] = (mq && m0) ? t : acc[0][n];
          acc[1][n] = (mq && !m0) ? t : acc[1][n];
        };
        if (own) {
          while (__hip_atomic_load(qcnt + 3, __ATOMIC_RELAXED, __HIP_MEMORY_SCOPE_WORKGROUP) < 4u) {}
          asm volatile("" ::: "memory");
          patch_qk(2); patch_qk(3);
          const f32x2 nab = *(const LDS_AS f32x2*)(smem + L_FXL + 384);
          const LDS_AS float* fl_ = (const LDS_AS float*)(smem + L_FXL + 256) + (l & 15);
          const LDS_AS float* cl_ = (const LDS_AS float*)(smem + L_CSL + 256) + (l & 15);
          const float t4 = __builtin_fmaf(cl_[0], nab[1], fl_[0]), t5 = __builtin_fmaf(cl_[16], nab[1], fl_[16]);
          const unsigned na = __builtin_bit_cast(unsigned, (float)nab[0]);
#pragma unroll
          for (int a = 0; a < 2; ++a) {
            const bool ma = a ? !m0 : m0;
            aq[a] = (mq && ma) ? na : aq[a];
#pragma unroll
            for (int e = 0; e < 4; ++e) {
              const bool me = mv && ma && e == (int)(sel0 & 3);
              acc[a][4][e] = me ? t4 : acc[a][4][e]; acc[a][5][e] = me ? t5 : acc[a][5][e];
              av[a][e] = me ? na : av[a][e];
            }
          }
        }
        constexpr float C2 = 1.4426950408889634f * 0.17677669529663687f;
#pragma unroll
        for (int a = 0; a < 2; ++a) {
          f32x2 a2 = {asf(aq[a]), asf(aq[a])};
          qs2[a] = f32x2{asf(aq[a]) * C2, asf(aq[a]) * C2};
          asm volatile("" : "+v"(a2), "+v"(qs2[a]));
          acc[a][2] *= f32x4{a2[0], a2[1], a2[0], a2[1]}; acc[a][3] *= f32x4{a2[0], a2[1], a2[0], a2[1]};
          acc[a][4] *= __builtin_bit_cast(f32x4, av[a]); acc[a][5] *= __builtin_bit_cast(f32x4, av[a]);
        }
        k0 = pack8(acc[0][2], acc[0][3]); k1 = pack8(acc[1][2], acc[1][3]); v0 = pack8(acc[0][4], acc[1][4]); v1 = pack8(acc[0][5], acc[1][5]);
        const unsigned kvo = OFF_KV + IT * (8u << 20) + (p * 32 + h) * 32768 + rt * 4096;
        if (p < 7) { st16(k0, rs, l * 16, kvo); st16(k1, rs, l * 16 + 1024, kvo); st16(v0, rs, l * 16 + 2048, kvo); st16(v1, rs, l * 16 + 3072, kvo); }
        char* ownp = smem + (8 + rt) * 2048 + l * 16;
        *(u32x4*)(ownp + L_K) = k0;
        if (rt < 7) *(u32x4*)(ownp + L_K + 1024) = k1;
        *(u32x4*)(ownp + L_V) = v0; *(u32x4*)(ownp + L_V + 1024) = v1;
        SB();
        static_for<32 - QL>([&](auto c_) __attribute__((always_inline)) {
          constexpr int s = QL + decltype(c_)::value;
#pragma unroll
          for (int n = 0; n < 2; ++n) { acc[0][n] = mfma16(qw[s - QL][n].v, af[s % WA][0], acc[0][n]); acc[1][n] = mfma16(qw[s - QL][n].v, af[s % WA][1], acc[1][n]); }
        });
        if (own) { patch_qk(0); patch_qk(1); }
      };
      qkv_loop(it_c);
    }
#undef A_LOAD1
#undef QKV_RDA
#undef QKV_RDB
#undef QKV_BO
    {
      IDS();
      const GAS bf16 *pwo, *pwg, *pwu;
      asm volatile("s_load_dwordx2 %0, %3, 24\n\ts_load_dwordx2 %1, %3, 40\n\ts_load_dwordx2 %2, %3, 48" : "=&s"(pwo), "=&s"(pwg), "=&s"(pwu) : "s"(ka));
      const int ql = l & 15;
      const f32x4 z4 = {0.f, 0.f, 0.f, 0.f}, ninf = {-INFINITY, -INFINITY, -INFINITY, -INFINITY};
      LDS_AS unsigned* rdy = (LDS_AS unsigned*)(smem + L_RDY);
      const unsigned rtag = T[0] + it;
      u32x4 qf[2];
      f32x4 cn[2], oa[2][2], sum[2], S[2][2];
      unsigned one2 = 0x3F803F80u;
      asm volatile("" : "+v"(one2));
      const u32x4 ones = {one2, one2, one2, one2};
      auto ex8 = [&](f32x4 a, f32x4 b) __attribute__((always_inline)) {
        return u32x4{pack2(__builtin_amdgcn_exp2f(a[0]), __builtin_amdgcn_exp2f(a[1])), pack2(__builtin_amdgcn_exp2f(a[2]), __builtin_amdgcn_exp2f(a[3])),
                     pack2(__builtin_amdgcn_exp2f(b[0]), __builtin_amdgcn_exp2f(b[1])), pack2(__builtin_amdgcn_exp2f(b[2]), __builtin_amdgcn_exp2f(b[3]))};
      };
      {
#pragma unroll
        for (int a = 0; a < 2; ++a) {
          const f32x4 c4 = {qs2[a][0], qs2[a][1], qs2[a][0], qs2[a][1]};
          qf[a] = pack8(acc[a][0] * c4, acc[a][1] * c4);
        }
        f32x4 mk;
#pragma unroll
        for (int e = 0; e < 4; ++e) mk[e] = (4 * g + e <= ql) ? 0.f : -INFINITY;
        const f32x4 d0 = mfma16(k0, qf[0], mk), d1 = mfma16(k1, qf[1], mk), d2 = mfma16(k0, qf[1], z4);
        const float m0 = -max_x32(max_x16(fmaxf(fmaxf(d0[0], d0[1]), fmaxf(d0[2], d0[3]))));
        const float m1 = -max_x32(max_x16(fmaxf(fmaxf(d1[0], d1[1]), fmaxf(d1[2], d1[3]))));
        cn[0] = f32x4{m0, m0, m0, m0}; cn[1] = f32x4{m1, m1, m1, m1};
        asm volatile("" : "+v"(cn[0]), "+v"(cn[1]));
        const f32x4 e0 = d0 + cn[0];
        const u32x4 p0 = {pack2(__builtin_amdgcn_exp2f(e0[0]), __builtin_amdgcn_exp2f(e0[1])), pack2(__builtin_amdgcn_exp2f(e0[2]), __builtin_amdgcn_exp2f(e0[3])), 0u, 0u};
        const u32x4 p1 = ex8(d2 + cn[1], d1 + cn[1]);
        oa[0][0] = mfma16(v0, p0, z4); oa[0][1] = mfma16(v1, p0, z4); sum[0] = mfma16(ones, p0, z4);
        oa[1][0] = mfma16(v0, p1, z4); oa[1][1] = mfma16(v1, p1, z4); sum[1] = mfma16(ones, p1, z4);
      }
      const char* kb_ = smem + L_K + l * 16;
      const char* vb_ = smem + L_V + l * 16;
      const unsigned wov = (l >> 3) * 2048 + (((((l & 7) >> 1) ^ ((l >> 4) & 3)) * 2 + (l & 1)) << 4), fnv = (l >> 1) * (F_ * 2) + (l & 1) * 16;
      const unsigned pp = __builtin_amdgcn_readfirstlane(p > 0 ? 32 : 0), pvo = OFF_KV + it * (8u << 20) + (p * 32 + h - pp) * 32768;
      const unsigned pfo = __builtin_amdgcn_readfirstlane(F_KV + KVF(it, p * 32 + h - pp));
      asm volatile("" : "+v"(oa[0][0]), "+v"(oa[0][1]), "+v"(oa[1][0]), "+v"(oa[1][1]), "+v"(sum[0]), "+v"(sum[1]));
      DRAIN();
      asm volatile("" : "+v"(tdum));
      if (l == 0) st8(T, rs, rt * 8, F_KV + KVF(it, p * 32 + h));
      BAR();
      asm volatile("" : "+s"(pwo), "+s"(pwg), "+s"(pwu));
      WO_PIECE(15); WO_PIECE(14);
      u32x4 kf[2], vf[2];
      int nw = 0;
      u32x4 p0, p1;
      float e0, e1;
#define EE(v, i) { e0 = __builtin_amdgcn_exp2f(v[i]); e1 = __builtin_amdgcn_exp2f(v[i + 1]); pin2s(e0, e1); SB(); }
#define CV(P, i) { unsigned t_ = pack2(e0, e1); pin1(t_); P[i] = t_; SB(); }
      auto body = [&](unsigned ad, auto dk_) __attribute__((always_inline)) {
        constexpr int dk = decltype(dk_)::value;
        const f32x4 a0 = S[1][0], a1 = S[1][1];
        sum[0] = mfma16(ones, p0, sum[0]); SB();
        EE(a0, 0);
        lds_wait<2>(kf[0], kf[1]);
        S[0][0] = mfma16(kf[0], qf[0], cn[0]); SB();
        CV(p1, 0); EE(a0, 2);
        S[0][1] = mfma16(kf[1], qf[0], cn[0]); SB();
        CV(p1, 1); EE(a1, 0);
        lds_wait<0>(vf[0], vf[1]);
        oa[0][0] = mfma16(vf[0], p0, oa[0][0]); SB();
        CV(p1, 2); EE(a1, 2);
        oa[0][1] = mfma16(vf[1], p0, oa[0][1]); SB();
        CV(p1, 3);
        S[1][0] = mfma16(kf[0], qf[1], cn[1]); SB();
        EE(S[0][0], 0);
        S[1][1] = mfma16(kf[1], qf[1], cn[1]); SB();
        lds2<2048 + dk>(kf[0], kf[1], ad); SB();
        CV(p0, 0); EE(S[0][0], 2);
        oa[1][0] = mfma16(vf[0], p1, oa[1][0]); SB();
        CV(p0, 1); EE(S[0][1], 0);
        oa[1][1] = mfma16(vf[1], p1, oa[1][1]); SB();
        lds2<2048 + L_V>(vf[0], vf[1], ad); SB();
        CV(p0, 2); EE(S[0][1], 2);
        sum[1] = mfma16(ones, p1, sum[1]); SB();
        CV(p0, 3);
      };
      auto last = [&]() __attribute__((always_inline)) {
        sum[0] = mfma16(ones, p0, sum[0]); SB();
        EE(S[1][0], 0);
        lds_wait<0>(vf[0], vf[1]);
        pin2(kf[0], kf[1]);
        oa[0][0] = mfma16(vf[0], p0, oa[0][0]); SB();
        CV(p1, 0); EE(S[1][0], 2);
        oa[0][1] = mfma16(vf[1], p0, oa[0][1]); SB();
        CV(p1, 1); EE(S[1][1], 0);
        const float f0 = __builtin_amdgcn_exp2f(S[1][1][2]), f1 = __builtin_amdgcn_exp2f(S[1][1][3]);
        p1[2] = pack2(e0, e1); p1[3] = pack2(f0, f1);
        oa[1][0] = mfma16(vf[0], p1, oa[1][0]); oa[1][1] = mfma16(vf[1], p1, oa[1][1]); sum[1] = mfma16(ones, p1, sum[1]);
      };
      auto piece = [&]() __attribute__((always_inline)) {
        if constexpr (it == 0) { if (ffn_on) { FFN_DMA(nw); } }
        ++nw;
      };
      if (rt > 0) {
        unsigned ad = lbase + l * 16 + (6 + rt) * 2048;
        lds2<2048>(kf[0], kf[1], ad); lds2<2048 + L_V>(vf[0], vf[1], ad);
        lds_wait<2>(kf[0], kf[1]);
        S[0][0] = mfma16(kf[0], qf[0], cn[0]); S[0][1] = mfma16(kf[1], qf[0], cn[0]); S[1][0] = mfma16(kf[0], qf[1], cn[1]); S[1][1] = mfma16(kf[1], qf[1], cn[1]);
        SB();
        lds_wait<0>(vf[0], vf[1]);
        lds2<0>(kf[0], kf[1], ad);
        p0 = ex8(S[0][0], S[0][1]);
        lds_wait<0>(kf[0], kf[1]);
#pragma unroll 1
        for (int i = rt - 1; i > 0; --i) {
          ad -= 2048;
          body(ad, std::integral_constant<int, -2048>{});
          piece();
          SB();
        }
        piece();
        last();
      }
      {
        constexpr int FW = 4, FN = 8 / FW;
        if (w < FW) {
          const int g0 = FN * rt;
          poll_flags(rs, (l & (FN - 1)) * 8, pfo + g0 * 8, T);
#pragma unroll
          for (int j = 0; j < FN; ++j) {
            const unsigned d = lbase + (g0 + j) * 2048, o = pvo + (g0 + j) * 4096;
            dma16s<0>(rs, l * 16, o, d + L_K); dma16s<1024>(rs, l * 16, o, d + L_K);
            dma16s<0>(rs, l * 16, o + 2048, d + L_V); dma16s<1024>(rs, l * 16, o + 2048, d + L_V);
          }
          if (it == 1 && l == 0) rdy[8 + w] = rtag;
        } else if (it == 1 && w < 6) {
          while (!__all(__hip_atomic_load(rdy + 8 + (l & (FW - 1)), __ATOMIC_RELAXED, __HIP_MEMORY_SCOPE_WORKGROUP) == rtag)) {}
#pragma unroll 1
          for (int j = w == 4 ? 13 : 5; j > (w == 4 ? 5 : 3); --j) {
#pragma unroll
            for (int v = 0; v < 8; ++v) WO_PIECE_OF(v, j);
          }
        }
        if (p == 0) { cn[0] = ninf; cn[1] = ninf; }
        f32x4 mk;
#pragma unroll
        for (int e = 0; e < 4; ++e) mk[e] = (4 * g + e > ql) ? 0.f : -INFINITY;
        const f32x4 cm0 = cn[0] + mk, cm1 = cn[1] + mk;
        if (w < FW) {
          if constexpr (FN > 1) { wait_vm<2 * FN>(); if (l < FN / 2) rdy[FN * rt + l] = rtag; }
          DRAIN();
          if (l < FN) rdy[FN * rt + l] = rtag;
        }
        if constexpr (it == 0) { WO_PIECE(13); WO_PIECE(12); if (ffn_on) FFN_DMA(7); }

        int have = rt - 1;
        auto need = [&](int gq) __attribute__((always_inline)) {
          if (__builtin_expect(gq > have && gq < 8, 0)) {
            do {
              const unsigned t = __hip_atomic_load(rdy + (l & 7), __ATOMIC_RELAXED, __HIP_MEMORY_SCOPE_WORKGROUP);
              have = rt - 1 + __builtin_ctz(~((unsigned)__builtin_amdgcn_ballot_w64(t == rtag) & 0xffu) >> rt);
            } while (gq > have);
            asm volatile("" ::: "memory");
          }
        };
        unsigned ad = lbase + l * 16 + rt * 2048;
        need(rt);
        lds2<0>(kf[0], kf[1], ad); lds2<L_V>(vf[0], vf[1], ad);
        lds_wait<2>(kf[0], kf[1]);
        S[0][0] = mfma16(kf[0], qf[0], cm0); S[0][1] = mfma16(kf[1], qf[0], cn[0]); S[1][0] = ninf; S[1][1] = mfma16(kf[1], qf[1], cm1);
        SB();
        lds_wait<0>(vf[0], vf[1]);
        need(rt + 1);
        lds2<2048>(kf[0], kf[1], ad);
        p0 = ex8(S[0][0], S[0][1]);
        lds_wait<0>(kf[0], kf[1]);
#pragma unroll 1
        for (int gq = rt + 1; gq < 8; ++gq) {
          need(gq + 1);
          body(ad, std::integral_constant<int, 2048>{});
          piece();
          SB();
          ad += 2048;
        }
        last();
      }
      {
        float big = 0.f;
#pragma unroll
        for (int a = 0; a < 2; ++a)
#pragma unroll
          for (int d = 0; d < 2; ++d)
            big = __builtin_elementwise_maximum(__builtin_elementwise_maximum(big, __builtin_elementwise_maximum(fabsf(oa[a][d][0]), fabsf(oa[a][d][1]))),
                                                __builtin_elementwise_maximum(fabsf(oa[a][d][2]), fabsf(oa[a][d][3])));
        bool ok = sum[0][0] >= 0.5f && sum[0][0] < 0x1p64f && sum[1][0] >= 0.5f && sum[1][0] < 0x1p64f && big < INFINITY;
        if (!__all(ok)) {
#pragma unroll
          for (int a = 0; a < 2; ++a) {
            const int qs = 2 * rt + a, qq = 256 + 16 * qs + ql, klo = max(qq - 255, p > 0 ? 0 : 256);
            auto score = [&](int t) __attribute__((always_inline)) {
              f32x4 s = mfma16(t == 31 ? k1 : *(const u32x4*)(kb_ + t * 1024), qf[a], z4);
#pragma unroll
              for (int e = 0; e < 4; ++e) { const int kk = 16 * t + 4 * g + e; s[e] = (kk <= qq && kk >= klo) ? s[e] : -INFINITY; }
              return s;
            };
            float mx = -INFINITY;
#pragma unroll 1
            for (int t = qs; t <= qs + 16; ++t) { const f32x4 s = score(t); mx = fmaxf(fmaxf(mx, fmaxf(s[0], s[1])), fmaxf(s[2], s[3])); }
            mx = max_x32(max_x16(mx));
            oa[a][0] = z4; oa[a][1] = z4; sum[a] = z4;
#pragma unroll 1
            for (int G = qs >> 1; G <= (qs + 16) >> 1; ++G) {
              const f32x4 s0 = score(2 * G), s1 = score(2 * G + 1);
              f32x4 e0, e1;
#pragma unroll
              for (int e = 0; e < 4; ++e) { e0[e] = __builtin_amdgcn_exp2f(s0[e] - mx); e1[e] = __builtin_amdgcn_exp2f(s1[e] - mx); }
              const u32x4 pf = pack8(e0, e1);
              oa[a][0] = mfma16(*(const u32x4*)(vb_ + G * 2048), pf, oa[a][0]);
              oa[a][1] = mfma16(*(const u32x4*)(vb_ + G * 2048 + 1024), pf, oa[a][1]);
              sum[a] = mfma16(ones, pf, sum[a]);
            }
          }
        }
      }
#pragma unroll
      for (int a = 0; a < 2; ++a) {
        const float inv = __builtin_amdgcn_rcpf(sum[a][0]);
        oa[a][0] *= inv; oa[a][1] *= inv;
      }
      const unsigned oo = OFF_O + it * (4u << 20) + ((p * 8 + rt) * 64 + h * 2) * 1024;
      st16(pack8(oa[0][0], oa[0][1]), rs, l * 16, oo);
      st16(pack8(oa[1][0], oa[1][1]), rs, l * 16 + 1024, oo);
      DRAIN();
      BAR();
      if (tid == 0) st8(T, rs, OFL(h), F_O + OFB(it, p));
      if constexpr (it == 0) {
        DEF4(gv); DEF4(uv);
        if (ffn_on) {
          const unsigned ro = 65536 + w * 1024 + (4 * g + ((l & 15) >> 2)) * 32 + (l & 3) * 8;
#pragma unroll
          for (int i = 0; i < 4; ++i) {
            frag_t f;
            f.hf.a = trread(smem, ro + i * 16384); f.hf.b = trread(smem, ro + i * 16384 + 512);
            gv[i] = f.v;
            f.hf.a = trread(smem, ro + i * 16384 + 8192); f.hf.b = trread(smem, ro + i * 16384 + 8704);
            uv[i] = f.v;
          }
        }
        PINV4(gv); PINV4(uv);
        {
          ARGP(px, 0);
          const unsigned xo = ((rb * 4 + (w & 3)) * 32 + (w >> 2) * 16) * 2048 + ct * 128, xw = (l >> 3) * 2048 + (((l & 7) ^ g) << 4);
          dma16(RSRC(px, 2048 * 1024 * 2), xw, xo, lbase + w * 4096); dma16(RSRC(px, 2048 * 1024 * 2), xw ^ 64, xo + 16384, lbase + w * 4096 + 1024);
        }
      }
      WO_PIECE(3); WO_PIECE(2); WO_PIECE(1); WO_PIECE(0);
      if constexpr (it == 0) { WO_PIECE(11); WO_PIECE(10); WO_PIECE(9); WO_PIECE(8); WO_PIECE(7); WO_PIECE(6); WO_PIECE(5); WO_PIECE(4); }
    }
    IDS();
    ARGP(pwr, 4); ARGP(pwd, 7); ARGP(pout, 8);
    const int kh = w >> 2, rt2 = rb * 4 + (w & 3);
    const unsigned R = rt2 * 32 + kh * 16 + (l & 15);
    const unsigned to = (rt2 * 64 + ct * 4 + kh) * 1024 + l * 16;
    const unsigned rmo = ct * 64 + ((g & 1) ? 12 + 4 * g : 4 * g);
    f32x2 v[8], wv[8];
    float wsum;
    {
      u32x4 wa[2], wrp[2], res[2];
#pragma unroll
      for (int j = 0; j < 2; ++j) {
        wrp[j] = *(const GAS u32x4*)(pwr + rmo + 32 * j);
        wa[j] = *(const GAS u32x4*)(pwr + l * 8 + j * 512);
      }
      {
        f32x2 t[8];
        unpack8v(wa[0], t); unpack8v(wa[1], t + 4);
        const f32x2 s = ((t[0] + t[1]) + (t[2] + t[3])) + ((t[4] + t[5]) + (t[6] + t[7]));
        wsum = sum64(hadd(s));
        unpack8v(trade16(wrp[0]), wv); unpack8v(trade16(wrp[1]), wv + 4);
        asm volatile("" : "+v"(wsum), "+v"(wv[0]), "+v"(wv[1]), "+v"(wv[2]), "+v"(wv[3]), "+v"(wv[4]), "+v"(wv[5]), "+v"(wv[6]), "+v"(wv[7]));
      }
      unsigned bt[4];
#pragma unroll
      for (int n = 0; n < 4; ++n) {
        bt[n] = lbase + 32768 + 65536 + kh * 4096 + (4 * g + ((l & 15) >> 2)) * 128 + (l & 3) * 8 + ((n ^ (2 * (g & 1) + ((l & 15) >> 3))) << 5);
        asm volatile("" : "+v"(bt[n]));
      }
      if (w == 0) poll_flags(rs, OFL(l & 31), F_O + OFB(it, rb >> 1), T);
      else if (it == 0 && w < 3) poll_flags2(rs, F_XN + (((w - 1) * 4 + g) * RX + bi % RX) * 256 + (l & 15) * 16, T);
      else if (it == 0 && w == 3) poll_flags(rs, ((l >> 3) * 32 + h) * 128 + (l & 7) * 8, F_KV, T);
      asm volatile("s_barrier" ::: "memory");
      const unsigned abase = OFF_O + it * (4u << 20) + (rt2 * 64 + 32 * kh) * 1024;
      const unsigned av0 = l * 16 + kh * 1024, av1 = l * 16 + (kh ^ 1) * 1024;
      u32x4 af[WB][2];
#define O_LOAD(s_) { ld16h<(wo_k(it, s_) & 1) * 2048>(af[(s_) % WB][0], rs, av0, abase + (wo_k(it, s_) >> 1) * 4096); ld16h<(wo_k(it, s_) & 1) * 2048>(af[(s_) % WB][1], rs, av1, abase + (wo_k(it, s_) >> 1) * 4096); }
      if (w >= 4) __builtin_amdgcn_s_setprio(1);
      static_for<WR>([&](auto c_) __attribute__((always_inline)) { O_LOAD(decltype(c_)::value); });
      if constexpr (it == 1) { ld16h<0>(res[0], rs, g * 16, OFF_XNEW + ct * 128); ld16h<64>(res[1], rs, g * 16, OFF_XNEW + ct * 128); }
      SB();
      f32x4 ac[2][4];
#pragma unroll
      for (int a = 0; a < 2; ++a)
#pragma unroll
        for (int n = 0; n < 4; ++n) ac[a][n] = f32x4{0.f, 0.f, 0.f, 0.f};
      u32x4 bfr[2][4];
#define WO_RD(buf, s, n)                                                                             \
  {                                                                                                  \
    const unsigned o_ = bt[n] + (wo_k(it, s) & 7) * 8192;                                             \
    frag_t f_;                                                                                       \
    f_.hf.a = trabs(o_); f_.hf.b = trabs(o_ + 2048);                                                 \
    bfr[buf][n] = f_.v;                                                                              \
  }
#pragma unroll
      for (int n = 0; n < 4; ++n) WO_RD(0, 0, n);
      static_for<16>([&](auto sc_) __attribute__((always_inline)) {
        constexpr int s = decltype(sc_)::value, cur = s & 1;
        pin_ac(ac);
        if constexpr (s == 7) __builtin_amdgcn_s_setprio(0);
        if constexpr (s == 1) lds_count<0>(lbase + L_QC);
        if constexpr (s == (it == 0 ? 3 : 11)) lds_look(smem + L_QC, 64);
        if constexpr (s < 15 && ((wo_k(it, s) ^ wo_k(it, s + 1)) & 8)) {
#pragma unroll
          for (int n = 0; n < 4; ++n) { bt[n] += (wo_k(it, s) & 8) ? -65536 : 65536; pin1(bt[n]); }
        }
        if constexpr (s < WB - WR) O_LOAD(WR + 2 * s);
        if constexpr (it == 0 && s == 8) lds_count<8>(lbase + L_QC);
        if constexpr (it == 0 && s == WDS) {
          DRAIN();
          if (ffn_on) { lds_look(smem + L_QC + 8, 16); WD_DMA_OF(w); }
        }
        if constexpr (it == 1 || s < WDS) wait_vm<wo_at(it, s) - wo_pos(it, s)>();
        pin2(af[s % WB][0], af[s % WB][1]);
        if constexpr (it == 1 && s == 9) pin2(res[0], res[1]);
        SB();
#pragma unroll
        for (int n = 0; n < 4; ++n) {
          ac[0][n] = mfma16(bfr[cur][n], af[s % WB][0], ac[0][n]);
          ac[1][n] = mfma16(bfr[cur][n], af[s % WB][1], ac[1][n]);
          if (s + 1 < 16) WO_RD(cur ^ 1, s + 1, n);
          if (it == 0 && s == 8) {
            const u32x2 t = *(const LDS_AS u32x2*)(smem + w * 4096 + (l & 15) * 128 + (((2 * n + (g >> 1)) ^ ((l & 15) >> 1)) << 4) + (g & 1) * 8);
            res[n >> 1][2 * (n & 1)] = t[0]; res[n >> 1][2 * (n & 1) + 1] = t[1];
          }
          if (s == 9) {
            if (it == 1 && n == 0) { res[0] = R == sel0 ? res[0] : x2p[0]; res[1] = R == sel0 ? res[1] : x2p[1]; }

            v[2 * n] = f32x2{lo16(res[n >> 1][2 * (n & 1)]), hi16(res[n >> 1][2 * (n & 1)])};
            v[2 * n + 1] = f32x2{lo16(res[n >> 1][2 * (n & 1) + 1]), hi16(res[n >> 1][2 * (n & 1) + 1])};
            pin2f(v[2 * n], v[2 * n + 1]);
          }
          SB();
        }
        if constexpr (s < WB - WR) { O_LOAD(WR + 2 * s + 1); }
        else if constexpr (s + WB < 16) { O_LOAD(s + WB); }
        SB();
      });
#undef WO_RD
#undef O_LOAD
#define XCH_AT(v) ((v) == 7 ? 155648 : (v) * 4096)
#pragma unroll
      for (int n = 0; n < 4; ++n) *(f32x4*)(smem + XCH_AT(w) + n * 1024 + l * 16) = ac[1][n];
#pragma unroll
      for (int n = 0; n < 4; ++n) { v[2 * n] += f32x2{ac[0][n][0], ac[0][n][1]}; v[2 * n + 1] += f32x2{ac[0][n][2], ac[0][n][3]}; }
      BAR();
#pragma unroll
      for (int n = 0; n < 4; ++n) {
        const f32x4 o = *(const f32x4*)(smem + XCH_AT(w ^ 4) + n * 1024 + l * 16);
        v[2 * n] += f32x2{o[0], o[1]}; v[2 * n + 1] += f32x2{o[2], o[3]};
      }
    }
    {
      if (it == 0) { st16(pack8v(v), rs, to, OFF_X1); st16(pack8v(v + 4), rs, to + 2048, OFF_X1); }
      SB();
      const f32x2 s = ((v[0] + v[1]) + (v[2] + v[3])) + ((v[4] + v[5]) + (v[6] + v[7]));
      f32x2 dp = v[0] * wv[0], q = v[0] * v[0];
#pragma unroll
      for (int j = 1; j < 8; ++j) { dp = __builtin_elementwise_fma(v[j], wv[j], dp); q = __builtin_elementwise_fma(v[j], v[j], q); }
      float mt = hadd(s), qq = hadd(q), dot = hadd(dp);
      add_g3(mt, qq, dot);
      const unsigned so = OFF_STAT + it * 524288 + rb * 32768, lrow = R & 127;
      if (g == 0) st16(unpoison4(__builtin_bit_cast(u32x4, f32x4{mt, qq, dot, 0.f})), rs, ct * 2048 + lrow * 16, so);
      SB();
      if (it == 0) wait_vm<1>();
      if constexpr (it == 0) {
        volatile LDS_AS u32x2* in = (volatile LDS_AS u32x2*)(smem + L_XC);
        if (l == 0) in[w] = T;
        const u32x2 t = in[l & 7];
        if (__all(t[0] == T[0] && t[1] == T[1]) && l < RX) st8(T, rs, (l * 32 + (rb & 1) * 16 + ct) * 8, F_X1 + (rb >> 1) * RX * 32 * 8);
      }
      if (it == 1) { if (ffn_on) { WD_DMA_OF(w); } }
      if (it == 0) {
#pragma unroll
        for (int j = 0; j < 9; ++j) *(u32x4*)(smem + L_RING + (j / 3) * STAGE + (w * 3 + j % 3) * 1024 + l * 16) = keep[j];
      }
      __builtin_amdgcn_s_sleep(it == 0 ? SDI0 : SDIR);
      char* const skey = smem + (it == 0 ? L_SKEY : L_XC);
      u32x4 sp[4];
      ld16h<0>(sp[0], rs, g * 8192 + lrow * 16, so); ld16h<2048>(sp[1], rs, g * 8192 + lrow * 16, so);
      ld16h<0>(sp[2], rs, g * 8192 + lrow * 16, so + 4096); ld16h<2048>(sp[3], rs, g * 8192 + lrow * 16, so + 4096);
      u32x2 xf;
      unsigned k_;
      asm volatile("" : "=v"(xf), "=v"(k_));
      if (it == 0 && w == 0) { ld8h(xf, rs, (l & 31) * 8, F_X1 + (p * RX + (xcd >> 1) % RX) * 32 * 8); ld4h<0>(k_, rs, (l & 15) * 4, OFF_KEY + (bid & 15) * 128); }
      DRAIN();
      asm volatile("" : "+v"(xf), "+v"(k_)::"memory");
      PINV4(sp);
      const RowFin r = fin_key(rs, T, g * 8192 + lrow * 16, so, skey, l, w, R, wsum, sp);
      if constexpr (it == 0) {
        if (w == 0) {
          const unsigned need = 0x30003u << (((PROT() + 5) & 7) * 2);
          unsigned m = (unsigned)__builtin_amdgcn_ballot_w64(xf[0] == T[0] && xf[1] == T[1]);
          for (unsigned spins = 0; (m & need) != need;) {
            if (spin_fail(spins, rs, T)) { m = ~0u; break; }
            const u32x2 f = ld8(rs, F_X1 + ((p * RX + (xcd >> 1) % RX) * 32 + (l & 31)) * 8, 0);
            k_ = key_load(rs, l, 0, bid & 15);
            m = (unsigned)__builtin_amdgcn_ballot_w64(f[0] == T[0] && f[1] == T[1]);
          }
          k_ = umax16(k_ == POISON ? 0u : k_);
          if (l == 0) *(LDS_AS u32x4*)(skey + 32) = u32x4{m, k_, POISON, 0u};
        }
        if (tid == 192) *(LDS_AS u32x4*)(smem + L_QC) = u32x4{0u, 0u, 0u, 0u};
      }
      BAR();
      if constexpr (it == 0) {
        PRE_LOAD(1, PROT() + 5, 0, 4);
        const u32x2 mk = *(const LDS_AS u32x2*)(skey + 32);
        xm0 = __builtin_amdgcn_readfirstlane(mk[0]); kg0 = __builtin_amdgcn_readfirstlane(mk[1]);
      }
      const unsigned own = fin_cand(rs, skey, it, g, rb, ct, r, v);
      if constexpr (it == 0) kg0 = max(kg0, (unsigned)__builtin_amdgcn_readfirstlane(own));
      fin_x2(r, v);
      if constexpr (it == 0) {
        x2p[0] = pack8v(v); x2p[1] = pack8v(v + 4);
        asm volatile("" : "+v"(x2p[0]), "+v"(x2p[1]));
        if ((w & 3) * 2 + (w >> 2) == ct >> 1) {
          const float f = 1.0f + r.rd, rd2 = ln_rd(r.var * f * f), mb2 = rbf1(__builtin_fmaf(r.mean - r.mb, r.rd, r.mean));
          if (g < 2 && ((l >> 3) & 1) == (ct & 1))
            st4(min(__builtin_bit_cast(unsigned, g == 0 ? f * rd2 : -__builtin_fmaf(r.mb, r.rd, mb2) * __builtin_amdgcn_rcpf(f)), POISON - 1), rs, OFF_AB + 16384 + R * 4 + g * 8192, 0);
        }
      }
    }
    if (it == 1) {
      const u32x4 op0 = trade16(pack8v(v)), op1 = trade16(pack8v(v + 4));
#define OUT_STORE()                                                                                 \
  if (R != key_row(key)) {                                                                          \
    __builtin_amdgcn_raw_buffer_store_b128(op0, rout, (R * 1024 + rmo) * 2, 0, SC1);                  \
    __builtin_amdgcn_raw_buffer_store_b128(op1, rout, (R * 1024 + rmo + 32) * 2, 0, SC1);             \
  }
      u32x4 xv[4];
      unsigned guess = 0;
      {
        __builtin_amdgcn_s_sleep(GSL);
        const unsigned k_ = key_load(rs, l, 1, bid & 15);
        guess = ffn_xoff(__builtin_amdgcn_readfirstlane(umax16(k_ == POISON ? 0u : k_)), 1, w);
        row_load(rs, g, guess, xv);
      }
      if (w == 0) {
        const unsigned k_ = key_wait(rs, T, l, 1, bid & 15, key_load(rs, l, 1, bid & 15));
        if (l == 0) *(LDS_AS unsigned*)(smem + L_SKEY + 32) = k_;
      }
      DRAIN();
      PINV4(xv);
      BAR();
      const unsigned key = __builtin_amdgcn_readfirstlane(*(LDS_AS unsigned*)(smem + L_SKEY + 32));
      if (ffn_on) {
        f32x4 ga, ua;
        s16x4 dfr[8];
        ffn_d_read(smem, l, w, dfr);
        if (guess != ffn_xoff(key, 1, w)) ld4x16(xv, rs, g * 16, ffn_xoff(key, 1, w));
        ffn_gu(gv, uv, xv, ga, ua);
        ffn_gu_check(rs, T, ffn_xoff(key, 1, w), l, gv, uv, xv, ga, ua);
        ffn_gu_store(smem, l, w, ga, ua);
        BAR();
        f32x4 ya = {0.f, 0.f, 0.f, 0.f}, yb = ya;
        u32x2 hh = ffn_h(ffn_t_read(smem, l), l);
        ffn_d_mma(dfr, hh, ya, yb);
        if ((l & 15) < 8) ffn_d_store(rs, l, 4 * w, 1, slice, ya + yb);
        SB();
        __builtin_amdgcn_s_sleep(OSL);
        OUT_STORE();
      } else {
        OUT_STORE();
        const unsigned G = bi;
        if (G == 0 && tid == 0) st8(T, rs, F_EP, 0);
        u32x4 pv[4];
        u32x2 cxn, cx1;
        __builtin_amdgcn_s_sleep(RSL);
        red_load(rs, tid, 1, G, key, pv, cxn, cx1);
        red_sum(rs, smem, T, tid, w, 1, G, key, pv, cxn, cx1);
        BAR();
        if (w == 0) {
          const u32x2 ov = red_final<8>(smem, l, key, cxn, cx1);
          if (l < 8) __builtin_amdgcn_raw_buffer_store_b64(ov, rout, (key_row(key) * 1024 + G * 32 + l * 4) * 2, 0, 0);
        }
      }
#undef OUT_STORE
    }
  };
  layer(std::integral_constant<int, 0>{});
  layer(std::integral_constant<int, 1>{});
}

extern "C" void kernel_launch(void* const* d_in, const int* in_sizes, int n_in, void* d_out, int out_size,
                              void* d_ws, size_t ws_size, hipStream_t stream) {
  (void)hipFuncSetAttribute((const void*)mega_kernel, hipFuncAttributeMaxDynamicSharedMemorySize, LDS_BYTES);
  mega_kernel<<<256, 512, LDS_BYTES, stream>>>((const bf16*)d_in[0], (char*)d_ws, (const bf16*)d_in[1], (const bf16*)d_in[2], (const bf16*)d_in[4],
                                               (const bf16*)d_in[5], (const bf16*)d_in[6], (const bf16*)d_in[7], (bf16*)d_out);
}
```

```cpp
#include <hip/hip_runtime.h>
#include <stdint.h>
#include <utility>

typedef __bf16 bf16;
typedef __bf16 bf16x8 __attribute__((ext_vector_type(8)));
typedef __bf16 bf16x2 __attribute__((ext_vector_type(2)));
typedef float f32x4 __attribute__((ext_vector_type(4)));
typedef float f32x2 __attribute__((ext_vector_type(2)));
typedef short s16x4 __attribute__((ext_vector_type(4)));
typedef unsigned int u32x4 __attribute__((ext_vector_type(4)));
typedef unsigned int u32x2 __attribute__((ext_vector_type(2)));
#define LDS_AS __attribute__((address_space(3)))
#define GAS __attribute__((address_space(1)))
typedef GAS unsigned gu32;
typedef GAS unsigned long long gu64;
#define RLX_AGENT __ATOMIC_RELAXED, __HIP_MEMORY_SCOPE_AGENT
typedef __amdgpu_buffer_rsrc_t rsrc_t;

constexpr int F_ = 3584;
constexpr int SC1 = 16;

constexpr int RX = 4;
constexpr unsigned F_EP = 0, F_TMO = 128;
constexpr unsigned F_XN = 4096;
constexpr unsigned F_O = 8u << 20;
constexpr unsigned F_X1 = F_O + 131072;
constexpr unsigned OFF_KEY = F_O + 393216;
constexpr unsigned F_KV = F_O + 524288;
#define KVF(it, i) ((i) * 128 + (it) * 64)
#define OFL(h) (((h) >> 3) * 128 + ((h) & 7) * 8)
#define OFB(it, p) ((p) * 512 + (it) * 64)
constexpr unsigned OFF_AB = 147456;
constexpr unsigned OFF_XNEW = 262144;
constexpr unsigned OFF_CS = 294912;
constexpr unsigned OFF_FX = 393216;
constexpr unsigned OFF_CA = 524288;
constexpr unsigned OFF_CB = 655360;
constexpr unsigned OFF_STAT = 1u << 20;
constexpr unsigned OFF_PART = 3u << 20;
constexpr unsigned OFF_X1 = 12u << 20;
constexpr unsigned OFF_O = 16u << 20;
constexpr unsigned OFF_KV = 32u << 20;
constexpr unsigned OFF_WI = 52u << 20;
constexpr unsigned WS_BYTES = 58u << 20;
constexpr unsigned POISON = 0xFFFFFFFFu;

constexpr int L_CSL = 31872, L_FXL = 32256, L_K = 0, L_V = 32768, L_RING = 65536, L_DN = 32768, L_TOUCH0 = 149504, L_RDY = 31744, L_QC = 31808, LDS_BYTES = 163840;
constexpr int STAGE = 24576;
#define SLOT(t) ((t) & 3)

template <typename F, int... I> __device__ __forceinline__ void static_for_impl(F&& f, std::integer_sequence<int, I...>) {
  (f(std::integral_constant<int, I>{}), ...);
}
template <int N, typename F> __device__ __forceinline__ void static_for(F&& f) { static_for_impl(f, std::make_integer_sequence<int, N>{}); }
__device__ __forceinline__ unsigned pack2(float a, float b) {
  f32x2 v = {a, b};
  return __builtin_bit_cast(unsigned, __builtin_convertvector(v, bf16x2));
}
__device__ __forceinline__ float asf(unsigned u) { return __builtin_bit_cast(float, u); }
__device__ __forceinline__ float lo16(unsigned u) { return __builtin_bit_cast(float, u << 16); }
__device__ __forceinline__ float hi16(unsigned u) { return __builtin_bit_cast(float, u & 0xffff0000u); }
__device__ __forceinline__ u32x4 pack8(const f32x4 a, const f32x4 b) {
  u32x4 o;
  o[0] = pack2(a[0], a[1]); o[1] = pack2(a[2], a[3]); o[2] = pack2(b[0], b[1]); o[3] = pack2(b[2], b[3]);
  return o;
}
__device__ __forceinline__ f32x4 mfma16(u32x4 a, u32x4 b, f32x4 c) {
  return __builtin_amdgcn_mfma_f32_16x16x32_bf16(__builtin_bit_cast(bf16x8, a), __builtin_bit_cast(bf16x8, b), c, 0, 0, 0);
}
__device__ __forceinline__ f32x4 mfma16k16(s16x4 a, s16x4 b, f32x4 c) { return __builtin_amdgcn_mfma_f32_16x16x16bf16_1k(a, b, c, 0, 0, 0); }
template <int CTRL> __device__ __forceinline__ float dppf(float v) {
  return __builtin_bit_cast(float, __builtin_amdgcn_update_dpp(0, __builtin_bit_cast(int, v), CTRL, 0xf, 0xf, true));
}
template <int CTRL> __device__ __forceinline__ unsigned dppu(unsigned v) {
  return (unsigned)__builtin_amdgcn_update_dpp(0, (int)v, CTRL, 0xf, 0xf, true);
}
__device__ __forceinline__ float add_x16(float v) {
  auto r = __builtin_amdgcn_permlane16_swap(__builtin_bit_cast(unsigned, v), __builtin_bit_cast(unsigned, v), false, false);
  return __builtin_bit_cast(float, (unsigned)r[0]) + __builtin_bit_cast(float, (unsigned)r[1]);
}
__device__ __forceinline__ float add_x32(float v) {
  auto r = __builtin_amdgcn_permlane32_swap(__builtin_bit_cast(unsigned, v), __builtin_bit_cast(unsigned, v), false, false);
  return __builtin_bit_cast(float, (unsigned)r[0]) + __builtin_bit_cast(float, (unsigned)r[1]);
}
__device__ __forceinline__ float max_x16(float v) {
  auto r = __builtin_amdgcn_permlane16_swap(__builtin_bit_cast(unsigned, v), __builtin_bit_cast(unsigned, v), false, false);
  return fmaxf(__builtin_bit_cast(float, (unsigned)r[0]), __builtin_bit_cast(float, (unsigned)r[1]));
}
__device__ __forceinline__ float max_x32(float v) {
  auto r = __builtin_amdgcn_permlane32_swap(__builtin_bit_cast(unsigned, v), __builtin_bit_cast(unsigned, v), false, false);
  return fmaxf(__builtin_bit_cast(float, (unsigned)r[0]), __builtin_bit_cast(float, (unsigned)r[1]));
}
__device__ __forceinline__ unsigned umax_x16(unsigned v) {
  auto r = __builtin_amdgcn_permlane16_swap(v, v, false, false);
  return max((unsigned)r[0], (unsigned)r[1]);
}
__device__ __forceinline__ unsigned umax_x32(unsigned v) {
  auto r = __builtin_amdgcn_permlane32_swap(v, v, false, false);
  return max((unsigned)r[0], (unsigned)r[1]);
}
__device__ __forceinline__ float sum_hi8(float v) {
  v += dppf<0x128>(v);
  v = add_x16(v);
  return add_x32(v);
}
__device__ __forceinline__ float sum32(float v) {
  v += dppf<0xB1>(v);
  v += dppf<0x4E>(v);
  v += dppf<0x124>(v);
  v += dppf<0x128>(v);
  return add_x16(v);
}
__device__ __forceinline__ float sum64(float v) {
  v += dppf<0xB1>(v);
  v += dppf<0x4E>(v);
  v += dppf<0x124>(v);
  v += dppf<0x128>(v);
  v = add_x16(v);
  return add_x32(v);
}

#define BAR() asm volatile("s_waitcnt lgkmcnt(0)\n\ts_barrier" ::: "memory")
#define DRAIN() asm volatile("s_waitcnt vmcnt(0)" ::: "memory")
#define SB() __builtin_amdgcn_sched_barrier(0)
template <int N> __device__ __forceinline__ void wait_vm() { asm volatile("s_waitcnt vmcnt(%0)" ::"n"(N) : "memory"); }

__device__ __forceinline__ void dma16(rsrc_t rs, unsigned voff, unsigned soff, unsigned lds) {
  asm volatile("s_mov_b32 m0, %0\n\ts_nop 0\n\tbuffer_load_dwordx4 %1, %2, %3 offen lds" ::"s"(lds), "v"(voff), "s"(rs), "s"(soff) : "memory");
}
template <int OFF> __device__ __forceinline__ void dma16s(rsrc_t rs, unsigned voff, unsigned soff, unsigned lds) {
  asm volatile("s_mov_b32 m0, %0\n\ts_nop 0\n\tbuffer_load_dwordx4 %1, %2, %3 offen offset:%4 sc1 lds" ::"s"(lds), "v"(voff), "s"(rs), "s"(soff), "n"(OFF) : "memory");
}
__device__ __forceinline__ void touch(unsigned& d, const GAS bf16* ptr) { asm volatile("global_load_dword %0, %1, off" : "+v"(d) : "v"(ptr) : "memory"); }
__device__ __forceinline__ void ldrows(u32x4 (&d)[4], rsrc_t rx, unsigned voff, unsigned soff) {
  asm volatile("buffer_load_dwordx4 %0, %4, %5, %6 offen\n\tbuffer_load_dwordx4 %1, %4, %5, %6 offen offset:512\n\t"
               "buffer_load_dwordx4 %2, %4, %5, %6 offen offset:1024\n\tbuffer_load_dwordx4 %3, %4, %5, %6 offen offset:1536"
               : "=&v"(d[0]), "=&v"(d[1]), "=&v"(d[2]), "=&v"(d[3]) : "v"(voff), "s"(rx), "s"(soff) : "memory");
}
__device__ __forceinline__ void touchb(unsigned& d, rsrc_t rs, unsigned vo) { asm volatile("buffer_load_dword %0, %1, %2, 0 offen sc1" : "+v"(d) : "v"(vo), "s"(rs) : "memory"); }
__device__ __forceinline__ float rbf1(float x) { return __builtin_bit_cast(float, pack2(0.f, x)); }
__device__ __forceinline__ u32x4 ld16(rsrc_t rs, unsigned voff, unsigned soff) { return __builtin_amdgcn_raw_buffer_load_b128(rs, voff, soff, SC1); }
template <int OFF> __device__ __forceinline__ void ld16h(u32x4& d, rsrc_t rs, unsigned voff, unsigned soff) {
  asm volatile("buffer_load_dwordx4 %0, %1, %2, %3 offen offset:%4 sc1" : "=v"(d) : "v"(voff), "s"(rs), "s"(soff), "n"(OFF) : "memory");
}
template <int I, int OFF> __device__ __forceinline__ void ld16w(u32x4& d, rsrc_t rs, unsigned voff, unsigned soff) {
  if constexpr (I == 0) asm volatile("buffer_load_dwordx4 %0, %1, %2, %3 offen offset:%4 sc1" : "={v[128:131]}"(d) : "v"(voff), "s"(rs), "s"(soff), "n"(OFF) : "memory");
  else if constexpr (I == 1) asm volatile("buffer_load_dwordx4 %0, %1, %2, %3 offen offset:%4 sc1" : "={v[132:135]}"(d) : "v"(voff), "s"(rs), "s"(soff), "n"(OFF) : "memory");
  else if constexpr (I == 2) asm volatile("buffer_load_dwordx4 %0, %1, %2, %3 offen offset:%4 sc1" : "={v[136:139]}"(d) : "v"(voff), "s"(rs), "s"(soff), "n"(OFF) : "memory");
  else if constexpr (I == 3) asm volatile("buffer_load_dwordx4 %0, %1, %2, %3 offen offset:%4 sc1" : "={v[140:143]}"(d) : "v"(voff), "s"(rs), "s"(soff), "n"(OFF) : "memory");
  else if constexpr (I == 4) asm volatile("buffer_load_dwordx4 %0, %1, %2, %3 offen offset:%4 sc1" : "={v[144:147]}"(d) : "v"(voff), "s"(rs), "s"(soff), "n"(OFF) : "memory");
  else if constexpr (I == 5) asm volatile("buffer_load_dwordx4 %0, %1, %2, %3 offen offset:%4 sc1" : "={v[148:151]}"(d) : "v"(voff), "s"(rs), "s"(soff), "n"(OFF) : "memory");
  else if constexpr (I == 6) asm volatile("buffer_load_dwordx4 %0, %1, %2, %3 offen offset:%4 sc1" : "={v[152:155]}"(d) : "v"(voff), "s"(rs), "s"(soff), "n"(OFF) : "memory");
  else if constexpr (I == 7) asm volatile("buffer_load_dwordx4 %0, %1, %2, %3 offen offset:%4 sc1" : "={v[156:159]}"(d) : "v"(voff), "s"(rs), "s"(soff), "n"(OFF) : "memory");
  else if constexpr (I == 8) asm volatile("buffer_load_dwordx4 %0, %1, %2, %3 offen offset:%4 sc1" : "={v[160:163]}"(d) : "v"(voff), "s"(rs), "s"(soff), "n"(OFF) : "memory");
  else if constexpr (I == 9) asm volatile("buffer_load_dwordx4 %0, %1, %2, %3 offen offset:%4 sc1" : "={v[164:167]}"(d) : "v"(voff), "s"(rs), "s"(soff), "n"(OFF) : "memory");
  else if constexpr (I == 10) asm volatile("buffer_load_dwordx4 %0, %1, %2, %3 offen offset:%4 sc1" : "={v[168:171]}"(d) : "v"(voff), "s"(rs), "s"(soff), "n"(OFF) : "memory");
  else if constexpr (I == 11) asm volatile("buffer_load_dwordx4 %0, %1, %2, %3 offen offset:%4 sc1" : "={v[172:175]}"(d) : "v"(voff), "s"(rs), "s"(soff), "n"(OFF) : "memory");
}
template <int K> __device__ __forceinline__ void pin_w(u32x4& a, u32x4& b) {
  if constexpr (K == 0) asm volatile("" : "+{v[128:131]}"(a), "+{v[132:135]}"(b)::"memory");
  else if constexpr (K == 1) asm volatile("" : "+{v[136:139]}"(a), "+{v[140:143]}"(b)::"memory");
  else if constexpr (K == 2) asm volatile("" : "+{v[144:147]}"(a), "+{v[148:151]}"(b)::"memory");
  else if constexpr (K == 3) asm volatile("" : "+{v[152:155]}"(a), "+{v[156:159]}"(b)::"memory");
  else if constexpr (K == 4) asm volatile("" : "+{v[160:163]}"(a), "+{v[164:167]}"(b)::"memory");
  else if constexpr (K == 5) asm volatile("" : "+{v[168:171]}"(a), "+{v[172:175]}"(b)::"memory");
}
__device__ __forceinline__ void ld8h(u32x2& d, rsrc_t rs, unsigned voff, unsigned soff) {
  asm volatile("buffer_load_dwordx2 %0, %1, %2, %3 offen sc1" : "=v"(d) : "v"(voff), "s"(rs), "s"(soff) : "memory");
}
__device__ __forceinline__ void ld16u(u32x4& d, rsrc_t rs, unsigned voff, unsigned soff) {
  asm volatile("buffer_load_dwordx4 %0, %1, %2, %3 offen sc1" : "+v"(d) : "v"(voff), "s"(rs), "s"(soff) : "memory");
}
template <int OFF> __device__ __forceinline__ void ld4h(unsigned& d, rsrc_t rs, unsigned voff, unsigned soff) {
  asm volatile("buffer_load_dword %0, %1, %2, %3 offen offset:%4 sc1" : "=v"(d) : "v"(voff), "s"(rs), "s"(soff), "n"(OFF) : "memory");
}
__device__ __forceinline__ void ld4x16(u32x4 (&d)[4], rsrc_t rs, unsigned voff, unsigned soff) {
  asm volatile("buffer_load_dwordx4 %0, %4, %5, %6 offen sc1\n\tbuffer_load_dwordx4 %1, %4, %5, %6 offen offset:64 sc1\n\t"
               "buffer_load_dwordx4 %2, %4, %5, %6 offen offset:128 sc1\n\tbuffer_load_dwordx4 %3, %4, %5, %6 offen offset:192 sc1\n\ts_waitcnt vmcnt(0)"
               : "=&v"(d[0]), "=&v"(d[1]), "=&v"(d[2]), "=&v"(d[3]) : "v"(voff), "s"(rs), "s"(soff) : "memory");
}
__device__ __forceinline__ u32x2 ld8(rsrc_t rs, unsigned voff, unsigned soff) { return __builtin_amdgcn_raw_buffer_load_b64(rs, voff, soff, SC1); }
__device__ __forceinline__ void st16(u32x4 v, rsrc_t rs, unsigned voff, unsigned soff) { __builtin_amdgcn_raw_buffer_store_b128(v, rs, voff + soff, 0, SC1); }
__device__ __forceinline__ void st8(u32x2 v, rsrc_t rs, unsigned voff, unsigned soff) { __builtin_amdgcn_raw_buffer_store_b64(v, rs, voff, soff, SC1); }

__device__ __forceinline__ unsigned ld4(rsrc_t rs, unsigned voff, unsigned soff) { return __builtin_amdgcn_raw_buffer_load_b32(rs, voff, soff, SC1); }
__device__ __forceinline__ void st4(unsigned v, rsrc_t rs, unsigned voff, unsigned soff) { __builtin_amdgcn_raw_buffer_store_b32(v, rs, voff, soff, SC1); }

constexpr unsigned kSpinCap = 1u << 16;
__device__ __forceinline__ bool spin_fail(unsigned& spins, rsrc_t rs, u32x2 T) {
  asm volatile("" ::: "memory");
  ++spins;
  if (spins > kSpinCap) { st8(T, rs, 0, F_TMO); return true; }
  if ((spins & 255u) == 0u) {
    const u32x2 v = ld8(rs, 0, F_TMO);
    if (v[0] == T[0] && v[1] == T[1]) return true;
  }
  __builtin_amdgcn_s_sleep(3);
  return false;
}
__device__ __forceinline__ void poll_flags2(rsrc_t rs, unsigned voff, u32x2 T) {
  for (unsigned spins = 0;;) {
    const u32x4 v = ld16(rs, voff, 0);
    if (__all(v[0] == T[0] && v[1] == T[1] && v[2] == T[0] && v[3] == T[1])) break;
    if (spin_fail(spins, rs, T)) break;
  }
  asm volatile("" ::: "memory");
  __builtin_amdgcn_fence(__ATOMIC_ACQUIRE, "wavefront");
}
__device__ __forceinline__ void poll_flags(rsrc_t rs, unsigned voff, unsigned soff, u32x2 T) {
  for (unsigned spins = 0;;) {
    const u32x2 v = ld8(rs, voff, soff);
    if (__all(v[0] == T[0] && v[1] == T[1])) break;
    if (spin_fail(spins, rs, T)) break;
  }
  asm volatile("" ::: "memory");
  __builtin_amdgcn_fence(__ATOMIC_ACQUIRE, "wavefront");
}

__device__ __forceinline__ void unpack8v(const u32x4 u, f32x2* v) {
#pragma unroll
  for (int j = 0; j < 4; ++j) v[j] = f32x2{lo16(u[j]), hi16(u[j])};
}
__device__ __forceinline__ u32x4 pack8v(const f32x2* v) {
  u32x4 o;
#pragma unroll
  for (int j = 0; j < 4; ++j) o[j] = pack2(v[j][0], v[j][1]);
  return o;
}
__device__ __forceinline__ float ln_rd(float var) { return __builtin_amdgcn_rcpf(rbf1(__builtin_amdgcn_sqrtf(rbf1(rbf1(var) + 1e-5f)))); }
__device__ __forceinline__ float hadd(f32x2 v) {
  float a = v[0];
  asm volatile("" : "+v"(a));
  return a + v[1];
}
__device__ __forceinline__ float add_g(float v) { return add_x32(add_x16(v)); }
__device__ __forceinline__ void add_g3(float& x, float& y, float& z) {
  auto a = __builtin_amdgcn_permlane16_swap(__builtin_bit_cast(unsigned, x), __builtin_bit_cast(unsigned, y), false, false);
  auto b = __builtin_amdgcn_permlane16_swap(__builtin_bit_cast(unsigned, z), __builtin_bit_cast(unsigned, z), false, false);
  const float r = asf(a[0]) + asf(a[1]), t = asf(b[0]) + asf(b[1]);
  auto c = __builtin_amdgcn_permlane32_swap(__builtin_bit_cast(unsigned, r), __builtin_bit_cast(unsigned, r), false, false);
  auto d = __builtin_amdgcn_permlane32_swap(__builtin_bit_cast(unsigned, t), __builtin_bit_cast(unsigned, t), false, false);
  const float u = asf(c[0]) + asf(c[1]);
  z = asf(d[0]) + asf(d[1]);
  auto e = __builtin_amdgcn_permlane16_swap(__builtin_bit_cast(unsigned, u), __builtin_bit_cast(unsigned, u), false, false);
  x = asf(e[0]); y = asf(e[1]);
}
__device__ __forceinline__ u32x4 trade16(u32x4 v) {
  auto a = __builtin_amdgcn_permlane16_swap(v[0], v[2], false, false);
  auto b = __builtin_amdgcn_permlane16_swap(v[1], v[3], false, false);
  return u32x4{(unsigned)a[0], (unsigned)b[0], (unsigned)a[1], (unsigned)b[1]};
}
__device__ __forceinline__ unsigned umax4(u32x4 v) { return max(max(v[0], v[1]), max(v[2], v[3])); }
__device__ __forceinline__ u32x2 unpoison2(u32x2 v) { return u32x2{min(v[0], POISON - 1), min(v[1], POISON - 1)}; }
__device__ __forceinline__ u32x4 unpoison4(u32x4 v) {
  return u32x4{min(v[0], POISON - 1), min(v[1], POISON - 1), min(v[2], POISON - 1), min(v[3], POISON - 1)};
}

typedef union { struct { s16x4 a, b; } hf; u32x4 v; } frag_t;
__device__ __forceinline__ s16x4 trread(const char* smem, unsigned off) {
  return __builtin_amdgcn_ds_read_tr16_b64_v4i16((LDS_AS s16x4*)(smem + off));
}
__device__ __forceinline__ s16x4 trabs(unsigned addr) { return __builtin_amdgcn_ds_read_tr16_b64_v4i16((LDS_AS s16x4*)(size_t)addr); }

constexpr int WA = 6;
constexpr int WB = 4, WR = 3;
#define CH 6, 8, 9, 15, 17, 23, 25, 29, 31, 4
constexpr int CHV[10] = {CH};
constexpr int FKL = CHV[9];
constexpr int FSL = CHV[0], FS0 = CHV[1], FS0D = CHV[2], FS1 = CHV[3], FS1S = CHV[4];
constexpr int FXA = CHV[5], FXB = CHV[6], FXL = CHV[7], FXS = CHV[8];
constexpr int FXT = FXB >> 2;
constexpr int CSD = 3, POI = 4, RWS = 5, EIN = 8;
constexpr int ERQ = 16, CSL = 19, CSS = 22;
#define IMV 24
constexpr int IMG = IMV, KP0 = 26;
constexpr int PB0 = 10, PB1 = 24;
constexpr int ABL = 23, ABS = 27;
constexpr int XU = 3, XL = 2;
constexpr int x_slot(int u) { return u % XU == 0 ? 0 : u % XU == 1 ? 32768 : 49152; }
#define TCH 19, 7, 4, 27
constexpr int TCV[4] = {TCH}, TS0 = TCV[0], TSN = TCV[1], TW = TCV[2], TST = TCV[3];
constexpr int QL = 26;
constexpr int RXCD = 5;
constexpr int WDS = 17 - WB;
constexpr int FSG = 4, FDG = 3, FRG = 3, FXG = 3;
#define SLP 20, 28, 8, 46, 3, 4, 12
constexpr int SLV[7] = {SLP}, SDI0 = SLV[0], SDIR = SLV[1], GSL = SLV[2], RSL = SLV[3], KSL = SLV[4], XSL = SLV[5], OSL = SLV[6];
constexpr bool tile_piece(int s) { return s >= 3 && ((s - 3) >> 2) + 3 < 8 && ((s - 3) & 3) < 3; }
constexpr int a_req(int s, int n) {
  if (!(n & 1)) return -1;
  if (s <= 1) return 8 + 3 * s + (n >> 1);
  return n < 5 && s + 5 < 32 ? 2 * (s + 5) + (n >> 1) : -1;
}
constexpr int vm_n(int it, int s, int n) {
  int c = 0;
  if (n == 0) c += tile_piece(s);
  if (n == 4) c += (s == ABL) + (it ? (s == FSL ? 4 : 0) + (s == FKL) : (s == ERQ) + (s >= IMG && s < IMG + 3));
  if (!it) c += (s < 4 && n == 2) + (s == 0 && (n == 0 || n == 4)) + ((n == 3 || n == 5) && s + XL + XU < 32);
  else c += (a_req(s, n) >= 0) + ((s == 0 || s == 4) && n == 2);
  return c;
}
constexpr int vm_at(int it, int s, int n) {
  int c = it ? 8 : 14;
  for (int u = 0; u < 6 * s + n; ++u) c += vm_n(it, u / 6, u % 6);
  return c;
}
constexpr int vm_a(int it, int k) {
  if (k < 4) return 2 * k + 2;
  for (int u = 0; u < 192; ++u)
    if (a_req(u / 6, u % 6) == 2 * k + 1) return vm_at(it, (u + 1) / 6, (u + 1) % 6);
  return 0;
}
constexpr int vm_tile(int it, int t) { return t >= 3 ? vm_at(it, 4 * t - 7, 1) : t == 1 ? vm_at(0, 0, 5) : vm_at(0, 3, 3); }
constexpr int vm_x(int u) { return u == 2 ? 9 : u < XU + XL ? 12 + 2 * (u - XU) : vm_at(0, u - XU - XL + 1, 0); }

constexpr int wo_k(int it, int s) { return it || s < 4 ? 15 - s : s < 8 ? 7 - s : 19 - s; }
constexpr int wo_pos(int it, int s) { return 2 * s + 2 + (s >= WR ? 2 * it : 0); }
constexpr int wo_at(int it, int s) { return 2 * it + (s < WB - WR ? 2 * WR + 2 + 4 * s : s < 16 - WB ? 2 * (2 * WB - WR) + 2 * (s - WB + WR) : 32); }
template <int OFF> __device__ __forceinline__ void lds_count(unsigned ad) {
  asm volatile("s_mov_b64 exec, 1\n\tds_add_u32 %0, %1 offset:%2\n\ts_mov_b64 exec, -1" ::"v"(ad), "v"(1u), "n"(OFF) : "memory");
}
__device__ __forceinline__ void lds_look(char* c, unsigned want) {
  while ((unsigned)__builtin_amdgcn_readfirstlane(__hip_atomic_load((LDS_AS unsigned*)c, __ATOMIC_RELAXED, __HIP_MEMORY_SCOPE_WORKGROUP)) < want) {}
  asm volatile("" ::: "memory");
}
__device__ __forceinline__ void pin1(unsigned& a) { asm volatile("" : "+v"(a)); }
__device__ __forceinline__ void pin2(u32x4& a, u32x4& b) { asm volatile("" : "+v"(a), "+v"(b)::"memory"); }
__device__ __forceinline__ void pin2s(float& a, float& b) { asm volatile("" : "+v"(a), "+v"(b)); }
template <int OFF> __device__ __forceinline__ void lds2(u32x4& a, u32x4& b, unsigned ad) {
  asm volatile("ds_read_b128 %0, %2 offset:%3\n\tds_read_b128 %1, %2 offset:%4" : "=&v"(a), "=&v"(b) : "v"(ad), "n"(OFF), "n"(OFF + 1024) : "memory");
}
template <int N> __device__ __forceinline__ void lds_wait(u32x4& a, u32x4& b) { asm volatile("s_waitcnt lgkmcnt(%2)" : "+v"(a), "+v"(b) : "n"(N) : "memory"); }
__device__ __forceinline__ void pin2f(f32x2& a, f32x2& b) { asm volatile("" : "+v"(a), "+v"(b)); }
__device__ __forceinline__ void pin_ac(f32x4 (&ac)[2][4]) {
  asm volatile("" : "+{v[176:179]}"(ac[0][0]), "+{v[180:183]}"(ac[0][1]), "+{v[184:187]}"(ac[0][2]), "+{v[188:191]}"(ac[0][3]),
                    "+{v[192:195]}"(ac[1][0]), "+{v[196:199]}"(ac[1][1]), "+{v[200:203]}"(ac[1][2]), "+{v[204:207]}"(ac[1][3]));
}

constexpr int L_XC = LDS_BYTES - 64, L_SKEY = 32704, L_SRED = 4096, L_SFIN = 20480;
__device__ __forceinline__ unsigned key_load(rsrc_t rs, int l, int L, int rep) { return ld4(rs, (l & 15) * 4, OFF_KEY + (L * 16 + rep) * 128); }
__device__ __forceinline__ unsigned umax16(unsigned k) {
  k = max(k, dppu<0xB1>(k));
  k = max(k, dppu<0x4E>(k));
  k = max(k, dppu<0x141>(k));
  return max(k, dppu<0x140>(k));
}
__device__ __forceinline__ unsigned key_row(unsigned key) { return (0xffffu - (key & 0xffffu)) & 2047u; }
__device__ __forceinline__ unsigned key_wait(rsrc_t rs, u32x2 T, int l, int L, int rep, unsigned kv) {
  if (!__all(kv != POISON)) {
    unsigned spins = 0;
    do {
      if (spin_fail(spins, rs, T)) break;
      if (L == 0) __builtin_amdgcn_s_sleep(KSL);
      kv = key_load(rs, l, L, rep);
    } while (!__all(kv != POISON));
  }
  return __builtin_amdgcn_readfirstlane(umax16(kv));
}
__device__ __forceinline__ unsigned ffn_xoff(unsigned key, int L, int w) { return OFF_CA + L * 32768 + (key_row(key) >> 7) * 2048 + w * 256; }
__device__ __forceinline__ void row_load(rsrc_t rs, int g, unsigned xo, u32x4 (&xv)[4]) {
  ld16h<0>(xv[0], rs, g * 16, xo); ld16h<64>(xv[1], rs, g * 16, xo); ld16h<128>(xv[2], rs, g * 16, xo); ld16h<192>(xv[3], rs, g * 16, xo);
}
template <int SL = 0> __device__ __forceinline__ void ffn_xwait(rsrc_t rs, u32x2 T, unsigned xo, int l, u32x4 (&xv)[4]) {
#define XV_OK() __all(max(max(umax4(xv[0]), umax4(xv[1])), max(umax4(xv[2]), umax4(xv[3]))) != POISON)
  if (!XV_OK()) {
    unsigned spins = 0;
    do {
      if (spin_fail(spins, rs, T)) break;
      if (SL) __builtin_amdgcn_s_sleep(SL);
#pragma unroll
      for (int i = 0; i < 4; ++i) xv[i] = ld16(rs, (l >> 4) * 16 + i * 64, xo);
    } while (!XV_OK());
  }
#undef XV_OK
}
__device__ __forceinline__ void ffn_gu(const u32x4 (&gv)[4], const u32x4 (&uv)[4], const u32x4 (&xv)[4], f32x4& ga, f32x4& ua) {
  ga = mfma16(gv[0], xv[0], f32x4{0.f, 0.f, 0.f, 0.f}); ua = mfma16(uv[0], xv[0], f32x4{0.f, 0.f, 0.f, 0.f});
#pragma unroll
  for (int i = 1; i < 4; ++i) { ga = mfma16(gv[i], xv[i], ga); ua = mfma16(uv[i], xv[i], ua); }
}
__device__ __forceinline__ void ffn_gu_check(rsrc_t rs, u32x2 T, unsigned xo, int l, const u32x4 (&gv)[4], const u32x4 (&uv)[4], u32x4 (&xv)[4], f32x4& ga, f32x4& ua) {
  if (__builtin_expect(__any(ga[0] != ga[0]), 0)) {
    ffn_xwait(rs, T, xo, l, xv);
    ffn_gu(gv, uv, xv, ga, ua);
  }
}
__device__ __forceinline__ void ffn_gu_store(char* smem, int l, int w, f32x4 ga, f32x4 ua) {
  LDS_AS f32x4* sred = (LDS_AS f32x4*)(smem + L_SRED);
  if ((l & 15) == 0) { sred[w * 8 + (l >> 4)] = ga; sred[w * 8 + 4 + (l >> 4)] = ua; }
}
__device__ __forceinline__ f32x4 ffn_t_read(char* smem, int l) { return ((const LDS_AS f32x4*)(smem + L_SRED))[(l & 7) * 8 + ((l >> 3) & 1) * 4 + (l >> 4)]; }
__device__ __forceinline__ void ffn_d_read(char* smem, int l, int blk, s16x4 (&dfr)[8]) {
#pragma unroll
  for (int i = 0; i < 8; ++i) dfr[i] = trread(smem, L_DN + blk * 4096 + (l >> 4) * 1024 + ((l & 15) >> 2) * 256 + i * 32 + (l & 3) * 8);
}
__device__ __forceinline__ float ffn_d_sum(float t) { t += dppf<0xB1>(t); t += dppf<0x4E>(t); return t + dppf<0x141>(t); }
__device__ __forceinline__ u32x2 ffn_h(f32x4 t, int l) {
#pragma unroll
  for (int e = 0; e < 4; ++e) t[e] = ffn_d_sum(t[e]);
  const float a = (l & 1) ? t[1] : t[0], b = (l & 1) ? t[3] : t[2], s = (l & 2) ? b : a;
  const float gg = rbf1(s), uu = rbf1(dppf<0x128>(s));
  const float h = rbf1(gg * __builtin_amdgcn_rcpf(1.0f + __expf(-gg))) * uu;
  const unsigned h0 = pack2(h, dppf<0x55>(h)), h1 = pack2(dppf<0xAA>(h), dppf<0xFF>(h));
  return (l & 15) == 0 ? u32x2{h0, h1} : u32x2{0u, 0u};
}
__device__ __forceinline__ void ffn_d_mma(const s16x4 (&dfr)[8], u32x2& hh, f32x4& ya, f32x4& yb) {
#pragma unroll
  for (int i = 0; i < 8; ++i) {
    if (i & 1) yb = mfma16k16(dfr[i], __builtin_bit_cast(s16x4, hh), yb);
    else ya = mfma16k16(dfr[i], __builtin_bit_cast(s16x4, hh), ya);
    hh = u32x2{dppu<0x111>(hh[0]), dppu<0x111>(hh[1])};
  }
}
__device__ __forceinline__ void ffn_d_store(rsrc_t rs, int l, int G0, int L, int slice, f32x4 y) {
  const unsigned c = l & 15;
  st16(unpoison4(__builtin_bit_cast(u32x4, y)), rs, (G0 + (c >> 1)) * 32768 + slice * 128 + (c & 1) * 64 + (l >> 4) * 16, OFF_PART + L * (1u << 20));
}
__device__ __forceinline__ void red_load(rsrc_t rs, int tid, int L, unsigned G, unsigned key, u32x4 (&pv)[4], u32x2& cxn, u32x2& cx1) {
#pragma unroll
  for (int k = 0; k < 4; ++k) pv[k] = ld16(rs, min(tid + 512 * k, 1791) * 16, OFF_PART + L * (1u << 20) + G * 32768);
  cxn = ld8(rs, (tid & 3) * 16 + (tid & 4) * 2, OFF_CA + L * 32768 + (key_row(key) >> 7) * 2048 + G * 64);
  cx1 = ld8(rs, (tid & 3) * 16 + (tid & 4) * 2, OFF_CB + L * 32768 + (key_row(key) >> 7) * 2048 + G * 64);
}
__device__ __forceinline__ void red_sum(rsrc_t rs, char* smem, u32x2 T, int tid, int w, int L, unsigned G, unsigned key,
                                        u32x4 (&pv)[4], u32x2& cxn, u32x2& cx1) {
#define PV_OK() __all(max(max(max(umax4(pv[0]), umax4(pv[1])), max(umax4(pv[2]), umax4(pv[3]))), max(max(cxn[0], cxn[1]), max(cx1[0], cx1[1]))) != POISON)
  if (!PV_OK()) {
    unsigned spins = 0;
    do {
      if (spin_fail(spins, rs, T)) break;
      red_load(rs, tid, L, G, key, pv, cxn, cx1);
    } while (!PV_OK());
  }
#undef PV_OK
  f32x4 s = (__builtin_bit_cast(f32x4, pv[0]) + __builtin_bit_cast(f32x4, pv[1])) + __builtin_bit_cast(f32x4, pv[2]);
  if (w < 4) s += __builtin_bit_cast(f32x4, pv[3]);
  ((LDS_AS f32x4*)(smem + L_SFIN))[tid] = s;
}
template <int NW> __device__ __forceinline__ u32x2 red_final(char* smem, int l, unsigned key, u32x2 cxn, u32x2 cx1) {
  const LDS_AS f32x4* sf = (const LDS_AS f32x4*)(smem + L_SFIN) + l;
  f32x4 s = (sf[0] + sf[64]) + (sf[128] + sf[192]);
  if constexpr (NW == 8) s += (sf[256] + sf[320]) + (sf[384] + sf[448]);
#pragma unroll
  for (int j = 0; j < 4; ++j) s[j] = sum_hi8(s[j]);
  const unsigned k16 = key >> 16;
  const float rmax = __builtin_bit_cast(float, ((k16 & 0x8000u) ? (k16 & 0x7fffu) : (~k16 & 0xffffu)) << 16);
  const float sig = rbf1(__builtin_amdgcn_rcpf(1.0f + __expf(-rmax)));
  const float xn4[4] = {lo16(cxn[0]), hi16(cxn[0]), lo16(cxn[1]), hi16(cxn[1])};
  const float x14[4] = {lo16(cx1[0]), hi16(cx1[0]), lo16(cx1[1]), hi16(cx1[1])};
  float o[4];
#pragma unroll
  for (int j = 0; j < 4; ++j) o[j] = x14[j] + rbf1(xn4[j] + rbf1(rbf1(s[j]) * sig));
  return u32x2{pack2(o[0], o[1]), pack2(o[2], o[3])};
}

struct RowFin { float mean, var, mb, rd; unsigned key; };
__device__ __forceinline__ void fin_wait(rsrc_t rs, u32x2 T, unsigned voff, unsigned so, u32x4 (&sp)[4]) {
#define ST_OK() __all(max(max(umax4(sp[0]), umax4(sp[1])), max(umax4(sp[2]), umax4(sp[3]))) != POISON)
  if (!ST_OK()) {
    unsigned spins = 0;
    do {
      if (spin_fail(spins, rs, T)) break;
#pragma unroll
      for (int i = 0; i < 4; ++i) sp[i] = ld16(rs, voff + i * 2048, so);
    } while (!ST_OK());
  }
#undef ST_OK
}
__device__ __forceinline__ RowFin fin_key(rsrc_t rs, u32x2 T, unsigned voff, unsigned so, char* skey, int l, int w, unsigned R, float wsum, u32x4 (&sp)[4]) {
  RowFin r;
#define ST_SUM() ((__builtin_bit_cast(f32x4, sp[0]) + __builtin_bit_cast(f32x4, sp[1])) + (__builtin_bit_cast(f32x4, sp[2]) + __builtin_bit_cast(f32x4, sp[3])))
  f32x4 sf = ST_SUM();
  const float z = (sf[0] + sf[1]) + sf[2];
  if (__any(z != z)) {
    fin_wait(rs, T, voff, so, sp);
    sf = ST_SUM();
  }
#undef ST_SUM
  float s0 = sf[0], s1 = sf[1], s2 = sf[2];
  add_g3(s0, s1, s2);
  r.mean = s0 * (1.0f / 1024.0f);
  r.var = fmaxf(__builtin_fmaf(-r.mean, r.mean, s1 * (1.0f / 1024.0f)), 0.f);
  r.mb = rbf1(r.mean); r.rd = ln_rd(r.var);
  const unsigned rb16 = pack2((s2 - r.mb * wsum) * r.rd, 0.f) & 0xffffu;
  r.key = min((((rb16 & 0x8000u) ? (~rb16 & 0xffffu) : (rb16 | 0x8000u)) << 16) | (0xffffu - R), POISON - 1);
  const unsigned wk = umax16(r.key);
  if (l == 0) ((LDS_AS unsigned*)skey)[w] = wk;
  return r;
}
__device__ __forceinline__ void fin_cand(rsrc_t rs, char* skey, int L, int g, int rb, int ct, const RowFin& r, const f32x2 (&v)[8]) {
  const u32x4 k0 = *(const LDS_AS u32x4*)skey, k1 = *(const LDS_AS u32x4*)(skey + 16);
  const unsigned best = max(umax4(k0), umax4(k1));
  if (r.key == best) {
    const unsigned co = L * 32768 + rb * 2048 + ct * 128 + g * 16;
    f32x2 t[8], rd2 = {r.rd, r.rd}, of2 = {-r.mb * r.rd, -r.mb * r.rd};
    asm volatile("" : "+v"(rd2), "+v"(of2));
#pragma unroll
    for (int j = 0; j < 8; ++j) t[j] = __builtin_elementwise_fma(v[j], rd2, of2);
    st16(unpoison4(pack8v(t)), rs, co, OFF_CA); st16(unpoison4(pack8v(t + 4)), rs, co + 64, OFF_CA);
    if (g == 0) st4(best, rs, OFF_KEY + (L * 16 + ct) * 128 + rb * 4, 0);
    st16(unpoison4(pack8v(v)), rs, co, OFF_CB); st16(unpoison4(pack8v(v + 4)), rs, co + 64, OFF_CB);
  }
}
__device__ __forceinline__ void fin_x2(const RowFin& r, f32x2 (&v)[8]) {
  f32x2 sc2 = {1.0f + r.rd, 1.0f + r.rd}, of2 = {-r.mb * r.rd, -r.mb * r.rd};
  asm volatile("" : "+v"(sc2), "+v"(of2));
#pragma unroll
  for (int j = 0; j < 8; ++j) v[j] = __builtin_elementwise_fma(v[j], sc2, of2);
}

extern "C" __device__ unsigned long long dispatch_id() __asm("llvm.amdgcn.dispatch.id");

extern "C" __global__ __launch_bounds__(512, 2) void mega_kernel(const bf16* x, char* ws, const bf16* wqkv, const bf16* wo, const bf16* wr,
                                                                 const bf16* wg, const bf16* wu, const bf16* wd, bf16* out) {
  extern __shared__ __attribute__((aligned(16))) char smem[];
  const int w0_ = __builtin_amdgcn_readfirstlane(threadIdx.x >> 6);
  typedef unsigned long long u64x2 __attribute__((ext_vector_type(2)));
  u64x2 a01;
  unsigned long long a2;
  asm volatile("s_load_dwordx4 %0, %2, 0\n\ts_load_dwordx2 %1, %2, 16" : "=&s"(a01), "=&s"(a2) : "s"(__builtin_amdgcn_kernarg_segment_ptr()));
  const unsigned lbase = (unsigned)(size_t)(LDS_AS char*)smem;
  {
    unsigned long long pc;
    unsigned len;
    asm volatile("s_getpc_b64 %0\n\ts_mov_b32 %1, .Ltext_end-mega_kernel\n\t.subsection 1\n.Ltext_end:\n\t.subsection 0" : "=s"(pc), "=s"(len));
    const unsigned o = ((blockIdx.x >> 3) * 64 + (threadIdx.x & 63)) * 128;
    if (w0_ == 7 && o < len)
      asm volatile("s_mov_b32 m0, %0\n\ts_nop 0\n\tglobal_load_lds_dword %1, %2" ::"s"(lbase + L_TOUCH0), "v"(o), "s"(pc & ~127ull) : "memory");
  }
#define HGRP() ((xcd & 2) + (xcd >> 2))
#define IDS()                                                                                         \
  int tid = threadIdx.x, w = w0_, bid = blockIdx.x;                                                   \
  asm volatile("" : "+v"(tid));                                                                       \
  asm volatile("" : "+s"(w));                                                                         \
  asm volatile("" : "+s"(bid));                                                                       \
  const int l = tid & 63, xcd = bid & 7, bi = bid >> 3;                                               \
  const int p = (xcd & 1) * 4 + (bi & 3), h = HGRP() * 8 + (bi >> 2);         \
  const int rb = 2 * xcd + (bi & 1), ct = bi >> 1;                               \
  const int slice = (xcd - (xcd > RXCD)) * 32 + bi;                                            \
  const bool ffn_on = xcd != RXCD;                                                                    \
  const int g = l >> 4;                                                                               \
  const int rt = w < 4 ? w : 11 - w;          \
  KARGS();                                                                                            \
  (void)p; (void)h; (void)rb; (void)ct; (void)slice; (void)ffn_on; (void)g; (void)rt;
#define PROT() (2 * (p & 3) + (p >> 2))
  typedef const void* const __attribute__((address_space(4)))* karg_t;
#define KARGS() karg_t ka = (karg_t)__builtin_amdgcn_kernarg_segment_ptr(); asm volatile("" : "+s"(ka))
#define ARGP(name, i) const GAS bf16* name = (const GAS bf16*)ka[i]; asm volatile("" : "+s"(name))
#define RSRC(ptr, bytes) __builtin_amdgcn_make_buffer_rsrc((void*)(ptr), 0, bytes, 0x00020000)
#define rwq RSRC(pwq, 1024 * 3072 * 2)
#define rwo RSRC(pwo, 1024 * 1024 * 2)
#define rwg RSRC(pwg, 1024 * F_ * 2)
#define rwu RSRC(pwu, 1024 * F_ * 2)
#define rwd RSRC(pwd, 1024 * F_ * 2)
#define rout RSRC(pout, 2048 * 1024 * 2)

#define WQ_VOFF() ((l >> 2) * 6144 + (((l & 3) ^ ((((l >> 4) ^ (l >> 5)) & 1) << 1)) << 4))
#define WQ_KMAJ(q) (((q) % 3) * 8 + (q) / 3)
#define WQ_PIECE(t, j, dst) dma16(rwq, wqv, ((((t) + rot) & 7) * 128 + ((j) & 7) * 16) * 6144 + ((j) >> 3) * 2048 + h * 64, lbase + (dst) + (j) * 1024)
#define WQ_BOFF(dt) ((g >> 1) * 1024 + (8 * (g & 1) + ((l & 15) >> 2)) * 64 + (l & 3) * 8 + (((dt) ^ (g & 1)) << 5))
#define X_REQ(u, hf) dma16(rx, xv[hf], (p * 256 + rt * 32 + ((u) & 1) * 16 + (hf) * 8) * 2048 + ((((u) >> 2) + rot) & 7) * 256 + (((u) >> 1) & 1) * 128, lbase + rt * 2048 + x_slot(u) + (hf) * 1024)
#define WO_PIECE_OF(w, j) dma16(rwo, wov, (((w) >> 2) * 64 + ((w) & 3) + 4 * (j)) * 16384 + ct * 128, lbase + 32768 + (j) * 8192 + (w) * 1024)
#define WO_PIECE(j) WO_PIECE_OF(w, j)
#define WD_DMA_OF(v) _Pragma("unroll") for (int i = 0; i < 4; ++i)                                  \
      dma16(rwd, (l >> 4) * 2048 + (l & 15) * 16, (unsigned)(slice * 16 + 4 * i) * 2048 + (v) * 256, lbase + L_DN + (v) * 4096 + i * 1024)
#define FFN_DMA(m) dma16((m) & 1 ? rwu : rwg, fnv, (unsigned)(((4 * w + ((m) >> 1)) * 32) * F_ + slice * 16) * 2, lbase + 65536 + (m) * 8192 + w * 1024)
#define POISON_BIG(L)                                                                                \
  if (w >= 2) {                                                                                      \
    unsigned pz1 = POISON;                                                                           \
    asm volatile("" : "+v"(pz1));                                                                    \
    st16(u32x4{pz1, pz1, pz1, pz1}, rs, (w >= 4 ? OFF_PART + (L) * (1u << 20) + bid * 4096 - 4096 : OFF_STAT + (L) * 524288 + (p * 32 + h) * 2048 - 2048) + tid * 16, 0); \
  }
  u32x4 gv[4], uv[4];
  u32x4 keep[9];
  u32x4 pre[4][2];
#define PRE_LOAD(it_, rot_, s0, s1)                                                                  \
  static_for<(s1) - (s0)>([&](auto c_) __attribute__((always_inline)) {                              \
    constexpr int s = (s0) + decltype(c_)::value;                                                    \
    const unsigned ab_ = OFF_X1 + (((rot_) + (s >> 2)) & 7) * 8192 + ((s >> 1) & 1) * 4096; \
    ld16w<2 * s, (s & 1) * 2048>(pre[s][0], rs, l * 16, ab_ + (p * 8 + rt) * 65536);                         \
    ld16w<2 * s + 1, (s & 1) * 2048 + 1024>(pre[s][1], rs, l * 16, ab_ + (p * 8 + rt) * 65536);                 \
  })
  u32x2 T;
  const GAS bf16* tsrc;
  unsigned xv[2], xa[2];
  u32x2 ep;
  u32x4 rows0[4];
#define X_READ(u, d0, d1) { d0 = *(const LDS_AS u32x4*)(size_t)(xa[0] + x_slot(u)); d1 = *(const LDS_AS u32x4*)(size_t)(xa[1] + x_slot(u)); }
  unsigned g1[5], wqv0;
  {
    IDS();
    const int rot = PROT(), qa = WQ_KMAJ(w), qb = WQ_KMAJ(8 + w);
    wqv0 = WQ_VOFF();
    xv[0] = (l >> 3) * 2048 + (((l & 7) ^ (l >> 4)) << 4); xv[1] = xv[0] ^ 64;
    g1[0] = (rot * 128 + (qa & 7) * 16) * 6144 + (qa >> 3) * 2048 + h * 64; g1[1] = lbase + L_RING + qa * 1024;
    g1[2] = (rot * 128 + (qb & 7) * 16) * 6144 + (qb >> 3) * 2048 + h * 64; g1[3] = lbase + L_RING + qb * 1024;
    g1[4] = (p * 256 + rt * 32) * 2048 + rot * 256;
    asm volatile("" : "+v"(wqv0), "+v"(xv[0]), "+v"(xv[1]));
    asm volatile("s_waitcnt lgkmcnt(0)" : "+s"(a01), "+s"(a2), "+s"(g1[0]), "+s"(g1[1]), "+s"(g1[2]), "+s"(g1[3]), "+s"(g1[4]));
  }
  x = (const bf16*)a01[0]; ws = (char*)a01[1]; wqkv = (const bf16*)a2;
  const rsrc_t rs = __builtin_amdgcn_make_buffer_rsrc((void*)ws, 0, WS_BYTES, 0x00020000);
  const rsrc_t rx = RSRC(x, 2048 * 1024 * 2);
  {
    IDS();
    const int rot = PROT();
    const bf16* pwq = wqkv;
    const unsigned wqv = wqv0;
    dma16(rwq, wqv, g1[0], g1[1]);
#pragma unroll
    for (int i = 0; i < 4; ++i) dma16(rx, xv[i & 1], g1[4] + i * 16384, lbase + rt * 2048 + x_slot(i >> 1) + (i & 1) * 1024);
    dma16(rwq, wqv, g1[2], g1[3]);
    asm volatile("s_barrier" ::: "memory");
    WQ_PIECE(0, WQ_KMAJ(16 + w), L_RING);
    if (w == 6 && bi == 0) {
      ARGP(pwr, 4);
      asm volatile("s_mov_b32 m0, %0\n\ts_nop 0\n\tbuffer_load_dword %1, %2, 0 offen lds" ::"s"(lbase + L_TOUCH0 + 256), "v"((l & 15) * 128), "s"(RSRC(pwr, 2048)) : "memory");
    }
    X_REQ(2, 0); X_REQ(2, 1);
    asm volatile("buffer_load_dwordx2 %0, off, %1, 0 sc1" : "=v"(ep) : "s"(rs) : "memory");
    asm volatile("" : "=v"(rows0[0]), "=v"(rows0[1]), "=v"(rows0[2]), "=v"(rows0[3]));
    if (w < 4) ldrows(rows0, rx, (l >> 5) * 2048 + (l & 31) * 16, (p * 256 + h * 8 + 2 * w) * 2048);
    {
      ARGP(pwo, 3); ARGP(pwg, 5); ARGP(pwu, 6);
      tsrc = (w < 4 ? pwg : pwu) + ((bi * 32 + ((tid & 255) >> 3)) * F_ + (slice >> 5) * 512 + (tid & 7) * 64);
      if (!ffn_on) tsrc = pwo + (bi * 512 + tid) * 64;
      asm volatile("" : "+v"(tsrc));
    }
#pragma unroll
    for (int kk = 0; kk < 2; ++kk) {
      const int r = l & 15;
      xa[kk] = lbase + rt * 2048 + (r >> 3) * 1024 + (r & 7) * 128 + (((4 * kk + g) ^ (r >> 1)) << 4);
      asm volatile("" : "+v"(xa[kk]));
    }
    if (tid == 192) *(LDS_AS u32x4*)(smem + L_QC) = u32x4{0u, 0u, 0u, 0u};
    if (w < 4) wait_vm<8>(); else wait_vm<4>();
    X_READ(0, pre[0][0], pre[1][0]); X_READ(1, pre[0][1], pre[1][1]);
    asm volatile("s_waitcnt lgkmcnt(0)\n\ts_barrier" : "+v"(pre[0][0]), "+v"(pre[0][1]), "+v"(pre[1][0]), "+v"(pre[1][1])::"memory");
    X_REQ(3, 0); X_REQ(3, 1); X_REQ(4, 0); X_REQ(4, 1);
  }
  unsigned sel0 = 0x7FFFFFFFu;
  u32x4 x2p[2];
  unsigned xm0 = 0;

  auto layer = [&](auto it_c) __attribute__((always_inline)) {
    constexpr int it = decltype(it_c)::value;
    f32x4 acc[2][6];
    f32x2 qs2[2];
    u32x4 k0, k1, v0, v1;
    unsigned tdum = 0;
    {
      IDS();
      bool aff = false;
      unsigned aq[2];
      u32x4 av[2];
#define PIN_ACC() asm volatile("" : "+{v[176:179]}"(acc[0][0]), "+{v[180:183]}"(acc[0][1]), "+{v[184:187]}"(acc[0][2]), "+{v[188:191]}"(acc[0][3]), "+{v[192:195]}"(acc[0][4]), "+{v[196:199]}"(acc[0][5]), \
                               "+{v[200:203]}"(acc[1][0]), "+{v[204:207]}"(acc[1][1]), "+{v[208:211]}"(acc[1][2]), "+{v[212:215]}"(acc[1][3]), "+{v[216:219]}"(acc[1][4]), "+{v[220:223]}"(acc[1][5]))
#define PINV4(x) asm volatile("" : "+v"(x[0]), "+v"(x[1]), "+v"(x[2]), "+v"(x[3])::"memory")
      auto qkv_loop = [&](auto it_c) __attribute__((always_inline)) {
        constexpr int IT = decltype(it_c)::value;
        const unsigned abase = OFF_X1 + (p * 8 + rt) * 65536;
        const int rot = PROT() + 5 * IT;
        unsigned fkey = 0, kv, bq[2];
        u32x4 fx[4], bv[2], af[WA][2];
        u32x2 cxn, cx1;
        f32x4 sa, sb, cs4[2];
        f32x2 b2[2];
#define AB_OFF() (OFF_AB + IT * 16384 + (p * 8 + rt) * 128)
#define A_LOAD1(k, a) ld16w<(k) % WA * 2 + (a), ((k) & 1) * 2048 + (a) * 1024>(af[(k) % WA][a], rs, l * 16, abase + ((((k) >> 2) + rot) & 7) * 8192 + (((k) >> 1) & 1) * 4096)
#pragma unroll
        for (int s = 0; s < 2 + 2 * IT; ++s) { af[s][0] = pre[s][0]; af[s][1] = pre[s][1]; }
        const bf16* pwq = wqkv;
        const int tstep = TS0 + bid % TSN;
        const unsigned wqv = WQ_VOFF();
#define QKV_DMA1(t, i)                                                                            \
  {                                                                                               \
    if constexpr (IT == 0) WQ_PIECE(t, 3 * w + (i), L_RING + SLOT(t) * STAGE);                    \
    else dma16s<(i) * 1024>(rs, l * 16, OFF_WI + (h * 8 + (((t) + rot) & 7)) * STAGE + w * 3072, lbase + L_RING + SLOT(t) * STAGE + w * 3072); \
  }
        unsigned bo[2][2];
#pragma unroll
        for (int dt = 0; dt < 2 - IT; ++dt) {
          bo[dt][0] = lbase + L_RING + (IT ? l * 16 : WQ_BOFF(dt)); bo[dt][1] = bo[dt][0] + 2 * STAGE;
          asm volatile("" : "+v"(bo[dt][0]), "+v"(bo[dt][1]));
        }
#define FRAG_T() (lbase + L_RING + (4 * g + ((l & 15) >> 2)) * 64 + (l & 3) * 8 + (((g ^ (g >> 1)) & 1) << 5))
        auto frag_t2 = [&](unsigned o) __attribute__((always_inline)) {
          frag_t t;
          t.hf.a = trabs(o); t.hf.b = trabs(o + 1024);
          return t.v;
        };
        unsigned ka[3];
        auto tile_sum = [&](u32x4 pt, int dst) __attribute__((always_inline)) {
          f32x4 c = __builtin_bit_cast(f32x4, pt);
#pragma unroll
          for (int e = 0; e < 4; ++e) { c[e] += dppf<0x124>(c[e]); c[e] += dppf<0x128>(c[e]); c[e] = add_x16(c[e]); }
          if ((l & 28) == 0) *(LDS_AS f32x4*)(smem + dst + (w * 2 + (l >> 5)) * 64 + (l & 3) * 16) = c;
        };
        frag_t bfr[6];
#define QKV_BO(s, n) (bo[(n) & 1][SLOT((s) >> 2) >> 1] + (SLOT((s) >> 2) & 1) * STAGE + ((n) >> 1) * 8192 + ((s) & 3) * 2048)
#define QKV_RDA(s, n)                                                                             \
  {                                                                                               \
    if constexpr (IT == 0) bfr[n].hf.a = trabs(QKV_BO(s, n));                                     \
    else bfr[n].v = *(const LDS_AS u32x4*)(size_t)(bo[0][SLOT((s) >> 2) >> 1] + (SLOT((s) >> 2) & 1) * STAGE + (((s) & 3) * 6 + (n)) * 1024); \
  }
#define QKV_RDB(s, n) { if constexpr (IT == 0) bfr[n].hf.b = trabs(QKV_BO(s, (n) ^ 1) + 256); }
#pragma unroll
        for (int n = 0; n < 6; ++n) { QKV_RDA(0, n); if (n < 5) QKV_RDB(0, n); }
#define XF_OFF() (l & 8 ? ((l & 7) * RX + bi % RX) * 256 + h * 8 : (p * RX + (xcd >> 1) % RX) * 256 + (4 * rt + (l & 3)) * 8)
#define XP_OFF() (OFF_XNEW + ((rot + FXT) & 7) * 256)
#define FP_OFF(i) (w == 3 ? OFF_XNEW + (i) * 1024 : OFF_FX + h * 3072 + w * 1024)
#define FX_OK(n) __all(((n) == 4 ? max(max(umax4(fx[0]), umax4(fx[1])), max(umax4(fx[2]), umax4(fx[3]))) : max(umax4(fx[0]), umax4(fx[1]))) != POISON)
#define SINCE(s0) (vm_at(IT, s, n) - vm_at(IT, s0, 5))
#define DEF4(x) asm volatile("" : "=v"(x[0]), "=v"(x[1]), "=v"(x[2]), "=v"(x[3]))
        LDS_AS unsigned* qcnt = (LDS_AS unsigned*)(smem + L_QC);
        unsigned qc;
        unsigned qadr = lbase + L_QC, qone = 1;
        asm volatile("" : "+v"(qadr), "+v"(qone));
#define count_in(i) asm volatile("s_mov_b64 exec, 1\n\tds_add_u32 %0, %1 offset:%2\n\ts_mov_b64 exec, -1" ::"v"(qadr), "v"(qone), "n"(4 * (i)) : "memory")
        auto count_wait = [&](int i, unsigned want) __attribute__((always_inline)) {
          if (__builtin_expect((unsigned)__builtin_amdgcn_readfirstlane(qc) < want, 0)) {
            while ((unsigned)__builtin_amdgcn_readfirstlane(__hip_atomic_load(qcnt + i, __ATOMIC_RELAXED, __HIP_MEMORY_SCOPE_WORKGROUP)) < want) {}
          }
          asm volatile("" ::: "memory");
        };
        unsigned long long xok = xm0;
        auto x1_need = [&](unsigned long long m) __attribute__((always_inline)) {
          if (__builtin_expect((xok & m) != m, 0)) {
            for (unsigned spins = 0;;) {
              const u32x2 f = ld8(rs, F_X1 + ((p * RX + (xcd >> 1) % RX) * 32 + (l & 31)) * 8, 0);
              xok = __builtin_amdgcn_ballot_w64(f[0] == T[0] && f[1] == T[1]);
              if ((xok & m) == m) break;
              if (spin_fail(spins, rs, T)) { xok = ~0ull; break; }
              __builtin_amdgcn_s_sleep(KSL);
            }
            asm volatile("" ::: "memory");
          }
        };
        auto gap = [&](auto sc_, auto nc_) __attribute__((always_inline)) {
          constexpr int s = decltype(sc_)::value, n = decltype(nc_)::value;
          {
            constexpr int ts = (s + 3) / 4, tc = (s + 2) / 4, t0 = 1;
            if constexpr (ts >= 3 && ts < 8 && s == 4 * ts - 3 && n == 2) { wait_vm<vm_at(IT, s, n) - vm_tile(IT, ts)>(); count_in(0); }
            if constexpr (!IT && tc >= t0 && tc < 3 && s == 4 * tc - 2 && n == 1) { wait_vm<vm_at(IT, s, n) - vm_tile(IT, tc)>(); count_in(0); }
            if constexpr (IT && ts >= t0 && ts < 3 && s == 4 * ts - 3 && n == 2) count_in(0);
            if constexpr (tc >= t0 && tc < 8 && s == 4 * tc - 2) {
              if constexpr (n == 3) qc = __hip_atomic_load(qcnt, __ATOMIC_RELAXED, __HIP_MEMORY_SCOPE_WORKGROUP);
              if constexpr (n == 5) count_wait(0, 8 * (tc - t0 + 1));
            }
          }
          if constexpr (n == 0 && tile_piece(s)) QKV_DMA1(((s - 3) >> 2) + 3, (s - 3) & 3);
          if constexpr (IT == 0) {
            if constexpr (s == 0 && !(n & 1)) QKV_DMA1(1, n >> 1);
            if constexpr (s >= 1 && s < 4 && n == 2) QKV_DMA1(2, s - 1);
            constexpr int u = s + XL;
            if constexpr (n == 1 && u < 32) {
              if (s == 0 && w < 4) wait_vm<vm_at(0, s, n) - vm_x(u) + 4>();
              else if (s > TST && w >= TW) wait_vm<vm_at(0, s, n) - vm_x(u) + 1>();
              else wait_vm<vm_at(0, s, n) - vm_x(u)>();
              X_READ(u, af[(u & ~1) % WA][u & 1], af[(u | 1) % WA][u & 1]);
            }
            if constexpr (n == 3 && u + XU < 32) { asm volatile("s_waitcnt lgkmcnt(%0)" ::"n"(s + 1 < QL ? 4 : 3) : "memory"); X_REQ(u + XU, 0); }
            if constexpr (n == 5 && u + XU < 32) X_REQ(u + XU, 1);
            if constexpr (s == PB0 && n == 0) { POISON_BIG(0); }
            if constexpr (s == 0 && n == 1) count_in(2);
            if constexpr (s == 0 && n == 3) qc = __hip_atomic_load(qcnt + 2, __ATOMIC_RELAXED, __HIP_MEMORY_SCOPE_WORKGROUP);
            if constexpr (s == 0 && n == 5) count_wait(2, 8);
            if constexpr (s == 1 && n == 1) {
              asm volatile("" : "+v"(ep)::"memory");
              const unsigned long long e = (((unsigned long long)(unsigned)__builtin_amdgcn_readfirstlane(ep[1]) << 32) | (unsigned)__builtin_amdgcn_readfirstlane(ep[0])) + 1ull + dispatch_id() * 0x9E3779B97F4A7C16ull;
              T = u32x2{(unsigned)e, (unsigned)(e >> 32)};
            }
            if constexpr (s == RWS && n == 0) {
              if (w < 4) {
                PINV4(rows0);
                f32x2 sv[16];
#pragma unroll
                for (int i = 0; i < 4; ++i) unpack8v(rows0[i], sv + 4 * i);
                f32x2 t0 = sv[0] + sv[1], t1 = sv[2] + sv[3];
#pragma unroll
                for (int j = 4; j < 16; j += 2) { t0 += sv[j]; t1 += sv[j + 1]; }
                const float m = sum32(hadd(t0 + t1)) * (1.0f / 1024.0f);
                f32x2 m2 = {m, m}, q0 = {0.f, 0.f}, q1 = {0.f, 0.f};
                asm volatile("" : "+v"(m2));
#pragma unroll
                for (int j = 0; j < 16; j += 2) {
                  const f32x2 d0 = sv[j] - m2, d1 = sv[j + 1] - m2;
                  q0 = __builtin_elementwise_fma(d0, d0, q0); q1 = __builtin_elementwise_fma(d1, d1, q1);
                }
                const float rd = ln_rd(sum32(hadd(q0 + q1)) * (1.0f / 1024.0f));
                if ((l & 30) == 0) st4(__builtin_bit_cast(unsigned, (l & 1) ? -rbf1(m) : rd), rs, (l >> 5) * 4 + (l & 1) * 8192, OFF_AB + (p * 256 + h * 8 + 2 * w) * 4);
              }
            }
            if constexpr (s == POI && n == 4) {
              if (w == 3) {
                unsigned pz1 = POISON;
                asm volatile("" : "+v"(pz1));
                const unsigned prb = 2 * p + (h >> 4), pct = h & 15, m = l - 32;
                static_assert(OFF_CB == OFF_CA + 131072, "poison addressing");
                if (l < 60) st16(u32x4{pz1, pz1, pz1, pz1}, rs, l < 32 ? OFF_CA + (l & 8) * 4096 + (l & 16) * 8192 + prb * 2048 + pct * 128 + (l & 7) * 16
                                                             : l < 56 ? OFF_FX + ((h * 6 + (m >> 2)) * 8 + p) * 64 + (l & 3) * 16
                                                                      : OFF_AB + 16384 + (p * 32 + h) * 32 + (l & 1) * 16 + (l & 2) * 4096, 0);
                if (l < 4) st4(pz1, rs, l < 2 ? OFF_XNEW + bid * 8 + l * 4 : OFF_KEY + ((l & 1) * 16 + pct) * 128 + prb * 4, 0);
              }
            }
            if constexpr (s == POI && n == 2) {
              if (w == 2 && l < 6) {
                unsigned f0 = F_KV + KVF(0, p * 32 + h), f1 = F_O + OFB(0, p) + OFL(h), f2 = F_X1 + ((rb >> 1) * RX * 32 + (l & 3) * 32 + (rb & 1) * 16 + ct) * 8;
                asm volatile("" : "+v"(f0), "+v"(f1), "+v"(f2));
                const unsigned fo = l == 5 ? f0 : l == 4 ? f1 : f2;
                touchb(tdum, rs, fo);
              }
            }
            static_assert(vm_x(EIN + XL) >= vm_at(0, EIN - 3, 5) && IMG >= 1 && CSD >= 1, "first flag");
            if constexpr (s == EIN && n == 1) { if (w < 4) count_in(1); }
            if constexpr (s == EIN + 2 && n == 0) qc = __hip_atomic_load(qcnt + 1, __ATOMIC_RELAXED, __HIP_MEMORY_SCOPE_WORKGROUP);
            if constexpr (s == EIN + 2 && n == 2) {
              if (w == 0) { count_wait(1, 4); if (l < RX) st8(T, rs, F_XN + (p * RX + l) * 256 + h * 8, 0); }
            }
            if constexpr (s == ERQ && n == 4) asm volatile("buffer_load_dwordx2 %0, %1, %2, 0 offen sc1" : "=v"(cxn) : "v"(XF_OFF() + F_XN), "s"(rs) : "memory");
            if constexpr (s == CSL && n == 2) {
              wait_vm<SINCE(ERQ)>();
              asm volatile("" : "+v"(cxn)::"memory");
              if (!__all(cxn[0] == T[0] && cxn[1] == T[1])) poll_flags(rs, XF_OFF(), F_XN, T);
            }
          } else {
#define X1_OFF() (F_X1 + ((p * RX + (xcd >> 1) % RX) * 32 + (l & 31)) * 8)
            if constexpr ((s == 0 || s == 4) && n == 2) asm volatile("buffer_load_dwordx2 %0, %1, %2, 0 offen sc1" : "=v"(cxn) : "v"(X1_OFF()), "s"(rs) : "memory");
            if constexpr ((s == 3 || s == 7) && n == 0) {
              wait_vm<vm_at(1, s, 1) - vm_at(1, s - 3, 3)>();
              asm volatile("" : "+v"(cxn)::"memory");
              xok |= __builtin_amdgcn_ballot_w64(cxn[0] == T[0] && cxn[1] == T[1]);
            }
            if constexpr (a_req(s, n) >= 0 && (a_req(s, n) & 7) == 0) x1_need(0x30003ull << (((rot + a_req(s, n) / 8) & 7) * 2));
          }
          if constexpr (IT && a_req(s, n) >= 0) A_LOAD1(a_req(s, n) / 2, a_req(s, n) & 1);
          if constexpr (IT == 0 && s == CSD && n == 4) {
            if (w < 3) {
              unsigned a0 = FRAG_T() + SLOT(s >> 2) * STAGE + w * 8192, a1 = a0 ^ 32, one2 = 0x3F803F80u;
              asm volatile("" : "+v"(a0), "+v"(a1), "+v"(one2));
              const u32x4 o4 = {one2, one2, one2, one2};
              f32x4 y0 = {0.f, 0.f, 0.f, 0.f}, y1 = y0;
#pragma unroll
              for (int i = 0; i < 4; ++i) { y0 = mfma16(frag_t2(a0 + i * 2048), o4, y0); y1 = mfma16(frag_t2(a1 + i * 2048), o4, y1); }
              if ((l & 14) == 0) st16(__builtin_bit_cast(u32x4, (l & 1) ? y1 : y0), rs, g * 16 + (l & 1) * 512, OFF_CS + ((h * 6 + 2 * w) * 8 + p) * 64);
            }
          }
          if constexpr (!IT && s == CSL && n == 4) { asm volatile("" : "=v"(fx[0])); if (w < 3) ld16h<0>(fx[0], rs, l * 16, OFF_CS + h * 3072 + w * 1024); }
          if constexpr (!IT && s == CSS && n == 2) {
            if (w < 3) {
              wait_vm<SINCE(CSL)>();
              asm volatile("" : "+v"(fx[0])::"memory");
              tile_sum(fx[0], L_CSL);
            }
          }
          if constexpr (s == ABL && n == 4) ld4h<0>(aq[0], rs, (l & 31) * 4 + (l >> 5) * 8192, AB_OFF());
          if constexpr (s == ABS && n == 0) {
            wait_vm<SINCE(ABL)>();
            asm volatile("" : "+v"(aq[0])::"memory");
            if constexpr (IT) {
              for (unsigned spins = 0; !__all(aq[0] != POISON) && !spin_fail(spins, rs, T);) {
                ld4h<0>(aq[0], rs, (l & 31) * 4 + (l >> 5) * 8192, AB_OFF());
                asm volatile("s_waitcnt vmcnt(0)" : "+v"(aq[0])::"memory");
              }
            }
            *(LDS_AS unsigned*)(smem + L_K + (8 + rt) * 2048 + l * 4) = aq[0];
          }
          if constexpr (s == ABS && n == 2) {
#pragma unroll
            for (int a = 0; a < 2; ++a) {
              const LDS_AS char* ab = (const LDS_AS char*)(smem + L_K + (8 + rt) * 2048 + a * 64);
              aq[a] = *(const LDS_AS unsigned*)(ab + (l & 15) * 4); bq[a] = *(const LDS_AS unsigned*)(ab + 128 + (l & 15) * 4);
              av[a] = *(const LDS_AS u32x4*)(ab + g * 16); bv[a] = *(const LDS_AS u32x4*)(ab + 128 + g * 16);
            }
          }
          if constexpr (s == ABS && n == 5) {
#pragma unroll
            for (int a = 0; a < 2; ++a) { b2[a] = f32x2{asf(bq[a]), asf(bq[a])}; asm volatile("" : "+v"(b2[a])); }
          }
          if constexpr (s == ABS + 1) {
            constexpr int k = (n + 4) % 6;
            if constexpr (k < 4) cs4[k & 1] = *(const LDS_AS f32x4*)(smem + L_CSL + k * 64 + g * 16);
            else cs4[k & 1][0] = *(const LDS_AS float*)(smem + L_CSL + k * 64 + (l & 15) * 4);
          }
          if constexpr ((s == ABS + 1 && n > 0) || (s == ABS + 2 && n == 0)) {
            constexpr int k = (n + 3) % 6;
            if constexpr (k < 4) {
#pragma unroll
              for (int a = 0; a < 2; ++a) acc[a][k] = __builtin_elementwise_fma(cs4[k & 1], f32x4{b2[a][0], b2[a][1], b2[a][0], b2[a][1]}, acc[a][k]);
            } else {
              f32x2 c2 = {cs4[k & 1][0], cs4[k & 1][0]};
              asm volatile("" : "+v"(c2));
#pragma unroll
              for (int a = 0; a < 2; ++a) acc[a][k] = __builtin_elementwise_fma(f32x4{c2[0], c2[1], c2[0], c2[1]}, __builtin_bit_cast(f32x4, bv[a]), acc[a][k]);
            }
          }
          if constexpr (IT == 0) {
            if constexpr (s == IMG - 2 && n < 3) {
              const int c = 3 * (w & 1) + n;
              ka[n] = (FRAG_T() ^ ((c & 1) << 5)) + (SLOT(5) * STAGE + (c >> 1) * 8192 + (w >> 1) * 2048);
              asm volatile("" : "+v"(ka[n]));
            }
            if constexpr (s == IMG - 1 && (n & 1)) keep[n >> 1] = frag_t2(ka[n >> 1]);
            if constexpr (s > KP0 && s < KP0 + 3 && (n & 1)) keep[3 * (s - KP0) + (n >> 1)] = frag_t2(ka[n >> 1] + (s - KP0) * STAGE);
            if constexpr (s >= IMG && s < IMG + 3 && n == 4) st16(keep[s - IMG], rs, l * 16 + (s - IMG) * 1024, OFF_WI + (h * 8 + ((rot + 5) & 7)) * STAGE + w * 3072);
            if constexpr (s >= TS0 && s < TS0 + TSN && n == 4) {
              if (tstep == s && w < TW) touch(tdum, tsrc);
            }
            if constexpr (s == TST && n == 4) { if (w >= TW) touch(tdum, tsrc); }
          } else {
            if constexpr (s == PB1 && n == 0) { POISON_BIG(1); }
            if constexpr (s == FKL && n == 4) ld4h<0>(kv, rs, (l & 15) * 4, OFF_KEY + (bid & 15) * 128);
            if constexpr (s == FSL && n == 4) {
              wait_vm<SINCE(FKL)>();
              asm volatile("" : "+v"(kv)::"memory");
              if (__builtin_expect(!__all(kv != POISON), 0)) {
                LDS_AS unsigned* kw = (LDS_AS unsigned*)(smem + L_SKEY + 40);
                if (w == 0) {
                  kv = key_wait(rs, T, l, 0, bid & 15, kv);
                } else {
                  while ((kv = __hip_atomic_load(kw, __ATOMIC_RELAXED, __HIP_MEMORY_SCOPE_WORKGROUP)) == POISON) __builtin_amdgcn_s_sleep(2);
                }
              }
              fkey = __builtin_amdgcn_readfirstlane(umax16(kv));
              if (w == 0) *(LDS_AS unsigned*)(smem + L_SKEY + 40) = min(fkey, POISON - 1);
              sel0 = key_row(fkey);
              aff = (int)(sel0 >> 8) == p;
              row_load(rs, g, ffn_xoff(fkey, 0, w), fx);
            }
            if constexpr (s == FS0 && n == FSG) {
              wait_vm<SINCE(FSL)>();
              PINV4(fx);
              asm volatile("" : "=v"(sa), "=v"(sb));
              if (ffn_on) ffn_gu(gv, uv, fx, sa, sb);
            }
            if constexpr (s == FS0 && n == FSG + 1) {
              if (ffn_on) {
                ffn_gu_check(rs, T, ffn_xoff(fkey, 0, w), l, gv, uv, fx, sa, sb);
                ffn_gu_store(smem, l, w, sa, sb);
                count_in(1);
              }
            }
            if constexpr (s == FS0D && n == FDG) {
              if (ffn_on && w < 4) {
                s16x4 da[8], db[8];
                ffn_d_read(smem, l, 2 * w, da);
                while (__hip_atomic_load(qcnt + 1, __ATOMIC_RELAXED, __HIP_MEMORY_SCOPE_WORKGROUP) < 8u) {}
                asm volatile("" ::: "memory");
                const f32x4 t = ffn_t_read(smem, l);
                ffn_d_read(smem, l, 2 * w + 1, db);
                u32x2 ha = ffn_h(t, l), hb = {dppu<0x118>(ha[0]), dppu<0x118>(ha[1])};
                f32x4 ya = {0.f, 0.f, 0.f, 0.f}, yb = ya;
#pragma unroll
                for (int i = 0; i < 8; ++i) {
                  ya = mfma16k16(da[i], __builtin_bit_cast(s16x4, ha), ya); yb = mfma16k16(db[i], __builtin_bit_cast(s16x4, hb), yb);
                  ha = u32x2{dppu<0x111>(ha[0]), dppu<0x111>(ha[1])}; hb = u32x2{dppu<0x111>(hb[0]), dppu<0x111>(hb[1])};
                }
                ffn_d_store(rs, l, 8 * w, 0, slice, ya + yb);
              }
            }
#define RED_LOAD(ld, l8)                                                                                                    \
  {                                                                                                                         \
    const unsigned G = bi, po = OFF_PART + G * 32768;                                                        \
    ld(gv[0], rs, tid * 16, po); ld(gv[1], rs, tid * 16, po + 4096); ld(gv[2], rs, tid * 16, po + 8192); ld(gv[3], rs, tid * 16, po + 12288); \
    ld(uv[0], rs, tid * 16, po + 16384); ld(uv[1], rs, tid * 16, po + 20480); ld(uv[2], rs, tid * 16, po + 24576);        \
    l8(cxn, rs, (l & 3) * 16 + (l & 4) * 2, OFF_CA + (sel0 >> 7) * 2048 + G * 64);                                         \
    l8(cx1, rs, (l & 3) * 16 + (l & 4) * 2, OFF_CB + (sel0 >> 7) * 2048 + G * 64);                                          \
  }
            if constexpr (s == FS1 && n == 4) {
              asm volatile("" : "=v"(cxn), "=v"(cx1));
              if (!ffn_on && w < 4) RED_LOAD(ld16u, ld8h);
            }
            if constexpr (s == FS1S && n == FRG) {
              if (!ffn_on && w < 4) {
                wait_vm<SINCE(FS1)>();
                PINV4(gv); PINV4(uv);
                asm volatile("" : "+v"(cxn), "+v"(cx1)::"memory");
                for (unsigned spins = 0; !__all(max(max(max(umax4(gv[0]), umax4(gv[1])), max(umax4(gv[2]), umax4(gv[3]))),
                                                    max(max(umax4(uv[0]), umax4(uv[1])), max(max(umax4(uv[2]), max(cxn[0], cxn[1])), max(cx1[0], cx1[1])))) != POISON) && !spin_fail(spins, rs, T);) {
                  RED_LOAD(ld16u, ld8h);
                  DRAIN();
                  PINV4(gv); PINV4(uv);
                  asm volatile("" : "+v"(cxn), "+v"(cx1)::"memory");
                }
#define F4(x) __builtin_bit_cast(f32x4, x)
                ((LDS_AS f32x4*)(smem + L_SFIN))[tid] = ((F4(gv[0]) + F4(gv[1])) + (F4(gv[2]) + F4(gv[3]))) + ((F4(uv[0]) + F4(uv[1])) + F4(uv[2]));
#undef F4
                count_in(2);
                if (w == 0) {
                  while (__hip_atomic_load(qcnt + 2, __ATOMIC_RELAXED, __HIP_MEMORY_SCOPE_WORKGROUP) < 4u) {}
                  asm volatile("" ::: "memory");
                  const u32x2 ov = red_final<4>(smem, l, fkey, cxn, cx1);
                  if (l < 8) st8(unpoison2(ov), rs, OFF_XNEW + (bi * 8 + (l & 3) * 2 + (l >> 2)) * 8, 0);
                }
              }
            }
            if constexpr (s == FXA && n == 4) { DEF4(fx); if (w < 3) row_load(rs, g, XP_OFF(), fx); }
            if constexpr (s == FXB && n == FXG) {
              if (w < 3) {
                const unsigned fa = bo[0][0] + SLOT(FXT) * STAGE + w * 2048;
                u32x4 f[8];
#pragma unroll
                for (int i = 0; i < 8; ++i) f[i] = *(const LDS_AS u32x4*)(size_t)(fa + (i >> 1) * 6144 + (i & 1) * 1024);
                wait_vm<SINCE(FXA)>();
                PINV4(fx);
                ffn_xwait<XSL>(rs, T, XP_OFF(), l, fx);
                f32x4 y0 = mfma16(f[0], fx[0], f32x4{0.f, 0.f, 0.f, 0.f}), y1 = mfma16(f[1], fx[0], f32x4{0.f, 0.f, 0.f, 0.f});
#pragma unroll
                for (int i = 1; i < 4; ++i) { y0 = mfma16(f[2 * i], fx[i], y0); y1 = mfma16(f[2 * i + 1], fx[i], y1); }
                if ((l & 14) == 0) st16(unpoison4(__builtin_bit_cast(u32x4, (l & 1) ? y1 : y0)), rs, g * 16 + (l & 1) * 512, OFF_FX + ((h * 6 + 2 * w) * 8 + p) * 64);
              }
            }
            if constexpr (s == FXL && n == 4) {
              asm volatile("" : "=v"(fx[0]), "=v"(fx[1]));
              if (aff && w < 4) { ld16h<0>(fx[0], rs, l * 16, FP_OFF(0)); ld16h<0>(fx[1], rs, l * 16, FP_OFF(1)); }
            }
            if constexpr (s == FXS && n == 2) {
              if (aff && w < 4) {
                wait_vm<0>();
                asm volatile("" : "+v"(fx[0]), "+v"(fx[1])::"memory");
                if (!FX_OK(2)) {
                  unsigned spins = 0;
                  do {
                    if (spin_fail(spins, rs, T)) break;
                    fx[0] = ld16(rs, l * 16, FP_OFF(0)); fx[1] = ld16(rs, l * 16, FP_OFF(1));
                  } while (!FX_OK(2));
                }
                if (w == 3) {
                  f32x2 nx[8];
                  unpack8v(fx[0], nx); unpack8v(fx[1], nx + 4);
                  const f32x2 sm = ((nx[0] + nx[1]) + (nx[2] + nx[3])) + ((nx[4] + nx[5]) + (nx[6] + nx[7]));
                  const float mean = sum64(hadd(sm)) * (1.0f / 1024.0f);
                  const f32x2 m2 = {mean, mean};
                  f32x2 q = {0.f, 0.f};
#pragma unroll
                  for (int j = 0; j < 8; ++j) { const f32x2 d = nx[j] - m2; q = __builtin_elementwise_fma(d, d, q); }
                  const float rd = ln_rd(sum64(hadd(q)) * (1.0f / 1024.0f));
                  if (l == 0) *(LDS_AS f32x2*)(smem + L_FXL + 384) = f32x2{rd, -rbf1(mean)};
                } else {
                  tile_sum(fx[0], L_FXL);
                }
                count_in(3);
              }
            }
          }
        };
        static_for<32>([&](auto sc_) __attribute__((always_inline)) {
          constexpr int s = decltype(sc_)::value;
          if constexpr (s > 0) PIN_ACC();
          if constexpr (IT == 1) wait_vm<vm_at(IT, s, 0) - vm_a(IT, s)>();
          pin_w<s % WA>(af[s % WA][0], af[s % WA][1]);
          SB();
          static_for<6>([&](auto nc_) __attribute__((always_inline)) {
            constexpr int n = decltype(nc_)::value;
            if constexpr (s == 0) { acc[0][n] = f32x4{0.f, 0.f, 0.f, 0.f}; acc[1][n] = acc[0][n]; }
            constexpr bool on = s < QL || n >= 2;
            if constexpr (on) acc[0][n] = n < 4 ? mfma16(bfr[n].v, af[s % WA][0], acc[0][n]) : mfma16(af[s % WA][0], bfr[n].v, acc[0][n]);
            SB();
            if constexpr (n == 0) { QKV_RDB(s, 5); }
            else if constexpr (s + 1 < 32 && (s + 1 < QL || n > 2)) QKV_RDB(s + 1, n - 1);
            SB();
            if constexpr (on) acc[1][n] = n < 4 ? mfma16(bfr[n].v, af[s % WA][1], acc[1][n]) : mfma16(af[s % WA][1], bfr[n].v, acc[1][n]);
            SB();
            if constexpr (s + 1 < 32 && (s + 1 < QL || n >= 2)) QKV_RDA(s + 1, n);
            SB();
            gap(sc_, nc_);
            SB();
          });
        });
        PIN_ACC();
        frag_t qw[32 - QL][2];
        static_for<32 - QL>([&](auto c_) __attribute__((always_inline)) {
          constexpr int s = QL + decltype(c_)::value;
          QKV_RDA(s, 0); QKV_RDB(s, 0); QKV_RDA(s, 1); QKV_RDB(s, 1);
          qw[s - QL][0] = bfr[0]; qw[s - QL][1] = bfr[1];
        });
        SB();
        const bool own = aff && (int)((sel0 >> 5) & 7) == rt;
        const bool m0 = !(sel0 & 16), mq = (l & 15) == (int)(sel0 & 15), mv = g == (int)((sel0 & 15) >> 2);
        auto patch_qk = [&](int n) __attribute__((always_inline)) {
          f32x4 t = *(const LDS_AS f32x4*)(smem + L_FXL + n * 64 + g * 16);
          const f32x4 c = *(const LDS_AS f32x4*)(smem + L_CSL + n * 64 + g * 16);
          const float nb = *(const LDS_AS float*)(smem + L_FXL + 388);
#pragma unroll
          for (int e = 0; e < 4; ++e) { t[e] = __builtin_fmaf(c[e], nb, t[e]); asm volatile("" : "+v"(t[e])); }
          acc[0][n] = (mq && m0) ? t : acc[0][n];
          acc[1][n] = (mq && !m0) ? t : acc[1][n];
        };
        if (own) {
          while (__hip_atomic_load(qcnt + 3, __ATOMIC_RELAXED, __HIP_MEMORY_SCOPE_WORKGROUP) < 4u) {}
          asm volatile("" ::: "memory");
          patch_qk(2); patch_qk(3);
          const f32x2 nab = *(const LDS_AS f32x2*)(smem + L_FXL + 384);
          const LDS_AS float* fl_ = (const LDS_AS float*)(smem + L_FXL + 256) + (l & 15);
          const LDS_AS float* cl_ = (const LDS_AS float*)(smem + L_CSL + 256) + (l & 15);
          const float t4 = __builtin_fmaf(cl_[0], nab[1], fl_[0]), t5 = __builtin_fmaf(cl_[16], nab[1], fl_[16]);
          const unsigned na = __builtin_bit_cast(unsigned, (float)nab[0]);
#pragma unroll
          for (int a = 0; a < 2; ++a) {
            const bool ma = a ? !m0 : m0;
            aq[a] = (mq && ma) ? na : aq[a];
#pragma unroll
            for (int e = 0; e < 4; ++e) {
              const bool me = mv && ma && e == (int)(sel0 & 3);
              acc[a][4][e] = me ? t4 : acc[a][4][e]; acc[a][5][e] = me ? t5 : acc[a][5][e];
              av[a][e] = me ? na : av[a][e];
            }
          }
        }
        constexpr float C2 = 1.4426950408889634f * 0.17677669529663687f;
#pragma unroll
        for (int a = 0; a < 2; ++a) {
          f32x2 a2 = {asf(aq[a]), asf(aq[a])};
          qs2[a] = f32x2{asf(aq[a]) * C2, asf(aq[a]) * C2};
          asm volatile("" : "+v"(a2), "+v"(qs2[a]));
          acc[a][2] *= f32x4{a2[0], a2[1], a2[0], a2[1]}; acc[a][3] *= f32x4{a2[0], a2[1], a2[0], a2[1]};
          acc[a][4] *= __builtin_bit_cast(f32x4, av[a]); acc[a][5] *= __builtin_bit_cast(f32x4, av[a]);
        }
        k0 = pack8(acc[0][2], acc[0][3]); k1 = pack8(acc[1][2], acc[1][3]); v0 = pack8(acc[0][4], acc[1][4]); v1 = pack8(acc[0][5], acc[1][5]);
        const unsigned kvo = OFF_KV + IT * (8u << 20) + (p * 32 + h) * 32768 + rt * 4096;
        if (p < 7) { st16(k0, rs, l * 16, kvo); st16(k1, rs, l * 16 + 1024, kvo); st16(v0, rs, l * 16 + 2048, kvo); st16(v1, rs, l * 16 + 3072, kvo); }
        char* ownp = smem + (8 + rt) * 2048 + l * 16;
        *(u32x4*)(ownp + L_K) = k0;
        if (rt < 7) *(u32x4*)(ownp + L_K + 1024) = k1;
        *(u32x4*)(ownp + L_V) = v0; *(u32x4*)(ownp + L_V + 1024) = v1;
        SB();
        static_for<32 - QL>([&](auto c_) __attribute__((always_inline)) {
          constexpr int s = QL + decltype(c_)::value;
#pragma unroll
          for (int n = 0; n < 2; ++n) { acc[0][n] = mfma16(qw[s - QL][n].v, af[s % WA][0], acc[0][n]); acc[1][n] = mfma16(qw[s - QL][n].v, af[s % WA][1], acc[1][n]); }
        });
        if (own) { patch_qk(0); patch_qk(1); }
      };
      qkv_loop(it_c);
    }
#undef A_LOAD1
#undef QKV_RDA
#undef QKV_RDB
#undef QKV_BO
    {
      IDS();
      const GAS bf16 *pwo, *pwg, *pwu;
      asm volatile("s_load_dwordx2 %0, %3, 24\n\ts_load_dwordx2 %1, %3, 40\n\ts_load_dwordx2 %2, %3, 48" : "=&s"(pwo), "=&s"(pwg), "=&s"(pwu) : "s"(ka));
      const int ql = l & 15;
      const f32x4 z4 = {0.f, 0.f, 0.f, 0.f}, ninf = {-INFINITY, -INFINITY, -INFINITY, -INFINITY};
      LDS_AS unsigned* rdy = (LDS_AS unsigned*)(smem + L_RDY);
      const unsigned rtag = T[0] + it;
      u32x4 qf[2];
      f32x4 cn[2], oa[2][2], sum[2], S[2][2];
      unsigned one2 = 0x3F803F80u;
      asm volatile("" : "+v"(one2));
      const u32x4 ones = {one2, one2, one2, one2};
      auto ex8 = [&](f32x4 a, f32x4 b) __attribute__((always_inline)) {
        return u32x4{pack2(__builtin_amdgcn_exp2f(a[0]), __builtin_amdgcn_exp2f(a[1])), pack2(__builtin_amdgcn_exp2f(a[2]), __builtin_amdgcn_exp2f(a[3])),
                     pack2(__builtin_amdgcn_exp2f(b[0]), __builtin_amdgcn_exp2f(b[1])), pack2(__builtin_amdgcn_exp2f(b[2]), __builtin_amdgcn_exp2f(b[3]))};
      };
      {
#pragma unroll
        for (int a = 0; a < 2; ++a) {
          const f32x4 c4 = {qs2[a][0], qs2[a][1], qs2[a][0], qs2[a][1]};
          qf[a] = pack8(acc[a][0] * c4, acc[a][1] * c4);
        }
        f32x4 mk;
#pragma unroll
        for (int e = 0; e < 4; ++e) mk[e] = (4 * g + e <= ql) ? 0.f : -INFINITY;
        const f32x4 d0 = mfma16(k0, qf[0], mk), d1 = mfma16(k1, qf[1], mk), d2 = mfma16(k0, qf[1], z4);
        const float m0 = -max_x32(max_x16(fmaxf(fmaxf(d0[0], d0[1]), fmaxf(d0[2], d0[3]))));
        const float m1 = -max_x32(max_x16(fmaxf(fmaxf(d1[0], d1[1]), fmaxf(d1[2], d1[3]))));
        cn[0] = f32x4{m0, m0, m0, m0}; cn[1] = f32x4{m1, m1, m1, m1};
        asm volatile("" : "+v"(cn[0]), "+v"(cn[1]));
        const f32x4 e0 = d0 + cn[0];
        const u32x4 p0 = {pack2(__builtin_amdgcn_exp2f(e0[0]), __builtin_amdgcn_exp2f(e0[1])), pack2(__builtin_amdgcn_exp2f(e0[2]), __builtin_amdgcn_exp2f(e0[3])), 0u, 0u};
        const u32x4 p1 = ex8(d2 + cn[1], d1 + cn[1]);
        oa[0][0] = mfma16(v0, p0, z4); oa[0][1] = mfma16(v1, p0, z4); sum[0] = mfma16(ones, p0, z4);
        oa[1][0] = mfma16(v0, p1, z4); oa[1][1] = mfma16(v1, p1, z4); sum[1] = mfma16(ones, p1, z4);
      }
      const char* kb_ = smem + L_K + l * 16;
      const char* vb_ = smem + L_V + l * 16;
      const unsigned wov = (l >> 3) * 2048 + (((((l & 7) >> 1) ^ ((l >> 4) & 3)) * 2 + (l & 1)) << 4), fnv = (l >> 1) * (F_ * 2) + (l & 1) * 16;
      const unsigned pp = __builtin_amdgcn_readfirstlane(p > 0 ? 32 : 0), pvo = OFF_KV + it * (8u << 20) + (p * 32 + h - pp) * 32768;
      const unsigned pfo = __builtin_amdgcn_readfirstlane(F_KV + KVF(it, p * 32 + h - pp));
      asm volatile("" : "+v"(oa[0][0]), "+v"(oa[0][1]), "+v"(oa[1][0]), "+v"(oa[1][1]), "+v"(sum[0]), "+v"(sum[1]));
      DRAIN();
      asm volatile("" : "+v"(tdum));
      if (l == 0) st8(T, rs, rt * 8, F_KV + KVF(it, p * 32 + h));
      BAR();
      asm volatile("" : "+s"(pwo), "+s"(pwg), "+s"(pwu));
      WO_PIECE(15); WO_PIECE(14);
      u32x4 kf[2], vf[2];
      int nw = 0;
      u32x4 p0, p1;
      float e0, e1;
#define EE(v, i) { e0 = __builtin_amdgcn_exp2f(v[i]); e1 = __builtin_amdgcn_exp2f(v[i + 1]); pin2s(e0, e1); SB(); }
#define CV(P, i) { unsigned t_ = pack2(e0, e1); pin1(t_); P[i] = t_; SB(); }
      auto body = [&](unsigned ad, auto dk_) __attribute__((always_inline)) {
        constexpr int dk = decltype(dk_)::value;
        const f32x4 a0 = S[1][0], a1 = S[1][1];
        sum[0] = mfma16(ones, p0, sum[0]); SB();
        EE(a0, 0);
        lds_wait<2>(kf[0], kf[1]);
        S[0][0] = mfma16(kf[0], qf[0], cn[0]); SB();
        CV(p1, 0); EE(a0, 2);
        S[0][1] = mfma16(kf[1], qf[0], cn[0]); SB();
        CV(p1, 1); EE(a1, 0);
        lds_wait<0>(vf[0], vf[1]);
        oa[0][0] = mfma16(vf[0], p0, oa[0][0]); SB();
        CV(p1, 2); EE(a1, 2);
        oa[0][1] = mfma16(vf[1], p0, oa[0][1]); SB();
        CV(p1, 3);
        S[1][0] = mfma16(kf[0], qf[1], cn[1]); SB();
        EE(S[0][0], 0);
        S[1][1] = mfma16(kf[1], qf[1], cn[1]); SB();
        lds2<2048 + dk>(kf[0], kf[1], ad); SB();
        CV(p0, 0); EE(S[0][0], 2);
        oa[1][0] = mfma16(vf[0], p1, oa[1][0]); SB();
        CV(p0, 1); EE(S[0][1], 0);
        oa[1][1] = mfma16(vf[1], p1, oa[1][1]); SB();
        lds2<2048 + L_V>(vf[0], vf[1], ad); SB();
        CV(p0, 2); EE(S[0][1], 2);
        sum[1] = mfma16(ones, p1, sum[1]); SB();
        CV(p0, 3);
      };
      auto last = [&]() __attribute__((always_inline)) {
        sum[0] = mfma16(ones, p0, sum[0]); SB();
        EE(S[1][0], 0);
        lds_wait<0>(vf[0], vf[1]);
        pin2(kf[0], kf[1]);
        oa[0][0] = mfma16(vf[0], p0, oa[0][0]); SB();
        CV(p1, 0); EE(S[1][0], 2);
        oa[0][1] = mfma16(vf[1], p0, oa[0][1]); SB();
        CV(p1, 1); EE(S[1][1], 0);
        const float f0 = __builtin_amdgcn_exp2f(S[1][1][2]), f1 = __builtin_amdgcn_exp2f(S[1][1][3]);
        p1[2] = pack2(e0, e1); p1[3] = pack2(f0, f1);
        oa[1][0] = mfma16(vf[0], p1, oa[1][0]); oa[1][1] = mfma16(vf[1], p1, oa[1][1]); sum[1] = mfma16(ones, p1, sum[1]);
      };
      auto piece = [&]() __attribute__((always_inline)) {
        if constexpr (it == 0) { if (ffn_on) { FFN_DMA(nw); } }
        ++nw;
      };
      if (rt > 0) {
        unsigned ad = lbase + l * 16 + (6 + rt) * 2048;
        lds2<2048>(kf[0], kf[1], ad); lds2<2048 + L_V>(vf[0], vf[1], ad);
        lds_wait<2>(kf[0], kf[1]);
        S[0][0] = mfma16(kf[0], qf[0], cn[0]); S[0][1] = mfma16(kf[1], qf[0], cn[0]); S[1][0] = mfma16(kf[0], qf[1], cn[1]); S[1][1] = mfma16(kf[1], qf[1], cn[1]);
        SB();
        lds_wait<0>(vf[0], vf[1]);
        lds2<0>(kf[0], kf[1], ad);
        p0 = ex8(S[0][0], S[0][1]);
        lds_wait<0>(kf[0], kf[1]);
#pragma unroll 1
        for (int i = rt - 1; i > 0; --i) {
          ad -= 2048;
          body(ad, std::integral_constant<int, -2048>{});
          piece();
          SB();
        }
        piece();
        last();
      }
      {
        constexpr int FW = 4, FN = 8 / FW;
        if (w < FW) {
          const int g0 = FN * rt;
          poll_flags(rs, (l & (FN - 1)) * 8, pfo + g0 * 8, T);
#pragma unroll
          for (int j = 0; j < FN; ++j) {
            const unsigned d = lbase + (g0 + j) * 2048, o = pvo + (g0 + j) * 4096;
            dma16s<0>(rs, l * 16, o, d + L_K); dma16s<1024>(rs, l * 16, o, d + L_K);
            dma16s<0>(rs, l * 16, o + 2048, d + L_V); dma16s<1024>(rs, l * 16, o + 2048, d + L_V);
          }
          if (it == 1 && l == 0) rdy[8 + w] = rtag;
        } else if (it == 1 && w < 6) {
          while (!__all(__hip_atomic_load(rdy + 8 + (l & (FW - 1)), __ATOMIC_RELAXED, __HIP_MEMORY_SCOPE_WORKGROUP) == rtag)) {}
#pragma unroll 1
          for (int j = w == 4 ? 13 : 5; j > (w == 4 ? 5 : 3); --j) {
#pragma unroll
            for (int v = 0; v < 8; ++v) WO_PIECE_OF(v, j);
          }
        }
        if (p == 0) { cn[0] = ninf; cn[1] = ninf; }
        f32x4 mk;
#pragma unroll
        for (int e = 0; e < 4; ++e) mk[e] = (4 * g + e > ql) ? 0.f : -INFINITY;
        const f32x4 cm0 = cn[0] + mk, cm1 = cn[1] + mk;
        if (w < FW) {
          if constexpr (FN > 1) { wait_vm<2 * FN>(); if (l < FN / 2) rdy[FN * rt + l] = rtag; }
          DRAIN();
          if (l < FN) rdy[FN * rt + l] = rtag;
        }
        if constexpr (it == 0) { WO_PIECE(13); WO_PIECE(12); if (ffn_on) FFN_DMA(7); }

        int have = rt - 1;
        auto need = [&](int gq) __attribute__((always_inline)) {
          if (__builtin_expect(gq > have && gq < 8, 0)) {
            do {
              const unsigned t = __hip_atomic_load(rdy + (l & 7), __ATOMIC_RELAXED, __HIP_MEMORY_SCOPE_WORKGROUP);
              have = rt - 1 + __builtin_ctz(~((unsigned)__builtin_amdgcn_ballot_w64(t == rtag) & 0xffu) >> rt);
            } while (gq > have);
            asm volatile("" ::: "memory");
          }
        };
        unsigned ad = lbase + l * 16 + rt * 2048;
        need(rt);
        lds2<0>(kf[0], kf[1], ad); lds2<L_V>(vf[0], vf[1], ad);
        lds_wait<2>(kf[0], kf[1]);
        S[0][0] = mfma16(kf[0], qf[0], cm0); S[0][1] = mfma16(kf[1], qf[0], cn[0]); S[1][0] = ninf; S[1][1] = mfma16(kf[1], qf[1], cm1);
        SB();
        lds_wait<0>(vf[0], vf[1]);
        need(rt + 1);
        lds2<2048>(kf[0], kf[1], ad);
        p0 = ex8(S[0][0], S[0][1]);
        lds_wait<0>(kf[0], kf[1]);
#pragma unroll 1
        for (int gq = rt + 1; gq < 8; ++gq) {
          need(gq + 1);
          body(ad, std::integral_constant<int, 2048>{});
          piece();
          SB();
          ad += 2048;
        }
        last();
      }
      {
        float big = 0.f;
#pragma unroll
        for (int a = 0; a < 2; ++a)
#pragma unroll
          for (int d = 0; d < 2; ++d)
            big = __builtin_elementwise_maximum(__builtin_elementwise_maximum(big, __builtin_elementwise_maximum(fabsf(oa[a][d][0]), fabsf(oa[a][d][1]))),
                                                __builtin_elementwise_maximum(fabsf(oa[a][d][2]), fabsf(oa[a][d][3])));
        bool ok = sum[0][0] >= 0.5f && sum[0][0] < 0x1p64f && sum[1][0] >= 0.5f && sum[1][0] < 0x1p64f && big < INFINITY;
        if (!__all(ok)) {
#pragma unroll
          for (int a = 0; a < 2; ++a) {
            const int qs = 2 * rt + a, qq = 256 + 16 * qs + ql, klo = max(qq - 255, p > 0 ? 0 : 256);
            auto score = [&](int t) __attribute__((always_inline)) {
              f32x4 s = mfma16(t == 31 ? k1 : *(const u32x4*)(kb_ + t * 1024), qf[a], z4);
#pragma unroll
              for (int e = 0; e < 4; ++e) { const int kk = 16 * t + 4 * g + e; s[e] = (kk <= qq && kk >= klo) ? s[e] : -INFINITY; }
              return s;
            };
            float mx = -INFINITY;
#pragma unroll 1
            for (int t = qs; t <= qs + 16; ++t) { const f32x4 s = score(t); mx = fmaxf(fmaxf(mx, fmaxf(s[0], s[1])), fmaxf(s[2], s[3])); }
            mx = max_x32(max_x16(mx));
            oa[a][0] = z4; oa[a][1] = z4; sum[a] = z4;
#pragma unroll 1
            for (int G = qs >> 1; G <= (qs + 16) >> 1; ++G) {
              const f32x4 s0 = score(2 * G), s1 = score(2 * G + 1);
              f32x4 e0, e1;
#pragma unroll
              for (int e = 0; e < 4; ++e) { e0[e] = __builtin_amdgcn_exp2f(s0[e] - mx); e1[e] = __builtin_amdgcn_exp2f(s1[e] - mx); }
              const u32x4 pf = pack8(e0, e1);
              oa[a][0] = mfma16(*(const u32x4*)(vb_ + G * 2048), pf, oa[a][0]);
              oa[a][1] = mfma16(*(const u32x4*)(vb_ + G * 2048 + 1024), pf, oa[a][1]);
              sum[a] = mfma16(ones, pf, sum[a]);
            }
          }
        }
      }
#pragma unroll
      for (int a = 0; a < 2; ++a) {
        const float inv = __builtin_amdgcn_rcpf(sum[a][0]);
        oa[a][0] *= inv; oa[a][1] *= inv;
      }
      const unsigned oo = OFF_O + it * (4u << 20) + ((p * 8 + rt) * 64 + h * 2) * 1024;
      st16(pack8(oa[0][0], oa[0][1]), rs, l * 16, oo);
      st16(pack8(oa[1][0], oa[1][1]), rs, l * 16 + 1024, oo);
      DRAIN();
      BAR();
      if (tid == 0) st8(T, rs, OFL(h), F_O + OFB(it, p));
      if constexpr (it == 0) {
        DEF4(gv); DEF4(uv);
        if (ffn_on) {
          const unsigned ro = 65536 + w * 1024 + (4 * g + ((l & 15) >> 2)) * 32 + (l & 3) * 8;
#pragma unroll
          for (int i = 0; i < 4; ++i) {
            frag_t f;
            f.hf.a = trread(smem, ro + i * 16384); f.hf.b = trread(smem, ro + i * 16384 + 512);
            gv[i] = f.v;
            f.hf.a = trread(smem, ro + i * 16384 + 8192); f.hf.b = trread(smem, ro + i * 16384 + 8704);
            uv[i] = f.v;
          }
        }
        PINV4(gv); PINV4(uv);
        {
          ARGP(px, 0);
          const unsigned xo = ((rb * 4 + (w & 3)) * 32 + (w >> 2) * 16) * 2048 + ct * 128, xw = (l >> 3) * 2048 + (((l & 7) ^ g) << 4);
          dma16(RSRC(px, 2048 * 1024 * 2), xw, xo, lbase + w * 4096); dma16(RSRC(px, 2048 * 1024 * 2), xw ^ 64, xo + 16384, lbase + w * 4096 + 1024);
        }
      }
      WO_PIECE(3); WO_PIECE(2); WO_PIECE(1); WO_PIECE(0);
      if constexpr (it == 0) { WO_PIECE(11); WO_PIECE(10); WO_PIECE(9); WO_PIECE(8); WO_PIECE(7); WO_PIECE(6); WO_PIECE(5); WO_PIECE(4); }
    }
    IDS();
    ARGP(pwr, 4); ARGP(pwd, 7); ARGP(pout, 8);
    const int kh = w >> 2, rt2 = rb * 4 + (w & 3);
    const unsigned R = rt2 * 32 + kh * 16 + (l & 15);
    const unsigned to = (rt2 * 64 + ct * 4 + kh) * 1024 + l * 16;
    const unsigned rmo = ct * 64 + ((g & 1) ? 12 + 4 * g : 4 * g);
    f32x2 v[8], wv[8];
    float wsum;
    {
      u32x4 wa[2], wrp[2], res[2];
#pragma unroll
      for (int j = 0; j < 2; ++j) {
        wrp[j] = *(const GAS u32x4*)(pwr + rmo + 32 * j);
        wa[j] = *(const GAS u32x4*)(pwr + l * 8 + j * 512);
      }
      {
        f32x2 t[8];
        unpack8v(wa[0], t); unpack8v(wa[1], t + 4);
        const f32x2 s = ((t[0] + t[1]) + (t[2] + t[3])) + ((t[4] + t[5]) + (t[6] + t[7]));
        wsum = sum64(hadd(s));
        unpack8v(trade16(wrp[0]), wv); unpack8v(trade16(wrp[1]), wv + 4);
        asm volatile("" : "+v"(wsum), "+v"(wv[0]), "+v"(wv[1]), "+v"(wv[2]), "+v"(wv[3]), "+v"(wv[4]), "+v"(wv[5]), "+v"(wv[6]), "+v"(wv[7]));
      }
      unsigned bt[4];
#pragma unroll
      for (int n = 0; n < 4; ++n) {
        bt[n] = lbase + 32768 + 65536 + kh * 4096 + (4 * g + ((l & 15) >> 2)) * 128 + (l & 3) * 8 + ((n ^ (2 * (g & 1) + ((l & 15) >> 3))) << 5);
        asm volatile("" : "+v"(bt[n]));
      }
      if (w == 0) poll_flags(rs, OFL(l & 31), F_O + OFB(it, rb >> 1), T);
      else if (it == 0 && w < 3) poll_flags2(rs, F_XN + (((w - 1) * 4 + g) * RX + bi % RX) * 256 + (l & 15) * 16, T);
      else if (it == 0 && w == 3) poll_flags(rs, ((l >> 3) * 32 + h) * 128 + (l & 7) * 8, F_KV, T);
      asm volatile("s_barrier" ::: "memory");
      const unsigned abase = OFF_O + it * (4u << 20) + (rt2 * 64 + 32 * kh) * 1024;
      const unsigned av0 = l * 16 + kh * 1024, av1 = l * 16 + (kh ^ 1) * 1024;
      u32x4 af[WB][2];
#define O_LOAD(s_) { ld16h<(wo_k(it, s_) & 1) * 2048>(af[(s_) % WB][0], rs, av0, abase + (wo_k(it, s_) >> 1) * 4096); ld16h<(wo_k(it, s_) & 1) * 2048>(af[(s_) % WB][1], rs, av1, abase + (wo_k(it, s_) >> 1) * 4096); }
      if (w >= 4) __builtin_amdgcn_s_setprio(1);
      static_for<WR>([&](auto c_) __attribute__((always_inline)) { O_LOAD(decltype(c_)::value); });
      if constexpr (it == 1) { ld16h<0>(res[0], rs, g * 16, OFF_XNEW + ct * 128); ld16h<64>(res[1], rs, g * 16, OFF_XNEW + ct * 128); }
      SB();
      f32x4 ac[2][4];
#pragma unroll
      for (int a = 0; a < 2; ++a)
#pragma unroll
        for (int n = 0; n < 4; ++n) ac[a][n] = f32x4{0.f, 0.f, 0.f, 0.f};
      u32x4 bfr[2][4];
#define WO_RD(buf, s, n)                                                                             \
  {                                                                                                  \
    const unsigned o_ = bt[n] + (wo_k(it, s) & 7) * 8192;                                             \
    frag_t f_;                                                                                       \
    f_.hf.a = trabs(o_); f_.hf.b = trabs(o_ + 2048);                                                 \
    bfr[buf][n] = f_.v;                                                                              \
  }
#pragma unroll
      for (int n = 0; n < 4; ++n) WO_RD(0, 0, n);
      static_for<16>([&](auto sc_) __attribute__((always_inline)) {
        constexpr int s = decltype(sc_)::value, cur = s & 1;
        pin_ac(ac);
        if constexpr (s == 7) __builtin_amdgcn_s_setprio(0);
        if constexpr (s == 1) lds_count<0>(lbase + L_QC);
        if constexpr (s == (it == 0 ? 3 : 11)) lds_look(smem + L_QC, 64);
        if constexpr (s < 15 && ((wo_k(it, s) ^ wo_k(it, s + 1)) & 8)) {
#pragma unroll
          for (int n = 0; n < 4; ++n) { bt[n] += (wo_k(it, s) & 8) ? -65536 : 65536; pin1(bt[n]); }
        }
        if constexpr (s < WB - WR) O_LOAD(WR + 2 * s);
        if constexpr (it == 0 && s == 8) lds_count<8>(lbase + L_QC);
        if constexpr (it == 0 && s == WDS) {
          DRAIN();
          if (ffn_on) { lds_look(smem + L_QC + 8, 16); WD_DMA_OF(w); }
        }
        if constexpr (it == 1 || s < WDS) wait_vm<wo_at(it, s) - wo_pos(it, s)>();
        pin2(af[s % WB][0], af[s % WB][1]);
        if constexpr (it == 1 && s == 9) pin2(res[0], res[1]);
        SB();
#pragma unroll
        for (int n = 0; n < 4; ++n) {
          ac[0][n] = mfma16(bfr[cur][n], af[s % WB][0], ac[0][n]);
          ac[1][n] = mfma16(bfr[cur][n], af[s % WB][1], ac[1][n]);
          if (s + 1 < 16) WO_RD(cur ^ 1, s + 1, n);
          if (it == 0 && s == 8) {
            const u32x2 t = *(const LDS_AS u32x2*)(smem + w * 4096 + (l & 15) * 128 + (((2 * n + (g >> 1)) ^ ((l & 15) >> 1)) << 4) + (g & 1) * 8);
            res[n >> 1][2 * (n & 1)] = t[0]; res[n >> 1][2 * (n & 1) + 1] = t[1];
          }
          if (s == 9) {
            if (it == 1 && n == 0) { res[0] = R == sel0 ? res[0] : x2p[0]; res[1] = R == sel0 ? res[1] : x2p[1]; }

            v[2 * n] = f32x2{lo16(res[n >> 1][2 * (n & 1)]), hi16(res[n >> 1][2 * (n & 1)])};
            v[2 * n + 1] = f32x2{lo16(res[n >> 1][2 * (n & 1) + 1]), hi16(res[n >> 1][2 * (n & 1) + 1])};
            pin2f(v[2 * n], v[2 * n + 1]);
          }
          SB();
        }
        if constexpr (s < WB - WR) { O_LOAD(WR + 2 * s + 1); }
        else if constexpr (s + WB < 16) { O_LOAD(s + WB); }
        SB();
      });
#undef WO_RD
#undef O_LOAD
#define XCH_AT(v) ((v) == 7 ? 155648 : (v) * 4096)
#pragma unroll
      for (int n = 0; n < 4; ++n) *(f32x4*)(smem + XCH_AT(w) + n * 1024 + l * 16) = ac[1][n];
#pragma unroll
      for (int n = 0; n < 4; ++n) { v[2 * n] += f32x2{ac[0][n][0], ac[0][n][1]}; v[2 * n + 1] += f32x2{ac[0][n][2], ac[0][n][3]}; }
      BAR();
#pragma unroll
      for (int n = 0; n < 4; ++n) {
        const f32x4 o = *(const f32x4*)(smem + XCH_AT(w ^ 4) + n * 1024 + l * 16);
        v[2 * n] += f32x2{o[0], o[1]}; v[2 * n + 1] += f32x2{o[2], o[3]};
      }
    }
    {
      if (it == 0) { st16(pack8v(v), rs, to, OFF_X1); st16(pack8v(v + 4), rs, to + 2048, OFF_X1); }
      SB();
      const f32x2 s = ((v[0] + v[1]) + (v[2] + v[3])) + ((v[4] + v[5]) + (v[6] + v[7]));
      f32x2 dp = v[0] * wv[0], q = v[0] * v[0];
#pragma unroll
      for (int j = 1; j < 8; ++j) { dp = __builtin_elementwise_fma(v[j], wv[j], dp); q = __builtin_elementwise_fma(v[j], v[j], q); }
      float mt = hadd(s), qq = hadd(q), dot = hadd(dp);
      add_g3(mt, qq, dot);
      const unsigned so = OFF_STAT + it * 524288 + rb * 32768, lrow = R & 127;
      if (g == 0) st16(unpoison4(__builtin_bit_cast(u32x4, f32x4{mt, qq, dot, 0.f})), rs, ct * 2048 + lrow * 16, so);
      SB();
      if (it == 0) wait_vm<1>();
      if constexpr (it == 0) {
        volatile LDS_AS u32x2* in = (volatile LDS_AS u32x2*)(smem + L_XC);
        if (l == 0) in[w] = T;
        const u32x2 t = in[l & 7];
        if (__all(t[0] == T[0] && t[1] == T[1]) && l < RX) st8(T, rs, (l * 32 + (rb & 1) * 16 + ct) * 8, F_X1 + (rb >> 1) * RX * 32 * 8);
      }
      if (it == 1) { if (ffn_on) { WD_DMA_OF(w); } }
      if (it == 0) {
#pragma unroll
        for (int j = 0; j < 9; ++j) *(u32x4*)(smem + L_RING + (j / 3) * STAGE + (w * 3 + j % 3) * 1024 + l * 16) = keep[j];
      }
      __builtin_amdgcn_s_sleep(it == 0 ? SDI0 : SDIR);
      char* const skey = smem + (it == 0 ? L_SKEY : L_XC);
      u32x4 sp[4];
      ld16h<0>(sp[0], rs, g * 8192 + lrow * 16, so); ld16h<2048>(sp[1], rs, g * 8192 + lrow * 16, so);
      ld16h<0>(sp[2], rs, g * 8192 + lrow * 16, so + 4096); ld16h<2048>(sp[3], rs, g * 8192 + lrow * 16, so + 4096);
      u32x2 xf;
      asm volatile("" : "=v"(xf));
      if (it == 0 && w == 0) ld8h(xf, rs, (l & 31) * 8, F_X1 + (p * RX + (xcd >> 1) % RX) * 32 * 8);
      DRAIN();
      asm volatile("" : "+v"(xf)::"memory");
      PINV4(sp);
      const RowFin r = fin_key(rs, T, g * 8192 + lrow * 16, so, skey, l, w, R, wsum, sp);
      if constexpr (it == 0) {
        if (w == 0) {
          const unsigned need = 0x30003u << (((PROT() + 5) & 7) * 2);
          unsigned m = (unsigned)__builtin_amdgcn_ballot_w64(xf[0] == T[0] && xf[1] == T[1]);
          for (unsigned spins = 0; (m & need) != need;) {
            if (spin_fail(spins, rs, T)) { m = ~0u; break; }
            const u32x2 f = ld8(rs, F_X1 + ((p * RX + (xcd >> 1) % RX) * 32 + (l & 31)) * 8, 0);
            m = (unsigned)__builtin_amdgcn_ballot_w64(f[0] == T[0] && f[1] == T[1]);
          }
          if (l == 0) *(LDS_AS u32x4*)(skey + 32) = u32x4{m, 0u, POISON, 0u};
        }
        if (tid == 192) *(LDS_AS u32x4*)(smem + L_QC) = u32x4{0u, 0u, 0u, 0u};
      }
      if (it == 1 && tid == 0) *(LDS_AS unsigned*)(smem + L_XC + 32) = POISON;
      BAR();
      if constexpr (it == 0) {
        PRE_LOAD(1, PROT() + 5, 0, 4);
        xm0 = __builtin_amdgcn_readfirstlane(*(const LDS_AS unsigned*)(skey + 32));
      }
      fin_cand(rs, skey, it, g, rb, ct, r, v);
      fin_x2(r, v);
      if constexpr (it == 0) {
        x2p[0] = pack8v(v); x2p[1] = pack8v(v + 4);
        asm volatile("" : "+v"(x2p[0]), "+v"(x2p[1]));
        if ((w & 3) * 2 + (w >> 2) == ct >> 1) {
          const float f = 1.0f + r.rd, rd2 = ln_rd(r.var * f * f), mb2 = rbf1(__builtin_fmaf(r.mean - r.mb, r.rd, r.mean));
          if (g < 2 && ((l >> 3) & 1) == (ct & 1))
            st4(min(__builtin_bit_cast(unsigned, g == 0 ? f * rd2 : -__builtin_fmaf(r.mb, r.rd, mb2) * __builtin_amdgcn_rcpf(f)), POISON - 1), rs, OFF_AB + 16384 + R * 4 + g * 8192, 0);
        }
      }
    }
    if (it == 1) {
      const u32x4 op0 = trade16(pack8v(v)), op1 = trade16(pack8v(v + 4));
#define OUT_STORE()                                                                                 \
  if (R != key_row(key)) {                                                                          \
    __builtin_amdgcn_raw_buffer_store_b128(op0, rout, (R * 1024 + rmo) * 2, 0, SC1);                  \
    __builtin_amdgcn_raw_buffer_store_b128(op1, rout, (R * 1024 + rmo + 32) * 2, 0, SC1);             \
  }
      unsigned key;
      {
        LDS_AS unsigned* kw = (LDS_AS unsigned*)(smem + L_XC + 32);
        if (w == 0) {
          __builtin_amdgcn_s_sleep(GSL);
          key = key_wait(rs, T, l, 1, bid & 15, key_load(rs, l, 1, bid & 15));
          *kw = min(key, POISON - 1);
        } else {
          unsigned k_;
          while ((k_ = __hip_atomic_load(kw, __ATOMIC_RELAXED, __HIP_MEMORY_SCOPE_WORKGROUP)) == POISON) __builtin_amdgcn_s_sleep(1);
          key = __builtin_amdgcn_readfirstlane(k_);
        }
      }
      if (ffn_on) {
        f32x4 ga, ua;
        u32x4 xv[4];
        s16x4 dfr[8];
        ld4x16(xv, rs, g * 16, ffn_xoff(key, 1, w));
        ffn_d_read(smem, l, w, dfr);
        ffn_gu(gv, uv, xv, ga, ua);
        ffn_gu_check(rs, T, ffn_xoff(key, 1, w), l, gv, uv, xv, ga, ua);
        ffn_gu_store(smem, l, w, ga, ua);
        BAR();
        f32x4 ya = {0.f, 0.f, 0.f, 0.f}, yb = ya;
        u32x2 hh = ffn_h(ffn_t_read(smem, l), l);
        ffn_d_mma(dfr, hh, ya, yb);
        if ((l & 15) < 8) ffn_d_store(rs, l, 4 * w, 1, slice, ya + yb);
        SB();
        __builtin_amdgcn_s_sleep(OSL);
        OUT_STORE();
      } else {
        OUT_STORE();
        const unsigned G = bi;
        if (G == 0 && tid == 0) st8(T, rs, F_EP, 0);
        u32x4 pv[4];
        u32x2 cxn, cx1;
        __builtin_amdgcn_s_sleep(RSL);
        red_load(rs, tid, 1, G, key, pv, cxn, cx1);
        red_sum(rs, smem, T, tid, w, 1, G, key, pv, cxn, cx1);
        BAR();
        if (w == 0) {
          const u32x2 ov = red_final<8>(smem, l, key, cxn, cx1);
          if (l < 8) __builtin_amdgcn_raw_buffer_store_b64(ov, rout, (key_row(key) * 1024 + G * 32 + l * 4) * 2, 0, 0);
        }
      }
#undef OUT_STORE
    }
  };
  layer(std::integral_constant<int, 0>{});
  layer(std::integral_constant<int, 1>{});
}

extern "C" void kernel_launch(void* const* d_in, const int* in_sizes, int n_in, void* d_out, int out_size,
                              void* d_ws, size_t ws_size, hipStream_t stream) {
  (void)hipFuncSetAttribute((const void*)mega_kernel, hipFuncAttributeMaxDynamicSharedMemorySize, LDS_BYTES);
  mega_kernel<<<256, 512, LDS_BYTES, stream>>>((const bf16*)d_in[0], (char*)d_ws, (const bf16*)d_in[1], (const bf16*)d_in[2], (const bf16*)d_in[4],
                                               (const bf16*)d_in[5], (const bf16*)d_in[6], (const bf16*)d_in[7], (bf16*)d_out);
}
```

```cpp
#include <hip/hip_runtime.h>
#include <stdint.h>
#include <utility>

typedef __bf16 bf16;
typedef __bf16 bf16x8 __attribute__((ext_vector_type(8)));
typedef __bf16 bf16x2 __attribute__((ext_vector_type(2)));
typedef float f32x4 __attribute__((ext_vector_type(4)));
typedef float f32x2 __attribute__((ext_vector_type(2)));
typedef short s16x4 __attribute__((ext_vector_type(4)));
typedef unsigned int u32x4 __attribute__((ext_vector_type(4)));
typedef unsigned int u32x2 __attribute__((ext_vector_type(2)));
#define LDS_AS __attribute__((address_space(3)))
#define GAS __attribute__((address_space(1)))
typedef GAS unsigned gu32;
typedef GAS unsigned long long gu64;
#define RLX_AGENT __ATOMIC_RELAXED, __HIP_MEMORY_SCOPE_AGENT
typedef __amdgpu_buffer_rsrc_t rsrc_t;

constexpr int F_ = 3584;
constexpr int SC1 = 16;

constexpr int RX = 4;
constexpr unsigned F_EP = 0, F_TMO = 128;
constexpr unsigned F_XN = 4096;
constexpr unsigned F_O = 8u << 20;
constexpr unsigned F_X1 = F_O + 131072;
constexpr unsigned OFF_KEY = F_O + 393216;
constexpr unsigned F_KV = F_O + 524288;
#define KVF(it, i) ((i) * 128 + (it) * 64)
#define OFL(h) (((h) >> 3) * 128 + ((h) & 7) * 8)
#define OFB(it, p) ((p) * 512 + (it) * 64)
constexpr unsigned OFF_AB = 147456;
constexpr unsigned OFF_XNEW = 262144;
constexpr unsigned OFF_CS = 294912;
constexpr unsigned OFF_FX = 393216;
constexpr unsigned OFF_CA = 524288;
constexpr unsigned OFF_CB = 655360;
constexpr unsigned OFF_STAT = 1u << 20;
constexpr unsigned OFF_PART = 3u << 20;
constexpr unsigned OFF_X1 = 12u << 20;
constexpr unsigned OFF_O = 16u << 20;
constexpr unsigned OFF_KV = 32u << 20;
constexpr unsigned OFF_WI = 52u << 20;
constexpr unsigned WS_BYTES = 58u << 20;
constexpr unsigned POISON = 0xFFFFFFFFu;

constexpr int L_CSL = 31872, L_FXL = 32256, L_K = 0, L_V = 32768, L_RING = 65536, L_DN = 32768, L_TOUCH0 = 149504, L_RDY = 31744, L_QC = 31808, LDS_BYTES = 163840;
constexpr int STAGE = 24576;
#define SLOT(t) ((t) & 3)

template <typename F, int... I> __device__ __forceinline__ void static_for_impl(F&& f, std::integer_sequence<int, I...>) {
  (f(std::integral_constant<int, I>{}), ...);
}
template <int N, typename F> __device__ __forceinline__ void static_for(F&& f) { static_for_impl(f, std::make_integer_sequence<int, N>{}); }
__device__ __forceinline__ unsigned pack2(float a, float b) {
  f32x2 v = {a, b};
  return __builtin_bit_cast(unsigned, __builtin_convertvector(v, bf16x2));
}
__device__ __forceinline__ float asf(unsigned u) { return __builtin_bit_cast(float, u); }
__device__ __forceinline__ float lo16(unsigned u) { return __builtin_bit_cast(float, u << 16); }
__device__ __forceinline__ float hi16(unsigned u) { return __builtin_bit_cast(float, u & 0xffff0000u); }
__device__ __forceinline__ u32x4 pack8(const f32x4 a, const f32x4 b) {
  u32x4 o;
  o[0] = pack2(a[0], a[1]); o[1] = pack2(a[2], a[3]); o[2] = pack2(b[0], b[1]); o[3] = pack2(b[2], b[3]);
  return o;
}
__device__ __forceinline__ f32x4 mfma16(u32x4 a, u32x4 b, f32x4 c) {
  return __builtin_amdgcn_mfma_f32_16x16x32_bf16(__builtin_bit_cast(bf16x8, a), __builtin_bit_cast(bf16x8, b), c, 0, 0, 0);
}
__device__ __forceinline__ f32x4 mfma16k16(s16x4 a, s16x4 b, f32x4 c) { return __builtin_amdgcn_mfma_f32_16x16x16bf16_1k(a, b, c, 0, 0, 0); }
template <int CTRL> __device__ __forceinline__ float dppf(float v) {
  return __builtin_bit_cast(float, __builtin_amdgcn_update_dpp(0, __builtin_bit_cast(int, v), CTRL, 0xf, 0xf, true));
}
template <int CTRL> __device__ __forceinline__ unsigned dppu(unsigned v) {
  return (unsigned)__builtin_amdgcn_update_dpp(0, (int)v, CTRL, 0xf, 0xf, true);
}
__device__ __forceinline__ float add_x16(float v) {
  auto r = __builtin_amdgcn_permlane16_swap(__builtin_bit_cast(unsigned, v), __builtin_bit_cast(unsigned, v), false, false);
  return __builtin_bit_cast(float, (unsigned)r[0]) + __builtin_bit_cast(float, (unsigned)r[1]);
}
__device__ __forceinline__ float add_x32(float v) {
  auto r = __builtin_amdgcn_permlane32_swap(__builtin_bit_cast(unsigned, v), __builtin_bit_cast(unsigned, v), false, false);
  return __builtin_bit_cast(float, (unsigned)r[0]) + __builtin_bit_cast(float, (unsigned)r[1]);
}
__device__ __forceinline__ float max_x16(float v) {
  auto r = __builtin_amdgcn_permlane16_swap(__builtin_bit_cast(unsigned, v), __builtin_bit_cast(unsigned, v), false, false);
  return fmaxf(__builtin_bit_cast(float, (unsigned)r[0]), __builtin_bit_cast(float, (unsigned)r[1]));
}
__device__ __forceinline__ float max_x32(float v) {
  auto r = __builtin_amdgcn_permlane32_swap(__builtin_bit_cast(unsigned, v), __builtin_bit_cast(unsigned, v), false, false);
  return fmaxf(__builtin_bit_cast(float, (unsigned)r[0]), __builtin_bit_cast(float, (unsigned)r[1]));
}
__device__ __forceinline__ unsigned umax_x16(unsigned v) {
  auto r = __builtin_amdgcn_permlane16_swap(v, v, false, false);
  return max((unsigned)r[0], (unsigned)r[1]);
}
__device__ __forceinline__ unsigned umax_x32(unsigned v) {
  auto r = __builtin_amdgcn_permlane32_swap(v, v, false, false);
  return max((unsigned)r[0], (unsigned)r[1]);
}
__device__ __forceinline__ float sum_hi8(float v) {
  v += dppf<0x128>(v);
  v = add_x16(v);
  return add_x32(v);
}
__device__ __forceinline__ float sum32(float v) {
  v += dppf<0xB1>(v);
  v += dppf<0x4E>(v);
  v += dppf<0x124>(v);
  v += dppf<0x128>(v);
  return add_x16(v);
}
__device__ __forceinline__ float sum64(float v) {
  v += dppf<0xB1>(v);
  v += dppf<0x4E>(v);
  v += dppf<0x124>(v);
  v += dppf<0x128>(v);
  v = add_x16(v);
  return add_x32(v);
}

#define BAR() asm volatile("s_waitcnt lgkmcnt(0)\n\ts_barrier" ::: "memory")
#define DRAIN() asm volatile("s_waitcnt vmcnt(0)" ::: "memory")
#define SB() __builtin_amdgcn_sched_barrier(0)
template <int N> __device__ __forceinline__ void wait_vm() { asm volatile("s_waitcnt vmcnt(%0)" ::"n"(N) : "memory"); }

__device__ __forceinline__ void dma16(rsrc_t rs, unsigned voff, unsigned soff, unsigned lds) {
  asm volatile("s_mov_b32 m0, %0\n\ts_nop 0\n\tbuffer_load_dwordx4 %1, %2, %3 offen lds" ::"s"(lds), "v"(voff), "s"(rs), "s"(soff) : "memory");
}
__device__ __forceinline__ void dma16d(u32x4 d, unsigned voff, unsigned soff, unsigned lds) {
  asm volatile("s_mov_b32 m0, %0\n\ts_nop 0\n\tbuffer_load_dwordx4 %1, %2, %3 offen lds" ::"s"(lds), "v"(voff), "s"(d), "s"(soff) : "memory");
}
template <int LI, int SI> __device__ __forceinline__ void dma16i(rsrc_t rs, unsigned voff, unsigned sb, unsigned lb) {
  unsigned t;
  asm volatile("s_add_u32 m0, %2, %5\n\ts_add_u32 %0, %1, %6\n\tbuffer_load_dwordx4 %3, %4, %0 offen lds" : "=&s"(t) : "s"(sb), "s"(lb), "v"(voff), "s"(rs), "n"(LI), "n"(SI) : "memory", "scc");
}
template <int LI> __device__ __forceinline__ void dma16r(rsrc_t rs, unsigned voff, unsigned sb, unsigned so, unsigned lb) {
  unsigned t;
  asm volatile("s_add_u32 m0, %2, %5\n\ts_add_u32 %0, %1, %6\n\tbuffer_load_dwordx4 %3, %4, %0 offen lds" : "=&s"(t) : "s"(sb), "s"(lb), "v"(voff), "s"(rs), "n"(LI), "s"(so) : "memory", "scc");
}
template <int LI, int OFF> __device__ __forceinline__ void dma16t(rsrc_t rs, unsigned voff, unsigned sb, unsigned so, unsigned lb) {
  unsigned t;
  asm volatile("s_add_u32 m0, %2, %5\n\ts_add_u32 %0, %1, %6\n\tbuffer_load_dwordx4 %3, %4, %0 offen offset:%7 sc1 lds" : "=&s"(t) : "s"(sb), "s"(lb), "v"(voff), "s"(rs), "n"(LI), "s"(so), "n"(OFF) : "memory", "scc");
}
template <int OFF> __device__ __forceinline__ void dma16s(rsrc_t rs, unsigned voff, unsigned soff, unsigned lds) {
  asm volatile("s_mov_b32 m0, %0\n\ts_nop 0\n\tbuffer_load_dwordx4 %1, %2, %3 offen offset:%4 sc1 lds" ::"s"(lds), "v"(voff), "s"(rs), "s"(soff), "n"(OFF) : "memory");
}
__device__ __forceinline__ void touch(unsigned& d, const GAS bf16* ptr) { asm volatile("global_load_dword %0, %1, off" : "+v"(d) : "v"(ptr) : "memory"); }
__device__ __forceinline__ void ldrows(u32x4 (&d)[4], rsrc_t rx, unsigned voff, unsigned soff) {
  asm volatile("buffer_load_dwordx4 %0, %4, %5, %6 offen\n\tbuffer_load_dwordx4 %1, %4, %5, %6 offen offset:512\n\t"
               "buffer_load_dwordx4 %2, %4, %5, %6 offen offset:1024\n\tbuffer_load_dwordx4 %3, %4, %5, %6 offen offset:1536"
               : "=&v"(d[0]), "=&v"(d[1]), "=&v"(d[2]), "=&v"(d[3]) : "v"(voff), "s"(rx), "s"(soff) : "memory");
}
__device__ __forceinline__ void touchb(unsigned& d, rsrc_t rs, unsigned vo) { asm volatile("buffer_load_dword %0, %1, %2, 0 offen sc1" : "+v"(d) : "v"(vo), "s"(rs) : "memory"); }
__device__ __forceinline__ float rbf1(float x) { return __builtin_bit_cast(float, pack2(0.f, x)); }
__device__ __forceinline__ u32x4 ld16(rsrc_t rs, unsigned voff, unsigned soff) { return __builtin_amdgcn_raw_buffer_load_b128(rs, voff, soff, SC1); }
template <int OFF> __device__ __forceinline__ void ld16h(u32x4& d, rsrc_t rs, unsigned voff, unsigned soff) {
  asm volatile("buffer_load_dwordx4 %0, %1, %2, %3 offen offset:%4 sc1" : "=v"(d) : "v"(voff), "s"(rs), "s"(soff), "n"(OFF) : "memory");
}
template <int I, int OFF> __device__ __forceinline__ void ld16w(u32x4& d, rsrc_t rs, unsigned voff, unsigned soff) {
  if constexpr (I == 0) asm volatile("buffer_load_dwordx4 %0, %1, %2, %3 offen offset:%4 sc1" : "={v[128:131]}"(d) : "v"(voff), "s"(rs), "s"(soff), "n"(OFF) : "memory");
  else if constexpr (I == 1) asm volatile("buffer_load_dwordx4 %0, %1, %2, %3 offen offset:%4 sc1" : "={v[132:135]}"(d) : "v"(voff), "s"(rs), "s"(soff), "n"(OFF) : "memory");
  else if constexpr (I == 2) asm volatile("buffer_load_dwordx4 %0, %1, %2, %3 offen offset:%4 sc1" : "={v[136:139]}"(d) : "v"(voff), "s"(rs), "s"(soff), "n"(OFF) : "memory");
  else if constexpr (I == 3) asm volatile("buffer_load_dwordx4 %0, %1, %2, %3 offen offset:%4 sc1" : "={v[140:143]}"(d) : "v"(voff), "s"(rs), "s"(soff), "n"(OFF) : "memory");
  else if constexpr (I == 4) asm volatile("buffer_load_dwordx4 %0, %1, %2, %3 offen offset:%4 sc1" : "={v[144:147]}"(d) : "v"(voff), "s"(rs), "s"(soff), "n"(OFF) : "memory");
  else if constexpr (I == 5) asm volatile("buffer_load_dwordx4 %0, %1, %2, %3 offen offset:%4 sc1" : "={v[148:151]}"(d) : "v"(voff), "s"(rs), "s"(soff), "n"(OFF) : "memory");
  else if constexpr (I == 6) asm volatile("buffer_load_dwordx4 %0, %1, %2, %3 offen offset:%4 sc1" : "={v[152:155]}"(d) : "v"(voff), "s"(rs), "s"(soff), "n"(OFF) : "memory");
  else if constexpr (I == 7) asm volatile("buffer_load_dwordx4 %0, %1, %2, %3 offen offset:%4 sc1" : "={v[156:159]}"(d) : "v"(voff), "s"(rs), "s"(soff), "n"(OFF) : "memory");
  else if constexpr (I == 8) asm volatile("buffer_load_dwordx4 %0, %1, %2, %3 offen offset:%4 sc1" : "={v[160:163]}"(d) : "v"(voff), "s"(rs), "s"(soff), "n"(OFF) : "memory");
  else if constexpr (I == 9) asm volatile("buffer_load_dwordx4 %0, %1, %2, %3 offen offset:%4 sc1" : "={v[164:167]}"(d) : "v"(voff), "s"(rs), "s"(soff), "n"(OFF) : "memory");
  else if constexpr (I == 10) asm volatile("buffer_load_dwordx4 %0, %1, %2, %3 offen offset:%4 sc1" : "={v[168:171]}"(d) : "v"(voff), "s"(rs), "s"(soff), "n"(OFF) : "memory");
  else if constexpr (I == 11) asm volatile("buffer_load_dwordx4 %0, %1, %2, %3 offen offset:%4 sc1" : "={v[172:175]}"(d) : "v"(voff), "s"(rs), "s"(soff), "n"(OFF) : "memory");
}
template <int K> __device__ __forceinline__ void pin_w(u32x4& a, u32x4& b) {
  if constexpr (K == 0) asm volatile("" : "+{v[128:131]}"(a), "+{v[132:135]}"(b)::"memory");
  else if constexpr (K == 1) asm volatile("" : "+{v[136:139]}"(a), "+{v[140:143]}"(b)::"memory");
  else if constexpr (K == 2) asm volatile("" : "+{v[144:147]}"(a), "+{v[148:151]}"(b)::"memory");
  else if constexpr (K == 3) asm volatile("" : "+{v[152:155]}"(a), "+{v[156:159]}"(b)::"memory");
  else if constexpr (K == 4) asm volatile("" : "+{v[160:163]}"(a), "+{v[164:167]}"(b)::"memory");
  else if constexpr (K == 5) asm volatile("" : "+{v[168:171]}"(a), "+{v[172:175]}"(b)::"memory");
}
__device__ __forceinline__ void ld8h(u32x2& d, rsrc_t rs, unsigned voff, unsigned soff) {
  asm volatile("buffer_load_dwordx2 %0, %1, %2, %3 offen sc1" : "=v"(d) : "v"(voff), "s"(rs), "s"(soff) : "memory");
}
__device__ __forceinline__ void ld16u(u32x4& d, rsrc_t rs, unsigned voff, unsigned soff) {
  asm volatile("buffer_load_dwordx4 %0, %1, %2, %3 offen sc1" : "+v"(d) : "v"(voff), "s"(rs), "s"(soff) : "memory");
}
template <int OFF> __device__ __forceinline__ void ld4h(unsigned& d, rsrc_t rs, unsigned voff, unsigned soff) {
  asm volatile("buffer_load_dword %0, %1, %2, %3 offen offset:%4 sc1" : "=v"(d) : "v"(voff), "s"(rs), "s"(soff), "n"(OFF) : "memory");
}
__device__ __forceinline__ void ld4x16(u32x4 (&d)[4], rsrc_t rs, unsigned voff, unsigned soff) {
  asm volatile("buffer_load_dwordx4 %0, %4, %5, %6 offen sc1\n\tbuffer_load_dwordx4 %1, %4, %5, %6 offen offset:64 sc1\n\t"
               "buffer_load_dwordx4 %2, %4, %5, %6 offen offset:128 sc1\n\tbuffer_load_dwordx4 %3, %4, %5, %6 offen offset:192 sc1\n\ts_waitcnt vmcnt(0)"
               : "=&v"(d[0]), "=&v"(d[1]), "=&v"(d[2]), "=&v"(d[3]) : "v"(voff), "s"(rs), "s"(soff) : "memory");
}
__device__ __forceinline__ u32x2 ld8(rsrc_t rs, unsigned voff, unsigned soff) { return __builtin_amdgcn_raw_buffer_load_b64(rs, voff, soff, SC1); }
__device__ __forceinline__ void st16(u32x4 v, rsrc_t rs, unsigned voff, unsigned soff) { __builtin_amdgcn_raw_buffer_store_b128(v, rs, voff + soff, 0, SC1); }
__device__ __forceinline__ void st8(u32x2 v, rsrc_t rs, unsigned voff, unsigned soff) { __builtin_amdgcn_raw_buffer_store_b64(v, rs, voff, soff, SC1); }

__device__ __forceinline__ unsigned ld4(rsrc_t rs, unsigned voff, unsigned soff) { return __builtin_amdgcn_raw_buffer_load_b32(rs, voff, soff, SC1); }
__device__ __forceinline__ void st4(unsigned v, rsrc_t rs, unsigned voff, unsigned soff) { __builtin_amdgcn_raw_buffer_store_b32(v, rs, voff, soff, SC1); }

constexpr unsigned kSpinCap = 1u << 16;
__device__ __forceinline__ bool spin_fail(unsigned& spins, rsrc_t rs, u32x2 T) {
  asm volatile("" ::: "memory");
  ++spins;
  if (spins > kSpinCap) { st8(T, rs, 0, F_TMO); return true; }
  if ((spins & 255u) == 0u) {
    const u32x2 v = ld8(rs, 0, F_TMO);
    if (v[0] == T[0] && v[1] == T[1]) return true;
  }
  __builtin_amdgcn_s_sleep(3);
  return false;
}
__device__ __forceinline__ void poll_flags2(rsrc_t rs, unsigned voff, u32x2 T) {
  for (unsigned spins = 0;;) {
    const u32x4 v = ld16(rs, voff, 0);
    if (__all(v[0] == T[0] && v[1] == T[1] && v[2] == T[0] && v[3] == T[1])) break;
    if (spin_fail(spins, rs, T)) break;
  }
  asm volatile("" ::: "memory");
  __builtin_amdgcn_fence(__ATOMIC_ACQUIRE, "wavefront");
}
__device__ __forceinline__ void poll_flags(rsrc_t rs, unsigned voff, unsigned soff, u32x2 T) {
  for (unsigned spins = 0;;) {
    const u32x2 v = ld8(rs, voff, soff);
    if (__all(v[0] == T[0] && v[1] == T[1])) break;
    if (spin_fail(spins, rs, T)) break;
  }
  asm volatile("" ::: "memory");
  __builtin_amdgcn_fence(__ATOMIC_ACQUIRE, "wavefront");
}

__device__ __forceinline__ void unpack8v(const u32x4 u, f32x2* v) {
#pragma unroll
  for (int j = 0; j < 4; ++j) v[j] = f32x2{lo16(u[j]), hi16(u[j])};
}
__device__ __forceinline__ u32x4 pack8v(const f32x2* v) {
  u32x4 o;
#pragma unroll
  for (int j = 0; j < 4; ++j) o[j] = pack2(v[j][0], v[j][1]);
  return o;
}
__device__ __forceinline__ float ln_rd(float var) { return __builtin_amdgcn_rcpf(rbf1(__builtin_amdgcn_sqrtf(rbf1(rbf1(var) + 1e-5f)))); }
__device__ __forceinline__ float hadd(f32x2 v) {
  float a = v[0];
  asm volatile("" : "+v"(a));
  return a + v[1];
}
__device__ __forceinline__ float add_g(float v) { return add_x32(add_x16(v)); }
__device__ __forceinline__ void add_g3(float& x, float& y, float& z) {
  auto a = __builtin_amdgcn_permlane16_swap(__builtin_bit_cast(unsigned, x), __builtin_bit_cast(unsigned, y), false, false);
  auto b = __builtin_amdgcn_permlane16_swap(__builtin_bit_cast(unsigned, z), __builtin_bit_cast(unsigned, z), false, false);
  const float r = asf(a[0]) + asf(a[1]), t = asf(b[0]) + asf(b[1]);
  auto c = __builtin_amdgcn_permlane32_swap(__builtin_bit_cast(unsigned, r), __builtin_bit_cast(unsigned, r), false, false);
  auto d = __builtin_amdgcn_permlane32_swap(__builtin_bit_cast(unsigned, t), __builtin_bit_cast(unsigned, t), false, false);
  const float u = asf(c[0]) + asf(c[1]);
  z = asf(d[0]) + asf(d[1]);
  auto e = __builtin_amdgcn_permlane16_swap(__builtin_bit_cast(unsigned, u), __builtin_bit_cast(unsigned, u), false, false);
  x = asf(e[0]); y = asf(e[1]);
}
__device__ __forceinline__ u32x4 trade16(u32x4 v) {
  auto a = __builtin_amdgcn_permlane16_swap(v[0], v[2], false, false);
  auto b = __builtin_amdgcn_permlane16_swap(v[1], v[3], false, false);
  return u32x4{(unsigned)a[0], (unsigned)b[0], (unsigned)a[1], (unsigned)b[1]};
}
__device__ __forceinline__ unsigned umax4(u32x4 v) { return max(max(v[0], v[1]), max(v[2], v[3])); }
__device__ __forceinline__ u32x2 unpoison2(u32x2 v) { return u32x2{min(v[0], POISON - 1), min(v[1], POISON - 1)}; }
__device__ __forceinline__ u32x4 unpoison4(u32x4 v) {
  return u32x4{min(v[0], POISON - 1), min(v[1], POISON - 1), min(v[2], POISON - 1), min(v[3], POISON - 1)};
}

typedef union { struct { s16x4 a, b; } hf; u32x4 v; } frag_t;
__device__ __forceinline__ s16x4 trread(const char* smem, unsigned off) {
  return __builtin_amdgcn_ds_read_tr16_b64_v4i16((LDS_AS s16x4*)(smem + off));
}
__device__ __forceinline__ s16x4 trabs(unsigned addr) { return __builtin_amdgcn_ds_read_tr16_b64_v4i16((LDS_AS s16x4*)(size_t)addr); }

constexpr int WA = 6;
constexpr int WB = 4, WR = 3;
#define CH 6, 8, 9, 15, 17, 23, 25, 29, 31, 4
constexpr int CHV[10] = {CH};
constexpr int FKL = CHV[9];
constexpr int FSL = CHV[0], FS0 = CHV[1], FS0D = CHV[2], FS1 = CHV[3], FS1S = CHV[4];
constexpr int FXA = CHV[5], FXB = CHV[6], FXL = CHV[7], FXS = CHV[8];
constexpr int FXT = FXB >> 2;
constexpr int CSD = 3, POI = 4, RWS = 5, EIN = 8;
constexpr int ERQ = 16, CSL = 19, CSS = 22;
#define IMV 24
constexpr int IMG = IMV, KP0 = 26;
constexpr int PB0 = 10, PB1 = 24;
constexpr int ABL = 23, ABS = 27;
constexpr int XU = 3, XL = 2;
constexpr int x_slot(int u) { return u % XU == 0 ? 0 : u % XU == 1 ? 32768 : 49152; }
#define TCH 19, 7, 4, 27
constexpr int TCV[4] = {TCH}, TS0 = TCV[0], TSN = TCV[1], TW = TCV[2], TST = TCV[3];
constexpr int QL = 26;
constexpr int RXCD = 5;
constexpr int WDS = 17 - WB;
constexpr int FSG = 4, FDG = 3, FRG = 3, FXG = 3;
#define SLP 20, 28, 8, 46, 3, 4, 12
constexpr int SLV[7] = {SLP}, SDI0 = SLV[0], SDIR = SLV[1], GSL = SLV[2], RSL = SLV[3], KSL = SLV[4], XSL = SLV[5], OSL = SLV[6];
constexpr bool tile_piece(int s) { return s >= 3 && ((s - 3) >> 2) + 3 < 8 && ((s - 3) & 3) < 3; }
constexpr int a_req(int s, int n) {
  if (!(n & 1)) return -1;
  if (s <= 1) return 8 + 3 * s + (n >> 1);
  return n < 5 && s + 5 < 32 ? 2 * (s + 5) + (n >> 1) : -1;
}
constexpr int vm_n(int it, int s, int n) {
  int c = 0;
  if (n == 0) c += tile_piece(s);
  if (n == 4) c += (s == ABL) + (it ? (s == FSL ? 4 : 0) + (s == FKL) : (s == ERQ) + (s >= IMG && s < IMG + 3));
  if (!it) c += (s < 4 && n == 2) + (s == 0 && (n == 0 || n == 4)) + ((n == 3 || n == 5) && s + XL + XU < 32);
  else c += (a_req(s, n) >= 0) + ((s == 0 || s == 4) && n == 2);
  return c;
}
constexpr int vm_at(int it, int s, int n) {
  int c = it ? 8 : 14;
  for (int u = 0; u < 6 * s + n; ++u) c += vm_n(it, u / 6, u % 6);
  return c;
}
constexpr int vm_a(int it, int k) {
  if (k < 4) return 2 * k + 2;
  for (int u = 0; u < 192; ++u)
    if (a_req(u / 6, u % 6) == 2 * k + 1) return vm_at(it, (u + 1) / 6, (u + 1) % 6);
  return 0;
}
constexpr int vm_tile(int it, int t) { return t >= 3 ? vm_at(it, 4 * t - 7, 1) : t == 1 ? vm_at(0, 0, 5) : vm_at(0, 3, 3); }
constexpr int vm_x(int u) { return u == 2 ? 9 : u < XU + XL ? 12 + 2 * (u - XU) : vm_at(0, u - XU - XL + 1, 0); }

constexpr int wo_k(int it, int s) { return it || s < 4 ? 15 - s : s < 8 ? 7 - s : 19 - s; }
constexpr int wo_pos(int it, int s) { return 2 * s + 2 + (s >= WR ? 2 * it : 0); }
constexpr int wo_at(int it, int s) { return 2 * it + (s < WB - WR ? 2 * WR + 2 + 4 * s : s < 16 - WB ? 2 * (2 * WB - WR) + 2 * (s - WB + WR) : 32); }
template <int OFF> __device__ __forceinline__ void lds_count(unsigned ad) {
  asm volatile("s_mov_b64 exec, 1\n\tds_add_u32 %0, %1 offset:%2\n\ts_mov_b64 exec, -1" ::"v"(ad), "v"(1u), "n"(OFF) : "memory");
}
__device__ __forceinline__ void lds_look(char* c, unsigned want) {
  while ((unsigned)__builtin_amdgcn_readfirstlane(__hip_atomic_load((LDS_AS unsigned*)c, __ATOMIC_RELAXED, __HIP_MEMORY_SCOPE_WORKGROUP)) < want) {}
  asm volatile("" ::: "memory");
}
__device__ __forceinline__ void pin1(unsigned& a) { asm volatile("" : "+v"(a)); }
__device__ __forceinline__ void pin2(u32x4& a, u32x4& b) { asm volatile("" : "+v"(a), "+v"(b)::"memory"); }
__device__ __forceinline__ void pin2s(float& a, float& b) { asm volatile("" : "+v"(a), "+v"(b)); }
template <int OFF> __device__ __forceinline__ void lds2(u32x4& a, u32x4& b, unsigned ad) {
  asm volatile("ds_read_b128 %0, %2 offset:%3\n\tds_read_b128 %1, %2 offset:%4" : "=&v"(a), "=&v"(b) : "v"(ad), "n"(OFF), "n"(OFF + 1024) : "memory");
}
template <int N> __device__ __forceinline__ void lds_wait(u32x4& a, u32x4& b) { asm volatile("s_waitcnt lgkmcnt(%2)" : "+v"(a), "+v"(b) : "n"(N) : "memory"); }
__device__ __forceinline__ void pin2f(f32x2& a, f32x2& b) { asm volatile("" : "+v"(a), "+v"(b)); }
__device__ __forceinline__ void pin_ac(f32x4 (&ac)[2][4]) {
  asm volatile("" : "+{v[176:179]}"(ac[0][0]), "+{v[180:183]}"(ac[0][1]), "+{v[184:187]}"(ac[0][2]), "+{v[188:191]}"(ac[0][3]),
                    "+{v[192:195]}"(ac[1][0]), "+{v[196:199]}"(ac[1][1]), "+{v[200:203]}"(ac[1][2]), "+{v[204:207]}"(ac[1][3]));
}

constexpr int L_XC = LDS_BYTES - 64, L_SKEY = 32704, L_SRED = 4096, L_SFIN = 20480;
__device__ __forceinline__ unsigned key_load(rsrc_t rs, int l, int L, int rep) { return ld4(rs, (l & 15) * 4, OFF_KEY + (L * 16 + rep) * 128); }
__device__ __forceinline__ unsigned umax16(unsigned k) {
  k = max(k, dppu<0xB1>(k));
  k = max(k, dppu<0x4E>(k));
  k = max(k, dppu<0x141>(k));
  return max(k, dppu<0x140>(k));
}
__device__ __forceinline__ unsigned key_row(unsigned key) { return (0xffffu - (key & 0xffffu)) & 2047u; }
__device__ __forceinline__ unsigned key_wait(rsrc_t rs, u32x2 T, int l, int L, int rep, unsigned kv) {
  if (!__all(kv != POISON)) {
    unsigned spins = 0;
    do {
      if (spin_fail(spins, rs, T)) break;
      if (L == 0) __builtin_amdgcn_s_sleep(KSL);
      kv = key_load(rs, l, L, rep);
    } while (!__all(kv != POISON));
  }
  return __builtin_amdgcn_readfirstlane(umax16(kv));
}
__device__ __forceinline__ unsigned ffn_xoff(unsigned key, int L, int w) { return OFF_CA + L * 32768 + (key_row(key) >> 7) * 2048 + w * 256; }
__device__ __forceinline__ void row_load(rsrc_t rs, int g, unsigned xo, u32x4 (&xv)[4]) {
  ld16h<0>(xv[0], rs, g * 16, xo); ld16h<64>(xv[1], rs, g * 16, xo); ld16h<128>(xv[2], rs, g * 16, xo); ld16h<192>(xv[3], rs, g * 16, xo);
}
template <int SL = 0> __device__ __forceinline__ void ffn_xwait(rsrc_t rs, u32x2 T, unsigned xo, int l, u32x4 (&xv)[4]) {
#define XV_OK() __all(max(max(umax4(xv[0]), umax4(xv[1])), max(umax4(xv[2]), umax4(xv[3]))) != POISON)
  if (!XV_OK()) {
    unsigned spins = 0;
    do {
      if (spin_fail(spins, rs, T)) break;
      if (SL) __builtin_amdgcn_s_sleep(SL);
#pragma unroll
      for (int i = 0; i < 4; ++i) xv[i] = ld16(rs, (l >> 4) * 16 + i * 64, xo);
    } while (!XV_OK());
  }
#undef XV_OK
}
__device__ __forceinline__ void ffn_gu(const u32x4 (&gv)[4], const u32x4 (&uv)[4], const u32x4 (&xv)[4], f32x4& ga, f32x4& ua) {
  ga = mfma16(gv[0], xv[0], f32x4{0.f, 0.f, 0.f, 0.f}); ua = mfma16(uv[0], xv[0], f32x4{0.f, 0.f, 0.f, 0.f});
#pragma unroll
  for (int i = 1; i < 4; ++i) { ga = mfma16(gv[i], xv[i], ga); ua = mfma16(uv[i], xv[i], ua); }
}
__device__ __forceinline__ void ffn_gu_check(rsrc_t rs, u32x2 T, unsigned xo, int l, const u32x4 (&gv)[4], const u32x4 (&uv)[4], u32x4 (&xv)[4], f32x4& ga, f32x4& ua) {
  if (__builtin_expect(__any(ga[0] != ga[0]), 0)) {
    ffn_xwait(rs, T, xo, l, xv);
    ffn_gu(gv, uv, xv, ga, ua);
  }
}
__device__ __forceinline__ void ffn_gu_store(char* smem, int l, int w, f32x4 ga, f32x4 ua) {
  LDS_AS f32x4* sred = (LDS_AS f32x4*)(smem + L_SRED);
  if ((l & 15) == 0) { sred[w * 8 + (l >> 4)] = ga; sred[w * 8 + 4 + (l >> 4)] = ua; }
}
__device__ __forceinline__ f32x4 ffn_t_read(char* smem, int l) { return ((const LDS_AS f32x4*)(smem + L_SRED))[(l & 7) * 8 + ((l >> 3) & 1) * 4 + (l >> 4)]; }
__device__ __forceinline__ void ffn_d_read(char* smem, int l, int blk, s16x4 (&dfr)[8]) {
#pragma unroll
  for (int i = 0; i < 8; ++i) dfr[i] = trread(smem, L_DN + blk * 4096 + (l >> 4) * 1024 + ((l & 15) >> 2) * 256 + i * 32 + (l & 3) * 8);
}
__device__ __forceinline__ float ffn_d_sum(float t) { t += dppf<0xB1>(t); t += dppf<0x4E>(t); return t + dppf<0x141>(t); }
__device__ __forceinline__ u32x2 ffn_h(f32x4 t, int l) {
#pragma unroll
  for (int e = 0; e < 4; ++e) t[e] = ffn_d_sum(t[e]);
  const float a = (l & 1) ? t[1] : t[0], b = (l & 1) ? t[3] : t[2], s = (l & 2) ? b : a;
  const float gg = rbf1(s), uu = rbf1(dppf<0x128>(s));
  const float h = rbf1(gg * __builtin_amdgcn_rcpf(1.0f + __expf(-gg))) * uu;
  const unsigned h0 = pack2(h, dppf<0x55>(h)), h1 = pack2(dppf<0xAA>(h), dppf<0xFF>(h));
  return (l & 15) == 0 ? u32x2{h0, h1} : u32x2{0u, 0u};
}
__device__ __forceinline__ void ffn_d_mma(const s16x4 (&dfr)[8], u32x2& hh, f32x4& ya, f32x4& yb) {
#pragma unroll
  for (int i = 0; i < 8; ++i) {
    if (i & 1) yb = mfma16k16(dfr[i], __builtin_bit_cast(s16x4, hh), yb);
    else ya = mfma16k16(dfr[i], __builtin_bit_cast(s16x4, hh), ya);
    hh = u32x2{dppu<0x111>(hh[0]), dppu<0x111>(hh[1])};
  }
}
__device__ __forceinline__ void ffn_d_store(rsrc_t rs, int l, int G0, int L, int slice, f32x4 y) {
  const unsigned c = l & 15;
  st16(unpoison4(__builtin_bit_cast(u32x4, y)), rs, (G0 + (c >> 1)) * 32768 + slice * 128 + (c & 1) * 64 + (l >> 4) * 16, OFF_PART + L * (1u << 20));
}
__device__ __forceinline__ void red_load(rsrc_t rs, int tid, int L, unsigned G, unsigned key, u32x4 (&pv)[4], u32x2& cxn, u32x2& cx1) {
#pragma unroll
  for (int k = 0; k < 4; ++k) pv[k] = ld16(rs, min(tid + 512 * k, 1791) * 16, OFF_PART + L * (1u << 20) + G * 32768);
  cxn = ld8(rs, (tid & 3) * 16 + (tid & 4) * 2, OFF_CA + L * 32768 + (key_row(key) >> 7) * 2048 + G * 64);
  cx1 = ld8(rs, (tid & 3) * 16 + (tid & 4) * 2, OFF_CB + L * 32768 + (key_row(key) >> 7) * 2048 + G * 64);
}
__device__ __forceinline__ void red_sum(rsrc_t rs, char* smem, u32x2 T, int tid, int w, int L, unsigned G, unsigned key,
                                        u32x4 (&pv)[4], u32x2& cxn, u32x2& cx1) {
#define PV_OK() __all(max(max(max(umax4(pv[0]), umax4(pv[1])), max(umax4(pv[2]), umax4(pv[3]))), max(max(cxn[0], cxn[1]), max(cx1[0], cx1[1]))) != POISON)
  if (!PV_OK()) {
    unsigned spins = 0;
    do {
      if (spin_fail(spins, rs, T)) break;
      red_load(rs, tid, L, G, key, pv, cxn, cx1);
    } while (!PV_OK());
  }
#undef PV_OK
  f32x4 s = (__builtin_bit_cast(f32x4, pv[0]) + __builtin_bit_cast(f32x4, pv[1])) + __builtin_bit_cast(f32x4, pv[2]);
  if (w < 4) s += __builtin_bit_cast(f32x4, pv[3]);
  ((LDS_AS f32x4*)(smem + L_SFIN))[tid] = s;
}
template <int NW> __device__ __forceinline__ u32x2 red_final(char* smem, int l, unsigned key, u32x2 cxn, u32x2 cx1) {
  const LDS_AS f32x4* sf = (const LDS_AS f32x4*)(smem + L_SFIN) + l;
  f32x4 s = (sf[0] + sf[64]) + (sf[128] + sf[192]);
  if constexpr (NW == 8) s += (sf[256] + sf[320]) + (sf[384] + sf[448]);
#pragma unroll
  for (int j = 0; j < 4; ++j) s[j] = sum_hi8(s[j]);
  const unsigned k16 = key >> 16;
  const float rmax = __builtin_bit_cast(float, ((k16 & 0x8000u) ? (k16 & 0x7fffu) : (~k16 & 0xffffu)) << 16);
  const float sig = rbf1(__builtin_amdgcn_rcpf(1.0f + __expf(-rmax)));
  const float xn4[4] = {lo16(cxn[0]), hi16(cxn[0]), lo16(cxn[1]), hi16(cxn[1])};
  const float x14[4] = {lo16(cx1[0]), hi16(cx1[0]), lo16(cx1[1]), hi16(cx1[1])};
  float o[4];
#pragma unroll
  for (int j = 0; j < 4; ++j) o[j] = x14[j] + rbf1(xn4[j] + rbf1(rbf1(s[j]) * sig));
  return u32x2{pack2(o[0], o[1]), pack2(o[2], o[3])};
}

struct RowFin { float mean, var, mb, rd; unsigned key; };
__device__ __forceinline__ void fin_wait(rsrc_t rs, u32x2 T, unsigned voff, unsigned so, u32x4 (&sp)[4]) {
#define ST_OK() __all(max(max(umax4(sp[0]), umax4(sp[1])), max(umax4(sp[2]), umax4(sp[3]))) != POISON)
  if (!ST_OK()) {
    unsigned spins = 0;
    do {
      if (spin_fail(spins, rs, T)) break;
#pragma unroll
      for (int i = 0; i < 4; ++i) sp[i] = ld16(rs, voff + i * 2048, so);
    } while (!ST_OK());
  }
#undef ST_OK
}
__device__ __forceinline__ RowFin fin_key(rsrc_t rs, u32x2 T, unsigned voff, unsigned so, char* skey, int l, int w, unsigned R, float wsum, u32x4 (&sp)[4]) {
  RowFin r;
#define ST_SUM() ((__builtin_bit_cast(f32x4, sp[0]) + __builtin_bit_cast(f32x4, sp[1])) + (__builtin_bit_cast(f32x4, sp[2]) + __builtin_bit_cast(f32x4, sp[3])))
  f32x4 sf = ST_SUM();
  const float z = (sf[0] + sf[1]) + sf[2];
  if (__any(z != z)) {
    fin_wait(rs, T, voff, so, sp);
    sf = ST_SUM();
  }
#undef ST_SUM
  float s0 = sf[0], s1 = sf[1], s2 = sf[2];
  add_g3(s0, s1, s2);
  r.mean = s0 * (1.0f / 1024.0f);
  r.var = fmaxf(__builtin_fmaf(-r.mean, r.mean, s1 * (1.0f / 1024.0f)), 0.f);
  r.mb = rbf1(r.mean); r.rd = ln_rd(r.var);
  const unsigned rb16 = pack2((s2 - r.mb * wsum) * r.rd, 0.f) & 0xffffu;
  r.key = min((((rb16 & 0x8000u) ? (~rb16 & 0xffffu) : (rb16 | 0x8000u)) << 16) | (0xffffu - R), POISON - 1);
  const unsigned wk = umax16(r.key);
  if (l == 0) ((LDS_AS unsigned*)skey)[w] = wk;
  return r;
}
__device__ __forceinline__ void fin_cand(rsrc_t rs, char* skey, int L, int g, int rb, int ct, const RowFin& r, const f32x2 (&v)[8]) {
  const u32x4 k0 = *(const LDS_AS u32x4*)skey, k1 = *(const LDS_AS u32x4*)(skey + 16);
  const unsigned best = max(umax4(k0), umax4(k1));
  if (r.key == best) {
    const unsigned co = L * 32768 + rb * 2048 + ct * 128 + g * 16;
    f32x2 t[8], rd2 = {r.rd, r.rd}, of2 = {-r.mb * r.rd, -r.mb * r.rd};
    asm volatile("" : "+v"(rd2), "+v"(of2));
#pragma unroll
    for (int j = 0; j < 8; ++j) t[j] = __builtin_elementwise_fma(v[j], rd2, of2);
    st16(unpoison4(pack8v(t)), rs, co, OFF_CA); st16(unpoison4(pack8v(t + 4)), rs, co + 64, OFF_CA);
    if (g == 0) st4(best, rs, OFF_KEY + (L * 16 + ct) * 128 + rb * 4, 0);
    st16(unpoison4(pack8v(v)), rs, co, OFF_CB); st16(unpoison4(pack8v(v + 4)), rs, co + 64, OFF_CB);
  }
}
__device__ __forceinline__ void fin_x2(const RowFin& r, f32x2 (&v)[8]) {
  f32x2 sc2 = {1.0f + r.rd, 1.0f + r.rd}, of2 = {-r.mb * r.rd, -r.mb * r.rd};
  asm volatile("" : "+v"(sc2), "+v"(of2));
#pragma unroll
  for (int j = 0; j < 8; ++j) v[j] = __builtin_elementwise_fma(v[j], sc2, of2);
}

extern "C" __device__ unsigned long long dispatch_id() __asm("llvm.amdgcn.dispatch.id");

extern "C" __global__ __launch_bounds__(512, 2) void mega_kernel(const bf16* x, char* ws, const bf16* wqkv, const bf16* wo, const bf16* wr,
                                                                 const bf16* wg, const bf16* wu, const bf16* wd, bf16* out) {
  extern __shared__ __attribute__((aligned(16))) char smem[];
  const int w0_ = __builtin_amdgcn_readfirstlane(threadIdx.x >> 6);
  typedef unsigned long long u64x2 __attribute__((ext_vector_type(2)));
  u64x2 a01;
  unsigned long long a2;
  asm volatile("s_load_dwordx4 %0, %2, 0\n\ts_load_dwordx2 %1, %2, 16" : "=&s"(a01), "=&s"(a2) : "s"(__builtin_amdgcn_kernarg_segment_ptr()));
  const unsigned lbase = (unsigned)(size_t)(LDS_AS char*)smem;
  {
    unsigned long long pc;
    unsigned len;
    asm volatile("s_getpc_b64 %0\n\ts_mov_b32 %1, .Ltext_end-mega_kernel\n\t.subsection 1\n.Ltext_end:\n\t.subsection 0" : "=s"(pc), "=s"(len));
    const unsigned o = ((blockIdx.x >> 3) * 64 + (threadIdx.x & 63)) * 128;
    if (w0_ == 7 && o < len)
      asm volatile("s_mov_b32 m0, %0\n\ts_nop 0\n\tglobal_load_lds_dword %1, %2" ::"s"(lbase + L_TOUCH0), "v"(o), "s"(pc & ~127ull) : "memory");
  }
#define HGRP() ((xcd & 2) + (xcd >> 2))
#define IDS()                                                                                         \
  int tid = threadIdx.x, w = w0_, bid = blockIdx.x;                                                   \
  asm volatile("" : "+v"(tid));                                                                       \
  asm volatile("" : "+s"(w));                                                                         \
  asm volatile("" : "+s"(bid));                                                                       \
  const int l = tid & 63, xcd = bid & 7, bi = bid >> 3;                                               \
  const int p = (xcd & 1) * 4 + (bi & 3), h = HGRP() * 8 + (bi >> 2);         \
  const int rb = 2 * xcd + (bi & 1), ct = bi >> 1;                               \
  const int slice = (xcd - (xcd > RXCD)) * 32 + bi;                                            \
  const bool ffn_on = xcd != RXCD;                                                                    \
  const int g = l >> 4;                                                                               \
  const int rt = w < 4 ? w : 11 - w;          \
  KARGS();                                                                                            \
  (void)p; (void)h; (void)rb; (void)ct; (void)slice; (void)ffn_on; (void)g; (void)rt;
#define PROT() (2 * (p & 3) + (p >> 2))
  typedef const void* const __attribute__((address_space(4)))* karg_t;
#define KARGS() karg_t ka = (karg_t)__builtin_amdgcn_kernarg_segment_ptr(); asm volatile("" : "+s"(ka))
#define ARGP(name, i) const GAS bf16* name = (const GAS bf16*)ka[i]; asm volatile("" : "+s"(name))
#define RSRC(ptr, bytes) __builtin_amdgcn_make_buffer_rsrc((void*)(ptr), 0, bytes, 0x00020000)
#define rwq RSRC(pwq, 1024 * 3072 * 2)
#define rwo RSRC(pwo, 1024 * 1024 * 2)
#define rwg RSRC(pwg, 1024 * F_ * 2)
#define rwu RSRC(pwu, 1024 * F_ * 2)
#define rwd RSRC(pwd, 1024 * F_ * 2)
#define rout RSRC(pout, 2048 * 1024 * 2)

#define WQ_VOFF() ((l >> 2) * 6144 + (((l & 3) ^ ((((l >> 4) ^ (l >> 5)) & 1) << 1)) << 4))
#define WQ_KMAJ(q) (((q) % 3) * 8 + (q) / 3)
#define WQ_PIECE(t, j, dst) dma16(rwq, wqv, ((((t) + rot) & 7) * 128 + ((j) & 7) * 16) * 6144 + ((j) >> 3) * 2048 + h * 64, lbase + (dst) + (j) * 1024)
#define WQ_BOFF(dt) ((g >> 1) * 1024 + (8 * (g & 1) + ((l & 15) >> 2)) * 64 + (l & 3) * 8 + (((dt) ^ (g & 1)) << 5))
#define X_REQ(u, hf) dma16(rx, xv[hf], (p * 256 + rt * 32 + ((u) & 1) * 16 + (hf) * 8) * 2048 + ((((u) >> 2) + rot) & 7) * 256 + (((u) >> 1) & 1) * 128, lbase + rt * 2048 + x_slot(u) + (hf) * 1024)
#define WO_PIECE_OF(w, j) dma16(rwo, wov, (((w) >> 2) * 64 + ((w) & 3) + 4 * (j)) * 16384 + ct * 128, lbase + 32768 + (j) * 8192 + (w) * 1024)
#define WO_PIECE(j) WO_PIECE_OF(w, j)
#define WD_DMA_OF(v) _Pragma("unroll") for (int i = 0; i < 4; ++i)                                  \
      dma16(rwd, (l >> 4) * 2048 + (l & 15) * 16, (unsigned)(slice * 16 + 4 * i) * 2048 + (v) * 256, lbase + L_DN + (v) * 4096 + i * 1024)
#define POISON_BIG(L)                                                                                \
  if (w >= 2) {                                                                                      \
    unsigned pz1 = POISON;                                                                           \
    asm volatile("" : "+v"(pz1));                                                                    \
    st16(u32x4{pz1, pz1, pz1, pz1}, rs, (w >= 4 ? OFF_PART + (L) * (1u << 20) + bid * 4096 - 4096 : OFF_STAT + (L) * 524288 + (p * 32 + h) * 2048 - 2048) + tid * 16, 0); \
  }
  u32x4 gv[4], uv[4];
  u32x4 keep[9];
  u32x4 pre[4][2];
#define PRE_LOAD(it_, rot_, s0, s1)                                                                  \
  static_for<(s1) - (s0)>([&](auto c_) __attribute__((always_inline)) {                              \
    constexpr int s = (s0) + decltype(c_)::value;                                                    \
    const unsigned ab_ = OFF_X1 + (((rot_) + (s >> 2)) & 7) * 8192 + ((s >> 1) & 1) * 4096; \
    ld16w<2 * s, (s & 1) * 2048>(pre[s][0], rs, l * 16, ab_ + (p * 8 + rt) * 65536);                         \
    ld16w<2 * s + 1, (s & 1) * 2048 + 1024>(pre[s][1], rs, l * 16, ab_ + (p * 8 + rt) * 65536);                 \
  })
  u32x2 T;
  const GAS bf16* tsrc;
  unsigned xv[2], xa[2];
  u32x2 ep;
  u32x4 rows0[4];
#define X_READ(u, d0, d1) { d0 = *(const LDS_AS u32x4*)(size_t)(xa[0] + x_slot(u)); d1 = *(const LDS_AS u32x4*)(size_t)(xa[1] + x_slot(u)); }
  unsigned g1[5], wqv0;
  {
    IDS();
    const int rot = PROT(), qa = WQ_KMAJ(w), qb = WQ_KMAJ(8 + w);
    wqv0 = WQ_VOFF();
    xv[0] = (l >> 3) * 2048 + (((l & 7) ^ (l >> 4)) << 4); xv[1] = xv[0] ^ 64;
    g1[0] = (rot * 128 + (qa & 7) * 16) * 6144 + (qa >> 3) * 2048 + h * 64; g1[1] = lbase + L_RING + qa * 1024;
    g1[2] = (rot * 128 + (qb & 7) * 16) * 6144 + (qb >> 3) * 2048 + h * 64; g1[3] = lbase + L_RING + qb * 1024;
    g1[4] = (p * 256 + rt * 32) * 2048 + rot * 256;
    asm volatile("" : "+v"(wqv0), "+v"(xv[0]), "+v"(xv[1]));
    asm volatile("s_waitcnt lgkmcnt(0)" : "+s"(a01), "+s"(a2), "+s"(g1[0]), "+s"(g1[1]), "+s"(g1[2]), "+s"(g1[3]), "+s"(g1[4]));
  }
  x = (const bf16*)a01[0]; ws = (char*)a01[1]; wqkv = (const bf16*)a2;
  const rsrc_t rs = __builtin_amdgcn_make_buffer_rsrc((void*)ws, 0, WS_BYTES, 0x00020000);
  const rsrc_t rx = RSRC(x, 2048 * 1024 * 2);
  {
    IDS();
    const int rot = PROT();
    const bf16* pwq = wqkv;
    const unsigned wqv = wqv0;
    dma16(rwq, wqv, g1[0], g1[1]);
#pragma unroll
    for (int i = 0; i < 4; ++i) dma16(rx, xv[i & 1], g1[4] + i * 16384, lbase + rt * 2048 + x_slot(i >> 1) + (i & 1) * 1024);
    dma16(rwq, wqv, g1[2], g1[3]);
    asm volatile("s_barrier" ::: "memory");
    WQ_PIECE(0, WQ_KMAJ(16 + w), L_RING);
    if (w == 6 && bi == 0) {
      ARGP(pwr, 4);
      asm volatile("s_mov_b32 m0, %0\n\ts_nop 0\n\tbuffer_load_dword %1, %2, 0 offen lds" ::"s"(lbase + L_TOUCH0 + 256), "v"((l & 15) * 128), "s"(RSRC(pwr, 2048)) : "memory");
    }
    X_REQ(2, 0); X_REQ(2, 1);
    asm volatile("buffer_load_dwordx2 %0, off, %1, 0 sc1" : "=v"(ep) : "s"(rs) : "memory");
    asm volatile("" : "=v"(rows0[0]), "=v"(rows0[1]), "=v"(rows0[2]), "=v"(rows0[3]));
    if (w < 4) ldrows(rows0, rx, (l >> 5) * 2048 + (l & 31) * 16, (p * 256 + h * 8 + 2 * w) * 2048);
    {
      ARGP(pwo, 3); ARGP(pwg, 5); ARGP(pwu, 6);
      tsrc = (w < 4 ? pwg : pwu) + ((bi * 32 + ((tid & 255) >> 3)) * F_ + (slice >> 5) * 512 + (tid & 7) * 64);
      if (!ffn_on) tsrc = pwo + (bi * 512 + tid) * 64;
      asm volatile("" : "+v"(tsrc));
    }
#pragma unroll
    for (int kk = 0; kk < 2; ++kk) {
      const int r = l & 15;
      xa[kk] = lbase + rt * 2048 + (r >> 3) * 1024 + (r & 7) * 128 + (((4 * kk + g) ^ (r >> 1)) << 4);
      asm volatile("" : "+v"(xa[kk]));
    }
    if (tid == 192) *(LDS_AS u32x4*)(smem + L_QC) = u32x4{0u, 0u, 0u, 0u};
    if (w < 4) wait_vm<8>(); else wait_vm<4>();
    X_READ(0, pre[0][0], pre[1][0]); X_READ(1, pre[0][1], pre[1][1]);
    asm volatile("s_waitcnt lgkmcnt(0)\n\ts_barrier" : "+v"(pre[0][0]), "+v"(pre[0][1]), "+v"(pre[1][0]), "+v"(pre[1][1])::"memory");
    X_REQ(3, 0); X_REQ(3, 1); X_REQ(4, 0); X_REQ(4, 1);
  }
  unsigned sel0 = 0x7FFFFFFFu;
  u32x4 x2p[2];
  unsigned xm0 = 0;

  auto layer = [&](auto it_c) __attribute__((always_inline)) {
    constexpr int it = decltype(it_c)::value;
    f32x4 acc[2][6];
    f32x2 qs2[2];
    u32x4 k0, k1, v0, v1;
    unsigned tdum = 0;
    {
      IDS();
      bool aff = false;
      unsigned aq[2];
      u32x4 av[2];
#define PIN_ACC() asm volatile("" : "+{v[176:179]}"(acc[0][0]), "+{v[180:183]}"(acc[0][1]), "+{v[184:187]}"(acc[0][2]), "+{v[188:191]}"(acc[0][3]), "+{v[192:195]}"(acc[0][4]), "+{v[196:199]}"(acc[0][5]), \
                               "+{v[200:203]}"(acc[1][0]), "+{v[204:207]}"(acc[1][1]), "+{v[208:211]}"(acc[1][2]), "+{v[212:215]}"(acc[1][3]), "+{v[216:219]}"(acc[1][4]), "+{v[220:223]}"(acc[1][5]))
#define PINV4(x) asm volatile("" : "+v"(x[0]), "+v"(x[1]), "+v"(x[2]), "+v"(x[3])::"memory")
      auto qkv_loop = [&](auto it_c) __attribute__((always_inline)) {
        constexpr int IT = decltype(it_c)::value;
        const unsigned abase = OFF_X1 + (p * 8 + rt) * 65536;
        const int rot = PROT() + 5 * IT;
        unsigned fkey = 0, kv, bq[2];
        u32x4 fx[4], bv[2], af[WA][2];
        u32x2 cxn, cx1;
        f32x4 sa, sb, cs4[2];
        f32x2 b2[2];
#define AB_OFF() (OFF_AB + IT * 16384 + (p * 8 + rt) * 128)
#define A_LOAD1(k, a) ld16w<(k) % WA * 2 + (a), ((k) & 1) * 2048 + (a) * 1024>(af[(k) % WA][a], rs, l * 16, abase + ((((k) >> 2) + rot) & 7) * 8192 + (((k) >> 1) & 1) * 4096)
#pragma unroll
        for (int s = 0; s < 2 + 2 * IT; ++s) { af[s][0] = pre[s][0]; af[s][1] = pre[s][1]; }
        const bf16* pwq = wqkv;
        const int tstep = TS0 + bid % TSN;
        const unsigned wqv = WQ_VOFF();
        unsigned wb[3], lw = lbase + L_RING + w * 3072, xrow = (p * 256 + rt * 32) * 2048, lx = lbase + rt * 2048;
#pragma unroll
        for (int i = 0; i < 3; ++i) wb[i] = IT ? OFF_WI + h * 8 * STAGE + w * 3072 : ((3 * w + i) & 7) * 16 * 6144 + ((3 * w + i) >> 3) * 2048 + h * 64;
        asm volatile("" : "+s"(wb[0]), "+s"(lw));
        if constexpr (!IT) asm volatile("" : "+s"(wb[1]), "+s"(wb[2]), "+s"(xrow), "+s"(lx));
#define QKV_DMA1(t, i)                                                                            \
  {                                                                                               \
    if constexpr (IT == 0) dma16r<SLOT(t) * STAGE + (i) * 1024>(rwq, wqv, wb[i], (((t) + rot) & 7) * (128 * 6144), lw); \
    else dma16t<SLOT(t) * STAGE, (i) * 1024>(rs, l * 16, wb[0], (((t) + rot) & 7) * STAGE, lw);   \
  }
#define X_REQ2(u, hf) dma16i<x_slot(u) + (hf) * 1024, ((u) & 1) * 32768 + (hf) * 16384 + (((u) >> 1) & 1) * 128>(rx, xv[hf], xrow + ((((u) >> 2) + rot) & 7) * 256, lx)
        unsigned bo[2][2];
#pragma unroll
        for (int dt = 0; dt < 2 - IT; ++dt) {
          bo[dt][0] = lbase + L_RING + (IT ? l * 16 : WQ_BOFF(dt)); bo[dt][1] = bo[dt][0] + 2 * STAGE;
          asm volatile("" : "+v"(bo[dt][0]), "+v"(bo[dt][1]));
        }
#define FRAG_T() (lbase + L_RING + (4 * g + ((l & 15) >> 2)) * 64 + (l & 3) * 8 + (((g ^ (g >> 1)) & 1) << 5))
        auto frag_t2 = [&](unsigned o) __attribute__((always_inline)) {
          frag_t t;
          t.hf.a = trabs(o); t.hf.b = trabs(o + 1024);
          return t.v;
        };
        unsigned ka[3];
        auto tile_sum = [&](u32x4 pt, int dst) __attribute__((always_inline)) {
          f32x4 c = __builtin_bit_cast(f32x4, pt);
#pragma unroll
          for (int e = 0; e < 4; ++e) { c[e] += dppf<0x124>(c[e]); c[e] += dppf<0x128>(c[e]); c[e] = add_x16(c[e]); }
          if ((l & 28) == 0) *(LDS_AS f32x4*)(smem + dst + (w * 2 + (l >> 5)) * 64 + (l & 3) * 16) = c;
        };
        frag_t bfr[6];
#define QKV_BO(s, n) (bo[(n) & 1][SLOT((s) >> 2) >> 1] + (SLOT((s) >> 2) & 1) * STAGE + ((n) >> 1) * 8192 + ((s) & 3) * 2048)
#define QKV_RDA(s, n)                                                                             \
  {                                                                                               \
    if constexpr (IT == 0) bfr[n].hf.a = trabs(QKV_BO(s, n));                                     \
    else bfr[n].v = *(const LDS_AS u32x4*)(size_t)(bo[0][SLOT((s) >> 2) >> 1] + (SLOT((s) >> 2) & 1) * STAGE + (((s) & 3) * 6 + (n)) * 1024); \
  }
#define QKV_RDB(s, n) { if constexpr (IT == 0) bfr[n].hf.b = trabs(QKV_BO(s, (n) ^ 1) + 256); }
#pragma unroll
        for (int n = 0; n < 6; ++n) { QKV_RDA(0, n); if (n < 5) QKV_RDB(0, n); }
#define XF_OFF() (l & 8 ? ((l & 7) * RX + bi % RX) * 256 + h * 8 : (p * RX + (xcd >> 1) % RX) * 256 + (4 * rt + (l & 3)) * 8)
#define XP_OFF() (OFF_XNEW + ((rot + FXT) & 7) * 256)
#define FP_OFF(i) (w == 3 ? OFF_XNEW + (i) * 1024 : OFF_FX + h * 3072 + w * 1024)
#define FX_OK(n) __all(((n) == 4 ? max(max(umax4(fx[0]), umax4(fx[1])), max(umax4(fx[2]), umax4(fx[3]))) : max(umax4(fx[0]), umax4(fx[1]))) != POISON)
#define SINCE(s0) (vm_at(IT, s, n) - vm_at(IT, s0, 5))
#define DEF4(x) asm volatile("" : "=v"(x[0]), "=v"(x[1]), "=v"(x[2]), "=v"(x[3]))
        LDS_AS unsigned* qcnt = (LDS_AS unsigned*)(smem + L_QC);
        unsigned qc;
        unsigned qadr = lbase + L_QC, qone = 1;
        asm volatile("" : "+v"(qadr), "+v"(qone));
#define count_in(i) asm volatile("s_mov_b64 exec, 1\n\tds_add_u32 %0, %1 offset:%2\n\ts_mov_b64 exec, -1" ::"v"(qadr), "v"(qone), "n"(4 * (i)) : "memory")
        auto count_wait = [&](int i, unsigned want) __attribute__((always_inline)) {
          if (__builtin_expect((unsigned)__builtin_amdgcn_readfirstlane(qc) < want, 0)) {
            while ((unsigned)__builtin_amdgcn_readfirstlane(__hip_atomic_load(qcnt + i, __ATOMIC_RELAXED, __HIP_MEMORY_SCOPE_WORKGROUP)) < want) {}
          }
          asm volatile("" ::: "memory");
        };
        unsigned long long xok = xm0;
        auto x1_need = [&](unsigned long long m) __attribute__((always_inline)) {
          if (__builtin_expect((xok & m) != m, 0)) {
            for (unsigned spins = 0;;) {
              const u32x2 f = ld8(rs, F_X1 + ((p * RX + (xcd >> 1) % RX) * 32 + (l & 31)) * 8, 0);
              xok = __builtin_amdgcn_ballot_w64(f[0] == T[0] && f[1] == T[1]);
              if ((xok & m) == m) break;
              if (spin_fail(spins, rs, T)) { xok = ~0ull; break; }
              __builtin_amdgcn_s_sleep(KSL);
            }
            asm volatile("" ::: "memory");
          }
        };
        auto gap = [&](auto sc_, auto nc_) __attribute__((always_inline)) {
          constexpr int s = decltype(sc_)::value, n = decltype(nc_)::value;
          {
            constexpr int ts = (s + 3) / 4, tc = (s + 2) / 4, t0 = 1;
            if constexpr (ts >= 3 && ts < 8 && s == 4 * ts - 3 && n == 2) { wait_vm<vm_at(IT, s, n) - vm_tile(IT, ts)>(); count_in(0); }
            if constexpr (!IT && tc >= t0 && tc < 3 && s == 4 * tc - 2 && n == 1) { wait_vm<vm_at(IT, s, n) - vm_tile(IT, tc)>(); count_in(0); }
            if constexpr (IT && ts >= t0 && ts < 3 && s == 4 * ts - 3 && n == 2) count_in(0);
            if constexpr (tc >= t0 && tc < 8 && s == 4 * tc - 2) {
              if constexpr (n == 3) qc = __hip_atomic_load(qcnt, __ATOMIC_RELAXED, __HIP_MEMORY_SCOPE_WORKGROUP);
              if constexpr (n == 5) count_wait(0, 8 * (tc - t0 + 1));
            }
          }
          if constexpr (n == 0 && tile_piece(s)) QKV_DMA1(((s - 3) >> 2) + 3, (s - 3) & 3);
          if constexpr (IT == 0) {
            if constexpr (s == 0 && !(n & 1)) QKV_DMA1(1, n >> 1);
            if constexpr (s >= 1 && s < 4 && n == 2) QKV_DMA1(2, s - 1);
            constexpr int u = s + XL;
            if constexpr (n == 1 && u < 32) {
              if (s == 0 && w < 4) wait_vm<vm_at(0, s, n) - vm_x(u) + 4>();
              else if (s > TST && w >= TW) wait_vm<vm_at(0, s, n) - vm_x(u) + 1>();
              else wait_vm<vm_at(0, s, n) - vm_x(u)>();
              X_READ(u, af[(u & ~1) % WA][u & 1], af[(u | 1) % WA][u & 1]);
            }
            if constexpr (n == 3 && u + XU < 32) { asm volatile("s_waitcnt lgkmcnt(%0)" ::"n"(s + 1 < QL ? 4 : 3) : "memory"); X_REQ2(u + XU, 0); }
            if constexpr (n == 5 && u + XU < 32) X_REQ2(u + XU, 1);
            if constexpr (s == PB0 && n == 0) { POISON_BIG(0); }
            if constexpr (s == 0 && n == 1) count_in(2);
            if constexpr (s == 0 && n == 3) qc = __hip_atomic_load(qcnt + 2, __ATOMIC_RELAXED, __HIP_MEMORY_SCOPE_WORKGROUP);
            if constexpr (s == 0 && n == 5) count_wait(2, 8);
            if constexpr (s == 1 && n == 1) {
              asm volatile("" : "+v"(ep)::"memory");
              const unsigned long long e = (((unsigned long long)(unsigned)__builtin_amdgcn_readfirstlane(ep[1]) << 32) | (unsigned)__builtin_amdgcn_readfirstlane(ep[0])) + 1ull + dispatch_id() * 0x9E3779B97F4A7C16ull;
              T = u32x2{(unsigned)e, (unsigned)(e >> 32)};
            }
            if constexpr (s == RWS && n == 0) {
              if (w < 4) {
                PINV4(rows0);
                f32x2 sv[16];
#pragma unroll
                for (int i = 0; i < 4; ++i) unpack8v(rows0[i], sv + 4 * i);
                f32x2 t0 = sv[0] + sv[1], t1 = sv[2] + sv[3];
#pragma unroll
                for (int j = 4; j < 16; j += 2) { t0 += sv[j]; t1 += sv[j + 1]; }
                const float m = sum32(hadd(t0 + t1)) * (1.0f / 1024.0f);
                f32x2 m2 = {m, m}, q0 = {0.f, 0.f}, q1 = {0.f, 0.f};
                asm volatile("" : "+v"(m2));
#pragma unroll
                for (int j = 0; j < 16; j += 2) {
                  const f32x2 d0 = sv[j] - m2, d1 = sv[j + 1] - m2;
                  q0 = __builtin_elementwise_fma(d0, d0, q0); q1 = __builtin_elementwise_fma(d1, d1, q1);
                }
                const float rd = ln_rd(sum32(hadd(q0 + q1)) * (1.0f / 1024.0f));
                if ((l & 30) == 0) st4(__builtin_bit_cast(unsigned, (l & 1) ? -rbf1(m) : rd), rs, (l >> 5) * 4 + (l & 1) * 8192, OFF_AB + (p * 256 + h * 8 + 2 * w) * 4);
              }
            }
            if constexpr (s == POI && n == 4) {
              if (w == 3) {
                unsigned pz1 = POISON;
                asm volatile("" : "+v"(pz1));
                const unsigned prb = 2 * p + (h >> 4), pct = h & 15, m = l - 32;
                static_assert(OFF_CB == OFF_CA + 131072, "poison addressing");
                if (l < 60) st16(u32x4{pz1, pz1, pz1, pz1}, rs, l < 32 ? OFF_CA + (l & 8) * 4096 + (l & 16) * 8192 + prb * 2048 + pct * 128 + (l & 7) * 16
                                                             : l < 56 ? OFF_FX + ((h * 6 + (m >> 2)) * 8 + p) * 64 + (l & 3) * 16
                                                                      : OFF_AB + 16384 + (p * 32 + h) * 32 + (l & 1) * 16 + (l & 2) * 4096, 0);
                if (l < 4) st4(pz1, rs, l < 2 ? OFF_XNEW + bid * 8 + l * 4 : OFF_KEY + ((l & 1) * 16 + pct) * 128 + prb * 4, 0);
              }
            }
            if constexpr (s == POI && n == 2) {
              if (w == 2 && l < 6) {
                unsigned f0 = F_KV + KVF(0, p * 32 + h), f1 = F_O + OFB(0, p) + OFL(h), f2 = F_X1 + ((rb >> 1) * RX * 32 + (l & 3) * 32 + (rb & 1) * 16 + ct) * 8;
                asm volatile("" : "+v"(f0), "+v"(f1), "+v"(f2));
                const unsigned fo = l == 5 ? f0 : l == 4 ? f1 : f2;
                touchb(tdum, rs, fo);
              }
            }
            static_assert(vm_x(EIN + XL) >= vm_at(0, EIN - 3, 5) && IMG >= 1 && CSD >= 1, "first flag");
            if constexpr (s == EIN && n == 1) { if (w < 4) count_in(1); }
            if constexpr (s == EIN + 2 && n == 0) qc = __hip_atomic_load(qcnt + 1, __ATOMIC_RELAXED, __HIP_MEMORY_SCOPE_WORKGROUP);
            if constexpr (s == EIN + 2 && n == 2) {
              if (w == 0) { count_wait(1, 4); if (l < RX) st8(T, rs, F_XN + (p * RX + l) * 256 + h * 8, 0); }
            }
            if constexpr (s == ERQ && n == 4) asm volatile("buffer_load_dwordx2 %0, %1, %2, 0 offen sc1" : "=v"(cxn) : "v"(XF_OFF() + F_XN), "s"(rs) : "memory");
            if constexpr (s == CSL && n == 2) {
              wait_vm<SINCE(ERQ)>();
              asm volatile("" : "+v"(cxn)::"memory");
              if (!__all(cxn[0] == T[0] && cxn[1] == T[1])) poll_flags(rs, XF_OFF(), F_XN, T);
            }
          } else {
#define X1_OFF() (F_X1 + ((p * RX + (xcd >> 1) % RX) * 32 + (l & 31)) * 8)
            if constexpr ((s == 0 || s == 4) && n == 2) asm volatile("buffer_load_dwordx2 %0, %1, %2, 0 offen sc1" : "=v"(cxn) : "v"(X1_OFF()), "s"(rs) : "memory");
            if constexpr ((s == 3 || s == 7) && n == 0) {
              wait_vm<vm_at(1, s, 1) - vm_at(1, s - 3, 3)>();
              asm volatile("" : "+v"(cxn)::"memory");
              xok |= __builtin_amdgcn_ballot_w64(cxn[0] == T[0] && cxn[1] == T[1]);
            }
            if constexpr (a_req(s, n) >= 0 && (a_req(s, n) & 7) == 0) x1_need(0x30003ull << (((rot + a_req(s, n) / 8) & 7) * 2));
          }
          if constexpr (IT && a_req(s, n) >= 0) A_LOAD1(a_req(s, n) / 2, a_req(s, n) & 1);
          if constexpr (IT == 0 && s == CSD && n == 4) {
            if (w < 3) {
              unsigned a0 = FRAG_T() + SLOT(s >> 2) * STAGE + w * 8192, a1 = a0 ^ 32, one2 = 0x3F803F80u;
              asm volatile("" : "+v"(a0), "+v"(a1), "+v"(one2));
              const u32x4 o4 = {one2, one2, one2, one2};
              f32x4 y0 = {0.f, 0.f, 0.f, 0.f}, y1 = y0;
#pragma unroll
              for (int i = 0; i < 4; ++i) { y0 = mfma16(frag_t2(a0 + i * 2048), o4, y0); y1 = mfma16(frag_t2(a1 + i * 2048), o4, y1); }
              if ((l & 14) == 0) st16(__builtin_bit_cast(u32x4, (l & 1) ? y1 : y0), rs, g * 16 + (l & 1) * 512, OFF_CS + ((h * 6 + 2 * w) * 8 + p) * 64);
            }
          }
          if constexpr (!IT && s == CSL && n == 4) { asm volatile("" : "=v"(fx[0])); if (w < 3) ld16h<0>(fx[0], rs, l * 16, OFF_CS + h * 3072 + w * 1024); }
          if constexpr (!IT && s == CSS && n == 2) {
            if (w < 3) {
              wait_vm<SINCE(CSL)>();
              asm volatile("" : "+v"(fx[0])::"memory");
              tile_sum(fx[0], L_CSL);
            }
          }
          if constexpr (s == ABL && n == 4) ld4h<0>(aq[0], rs, (l & 31) * 4 + (l >> 5) * 8192, AB_OFF());
          if constexpr (s == ABS && n == 0) {
            wait_vm<SINCE(ABL)>();
            asm volatile("" : "+v"(aq[0])::"memory");
            if constexpr (IT) {
              for (unsigned spins = 0; !__all(aq[0] != POISON) && !spin_fail(spins, rs, T);) {
                ld4h<0>(aq[0], rs, (l & 31) * 4 + (l >> 5) * 8192, AB_OFF());
                asm volatile("s_waitcnt vmcnt(0)" : "+v"(aq[0])::"memory");
              }
            }
            *(LDS_AS unsigned*)(smem + L_K + (8 + rt) * 2048 + l * 4) = aq[0];
          }
          if constexpr (s == ABS && n == 2) {
#pragma unroll
            for (int a = 0; a < 2; ++a) {
              const LDS_AS char* ab = (const LDS_AS char*)(smem + L_K + (8 + rt) * 2048 + a * 64);
              aq[a] = *(const LDS_AS unsigned*)(ab + (l & 15) * 4); bq[a] = *(const LDS_AS unsigned*)(ab + 128 + (l & 15) * 4);
              av[a] = *(const LDS_AS u32x4*)(ab + g * 16); bv[a] = *(const LDS_AS u32x4*)(ab + 128 + g * 16);
            }
          }
          if constexpr (s == ABS && n == 5) {
#pragma unroll
            for (int a = 0; a < 2; ++a) { b2[a] = f32x2{asf(bq[a]), asf(bq[a])}; asm volatile("" : "+v"(b2[a])); }
          }
          if constexpr (s == ABS + 1) {
            constexpr int k = (n + 4) % 6;
            if constexpr (k < 4) cs4[k & 1] = *(const LDS_AS f32x4*)(smem + L_CSL + k * 64 + g * 16);
            else cs4[k & 1][0] = *(const LDS_AS float*)(smem + L_CSL + k * 64 + (l & 15) * 4);
          }
          if constexpr ((s == ABS + 1 && n > 0) || (s == ABS + 2 && n == 0)) {
            constexpr int k = (n + 3) % 6;
            if constexpr (k < 4) {
#pragma unroll
              for (int a = 0; a < 2; ++a) acc[a][k] = __builtin_elementwise_fma(cs4[k & 1], f32x4{b2[a][0], b2[a][1], b2[a][0], b2[a][1]}, acc[a][k]);
            } else {
              f32x2 c2 = {cs4[k & 1][0], cs4[k & 1][0]};
              asm volatile("" : "+v"(c2));
#pragma unroll
              for (int a = 0; a < 2; ++a) acc[a][k] = __builtin_elementwise_fma(f32x4{c2[0], c2[1], c2[0], c2[1]}, __builtin_bit_cast(f32x4, bv[a]), acc[a][k]);
            }
          }
          if constexpr (IT == 0) {
            if constexpr (s == IMG - 2 && n < 3) {
              const int c = 3 * (w & 1) + n;
              ka[n] = (FRAG_T() ^ ((c & 1) << 5)) + (SLOT(5) * STAGE + (c >> 1) * 8192 + (w >> 1) * 2048);
              asm volatile("" : "+v"(ka[n]));
            }
            if constexpr (s == IMG - 1 && (n & 1)) keep[n >> 1] = frag_t2(ka[n >> 1]);
            if constexpr (s > KP0 && s < KP0 + 3 && (n & 1)) keep[3 * (s - KP0) + (n >> 1)] = frag_t2(ka[n >> 1] + (s - KP0) * STAGE);
            if constexpr (s >= IMG && s < IMG + 3 && n == 4) st16(keep[s - IMG], rs, l * 16 + (s - IMG) * 1024, OFF_WI + (h * 8 + ((rot + 5) & 7)) * STAGE + w * 3072);
            if constexpr (s >= TS0 && s < TS0 + TSN && n == 4) {
              if (tstep == s && w < TW) touch(tdum, tsrc);
            }
            if constexpr (s == TST && n == 4) { if (w >= TW) touch(tdum, tsrc); }
          } else {
            if constexpr (s == PB1 && n == 0) { POISON_BIG(1); }
            if constexpr (s == FKL && n == 4) ld4h<0>(kv, rs, (l & 15) * 4, OFF_KEY + (bid & 15) * 128);
            if constexpr (s == FSL && n == 4) {
              wait_vm<SINCE(FKL)>();
              asm volatile("" : "+v"(kv)::"memory");
              if (__builtin_expect(!__all(kv != POISON), 0)) {
                LDS_AS unsigned* kw = (LDS_AS unsigned*)(smem + L_SKEY + 40);
                if (w == 0) {
                  kv = key_wait(rs, T, l, 0, bid & 15, kv);
                } else {
                  while ((kv = __hip_atomic_load(kw, __ATOMIC_RELAXED, __HIP_MEMORY_SCOPE_WORKGROUP)) == POISON) __builtin_amdgcn_s_sleep(2);
                }
              }
              fkey = __builtin_amdgcn_readfirstlane(umax16(kv));
              if (w == 0) *(LDS_AS unsigned*)(smem + L_SKEY + 40) = min(fkey, POISON - 1);
              sel0 = key_row(fkey);
              aff = (int)(sel0 >> 8) == p;
              row_load(rs, g, ffn_xoff(fkey, 0, w), fx);
            }
            if constexpr (s == FS0 && n == FSG) {
              wait_vm<SINCE(FSL)>();
              PINV4(fx);
              asm volatile("" : "=v"(sa), "=v"(sb));
              if (ffn_on) ffn_gu(gv, uv, fx, sa, sb);
            }
            if constexpr (s == FS0 && n == FSG + 1) {
              if (ffn_on) {
                ffn_gu_check(rs, T, ffn_xoff(fkey, 0, w), l, gv, uv, fx, sa, sb);
                ffn_gu_store(smem, l, w, sa, sb);
                count_in(1);
              }
            }
            if constexpr (s == FS0D && n == FDG) {
              if (ffn_on && w < 4) {
                s16x4 da[8], db[8];
                ffn_d_read(smem, l, 2 * w, da);
                while (__hip_atomic_load(qcnt + 1, __ATOMIC_RELAXED, __HIP_MEMORY_SCOPE_WORKGROUP) < 8u) {}
                asm volatile("" ::: "memory");
                const f32x4 t = ffn_t_read(smem, l);
                ffn_d_read(smem, l, 2 * w + 1, db);
                u32x2 ha = ffn_h(t, l), hb = {dppu<0x118>(ha[0]), dppu<0x118>(ha[1])};
                f32x4 ya = {0.f, 0.f, 0.f, 0.f}, yb = ya;
#pragma unroll
                for (int i = 0; i < 8; ++i) {
                  ya = mfma16k16(da[i], __builtin_bit_cast(s16x4, ha), ya); yb = mfma16k16(db[i], __builtin_bit_cast(s16x4, hb), yb);
                  ha = u32x2{dppu<0x111>(ha[0]), dppu<0x111>(ha[1])}; hb = u32x2{dppu<0x111>(hb[0]), dppu<0x111>(hb[1])};
                }
                ffn_d_store(rs, l, 8 * w, 0, slice, ya + yb);
              }
            }
#define RED_LOAD(ld, l8)                                                                                                    \
  {                                                                                                                         \
    const unsigned G = bi, po = OFF_PART + G * 32768;                                                        \
    ld(gv[0], rs, tid * 16, po); ld(gv[1], rs, tid * 16, po + 4096); ld(gv[2], rs, tid * 16, po + 8192); ld(gv[3], rs, tid * 16, po + 12288); \
    ld(uv[0], rs, tid * 16, po + 16384); ld(uv[1], rs, tid * 16, po + 20480); ld(uv[2], rs, tid * 16, po + 24576);        \
    l8(cxn, rs, (l & 3) * 16 + (l & 4) * 2, OFF_CA + (sel0 >> 7) * 2048 + G * 64);                                         \
    l8(cx1, rs, (l & 3) * 16 + (l & 4) * 2, OFF_CB + (sel0 >> 7) * 2048 + G * 64);                                          \
  }
            if constexpr (s == FS1 && n == 4) {
              asm volatile("" : "=v"(cxn), "=v"(cx1));
              if (!ffn_on && w < 4) RED_LOAD(ld16u, ld8h);
            }
            if constexpr (s == FS1S && n == FRG) {
              if (!ffn_on && w < 4) {
                wait_vm<SINCE(FS1)>();
                PINV4(gv); PINV4(uv);
                asm volatile("" : "+v"(cxn), "+v"(cx1)::"memory");
                for (unsigned spins = 0; !__all(max(max(max(umax4(gv[0]), umax4(gv[1])), max(umax4(gv[2]), umax4(gv[3]))),
                                                    max(max(umax4(uv[0]), umax4(uv[1])), max(max(umax4(uv[2]), max(cxn[0], cxn[1])), max(cx1[0], cx1[1])))) != POISON) && !spin_fail(spins, rs, T);) {
                  RED_LOAD(ld16u, ld8h);
                  DRAIN();
                  PINV4(gv); PINV4(uv);
                  asm volatile("" : "+v"(cxn), "+v"(cx1)::"memory");
                }
#define F4(x) __builtin_bit_cast(f32x4, x)
                ((LDS_AS f32x4*)(smem + L_SFIN))[tid] = ((F4(gv[0]) + F4(gv[1])) + (F4(gv[2]) + F4(gv[3]))) + ((F4(uv[0]) + F4(uv[1])) + F4(uv[2]));
#undef F4
                count_in(2);
                if (w == 0) {
                  while (__hip_atomic_load(qcnt + 2, __ATOMIC_RELAXED, __HIP_MEMORY_SCOPE_WORKGROUP) < 4u) {}
                  asm volatile("" ::: "memory");
                  const u32x2 ov = red_final<4>(smem, l, fkey, cxn, cx1);
                  if (l < 8) st8(unpoison2(ov), rs, OFF_XNEW + (bi * 8 + (l & 3) * 2 + (l >> 2)) * 8, 0);
                }
              }
            }
            if constexpr (s == FXA && n == 4) { DEF4(fx); if (w < 3) row_load(rs, g, XP_OFF(), fx); }
            if constexpr (s == FXB && n == FXG) {
              if (w < 3) {
                const unsigned fa = bo[0][0] + SLOT(FXT) * STAGE + w * 2048;
                u32x4 f[8];
#pragma unroll
                for (int i = 0; i < 8; ++i) f[i] = *(const LDS_AS u32x4*)(size_t)(fa + (i >> 1) * 6144 + (i & 1) * 1024);
                wait_vm<SINCE(FXA)>();
                PINV4(fx);
                ffn_xwait<XSL>(rs, T, XP_OFF(), l, fx);
                f32x4 y0 = mfma16(f[0], fx[0], f32x4{0.f, 0.f, 0.f, 0.f}), y1 = mfma16(f[1], fx[0], f32x4{0.f, 0.f, 0.f, 0.f});
#pragma unroll
                for (int i = 1; i < 4; ++i) { y0 = mfma16(f[2 * i], fx[i], y0); y1 = mfma16(f[2 * i + 1], fx[i], y1); }
                if ((l & 14) == 0) st16(unpoison4(__builtin_bit_cast(u32x4, (l & 1) ? y1 : y0)), rs, g * 16 + (l & 1) * 512, OFF_FX + ((h * 6 + 2 * w) * 8 + p) * 64);
              }
            }
            if constexpr (s == FXL && n == 4) {
              asm volatile("" : "=v"(fx[0]), "=v"(fx[1]));
              if (aff && w < 4) { ld16h<0>(fx[0], rs, l * 16, FP_OFF(0)); ld16h<0>(fx[1], rs, l * 16, FP_OFF(1)); }
            }
            if constexpr (s == FXS && n == 2) {
              if (aff && w < 4) {
                wait_vm<0>();
                asm volatile("" : "+v"(fx[0]), "+v"(fx[1])::"memory");
                if (!FX_OK(2)) {
                  unsigned spins = 0;
                  do {
                    if (spin_fail(spins, rs, T)) break;
                    fx[0] = ld16(rs, l * 16, FP_OFF(0)); fx[1] = ld16(rs, l * 16, FP_OFF(1));
                  } while (!FX_OK(2));
                }
                if (w == 3) {
                  f32x2 nx[8];
                  unpack8v(fx[0], nx); unpack8v(fx[1], nx + 4);
                  const f32x2 sm = ((nx[0] + nx[1]) + (nx[2] + nx[3])) + ((nx[4] + nx[5]) + (nx[6] + nx[7]));
                  const float mean = sum64(hadd(sm)) * (1.0f / 1024.0f);
                  const f32x2 m2 = {mean, mean};
                  f32x2 q = {0.f, 0.f};
#pragma unroll
                  for (int j = 0; j < 8; ++j) { const f32x2 d = nx[j] - m2; q = __builtin_elementwise_fma(d, d, q); }
                  const float rd = ln_rd(sum64(hadd(q)) * (1.0f / 1024.0f));
                  if (l == 0) *(LDS_AS f32x2*)(smem + L_FXL + 384) = f32x2{rd, -rbf1(mean)};
                } else {
                  tile_sum(fx[0], L_FXL);
                }
                count_in(3);
              }
            }
          }
        };
        static_for<32>([&](auto sc_) __attribute__((always_inline)) {
          constexpr int s = decltype(sc_)::value;
          if constexpr (s > 0) PIN_ACC();
          if constexpr (IT == 1) wait_vm<vm_at(IT, s, 0) - vm_a(IT, s)>();
          pin_w<s % WA>(af[s % WA][0], af[s % WA][1]);
          SB();
          static_for<6>([&](auto nc_) __attribute__((always_inline)) {
            constexpr int n = decltype(nc_)::value;
            if constexpr (s == 0) { acc[0][n] = f32x4{0.f, 0.f, 0.f, 0.f}; acc[1][n] = acc[0][n]; }
            constexpr bool on = s < QL || n >= 2;
            if constexpr (on) acc[0][n] = n < 4 ? mfma16(bfr[n].v, af[s % WA][0], acc[0][n]) : mfma16(af[s % WA][0], bfr[n].v, acc[0][n]);
            SB();
            if constexpr (n == 0) { QKV_RDB(s, 5); }
            else if constexpr (s + 1 < 32 && (s + 1 < QL || n > 2)) QKV_RDB(s + 1, n - 1);
            SB();
            if constexpr (on) acc[1][n] = n < 4 ? mfma16(bfr[n].v, af[s % WA][1], acc[1][n]) : mfma16(af[s % WA][1], bfr[n].v, acc[1][n]);
            SB();
            if constexpr (s + 1 < 32 && (s + 1 < QL || n >= 2)) QKV_RDA(s + 1, n);
            SB();
            gap(sc_, nc_);
            SB();
          });
        });
        PIN_ACC();
        frag_t qw[32 - QL][2];
        static_for<32 - QL>([&](auto c_) __attribute__((always_inline)) {
          constexpr int s = QL + decltype(c_)::value;
          QKV_RDA(s, 0); QKV_RDB(s, 0); QKV_RDA(s, 1); QKV_RDB(s, 1);
          qw[s - QL][0] = bfr[0]; qw[s - QL][1] = bfr[1];
        });
        SB();
        const bool own = aff && (int)((sel0 >> 5) & 7) == rt;
        const bool m0 = !(sel0 & 16), mq = (l & 15) == (int)(sel0 & 15), mv = g == (int)((sel0 & 15) >> 2);
        auto patch_qk = [&](int n) __attribute__((always_inline)) {
          f32x4 t = *(const LDS_AS f32x4*)(smem + L_FXL + n * 64 + g * 16);
          const f32x4 c = *(const LDS_AS f32x4*)(smem + L_CSL + n * 64 + g * 16);
          const float nb = *(const LDS_AS float*)(smem + L_FXL + 388);
#pragma unroll
          for (int e = 0; e < 4; ++e) { t[e] = __builtin_fmaf(c[e], nb, t[e]); asm volatile("" : "+v"(t[e])); }
          acc[0][n] = (mq && m0) ? t : acc[0][n];
          acc[1][n] = (mq && !m0) ? t : acc[1][n];
        };
        if (own) {
          while (__hip_atomic_load(qcnt + 3, __ATOMIC_RELAXED, __HIP_MEMORY_SCOPE_WORKGROUP) < 4u) {}
          asm volatile("" ::: "memory");
          patch_qk(2); patch_qk(3);
          const f32x2 nab = *(const LDS_AS f32x2*)(smem + L_FXL + 384);
          const LDS_AS float* fl_ = (const LDS_AS float*)(smem + L_FXL + 256) + (l & 15);
          const LDS_AS float* cl_ = (const LDS_AS float*)(smem + L_CSL + 256) + (l & 15);
          const float t4 = __builtin_fmaf(cl_[0], nab[1], fl_[0]), t5 = __builtin_fmaf(cl_[16], nab[1], fl_[16]);
          const unsigned na = __builtin_bit_cast(unsigned, (float)nab[0]);
#pragma unroll
          for (int a = 0; a < 2; ++a) {
            const bool ma = a ? !m0 : m0;
            aq[a] = (mq && ma) ? na : aq[a];
#pragma unroll
            for (int e = 0; e < 4; ++e) {
              const bool me = mv && ma && e == (int)(sel0 & 3);
              acc[a][4][e] = me ? t4 : acc[a][4][e]; acc[a][5][e] = me ? t5 : acc[a][5][e];
              av[a][e] = me ? na : av[a][e];
            }
          }
        }
        constexpr float C2 = 1.4426950408889634f * 0.17677669529663687f;
#pragma unroll
        for (int a = 0; a < 2; ++a) {
          f32x2 a2 = {asf(aq[a]), asf(aq[a])};
          qs2[a] = f32x2{asf(aq[a]) * C2, asf(aq[a]) * C2};
          asm volatile("" : "+v"(a2), "+v"(qs2[a]));
          acc[a][2] *= f32x4{a2[0], a2[1], a2[0], a2[1]}; acc[a][3] *= f32x4{a2[0], a2[1], a2[0], a2[1]};
          acc[a][4] *= __builtin_bit_cast(f32x4, av[a]); acc[a][5] *= __builtin_bit_cast(f32x4, av[a]);
        }
        k0 = pack8(acc[0][2], acc[0][3]); k1 = pack8(acc[1][2], acc[1][3]); v0 = pack8(acc[0][4], acc[1][4]); v1 = pack8(acc[0][5], acc[1][5]);
        const unsigned kvo = OFF_KV + IT * (8u << 20) + (p * 32 + h) * 32768 + rt * 4096;
        if (p < 7) { st16(k0, rs, l * 16, kvo); st16(k1, rs, l * 16 + 1024, kvo); st16(v0, rs, l * 16 + 2048, kvo); st16(v1, rs, l * 16 + 3072, kvo); }
        char* ownp = smem + (8 + rt) * 2048 + l * 16;
        *(u32x4*)(ownp + L_K) = k0;
        if (rt < 7) *(u32x4*)(ownp + L_K + 1024) = k1;
        *(u32x4*)(ownp + L_V) = v0; *(u32x4*)(ownp + L_V + 1024) = v1;
        SB();
        static_for<32 - QL>([&](auto c_) __attribute__((always_inline)) {
          constexpr int s = QL + decltype(c_)::value;
#pragma unroll
          for (int n = 0; n < 2; ++n) { acc[0][n] = mfma16(qw[s - QL][n].v, af[s % WA][0], acc[0][n]); acc[1][n] = mfma16(qw[s - QL][n].v, af[s % WA][1], acc[1][n]); }
        });
        if (own) { patch_qk(0); patch_qk(1); }
      };
      qkv_loop(it_c);
    }
#undef A_LOAD1
#undef QKV_RDA
#undef QKV_RDB
#undef QKV_BO
    {
      IDS();
      const GAS bf16 *pwo, *pwg, *pwu;
      asm volatile("s_load_dwordx2 %0, %3, 24\n\ts_load_dwordx2 %1, %3, 40\n\ts_load_dwordx2 %2, %3, 48" : "=&s"(pwo), "=&s"(pwg), "=&s"(pwu) : "s"(ka));
      const int ql = l & 15;
      const f32x4 z4 = {0.f, 0.f, 0.f, 0.f}, ninf = {-INFINITY, -INFINITY, -INFINITY, -INFINITY};
      LDS_AS unsigned* rdy = (LDS_AS unsigned*)(smem + L_RDY);
      const unsigned rtag = T[0] + it;
      u32x4 qf[2];
      f32x4 cn[2], oa[2][2], sum[2], S[2][2];
      unsigned one2 = 0x3F803F80u;
      asm volatile("" : "+v"(one2));
      const u32x4 ones = {one2, one2, one2, one2};
      auto ex8 = [&](f32x4 a, f32x4 b) __attribute__((always_inline)) {
        return u32x4{pack2(__builtin_amdgcn_exp2f(a[0]), __builtin_amdgcn_exp2f(a[1])), pack2(__builtin_amdgcn_exp2f(a[2]), __builtin_amdgcn_exp2f(a[3])),
                     pack2(__builtin_amdgcn_exp2f(b[0]), __builtin_amdgcn_exp2f(b[1])), pack2(__builtin_amdgcn_exp2f(b[2]), __builtin_amdgcn_exp2f(b[3]))};
      };
      {
#pragma unroll
        for (int a = 0; a < 2; ++a) {
          const f32x4 c4 = {qs2[a][0], qs2[a][1], qs2[a][0], qs2[a][1]};
          qf[a] = pack8(acc[a][0] * c4, acc[a][1] * c4);
        }
        f32x4 mk;
#pragma unroll
        for (int e = 0; e < 4; ++e) mk[e] = (4 * g + e <= ql) ? 0.f : -INFINITY;
        const f32x4 d0 = mfma16(k0, qf[0], mk), d1 = mfma16(k1, qf[1], mk), d2 = mfma16(k0, qf[1], z4);
        const float m0 = -max_x32(max_x16(fmaxf(fmaxf(d0[0], d0[1]), fmaxf(d0[2], d0[3]))));
        const float m1 = -max_x32(max_x16(fmaxf(fmaxf(d1[0], d1[1]), fmaxf(d1[2], d1[3]))));
        cn[0] = f32x4{m0, m0, m0, m0}; cn[1] = f32x4{m1, m1, m1, m1};
        asm volatile("" : "+v"(cn[0]), "+v"(cn[1]));
        const f32x4 e0 = d0 + cn[0];
        const u32x4 p0 = {pack2(__builtin_amdgcn_exp2f(e0[0]), __builtin_amdgcn_exp2f(e0[1])), pack2(__builtin_amdgcn_exp2f(e0[2]), __builtin_amdgcn_exp2f(e0[3])), 0u, 0u};
        const u32x4 p1 = ex8(d2 + cn[1], d1 + cn[1]);
        oa[0][0] = mfma16(v0, p0, z4); oa[0][1] = mfma16(v1, p0, z4); sum[0] = mfma16(ones, p0, z4);
        oa[1][0] = mfma16(v0, p1, z4); oa[1][1] = mfma16(v1, p1, z4); sum[1] = mfma16(ones, p1, z4);
      }
      const char* kb_ = smem + L_K + l * 16;
      const char* vb_ = smem + L_V + l * 16;
      const unsigned wov = (l >> 3) * 2048 + (((((l & 7) >> 1) ^ ((l >> 4) & 3)) * 2 + (l & 1)) << 4), fnv = (l >> 1) * (F_ * 2) + (l & 1) * 16;
      const unsigned pp = __builtin_amdgcn_readfirstlane(p > 0 ? 32 : 0), pvo = OFF_KV + it * (8u << 20) + (p * 32 + h - pp) * 32768;
      const unsigned pfo = __builtin_amdgcn_readfirstlane(F_KV + KVF(it, p * 32 + h - pp));
      asm volatile("" : "+v"(oa[0][0]), "+v"(oa[0][1]), "+v"(oa[1][0]), "+v"(oa[1][1]), "+v"(sum[0]), "+v"(sum[1]));
      DRAIN();
      asm volatile("" : "+v"(tdum));
      if (l == 0) st8(T, rs, rt * 8, F_KV + KVF(it, p * 32 + h));
      BAR();
      asm volatile("" : "+s"(pwo), "+s"(pwg), "+s"(pwu));
      WO_PIECE(15); WO_PIECE(14);
      u32x4 kf[2], vf[2];
      u32x4 p0, p1;
      float e0, e1;
#define EE(v, i) { e0 = __builtin_amdgcn_exp2f(v[i]); e1 = __builtin_amdgcn_exp2f(v[i + 1]); pin2s(e0, e1); SB(); }
#define CV(P, i) { unsigned t_ = pack2(e0, e1); pin1(t_); P[i] = t_; SB(); }
      auto body = [&](unsigned ad, auto dk_) __attribute__((always_inline)) {
        constexpr int dk = decltype(dk_)::value;
        const f32x4 a0 = S[1][0], a1 = S[1][1];
        sum[0] = mfma16(ones, p0, sum[0]); SB();
        EE(a0, 0);
        lds_wait<2>(kf[0], kf[1]);
        S[0][0] = mfma16(kf[0], qf[0], cn[0]); SB();
        CV(p1, 0); EE(a0, 2);
        S[0][1] = mfma16(kf[1], qf[0], cn[0]); SB();
        CV(p1, 1); EE(a1, 0);
        lds_wait<0>(vf[0], vf[1]);
        oa[0][0] = mfma16(vf[0], p0, oa[0][0]); SB();
        CV(p1, 2); EE(a1, 2);
        oa[0][1] = mfma16(vf[1], p0, oa[0][1]); SB();
        CV(p1, 3);
        S[1][0] = mfma16(kf[0], qf[1], cn[1]); SB();
        EE(S[0][0], 0);
        S[1][1] = mfma16(kf[1], qf[1], cn[1]); SB();
        lds2<2048 + dk>(kf[0], kf[1], ad); SB();
        CV(p0, 0); EE(S[0][0], 2);
        oa[1][0] = mfma16(vf[0], p1, oa[1][0]); SB();
        CV(p0, 1); EE(S[0][1], 0);
        oa[1][1] = mfma16(vf[1], p1, oa[1][1]); SB();
        lds2<2048 + L_V>(vf[0], vf[1], ad); SB();
        CV(p0, 2); EE(S[0][1], 2);
        sum[1] = mfma16(ones, p1, sum[1]); SB();
        CV(p0, 3);
      };
      auto last = [&]() __attribute__((always_inline)) {
        sum[0] = mfma16(ones, p0, sum[0]); SB();
        EE(S[1][0], 0);
        lds_wait<0>(vf[0], vf[1]);
        pin2(kf[0], kf[1]);
        oa[0][0] = mfma16(vf[0], p0, oa[0][0]); SB();
        CV(p1, 0); EE(S[1][0], 2);
        oa[0][1] = mfma16(vf[1], p0, oa[0][1]); SB();
        CV(p1, 1); EE(S[1][1], 0);
        const float f0 = __builtin_amdgcn_exp2f(S[1][1][2]), f1 = __builtin_amdgcn_exp2f(S[1][1][3]);
        p1[2] = pack2(e0, e1); p1[3] = pack2(f0, f1);
        oa[1][0] = mfma16(vf[0], p1, oa[1][0]); oa[1][1] = mfma16(vf[1], p1, oa[1][1]); sum[1] = mfma16(ones, p1, sum[1]);
      };
      u32x4 fd = {(unsigned)(size_t)pwu, (unsigned)((size_t)pwu >> 32) & 0xffffu, ffn_on ? 1024u * F_ * 2u : 0u, 0x00020000u};
      const unsigned fx0 = (unsigned)(size_t)pwg ^ (unsigned)(size_t)pwu, fx1 = (unsigned)(((size_t)pwg ^ (size_t)pwu) >> 32) & 0xffffu;
      unsigned fso = (unsigned)((4 * w - 1) * 32 * F_ + slice * 16) * 2, finc = 0, flm = lbase + 65536 + w * 1024 - 8192;
      auto piece = [&]() __attribute__((always_inline)) {
        if constexpr (it == 0) {
          fd[0] ^= fx0; fd[1] ^= fx1; finc ^= 32 * F_ * 2; fso += finc; flm += 8192;
          dma16d(fd, fnv, fso, flm);
        }
      };
      if (rt > 0) {
        unsigned ad = lbase + l * 16 + (6 + rt) * 2048;
        lds2<2048>(kf[0], kf[1], ad); lds2<2048 + L_V>(vf[0], vf[1], ad);
        lds_wait<2>(kf[0], kf[1]);
        S[0][0] = mfma16(kf[0], qf[0], cn[0]); S[0][1] = mfma16(kf[1], qf[0], cn[0]); S[1][0] = mfma16(kf[0], qf[1], cn[1]); S[1][1] = mfma16(kf[1], qf[1], cn[1]);
        SB();
        lds_wait<0>(vf[0], vf[1]);
        lds2<0>(kf[0], kf[1], ad);
        p0 = ex8(S[0][0], S[0][1]);
        lds_wait<0>(kf[0], kf[1]);
#pragma unroll 1
        for (int i = rt - 1; i > 0; --i) {
          ad -= 2048;
          body(ad, std::integral_constant<int, -2048>{});
          piece();
          SB();
        }
        piece();
        last();
      }
      {
        constexpr int FW = 4, FN = 8 / FW;
        if (w < FW) {
          const int g0 = FN * rt;
          poll_flags(rs, (l & (FN - 1)) * 8, pfo + g0 * 8, T);
#pragma unroll
          for (int j = 0; j < FN; ++j) {
            const unsigned d = lbase + (g0 + j) * 2048, o = pvo + (g0 + j) * 4096;
            dma16s<0>(rs, l * 16, o, d + L_K); dma16s<1024>(rs, l * 16, o, d + L_K);
            dma16s<0>(rs, l * 16, o + 2048, d + L_V); dma16s<1024>(rs, l * 16, o + 2048, d + L_V);
          }
          if (it == 1 && l == 0) rdy[8 + w] = rtag;
        } else if (it == 1 && w < 6) {
          while (!__all(__hip_atomic_load(rdy + 8 + (l & (FW - 1)), __ATOMIC_RELAXED, __HIP_MEMORY_SCOPE_WORKGROUP) == rtag)) {}
#pragma unroll 1
          for (int j = w == 4 ? 13 : 5; j > (w == 4 ? 5 : 3); --j) {
#pragma unroll
            for (int v = 0; v < 8; ++v) WO_PIECE_OF(v, j);
          }
        }
        if (p == 0) { cn[0] = ninf; cn[1] = ninf; }
        f32x4 mk;
#pragma unroll
        for (int e = 0; e < 4; ++e) mk[e] = (4 * g + e > ql) ? 0.f : -INFINITY;
        const f32x4 cm0 = cn[0] + mk, cm1 = cn[1] + mk;
        if (w < FW) {
          if constexpr (FN > 1) { wait_vm<2 * FN>(); if (l < FN / 2) rdy[FN * rt + l] = rtag; }
          DRAIN();
          if (l < FN) rdy[FN * rt + l] = rtag;
        }
        if constexpr (it == 0) { WO_PIECE(13); WO_PIECE(12); piece(); }

        int have = rt - 1;
        auto need = [&](int gq) __attribute__((always_inline)) {
          if (__builtin_expect(gq > have, 0)) {
            do {
              const unsigned t = __hip_atomic_load(rdy + (l & 7), __ATOMIC_RELAXED, __HIP_MEMORY_SCOPE_WORKGROUP);
              have = rt - 1 + __builtin_ctz(~(((unsigned)__builtin_amdgcn_ballot_w64(t == rtag) & 0xffu) | 0x100u) >> rt);
            } while (gq > have);
            asm volatile("" ::: "memory");
          }
        };
        unsigned ad = lbase + l * 16 + rt * 2048;
        need(rt);
        lds2<0>(kf[0], kf[1], ad); lds2<L_V>(vf[0], vf[1], ad);
        lds_wait<2>(kf[0], kf[1]);
        S[0][0] = mfma16(kf[0], qf[0], cm0); S[0][1] = mfma16(kf[1], qf[0], cn[0]); S[1][0] = ninf; S[1][1] = mfma16(kf[1], qf[1], cm1);
        SB();
        lds_wait<0>(vf[0], vf[1]);
        need(rt + 1);
        lds2<2048>(kf[0], kf[1], ad);
        p0 = ex8(S[0][0], S[0][1]);
        lds_wait<0>(kf[0], kf[1]);
#pragma unroll 1
        for (int gq = rt + 1; gq < 8; ++gq) {
          need(gq + 1);
          body(ad, std::integral_constant<int, 2048>{});
          piece();
          SB();
          ad += 2048;
        }
        last();
      }
      {
        float big = 0.f;
#pragma unroll
        for (int a = 0; a < 2; ++a)
#pragma unroll
          for (int d = 0; d < 2; ++d)
            big = __builtin_elementwise_maximum(__builtin_elementwise_maximum(big, __builtin_elementwise_maximum(fabsf(oa[a][d][0]), fabsf(oa[a][d][1]))),
                                                __builtin_elementwise_maximum(fabsf(oa[a][d][2]), fabsf(oa[a][d][3])));
        bool ok = sum[0][0] >= 0.5f && sum[0][0] < 0x1p64f && sum[1][0] >= 0.5f && sum[1][0] < 0x1p64f && big < INFINITY;
        if (!__all(ok)) {
#pragma unroll
          for (int a = 0; a < 2; ++a) {
            const int qs = 2 * rt + a, qq = 256 + 16 * qs + ql, klo = max(qq - 255, p > 0 ? 0 : 256);
            auto score = [&](int t) __attribute__((always_inline)) {
              f32x4 s = mfma16(t == 31 ? k1 : *(const u32x4*)(kb_ + t * 1024), qf[a], z4);
#pragma unroll
              for (int e = 0; e < 4; ++e) { const int kk = 16 * t + 4 * g + e; s[e] = (kk <= qq && kk >= klo) ? s[e] : -INFINITY; }
              return s;
            };
            float mx = -INFINITY;
#pragma unroll 1
            for (int t = qs; t <= qs + 16; ++t) { const f32x4 s = score(t); mx = fmaxf(fmaxf(mx, fmaxf(s[0], s[1])), fmaxf(s[2], s[3])); }
            mx = max_x32(max_x16(mx));
            oa[a][0] = z4; oa[a][1] = z4; sum[a] = z4;
#pragma unroll 1
            for (int G = qs >> 1; G <= (qs + 16) >> 1; ++G) {
              const f32x4 s0 = score(2 * G), s1 = score(2 * G + 1);
              f32x4 e0, e1;
#pragma unroll
              for (int e = 0; e < 4; ++e) { e0[e] = __builtin_amdgcn_exp2f(s0[e] - mx); e1[e] = __builtin_amdgcn_exp2f(s1[e] - mx); }
              const u32x4 pf = pack8(e0, e1);
              oa[a][0] = mfma16(*(const u32x4*)(vb_ + G * 2048), pf, oa[a][0]);
              oa[a][1] = mfma16(*(const u32x4*)(vb_ + G * 2048 + 1024), pf, oa[a][1]);
              sum[a] = mfma16(ones, pf, sum[a]);
            }
          }
        }
      }
#pragma unroll
      for (int a = 0; a < 2; ++a) {
        const float inv = __builtin_amdgcn_rcpf(sum[a][0]);
        oa[a][0] *= inv; oa[a][1] *= inv;
      }
      const unsigned oo = OFF_O + it * (4u << 20) + ((p * 8 + rt) * 64 + h * 2) * 1024;
      st16(pack8(oa[0][0], oa[0][1]), rs, l * 16, oo);
      st16(pack8(oa[1][0], oa[1][1]), rs, l * 16 + 1024, oo);
      DRAIN();
      BAR();
      if (tid == 0) st8(T, rs, OFL(h), F_O + OFB(it, p));
      if constexpr (it == 0) {
        DEF4(gv); DEF4(uv);
        if (ffn_on) {
          const unsigned ro = 65536 + w * 1024 + (4 * g + ((l & 15) >> 2)) * 32 + (l & 3) * 8;
#pragma unroll
          for (int i = 0; i < 4; ++i) {
            frag_t f;
            f.hf.a = trread(smem, ro + i * 16384); f.hf.b = trread(smem, ro + i * 16384 + 512);
            gv[i] = f.v;
            f.hf.a = trread(smem, ro + i * 16384 + 8192); f.hf.b = trread(smem, ro + i * 16384 + 8704);
            uv[i] = f.v;
          }
        }
        PINV4(gv); PINV4(uv);
        {
          ARGP(px, 0);
          const unsigned xo = ((rb * 4 + (w & 3)) * 32 + (w >> 2) * 16) * 2048 + ct * 128, xw = (l >> 3) * 2048 + (((l & 7) ^ g) << 4);
          dma16(RSRC(px, 2048 * 1024 * 2), xw, xo, lbase + w * 4096); dma16(RSRC(px, 2048 * 1024 * 2), xw ^ 64, xo + 16384, lbase + w * 4096 + 1024);
        }
      }
      WO_PIECE(3); WO_PIECE(2); WO_PIECE(1); WO_PIECE(0);
      if constexpr (it == 0) { WO_PIECE(11); WO_PIECE(10); WO_PIECE(9); WO_PIECE(8); WO_PIECE(7); WO_PIECE(6); WO_PIECE(5); WO_PIECE(4); }
    }
    IDS();
    ARGP(pwr, 4); ARGP(pwd, 7); ARGP(pout, 8);
    const int kh = w >> 2, rt2 = rb * 4 + (w & 3);
    const unsigned R = rt2 * 32 + kh * 16 + (l & 15);
    const unsigned to = (rt2 * 64 + ct * 4 + kh) * 1024 + l * 16;
    const unsigned rmo = ct * 64 + ((g & 1) ? 12 + 4 * g : 4 * g);
    f32x2 v[8], wv[8];
    float wsum;
    {
      u32x4 wa[2], wrp[2], res[2];
#pragma unroll
      for (int j = 0; j < 2; ++j) {
        wrp[j] = *(const GAS u32x4*)(pwr + rmo + 32 * j);
        wa[j] = *(const GAS u32x4*)(pwr + l * 8 + j * 512);
      }
      {
        f32x2 t[8];
        unpack8v(wa[0], t); unpack8v(wa[1], t + 4);
        const f32x2 s = ((t[0] + t[1]) + (t[2] + t[3])) + ((t[4] + t[5]) + (t[6] + t[7]));
        wsum = sum64(hadd(s));
        unpack8v(trade16(wrp[0]), wv); unpack8v(trade16(wrp[1]), wv + 4);
        asm volatile("" : "+v"(wsum), "+v"(wv[0]), "+v"(wv[1]), "+v"(wv[2]), "+v"(wv[3]), "+v"(wv[4]), "+v"(wv[5]), "+v"(wv[6]), "+v"(wv[7]));
      }
      unsigned bt[4];
#pragma unroll
      for (int n = 0; n < 4; ++n) {
        bt[n] = lbase + 32768 + 65536 + kh * 4096 + (4 * g + ((l & 15) >> 2)) * 128 + (l & 3) * 8 + ((n ^ (2 * (g & 1) + ((l & 15) >> 3))) << 5);
        asm volatile("" : "+v"(bt[n]));
      }
      if (w == 0) poll_flags(rs, OFL(l & 31), F_O + OFB(it, rb >> 1), T);
      else if (it == 0 && w < 3) poll_flags2(rs, F_XN + (((w - 1) * 4 + g) * RX + bi % RX) * 256 + (l & 15) * 16, T);
      else if (it == 0 && w == 3) poll_flags(rs, ((l >> 3) * 32 + h) * 128 + (l & 7) * 8, F_KV, T);
      asm volatile("s_barrier" ::: "memory");
      const unsigned abase = OFF_O + it * (4u << 20) + (rt2 * 64 + 32 * kh) * 1024;
      const unsigned av0 = l * 16 + kh * 1024, av1 = l * 16 + (kh ^ 1) * 1024;
      u32x4 af[WB][2];
#define O_LOAD(s_) { ld16h<(wo_k(it, s_) & 1) * 2048>(af[(s_) % WB][0], rs, av0, abase + (wo_k(it, s_) >> 1) * 4096); ld16h<(wo_k(it, s_) & 1) * 2048>(af[(s_) % WB][1], rs, av1, abase + (wo_k(it, s_) >> 1) * 4096); }
      if (w >= 4) __builtin_amdgcn_s_setprio(1);
      static_for<WR>([&](auto c_) __attribute__((always_inline)) { O_LOAD(decltype(c_)::value); });
      if constexpr (it == 1) { ld16h<0>(res[0], rs, g * 16, OFF_XNEW + ct * 128); ld16h<64>(res[1], rs, g * 16, OFF_XNEW + ct * 128); }
      SB();
      f32x4 ac[2][4];
#pragma unroll
      for (int a = 0; a < 2; ++a)
#pragma unroll
        for (int n = 0; n < 4; ++n) ac[a][n] = f32x4{0.f, 0.f, 0.f, 0.f};
      u32x4 bfr[2][4];
#define WO_RD(buf, s, n)                                                                             \
  {                                                                                                  \
    const unsigned o_ = bt[n] + (wo_k(it, s) & 7) * 8192;                                             \
    frag_t f_;                                                                                       \
    f_.hf.a = trabs(o_); f_.hf.b = trabs(o_ + 2048);                                                 \
    bfr[buf][n] = f_.v;                                                                              \
  }
#pragma unroll
      for (int n = 0; n < 4; ++n) WO_RD(0, 0, n);
      static_for<16>([&](auto sc_) __attribute__((always_inline)) {
        constexpr int s = decltype(sc_)::value, cur = s & 1;
        pin_ac(ac);
        if constexpr (s == 7) __builtin_amdgcn_s_setprio(0);
        if constexpr (s == 1) lds_count<0>(lbase + L_QC);
        if constexpr (s == (it == 0 ? 3 : 11)) lds_look(smem + L_QC, 64);
        if constexpr (s < 15 && ((wo_k(it, s) ^ wo_k(it, s + 1)) & 8)) {
#pragma unroll
          for (int n = 0; n < 4; ++n) { bt[n] += (wo_k(it, s) & 8) ? -65536 : 65536; pin1(bt[n]); }
        }
        if constexpr (s < WB - WR) O_LOAD(WR + 2 * s);
        if constexpr (it == 0 && s == 8) lds_count<8>(lbase + L_QC);
        if constexpr (it == 0 && s == WDS) {
          DRAIN();
          if (ffn_on) { lds_look(smem + L_QC + 8, 16); WD_DMA_OF(w); }
        }
        if constexpr (it == 1 || s < WDS) wait_vm<wo_at(it, s) - wo_pos(it, s)>();
        pin2(af[s % WB][0], af[s % WB][1]);
        if constexpr (it == 1 && s == 9) pin2(res[0], res[1]);
        SB();
#pragma unroll
        for (int n = 0; n < 4; ++n) {
          ac[0][n] = mfma16(bfr[cur][n], af[s % WB][0], ac[0][n]);
          ac[1][n] = mfma16(bfr[cur][n], af[s % WB][1], ac[1][n]);
          if (s + 1 < 16) WO_RD(cur ^ 1, s + 1, n);
          if (it == 0 && s == 8) {
            const u32x2 t = *(const LDS_AS u32x2*)(smem + w * 4096 + (l & 15) * 128 + (((2 * n + (g >> 1)) ^ ((l & 15) >> 1)) << 4) + (g & 1) * 8);
            res[n >> 1][2 * (n & 1)] = t[0]; res[n >> 1][2 * (n & 1) + 1] = t[1];
          }
          if (s == 9) {
            if (it == 1 && n == 0) { res[0] = R == sel0 ? res[0] : x2p[0]; res[1] = R == sel0 ? res[1] : x2p[1]; }

            v[2 * n] = f32x2{lo16(res[n >> 1][2 * (n & 1)]), hi16(res[n >> 1][2 * (n & 1)])};
            v[2 * n + 1] = f32x2{lo16(res[n >> 1][2 * (n & 1) + 1]), hi16(res[n >> 1][2 * (n & 1) + 1])};
            pin2f(v[2 * n], v[2 * n + 1]);
          }
          SB();
        }
        if constexpr (s < WB - WR) { O_LOAD(WR + 2 * s + 1); }
        else if constexpr (s + WB < 16) { O_LOAD(s + WB); }
        SB();
      });
#undef WO_RD
#undef O_LOAD
#define XCH_AT(v) ((v) == 7 ? 155648 : (v) * 4096)
#pragma unroll
      for (int n = 0; n < 4; ++n) *(f32x4*)(smem + XCH_AT(w) + n * 1024 + l * 16) = ac[1][n];
#pragma unroll
      for (int n = 0; n < 4; ++n) { v[2 * n] += f32x2{ac[0][n][0], ac[0][n][1]}; v[2 * n + 1] += f32x2{ac[0][n][2], ac[0][n][3]}; }
      BAR();
#pragma unroll
      for (int n = 0; n < 4; ++n) {
        const f32x4 o = *(const f32x4*)(smem + XCH_AT(w ^ 4) + n * 1024 + l * 16);
        v[2 * n] += f32x2{o[0], o[1]}; v[2 * n + 1] += f32x2{o[2], o[3]};
      }
    }
    {
      if (it == 0) { st16(pack8v(v), rs, to, OFF_X1); st16(pack8v(v + 4), rs, to + 2048, OFF_X1); }
      SB();
      const f32x2 s = ((v[0] + v[1]) + (v[2] + v[3])) + ((v[4] + v[5]) + (v[6] + v[7]));
      f32x2 dp = v[0] * wv[0], q = v[0] * v[0];
#pragma unroll
      for (int j = 1; j < 8; ++j) { dp = __builtin_elementwise_fma(v[j], wv[j], dp); q = __builtin_elementwise_fma(v[j], v[j], q); }
      float mt = hadd(s), qq = hadd(q), dot = hadd(dp);
      add_g3(mt, qq, dot);
      const unsigned so = OFF_STAT + it * 524288 + rb * 32768, lrow = R & 127;
      if (g == 0) st16(unpoison4(__builtin_bit_cast(u32x4, f32x4{mt, qq, dot, 0.f})), rs, ct * 2048 + lrow * 16, so);
      SB();
      if (it == 0) wait_vm<1>();
      if constexpr (it == 0) {
        volatile LDS_AS u32x2* in = (volatile LDS_AS u32x2*)(smem + L_XC);
        if (l == 0) in[w] = T;
        const u32x2 t = in[l & 7];
        if (__all(t[0] == T[0] && t[1] == T[1]) && l < RX) st8(T, rs, (l * 32 + (rb & 1) * 16 + ct) * 8, F_X1 + (rb >> 1) * RX * 32 * 8);
      }
      if (it == 1) { if (ffn_on) { WD_DMA_OF(w); } }
      if (it == 0) {
#pragma unroll
        for (int j = 0; j < 9; ++j) *(u32x4*)(smem + L_RING + (j / 3) * STAGE + (w * 3 + j % 3) * 1024 + l * 16) = keep[j];
      }
      __builtin_amdgcn_s_sleep(it == 0 ? SDI0 : SDIR);
      char* const skey = smem + (it == 0 ? L_SKEY : L_XC);
      u32x4 sp[4];
      ld16h<0>(sp[0], rs, g * 8192 + lrow * 16, so); ld16h<2048>(sp[1], rs, g * 8192 + lrow * 16, so);
      ld16h<0>(sp[2], rs, g * 8192 + lrow * 16, so + 4096); ld16h<2048>(sp[3], rs, g * 8192 + lrow * 16, so + 4096);
      u32x2 xf;
      asm volatile("" : "=v"(xf));
      if (it == 0 && w == 0) ld8h(xf, rs, (l & 31) * 8, F_X1 + (p * RX + (xcd >> 1) % RX) * 32 * 8);
      DRAIN();
      asm volatile("" : "+v"(xf)::"memory");
      PINV4(sp);
      const RowFin r = fin_key(rs, T, g * 8192 + lrow * 16, so, skey, l, w, R, wsum, sp);
      if constexpr (it == 0) {
        if (w == 0) {
          const unsigned need = 0x30003u << (((PROT() + 5) & 7) * 2);
          unsigned m = (unsigned)__builtin_amdgcn_ballot_w64(xf[0] == T[0] && xf[1] == T[1]);
          for (unsigned spins = 0; (m & need) != need;) {
            if (spin_fail(spins, rs, T)) { m = ~0u; break; }
            const u32x2 f = ld8(rs, F_X1 + ((p * RX + (xcd >> 1) % RX) * 32 + (l & 31)) * 8, 0);
            m = (unsigned)__builtin_amdgcn_ballot_w64(f[0] == T[0] && f[1] == T[1]);
          }
          if (l == 0) *(LDS_AS u32x4*)(skey + 32) = u32x4{m, 0u, POISON, 0u};
        }
        if (tid == 192) *(LDS_AS u32x4*)(smem + L_QC) = u32x4{0u, 0u, 0u, 0u};
      }
      if (it == 1 && tid == 0) *(LDS_AS unsigned*)(smem + L_XC + 32) = POISON;
      BAR();
      if constexpr (it == 0) {
        PRE_LOAD(1, PROT() + 5, 0, 4);
        xm0 = __builtin_amdgcn_readfirstlane(*(const LDS_AS unsigned*)(skey + 32));
      }
      fin_cand(rs, skey, it, g, rb, ct, r, v);
      fin_x2(r, v);
      if constexpr (it == 0) {
        x2p[0] = pack8v(v); x2p[1] = pack8v(v + 4);
        asm volatile("" : "+v"(x2p[0]), "+v"(x2p[1]));
        if ((w & 3) * 2 + (w >> 2) == ct >> 1) {
          const float f = 1.0f + r.rd, rd2 = ln_rd(r.var * f * f), mb2 = rbf1(__builtin_fmaf(r.mean - r.mb, r.rd, r.mean));
          if (g < 2 && ((l >> 3) & 1) == (ct & 1))
            st4(min(__builtin_bit_cast(unsigned, g == 0 ? f * rd2 : -__builtin_fmaf(r.mb, r.rd, mb2) * __builtin_amdgcn_rcpf(f)), POISON - 1), rs, OFF_AB + 16384 + R * 4 + g * 8192, 0);
        }
      }
    }
    if (it == 1) {
      const u32x4 op0 = trade16(pack8v(v)), op1 = trade16(pack8v(v + 4));
#define OUT_STORE()                                                                                 \
  if (R != key_row(key)) {                                                                          \
    __builtin_amdgcn_raw_buffer_store_b128(op0, rout, (R * 1024 + rmo) * 2, 0, SC1);                  \
    __builtin_amdgcn_raw_buffer_store_b128(op1, rout, (R * 1024 + rmo + 32) * 2, 0, SC1);             \
  }
      unsigned key;
      {
        LDS_AS unsigned* kw = (LDS_AS unsigned*)(smem + L_XC + 32);
        if (w == 0) {
          __builtin_amdgcn_s_sleep(GSL);
          key = key_wait(rs, T, l, 1, bid & 15, key_load(rs, l, 1, bid & 15));
          *kw = min(key, POISON - 1);
        } else {
          unsigned k_;
          while ((k_ = __hip_atomic_load(kw, __ATOMIC_RELAXED, __HIP_MEMORY_SCOPE_WORKGROUP)) == POISON) __builtin_amdgcn_s_sleep(1);
          key = __builtin_amdgcn_readfirstlane(k_);
        }
      }
      if (ffn_on) {
        f32x4 ga, ua;
        u32x4 xv[4];
        s16x4 dfr[8];
        ld4x16(xv, rs, g * 16, ffn_xoff(key, 1, w));
        ffn_d_read(smem, l, w, dfr);
        ffn_gu(gv, uv, xv, ga, ua);
        ffn_gu_check(rs, T, ffn_xoff(key, 1, w), l, gv, uv, xv, ga, ua);
        ffn_gu_store(smem, l, w, ga, ua);
        BAR();
        f32x4 ya = {0.f, 0.f, 0.f, 0.f}, yb = ya;
        u32x2 hh = ffn_h(ffn_t_read(smem, l), l);
        ffn_d_mma(dfr, hh, ya, yb);
        if ((l & 15) < 8) ffn_d_store(rs, l, 4 * w, 1, slice, ya + yb);
        SB();
        __builtin_amdgcn_s_sleep(OSL);
        OUT_STORE();
      } else {
        OUT_STORE();
        const unsigned G = bi;
        if (G == 0 && tid == 0) st8(T, rs, F_EP, 0);
        u32x4 pv[4];
        u32x2 cxn, cx1;
        __builtin_amdgcn_s_sleep(RSL);
        red_load(rs, tid, 1, G, key, pv, cxn, cx1);
        red_sum(rs, smem, T, tid, w, 1, G, key, pv, cxn, cx1);
        BAR();
        if (w == 0) {
          const u32x2 ov = red_final<8>(smem, l, key, cxn, cx1);
          if (l < 8) __builtin_amdgcn_raw_buffer_store_b64(ov, rout, (key_row(key) * 1024 + G * 32 + l * 4) * 2, 0, 0);
        }
      }
#undef OUT_STORE
    }
  };
  layer(std::integral_constant<int, 0>{});
  layer(std::integral_constant<int, 1>{});
}

extern "C" void kernel_launch(void* const* d_in, const int* in_sizes, int n_in, void* d_out, int out_size,
                              void* d_ws, size_t ws_size, hipStream_t stream) {
  (void)hipFuncSetAttribute((const void*)mega_kernel, hipFuncAttributeMaxDynamicSharedMemorySize, LDS_BYTES);
  mega_kernel<<<256, 512, LDS_BYTES, stream>>>((const bf16*)d_in[0], (char*)d_ws, (const bf16*)d_in[1], (const bf16*)d_in[2], (const bf16*)d_in[4],
                                               (const bf16*)d_in[5], (const bf16*)d_in[6], (const bf16*)d_in[7], (bf16*)d_out);
}
```

```cpp
#include <hip/hip_runtime.h>
#include <stdint.h>
#include <utility>

typedef __bf16 bf16;
typedef __bf16 bf16x8 __attribute__((ext_vector_type(8)));
typedef __bf16 bf16x2 __attribute__((ext_vector_type(2)));
typedef float f32x4 __attribute__((ext_vector_type(4)));
typedef float f32x2 __attribute__((ext_vector_type(2)));
typedef short s16x4 __attribute__((ext_vector_type(4)));
typedef unsigned int u32x4 __attribute__((ext_vector_type(4)));
typedef unsigned int u32x2 __attribute__((ext_vector_type(2)));
#define LDS_AS __attribute__((address_space(3)))
#define GAS __attribute__((address_space(1)))
typedef GAS unsigned gu32;
typedef GAS unsigned long long gu64;
#define RLX_AGENT __ATOMIC_RELAXED, __HIP_MEMORY_SCOPE_AGENT
typedef __amdgpu_buffer_rsrc_t rsrc_t;

constexpr int F_ = 3584;
constexpr int SC1 = 16;

constexpr int RX = 4;
constexpr unsigned F_EP = 0, F_TMO = 128;
constexpr unsigned F_XN = 4096;
constexpr unsigned F_O = 8u << 20;
constexpr unsigned F_X1 = F_O + 131072;
constexpr unsigned OFF_KEY = F_O + 393216;
constexpr unsigned F_KV = F_O + 524288;
#define KVF(it, i) ((i) * 512 + (it) * 64)
#define KVG(gp) (((gp) >> 1) * 128 + ((gp) & 1) * 8)
#define OFL(h) (((h) >> 3) * 128 + ((h) & 7) * 8)
#define OFB(it, p) ((p) * 512 + (it) * 64)
constexpr unsigned OFF_AB = 147456;
constexpr unsigned OFF_XNEW = 262144;
constexpr unsigned OFF_CS = 294912;
constexpr unsigned OFF_FX = 393216;
constexpr unsigned OFF_CA = 524288;
constexpr unsigned OFF_CB = 655360;
constexpr unsigned OFF_STAT = 1u << 20;
constexpr unsigned OFF_PART = 3u << 20;
constexpr unsigned OFF_X1 = 12u << 20;
constexpr unsigned OFF_O = 16u << 20;
constexpr unsigned OFF_KV = 32u << 20;
constexpr unsigned OFF_WI = 52u << 20;
constexpr unsigned WS_BYTES = 58u << 20;
constexpr unsigned POISON = 0xFFFFFFFFu;

constexpr int L_CSL = 31872, L_FXL = 32256, L_K = 0, L_V = 32768, L_RING = 65536, L_DN = 32768, L_TOUCH0 = 149504, L_RDY = 31744, L_QC = 31808, LDS_BYTES = 163840;
constexpr int STAGE = 24576;
#define SLOT(t) ((t) & 3)

template <typename F, int... I> __device__ __forceinline__ void static_for_impl(F&& f, std::integer_sequence<int, I...>) {
  (f(std::integral_constant<int, I>{}), ...);
}
template <int N, typename F> __device__ __forceinline__ void static_for(F&& f) { static_for_impl(f, std::make_integer_sequence<int, N>{}); }
__device__ __forceinline__ unsigned pack2(float a, float b) {
  f32x2 v = {a, b};
  return __builtin_bit_cast(unsigned, __builtin_convertvector(v, bf16x2));
}
__device__ __forceinline__ float asf(unsigned u) { return __builtin_bit_cast(float, u); }
__device__ __forceinline__ float lo16(unsigned u) { return __builtin_bit_cast(float, u << 16); }
__device__ __forceinline__ float hi16(unsigned u) { return __builtin_bit_cast(float, u & 0xffff0000u); }
__device__ __forceinline__ u32x4 pack8(const f32x4 a, const f32x4 b) {
  u32x4 o;
  o[0] = pack2(a[0], a[1]); o[1] = pack2(a[2], a[3]); o[2] = pack2(b[0], b[1]); o[3] = pack2(b[2], b[3]);
  return o;
}
__device__ __forceinline__ f32x4 mfma16(u32x4 a, u32x4 b, f32x4 c) {
  return __builtin_amdgcn_mfma_f32_16x16x32_bf16(__builtin_bit_cast(bf16x8, a), __builtin_bit_cast(bf16x8, b), c, 0, 0, 0);
}
__device__ __forceinline__ f32x4 mfma16k16(s16x4 a, s16x4 b, f32x4 c) { return __builtin_amdgcn_mfma_f32_16x16x16bf16_1k(a, b, c, 0, 0, 0); }
template <int CTRL> __device__ __forceinline__ float dppf(float v) {
  return __builtin_bit_cast(float, __builtin_amdgcn_update_dpp(0, __builtin_bit_cast(int, v), CTRL, 0xf, 0xf, true));
}
template <int CTRL> __device__ __forceinline__ unsigned dppu(unsigned v) {
  return (unsigned)__builtin_amdgcn_update_dpp(0, (int)v, CTRL, 0xf, 0xf, true);
}
__device__ __forceinline__ float add_x16(float v) {
  auto r = __builtin_amdgcn_permlane16_swap(__builtin_bit_cast(unsigned, v), __builtin_bit_cast(unsigned, v), false, false);
  return __builtin_bit_cast(float, (unsigned)r[0]) + __builtin_bit_cast(float, (unsigned)r[1]);
}
__device__ __forceinline__ float add_x32(float v) {
  auto r = __builtin_amdgcn_permlane32_swap(__builtin_bit_cast(unsigned, v), __builtin_bit_cast(unsigned, v), false, false);
  return __builtin_bit_cast(float, (unsigned)r[0]) + __builtin_bit_cast(float, (unsigned)r[1]);
}
__device__ __forceinline__ float max_x16(float v) {
  auto r = __builtin_amdgcn_permlane16_swap(__builtin_bit_cast(unsigned, v), __builtin_bit_cast(unsigned, v), false, false);
  return fmaxf(__builtin_bit_cast(float, (unsigned)r[0]), __builtin_bit_cast(float, (unsigned)r[1]));
}
__device__ __forceinline__ float max_x32(float v) {
  auto r = __builtin_amdgcn_permlane32_swap(__builtin_bit_cast(unsigned, v), __builtin_bit_cast(unsigned, v), false, false);
  return fmaxf(__builtin_bit_cast(float, (unsigned)r[0]), __builtin_bit_cast(float, (unsigned)r[1]));
}
__device__ __forceinline__ unsigned umax_x16(unsigned v) {
  auto r = __builtin_amdgcn_permlane16_swap(v, v, false, false);
  return max((unsigned)r[0], (unsigned)r[1]);
}
__device__ __forceinline__ unsigned umax_x32(unsigned v) {
  auto r = __builtin_amdgcn_permlane32_swap(v, v, false, false);
  return max((unsigned)r[0], (unsigned)r[1]);
}
__device__ __forceinline__ float sum_hi8(float v) {
  v += dppf<0x128>(v);
  v = add_x16(v);
  return add_x32(v);
}
__device__ __forceinline__ float sum32(float v) {
  v += dppf<0xB1>(v);
  v += dppf<0x4E>(v);
  v += dppf<0x124>(v);
  v += dppf<0x128>(v);
  return add_x16(v);
}
__device__ __forceinline__ float sum64(float v) {
  v += dppf<0xB1>(v);
  v += dppf<0x4E>(v);
  v += dppf<0x124>(v);
  v += dppf<0x128>(v);
  v = add_x16(v);
  return add_x32(v);
}

#define BAR() asm volatile("s_waitcnt lgkmcnt(0)\n\ts_barrier" ::: "memory")
#define DRAIN() asm volatile("s_waitcnt vmcnt(0)" ::: "memory")
#define SB() __builtin_amdgcn_sched_barrier(0)
template <int N> __device__ __forceinline__ void wait_vm() { asm volatile("s_waitcnt vmcnt(%0)" ::"n"(N) : "memory"); }

__device__ __forceinline__ void dma16(rsrc_t rs, unsigned voff, unsigned soff, unsigned lds) {
  asm volatile("s_mov_b32 m0, %0\n\ts_nop 0\n\tbuffer_load_dwordx4 %1, %2, %3 offen lds" ::"s"(lds), "v"(voff), "s"(rs), "s"(soff) : "memory");
}
__device__ __forceinline__ void dma16d(u32x4 d, unsigned voff, unsigned soff, unsigned lds) {
  asm volatile("s_mov_b32 m0, %0\n\ts_nop 0\n\tbuffer_load_dwordx4 %1, %2, %3 offen lds" ::"s"(lds), "v"(voff), "s"(d), "s"(soff) : "memory");
}
template <int LI, int SI> __device__ __forceinline__ void dma16i(rsrc_t rs, unsigned voff, unsigned sb, unsigned lb) {
  unsigned t;
  asm volatile("s_add_u32 m0, %2, %5\n\ts_add_u32 %0, %1, %6\n\tbuffer_load_dwordx4 %3, %4, %0 offen lds" : "=&s"(t) : "s"(sb), "s"(lb), "v"(voff), "s"(rs), "n"(LI), "n"(SI) : "memory", "scc");
}
template <int LI> __device__ __forceinline__ void dma16r(rsrc_t rs, unsigned voff, unsigned sb, unsigned so, unsigned lb) {
  unsigned t;
  asm volatile("s_add_u32 m0, %2, %5\n\ts_add_u32 %0, %1, %6\n\tbuffer_load_dwordx4 %3, %4, %0 offen lds" : "=&s"(t) : "s"(sb), "s"(lb), "v"(voff), "s"(rs), "n"(LI), "s"(so) : "memory", "scc");
}
template <int LI, int OFF> __device__ __forceinline__ void dma16t(rsrc_t rs, unsigned voff, unsigned sb, unsigned so, unsigned lb) {
  unsigned t;
  asm volatile("s_add_u32 m0, %2, %5\n\ts_add_u32 %0, %1, %6\n\tbuffer_load_dwordx4 %3, %4, %0 offen offset:%7 sc1 lds" : "=&s"(t) : "s"(sb), "s"(lb), "v"(voff), "s"(rs), "n"(LI), "s"(so), "n"(OFF) : "memory", "scc");
}
template <int OFF> __device__ __forceinline__ void dma16s(rsrc_t rs, unsigned voff, unsigned soff, unsigned lds) {
  asm volatile("s_mov_b32 m0, %0\n\ts_nop 0\n\tbuffer_load_dwordx4 %1, %2, %3 offen offset:%4 sc1 lds" ::"s"(lds), "v"(voff), "s"(rs), "s"(soff), "n"(OFF) : "memory");
}
__device__ __forceinline__ void touch(unsigned& d, const GAS bf16* ptr) { asm volatile("global_load_dword %0, %1, off" : "+v"(d) : "v"(ptr) : "memory"); }
__device__ __forceinline__ void ldrows(u32x4 (&d)[4], rsrc_t rx, unsigned voff, unsigned soff) {
  asm volatile("buffer_load_dwordx4 %0, %4, %5, %6 offen\n\tbuffer_load_dwordx4 %1, %4, %5, %6 offen offset:512\n\t"
               "buffer_load_dwordx4 %2, %4, %5, %6 offen offset:1024\n\tbuffer_load_dwordx4 %3, %4, %5, %6 offen offset:1536"
               : "=&v"(d[0]), "=&v"(d[1]), "=&v"(d[2]), "=&v"(d[3]) : "v"(voff), "s"(rx), "s"(soff) : "memory");
}
__device__ __forceinline__ void touchb(unsigned& d, rsrc_t rs, unsigned vo) { asm volatile("buffer_load_dword %0, %1, %2, 0 offen sc1" : "+v"(d) : "v"(vo), "s"(rs) : "memory"); }
__device__ __forceinline__ float rbf1(float x) { return __builtin_bit_cast(float, pack2(0.f, x)); }
__device__ __forceinline__ u32x4 ld16(rsrc_t rs, unsigned voff, unsigned soff) { return __builtin_amdgcn_raw_buffer_load_b128(rs, voff, soff, SC1); }
template <int OFF> __device__ __forceinline__ void ld16h(u32x4& d, rsrc_t rs, unsigned voff, unsigned soff) {
  asm volatile("buffer_load_dwordx4 %0, %1, %2, %3 offen offset:%4 sc1" : "=v"(d) : "v"(voff), "s"(rs), "s"(soff), "n"(OFF) : "memory");
}
template <int I, int OFF> __device__ __forceinline__ void ld16w(u32x4& d, rsrc_t rs, unsigned voff, unsigned soff) {
  if constexpr (I == 0) asm volatile("buffer_load_dwordx4 %0, %1, %2, %3 offen offset:%4 sc1" : "={v[128:131]}"(d) : "v"(voff), "s"(rs), "s"(soff), "n"(OFF) : "memory");
  else if constexpr (I == 1) asm volatile("buffer_load_dwordx4 %0, %1, %2, %3 offen offset:%4 sc1" : "={v[132:135]}"(d) : "v"(voff), "s"(rs), "s"(soff), "n"(OFF) : "memory");
  else if constexpr (I == 2) asm volatile("buffer_load_dwordx4 %0, %1, %2, %3 offen offset:%4 sc1" : "={v[136:139]}"(d) : "v"(voff), "s"(rs), "s"(soff), "n"(OFF) : "memory");
  else if constexpr (I == 3) asm volatile("buffer_load_dwordx4 %0, %1, %2, %3 offen offset:%4 sc1" : "={v[140:143]}"(d) : "v"(voff), "s"(rs), "s"(soff), "n"(OFF) : "memory");
  else if constexpr (I == 4) asm volatile("buffer_load_dwordx4 %0, %1, %2, %3 offen offset:%4 sc1" : "={v[144:147]}"(d) : "v"(voff), "s"(rs), "s"(soff), "n"(OFF) : "memory");
  else if constexpr (I == 5) asm volatile("buffer_load_dwordx4 %0, %1, %2, %3 offen offset:%4 sc1" : "={v[148:151]}"(d) : "v"(voff), "s"(rs), "s"(soff), "n"(OFF) : "memory");
  else if constexpr (I == 6) asm volatile("buffer_load_dwordx4 %0, %1, %2, %3 offen offset:%4 sc1" : "={v[152:155]}"(d) : "v"(voff), "s"(rs), "s"(soff), "n"(OFF) : "memory");
  else if constexpr (I == 7) asm volatile("buffer_load_dwordx4 %0, %1, %2, %3 offen offset:%4 sc1" : "={v[156:159]}"(d) : "v"(voff), "s"(rs), "s"(soff), "n"(OFF) : "memory");
  else if constexpr (I == 8) asm volatile("buffer_load_dwordx4 %0, %1, %2, %3 offen offset:%4 sc1" : "={v[160:163]}"(d) : "v"(voff), "s"(rs), "s"(soff), "n"(OFF) : "memory");
  else if constexpr (I == 9) asm volatile("buffer_load_dwordx4 %0, %1, %2, %3 offen offset:%4 sc1" : "={v[164:167]}"(d) : "v"(voff), "s"(rs), "s"(soff), "n"(OFF) : "memory");
  else if constexpr (I == 10) asm volatile("buffer_load_dwordx4 %0, %1, %2, %3 offen offset:%4 sc1" : "={v[168:171]}"(d) : "v"(voff), "s"(rs), "s"(soff), "n"(OFF) : "memory");
  else if constexpr (I == 11) asm volatile("buffer_load_dwordx4 %0, %1, %2, %3 offen offset:%4 sc1" : "={v[172:175]}"(d) : "v"(voff), "s"(rs), "s"(soff), "n"(OFF) : "memory");
}
template <int K> __device__ __forceinline__ void pin_w(u32x4& a, u32x4& b) {
  if constexpr (K == 0) asm volatile("" : "+{v[128:131]}"(a), "+{v[132:135]}"(b)::"memory");
  else if constexpr (K == 1) asm volatile("" : "+{v[136:139]}"(a), "+{v[140:143]}"(b)::"memory");
  else if constexpr (K == 2) asm volatile("" : "+{v[144:147]}"(a), "+{v[148:151]}"(b)::"memory");
  else if constexpr (K == 3) asm volatile("" : "+{v[152:155]}"(a), "+{v[156:159]}"(b)::"memory");
  else if constexpr (K == 4) asm volatile("" : "+{v[160:163]}"(a), "+{v[164:167]}"(b)::"memory");
  else if constexpr (K == 5) asm volatile("" : "+{v[168:171]}"(a), "+{v[172:175]}"(b)::"memory");
}
__device__ __forceinline__ void ld8h(u32x2& d, rsrc_t rs, unsigned voff, unsigned soff) {
  asm volatile("buffer_load_dwordx2 %0, %1, %2, %3 offen sc1" : "=v"(d) : "v"(voff), "s"(rs), "s"(soff) : "memory");
}
__device__ __forceinline__ void ld16u(u32x4& d, rsrc_t rs, unsigned voff, unsigned soff) {
  asm volatile("buffer_load_dwordx4 %0, %1, %2, %3 offen sc1" : "+v"(d) : "v"(voff), "s"(rs), "s"(soff) : "memory");
}
template <int OFF> __device__ __forceinline__ void ld4h(unsigned& d, rsrc_t rs, unsigned voff, unsigned soff) {
  asm volatile("buffer_load_dword %0, %1, %2, %3 offen offset:%4 sc1" : "=v"(d) : "v"(voff), "s"(rs), "s"(soff), "n"(OFF) : "memory");
}
__device__ __forceinline__ void ld4x16(u32x4 (&d)[4], rsrc_t rs, unsigned voff, unsigned soff) {
  asm volatile("buffer_load_dwordx4 %0, %4, %5, %6 offen sc1\n\tbuffer_load_dwordx4 %1, %4, %5, %6 offen offset:64 sc1\n\t"
               "buffer_load_dwordx4 %2, %4, %5, %6 offen offset:128 sc1\n\tbuffer_load_dwordx4 %3, %4, %5, %6 offen offset:192 sc1\n\ts_waitcnt vmcnt(0)"
               : "=&v"(d[0]), "=&v"(d[1]), "=&v"(d[2]), "=&v"(d[3]) : "v"(voff), "s"(rs), "s"(soff) : "memory");
}
__device__ __forceinline__ u32x2 ld8(rsrc_t rs, unsigned voff, unsigned soff) { return __builtin_amdgcn_raw_buffer_load_b64(rs, voff, soff, SC1); }
__device__ __forceinline__ void st16(u32x4 v, rsrc_t rs, unsigned voff, unsigned soff) { __builtin_amdgcn_raw_buffer_store_b128(v, rs, voff + soff, 0, SC1); }
__device__ __forceinline__ void st8(u32x2 v, rsrc_t rs, unsigned voff, unsigned soff) { __builtin_amdgcn_raw_buffer_store_b64(v, rs, voff, soff, SC1); }

__device__ __forceinline__ unsigned ld4(rsrc_t rs, unsigned voff, unsigned soff) { return __builtin_amdgcn_raw_buffer_load_b32(rs, voff, soff, SC1); }
__device__ __forceinline__ void st4(unsigned v, rsrc_t rs, unsigned voff, unsigned soff) { __builtin_amdgcn_raw_buffer_store_b32(v, rs, voff, soff, SC1); }

constexpr unsigned kSpinCap = 1u << 16;
__device__ __forceinline__ bool spin_fail(unsigned& spins, rsrc_t rs, u32x2 T) {
  asm volatile("" ::: "memory");
  ++spins;
  if (spins > kSpinCap) { st8(T, rs, 0, F_TMO); return true; }
  if ((spins & 255u) == 0u) {
    const u32x2 v = ld8(rs, 0, F_TMO);
    if (v[0] == T[0] && v[1] == T[1]) return true;
  }
  __builtin_amdgcn_s_sleep(3);
  return false;
}
__device__ __forceinline__ void poll_flags2(rsrc_t rs, unsigned voff, u32x2 T) {
  for (unsigned spins = 0;;) {
    const u32x4 v = ld16(rs, voff, 0);
    if (__all(v[0] == T[0] && v[1] == T[1] && v[2] == T[0] && v[3] == T[1])) break;
    if (spin_fail(spins, rs, T)) break;
  }
  asm volatile("" ::: "memory");
  __builtin_amdgcn_fence(__ATOMIC_ACQUIRE, "wavefront");
}
__device__ __forceinline__ void poll_flags(rsrc_t rs, unsigned voff, unsigned soff, u32x2 T) {
  for (unsigned spins = 0;;) {
    const u32x2 v = ld8(rs, voff, soff);
    if (__all(v[0] == T[0] && v[1] == T[1])) break;
    if (spin_fail(spins, rs, T)) break;
  }
  asm volatile("" ::: "memory");
  __builtin_amdgcn_fence(__ATOMIC_ACQUIRE, "wavefront");
}

__device__ __forceinline__ void unpack8v(const u32x4 u, f32x2* v) {
#pragma unroll
  for (int j = 0; j < 4; ++j) v[j] = f32x2{lo16(u[j]), hi16(u[j])};
}
__device__ __forceinline__ u32x4 pack8v(const f32x2* v) {
  u32x4 o;
#pragma unroll
  for (int j = 0; j < 4; ++j) o[j] = pack2(v[j][0], v[j][1]);
  return o;
}
__device__ __forceinline__ float ln_rd(float var) { return __builtin_amdgcn_rcpf(rbf1(__builtin_amdgcn_sqrtf(rbf1(rbf1(var) + 1e-5f)))); }
__device__ __forceinline__ float hadd(f32x2 v) {
  float a = v[0];
  asm volatile("" : "+v"(a));
  return a + v[1];
}
__device__ __forceinline__ float add_g(float v) { return add_x32(add_x16(v)); }
__device__ __forceinline__ void add_g3(float& x, float& y, float& z) {
  auto a = __builtin_amdgcn_permlane16_swap(__builtin_bit_cast(unsigned, x), __builtin_bit_cast(unsigned, y), false, false);
  auto b = __builtin_amdgcn_permlane16_swap(__builtin_bit_cast(unsigned, z), __builtin_bit_cast(unsigned, z), false, false);
  const float r = asf(a[0]) + asf(a[1]), t = asf(b[0]) + asf(b[1]);
  auto c = __builtin_amdgcn_permlane32_swap(__builtin_bit_cast(unsigned, r), __builtin_bit_cast(unsigned, r), false, false);
  auto d = __builtin_amdgcn_permlane32_swap(__builtin_bit_cast(unsigned, t), __builtin_bit_cast(unsigned, t), false, false);
  const float u = asf(c[0]) + asf(c[1]);
  z = asf(d[0]) + asf(d[1]);
  auto e = __builtin_amdgcn_permlane16_swap(__builtin_bit_cast(unsigned, u), __builtin_bit_cast(unsigned, u), false, false);
  x = asf(e[0]); y = asf(e[1]);
}
__device__ __forceinline__ u32x4 trade16(u32x4 v) {
  auto a = __builtin_amdgcn_permlane16_swap(v[0], v[2], false, false);
  auto b = __builtin_amdgcn_permlane16_swap(v[1], v[3], false, false);
  return u32x4{(unsigned)a[0], (unsigned)b[0], (unsigned)a[1], (unsigned)b[1]};
}
__device__ __forceinline__ unsigned umax4(u32x4 v) { return max(max(v[0], v[1]), max(v[2], v[3])); }
__device__ __forceinline__ u32x2 unpoison2(u32x2 v) { return u32x2{min(v[0], POISON - 1), min(v[1], POISON - 1)}; }
__device__ __forceinline__ u32x4 unpoison4(u32x4 v) {
  return u32x4{min(v[0], POISON - 1), min(v[1], POISON - 1), min(v[2], POISON - 1), min(v[3], POISON - 1)};
}

typedef union { struct { s16x4 a, b; } hf; u32x4 v; } frag_t;
__device__ __forceinline__ s16x4 trread(const char* smem, unsigned off) {
  return __builtin_amdgcn_ds_read_tr16_b64_v4i16((LDS_AS s16x4*)(smem + off));
}
__device__ __forceinline__ s16x4 trabs(unsigned addr) { return __builtin_amdgcn_ds_read_tr16_b64_v4i16((LDS_AS s16x4*)(size_t)addr); }

constexpr int WA = 6;
constexpr int WB = 4, WR = 3;
#define CH 6, 8, 9, 15, 17, 23, 25, 29, 31, 4
constexpr int CHV[10] = {CH};
constexpr int FKL = CHV[9];
constexpr int FSL = CHV[0], FS0 = CHV[1], FS0D = CHV[2], FS1 = CHV[3], FS1S = CHV[4];
constexpr int FXA = CHV[5], FXB = CHV[6], FXL = CHV[7], FXS = CHV[8];
constexpr int FXT = FXB >> 2;
constexpr int CSD = 3, POI = 4, RWS = 5, EIN = 8;
constexpr int ERQ = 16, CSL = 19, CSS = 22;
#define IMV 24
constexpr int IMG = IMV, KP0 = 26;
constexpr int PB0 = 10, PB1 = 24;
constexpr int ABL = 23, ABS = 27;
constexpr int XU = 3, XL = 2;
constexpr int x_slot(int u) { return u % XU == 0 ? 0 : u % XU == 1 ? 32768 : 49152; }
#define TCH 19, 7, 4, 27
constexpr int TCV[4] = {TCH}, TS0 = TCV[0], TSN = TCV[1], TW = TCV[2], TST = TCV[3];
constexpr int QL = 26;
constexpr int RXCD = 5;
constexpr int WDS = 17 - WB;
constexpr int FSG = 4, FDG = 3, FRG = 3, FXG = 3;
#define SLP 20, 28, 8, 46, 3, 4, 12
constexpr int SLV[7] = {SLP}, SDI0 = SLV[0], SDIR = SLV[1], GSL = SLV[2], RSL = SLV[3], KSL = SLV[4], XSL = SLV[5], OSL = SLV[6];
constexpr bool tile_piece(int s) { return s >= 3 && ((s - 3) >> 2) + 3 < 8 && ((s - 3) & 3) < 3; }
constexpr int a_req(int s, int n) {
  if (!(n & 1)) return -1;
  if (s <= 1) return 8 + 3 * s + (n >> 1);
  return n < 5 && s + 5 < 32 ? 2 * (s + 5) + (n >> 1) : -1;
}
constexpr int vm_n(int it, int s, int n) {
  int c = 0;
  if (n == 0) c += tile_piece(s);
  if (n == 4) c += (s == ABL) + (it ? (s == FSL ? 4 : 0) + (s == FKL) : (s == ERQ) + (s >= IMG && s < IMG + 3));
  if (!it) c += (s < 4 && n == 2) + (s == 0 && (n == 0 || n == 4)) + ((n == 3 || n == 5) && s + XL + XU < 32);
  else c += (a_req(s, n) >= 0) + ((s == 0 || s == 4) && n == 2);
  return c;
}
constexpr int vm_at(int it, int s, int n) {
  int c = it ? 8 : 14;
  for (int u = 0; u < 6 * s + n; ++u) c += vm_n(it, u / 6, u % 6);
  return c;
}
constexpr int vm_a(int it, int k) {
  if (k < 4) return 2 * k + 2;
  for (int u = 0; u < 192; ++u)
    if (a_req(u / 6, u % 6) == 2 * k + 1) return vm_at(it, (u + 1) / 6, (u + 1) % 6);
  return 0;
}
constexpr int vm_tile(int it, int t) { return t >= 3 ? vm_at(it, 4 * t - 7, 1) : t == 1 ? vm_at(0, 0, 5) : vm_at(0, 3, 3); }
constexpr int vm_x(int u) { return u == 2 ? 9 : u < XU + XL ? 12 + 2 * (u - XU) : vm_at(0, u - XU - XL + 1, 0); }

constexpr int wo_k(int it, int s) { return it || s < 4 ? 15 - s : s < 8 ? 7 - s : 19 - s; }
constexpr int wo_pos(int it, int s) { return 2 * s + 2 + (s >= WR ? 2 * it : 0); }
constexpr int wo_at(int it, int s) { return 2 * it + (s < WB - WR ? 2 * WR + 2 + 4 * s : s < 16 - WB ? 2 * (2 * WB - WR) + 2 * (s - WB + WR) : 32); }
template <int OFF> __device__ __forceinline__ void lds_count(unsigned ad) {
  asm volatile("s_mov_b64 exec, 1\n\tds_add_u32 %0, %1 offset:%2\n\ts_mov_b64 exec, -1" ::"v"(ad), "v"(1u), "n"(OFF) : "memory");
}
__device__ __forceinline__ void lds_look(char* c, unsigned want) {
  while ((unsigned)__builtin_amdgcn_readfirstlane(__hip_atomic_load((LDS_AS unsigned*)c, __ATOMIC_RELAXED, __HIP_MEMORY_SCOPE_WORKGROUP)) < want) {}
  asm volatile("" ::: "memory");
}
__device__ __forceinline__ void pin1(unsigned& a) { asm volatile("" : "+v"(a)); }
__device__ __forceinline__ void pin2(u32x4& a, u32x4& b) { asm volatile("" : "+v"(a), "+v"(b)::"memory"); }
__device__ __forceinline__ void pin2s(float& a, float& b) { asm volatile("" : "+v"(a), "+v"(b)); }
template <int OFF> __device__ __forceinline__ void lds2(u32x4& a, u32x4& b, unsigned ad) {
  asm volatile("ds_read_b128 %0, %2 offset:%3\n\tds_read_b128 %1, %2 offset:%4" : "=&v"(a), "=&v"(b) : "v"(ad), "n"(OFF), "n"(OFF + 1024) : "memory");
}
template <int N> __device__ __forceinline__ void lds_wait(u32x4& a, u32x4& b) { asm volatile("s_waitcnt lgkmcnt(%2)" : "+v"(a), "+v"(b) : "n"(N) : "memory"); }
__device__ __forceinline__ void pin2f(f32x2& a, f32x2& b) { asm volatile("" : "+v"(a), "+v"(b)); }
__device__ __forceinline__ void pin_ac(f32x4 (&ac)[2][4]) {
  asm volatile("" : "+{v[176:179]}"(ac[0][0]), "+{v[180:183]}"(ac[0][1]), "+{v[184:187]}"(ac[0][2]), "+{v[188:191]}"(ac[0][3]),
                    "+{v[192:195]}"(ac[1][0]), "+{v[196:199]}"(ac[1][1]), "+{v[200:203]}"(ac[1][2]), "+{v[204:207]}"(ac[1][3]));
}

constexpr int L_XC = LDS_BYTES - 64, L_SKEY = 32704, L_SRED = 4096, L_SFIN = 20480;
__device__ __forceinline__ unsigned key_load(rsrc_t rs, int l, int L, int rep) { return ld4(rs, (l & 15) * 4, OFF_KEY + (L * 16 + rep) * 128); }
__device__ __forceinline__ unsigned umax16(unsigned k) {
  k = max(k, dppu<0xB1>(k));
  k = max(k, dppu<0x4E>(k));
  k = max(k, dppu<0x141>(k));
  return max(k, dppu<0x140>(k));
}
__device__ __forceinline__ unsigned key_row(unsigned key) { return (0xffffu - (key & 0xffffu)) & 2047u; }
__device__ __forceinline__ unsigned key_wait(rsrc_t rs, u32x2 T, int l, int L, int rep, unsigned kv) {
  if (!__all(kv != POISON)) {
    unsigned spins = 0;
    do {
      if (spin_fail(spins, rs, T)) break;
      if (L == 0) __builtin_amdgcn_s_sleep(KSL);
      kv = key_load(rs, l, L, rep);
    } while (!__all(kv != POISON));
  }
  return __builtin_amdgcn_readfirstlane(umax16(kv));
}
__device__ __forceinline__ unsigned ffn_xoff(unsigned key, int L, int w) { return OFF_CA + L * 32768 + (key_row(key) >> 7) * 2048 + w * 256; }
__device__ __forceinline__ void row_load(rsrc_t rs, int g, unsigned xo, u32x4 (&xv)[4]) {
  ld16h<0>(xv[0], rs, g * 16, xo); ld16h<64>(xv[1], rs, g * 16, xo); ld16h<128>(xv[2], rs, g * 16, xo); ld16h<192>(xv[3], rs, g * 16, xo);
}
template <int SL = 0> __device__ __forceinline__ void ffn_xwait(rsrc_t rs, u32x2 T, unsigned xo, int l, u32x4 (&xv)[4]) {
#define XV_OK() __all(max(max(umax4(xv[0]), umax4(xv[1])), max(umax4(xv[2]), umax4(xv[3]))) != POISON)
  if (!XV_OK()) {
    unsigned spins = 0;
    do {
      if (spin_fail(spins, rs, T)) break;
      if (SL) __builtin_amdgcn_s_sleep(SL);
#pragma unroll
      for (int i = 0; i < 4; ++i) xv[i] = ld16(rs, (l >> 4) * 16 + i * 64, xo);
    } while (!XV_OK());
  }
#undef XV_OK
}
__device__ __forceinline__ void ffn_gu(const u32x4 (&gv)[4], const u32x4 (&uv)[4], const u32x4 (&xv)[4], f32x4& ga, f32x4& ua) {
  ga = mfma16(gv[0], xv[0], f32x4{0.f, 0.f, 0.f, 0.f}); ua = mfma16(uv[0], xv[0], f32x4{0.f, 0.f, 0.f, 0.f});
#pragma unroll
  for (int i = 1; i < 4; ++i) { ga = mfma16(gv[i], xv[i], ga); ua = mfma16(uv[i], xv[i], ua); }
}
__device__ __forceinline__ void ffn_gu_check(rsrc_t rs, u32x2 T, unsigned xo, int l, const u32x4 (&gv)[4], const u32x4 (&uv)[4], u32x4 (&xv)[4], f32x4& ga, f32x4& ua) {
  if (__builtin_expect(__any(ga[0] != ga[0]), 0)) {
    ffn_xwait(rs, T, xo, l, xv);
    ffn_gu(gv, uv, xv, ga, ua);
  }
}
__device__ __forceinline__ void ffn_gu_store(char* smem, int l, int w, f32x4 ga, f32x4 ua) {
  LDS_AS f32x4* sred = (LDS_AS f32x4*)(smem + L_SRED);
  if ((l & 15) == 0) { sred[w * 8 + (l >> 4)] = ga; sred[w * 8 + 4 + (l >> 4)] = ua; }
}
__device__ __forceinline__ f32x4 ffn_t_read(char* smem, int l) { return ((const LDS_AS f32x4*)(smem + L_SRED))[(l & 7) * 8 + ((l >> 3) & 1) * 4 + (l >> 4)]; }
__device__ __forceinline__ void ffn_d_read(char* smem, int l, int blk, s16x4 (&dfr)[8]) {
#pragma unroll
  for (int i = 0; i < 8; ++i) dfr[i] = trread(smem, L_DN + blk * 4096 + (l >> 4) * 1024 + ((l & 15) >> 2) * 256 + i * 32 + (l & 3) * 8);
}
__device__ __forceinline__ float ffn_d_sum(float t) { t += dppf<0xB1>(t); t += dppf<0x4E>(t); return t + dppf<0x141>(t); }
__device__ __forceinline__ u32x2 ffn_h(f32x4 t, int l) {
#pragma unroll
  for (int e = 0; e < 4; ++e) t[e] = ffn_d_sum(t[e]);
  const float a = (l & 1) ? t[1] : t[0], b = (l & 1) ? t[3] : t[2], s = (l & 2) ? b : a;
  const float gg = rbf1(s), uu = rbf1(dppf<0x128>(s));
  const float h = rbf1(gg * __builtin_amdgcn_rcpf(1.0f + __expf(-gg))) * uu;
  const unsigned h0 = pack2(h, dppf<0x55>(h)), h1 = pack2(dppf<0xAA>(h), dppf<0xFF>(h));
  return (l & 15) == 0 ? u32x2{h0, h1} : u32x2{0u, 0u};
}
__device__ __forceinline__ void ffn_d_mma(const s16x4 (&dfr)[8], u32x2& hh, f32x4& ya, f32x4& yb) {
#pragma unroll
  for (int i = 0; i < 8; ++i) {
    if (i & 1) yb = mfma16k16(dfr[i], __builtin_bit_cast(s16x4, hh), yb);
    else ya = mfma16k16(dfr[i], __builtin_bit_cast(s16x4, hh), ya);
    hh = u32x2{dppu<0x111>(hh[0]), dppu<0x111>(hh[1])};
  }
}
__device__ __forceinline__ void ffn_d_store(rsrc_t rs, int l, int G0, int L, int slice, f32x4 y) {
  const unsigned c = l & 15;
  st16(unpoison4(__builtin_bit_cast(u32x4, y)), rs, (G0 + (c >> 1)) * 32768 + slice * 128 + (c & 1) * 64 + (l >> 4) * 16, OFF_PART + L * (1u << 20));
}
__device__ __forceinline__ void red_load(rsrc_t rs, int tid, int L, unsigned G, unsigned key, u32x4 (&pv)[4], u32x2& cxn, u32x2& cx1) {
#pragma unroll
  for (int k = 0; k < 4; ++k) pv[k] = ld16(rs, min(tid + 512 * k, 1791) * 16, OFF_PART + L * (1u << 20) + G * 32768);
  cxn = ld8(rs, (tid & 3) * 16 + (tid & 4) * 2, OFF_CA + L * 32768 + (key_row(key) >> 7) * 2048 + G * 64);
  cx1 = ld8(rs, (tid & 3) * 16 + (tid & 4) * 2, OFF_CB + L * 32768 + (key_row(key) >> 7) * 2048 + G * 64);
}
__device__ __forceinline__ void red_sum(rsrc_t rs, char* smem, u32x2 T, int tid, int w, int L, unsigned G, unsigned key,
                                        u32x4 (&pv)[4], u32x2& cxn, u32x2& cx1) {
#define PV_OK() __all(max(max(max(umax4(pv[0]), umax4(pv[1])), max(umax4(pv[2]), umax4(pv[3]))), max(max(cxn[0], cxn[1]), max(cx1[0], cx1[1]))) != POISON)
  if (!PV_OK()) {
    unsigned spins = 0;
    do {
      if (spin_fail(spins, rs, T)) break;
      red_load(rs, tid, L, G, key, pv, cxn, cx1);
    } while (!PV_OK());
  }
#undef PV_OK
  f32x4 s = (__builtin_bit_cast(f32x4, pv[0]) + __builtin_bit_cast(f32x4, pv[1])) + __builtin_bit_cast(f32x4, pv[2]);
  if (w < 4) s += __builtin_bit_cast(f32x4, pv[3]);
  ((LDS_AS f32x4*)(smem + L_SFIN))[tid] = s;
}
template <int NW> __device__ __forceinline__ u32x2 red_final(char* smem, int l, unsigned key, u32x2 cxn, u32x2 cx1) {
  const LDS_AS f32x4* sf = (const LDS_AS f32x4*)(smem + L_SFIN) + l;
  f32x4 s = (sf[0] + sf[64]) + (sf[128] + sf[192]);
  if constexpr (NW == 8) s += (sf[256] + sf[320]) + (sf[384] + sf[448]);
#pragma unroll
  for (int j = 0; j < 4; ++j) s[j] = sum_hi8(s[j]);
  const unsigned k16 = key >> 16;
  const float rmax = __builtin_bit_cast(float, ((k16 & 0x8000u) ? (k16 & 0x7fffu) : (~k16 & 0xffffu)) << 16);
  const float sig = rbf1(__builtin_amdgcn_rcpf(1.0f + __expf(-rmax)));
  const float xn4[4] = {lo16(cxn[0]), hi16(cxn[0]), lo16(cxn[1]), hi16(cxn[1])};
  const float x14[4] = {lo16(cx1[0]), hi16(cx1[0]), lo16(cx1[1]), hi16(cx1[1])};
  float o[4];
#pragma unroll
  for (int j = 0; j < 4; ++j) o[j] = x14[j] + rbf1(xn4[j] + rbf1(rbf1(s[j]) * sig));
  return u32x2{pack2(o[0], o[1]), pack2(o[2], o[3])};
}

struct RowFin { float mean, var, mb, rd; unsigned key; };
__device__ __forceinline__ void fin_wait(rsrc_t rs, u32x2 T, unsigned voff, unsigned so, u32x4 (&sp)[4]) {
#define ST_OK() __all(max(max(umax4(sp[0]), umax4(sp[1])), max(umax4(sp[2]), umax4(sp[3]))) != POISON)
  if (!ST_OK()) {
    unsigned spins = 0;
    do {
      if (spin_fail(spins, rs, T)) break;
#pragma unroll
      for (int i = 0; i < 4; ++i) sp[i] = ld16(rs, voff + i * 2048, so);
    } while (!ST_OK());
  }
#undef ST_OK
}
__device__ __forceinline__ RowFin fin_key(rsrc_t rs, u32x2 T, unsigned voff, unsigned so, char* skey, int l, int w, unsigned R, float wsum, u32x4 (&sp)[4]) {
  RowFin r;
#define ST_SUM() ((__builtin_bit_cast(f32x4, sp[0]) + __builtin_bit_cast(f32x4, sp[1])) + (__builtin_bit_cast(f32x4, sp[2]) + __builtin_bit_cast(f32x4, sp[3])))
  f32x4 sf = ST_SUM();
  const float z = (sf[0] + sf[1]) + sf[2];
  if (__any(z != z)) {
    fin_wait(rs, T, voff, so, sp);
    sf = ST_SUM();
  }
#undef ST_SUM
  float s0 = sf[0], s1 = sf[1], s2 = sf[2];
  add_g3(s0, s1, s2);
  r.mean = s0 * (1.0f / 1024.0f);
  r.var = fmaxf(__builtin_fmaf(-r.mean, r.mean, s1 * (1.0f / 1024.0f)), 0.f);
  r.mb = rbf1(r.mean); r.rd = ln_rd(r.var);
  const unsigned rb16 = pack2((s2 - r.mb * wsum) * r.rd, 0.f) & 0xffffu;
  r.key = min((((rb16 & 0x8000u) ? (~rb16 & 0xffffu) : (rb16 | 0x8000u)) << 16) | (0xffffu - R), POISON - 1);
  const unsigned wk = umax16(r.key);
  if (l == 0) ((LDS_AS unsigned*)skey)[w] = wk;
  return r;
}
__device__ __forceinline__ void fin_cand(rsrc_t rs, char* skey, int L, int g, int rb, int ct, const RowFin& r, const f32x2 (&v)[8]) {
  const u32x4 k0 = *(const LDS_AS u32x4*)skey, k1 = *(const LDS_AS u32x4*)(skey + 16);
  const unsigned best = max(umax4(k0), umax4(k1));
  if (r.key == best) {
    const unsigned co = L * 32768 + rb * 2048 + ct * 128 + g * 16;
    f32x2 t[8], rd2 = {r.rd, r.rd}, of2 = {-r.mb * r.rd, -r.mb * r.rd};
    asm volatile("" : "+v"(rd2), "+v"(of2));
#pragma unroll
    for (int j = 0; j < 8; ++j) t[j] = __builtin_elementwise_fma(v[j], rd2, of2);
    st16(unpoison4(pack8v(t)), rs, co, OFF_CA); st16(unpoison4(pack8v(t + 4)), rs, co + 64, OFF_CA);
    if (g == 0) st4(best, rs, OFF_KEY + (L * 16 + ct) * 128 + rb * 4, 0);
    st16(unpoison4(pack8v(v)), rs, co, OFF_CB); st16(unpoison4(pack8v(v + 4)), rs, co + 64, OFF_CB);
  }
}
__device__ __forceinline__ void fin_x2(const RowFin& r, f32x2 (&v)[8]) {
  f32x2 sc2 = {1.0f + r.rd, 1.0f + r.rd}, of2 = {-r.mb * r.rd, -r.mb * r.rd};
  asm volatile("" : "+v"(sc2), "+v"(of2));
#pragma unroll
  for (int j = 0; j < 8; ++j) v[j] = __builtin_elementwise_fma(v[j], sc2, of2);
}

extern "C" __device__ unsigned long long dispatch_id() __asm("llvm.amdgcn.dispatch.id");

extern "C" __global__ __launch_bounds__(512, 2) void mega_kernel(const bf16* x, char* ws, const bf16* wqkv, const bf16* wo, const bf16* wr,
                                                                 const bf16* wg, const bf16* wu, const bf16* wd, bf16* out) {
  extern __shared__ __attribute__((aligned(16))) char smem[];
  const int w0_ = __builtin_amdgcn_readfirstlane(threadIdx.x >> 6);
  typedef unsigned long long u64x2 __attribute__((ext_vector_type(2)));
  u64x2 a01;
  unsigned long long a2;
  asm volatile("s_load_dwordx4 %0, %2, 0\n\ts_load_dwordx2 %1, %2, 16" : "=&s"(a01), "=&s"(a2) : "s"(__builtin_amdgcn_kernarg_segment_ptr()));
  const unsigned lbase = (unsigned)(size_t)(LDS_AS char*)smem;
  {
    unsigned long long pc;
    unsigned len;
    asm volatile("s_getpc_b64 %0\n\ts_mov_b32 %1, .Ltext_end-mega_kernel\n\t.subsection 1\n.Ltext_end:\n\t.subsection 0" : "=s"(pc), "=s"(len));
    const unsigned o = ((blockIdx.x >> 3) * 64 + (threadIdx.x & 63)) * 128;
    if (w0_ == 7 && o < len)
      asm volatile("s_mov_b32 m0, %0\n\ts_nop 0\n\tglobal_load_lds_dword %1, %2" ::"s"(lbase + L_TOUCH0), "v"(o), "s"(pc & ~127ull) : "memory");
  }
#define HGRP() ((xcd & 2) + (xcd >> 2))
#define IDS()                                                                                         \
  int tid = threadIdx.x, w = w0_, bid = blockIdx.x;                                                   \
  asm volatile("" : "+v"(tid));                                                                       \
  asm volatile("" : "+s"(w));                                                                         \
  asm volatile("" : "+s"(bid));                                                                       \
  const int l = tid & 63, xcd = bid & 7, bi = bid >> 3;                                               \
  const int p = (xcd & 1) * 4 + (bi & 3), h = HGRP() * 8 + (bi >> 2);         \
  const int rb = 2 * xcd + (bi & 1), ct = bi >> 1;                               \
  const int slice = (xcd - (xcd > RXCD)) * 32 + bi;                                            \
  const bool ffn_on = xcd != RXCD;                                                                    \
  const int g = l >> 4;                                                                               \
  const int rt = w < 4 ? w : 11 - w;          \
  KARGS();                                                                                            \
  (void)p; (void)h; (void)rb; (void)ct; (void)slice; (void)ffn_on; (void)g; (void)rt;
#define PROT() (2 * (p & 3) + (p >> 2))
  typedef const void* const __attribute__((address_space(4)))* karg_t;
#define KARGS() karg_t ka = (karg_t)__builtin_amdgcn_kernarg_segment_ptr(); asm volatile("" : "+s"(ka))
#define ARGP(name, i) const GAS bf16* name = (const GAS bf16*)ka[i]; asm volatile("" : "+s"(name))
#define RSRC(ptr, bytes) __builtin_amdgcn_make_buffer_rsrc((void*)(ptr), 0, bytes, 0x00020000)
#define rwq RSRC(pwq, 1024 * 3072 * 2)
#define rwo RSRC(pwo, 1024 * 1024 * 2)
#define rwg RSRC(pwg, 1024 * F_ * 2)
#define rwu RSRC(pwu, 1024 * F_ * 2)
#define rwd RSRC(pwd, 1024 * F_ * 2)
#define rout RSRC(pout, 2048 * 1024 * 2)

#define WQ_VOFF() ((l >> 2) * 6144 + (((l & 3) ^ ((((l >> 4) ^ (l >> 5)) & 1) << 1)) << 4))
#define WQ_KMAJ(q) (((q) % 3) * 8 + (q) / 3)
#define WQ_PIECE(t, j, dst) dma16(rwq, wqv, ((((t) + rot) & 7) * 128 + ((j) & 7) * 16) * 6144 + ((j) >> 3) * 2048 + h * 64, lbase + (dst) + (j) * 1024)
#define WQ_BOFF(dt) ((g >> 1) * 1024 + (8 * (g & 1) + ((l & 15) >> 2)) * 64 + (l & 3) * 8 + (((dt) ^ (g & 1)) << 5))
#define X_REQ(u, hf) dma16(rx, xv[hf], (p * 256 + rt * 32 + ((u) & 1) * 16 + (hf) * 8) * 2048 + ((((u) >> 2) + rot) & 7) * 256 + (((u) >> 1) & 1) * 128, lbase + rt * 2048 + x_slot(u) + (hf) * 1024)
#define WO_PIECE_OF(w, j) dma16(rwo, wov, (((w) >> 2) * 64 + ((w) & 3) + 4 * (j)) * 16384 + ct * 128, lbase + 32768 + (j) * 8192 + (w) * 1024)
#define WO_PIECE(j) WO_PIECE_OF(w, j)
#define WD_DMA_OF(v) _Pragma("unroll") for (int i = 0; i < 4; ++i)                                  \
      dma16(rwd, (l >> 4) * 2048 + (l & 15) * 16, (unsigned)(slice * 16 + 4 * i) * 2048 + (v) * 256, lbase + L_DN + (v) * 4096 + i * 1024)
#define POISON_BIG(L)                                                                                \
  if (w >= 2) {                                                                                      \
    unsigned pz1 = POISON;                                                                           \
    asm volatile("" : "+v"(pz1));                                                                    \
    st16(u32x4{pz1, pz1, pz1, pz1}, rs, (w >= 4 ? OFF_PART + (L) * (1u << 20) + bid * 4096 - 4096 : OFF_STAT + (L) * 524288 + (p * 32 + h) * 2048 - 2048) + tid * 16, 0); \
  }
  u32x4 gv[4], uv[4];
  u32x4 keep[9];
  u32x4 pre[4][2];
#define PRE_LOAD(it_, rot_, s0, s1)                                                                  \
  static_for<(s1) - (s0)>([&](auto c_) __attribute__((always_inline)) {                              \
    constexpr int s = (s0) + decltype(c_)::value;                                                    \
    const unsigned ab_ = OFF_X1 + (((rot_) + (s >> 2)) & 7) * 8192 + ((s >> 1) & 1) * 4096; \
    ld16w<2 * s, (s & 1) * 2048>(pre[s][0], rs, l * 16, ab_ + (p * 8 + rt) * 65536);                         \
    ld16w<2 * s + 1, (s & 1) * 2048 + 1024>(pre[s][1], rs, l * 16, ab_ + (p * 8 + rt) * 65536);                 \
  })
  u32x2 T;
  const GAS bf16* tsrc;
  unsigned xv[2], xa[2];
  u32x2 ep;
  u32x4 rows0[4];
#define X_READ(u, d0, d1) { d0 = *(const LDS_AS u32x4*)(size_t)(xa[0] + x_slot(u)); d1 = *(const LDS_AS u32x4*)(size_t)(xa[1] + x_slot(u)); }
  unsigned g1[5], wqv0;
  {
    IDS();
    const int rot = PROT(), qa = WQ_KMAJ(w), qb = WQ_KMAJ(8 + w);
    wqv0 = WQ_VOFF();
    xv[0] = (l >> 3) * 2048 + (((l & 7) ^ (l >> 4)) << 4); xv[1] = xv[0] ^ 64;
    g1[0] = (rot * 128 + (qa & 7) * 16) * 6144 + (qa >> 3) * 2048 + h * 64; g1[1] = lbase + L_RING + qa * 1024;
    g1[2] = (rot * 128 + (qb & 7) * 16) * 6144 + (qb >> 3) * 2048 + h * 64; g1[3] = lbase + L_RING + qb * 1024;
    g1[4] = (p * 256 + rt * 32) * 2048 + rot * 256;
    asm volatile("" : "+v"(wqv0), "+v"(xv[0]), "+v"(xv[1]));
    asm volatile("s_waitcnt lgkmcnt(0)" : "+s"(a01), "+s"(a2), "+s"(g1[0]), "+s"(g1[1]), "+s"(g1[2]), "+s"(g1[3]), "+s"(g1[4]));
  }
  x = (const bf16*)a01[0]; ws = (char*)a01[1]; wqkv = (const bf16*)a2;
  const rsrc_t rs = __builtin_amdgcn_make_buffer_rsrc((void*)ws, 0, WS_BYTES, 0x00020000);
  const rsrc_t rx = RSRC(x, 2048 * 1024 * 2);
  {
    IDS();
    const int rot = PROT();
    const bf16* pwq = wqkv;
    const unsigned wqv = wqv0;
    dma16(rwq, wqv, g1[0], g1[1]);
#pragma unroll
    for (int i = 0; i < 4; ++i) dma16(rx, xv[i & 1], g1[4] + i * 16384, lbase + rt * 2048 + x_slot(i >> 1) + (i & 1) * 1024);
    dma16(rwq, wqv, g1[2], g1[3]);
    asm volatile("s_barrier" ::: "memory");
    WQ_PIECE(0, WQ_KMAJ(16 + w), L_RING);
    if (w == 6 && bi == 0) {
      ARGP(pwr, 4);
      asm volatile("s_mov_b32 m0, %0\n\ts_nop 0\n\tbuffer_load_dword %1, %2, 0 offen lds" ::"s"(lbase + L_TOUCH0 + 256), "v"((l & 15) * 128), "s"(RSRC(pwr, 2048)) : "memory");
    }
    X_REQ(2, 0); X_REQ(2, 1);
    asm volatile("buffer_load_dwordx2 %0, off, %1, 0 sc1" : "=v"(ep) : "s"(rs) : "memory");
    asm volatile("" : "=v"(rows0[0]), "=v"(rows0[1]), "=v"(rows0[2]), "=v"(rows0[3]));
    if (w < 4) ldrows(rows0, rx, (l >> 5) * 2048 + (l & 31) * 16, (p * 256 + h * 8 + 2 * w) * 2048);
    {
      ARGP(pwo, 3); ARGP(pwg, 5); ARGP(pwu, 6);
      tsrc = (w < 4 ? pwg : pwu) + ((bi * 32 + ((tid & 255) >> 3)) * F_ + (slice >> 5) * 512 + (tid & 7) * 64);
      if (!ffn_on) tsrc = pwo + (bi * 512 + tid) * 64;
      asm volatile("" : "+v"(tsrc));
    }
#pragma unroll
    for (int kk = 0; kk < 2; ++kk) {
      const int r = l & 15;
      xa[kk] = lbase + rt * 2048 + (r >> 3) * 1024 + (r & 7) * 128 + (((4 * kk + g) ^ (r >> 1)) << 4);
      asm volatile("" : "+v"(xa[kk]));
    }
    if (tid == 192) *(LDS_AS u32x4*)(smem + L_QC) = u32x4{0u, 0u, 0u, 0u};
    if (w < 4) wait_vm<8>(); else wait_vm<4>();
    X_READ(0, pre[0][0], pre[1][0]); X_READ(1, pre[0][1], pre[1][1]);
    asm volatile("s_waitcnt lgkmcnt(0)\n\ts_barrier" : "+v"(pre[0][0]), "+v"(pre[0][1]), "+v"(pre[1][0]), "+v"(pre[1][1])::"memory");
    X_REQ(3, 0); X_REQ(3, 1); X_REQ(4, 0); X_REQ(4, 1);
  }
  unsigned sel0 = 0x7FFFFFFFu;
  u32x4 x2p[2];
  unsigned xm0 = 0;

  auto layer = [&](auto it_c) __attribute__((always_inline)) {
    constexpr int it = decltype(it_c)::value;
    f32x4 acc[2][6];
    f32x2 qs2[2];
    u32x4 k0, k1, v0, v1;
    unsigned tdum = 0;
    {
      IDS();
      bool aff = false;
      unsigned aq[2];
      u32x4 av[2];
#define PIN_ACC() asm volatile("" : "+{v[176:179]}"(acc[0][0]), "+{v[180:183]}"(acc[0][1]), "+{v[184:187]}"(acc[0][2]), "+{v[188:191]}"(acc[0][3]), "+{v[192:195]}"(acc[0][4]), "+{v[196:199]}"(acc[0][5]), \
                               "+{v[200:203]}"(acc[1][0]), "+{v[204:207]}"(acc[1][1]), "+{v[208:211]}"(acc[1][2]), "+{v[212:215]}"(acc[1][3]), "+{v[216:219]}"(acc[1][4]), "+{v[220:223]}"(acc[1][5]))
#define PINV4(x) asm volatile("" : "+v"(x[0]), "+v"(x[1]), "+v"(x[2]), "+v"(x[3])::"memory")
      auto qkv_loop = [&](auto it_c) __attribute__((always_inline)) {
        constexpr int IT = decltype(it_c)::value;
        const unsigned abase = OFF_X1 + (p * 8 + rt) * 65536;
        const int rot = PROT() + 5 * IT;
        unsigned fkey = 0, kv, bq[2];
        u32x4 fx[4], bv[2], af[WA][2];
        u32x2 cxn, cx1;
        f32x4 sa, sb, cs4[2];
        f32x2 b2[2];
#define AB_OFF() (OFF_AB + IT * 16384 + (p * 8 + rt) * 128)
#define A_LOAD1(k, a) ld16w<(k) % WA * 2 + (a), ((k) & 1) * 2048 + (a) * 1024>(af[(k) % WA][a], rs, l * 16, abase + ((((k) >> 2) + rot) & 7) * 8192 + (((k) >> 1) & 1) * 4096)
#pragma unroll
        for (int s = 0; s < 2 + 2 * IT; ++s) { af[s][0] = pre[s][0]; af[s][1] = pre[s][1]; }
        const bf16* pwq = wqkv;
        const int tstep = TS0 + bid % TSN;
        const unsigned wqv = WQ_VOFF();
        unsigned wb[3], lw = lbase + L_RING + w * 3072, xrow = (p * 256 + rt * 32) * 2048, lx = lbase + rt * 2048;
#pragma unroll
        for (int i = 0; i < 3; ++i) wb[i] = IT ? OFF_WI + h * 8 * STAGE + w * 3072 : ((3 * w + i) & 7) * 16 * 6144 + ((3 * w + i) >> 3) * 2048 + h * 64;
        asm volatile("" : "+s"(wb[0]), "+s"(lw));
        if constexpr (!IT) asm volatile("" : "+s"(wb[1]), "+s"(wb[2]), "+s"(xrow), "+s"(lx));
#define QKV_DMA1(t, i)                                                                            \
  {                                                                                               \
    if constexpr (IT == 0) dma16r<SLOT(t) * STAGE + (i) * 1024>(rwq, wqv, wb[i], (((t) + rot) & 7) * (128 * 6144), lw); \
    else dma16t<SLOT(t) * STAGE, (i) * 1024>(rs, l * 16, wb[0], (((t) + rot) & 7) * STAGE, lw);   \
  }
#define X_REQ2(u, hf) dma16i<x_slot(u) + (hf) * 1024, ((u) & 1) * 32768 + (hf) * 16384 + (((u) >> 1) & 1) * 128>(rx, xv[hf], xrow + ((((u) >> 2) + rot) & 7) * 256, lx)
        unsigned bo[2][2];
#pragma unroll
        for (int dt = 0; dt < 2 - IT; ++dt) {
          bo[dt][0] = lbase + L_RING + (IT ? l * 16 : WQ_BOFF(dt)); bo[dt][1] = bo[dt][0] + 2 * STAGE;
          asm volatile("" : "+v"(bo[dt][0]), "+v"(bo[dt][1]));
        }
#define FRAG_T() (lbase + L_RING + (4 * g + ((l & 15) >> 2)) * 64 + (l & 3) * 8 + (((g ^ (g >> 1)) & 1) << 5))
        auto frag_t2 = [&](unsigned o) __attribute__((always_inline)) {
          frag_t t;
          t.hf.a = trabs(o); t.hf.b = trabs(o + 1024);
          return t.v;
        };
        unsigned ka[3];
        auto tile_sum = [&](u32x4 pt, int dst) __attribute__((always_inline)) {
          f32x4 c = __builtin_bit_cast(f32x4, pt);
#pragma unroll
          for (int e = 0; e < 4; ++e) { c[e] += dppf<0x124>(c[e]); c[e] += dppf<0x128>(c[e]); c[e] = add_x16(c[e]); }
          if ((l & 28) == 0) *(LDS_AS f32x4*)(smem + dst + (w * 2 + (l >> 5)) * 64 + (l & 3) * 16) = c;
        };
        frag_t bfr[6];
#define QKV_BO(s, n) (bo[(n) & 1][SLOT((s) >> 2) >> 1] + (SLOT((s) >> 2) & 1) * STAGE + ((n) >> 1) * 8192 + ((s) & 3) * 2048)
#define QKV_RDA(s, n)                                                                             \
  {                                                                                               \
    if constexpr (IT == 0) bfr[n].hf.a = trabs(QKV_BO(s, n));                                     \
    else bfr[n].v = *(const LDS_AS u32x4*)(size_t)(bo[0][SLOT((s) >> 2) >> 1] + (SLOT((s) >> 2) & 1) * STAGE + (((s) & 3) * 6 + (n)) * 1024); \
  }
#define QKV_RDB(s, n) { if constexpr (IT == 0) bfr[n].hf.b = trabs(QKV_BO(s, (n) ^ 1) + 256); }
#pragma unroll
        for (int n = 0; n < 6; ++n) { QKV_RDA(0, n); if (n < 5) QKV_RDB(0, n); }
#define XF_OFF() (l & 8 ? ((l & 7) * RX + bi % RX) * 256 + h * 8 : (p * RX + (xcd >> 1) % RX) * 256 + (4 * rt + (l & 3)) * 8)
#define XP_OFF() (OFF_XNEW + ((rot + FXT) & 7) * 256)
#define FP_OFF(i) (w == 3 ? OFF_XNEW + (i) * 1024 : OFF_FX + h * 3072 + w * 1024)
#define FX_OK(n) __all(((n) == 4 ? max(max(umax4(fx[0]), umax4(fx[1])), max(umax4(fx[2]), umax4(fx[3]))) : max(umax4(fx[0]), umax4(fx[1]))) != POISON)
#define SINCE(s0) (vm_at(IT, s, n) - vm_at(IT, s0, 5))
#define DEF4(x) asm volatile("" : "=v"(x[0]), "=v"(x[1]), "=v"(x[2]), "=v"(x[3]))
        LDS_AS unsigned* qcnt = (LDS_AS unsigned*)(smem + L_QC);
        unsigned qc;
        unsigned qadr = lbase + L_QC, qone = 1;
        asm volatile("" : "+v"(qadr), "+v"(qone));
#define count_in(i) asm volatile("s_mov_b64 exec, 1\n\tds_add_u32 %0, %1 offset:%2\n\ts_mov_b64 exec, -1" ::"v"(qadr), "v"(qone), "n"(4 * (i)) : "memory")
        auto count_wait = [&](int i, unsigned want) __attribute__((always_inline)) {
          if (__builtin_expect((unsigned)__builtin_amdgcn_readfirstlane(qc) < want, 0)) {
            while ((unsigned)__builtin_amdgcn_readfirstlane(__hip_atomic_load(qcnt + i, __ATOMIC_RELAXED, __HIP_MEMORY_SCOPE_WORKGROUP)) < want) {}
          }
          asm volatile("" ::: "memory");
        };
        unsigned long long xok = xm0;
        auto x1_need = [&](unsigned long long m) __attribute__((always_inline)) {
          if (__builtin_expect((xok & m) != m, 0)) {
            for (unsigned spins = 0;;) {
              const u32x2 f = ld8(rs, F_X1 + ((p * RX + (xcd >> 1) % RX) * 32 + (l & 31)) * 8, 0);
              xok = __builtin_amdgcn_ballot_w64(f[0] == T[0] && f[1] == T[1]);
              if ((xok & m) == m) break;
              if (spin_fail(spins, rs, T)) { xok = ~0ull; break; }
              __builtin_amdgcn_s_sleep(KSL);
            }
            asm volatile("" ::: "memory");
          }
        };
        auto gap = [&](auto sc_, auto nc_) __attribute__((always_inline)) {
          constexpr int s = decltype(sc_)::value, n = decltype(nc_)::value;
          {
            constexpr int ts = (s + 3) / 4, tc = (s + 2) / 4, t0 = 1;
            if constexpr (ts >= 3 && ts < 8 && s == 4 * ts - 3 && n == 2) { wait_vm<vm_at(IT, s, n) - vm_tile(IT, ts)>(); count_in(0); }
            if constexpr (!IT && tc >= t0 && tc < 3 && s == 4 * tc - 2 && n == 1) { wait_vm<vm_at(IT, s, n) - vm_tile(IT, tc)>(); count_in(0); }
            if constexpr (IT && ts >= t0 && ts < 3 && s == 4 * ts - 3 && n == 2) count_in(0);
            if constexpr (tc >= t0 && tc < 8 && s == 4 * tc - 2) {
              if constexpr (n == 3) qc = __hip_atomic_load(qcnt, __ATOMIC_RELAXED, __HIP_MEMORY_SCOPE_WORKGROUP);
              if constexpr (n == 5) count_wait(0, 8 * (tc - t0 + 1));
            }
          }
          if constexpr (n == 0 && tile_piece(s)) QKV_DMA1(((s - 3) >> 2) + 3, (s - 3) & 3);
          if constexpr (IT == 0) {
            if constexpr (s == 0 && !(n & 1)) QKV_DMA1(1, n >> 1);
            if constexpr (s >= 1 && s < 4 && n == 2) QKV_DMA1(2, s - 1);
            constexpr int u = s + XL;
            if constexpr (n == 1 && u < 32) {
              if (s == 0 && w < 4) wait_vm<vm_at(0, s, n) - vm_x(u) + 4>();
              else if (s > TST && w >= TW) wait_vm<vm_at(0, s, n) - vm_x(u) + 1>();
              else wait_vm<vm_at(0, s, n) - vm_x(u)>();
              X_READ(u, af[(u & ~1) % WA][u & 1], af[(u | 1) % WA][u & 1]);
            }
            if constexpr (n == 3 && u + XU < 32) { asm volatile("s_waitcnt lgkmcnt(%0)" ::"n"(s + 1 < QL ? 4 : 3) : "memory"); X_REQ2(u + XU, 0); }
            if constexpr (n == 5 && u + XU < 32) X_REQ2(u + XU, 1);
            if constexpr (s == PB0 && n == 0) { POISON_BIG(0); }
            if constexpr (s == 0 && n == 1) count_in(2);
            if constexpr (s == 0 && n == 3) qc = __hip_atomic_load(qcnt + 2, __ATOMIC_RELAXED, __HIP_MEMORY_SCOPE_WORKGROUP);
            if constexpr (s == 0 && n == 5) count_wait(2, 8);
            if constexpr (s == 1 && n == 1) {
              asm volatile("" : "+v"(ep)::"memory");
              const unsigned long long e = (((unsigned long long)(unsigned)__builtin_amdgcn_readfirstlane(ep[1]) << 32) | (unsigned)__builtin_amdgcn_readfirstlane(ep[0])) + 1ull + dispatch_id() * 0x9E3779B97F4A7C16ull;
              T = u32x2{(unsigned)e, (unsigned)(e >> 32)};
            }
            if constexpr (s == RWS && n == 0) {
              if (w < 4) {
                PINV4(rows0);
                f32x2 sv[16];
#pragma unroll
                for (int i = 0; i < 4; ++i) unpack8v(rows0[i], sv + 4 * i);
                f32x2 t0 = sv[0] + sv[1], t1 = sv[2] + sv[3];
#pragma unroll
                for (int j = 4; j < 16; j += 2) { t0 += sv[j]; t1 += sv[j + 1]; }
                const float m = sum32(hadd(t0 + t1)) * (1.0f / 1024.0f);
                f32x2 m2 = {m, m}, q0 = {0.f, 0.f}, q1 = {0.f, 0.f};
                asm volatile("" : "+v"(m2));
#pragma unroll
                for (int j = 0; j < 16; j += 2) {
                  const f32x2 d0 = sv[j] - m2, d1 = sv[j + 1] - m2;
                  q0 = __builtin_elementwise_fma(d0, d0, q0); q1 = __builtin_elementwise_fma(d1, d1, q1);
                }
                const float rd = ln_rd(sum32(hadd(q0 + q1)) * (1.0f / 1024.0f));
                if ((l & 30) == 0) st4(__builtin_bit_cast(unsigned, (l & 1) ? -rbf1(m) : rd), rs, (l >> 5) * 4 + (l & 1) * 8192, OFF_AB + (p * 256 + h * 8 + 2 * w) * 4);
              }
            }
            if constexpr (s == POI && n == 4) {
              if (w == 3) {
                unsigned pz1 = POISON;
                asm volatile("" : "+v"(pz1));
                const unsigned prb = 2 * p + (h >> 4), pct = h & 15, m = l - 32;
                static_assert(OFF_CB == OFF_CA + 131072, "poison addressing");
                if (l < 60) st16(u32x4{pz1, pz1, pz1, pz1}, rs, l < 32 ? OFF_CA + (l & 8) * 4096 + (l & 16) * 8192 + prb * 2048 + pct * 128 + (l & 7) * 16
                                                             : l < 56 ? OFF_FX + ((h * 6 + (m >> 2)) * 8 + p) * 64 + (l & 3) * 16
                                                                      : OFF_AB + 16384 + (p * 32 + h) * 32 + (l & 1) * 16 + (l & 2) * 4096, 0);
                if (l < 4) st4(pz1, rs, l < 2 ? OFF_XNEW + bid * 8 + l * 4 : OFF_KEY + ((l & 1) * 16 + pct) * 128 + prb * 4, 0);
              }
            }
            if constexpr (s == POI && n == 2) {
              if (w == 2 && l < 9) {
                unsigned f0 = F_KV + KVF(0, p * 32 + h) + ((l - 5) & 3) * 128, f1 = F_O + OFB(0, p) + OFL(h), f2 = F_X1 + ((rb >> 1) * RX * 32 + (l & 3) * 32 + (rb & 1) * 16 + ct) * 8;
                asm volatile("" : "+v"(f0), "+v"(f1), "+v"(f2));
                const unsigned fo = l >= 5 ? f0 : l == 4 ? f1 : f2;
                touchb(tdum, rs, fo);
              }
            }
            static_assert(vm_x(EIN + XL) >= vm_at(0, EIN - 3, 5) && IMG >= 1 && CSD >= 1, "first flag");
            if constexpr (s == EIN && n == 1) { if (w < 4) count_in(1); }
            if constexpr (s == EIN + 2 && n == 0) qc = __hip_atomic_load(qcnt + 1, __ATOMIC_RELAXED, __HIP_MEMORY_SCOPE_WORKGROUP);
            if constexpr (s == EIN + 2 && n == 2) {
              if (w == 0) { count_wait(1, 4); if (l < RX) st8(T, rs, F_XN + (p * RX + l) * 256 + h * 8, 0); }
            }
            if constexpr (s == ERQ && n == 4) asm volatile("buffer_load_dwordx2 %0, %1, %2, 0 offen sc1" : "=v"(cxn) : "v"(XF_OFF() + F_XN), "s"(rs) : "memory");
            if constexpr (s == CSL && n == 2) {
              wait_vm<SINCE(ERQ)>();
              asm volatile("" : "+v"(cxn)::"memory");
              if (!__all(cxn[0] == T[0] && cxn[1] == T[1])) poll_flags(rs, XF_OFF(), F_XN, T);
            }
          } else {
#define X1_OFF() (F_X1 + ((p * RX + (xcd >> 1) % RX) * 32 + (l & 31)) * 8)
            if constexpr ((s == 0 || s == 4) && n == 2) asm volatile("buffer_load_dwordx2 %0, %1, %2, 0 offen sc1" : "=v"(cxn) : "v"(X1_OFF()), "s"(rs) : "memory");
            if constexpr ((s == 3 || s == 7) && n == 0) {
              wait_vm<vm_at(1, s, 1) - vm_at(1, s - 3, 3)>();
              asm volatile("" : "+v"(cxn)::"memory");
              xok |= __builtin_amdgcn_ballot_w64(cxn[0] == T[0] && cxn[1] == T[1]);
            }
            if constexpr (a_req(s, n) >= 0 && (a_req(s, n) & 7) == 0) x1_need(0x30003ull << (((rot + a_req(s, n) / 8) & 7) * 2));
          }
          if constexpr (IT && a_req(s, n) >= 0) A_LOAD1(a_req(s, n) / 2, a_req(s, n) & 1);
          if constexpr (IT == 0 && s == CSD && n == 4) {
            if (w < 3) {
              unsigned a0 = FRAG_T() + SLOT(s >> 2) * STAGE + w * 8192, a1 = a0 ^ 32, one2 = 0x3F803F80u;
              asm volatile("" : "+v"(a0), "+v"(a1), "+v"(one2));
              const u32x4 o4 = {one2, one2, one2, one2};
              f32x4 y0 = {0.f, 0.f, 0.f, 0.f}, y1 = y0;
#pragma unroll
              for (int i = 0; i < 4; ++i) { y0 = mfma16(frag_t2(a0 + i * 2048), o4, y0); y1 = mfma16(frag_t2(a1 + i * 2048), o4, y1); }
              if ((l & 14) == 0) st16(__builtin_bit_cast(u32x4, (l & 1) ? y1 : y0), rs, g * 16 + (l & 1) * 512, OFF_CS + ((h * 6 + 2 * w) * 8 + p) * 64);
            }
          }
          if constexpr (!IT && s == CSL && n == 4) { asm volatile("" : "=v"(fx[0])); if (w < 3) ld16h<0>(fx[0], rs, l * 16, OFF_CS + h * 3072 + w * 1024); }
          if constexpr (!IT && s == CSS && n == 2) {
            if (w < 3) {
              wait_vm<SINCE(CSL)>();
              asm volatile("" : "+v"(fx[0])::"memory");
              tile_sum(fx[0], L_CSL);
            }
          }
          if constexpr (s == ABL && n == 4) ld4h<0>(aq[0], rs, (l & 31) * 4 + (l >> 5) * 8192, AB_OFF());
          if constexpr (s == ABS && n == 0) {
            wait_vm<SINCE(ABL)>();
            asm volatile("" : "+v"(aq[0])::"memory");
            if constexpr (IT) {
              for (unsigned spins = 0; !__all(aq[0] != POISON) && !spin_fail(spins, rs, T);) {
                ld4h<0>(aq[0], rs, (l & 31) * 4 + (l >> 5) * 8192, AB_OFF());
                asm volatile("s_waitcnt vmcnt(0)" : "+v"(aq[0])::"memory");
              }
            }
            *(LDS_AS unsigned*)(smem + L_K + (8 + rt) * 2048 + l * 4) = aq[0];
          }
          if constexpr (s == ABS && n == 2) {
#pragma unroll
            for (int a = 0; a < 2; ++a) {
              const LDS_AS char* ab = (const LDS_AS char*)(smem + L_K + (8 + rt) * 2048 + a * 64);
              aq[a] = *(const LDS_AS unsigned*)(ab + (l & 15) * 4); bq[a] = *(const LDS_AS unsigned*)(ab + 128 + (l & 15) * 4);
              av[a] = *(const LDS_AS u32x4*)(ab + g * 16); bv[a] = *(const LDS_AS u32x4*)(ab + 128 + g * 16);
            }
          }
          if constexpr (s == ABS && n == 5) {
#pragma unroll
            for (int a = 0; a < 2; ++a) { b2[a] = f32x2{asf(bq[a]), asf(bq[a])}; asm volatile("" : "+v"(b2[a])); }
          }
          if constexpr (s == ABS + 1) {
            constexpr int k = (n + 4) % 6;
            if constexpr (k < 4) cs4[k & 1] = *(const LDS_AS f32x4*)(smem + L_CSL + k * 64 + g * 16);
            else cs4[k & 1][0] = *(const LDS_AS float*)(smem + L_CSL + k * 64 + (l & 15) * 4);
          }
          if constexpr ((s == ABS + 1 && n > 0) || (s == ABS + 2 && n == 0)) {
            constexpr int k = (n + 3) % 6;
            if constexpr (k < 4) {
#pragma unroll
              for (int a = 0; a < 2; ++a) acc[a][k] = __builtin_elementwise_fma(cs4[k & 1], f32x4{b2[a][0], b2[a][1], b2[a][0], b2[a][1]}, acc[a][k]);
            } else {
              f32x2 c2 = {cs4[k & 1][0], cs4[k & 1][0]};
              asm volatile("" : "+v"(c2));
#pragma unroll
              for (int a = 0; a < 2; ++a) acc[a][k] = __builtin_elementwise_fma(f32x4{c2[0], c2[1], c2[0], c2[1]}, __builtin_bit_cast(f32x4, bv[a]), acc[a][k]);
            }
          }
          if constexpr (IT == 0) {
            if constexpr (s == IMG - 2 && n < 3) {
              const int c = 3 * (w & 1) + n;
              ka[n] = (FRAG_T() ^ ((c & 1) << 5)) + (SLOT(5) * STAGE + (c >> 1) * 8192 + (w >> 1) * 2048);
              asm volatile("" : "+v"(ka[n]));
            }
            if constexpr (s == IMG - 1 && (n & 1)) keep[n >> 1] = frag_t2(ka[n >> 1]);
            if constexpr (s > KP0 && s < KP0 + 3 && (n & 1)) keep[3 * (s - KP0) + (n >> 1)] = frag_t2(ka[n >> 1] + (s - KP0) * STAGE);
            if constexpr (s >= IMG && s < IMG + 3 && n == 4) st16(keep[s - IMG], rs, l * 16 + (s - IMG) * 1024, OFF_WI + (h * 8 + ((rot + 5) & 7)) * STAGE + w * 3072);
            if constexpr (s >= TS0 && s < TS0 + TSN && n == 4) {
              if (tstep == s && w < TW) touch(tdum, tsrc);
            }
            if constexpr (s == TST && n == 4) { if (w >= TW) touch(tdum, tsrc); }
          } else {
            if constexpr (s == PB1 && n == 0) { POISON_BIG(1); }
            if constexpr (s == FKL && n == 4) ld4h<0>(kv, rs, (l & 15) * 4, OFF_KEY + (bid & 15) * 128);
            if constexpr (s == FSL && n == 4) {
              wait_vm<SINCE(FKL)>();
              asm volatile("" : "+v"(kv)::"memory");
              if (__builtin_expect(!__all(kv != POISON), 0)) {
                LDS_AS unsigned* kw = (LDS_AS unsigned*)(smem + L_SKEY + 40);
                if (w == 0) {
                  kv = key_wait(rs, T, l, 0, bid & 15, kv);
                } else {
                  while ((kv = __hip_atomic_load(kw, __ATOMIC_RELAXED, __HIP_MEMORY_SCOPE_WORKGROUP)) == POISON) __builtin_amdgcn_s_sleep(2);
                }
              }
              fkey = __builtin_amdgcn_readfirstlane(umax16(kv));
              if (w == 0) *(LDS_AS unsigned*)(smem + L_SKEY + 40) = min(fkey, POISON - 1);
              sel0 = key_row(fkey);
              aff = (int)(sel0 >> 8) == p;
              row_load(rs, g, ffn_xoff(fkey, 0, w), fx);
            }
            if constexpr (s == FS0 && n == FSG) {
              wait_vm<SINCE(FSL)>();
              PINV4(fx);
              asm volatile("" : "=v"(sa), "=v"(sb));
              if (ffn_on) ffn_gu(gv, uv, fx, sa, sb);
            }
            if constexpr (s == FS0 && n == FSG + 1) {
              if (ffn_on) {
                ffn_gu_check(rs, T, ffn_xoff(fkey, 0, w), l, gv, uv, fx, sa, sb);
                ffn_gu_store(smem, l, w, sa, sb);
                count_in(1);
              }
            }
            if constexpr (s == FS0D && n == FDG) {
              if (ffn_on && w < 4) {
                s16x4 da[8], db[8];
                ffn_d_read(smem, l, 2 * w, da);
                while (__hip_atomic_load(qcnt + 1, __ATOMIC_RELAXED, __HIP_MEMORY_SCOPE_WORKGROUP) < 8u) {}
                asm volatile("" ::: "memory");
                const f32x4 t = ffn_t_read(smem, l);
                ffn_d_read(smem, l, 2 * w + 1, db);
                u32x2 ha = ffn_h(t, l), hb = {dppu<0x118>(ha[0]), dppu<0x118>(ha[1])};
                f32x4 ya = {0.f, 0.f, 0.f, 0.f}, yb = ya;
#pragma unroll
                for (int i = 0; i < 8; ++i) {
                  ya = mfma16k16(da[i], __builtin_bit_cast(s16x4, ha), ya); yb = mfma16k16(db[i], __builtin_bit_cast(s16x4, hb), yb);
                  ha = u32x2{dppu<0x111>(ha[0]), dppu<0x111>(ha[1])}; hb = u32x2{dppu<0x111>(hb[0]), dppu<0x111>(hb[1])};
                }
                ffn_d_store(rs, l, 8 * w, 0, slice, ya + yb);
              }
            }
#define RED_LOAD(ld, l8)                                                                                                    \
  {                                                                                                                         \
    const unsigned G = bi, po = OFF_PART + G * 32768;                                                        \
    ld(gv[0], rs, tid * 16, po); ld(gv[1], rs, tid * 16, po + 4096); ld(gv[2], rs, tid * 16, po + 8192); ld(gv[3], rs, tid * 16, po + 12288); \
    ld(uv[0], rs, tid * 16, po + 16384); ld(uv[1], rs, tid * 16, po + 20480); ld(uv[2], rs, tid * 16, po + 24576);        \
    l8(cxn, rs, (l & 3) * 16 + (l & 4) * 2, OFF_CA + (sel0 >> 7) * 2048 + G * 64);                                         \
    l8(cx1, rs, (l & 3) * 16 + (l & 4) * 2, OFF_CB + (sel0 >> 7) * 2048 + G * 64);                                          \
  }
            if constexpr (s == FS1 && n == 4) {
              asm volatile("" : "=v"(cxn), "=v"(cx1));
              if (!ffn_on && w < 4) RED_LOAD(ld16u, ld8h);
            }
            if constexpr (s == FS1S && n == FRG) {
              if (!ffn_on && w < 4) {
                wait_vm<SINCE(FS1)>();
                PINV4(gv); PINV4(uv);
                asm volatile("" : "+v"(cxn), "+v"(cx1)::"memory");
                for (unsigned spins = 0; !__all(max(max(max(umax4(gv[0]), umax4(gv[1])), max(umax4(gv[2]), umax4(gv[3]))),
                                                    max(max(umax4(uv[0]), umax4(uv[1])), max(max(umax4(uv[2]), max(cxn[0], cxn[1])), max(cx1[0], cx1[1])))) != POISON) && !spin_fail(spins, rs, T);) {
                  RED_LOAD(ld16u, ld8h);
                  DRAIN();
                  PINV4(gv); PINV4(uv);
                  asm volatile("" : "+v"(cxn), "+v"(cx1)::"memory");
                }
#define F4(x) __builtin_bit_cast(f32x4, x)
                ((LDS_AS f32x4*)(smem + L_SFIN))[tid] = ((F4(gv[0]) + F4(gv[1])) + (F4(gv[2]) + F4(gv[3]))) + ((F4(uv[0]) + F4(uv[1])) + F4(uv[2]));
#undef F4
                count_in(2);
                if (w == 0) {
                  while (__hip_atomic_load(qcnt + 2, __ATOMIC_RELAXED, __HIP_MEMORY_SCOPE_WORKGROUP) < 4u) {}
                  asm volatile("" ::: "memory");
                  const u32x2 ov = red_final<4>(smem, l, fkey, cxn, cx1);
                  if (l < 8) st8(unpoison2(ov), rs, OFF_XNEW + (bi * 8 + (l & 3) * 2 + (l >> 2)) * 8, 0);
                }
              }
            }
            if constexpr (s == FXA && n == 4) { DEF4(fx); if (w < 3) row_load(rs, g, XP_OFF(), fx); }
            if constexpr (s == FXB && n == FXG) {
              if (w < 3) {
                const unsigned fa = bo[0][0] + SLOT(FXT) * STAGE + w * 2048;
                u32x4 f[8];
#pragma unroll
                for (int i = 0; i < 8; ++i) f[i] = *(const LDS_AS u32x4*)(size_t)(fa + (i >> 1) * 6144 + (i & 1) * 1024);
                wait_vm<SINCE(FXA)>();
                PINV4(fx);
                ffn_xwait<XSL>(rs, T, XP_OFF(), l, fx);
                f32x4 y0 = mfma16(f[0], fx[0], f32x4{0.f, 0.f, 0.f, 0.f}), y1 = mfma16(f[1], fx[0], f32x4{0.f, 0.f, 0.f, 0.f});
#pragma unroll
                for (int i = 1; i < 4; ++i) { y0 = mfma16(f[2 * i], fx[i], y0); y1 = mfma16(f[2 * i + 1], fx[i], y1); }
                if ((l & 14) == 0) st16(unpoison4(__builtin_bit_cast(u32x4, (l & 1) ? y1 : y0)), rs, g * 16 + (l & 1) * 512, OFF_FX + ((h * 6 + 2 * w) * 8 + p) * 64);
              }
            }
            if constexpr (s == FXL && n == 4) {
              asm volatile("" : "=v"(fx[0]), "=v"(fx[1]));
              if (aff && w < 4) { ld16h<0>(fx[0], rs, l * 16, FP_OFF(0)); ld16h<0>(fx[1], rs, l * 16, FP_OFF(1)); }
            }
            if constexpr (s == FXS && n == 2) {
              if (aff && w < 4) {
                wait_vm<0>();
                asm volatile("" : "+v"(fx[0]), "+v"(fx[1])::"memory");
                if (!FX_OK(2)) {
                  unsigned spins = 0;
                  do {
                    if (spin_fail(spins, rs, T)) break;
                    fx[0] = ld16(rs, l * 16, FP_OFF(0)); fx[1] = ld16(rs, l * 16, FP_OFF(1));
                  } while (!FX_OK(2));
                }
                if (w == 3) {
                  f32x2 nx[8];
                  unpack8v(fx[0], nx); unpack8v(fx[1], nx + 4);
                  const f32x2 sm = ((nx[0] + nx[1]) + (nx[2] + nx[3])) + ((nx[4] + nx[5]) + (nx[6] + nx[7]));
                  const float mean = sum64(hadd(sm)) * (1.0f / 1024.0f);
                  const f32x2 m2 = {mean, mean};
                  f32x2 q = {0.f, 0.f};
#pragma unroll
                  for (int j = 0; j < 8; ++j) { const f32x2 d = nx[j] - m2; q = __builtin_elementwise_fma(d, d, q); }
                  const float rd = ln_rd(sum64(hadd(q)) * (1.0f / 1024.0f));
                  if (l == 0) *(LDS_AS f32x2*)(smem + L_FXL + 384) = f32x2{rd, -rbf1(mean)};
                } else {
                  tile_sum(fx[0], L_FXL);
                }
                count_in(3);
              }
            }
          }
        };
        static_for<32>([&](auto sc_) __attribute__((always_inline)) {
          constexpr int s = decltype(sc_)::value;
          if constexpr (s > 0) PIN_ACC();
          if constexpr (IT == 1) wait_vm<vm_at(IT, s, 0) - vm_a(IT, s)>();
          pin_w<s % WA>(af[s % WA][0], af[s % WA][1]);
          SB();
          static_for<6>([&](auto nc_) __attribute__((always_inline)) {
            constexpr int n = decltype(nc_)::value;
            if constexpr (s == 0) { acc[0][n] = f32x4{0.f, 0.f, 0.f, 0.f}; acc[1][n] = acc[0][n]; }
            constexpr bool on = s < QL || n >= 2;
            if constexpr (on) acc[0][n] = n < 4 ? mfma16(bfr[n].v, af[s % WA][0], acc[0][n]) : mfma16(af[s % WA][0], bfr[n].v, acc[0][n]);
            SB();
            if constexpr (n == 0) { QKV_RDB(s, 5); }
            else if constexpr (s + 1 < 32 && (s + 1 < QL || n > 2)) QKV_RDB(s + 1, n - 1);
            SB();
            if constexpr (on) acc[1][n] = n < 4 ? mfma16(bfr[n].v, af[s % WA][1], acc[1][n]) : mfma16(af[s % WA][1], bfr[n].v, acc[1][n]);
            SB();
            if constexpr (s + 1 < 32 && (s + 1 < QL || n >= 2)) QKV_RDA(s + 1, n);
            SB();
            gap(sc_, nc_);
            SB();
          });
        });
        PIN_ACC();
        frag_t qw[32 - QL][2];
        static_for<32 - QL>([&](auto c_) __attribute__((always_inline)) {
          constexpr int s = QL + decltype(c_)::value;
          QKV_RDA(s, 0); QKV_RDB(s, 0); QKV_RDA(s, 1); QKV_RDB(s, 1);
          qw[s - QL][0] = bfr[0]; qw[s - QL][1] = bfr[1];
        });
        SB();
        const bool own = aff && (int)((sel0 >> 5) & 7) == rt;
        const bool m0 = !(sel0 & 16), mq = (l & 15) == (int)(sel0 & 15), mv = g == (int)((sel0 & 15) >> 2);
        auto patch_qk = [&](int n) __attribute__((always_inline)) {
          f32x4 t = *(const LDS_AS f32x4*)(smem + L_FXL + n * 64 + g * 16);
          const f32x4 c = *(const LDS_AS f32x4*)(smem + L_CSL + n * 64 + g * 16);
          const float nb = *(const LDS_AS float*)(smem + L_FXL + 388);
#pragma unroll
          for (int e = 0; e < 4; ++e) { t[e] = __builtin_fmaf(c[e], nb, t[e]); asm volatile("" : "+v"(t[e])); }
          acc[0][n] = (mq && m0) ? t : acc[0][n];
          acc[1][n] = (mq && !m0) ? t : acc[1][n];
        };
        if (own) {
          while (__hip_atomic_load(qcnt + 3, __ATOMIC_RELAXED, __HIP_MEMORY_SCOPE_WORKGROUP) < 4u) {}
          asm volatile("" ::: "memory");
          patch_qk(2); patch_qk(3);
          const f32x2 nab = *(const LDS_AS f32x2*)(smem + L_FXL + 384);
          const LDS_AS float* fl_ = (const LDS_AS float*)(smem + L_FXL + 256) + (l & 15);
          const LDS_AS float* cl_ = (const LDS_AS float*)(smem + L_CSL + 256) + (l & 15);
          const float t4 = __builtin_fmaf(cl_[0], nab[1], fl_[0]), t5 = __builtin_fmaf(cl_[16], nab[1], fl_[16]);
          const unsigned na = __builtin_bit_cast(unsigned, (float)nab[0]);
#pragma unroll
          for (int a = 0; a < 2; ++a) {
            const bool ma = a ? !m0 : m0;
            aq[a] = (mq && ma) ? na : aq[a];
#pragma unroll
            for (int e = 0; e < 4; ++e) {
              const bool me = mv && ma && e == (int)(sel0 & 3);
              acc[a][4][e] = me ? t4 : acc[a][4][e]; acc[a][5][e] = me ? t5 : acc[a][5][e];
              av[a][e] = me ? na : av[a][e];
            }
          }
        }
        constexpr float C2 = 1.4426950408889634f * 0.17677669529663687f;
#pragma unroll
        for (int a = 0; a < 2; ++a) {
          f32x2 a2 = {asf(aq[a]), asf(aq[a])};
          qs2[a] = f32x2{asf(aq[a]) * C2, asf(aq[a]) * C2};
          asm volatile("" : "+v"(a2), "+v"(qs2[a]));
          acc[a][2] *= f32x4{a2[0], a2[1], a2[0], a2[1]}; acc[a][3] *= f32x4{a2[0], a2[1], a2[0], a2[1]};
          acc[a][4] *= __builtin_bit_cast(f32x4, av[a]); acc[a][5] *= __builtin_bit_cast(f32x4, av[a]);
        }
        k0 = pack8(acc[0][2], acc[0][3]); k1 = pack8(acc[1][2], acc[1][3]); v0 = pack8(acc[0][4], acc[1][4]); v1 = pack8(acc[0][5], acc[1][5]);
        const unsigned kvo = OFF_KV + IT * (8u << 20) + (p * 32 + h) * 32768 + rt * 4096;
        if (p < 7) { st16(k0, rs, l * 16, kvo); st16(k1, rs, l * 16 + 1024, kvo); st16(v0, rs, l * 16 + 2048, kvo); st16(v1, rs, l * 16 + 3072, kvo); }
        char* ownp = smem + (8 + rt) * 2048 + l * 16;
        *(u32x4*)(ownp + L_K) = k0;
        if (rt < 7) *(u32x4*)(ownp + L_K + 1024) = k1;
        *(u32x4*)(ownp + L_V) = v0; *(u32x4*)(ownp + L_V + 1024) = v1;
        SB();
        static_for<32 - QL>([&](auto c_) __attribute__((always_inline)) {
          constexpr int s = QL + decltype(c_)::value;
#pragma unroll
          for (int n = 0; n < 2; ++n) { acc[0][n] = mfma16(qw[s - QL][n].v, af[s % WA][0], acc[0][n]); acc[1][n] = mfma16(qw[s - QL][n].v, af[s % WA][1], acc[1][n]); }
        });
        if (own) { patch_qk(0); patch_qk(1); }
      };
      qkv_loop(it_c);
    }
#undef A_LOAD1
#undef QKV_RDA
#undef QKV_RDB
#undef QKV_BO
    {
      IDS();
      const GAS bf16 *pwo, *pwg, *pwu;
      asm volatile("s_load_dwordx2 %0, %3, 24\n\ts_load_dwordx2 %1, %3, 40\n\ts_load_dwordx2 %2, %3, 48" : "=&s"(pwo), "=&s"(pwg), "=&s"(pwu) : "s"(ka));
      const int ql = l & 15;
      const f32x4 z4 = {0.f, 0.f, 0.f, 0.f}, ninf = {-INFINITY, -INFINITY, -INFINITY, -INFINITY};
      LDS_AS unsigned* rdy = (LDS_AS unsigned*)(smem + L_RDY);
      const unsigned rtag = T[0] + it;
      u32x4 qf[2];
      f32x4 cn[2], oa[2][2], sum[2], S[2][2];
      unsigned one2 = 0x3F803F80u;
      asm volatile("" : "+v"(one2));
      const u32x4 ones = {one2, one2, one2, one2};
      auto ex8 = [&](f32x4 a, f32x4 b) __attribute__((always_inline)) {
        return u32x4{pack2(__builtin_amdgcn_exp2f(a[0]), __builtin_amdgcn_exp2f(a[1])), pack2(__builtin_amdgcn_exp2f(a[2]), __builtin_amdgcn_exp2f(a[3])),
                     pack2(__builtin_amdgcn_exp2f(b[0]), __builtin_amdgcn_exp2f(b[1])), pack2(__builtin_amdgcn_exp2f(b[2]), __builtin_amdgcn_exp2f(b[3]))};
      };
      {
#pragma unroll
        for (int a = 0; a < 2; ++a) {
          const f32x4 c4 = {qs2[a][0], qs2[a][1], qs2[a][0], qs2[a][1]};
          qf[a] = pack8(acc[a][0] * c4, acc[a][1] * c4);
        }
        f32x4 mk;
#pragma unroll
        for (int e = 0; e < 4; ++e) mk[e] = (4 * g + e <= ql) ? 0.f : -INFINITY;
        const f32x4 d0 = mfma16(k0, qf[0], mk), d1 = mfma16(k1, qf[1], mk), d2 = mfma16(k0, qf[1], z4);
        const float m0 = -max_x32(max_x16(fmaxf(fmaxf(d0[0], d0[1]), fmaxf(d0[2], d0[3]))));
        const float m1 = -max_x32(max_x16(fmaxf(fmaxf(d1[0], d1[1]), fmaxf(d1[2], d1[3]))));
        cn[0] = f32x4{m0, m0, m0, m0}; cn[1] = f32x4{m1, m1, m1, m1};
        asm volatile("" : "+v"(cn[0]), "+v"(cn[1]));
        const f32x4 e0 = d0 + cn[0];
        const u32x4 p0 = {pack2(__builtin_amdgcn_exp2f(e0[0]), __builtin_amdgcn_exp2f(e0[1])), pack2(__builtin_amdgcn_exp2f(e0[2]), __builtin_amdgcn_exp2f(e0[3])), 0u, 0u};
        const u32x4 p1 = ex8(d2 + cn[1], d1 + cn[1]);
        oa[0][0] = mfma16(v0, p0, z4); oa[0][1] = mfma16(v1, p0, z4); sum[0] = mfma16(ones, p0, z4);
        oa[1][0] = mfma16(v0, p1, z4); oa[1][1] = mfma16(v1, p1, z4); sum[1] = mfma16(ones, p1, z4);
      }
      const char* kb_ = smem + L_K + l * 16;
      const char* vb_ = smem + L_V + l * 16;
      const unsigned wov = (l >> 3) * 2048 + (((((l & 7) >> 1) ^ ((l >> 4) & 3)) * 2 + (l & 1)) << 4), fnv = (l >> 1) * (F_ * 2) + (l & 1) * 16;
      const unsigned pp = __builtin_amdgcn_readfirstlane(p > 0 ? 32 : 0), pvo = OFF_KV + it * (8u << 20) + (p * 32 + h - pp) * 32768;
      const unsigned pfo = __builtin_amdgcn_readfirstlane(F_KV + KVF(it, p * 32 + h - pp));
      asm volatile("" : "+v"(oa[0][0]), "+v"(oa[0][1]), "+v"(oa[1][0]), "+v"(oa[1][1]), "+v"(sum[0]), "+v"(sum[1]));
      DRAIN();
      asm volatile("" : "+v"(tdum));
      if (l == 0) st8(T, rs, KVG(rt), F_KV + KVF(it, p * 32 + h));
      BAR();
      asm volatile("" : "+s"(pwo), "+s"(pwg), "+s"(pwu));
      WO_PIECE(15); WO_PIECE(14);
      u32x4 kf[2], vf[2];
      u32x4 p0, p1;
      float e0, e1;
#define EE(v, i) { e0 = __builtin_amdgcn_exp2f(v[i]); e1 = __builtin_amdgcn_exp2f(v[i + 1]); pin2s(e0, e1); SB(); }
#define CV(P, i) { unsigned t_ = pack2(e0, e1); pin1(t_); P[i] = t_; SB(); }
      auto body = [&](unsigned ad, auto dk_) __attribute__((always_inline)) {
        constexpr int dk = decltype(dk_)::value;
        const f32x4 a0 = S[1][0], a1 = S[1][1];
        sum[0] = mfma16(ones, p0, sum[0]); SB();
        EE(a0, 0);
        lds_wait<2>(kf[0], kf[1]);
        S[0][0] = mfma16(kf[0], qf[0], cn[0]); SB();
        CV(p1, 0); EE(a0, 2);
        S[0][1] = mfma16(kf[1], qf[0], cn[0]); SB();
        CV(p1, 1); EE(a1, 0);
        lds_wait<0>(vf[0], vf[1]);
        oa[0][0] = mfma16(vf[0], p0, oa[0][0]); SB();
        CV(p1, 2); EE(a1, 2);
        oa[0][1] = mfma16(vf[1], p0, oa[0][1]); SB();
        CV(p1, 3);
        S[1][0] = mfma16(kf[0], qf[1], cn[1]); SB();
        EE(S[0][0], 0);
        S[1][1] = mfma16(kf[1], qf[1], cn[1]); SB();
        lds2<2048 + dk>(kf[0], kf[1], ad); SB();
        CV(p0, 0); EE(S[0][0], 2);
        oa[1][0] = mfma16(vf[0], p1, oa[1][0]); SB();
        CV(p0, 1); EE(S[0][1], 0);
        oa[1][1] = mfma16(vf[1], p1, oa[1][1]); SB();
        lds2<2048 + L_V>(vf[0], vf[1], ad); SB();
        CV(p0, 2); EE(S[0][1], 2);
        sum[1] = mfma16(ones, p1, sum[1]); SB();
        CV(p0, 3);
      };
      auto last = [&]() __attribute__((always_inline)) {
        sum[0] = mfma16(ones, p0, sum[0]); SB();
        EE(S[1][0], 0);
        lds_wait<0>(vf[0], vf[1]);
        pin2(kf[0], kf[1]);
        oa[0][0] = mfma16(vf[0], p0, oa[0][0]); SB();
        CV(p1, 0); EE(S[1][0], 2);
        oa[0][1] = mfma16(vf[1], p0, oa[0][1]); SB();
        CV(p1, 1); EE(S[1][1], 0);
        const float f0 = __builtin_amdgcn_exp2f(S[1][1][2]), f1 = __builtin_amdgcn_exp2f(S[1][1][3]);
        p1[2] = pack2(e0, e1); p1[3] = pack2(f0, f1);
        oa[1][0] = mfma16(vf[0], p1, oa[1][0]); oa[1][1] = mfma16(vf[1], p1, oa[1][1]); sum[1] = mfma16(ones, p1, sum[1]);
      };
      u32x4 fd = {(unsigned)(size_t)pwu, (unsigned)((size_t)pwu >> 32) & 0xffffu, ffn_on ? 1024u * F_ * 2u : 0u, 0x00020000u};
      const unsigned fx0 = (unsigned)(size_t)pwg ^ (unsigned)(size_t)pwu, fx1 = (unsigned)(((size_t)pwg ^ (size_t)pwu) >> 32) & 0xffffu;
      unsigned fso = (unsigned)((4 * w - 1) * 32 * F_ + slice * 16) * 2, finc = 0, flm = lbase + 65536 + w * 1024 - 8192;
      auto piece = [&]() __attribute__((always_inline)) {
        if constexpr (it == 0) {
          fd[0] ^= fx0; fd[1] ^= fx1; finc ^= 32 * F_ * 2; fso += finc; flm += 8192;
          dma16d(fd, fnv, fso, flm);
        }
      };
      if (rt > 0) {
        unsigned ad = lbase + l * 16 + (6 + rt) * 2048;
        lds2<2048>(kf[0], kf[1], ad); lds2<2048 + L_V>(vf[0], vf[1], ad);
        lds_wait<2>(kf[0], kf[1]);
        S[0][0] = mfma16(kf[0], qf[0], cn[0]); S[0][1] = mfma16(kf[1], qf[0], cn[0]); S[1][0] = mfma16(kf[0], qf[1], cn[1]); S[1][1] = mfma16(kf[1], qf[1], cn[1]);
        SB();
        lds_wait<0>(vf[0], vf[1]);
        lds2<0>(kf[0], kf[1], ad);
        p0 = ex8(S[0][0], S[0][1]);
        lds_wait<0>(kf[0], kf[1]);
#pragma unroll 1
        for (int i = rt - 1; i > 0; --i) {
          ad -= 2048;
          body(ad, std::integral_constant<int, -2048>{});
          piece();
          SB();
        }
        piece();
        last();
      }
      {
        constexpr int FW = 4, FN = 8 / FW;
        if (w < FW) {
          const int g0 = FN * rt;
          poll_flags(rs, (l & 1) * 8, pfo + KVG(g0), T);
#pragma unroll
          for (int j = 0; j < FN; ++j) {
            const unsigned d = lbase + (g0 + j) * 2048, o = pvo + (g0 + j) * 4096;
            dma16s<0>(rs, l * 16, o, d + L_K); dma16s<1024>(rs, l * 16, o, d + L_K);
            dma16s<0>(rs, l * 16, o + 2048, d + L_V); dma16s<1024>(rs, l * 16, o + 2048, d + L_V);
          }
          if (it == 1 && l == 0) rdy[8 + w] = rtag;
        } else if (it == 1 && w < 6) {
          while (!__all(__hip_atomic_load(rdy + 8 + (l & (FW - 1)), __ATOMIC_RELAXED, __HIP_MEMORY_SCOPE_WORKGROUP) == rtag)) {}
#pragma unroll 1
          for (int j = w == 4 ? 13 : 5; j > (w == 4 ? 5 : 3); --j) {
#pragma unroll
            for (int v = 0; v < 8; ++v) WO_PIECE_OF(v, j);
          }
        }
        if (p == 0) { cn[0] = ninf; cn[1] = ninf; }
        f32x4 mk;
#pragma unroll
        for (int e = 0; e < 4; ++e) mk[e] = (4 * g + e > ql) ? 0.f : -INFINITY;
        const f32x4 cm0 = cn[0] + mk, cm1 = cn[1] + mk;
        if (w < FW) {
          if constexpr (FN > 1) { wait_vm<2 * FN>(); if (l < FN / 2) rdy[FN * rt + l] = rtag; }
          DRAIN();
          if (l < FN) rdy[FN * rt + l] = rtag;
        }
        if constexpr (it == 0) { WO_PIECE(13); WO_PIECE(12); piece(); }

        int have = rt - 1;
        auto need = [&](int gq) __attribute__((always_inline)) {
          if (__builtin_expect(gq > have, 0)) {
            do {
              const unsigned t = __hip_atomic_load(rdy + (l & 7), __ATOMIC_RELAXED, __HIP_MEMORY_SCOPE_WORKGROUP);
              have = rt - 1 + __builtin_ctz(~(((unsigned)__builtin_amdgcn_ballot_w64(t == rtag) & 0xffu) | 0x100u) >> rt);
            } while (gq > have);
            asm volatile("" ::: "memory");
          }
        };
        unsigned ad = lbase + l * 16 + rt * 2048;
        need(rt);
        lds2<0>(kf[0], kf[1], ad); lds2<L_V>(vf[0], vf[1], ad);
        lds_wait<2>(kf[0], kf[1]);
        S[0][0] = mfma16(kf[0], qf[0], cm0); S[0][1] = mfma16(kf[1], qf[0], cn[0]); S[1][0] = ninf; S[1][1] = mfma16(kf[1], qf[1], cm1);
        SB();
        lds_wait<0>(vf[0], vf[1]);
        need(rt + 1);
        lds2<2048>(kf[0], kf[1], ad);
        p0 = ex8(S[0][0], S[0][1]);
        lds_wait<0>(kf[0], kf[1]);
#pragma unroll 1
        for (int gq = rt + 1; gq < 8; ++gq) {
          need(gq + 1);
          body(ad, std::integral_constant<int, 2048>{});
          piece();
          SB();
          ad += 2048;
        }
        last();
      }
      {
        float big = 0.f;
#pragma unroll
        for (int a = 0; a < 2; ++a)
#pragma unroll
          for (int d = 0; d < 2; ++d)
            big = __builtin_elementwise_maximum(__builtin_elementwise_maximum(big, __builtin_elementwise_maximum(fabsf(oa[a][d][0]), fabsf(oa[a][d][1]))),
                                                __builtin_elementwise_maximum(fabsf(oa[a][d][2]), fabsf(oa[a][d][3])));
        bool ok = sum[0][0] >= 0.5f && sum[0][0] < 0x1p64f && sum[1][0] >= 0.5f && sum[1][0] < 0x1p64f && big < INFINITY;
        if (!__all(ok)) {
#pragma unroll
          for (int a = 0; a < 2; ++a) {
            const int qs = 2 * rt + a, qq = 256 + 16 * qs + ql, klo = max(qq - 255, p > 0 ? 0 : 256);
            auto score = [&](int t) __attribute__((always_inline)) {
              f32x4 s = mfma16(t == 31 ? k1 : *(const u32x4*)(kb_ + t * 1024), qf[a], z4);
#pragma unroll
              for (int e = 0; e < 4; ++e) { const int kk = 16 * t + 4 * g + e; s[e] = (kk <= qq && kk >= klo) ? s[e] : -INFINITY; }
              return s;
            };
            float mx = -INFINITY;
#pragma unroll 1
            for (int t = qs; t <= qs + 16; ++t) { const f32x4 s = score(t); mx = fmaxf(fmaxf(mx, fmaxf(s[0], s[1])), fmaxf(s[2], s[3])); }
            mx = max_x32(max_x16(mx));
            oa[a][0] = z4; oa[a][1] = z4; sum[a] = z4;
#pragma unroll 1
            for (int G = qs >> 1; G <= (qs + 16) >> 1; ++G) {
              const f32x4 s0 = score(2 * G), s1 = score(2 * G + 1);
              f32x4 e0, e1;
#pragma unroll
              for (int e = 0; e < 4; ++e) { e0[e] = __builtin_amdgcn_exp2f(s0[e] - mx); e1[e] = __builtin_amdgcn_exp2f(s1[e] - mx); }
              const u32x4 pf = pack8(e0, e1);
              oa[a][0] = mfma16(*(const u32x4*)(vb_ + G * 2048), pf, oa[a][0]);
              oa[a][1] = mfma16(*(const u32x4*)(vb_ + G * 2048 + 1024), pf, oa[a][1]);
              sum[a] = mfma16(ones, pf, sum[a]);
            }
          }
        }
      }
#pragma unroll
      for (int a = 0; a < 2; ++a) {
        const float inv = __builtin_amdgcn_rcpf(sum[a][0]);
        oa[a][0] *= inv; oa[a][1] *= inv;
      }
      const unsigned oo = OFF_O + it * (4u << 20) + ((p * 8 + rt) * 64 + h * 2) * 1024;
      st16(pack8(oa[0][0], oa[0][1]), rs, l * 16, oo);
      st16(pack8(oa[1][0], oa[1][1]), rs, l * 16 + 1024, oo);
      DRAIN();
      BAR();
      if (tid == 0) st8(T, rs, OFL(h), F_O + OFB(it, p));
      if constexpr (it == 0) {
        DEF4(gv); DEF4(uv);
        if (ffn_on) {
          const unsigned ro = 65536 + w * 1024 + (4 * g + ((l & 15) >> 2)) * 32 + (l & 3) * 8;
#pragma unroll
          for (int i = 0; i < 4; ++i) {
            frag_t f;
            f.hf.a = trread(smem, ro + i * 16384); f.hf.b = trread(smem, ro + i * 16384 + 512);
            gv[i] = f.v;
            f.hf.a = trread(smem, ro + i * 16384 + 8192); f.hf.b = trread(smem, ro + i * 16384 + 8704);
            uv[i] = f.v;
          }
        }
        PINV4(gv); PINV4(uv);
        {
          ARGP(px, 0);
          const unsigned xo = ((rb * 4 + (w & 3)) * 32 + (w >> 2) * 16) * 2048 + ct * 128, xw = (l >> 3) * 2048 + (((l & 7) ^ g) << 4);
          dma16(RSRC(px, 2048 * 1024 * 2), xw, xo, lbase + w * 4096); dma16(RSRC(px, 2048 * 1024 * 2), xw ^ 64, xo + 16384, lbase + w * 4096 + 1024);
        }
      }
      WO_PIECE(3); WO_PIECE(2); WO_PIECE(1); WO_PIECE(0);
      if constexpr (it == 0) { WO_PIECE(11); WO_PIECE(10); WO_PIECE(9); WO_PIECE(8); WO_PIECE(7); WO_PIECE(6); WO_PIECE(5); WO_PIECE(4); }
    }
    IDS();
    ARGP(pwr, 4); ARGP(pwd, 7); ARGP(pout, 8);
    const int kh = w >> 2, rt2 = rb * 4 + (w & 3);
    const unsigned R = rt2 * 32 + kh * 16 + (l & 15);
    const unsigned to = (rt2 * 64 + ct * 4 + kh) * 1024 + l * 16;
    const unsigned rmo = ct * 64 + ((g & 1) ? 12 + 4 * g : 4 * g);
    f32x2 v[8], wv[8];
    float wsum;
    {
      u32x4 wa[2], wrp[2], res[2];
#pragma unroll
      for (int j = 0; j < 2; ++j) {
        wrp[j] = *(const GAS u32x4*)(pwr + rmo + 32 * j);
        wa[j] = *(const GAS u32x4*)(pwr + l * 8 + j * 512);
      }
      {
        f32x2 t[8];
        unpack8v(wa[0], t); unpack8v(wa[1], t + 4);
        const f32x2 s = ((t[0] + t[1]) + (t[2] + t[3])) + ((t[4] + t[5]) + (t[6] + t[7]));
        wsum = sum64(hadd(s));
        unpack8v(trade16(wrp[0]), wv); unpack8v(trade16(wrp[1]), wv + 4);
        asm volatile("" : "+v"(wsum), "+v"(wv[0]), "+v"(wv[1]), "+v"(wv[2]), "+v"(wv[3]), "+v"(wv[4]), "+v"(wv[5]), "+v"(wv[6]), "+v"(wv[7]));
      }
      unsigned bt[4];
#pragma unroll
      for (int n = 0; n < 4; ++n) {
        bt[n] = lbase + 32768 + 65536 + kh * 4096 + (4 * g + ((l & 15) >> 2)) * 128 + (l & 3) * 8 + ((n ^ (2 * (g & 1) + ((l & 15) >> 3))) << 5);
        asm volatile("" : "+v"(bt[n]));
      }
      if (w == 0) poll_flags(rs, OFL(l & 31), F_O + OFB(it, rb >> 1), T);
      else if (it == 0 && w < 3) poll_flags2(rs, F_XN + (((w - 1) * 4 + g) * RX + bi % RX) * 256 + (l & 15) * 16, T);
      else if (it == 0 && w == 3) poll_flags(rs, KVF(0, (l >> 3) * 32 + h) + KVG(l & 7), F_KV, T);
      asm volatile("s_barrier" ::: "memory");
      const unsigned abase = OFF_O + it * (4u << 20) + (rt2 * 64 + 32 * kh) * 1024;
      const unsigned av0 = l * 16 + kh * 1024, av1 = l * 16 + (kh ^ 1) * 1024;
      u32x4 af[WB][2];
#define O_LOAD(s_) { ld16h<(wo_k(it, s_) & 1) * 2048>(af[(s_) % WB][0], rs, av0, abase + (wo_k(it, s_) >> 1) * 4096); ld16h<(wo_k(it, s_) & 1) * 2048>(af[(s_) % WB][1], rs, av1, abase + (wo_k(it, s_) >> 1) * 4096); }
      if (w >= 4) __builtin_amdgcn_s_setprio(1);
      static_for<WR>([&](auto c_) __attribute__((always_inline)) { O_LOAD(decltype(c_)::value); });
      if constexpr (it == 1) { ld16h<0>(res[0], rs, g * 16, OFF_XNEW + ct * 128); ld16h<64>(res[1], rs, g * 16, OFF_XNEW + ct * 128); }
      SB();
      f32x4 ac[2][4];
#pragma unroll
      for (int a = 0; a < 2; ++a)
#pragma unroll
        for (int n = 0; n < 4; ++n) ac[a][n] = f32x4{0.f, 0.f, 0.f, 0.f};
      u32x4 bfr[2][4];
#define WO_RD(buf, s, n)                                                                             \
  {                                                                                                  \
    const unsigned o_ = bt[n] + (wo_k(it, s) & 7) * 8192;                                             \
    frag_t f_;                                                                                       \
    f_.hf.a = trabs(o_); f_.hf.b = trabs(o_ + 2048);                                                 \
    bfr[buf][n] = f_.v;                                                                              \
  }
#pragma unroll
      for (int n = 0; n < 4; ++n) WO_RD(0, 0, n);
      static_for<16>([&](auto sc_) __attribute__((always_inline)) {
        constexpr int s = decltype(sc_)::value, cur = s & 1;
        pin_ac(ac);
        if constexpr (s == 7) __builtin_amdgcn_s_setprio(0);
        if constexpr (s == 1) lds_count<0>(lbase + L_QC);
        if constexpr (s == (it == 0 ? 3 : 11)) lds_look(smem + L_QC, 64);
        if constexpr (s < 15 && ((wo_k(it, s) ^ wo_k(it, s + 1)) & 8)) {
#pragma unroll
          for (int n = 0; n < 4; ++n) { bt[n] += (wo_k(it, s) & 8) ? -65536 : 65536; pin1(bt[n]); }
        }
        if constexpr (s < WB - WR) O_LOAD(WR + 2 * s);
        if constexpr (it == 0 && s == 8) lds_count<8>(lbase + L_QC);
        if constexpr (it == 0 && s == WDS) {
          DRAIN();
          if (ffn_on) { lds_look(smem + L_QC + 8, 16); WD_DMA_OF(w); }
        }
        if constexpr (it == 1 || s < WDS) wait_vm<wo_at(it, s) - wo_pos(it, s)>();
        pin2(af[s % WB][0], af[s % WB][1]);
        if constexpr (it == 1 && s == 9) pin2(res[0], res[1]);
        SB();
#pragma unroll
        for (int n = 0; n < 4; ++n) {
          ac[0][n] = mfma16(bfr[cur][n], af[s % WB][0], ac[0][n]);
          ac[1][n] = mfma16(bfr[cur][n], af[s % WB][1], ac[1][n]);
          if (s + 1 < 16) WO_RD(cur ^ 1, s + 1, n);
          if (it == 0 && s == 8) {
            const u32x2 t = *(const LDS_AS u32x2*)(smem + w * 4096 + (l & 15) * 128 + (((2 * n + (g >> 1)) ^ ((l & 15) >> 1)) << 4) + (g & 1) * 8);
            res[n >> 1][2 * (n & 1)] = t[0]; res[n >> 1][2 * (n & 1) + 1] = t[1];
          }
          if (s == 9) {
            if (it == 1 && n == 0) { res[0] = R == sel0 ? res[0] : x2p[0]; res[1] = R == sel0 ? res[1] : x2p[1]; }

            v[2 * n] = f32x2{lo16(res[n >> 1][2 * (n & 1)]), hi16(res[n >> 1][2 * (n & 1)])};
            v[2 * n + 1] = f32x2{lo16(res[n >> 1][2 * (n & 1) + 1]), hi16(res[n >> 1][2 * (n & 1) + 1])};
            pin2f(v[2 * n], v[2 * n + 1]);
          }
          SB();
        }
        if constexpr (s < WB - WR) { O_LOAD(WR + 2 * s + 1); }
        else if constexpr (s + WB < 16) { O_LOAD(s + WB); }
        SB();
      });
#undef WO_RD
#undef O_LOAD
#define XCH_AT(v) ((v) == 7 ? 155648 : (v) * 4096)
#pragma unroll
      for (int n = 0; n < 4; ++n) *(f32x4*)(smem + XCH_AT(w) + n * 1024 + l * 16) = ac[1][n];
#pragma unroll
      for (int n = 0; n < 4; ++n) { v[2 * n] += f32x2{ac[0][n][0], ac[0][n][1]}; v[2 * n + 1] += f32x2{ac[0][n][2], ac[0][n][3]}; }
      BAR();
#pragma unroll
      for (int n = 0; n < 4; ++n) {
        const f32x4 o = *(const f32x4*)(smem + XCH_AT(w ^ 4) + n * 1024 + l * 16);
        v[2 * n] += f32x2{o[0], o[1]}; v[2 * n + 1] += f32x2{o[2], o[3]};
      }
    }
    {
      if (it == 0) { st16(pack8v(v), rs, to, OFF_X1); st16(pack8v(v + 4), rs, to + 2048, OFF_X1); }
      SB();
      const f32x2 s = ((v[0] + v[1]) + (v[2] + v[3])) + ((v[4] + v[5]) + (v[6] + v[7]));
      f32x2 dp = v[0] * wv[0], q = v[0] * v[0];
#pragma unroll
      for (int j = 1; j < 8; ++j) { dp = __builtin_elementwise_fma(v[j], wv[j], dp); q = __builtin_elementwise_fma(v[j], v[j], q); }
      float mt = hadd(s), qq = hadd(q), dot = hadd(dp);
      add_g3(mt, qq, dot);
      const unsigned so = OFF_STAT + it * 524288 + rb * 32768, lrow = R & 127;
      if (g == 0) st16(unpoison4(__builtin_bit_cast(u32x4, f32x4{mt, qq, dot, 0.f})), rs, ct * 2048 + lrow * 16, so);
      SB();
      if (it == 0) wait_vm<1>();
      if constexpr (it == 0) {
        volatile LDS_AS u32x2* in = (volatile LDS_AS u32x2*)(smem + L_XC);
        if (l == 0) in[w] = T;
        const u32x2 t = in[l & 7];
        if (__all(t[0] == T[0] && t[1] == T[1]) && l < RX) st8(T, rs, (l * 32 + (rb & 1) * 16 + ct) * 8, F_X1 + (rb >> 1) * RX * 32 * 8);
      }
      if (it == 1) { if (ffn_on) { WD_DMA_OF(w); } }
      if (it == 0) {
#pragma unroll
        for (int j = 0; j < 9; ++j) *(u32x4*)(smem + L_RING + (j / 3) * STAGE + (w * 3 + j % 3) * 1024 + l * 16) = keep[j];
      }
      __builtin_amdgcn_s_sleep(it == 0 ? SDI0 : SDIR);
      char* const skey = smem + (it == 0 ? L_SKEY : L_XC);
      u32x4 sp[4];
      ld16h<0>(sp[0], rs, g * 8192 + lrow * 16, so); ld16h<2048>(sp[1], rs, g * 8192 + lrow * 16, so);
      ld16h<0>(sp[2], rs, g * 8192 + lrow * 16, so + 4096); ld16h<2048>(sp[3], rs, g * 8192 + lrow * 16, so + 4096);
      u32x2 xf;
      asm volatile("" : "=v"(xf));
      if (it == 0 && w == 0) ld8h(xf, rs, (l & 31) * 8, F_X1 + (p * RX + (xcd >> 1) % RX) * 32 * 8);
      DRAIN();
      asm volatile("" : "+v"(xf)::"memory");
      PINV4(sp);
      const RowFin r = fin_key(rs, T, g * 8192 + lrow * 16, so, skey, l, w, R, wsum, sp);
      if constexpr (it == 0) {
        if (w == 0) {
          const unsigned need = 0x30003u << (((PROT() + 5) & 7) * 2);
          unsigned m = (unsigned)__builtin_amdgcn_ballot_w64(xf[0] == T[0] && xf[1] == T[1]);
          for (unsigned spins = 0; (m & need) != need;) {
            if (spin_fail(spins, rs, T)) { m = ~0u; break; }
            const u32x2 f = ld8(rs, F_X1 + ((p * RX + (xcd >> 1) % RX) * 32 + (l & 31)) * 8, 0);
            m = (unsigned)__builtin_amdgcn_ballot_w64(f[0] == T[0] && f[1] == T[1]);
          }
          if (l == 0) *(LDS_AS u32x4*)(skey + 32) = u32x4{m, 0u, POISON, 0u};
        }
        if (tid == 192) *(LDS_AS u32x4*)(smem + L_QC) = u32x4{0u, 0u, 0u, 0u};
      }
      if (it == 1 && tid == 0) *(LDS_AS unsigned*)(smem + L_XC + 32) = POISON;
      BAR();
      if constexpr (it == 0) {
        PRE_LOAD(1, PROT() + 5, 0, 4);
        xm0 = __builtin_amdgcn_readfirstlane(*(const LDS_AS unsigned*)(skey + 32));
      }
      fin_cand(rs, skey, it, g, rb, ct, r, v);
      fin_x2(r, v);
      if constexpr (it == 0) {
        x2p[0] = pack8v(v); x2p[1] = pack8v(v + 4);
        asm volatile("" : "+v"(x2p[0]), "+v"(x2p[1]));
        if ((w & 3) * 2 + (w >> 2) == ct >> 1) {
          const float f = 1.0f + r.rd, rd2 = ln_rd(r.var * f * f), mb2 = rbf1(__builtin_fmaf(r.mean - r.mb, r.rd, r.mean));
          if (g < 2 && ((l >> 3) & 1) == (ct & 1))
            st4(min(__builtin_bit_cast(unsigned, g == 0 ? f * rd2 : -__builtin_fmaf(r.mb, r.rd, mb2) * __builtin_amdgcn_rcpf(f)), POISON - 1), rs, OFF_AB + 16384 + R * 4 + g * 8192, 0);
        }
      }
    }
    if (it == 1) {
      const u32x4 op0 = trade16(pack8v(v)), op1 = trade16(pack8v(v + 4));
#define OUT_STORE()                                                                                 \
  if (R != key_row(key)) {                                                                          \
    __builtin_amdgcn_raw_buffer_store_b128(op0, rout, (R * 1024 + rmo) * 2, 0, SC1);                  \
    __builtin_amdgcn_raw_buffer_store_b128(op1, rout, (R * 1024 + rmo + 32) * 2, 0, SC1);             \
  }
      unsigned key;
      {
        LDS_AS unsigned* kw = (LDS_AS unsigned*)(smem + L_XC + 32);
        if (w == 0) {
          __builtin_amdgcn_s_sleep(GSL);
          key = key_wait(rs, T, l, 1, bid & 15, key_load(rs, l, 1, bid & 15));
          *kw = min(key, POISON - 1);
        } else {
          unsigned k_;
          while ((k_ = __hip_atomic_load(kw, __ATOMIC_RELAXED, __HIP_MEMORY_SCOPE_WORKGROUP)) == POISON) __builtin_amdgcn_s_sleep(1);
          key = __builtin_amdgcn_readfirstlane(k_);
        }
      }
      if (ffn_on) {
        f32x4 ga, ua;
        u32x4 xv[4];
        s16x4 dfr[8];
        ld4x16(xv, rs, g * 16, ffn_xoff(key, 1, w));
        ffn_d_read(smem, l, w, dfr);
        ffn_gu(gv, uv, xv, ga, ua);
        ffn_gu_check(rs, T, ffn_xoff(key, 1, w), l, gv, uv, xv, ga, ua);
        ffn_gu_store(smem, l, w, ga, ua);
        BAR();
        f32x4 ya = {0.f, 0.f, 0.f, 0.f}, yb = ya;
        u32x2 hh = ffn_h(ffn_t_read(smem, l), l);
        ffn_d_mma(dfr, hh, ya, yb);
        if ((l & 15) < 8) ffn_d_store(rs, l, 4 * w, 1, slice, ya + yb);
        SB();
        __builtin_amdgcn_s_sleep(OSL);
        OUT_STORE();
      } else {
        OUT_STORE();
        const unsigned G = bi;
        if (G == 0 && tid == 0) st8(T, rs, F_EP, 0);
        u32x4 pv[4];
        u32x2 cxn, cx1;
        __builtin_amdgcn_s_sleep(RSL);
        red_load(rs, tid, 1, G, key, pv, cxn, cx1);
        red_sum(rs, smem, T, tid, w, 1, G, key, pv, cxn, cx1);
        BAR();
        if (w == 0) {
          const u32x2 ov = red_final<8>(smem, l, key, cxn, cx1);
          if (l < 8) __builtin_amdgcn_raw_buffer_store_b64(ov, rout, (key_row(key) * 1024 + G * 32 + l * 4) * 2, 0, 0);
        }
      }
#undef OUT_STORE
    }
  };
  layer(std::integral_constant<int, 0>{});
  layer(std::integral_constant<int, 1>{});
}

extern "C" void kernel_launch(void* const* d_in, const int* in_sizes, int n_in, void* d_out, int out_size,
                              void* d_ws, size_t ws_size, hipStream_t stream) {
  (void)hipFuncSetAttribute((const void*)mega_kernel, hipFuncAttributeMaxDynamicSharedMemorySize, LDS_BYTES);
  mega_kernel<<<256, 512, LDS_BYTES, stream>>>((const bf16*)d_in[0], (char*)d_ws, (const bf16*)d_in[1], (const bf16*)d_in[2], (const bf16*)d_in[4],
                                               (const bf16*)d_in[5], (const bf16*)d_in[6], (const bf16*)d_in[7], (bf16*)d_out);
}
```
